```python
import torch

def _softmax(x, axis=-1):
    return torch.softmax(x, dim=axis)

def _gelu(x):
    return torch.nn.functional.gelu(x, approximate='tanh')

def _silu(x):
    return torch.nn.functional.silu(x)

def _sigmoid(x):
    return torch.sigmoid(x)

def _layernorm(x, axis=-1, eps=1e-05):
    m = x.mean(dim=axis, keepdim=True)
    v = x.var(dim=axis, keepdim=True, unbiased=False)
    return (x - m) / torch.sqrt(v + eps)

def _scatter_add(tgt, idx, src, axis=0):
    return tgt.clone().index_add_(axis, idx, src)

def _segment_sum(src, seg, n):
    out = torch.zeros((n,) + src.shape[1:], dtype=src.dtype)
    return out.index_add_(0, seg, src)

def _pad(x, pw):
    flat = tuple((v for pair in reversed(pw) for v in pair))
    return torch.nn.functional.pad(x, flat)
kernel_name = 'concat_heads__memory_aug_recurrent_mem_tr__ssm_conv_h3__prompt'

def setup_inputs():

    _w = lambda a: (lambda t: t.to(torch.bfloat16) if t.is_floating_point() else t)(torch.as_tensor(a))

    def _A_setup():
        B, S, Dm = (1, 2048, 1024)
        H, Dh = (32, 128)
        M, seg = (8, 32)
        seg = min(seg, S)
        S = S // seg * seg
        return {'x': _w((lambda *s: torch.randn(*s, dtype=torch.bfloat16) * 0.05)(B, S, Dm)), 'mem0': _w((lambda *s: torch.randn(*s, dtype=torch.bfloat16) * 0.4)(B, M, Dm)), 'Wq': _w((lambda *s: torch.randn(*s, dtype=torch.bfloat16) * 6.4)(Dm, H, Dh) * Dm ** (-0.5)), 'Wk': _w((lambda *s: torch.randn(*s, dtype=torch.bfloat16) * 6.4)(Dm, H, Dh) * Dm ** (-0.5)), 'Wv': _w((lambda *s: torch.randn(*s, dtype=torch.bfloat16) * 0.4)(Dm, H, Dh) * Dm ** (-0.5)), 'Wo': _w((lambda *s: torch.randn(*s, dtype=torch.bfloat16) * 0.4)(H, Dh, Dm) * (H * Dh) ** (-0.5)), 'seg_len': seg}
    _A_in = _A_setup()

    def _B_setup():
        B, S, D = (1, 2048, 1024)
        ks = 4
        return {'q': _w((lambda *s: torch.randn(*s, dtype=torch.bfloat16) * 0.05)(B, S, D)), 'k': _w((lambda *s: torch.randn(*s, dtype=torch.bfloat16) * 0.4)(B, S, D)), 'v': _w((lambda *s: torch.randn(*s, dtype=torch.bfloat16) * 0.4)(B, S, D)), 'shift_k': _w((lambda *s: torch.randn(*s, dtype=torch.bfloat16) * 0.4)(D, ks) * 0.5), 'ssm_k': _w((lambda *s: torch.randn(*s, dtype=torch.bfloat16) * 0.4)(D, S) * (1.0 / S ** 0.5)), 'ssm_D': _w((lambda *s: torch.randn(*s, dtype=torch.bfloat16) * 0.4)(D))}
    _B_in = _B_setup()
    return {'A_x': _A_in['x'], 'A_mem0': _A_in['mem0'], 'A_Wq': _A_in['Wq'], 'A_Wk': _A_in['Wk'], 'A_Wv': _A_in['Wv'], 'A_Wo': _A_in['Wo'], 'A_seg_len': _A_in['seg_len'], 'B_q': _B_in['q'], 'B_k': _B_in['k'], 'B_v': _B_in['v'], 'B_shift_k': _B_in['shift_k'], 'B_ssm_k': _B_in['ssm_k'], 'B_ssm_D': _B_in['ssm_D']}

def reference(A_x, A_mem0, A_Wq, A_Wk, A_Wv, A_Wo, A_seg_len, B_q, B_k, B_v, B_shift_k, B_ssm_k, B_ssm_D):

    def _A_ref(x, mem0, Wq, Wk, Wv, Wo, seg_len):
        B, S, Dm = x.shape
        H, Dh = (Wq.shape[1], Wq.shape[2])
        M = mem0.shape[1]
        scale = Dh ** (-0.5)
        n_seg = S // seg_len
        L = M + seg_len
        idx = torch.arange(L)
        mask = (idx[:, None] < M) | (idx[:, None] >= idx[None, :])
        mem = mem0
        outs = []
        for s in range(n_seg):
            xs = x[:, s * seg_len:(s + 1) * seg_len, :].clone()
            h = torch.concatenate([mem, xs], axis=1)
            q = torch.einsum('btd,dhe->bhte', h, Wq)
            k = torch.einsum('btd,dhe->bhte', h, Wk)
            v = torch.einsum('btd,dhe->bhte', h, Wv)
            scores = torch.einsum('bhqe,bhke->bhqk', q, k) * scale
            scores = torch.where(mask[None, None], scores, -1000000000.0)
            p = _softmax(scores, axis=-1)
            o = torch.einsum('bhqk,bhke->bhqe', p, v)
            o = torch.einsum('bhte,hed->btd', o, Wo)
            mem = o[:, :M, :].clone()
            outs.append(o[:, M:, :])
        return torch.concatenate(outs, axis=1)

    def _B_ref(q, k, v, shift_k, ssm_k, ssm_D):
        B, S, D = q.shape
        ks = shift_k.shape[-1]
        k_pad = _pad(k, ((0, 0), (ks - 1, 0), (0, 0)))
        k_shift = torch.zeros_like(k)
        for t in range(ks):
            k_shift = k_shift + k_pad[:, t:t + S, :] * shift_k[:, ks - 1 - t][None, None, :]
        kv = k_shift * v
        L = 2 * S
        kv_pad = _pad(kv, ((0, 0), (0, S), (0, 0)))
        kf_pad = _pad(ssm_k, ((0, 0), (0, S)))
        kv_f = (lambda x, axes=(-2, -1): torch.fft.rfft2(x.float(), dim=axes))(kv_pad[:, None, :, :], axes=(1, 2))
        kf_f = (lambda x, axes=(-2, -1): torch.fft.rfft2(x.float(), dim=axes))(kf_pad[:, None, :], axes=(1, 2))
        kf_f = (lambda x, *d: x.permute(*d))(kf_f, 1, 2, 0)
        y_f = kv_f * kf_f[None, :, :, :]
        y = (lambda x, s, axes=(-2, -1): torch.fft.irfft2(x, s=s, dim=axes).to(torch.bfloat16))(y_f, (1, L), axes=(1, 2))[:, 0, :S, :]
        y = y + kv * ssm_D[None, None, :]
        return q * y
    a = _A_ref(A_x, A_mem0, A_Wq, A_Wk, A_Wv, A_Wo, A_seg_len)
    b = _B_ref(B_q, B_k, B_v, B_shift_k, B_ssm_k, B_ssm_D)
    return torch.concatenate([a, b], axis=-1)
```

```cpp
#include <hip/hip_runtime.h>
#include <hip/hip_bf16.h>

namespace mk {

#define GAS __attribute__((address_space(1)))
#define LAS __attribute__((address_space(3)))
#define DEV __device__ __forceinline__
#define RLX_AGENT __ATOMIC_RELAXED, __HIP_MEMORY_SCOPE_AGENT
typedef unsigned short u16;
typedef unsigned v2u __attribute__((ext_vector_type(2)));
typedef unsigned v4u __attribute__((ext_vector_type(4)));
typedef unsigned v8u __attribute__((ext_vector_type(8)));
typedef unsigned v16u __attribute__((ext_vector_type(16)));
typedef unsigned v32u __attribute__((ext_vector_type(32)));
typedef float f32x2 __attribute__((ext_vector_type(2)));
typedef float f32x4 __attribute__((ext_vector_type(4)));
typedef float f32x16 __attribute__((ext_vector_type(16)));
typedef __bf16 bf16x2 __attribute__((ext_vector_type(2)));
typedef __bf16 bf16x8 __attribute__((ext_vector_type(8)));
typedef __bf16 bf16x16 __attribute__((ext_vector_type(16)));
typedef GAS unsigned gu32;
typedef __amdgpu_buffer_rsrc_t rsrc_t;
typedef LAS unsigned char* lds_t;

constexpr int NSEG = 64;
constexpr unsigned SENT = 0xFFFFFFFFu;
constexpr int AUX_SC1 = 16, AUX_NT = 2;
constexpr int T_LAG = 61;
constexpr int G_AHEAD = 4;
constexpr unsigned SPIN_CAP = 1u << 21;
constexpr int LDS_BYTES = 163840;

constexpr int C_ECHO = 0, C_CLAIM = 1024, C_VERDICT = 3072, C_TMO = 3328, C_CONV = 3456, C_PROG = 3840, C_FLAG = 4096, C_ASK = C_FLAG + 224 * 128;
static_assert(C_TMO == 3328, "known to the generated code");
constexpr int F_Q = 0, F_K = 32, F_V = 64, F_Z = 96;
constexpr int CTRL_BYTES = C_ASK + 256 * 64;
constexpr int MB = 1 << 20;
constexpr int R_U = 0;
constexpr int R_MEM = 2 * MB;
constexpr int R_P = R_MEM + 2 * 65536;
constexpr int R_PT = 3 * MB;
#define ZRN 12
constexpr int R_QX = 4 * MB, R_KX = 12 * MB, R_Z = 20 * MB, R_KVT = R_Z + ZRN * 4 * MB, R_YT = R_KVT + 4 * MB, R_END = R_YT + 4 * MB;
DEV int hblock(int h) { return h * 262144 + ((h & 8) << 10); }

struct Params {
  const u16 *x, *mem0, *Wq, *Wk, *Wv, *Wo, *bq, *bk, *bv, *shift, *ssmk, *ssmD;
  u16* out;
  unsigned char* ctrl;
  unsigned char* data;
};

typedef const __attribute__((address_space(4))) Params* KArg;
DEV KArg args() {
  KArg a = (KArg)__builtin_amdgcn_kernarg_segment_ptr(); asm volatile("" : "+s"(a)); return a;
}
DEV rsrc_t mkrs(const void* p, unsigned bytes) { return __builtin_amdgcn_make_buffer_rsrc((void*)p, 0, bytes, 0x00020000); }
DEV v4u ld16s(rsrc_t rs, int v, int s) { return __builtin_amdgcn_raw_buffer_load_b128(rs, v, s, AUX_SC1); }
DEV v2u ld8s(rsrc_t rs, int v, int s) { return __builtin_amdgcn_raw_buffer_load_b64(rs, v, s, AUX_SC1); }
DEV v4u ld16(rsrc_t rs, int v, int s) { return __builtin_amdgcn_raw_buffer_load_b128(rs, v, s, 0); }
DEV v2u ld8(rsrc_t rs, int v, int s) { return __builtin_amdgcn_raw_buffer_load_b64(rs, v, s, 0); }
DEV void st16s(rsrc_t rs, int v, int s, v4u d) { __builtin_amdgcn_raw_buffer_store_b128(d, rs, v, s, AUX_SC1); }
DEV void st8s(rsrc_t rs, int v, int s, v2u d) { __builtin_amdgcn_raw_buffer_store_b64(d, rs, v, s, AUX_SC1); }
DEV void st4s(rsrc_t rs, int v, int s, unsigned d) { __builtin_amdgcn_raw_buffer_store_b32(d, rs, v, s, AUX_SC1); }
DEV int amap(int o, int par) { return ((o >> 13) << 14) | (par << 13) | (o & 8191); }
DEV unsigned umax(unsigned a, unsigned b) { return a > b ? a : b; }
DEV unsigned top(v4u v) { return umax(umax(v.x, v.y), umax(v.z, v.w)); }
DEV unsigned pk(float a, float b) { bf16x2 v = {(__bf16)a, (__bf16)b}; return __builtin_bit_cast(unsigned, v); }
DEV unsigned pks(float a, float b) { unsigned u = pk(a, b); return u < SENT - 1u ? u : SENT - 1u; }
DEV float blo(unsigned u) { return __builtin_bit_cast(float, u << 16); }
DEV float bhi(unsigned u) { return __builtin_bit_cast(float, u & 0xFFFF0000u); }
DEV float rb(float a) { return blo(pk(a, a)); }
DEV unsigned fu(float f) { return __builtin_bit_cast(unsigned, f); }
DEV float uf(unsigned u) { return __builtin_bit_cast(float, u); }
DEV f32x4 mfma16(v4u a, v4u b, f32x4 c) {
  return __builtin_amdgcn_mfma_f32_16x16x32_bf16(__builtin_bit_cast(bf16x8, a), __builtin_bit_cast(bf16x8, b), c, 0, 0, 0);
}
DEV f32x16 mfma32(v4u a, v4u b, f32x16 c) {
  return __builtin_amdgcn_mfma_f32_32x32x16_bf16(__builtin_bit_cast(bf16x8, a), __builtin_bit_cast(bf16x8, b), c, 0, 0, 0);
}
DEV f32x4 smf(v4u a, v8u b, f32x4 c, int idx) {
  return __builtin_amdgcn_smfmac_f32_16x16x64_bf16(__builtin_bit_cast(bf16x8, a), __builtin_bit_cast(bf16x16, b), c, idx, 0, 0);
}
DEV v8u cat(v4u a, v4u b) { return __builtin_shufflevector(a, b, 0, 1, 2, 3, 4, 5, 6, 7); }
DEV v4u lo4(v8u a) { return __builtin_shufflevector(a, a, 0, 1, 2, 3); }
DEV v4u hi4(v8u a) { return __builtin_shufflevector(a, a, 4, 5, 6, 7); }
DEV v4u lds16(lds_t L, int o) { return *(LAS v4u*)(L + o); }
DEV void sts16(lds_t L, int o, v4u v) { *(LAS v4u*)(L + o) = v; }
DEV void sts8(lds_t L, int o, v2u v) { *(LAS v2u*)(L + o) = v; }
#define SCHED() __builtin_amdgcn_sched_barrier(0)
#define PIN_A(x) asm volatile("" : "+a"(x))
#define PIN_V(x) asm volatile("" : "+v"(x))
DEV int opq(int v) { asm volatile("" : "+v"(v)); return v; }
DEV int fresh_lane() {
  int l; asm volatile("v_mbcnt_lo_u32_b32 %0, -1, 0\n\tv_mbcnt_hi_u32_b32 %0, -1, %0" : "=v"(l)); return l;
}
DEV f32x4 fresh_zero() {
  f32x2 a, b;
  asm volatile("v_mov_b64 %0, 0" : "=v"(a));
  asm volatile("v_mov_b64 %0, 0" : "=v"(b));
  return __builtin_shufflevector(a, b, 0, 1, 2, 3);
}
DEV unsigned fresh_sent() { unsigned z; asm volatile("v_mov_b32 %0, -1" : "=v"(z)); return z; }
#define SMF "v_smfmac_f32_16x16x64_bf16 "
#define MF16 "v_mfma_f32_16x16x32_bf16 "
#define SMF1(cls, d, a, b) asm volatile(SMF "%0, %1, %2, %3" : "+v"(d) : "v"(a), cls(b), "v"(idx))
#define SMF1F(cls, lo, hi, d, a, b) asm volatile(SMF "v[" #lo ":" #hi "], %1, %2, %3" : "+{v[" #lo ":" #hi "]}"(d) : "v"(a), cls(b), "v"(idx))
#define AT_HOME(M, j) do { const int j_ = (j); if (j_ == 0) M("{v[32:39]}"); if (j_ == 1) M("{v[40:47]}"); if (j_ == 2) M("{v[48:55]}"); if (j_ == 3) M("{v[56:63]}"); if (j_ == 4) M("{v[64:71]}"); if (j_ == 5) M("{v[72:79]}"); if (j_ == 6) M("{v[80:87]}"); if (j_ == 7) M("{v[88:95]}"); if (j_ == 8) M("{v[96:103]}"); if (j_ == 9) M("{v[104:111]}"); if (j_ == 10) M("{v[112:119]}"); if (j_ == 11) M("{v[120:127]}"); if (j_ == 12) M("{v[128:135]}"); if (j_ == 13) M("{v[136:143]}"); if (j_ == 14) M("{v[144:151]}"); if (j_ == 15) M("{v[152:159]}"); } while (0)
#define MF1(d, a, b) asm volatile(MF16 "%0, %1, %2, %0" : "+v"(d) : "v"(a), "v"(b))
#define MF1Z(d, a, b) asm volatile(MF16 "%0, %1, %2, 0" : "=&v"(d) : "v"(a), "v"(b))
#define LGKM(n) asm volatile("s_waitcnt lgkmcnt(" #n ")" ::: "memory")
#define RAWBAR() do { __builtin_amdgcn_s_barrier(); asm volatile("" ::: "memory"); } while (0)
DEV void settle3(f32x4& s0, f32x4& s1, f32x4& s2) { asm volatile("s_nop 7" : "+v"(s0), "+v"(s1), "+v"(s2)); }
DEV void settle4(f32x4 (&d)[4]) { asm volatile("s_nop 7" : "+v"(d[0]), "+v"(d[1]), "+v"(d[2]), "+v"(d[3])); }
#define BAR() do { asm volatile("s_waitcnt lgkmcnt(0)" ::: "memory"); __builtin_amdgcn_s_barrier(); asm volatile("" ::: "memory"); } while (0)
#define VMWAIT(n) asm volatile("s_waitcnt vmcnt(" #n ")" ::: "memory")
DEV float add1(float a, float b) {
  float o; asm("v_add_f32 %0, %1, %2" : "=v"(o) : "v"(a), "v"(b)); return o;
}
DEV float xmax(float v) {
  auto r = __builtin_amdgcn_permlane32_swap(fu(v), fu(v), false, false); v = fmaxf(uf(r[0]), uf(r[1]));
  auto q = __builtin_amdgcn_permlane16_swap(fu(v), fu(v), false, false); return fmaxf(uf(q[0]), uf(q[1]));
}
DEV float xsum(float v) {
  auto r = __builtin_amdgcn_permlane32_swap(fu(v), fu(v), false, false); v = uf(r[0]) + uf(r[1]);
  auto q = __builtin_amdgcn_permlane16_swap(fu(v), fu(v), false, false); return uf(q[0]) + uf(q[1]);
}

DEV float ror8(float v) { return uf(__builtin_amdgcn_update_dpp(0u, fu(v), 0x128, 0xf, 0xf, true)); }
DEV float fmax2(float a, float b) { return __builtin_amdgcn_fmed3f(a, b, __builtin_inff()); }
DEV float fmax3(float a, float b, float c) { float r; asm("v_max3_f32 %0, %1, %2, %3" : "=v"(r) : "v"(a), "v"(b), "v"(c)); return r; }
DEV bool allok(unsigned t) { return __all(t != SENT); }

template <int N> struct IC { static constexpr int value = N; };
struct Ctx {
  gu32* ctrl;
  rsrc_t rs;
  lds_t L;
  int w, lane;
  int b, slot, att;
  unsigned ask;
  unsigned budget;
};
DEV KArg enter(Ctx& c) {
  const KArg a = args();
  c.ctrl = (gu32*)a->ctrl;
  c.rs = mkrs(a->data, R_END);
  asm volatile("" : "+s"(c.w), "+s"(c.b), "+s"(c.att), "+s"(c.slot));
  return a;
}
DEV bool spend(Ctx& c) {
  if (c.budget == 0u) return true;
  c.budget -= 1u;
  return false;
}
DEV unsigned wait_flag(Ctx& c, int flag, unsigned need) {
  gu32* f = c.ctrl + (C_FLAG + flag * 128) / 4;
  unsigned v;
#pragma unroll 1
  for (;;) {
    v = __builtin_amdgcn_readfirstlane(__hip_atomic_load(f, RLX_AGENT));
    if (v >= need || spend(c)) break;
    __builtin_amdgcn_s_sleep(2);
  }
  __builtin_amdgcn_fence(__ATOMIC_ACQUIRE, "wavefront");
  return v;
}
DEV void post_flag(Ctx& c, int flag, unsigned v, int off = 0) {
  st4s(mkrs((const void*)c.ctrl, CTRL_BYTES), (c.w | off | fresh_lane()) ? 1 << 30 : 0, C_FLAG + flag * 128, v);
}
DEV void claim(Ctx& c, unsigned votes) {
  if (c.w != 0) return;
  if (c.att == 0) {
#pragma unroll 1
    while (__builtin_amdgcn_readfirstlane(__hip_atomic_load(c.ctrl + C_ECHO / 4 + c.b, RLX_AGENT)) != c.ask) {
      if (spend(c)) break;
      __builtin_amdgcn_s_sleep(2);
    }
  }
  if (fresh_lane() == 0) __hip_atomic_fetch_add(c.ctrl + (C_CLAIM + c.att * 1024) / 4 + c.slot, 1u + votes, RLX_AGENT);
}
DEV unsigned wait_verdict(Ctx& c) {
  unsigned v;
#pragma unroll 1
  for (;;) {
    v = __builtin_amdgcn_readfirstlane(__hip_atomic_load(c.ctrl + (C_VERDICT + c.att * 128) / 4, RLX_AGENT));
    if ((v & 1u) || spend(c)) break;
    __builtin_amdgcn_s_sleep(2);
  }
  __builtin_amdgcn_fence(__ATOMIC_ACQUIRE, "wavefront");
  if ((v & 3u) == 3u && c.att == 0) {
    asm volatile("s_waitcnt vmcnt(0) lgkmcnt(0)\n\ts_barrier\n\ts_mov_b64 s[0:1], %0\n\ts_mov_b32 s2, %1\n\tv_mov_b32 v0, %2\n\t"
                 "s_getpc_b64 s[4:5]\n1:\n\ts_sub_u32 s4, s4, 1b-mk_mega\n\ts_subb_u32 s5, s5, 0\n\ts_setpc_b64 s[4:5]"
                 :: "s"(__builtin_amdgcn_kernarg_segment_ptr()), "s"(c.b + 256), "v"(c.w * 64 + fresh_lane()) : "s0", "s1", "s2", "s4", "s5", "v0", "scc", "memory");
    __builtin_unreachable();
  }
  return v;
}
DEV void serve(Ctx& c) {
  c.budget = SPIN_CAP;
  const int lane = fresh_lane();
  const rsrc_t rc = mkrs((const void*)c.ctrl, CTRL_BYTES);
  if (c.att == 0) {
#pragma unroll
    for (int i = C_CLAIM / 1024; i < C_ASK / 1024; ++i) st16s(rc, lane * 16, i * 1024, v4u{0, 0, 0, 0});
    VMWAIT(0);
  }
#pragma unroll 1
  for (;;) {
    asm volatile("" ::: "memory");
    const v4u cl = ld16s(rc, lane * 16, C_CLAIM + c.att * 1024);
    if (c.att == 0) {
      v4u e;
#pragma unroll
      for (int i = 0; i < 4; ++i) e[i] = __builtin_amdgcn_raw_buffer_load_b32(rc, (lane * 4 + i) * 64, C_ASK, AUX_SC1);
      st16s(rc, lane * 16, C_ECHO, e);
    }
    unsigned n = 0, one = 1, va = 0, vb = 0;
#pragma unroll
    for (int i = 0; i < 4; ++i) { n += cl[i] & 0xFFFFu; one &= (cl[i] & 0xFFFFu) == 1u; va += (cl[i] >> 16) & 1u; vb += (cl[i] >> 24) & 1u; }
    unsigned sum = n | (va << 12) | (vb << 22);
#pragma unroll
    for (int o = 1; o < 64; o <<= 1) sum += (unsigned)__shfl_xor((int)sum, o);
    sum = __builtin_amdgcn_readfirstlane(sum);
    if ((sum & 0xFFFu) >= 256u) {
      const unsigned v = 1u | (__all(one) ? 0u : 2u) | (((sum >> 12) & 0x3FFu) > 32u ? 4u : 0u) | ((sum >> 22) > 32u ? 8u : 0u);
      if (lane == 0) __hip_atomic_store(c.ctrl + (C_VERDICT + c.att * 128) / 4, v, RLX_AGENT);
      break;
    }
    if (spend(c)) break;
  }
}
DEV int vote_a(unsigned v) { return (int)((v >> 2) & 1u); }
DEV int vote_b(unsigned v) { return (int)((v >> 3) & 1u); }

#define STAMP(slot) do {} while (0)
#define TICK_DECL
#define TICK(i) do {} while (0)
#define TICK_SUM(n) do {} while (0)
#define TICK_OUT() do {} while (0)
#define TKS(x) ""
#define TK_CLOB
#define TK0 TKS("s_memtime s[72:73]\n\t")
#define TK1 TKS("s_memtime s[74:75]\n\t")
#define TK2 TKS("s_memtime s[76:77]\n\t")
#define TK3 TKS("s_memtime s[78:79]\n\t")
#define TK4 TKS("s_memtime s[80:81]\n\t")
#define TK5 TKS("s_memtime s[82:83]\n\t")
#define TK6 TKS("s_memtime s[84:85]\n\t")
#define TK7 TKS("s_memtime s[86:87]\n\t")
#define DIAG(slot, v) do {} while (0)
#define STS(x) ""
#define STC(x) ""
#define STF(x) ""

DEV constexpr int krow(int rho8, int i) { return 32 * (rho8 >> 2) + 8 * (rho8 & 1) + 2 * ((rho8 >> 1) & 1) + i; }
DEV int krow_lane(int g) { return 16 * (g >> 1) + 4 * (g & 1); }
DEV unsigned prm(unsigned hi, unsigned lo, int odd) { return __builtin_amdgcn_perm(hi, lo, odd ? 0x07060302u : 0x05040100u); }

#define ISBAD "v_cmp_eq_u32 vcc, -1, %[t]\n\t"
#define SPEND(lbl) "s_cmp_eq_u32 %[bd], 0\n\ts_cbranch_scc1 " lbl "\n\ts_sub_u32 %[bd], %[bd], 1\n\t"
#define KNOB(i, d) (d##u)
#define CQ0 300
#define CV0 200
#define C1 KNOB(0, 20)
#define C2 KNOB(1, 296)
#define CU KNOB(2, 40)
#define CUG KNOB(3, 20)
#define KVO KNOB(4, 30)
#define KH KNOB(5, 284)
#define ZDLY KNOB(7, 12)
#define ZRING 63
#define HACK KNOB(6, 16)
#define PACE 0, 0, C2, KVO, 0, 0
DEV unsigned uni(unsigned v) { return __builtin_amdgcn_readfirstlane(v); }
#define ALL_REGS "=&{a[0:31]}"(wa[0]), "=&{a[32:63]}"(wa[1]), "=&{a[64:95]}"(wa[2]), "=&{a[96:127]}"(wa[3]), "=&{a[128:159]}"(wa[4]), "=&{a[160:191]}"(wa[5]), "=&{a[192:223]}"(wa[6]), \
    "=&{a[224:255]}"(wa[7]), "=&{v[32:63]}"(wv[0]), "=&{v[64:95]}"(wv[1]), "=&{v[96:127]}"(wv[2]), "=&{v[128:159]}"(wv[3]), "=&{v[224:255]}"(wv[4])
#define S_CLOB "s8", "s9", "s10", "s11", "s12", "s13", "s14", "s15", "s16", "s17", "s18", "s19", "s20", "s21", "s22", "s23", "s24", "s25", "s26", "s27", "s28", "s29", "s30", "s31", "s33", "s34", "s35", "s36", "s37", "s38", "s39", "s40", "s41", "s42", "s43", "s44", "s45", "s46", "s47", "s48", "s49", "s50", "s51", "s52", "s53", "s54", "s55", "s56", "s57", "s58", "s59", "s60", "s61", "s62", "s63", "s64", "s65", "s66", "s67", "s68", "s69", "s70", "s71"
#define ROTS(x) (unsigned)(((x) & 3) << 19), (unsigned)((((x) + 1) & 3) << 19), (unsigned)((((x) + 2) & 3) << 19), (unsigned)((((x) + 3) & 3) << 19)
#define RSRCS plo(p->data), phi(p->data), R_END, 0x00020000, plo(p->ctrl), phi(p->ctrl), CTRL_BYTES, 0x00020000
DEV unsigned plo(const void* q) { return (unsigned)(size_t)q; }
DEV unsigned phi(const void* q) { return (unsigned)((size_t)q >> 32); }

DEV constexpr int qk_r(int j) { return j < 4 ? 0 : (j - 4) % 3 + 1; }
DEV constexpr int qk_i(int j) { return j < 4 ? j : (j - 4) / 3; }
DEV void qk_role(Ctx& c, int h) {
  const KArg p = enter(c);
  constexpr int WL = 0, MEMS = 131072, KXL = 147456, QKX = 155648, QROW = 272, CTL = 160000;
  c.budget = SPIN_CAP;
  const int w = c.w, lane = fresh_lane(), g = lane >> 4, cc = lane & 15, q = (lane >> 2) & 3, pp = lane & 3;
  const int prow = (2 * g + (cc & 1)) * 2 + ((cc >> 1) & 1);
  v32u k;
#pragma unroll
  for (int i = 0; i < 32; ++i) k[i] = 0;
  k[0] = amap(w * 4096 + lane * 16, 0);
  k[1] = MEMS + w * 4096 + lane * 16;
#pragma unroll
  for (int r = 0; r < 3; ++r) k[2 + r] = MEMS + ((w + r + 1) & 3) * 4096 + lane * 16;
  k[5] = WL + w * 32768 + lane * 16;
  k[6] = (lane & 1) ? 0xEEEE : 0x4444;
  k[7] = w < 2 ? QKX + (4 * w + (cc >> 2)) * 256 + g * 64 + (cc & 3) * 4 : QKX + (8 + 2 * g) * QROW + (64 * (w & 1) + 4 * cc) * 2;
  k[8] = QKX + g * 16 + (8 + (cc & 7)) * QROW;
  k[9] = KXL + g * 512 + cc * 16;
  k[10] = w == 0 ? amap((h >> 1) * 1024 + ((2 * (h & 1) + (cc >> 3)) * 16 + prow) * 16 + ((cc >> 2) & 1) * 8, 0)
        : w == 1 ? amap(h * 512 + (g & 1) * 256 + cc * 16 + (g >> 1) * 8, 0) : w == 2 ? amap(16384 + h * 1024 + lane * 16, 0) : lane * 16;
  k[11] = w == 0 ? (cc < 8 ? amap(16384 + h * 128 + prow * 8 + (cc >> 2) * 4, 0) : 1 << 30)
        : w < 3 ? (g < 2 ? amap(49152 + ((w - 1) * 32 + h) * 256 + cc * 16 + g * 8, 0) : 1 << 30) : lane * 128;
  k[12] = w == 3 ? ((lane | h) ? 1 << 30 : C_PROG) : g * 512 + (16 * ((w + 1) & 1) + cc) * 16;
  k[13] = fu((w == 0 ? cc < 8 : g < 2) ? 1.0f : 0.0f);
  k[14] = ((lane >> 2) | w) ? 1 << 30 : amap(24576 + lane * 4096 + h * 128, 0);
  k[15] = amap(24576 + (h >> 3) * 4096 + (lane & 31) * 128, 0);
  k[16] = CTL;
#pragma unroll
  for (int i = 0; i < 2; ++i) k[20 + i] = (lane >> 3) * 8192 + (h * 128 + 64 * (w & 1)) * 2 + 16 * ((lane & 7) ^ (4 * i + 2 * (lane >> 5)));
#pragma unroll
  for (int t = 0; t < 4; ++t) k[22 + t] = WL + w * 32768 + (8 * (q >> 1) + (q & 1) + krow_lane(g)) * 128 + 16 * ((2 * t + (pp >> 1)) ^ (4 * (q >> 1) + 2 * (g & 1))) + 8 * (pp & 1);
  const u16* W = (w >> 1) ? p->Wk : p->Wq;
  asm volatile(
      "s_add_u32 s50, s56, s64\n\ts_add_u32 m0, s49, 0\n\ts_add_u32 s48, s50, 0x0\n\tbuffer_load_dwordx4 v20, s[40:43], s48 offen lds\n\ts_add_u32 m0, s49, 1024\n\t"
      "s_add_u32 s48, s50, 0x10000\n\tbuffer_load_dwordx4 v21, s[40:43], s48 offen lds\n\ts_add_u32 m0, s49, 2048\n\ts_add_u32 s48, s50, 0x20000\n\t"
      "buffer_load_dwordx4 v20, s[40:43], s48 offen lds\n\ts_add_u32 m0, s49, 3072\n\ts_add_u32 s48, s50, 0x30000\n\tbuffer_load_dwordx4 v21, s[40:43], s48 offen lds\n\t"
      "s_add_u32 m0, s49, 4096\n\ts_add_u32 s48, s50, 0x40000\n\tbuffer_load_dwordx4 v20, s[40:43], s48 offen lds\n\ts_add_u32 m0, s49, 5120\n\ts_add_u32 s48, s50, 0x50000\n\t"
      "buffer_load_dwordx4 v21, s[40:43], s48 offen lds\n\ts_add_u32 m0, s49, 6144\n\ts_add_u32 s48, s50, 0x60000\n\tbuffer_load_dwordx4 v20, s[40:43], s48 offen lds\n\t"
      "s_add_u32 m0, s49, 7168\n\ts_add_u32 s48, s50, 0x70000\n\tbuffer_load_dwordx4 v21, s[40:43], s48 offen lds\n\ts_add_u32 s50, s56, s65\n\ts_add_u32 m0, s49, 8192\n\t"
      "s_add_u32 s48, s50, 0x0\n\tbuffer_load_dwordx4 v20, s[40:43], s48 offen lds\n\ts_add_u32 m0, s49, 9216\n\ts_add_u32 s48, s50, 0x10000\n\t"
      "buffer_load_dwordx4 v21, s[40:43], s48 offen lds\n\ts_add_u32 m0, s49, 10240\n\ts_add_u32 s48, s50, 0x20000\n\tbuffer_load_dwordx4 v20, s[40:43], s48 offen lds\n\t"
      "s_add_u32 m0, s49, 11264\n\ts_add_u32 s48, s50, 0x30000\n\tbuffer_load_dwordx4 v21, s[40:43], s48 offen lds\n\ts_add_u32 m0, s49, 12288\n\ts_add_u32 s48, s50, 0x40000\n\t"
      "buffer_load_dwordx4 v20, s[40:43], s48 offen lds\n\ts_add_u32 m0, s49, 13312\n\ts_add_u32 s48, s50, 0x50000\n\tbuffer_load_dwordx4 v21, s[40:43], s48 offen lds\n\t"
      "s_add_u32 m0, s49, 14336\n\ts_add_u32 s48, s50, 0x60000\n\tbuffer_load_dwordx4 v20, s[40:43], s48 offen lds\n\ts_add_u32 m0, s49, 15360\n\ts_add_u32 s48, s50, 0x70000\n\t"
      "buffer_load_dwordx4 v21, s[40:43], s48 offen lds\n\ts_add_u32 s50, s56, s66\n\ts_add_u32 m0, s49, 16384\n\ts_add_u32 s48, s50, 0x0\n\t"
      "buffer_load_dwordx4 v20, s[40:43], s48 offen lds\n\ts_add_u32 m0, s49, 17408\n\ts_add_u32 s48, s50, 0x10000\n\tbuffer_load_dwordx4 v21, s[40:43], s48 offen lds\n\t"
      "s_add_u32 m0, s49, 18432\n\ts_add_u32 s48, s50, 0x20000\n\tbuffer_load_dwordx4 v20, s[40:43], s48 offen lds\n\ts_add_u32 m0, s49, 19456\n\ts_add_u32 s48, s50, 0x30000\n\t"
      "buffer_load_dwordx4 v21, s[40:43], s48 offen lds\n\ts_add_u32 m0, s49, 20480\n\ts_add_u32 s48, s50, 0x40000\n\tbuffer_load_dwordx4 v20, s[40:43], s48 offen lds\n\t"
      "s_add_u32 m0, s49, 21504\n\ts_add_u32 s48, s50, 0x50000\n\tbuffer_load_dwordx4 v21, s[40:43], s48 offen lds\n\ts_add_u32 m0, s49, 22528\n\ts_add_u32 s48, s50, 0x60000\n\t"
      "buffer_load_dwordx4 v20, s[40:43], s48 offen lds\n\ts_add_u32 m0, s49, 23552\n\ts_add_u32 s48, s50, 0x70000\n\tbuffer_load_dwordx4 v21, s[40:43], s48 offen lds\n\t"
      "s_add_u32 s50, s56, s67\n\ts_add_u32 m0, s49, 24576\n\ts_add_u32 s48, s50, 0x0\n\tbuffer_load_dwordx4 v20, s[40:43], s48 offen lds\n\ts_add_u32 m0, s49, 25600\n\t"
      "s_add_u32 s48, s50, 0x10000\n\tbuffer_load_dwordx4 v21, s[40:43], s48 offen lds\n\ts_add_u32 m0, s49, 26624\n\ts_add_u32 s48, s50, 0x20000\n\t"
      "buffer_load_dwordx4 v20, s[40:43], s48 offen lds\n\ts_add_u32 m0, s49, 27648\n\ts_add_u32 s48, s50, 0x30000\n\tbuffer_load_dwordx4 v21, s[40:43], s48 offen lds\n\t"
      "s_add_u32 m0, s49, 28672\n\ts_add_u32 s48, s50, 0x40000\n\tbuffer_load_dwordx4 v20, s[40:43], s48 offen lds\n\ts_add_u32 m0, s49, 29696\n\ts_add_u32 s48, s50, 0x50000\n\t"
      "buffer_load_dwordx4 v21, s[40:43], s48 offen lds\n\ts_add_u32 m0, s49, 30720\n\ts_add_u32 s48, s50, 0x60000\n\tbuffer_load_dwordx4 v20, s[40:43], s48 offen lds\n\t"
      "s_add_u32 m0, s49, 31744\n\ts_add_u32 s48, s50, 0x70000\n\tbuffer_load_dwordx4 v21, s[40:43], s48 offen lds\n\t"
      :: "{v[20:21]}"(v2u{k[20], k[21]}), "{s[40:43]}"(mkrs(W, 1024 * 4096 * 2)), "{s49}"(WL + w * 32768), "{s56}"(w << 21), "{s[64:67]}"(v4u{ROTS(h >> 3)})
      : "memory", "scc", "s48", "s50");
  claim(c, 0);
  constexpr float SC = 0.08838834764831845f, CE = 1.4426950408889634f * SC;
  v32u s0 = {RSRCS, (unsigned)w, ~0u, c.budget, 0, CQ0 * 16, 0, (unsigned)(R_QX + hblock(h) + 15 * 16384), C2, 0, 0, 0, 0, fu(CE), fu(CE), fu(-1e9f / SC), (unsigned)(R_KX + hblock(h))};
#pragma unroll
  for (int i = 0; i < 4; ++i) { const unsigned long long m = __ballot(4 * g + i <= cc); s0[24 + 2 * i] = (unsigned)m; s0[25 + 2 * i] = (unsigned)(m >> 32); }
  v32u s1 = {plo(W), phi(W), 1024 * 4096 * 2, 0x00020000, 0, (unsigned)(C_FLAG + (F_Q + h) * 128), 0, 0, 0, (unsigned)(WL + w * 32768), 0, 0, 0, 0, 0, 0,
             (unsigned)(w << 21), (unsigned)(((w + 1) & 3) << 21), (unsigned)(((w + 2) & 3) << 21), (unsigned)(((w + 3) & 3) << 21), plo((const void*)p), phi((const void*)p),
             c.att ? 0u : (unsigned)c.b + 256u, (unsigned)(C_VERDICT + c.att * 128), ROTS(h >> 3)};
  v32u wa[8], wv[5];
  asm volatile(
      "s_nop 0\n\tv_readfirstlane_b32 s8, v160\n\tv_readfirstlane_b32 s9, v161\n\tv_readfirstlane_b32 s10, v162\n\tv_readfirstlane_b32 s11, v163\n\t"
      "v_readfirstlane_b32 s12, v164\n\tv_readfirstlane_b32 s13, v165\n\tv_readfirstlane_b32 s14, v166\n\tv_readfirstlane_b32 s15, v167\n\tv_readfirstlane_b32 s16, v168\n\t"
      "v_readfirstlane_b32 s17, v169\n\tv_readfirstlane_b32 s18, v170\n\tv_readfirstlane_b32 s19, v171\n\tv_readfirstlane_b32 s20, v172\n\tv_readfirstlane_b32 s21, v173\n\t"
      "v_readfirstlane_b32 s22, v174\n\tv_readfirstlane_b32 s23, v175\n\tv_readfirstlane_b32 s24, v176\n\tv_readfirstlane_b32 s25, v177\n\tv_readfirstlane_b32 s26, v178\n\t"
      "v_readfirstlane_b32 s27, v179\n\tv_readfirstlane_b32 s28, v180\n\tv_readfirstlane_b32 s29, v181\n\tv_readfirstlane_b32 s30, v182\n\tv_readfirstlane_b32 s31, v183\n\t"
      "v_readfirstlane_b32 s32, v184\n\tv_readfirstlane_b32 s33, v185\n\tv_readfirstlane_b32 s34, v186\n\tv_readfirstlane_b32 s35, v187\n\tv_readfirstlane_b32 s36, v188\n\t"
      "v_readfirstlane_b32 s37, v189\n\tv_readfirstlane_b32 s38, v190\n\tv_readfirstlane_b32 s39, v191\n\tv_readfirstlane_b32 s40, v192\n\tv_readfirstlane_b32 s41, v193\n\t"
      "v_readfirstlane_b32 s42, v194\n\tv_readfirstlane_b32 s43, v195\n\tv_readfirstlane_b32 s45, v197\n\tv_readfirstlane_b32 s49, v201\n\tv_readfirstlane_b32 s54, v206\n\t"
      "v_readfirstlane_b32 s55, v207\n\tv_readfirstlane_b32 s56, v208\n\tv_readfirstlane_b32 s57, v209\n\tv_readfirstlane_b32 s58, v210\n\tv_readfirstlane_b32 s59, v211\n\t"
      "v_readfirstlane_b32 s60, v212\n\tv_readfirstlane_b32 s61, v213\n\tv_readfirstlane_b32 s62, v214\n\tv_readfirstlane_b32 s63, v215\n\tv_readfirstlane_b32 s64, v216\n\t"
      "v_readfirstlane_b32 s65, v217\n\tv_readfirstlane_b32 s66, v218\n\tv_readfirstlane_b32 s67, v219\n\ts_nop 4\n\t"
      TKS("s_mul_i32 s48, s16, 72\n\ts_add_u32 s48, s48, 162080\n\tv_mov_b32 v224, s48\n\tv_mov_b32 v225, s72\n\tds_write_b32 v224, v225 offset:0\n\tv_mov_b32 v225, s73\n\tds_write_b32 v224, v225 offset:4\n\tv_mov_b32 v225, s74\n\tds_write_b32 v224, v225 offset:8\n\tv_mov_b32 v225, s75\n\tds_write_b32 v224, v225 offset:12\n\tv_mov_b32 v225, s76\n\tds_write_b32 v224, v225 offset:16\n\tv_mov_b32 v225, s77\n\tds_write_b32 v224, v225 offset:20\n\tv_mov_b32 v225, s78\n\tds_write_b32 v224, v225 offset:24\n\tv_mov_b32 v225, s79\n\tds_write_b32 v224, v225 offset:28\n\tv_mov_b32 v225, s80\n\tds_write_b32 v224, v225 offset:32\n\tv_mov_b32 v225, s81\n\tds_write_b32 v224, v225 offset:36\n\tv_mov_b32 v225, s82\n\tds_write_b32 v224, v225 offset:40\n\tv_mov_b32 v225, s83\n\tds_write_b32 v224, v225 offset:44\n\tv_mov_b32 v225, s84\n\tds_write_b32 v224, v225 offset:48\n\tv_mov_b32 v225, s85\n\tds_write_b32 v224, v225 offset:52\n\tv_mov_b32 v225, s86\n\tds_write_b32 v224, v225 offset:56\n\tv_mov_b32 v225, s87\n\tds_write_b32 v224, v225 offset:60\n\tv_mov_b32 v225, s88\n\tds_write_b32 v224, v225 offset:64\n\tv_mov_b32 v225, s89\n\tds_write_b32 v224, v225 offset:68\n\t")
      "s_waitcnt vmcnt(24)\n\tds_read_b64_tr_b16 a[0:1], v22 offset:0\n\tds_read_b64_tr_b16 a[2:3], v22 offset:256\n\tds_read_b64_tr_b16 a[4:5], v22 offset:4096\n\t"
      "ds_read_b64_tr_b16 a[6:7], v22 offset:4352\n\tds_read_b64_tr_b16 a[8:9], v23 offset:0\n\tds_read_b64_tr_b16 a[10:11], v23 offset:256\n\t"
      "ds_read_b64_tr_b16 a[12:13], v23 offset:4096\n\tds_read_b64_tr_b16 a[14:15], v23 offset:4352\n\tds_read_b64_tr_b16 v[32:33], v24 offset:0\n\t"
      "ds_read_b64_tr_b16 v[34:35], v24 offset:256\n\tds_read_b64_tr_b16 v[36:37], v24 offset:4096\n\tds_read_b64_tr_b16 v[38:39], v24 offset:4352\n\t"
      "ds_read_b64_tr_b16 v[152:153], v25 offset:0\n\tds_read_b64_tr_b16 v[154:155], v25 offset:256\n\tds_read_b64_tr_b16 v[156:157], v25 offset:4096\n\t"
      "ds_read_b64_tr_b16 v[158:159], v25 offset:4352\n\ts_waitcnt lgkmcnt(0)\n\ts_add_u32 s50, s57, s64\n\ts_add_u32 m0, s49, 0\n\ts_add_u32 s48, s50, 0x0\n\t"
      "buffer_load_dwordx4 v20, s[40:43], s48 offen lds\n\ts_add_u32 m0, s49, 1024\n\ts_add_u32 s48, s50, 0x10000\n\tbuffer_load_dwordx4 v21, s[40:43], s48 offen lds\n\t"
      "s_add_u32 m0, s49, 2048\n\ts_add_u32 s48, s50, 0x20000\n\tbuffer_load_dwordx4 v20, s[40:43], s48 offen lds\n\ts_add_u32 m0, s49, 3072\n\ts_add_u32 s48, s50, 0x30000\n\t"
      "buffer_load_dwordx4 v21, s[40:43], s48 offen lds\n\ts_add_u32 m0, s49, 4096\n\ts_add_u32 s48, s50, 0x40000\n\tbuffer_load_dwordx4 v20, s[40:43], s48 offen lds\n\t"
      "s_add_u32 m0, s49, 5120\n\ts_add_u32 s48, s50, 0x50000\n\tbuffer_load_dwordx4 v21, s[40:43], s48 offen lds\n\ts_add_u32 m0, s49, 6144\n\ts_add_u32 s48, s50, 0x60000\n\t"
      "buffer_load_dwordx4 v20, s[40:43], s48 offen lds\n\ts_add_u32 m0, s49, 7168\n\ts_add_u32 s48, s50, 0x70000\n\tbuffer_load_dwordx4 v21, s[40:43], s48 offen lds\n\t"
      "s_waitcnt vmcnt(24)\n\tds_read_b64_tr_b16 a[16:17], v22 offset:8192\n\tds_read_b64_tr_b16 a[18:19], v22 offset:8448\n\tds_read_b64_tr_b16 a[20:21], v22 offset:12288\n\t"
      "ds_read_b64_tr_b16 a[22:23], v22 offset:12544\n\tds_read_b64_tr_b16 a[24:25], v23 offset:8192\n\tds_read_b64_tr_b16 a[26:27], v23 offset:8448\n\t"
      "ds_read_b64_tr_b16 a[28:29], v23 offset:12288\n\tds_read_b64_tr_b16 a[30:31], v23 offset:12544\n\tds_read_b64_tr_b16 v[40:41], v24 offset:8192\n\t"
      "ds_read_b64_tr_b16 v[42:43], v24 offset:8448\n\tds_read_b64_tr_b16 v[44:45], v24 offset:12288\n\tds_read_b64_tr_b16 v[46:47], v24 offset:12544\n\t"
      "ds_read_b64_tr_b16 v[160:161], v25 offset:8192\n\tds_read_b64_tr_b16 v[162:163], v25 offset:8448\n\tds_read_b64_tr_b16 v[164:165], v25 offset:12288\n\t"
      "ds_read_b64_tr_b16 v[166:167], v25 offset:12544\n\ts_waitcnt lgkmcnt(0)\n\ts_add_u32 s50, s58, s64\n\ts_add_u32 m0, s49, 8192\n\ts_add_u32 s48, s50, 0x0\n\t"
      "buffer_load_dwordx4 v20, s[40:43], s48 offen lds\n\ts_add_u32 m0, s49, 9216\n\ts_add_u32 s48, s50, 0x10000\n\tbuffer_load_dwordx4 v21, s[40:43], s48 offen lds\n\t"
      "s_add_u32 m0, s49, 10240\n\ts_add_u32 s48, s50, 0x20000\n\tbuffer_load_dwordx4 v20, s[40:43], s48 offen lds\n\ts_add_u32 m0, s49, 11264\n\ts_add_u32 s48, s50, 0x30000\n\t"
      "buffer_load_dwordx4 v21, s[40:43], s48 offen lds\n\ts_add_u32 m0, s49, 12288\n\ts_add_u32 s48, s50, 0x40000\n\tbuffer_load_dwordx4 v20, s[40:43], s48 offen lds\n\t"
      "s_add_u32 m0, s49, 13312\n\ts_add_u32 s48, s50, 0x50000\n\tbuffer_load_dwordx4 v21, s[40:43], s48 offen lds\n\ts_add_u32 m0, s49, 14336\n\ts_add_u32 s48, s50, 0x60000\n\t"
      "buffer_load_dwordx4 v20, s[40:43], s48 offen lds\n\ts_add_u32 m0, s49, 15360\n\ts_add_u32 s48, s50, 0x70000\n\tbuffer_load_dwordx4 v21, s[40:43], s48 offen lds\n\t"
      "s_waitcnt vmcnt(24)\n\tds_read_b64_tr_b16 a[32:33], v22 offset:16384\n\tds_read_b64_tr_b16 a[34:35], v22 offset:16640\n\tds_read_b64_tr_b16 a[36:37], v22 offset:20480\n\t"
      "ds_read_b64_tr_b16 a[38:39], v22 offset:20736\n\tds_read_b64_tr_b16 a[40:41], v23 offset:16384\n\tds_read_b64_tr_b16 a[42:43], v23 offset:16640\n\t"
      "ds_read_b64_tr_b16 a[44:45], v23 offset:20480\n\tds_read_b64_tr_b16 a[46:47], v23 offset:20736\n\tds_read_b64_tr_b16 v[48:49], v24 offset:16384\n\t"
      "ds_read_b64_tr_b16 v[50:51], v24 offset:16640\n\tds_read_b64_tr_b16 v[52:53], v24 offset:20480\n\tds_read_b64_tr_b16 v[54:55], v24 offset:20736\n\t"
      "ds_read_b64_tr_b16 v[168:169], v25 offset:16384\n\tds_read_b64_tr_b16 v[170:171], v25 offset:16640\n\tds_read_b64_tr_b16 v[172:173], v25 offset:20480\n\t"
      "ds_read_b64_tr_b16 v[174:175], v25 offset:20736\n\ts_waitcnt lgkmcnt(0)\n\ts_add_u32 s50, s59, s64\n\ts_add_u32 m0, s49, 16384\n\ts_add_u32 s48, s50, 0x0\n\t"
      "buffer_load_dwordx4 v20, s[40:43], s48 offen lds\n\ts_add_u32 m0, s49, 17408\n\ts_add_u32 s48, s50, 0x10000\n\tbuffer_load_dwordx4 v21, s[40:43], s48 offen lds\n\t"
      "s_add_u32 m0, s49, 18432\n\ts_add_u32 s48, s50, 0x20000\n\tbuffer_load_dwordx4 v20, s[40:43], s48 offen lds\n\ts_add_u32 m0, s49, 19456\n\ts_add_u32 s48, s50, 0x30000\n\t"
      "buffer_load_dwordx4 v21, s[40:43], s48 offen lds\n\ts_add_u32 m0, s49, 20480\n\ts_add_u32 s48, s50, 0x40000\n\tbuffer_load_dwordx4 v20, s[40:43], s48 offen lds\n\t"
      "s_add_u32 m0, s49, 21504\n\ts_add_u32 s48, s50, 0x50000\n\tbuffer_load_dwordx4 v21, s[40:43], s48 offen lds\n\ts_add_u32 m0, s49, 22528\n\ts_add_u32 s48, s50, 0x60000\n\t"
      "buffer_load_dwordx4 v20, s[40:43], s48 offen lds\n\ts_add_u32 m0, s49, 23552\n\ts_add_u32 s48, s50, 0x70000\n\tbuffer_load_dwordx4 v21, s[40:43], s48 offen lds\n\t"
      "s_waitcnt vmcnt(24)\n\tds_read_b64_tr_b16 a[48:49], v22 offset:24576\n\tds_read_b64_tr_b16 a[50:51], v22 offset:24832\n\tds_read_b64_tr_b16 a[52:53], v22 offset:28672\n\t"
      "ds_read_b64_tr_b16 a[54:55], v22 offset:28928\n\tds_read_b64_tr_b16 a[56:57], v23 offset:24576\n\tds_read_b64_tr_b16 a[58:59], v23 offset:24832\n\t"
      "ds_read_b64_tr_b16 a[60:61], v23 offset:28672\n\tds_read_b64_tr_b16 a[62:63], v23 offset:28928\n\tds_read_b64_tr_b16 v[56:57], v24 offset:24576\n\t"
      "ds_read_b64_tr_b16 v[58:59], v24 offset:24832\n\tds_read_b64_tr_b16 v[60:61], v24 offset:28672\n\tds_read_b64_tr_b16 v[62:63], v24 offset:28928\n\t"
      "ds_read_b64_tr_b16 v[176:177], v25 offset:24576\n\tds_read_b64_tr_b16 v[178:179], v25 offset:24832\n\tds_read_b64_tr_b16 v[180:181], v25 offset:28672\n\t"
      "ds_read_b64_tr_b16 v[182:183], v25 offset:28928\n\ts_waitcnt lgkmcnt(0)\n\ts_add_u32 s50, s57, s65\n\ts_add_u32 m0, s49, 24576\n\ts_add_u32 s48, s50, 0x0\n\t"
      "buffer_load_dwordx4 v20, s[40:43], s48 offen lds\n\ts_add_u32 m0, s49, 25600\n\ts_add_u32 s48, s50, 0x10000\n\tbuffer_load_dwordx4 v21, s[40:43], s48 offen lds\n\t"
      "s_add_u32 m0, s49, 26624\n\ts_add_u32 s48, s50, 0x20000\n\tbuffer_load_dwordx4 v20, s[40:43], s48 offen lds\n\ts_add_u32 m0, s49, 27648\n\ts_add_u32 s48, s50, 0x30000\n\t"
      "buffer_load_dwordx4 v21, s[40:43], s48 offen lds\n\ts_add_u32 m0, s49, 28672\n\ts_add_u32 s48, s50, 0x40000\n\tbuffer_load_dwordx4 v20, s[40:43], s48 offen lds\n\t"
      "s_add_u32 m0, s49, 29696\n\ts_add_u32 s48, s50, 0x50000\n\tbuffer_load_dwordx4 v21, s[40:43], s48 offen lds\n\ts_add_u32 m0, s49, 30720\n\ts_add_u32 s48, s50, 0x60000\n\t"
      "buffer_load_dwordx4 v20, s[40:43], s48 offen lds\n\ts_add_u32 m0, s49, 31744\n\ts_add_u32 s48, s50, 0x70000\n\tbuffer_load_dwordx4 v21, s[40:43], s48 offen lds\n\t"
      "s_waitcnt vmcnt(24)\n\tds_read_b64_tr_b16 a[64:65], v22 offset:0\n\tds_read_b64_tr_b16 a[66:67], v22 offset:256\n\tds_read_b64_tr_b16 a[68:69], v22 offset:4096\n\t"
      "ds_read_b64_tr_b16 a[70:71], v22 offset:4352\n\tds_read_b64_tr_b16 a[72:73], v23 offset:0\n\tds_read_b64_tr_b16 a[74:75], v23 offset:256\n\t"
      "ds_read_b64_tr_b16 a[76:77], v23 offset:4096\n\tds_read_b64_tr_b16 a[78:79], v23 offset:4352\n\tds_read_b64_tr_b16 v[64:65], v24 offset:0\n\t"
      "ds_read_b64_tr_b16 v[66:67], v24 offset:256\n\tds_read_b64_tr_b16 v[68:69], v24 offset:4096\n\tds_read_b64_tr_b16 v[70:71], v24 offset:4352\n\t"
      "ds_read_b64_tr_b16 v[184:185], v25 offset:0\n\tds_read_b64_tr_b16 v[186:187], v25 offset:256\n\tds_read_b64_tr_b16 v[188:189], v25 offset:4096\n\t"
      "ds_read_b64_tr_b16 v[190:191], v25 offset:4352\n\ts_waitcnt lgkmcnt(0)\n\ts_add_u32 s50, s58, s65\n\ts_add_u32 m0, s49, 0\n\ts_add_u32 s48, s50, 0x0\n\t"
      "buffer_load_dwordx4 v20, s[40:43], s48 offen lds\n\ts_add_u32 m0, s49, 1024\n\ts_add_u32 s48, s50, 0x10000\n\tbuffer_load_dwordx4 v21, s[40:43], s48 offen lds\n\t"
      "s_add_u32 m0, s49, 2048\n\ts_add_u32 s48, s50, 0x20000\n\tbuffer_load_dwordx4 v20, s[40:43], s48 offen lds\n\ts_add_u32 m0, s49, 3072\n\ts_add_u32 s48, s50, 0x30000\n\t"
      "buffer_load_dwordx4 v21, s[40:43], s48 offen lds\n\ts_add_u32 m0, s49, 4096\n\ts_add_u32 s48, s50, 0x40000\n\tbuffer_load_dwordx4 v20, s[40:43], s48 offen lds\n\t"
      "s_add_u32 m0, s49, 5120\n\ts_add_u32 s48, s50, 0x50000\n\tbuffer_load_dwordx4 v21, s[40:43], s48 offen lds\n\ts_add_u32 m0, s49, 6144\n\ts_add_u32 s48, s50, 0x60000\n\t"
      "buffer_load_dwordx4 v20, s[40:43], s48 offen lds\n\ts_add_u32 m0, s49, 7168\n\ts_add_u32 s48, s50, 0x70000\n\tbuffer_load_dwordx4 v21, s[40:43], s48 offen lds\n\t"
      "s_waitcnt vmcnt(24)\n\tds_read_b64_tr_b16 a[80:81], v22 offset:8192\n\tds_read_b64_tr_b16 a[82:83], v22 offset:8448\n\tds_read_b64_tr_b16 a[84:85], v22 offset:12288\n\t"
      "ds_read_b64_tr_b16 a[86:87], v22 offset:12544\n\tds_read_b64_tr_b16 a[88:89], v23 offset:8192\n\tds_read_b64_tr_b16 a[90:91], v23 offset:8448\n\t"
      "ds_read_b64_tr_b16 a[92:93], v23 offset:12288\n\tds_read_b64_tr_b16 a[94:95], v23 offset:12544\n\tds_read_b64_tr_b16 v[72:73], v24 offset:8192\n\t"
      "ds_read_b64_tr_b16 v[74:75], v24 offset:8448\n\tds_read_b64_tr_b16 v[76:77], v24 offset:12288\n\tds_read_b64_tr_b16 v[78:79], v24 offset:12544\n\t"
      "ds_read_b64_tr_b16 v[192:193], v25 offset:8192\n\tds_read_b64_tr_b16 v[194:195], v25 offset:8448\n\tds_read_b64_tr_b16 v[196:197], v25 offset:12288\n\t"
      "ds_read_b64_tr_b16 v[198:199], v25 offset:12544\n\ts_waitcnt lgkmcnt(0)\n\ts_add_u32 s50, s59, s65\n\ts_add_u32 m0, s49, 8192\n\ts_add_u32 s48, s50, 0x0\n\t"
      "buffer_load_dwordx4 v20, s[40:43], s48 offen lds\n\ts_add_u32 m0, s49, 9216\n\ts_add_u32 s48, s50, 0x10000\n\tbuffer_load_dwordx4 v21, s[40:43], s48 offen lds\n\t"
      "s_add_u32 m0, s49, 10240\n\ts_add_u32 s48, s50, 0x20000\n\tbuffer_load_dwordx4 v20, s[40:43], s48 offen lds\n\ts_add_u32 m0, s49, 11264\n\ts_add_u32 s48, s50, 0x30000\n\t"
      "buffer_load_dwordx4 v21, s[40:43], s48 offen lds\n\ts_add_u32 m0, s49, 12288\n\ts_add_u32 s48, s50, 0x40000\n\tbuffer_load_dwordx4 v20, s[40:43], s48 offen lds\n\t"
      "s_add_u32 m0, s49, 13312\n\ts_add_u32 s48, s50, 0x50000\n\tbuffer_load_dwordx4 v21, s[40:43], s48 offen lds\n\ts_add_u32 m0, s49, 14336\n\ts_add_u32 s48, s50, 0x60000\n\t"
      "buffer_load_dwordx4 v20, s[40:43], s48 offen lds\n\ts_add_u32 m0, s49, 15360\n\ts_add_u32 s48, s50, 0x70000\n\tbuffer_load_dwordx4 v21, s[40:43], s48 offen lds\n\t"
      "s_waitcnt vmcnt(24)\n\tds_read_b64_tr_b16 a[96:97], v22 offset:16384\n\tds_read_b64_tr_b16 a[98:99], v22 offset:16640\n\t"
      "ds_read_b64_tr_b16 a[100:101], v22 offset:20480\n\tds_read_b64_tr_b16 a[102:103], v22 offset:20736\n\tds_read_b64_tr_b16 a[104:105], v23 offset:16384\n\t"
      "ds_read_b64_tr_b16 a[106:107], v23 offset:16640\n\tds_read_b64_tr_b16 a[108:109], v23 offset:20480\n\tds_read_b64_tr_b16 a[110:111], v23 offset:20736\n\t"
      "ds_read_b64_tr_b16 v[80:81], v24 offset:16384\n\tds_read_b64_tr_b16 v[82:83], v24 offset:16640\n\tds_read_b64_tr_b16 v[84:85], v24 offset:20480\n\t"
      "ds_read_b64_tr_b16 v[86:87], v24 offset:20736\n\tds_read_b64_tr_b16 v[200:201], v25 offset:16384\n\tds_read_b64_tr_b16 v[202:203], v25 offset:16640\n\t"
      "ds_read_b64_tr_b16 v[204:205], v25 offset:20480\n\tds_read_b64_tr_b16 v[206:207], v25 offset:20736\n\ts_waitcnt lgkmcnt(0)\n\ts_add_u32 s50, s57, s66\n\t"
      "s_add_u32 m0, s49, 16384\n\ts_add_u32 s48, s50, 0x0\n\tbuffer_load_dwordx4 v20, s[40:43], s48 offen lds\n\ts_add_u32 m0, s49, 17408\n\ts_add_u32 s48, s50, 0x10000\n\t"
      "buffer_load_dwordx4 v21, s[40:43], s48 offen lds\n\ts_add_u32 m0, s49, 18432\n\ts_add_u32 s48, s50, 0x20000\n\tbuffer_load_dwordx4 v20, s[40:43], s48 offen lds\n\t"
      "s_add_u32 m0, s49, 19456\n\ts_add_u32 s48, s50, 0x30000\n\tbuffer_load_dwordx4 v21, s[40:43], s48 offen lds\n\ts_add_u32 m0, s49, 20480\n\ts_add_u32 s48, s50, 0x40000\n\t"
      "buffer_load_dwordx4 v20, s[40:43], s48 offen lds\n\ts_add_u32 m0, s49, 21504\n\ts_add_u32 s48, s50, 0x50000\n\tbuffer_load_dwordx4 v21, s[40:43], s48 offen lds\n\t"
      "s_add_u32 m0, s49, 22528\n\ts_add_u32 s48, s50, 0x60000\n\tbuffer_load_dwordx4 v20, s[40:43], s48 offen lds\n\ts_add_u32 m0, s49, 23552\n\ts_add_u32 s48, s50, 0x70000\n\t"
      "buffer_load_dwordx4 v21, s[40:43], s48 offen lds\n\ts_waitcnt vmcnt(24)\n\tds_read_b64_tr_b16 a[112:113], v22 offset:24576\n\t"
      "ds_read_b64_tr_b16 a[114:115], v22 offset:24832\n\tds_read_b64_tr_b16 a[116:117], v22 offset:28672\n\tds_read_b64_tr_b16 a[118:119], v22 offset:28928\n\t"
      "ds_read_b64_tr_b16 a[120:121], v23 offset:24576\n\tds_read_b64_tr_b16 a[122:123], v23 offset:24832\n\tds_read_b64_tr_b16 a[124:125], v23 offset:28672\n\t"
      "ds_read_b64_tr_b16 a[126:127], v23 offset:28928\n\tds_read_b64_tr_b16 v[88:89], v24 offset:24576\n\tds_read_b64_tr_b16 v[90:91], v24 offset:24832\n\t"
      "ds_read_b64_tr_b16 v[92:93], v24 offset:28672\n\tds_read_b64_tr_b16 v[94:95], v24 offset:28928\n\tds_read_b64_tr_b16 v[208:209], v25 offset:24576\n\t"
      "ds_read_b64_tr_b16 v[210:211], v25 offset:24832\n\tds_read_b64_tr_b16 v[212:213], v25 offset:28672\n\tds_read_b64_tr_b16 v[214:215], v25 offset:28928\n\t"
      "s_waitcnt lgkmcnt(0)\n\ts_add_u32 s50, s58, s66\n\ts_add_u32 m0, s49, 24576\n\ts_add_u32 s48, s50, 0x0\n\tbuffer_load_dwordx4 v20, s[40:43], s48 offen lds\n\t"
      "s_add_u32 m0, s49, 25600\n\ts_add_u32 s48, s50, 0x10000\n\tbuffer_load_dwordx4 v21, s[40:43], s48 offen lds\n\ts_add_u32 m0, s49, 26624\n\ts_add_u32 s48, s50, 0x20000\n\t"
      "buffer_load_dwordx4 v20, s[40:43], s48 offen lds\n\ts_add_u32 m0, s49, 27648\n\ts_add_u32 s48, s50, 0x30000\n\tbuffer_load_dwordx4 v21, s[40:43], s48 offen lds\n\t"
      "s_add_u32 m0, s49, 28672\n\ts_add_u32 s48, s50, 0x40000\n\tbuffer_load_dwordx4 v20, s[40:43], s48 offen lds\n\ts_add_u32 m0, s49, 29696\n\ts_add_u32 s48, s50, 0x50000\n\t"
      "buffer_load_dwordx4 v21, s[40:43], s48 offen lds\n\ts_add_u32 m0, s49, 30720\n\ts_add_u32 s48, s50, 0x60000\n\tbuffer_load_dwordx4 v20, s[40:43], s48 offen lds\n\t"
      "s_add_u32 m0, s49, 31744\n\ts_add_u32 s48, s50, 0x70000\n\tbuffer_load_dwordx4 v21, s[40:43], s48 offen lds\n\ts_waitcnt vmcnt(24)\n\t"
      "ds_read_b64_tr_b16 a[128:129], v22 offset:0\n\tds_read_b64_tr_b16 a[130:131], v22 offset:256\n\tds_read_b64_tr_b16 a[132:133], v22 offset:4096\n\t"
      "ds_read_b64_tr_b16 a[134:135], v22 offset:4352\n\tds_read_b64_tr_b16 a[136:137], v23 offset:0\n\tds_read_b64_tr_b16 a[138:139], v23 offset:256\n\t"
      "ds_read_b64_tr_b16 a[140:141], v23 offset:4096\n\tds_read_b64_tr_b16 a[142:143], v23 offset:4352\n\tds_read_b64_tr_b16 v[96:97], v24 offset:0\n\t"
      "ds_read_b64_tr_b16 v[98:99], v24 offset:256\n\tds_read_b64_tr_b16 v[100:101], v24 offset:4096\n\tds_read_b64_tr_b16 v[102:103], v24 offset:4352\n\t"
      "ds_read_b64_tr_b16 v[216:217], v25 offset:0\n\tds_read_b64_tr_b16 v[218:219], v25 offset:256\n\tds_read_b64_tr_b16 v[220:221], v25 offset:4096\n\t"
      "ds_read_b64_tr_b16 v[222:223], v25 offset:4352\n\ts_waitcnt lgkmcnt(0)\n\ts_add_u32 s50, s59, s66\n\ts_add_u32 m0, s49, 0\n\ts_add_u32 s48, s50, 0x0\n\t"
      "buffer_load_dwordx4 v20, s[40:43], s48 offen lds\n\ts_add_u32 m0, s49, 1024\n\ts_add_u32 s48, s50, 0x10000\n\tbuffer_load_dwordx4 v21, s[40:43], s48 offen lds\n\t"
      "s_add_u32 m0, s49, 2048\n\ts_add_u32 s48, s50, 0x20000\n\tbuffer_load_dwordx4 v20, s[40:43], s48 offen lds\n\ts_add_u32 m0, s49, 3072\n\ts_add_u32 s48, s50, 0x30000\n\t"
      "buffer_load_dwordx4 v21, s[40:43], s48 offen lds\n\ts_add_u32 m0, s49, 4096\n\ts_add_u32 s48, s50, 0x40000\n\tbuffer_load_dwordx4 v20, s[40:43], s48 offen lds\n\t"
      "s_add_u32 m0, s49, 5120\n\ts_add_u32 s48, s50, 0x50000\n\tbuffer_load_dwordx4 v21, s[40:43], s48 offen lds\n\ts_add_u32 m0, s49, 6144\n\ts_add_u32 s48, s50, 0x60000\n\t"
      "buffer_load_dwordx4 v20, s[40:43], s48 offen lds\n\ts_add_u32 m0, s49, 7168\n\ts_add_u32 s48, s50, 0x70000\n\tbuffer_load_dwordx4 v21, s[40:43], s48 offen lds\n\t"
      "s_waitcnt vmcnt(24)\n\tds_read_b64_tr_b16 a[144:145], v22 offset:8192\n\tds_read_b64_tr_b16 a[146:147], v22 offset:8448\n\t"
      "ds_read_b64_tr_b16 a[148:149], v22 offset:12288\n\tds_read_b64_tr_b16 a[150:151], v22 offset:12544\n\tds_read_b64_tr_b16 a[152:153], v23 offset:8192\n\t"
      "ds_read_b64_tr_b16 a[154:155], v23 offset:8448\n\tds_read_b64_tr_b16 a[156:157], v23 offset:12288\n\tds_read_b64_tr_b16 a[158:159], v23 offset:12544\n\t"
      "ds_read_b64_tr_b16 v[104:105], v24 offset:8192\n\tds_read_b64_tr_b16 v[106:107], v24 offset:8448\n\tds_read_b64_tr_b16 v[108:109], v24 offset:12288\n\t"
      "ds_read_b64_tr_b16 v[110:111], v24 offset:12544\n\tds_read_b64_tr_b16 v[224:225], v25 offset:8192\n\tds_read_b64_tr_b16 v[226:227], v25 offset:8448\n\t"
      "ds_read_b64_tr_b16 v[228:229], v25 offset:12288\n\tds_read_b64_tr_b16 v[230:231], v25 offset:12544\n\ts_waitcnt lgkmcnt(0)\n\ts_add_u32 s50, s57, s67\n\t"
      "s_add_u32 m0, s49, 8192\n\ts_add_u32 s48, s50, 0x0\n\tbuffer_load_dwordx4 v20, s[40:43], s48 offen lds\n\ts_add_u32 m0, s49, 9216\n\ts_add_u32 s48, s50, 0x10000\n\t"
      "buffer_load_dwordx4 v21, s[40:43], s48 offen lds\n\ts_add_u32 m0, s49, 10240\n\ts_add_u32 s48, s50, 0x20000\n\tbuffer_load_dwordx4 v20, s[40:43], s48 offen lds\n\t"
      "s_add_u32 m0, s49, 11264\n\ts_add_u32 s48, s50, 0x30000\n\tbuffer_load_dwordx4 v21, s[40:43], s48 offen lds\n\ts_add_u32 m0, s49, 12288\n\ts_add_u32 s48, s50, 0x40000\n\t"
      "buffer_load_dwordx4 v20, s[40:43], s48 offen lds\n\ts_add_u32 m0, s49, 13312\n\ts_add_u32 s48, s50, 0x50000\n\tbuffer_load_dwordx4 v21, s[40:43], s48 offen lds\n\t"
      "s_add_u32 m0, s49, 14336\n\ts_add_u32 s48, s50, 0x60000\n\tbuffer_load_dwordx4 v20, s[40:43], s48 offen lds\n\ts_add_u32 m0, s49, 15360\n\ts_add_u32 s48, s50, 0x70000\n\t"
      "buffer_load_dwordx4 v21, s[40:43], s48 offen lds\n\ts_waitcnt vmcnt(24)\n\tds_read_b64_tr_b16 a[160:161], v22 offset:16384\n\t"
      "ds_read_b64_tr_b16 a[162:163], v22 offset:16640\n\tds_read_b64_tr_b16 a[164:165], v22 offset:20480\n\tds_read_b64_tr_b16 a[166:167], v22 offset:20736\n\t"
      "ds_read_b64_tr_b16 a[168:169], v23 offset:16384\n\tds_read_b64_tr_b16 a[170:171], v23 offset:16640\n\tds_read_b64_tr_b16 a[172:173], v23 offset:20480\n\t"
      "ds_read_b64_tr_b16 a[174:175], v23 offset:20736\n\tds_read_b64_tr_b16 v[112:113], v24 offset:16384\n\tds_read_b64_tr_b16 v[114:115], v24 offset:16640\n\t"
      "ds_read_b64_tr_b16 v[116:117], v24 offset:20480\n\tds_read_b64_tr_b16 v[118:119], v24 offset:20736\n\tds_read_b64_tr_b16 v[232:233], v25 offset:16384\n\t"
      "ds_read_b64_tr_b16 v[234:235], v25 offset:16640\n\tds_read_b64_tr_b16 v[236:237], v25 offset:20480\n\tds_read_b64_tr_b16 v[238:239], v25 offset:20736\n\t"
      "s_waitcnt lgkmcnt(0)\n\ts_add_u32 s50, s58, s67\n\ts_add_u32 m0, s49, 16384\n\ts_add_u32 s48, s50, 0x0\n\tbuffer_load_dwordx4 v20, s[40:43], s48 offen lds\n\t"
      "s_add_u32 m0, s49, 17408\n\ts_add_u32 s48, s50, 0x10000\n\tbuffer_load_dwordx4 v21, s[40:43], s48 offen lds\n\ts_add_u32 m0, s49, 18432\n\ts_add_u32 s48, s50, 0x20000\n\t"
      "buffer_load_dwordx4 v20, s[40:43], s48 offen lds\n\ts_add_u32 m0, s49, 19456\n\ts_add_u32 s48, s50, 0x30000\n\tbuffer_load_dwordx4 v21, s[40:43], s48 offen lds\n\t"
      "s_add_u32 m0, s49, 20480\n\ts_add_u32 s48, s50, 0x40000\n\tbuffer_load_dwordx4 v20, s[40:43], s48 offen lds\n\ts_add_u32 m0, s49, 21504\n\ts_add_u32 s48, s50, 0x50000\n\t"
      "buffer_load_dwordx4 v21, s[40:43], s48 offen lds\n\ts_add_u32 m0, s49, 22528\n\ts_add_u32 s48, s50, 0x60000\n\tbuffer_load_dwordx4 v20, s[40:43], s48 offen lds\n\t"
      "s_add_u32 m0, s49, 23552\n\ts_add_u32 s48, s50, 0x70000\n\tbuffer_load_dwordx4 v21, s[40:43], s48 offen lds\n\ts_waitcnt vmcnt(24)\n\t"
      "ds_read_b64_tr_b16 a[176:177], v22 offset:24576\n\tds_read_b64_tr_b16 a[178:179], v22 offset:24832\n\tds_read_b64_tr_b16 a[180:181], v22 offset:28672\n\t"
      "ds_read_b64_tr_b16 a[182:183], v22 offset:28928\n\tds_read_b64_tr_b16 a[184:185], v23 offset:24576\n\tds_read_b64_tr_b16 a[186:187], v23 offset:24832\n\t"
      "ds_read_b64_tr_b16 a[188:189], v23 offset:28672\n\tds_read_b64_tr_b16 a[190:191], v23 offset:28928\n\tds_read_b64_tr_b16 v[120:121], v24 offset:24576\n\t"
      "ds_read_b64_tr_b16 v[122:123], v24 offset:24832\n\tds_read_b64_tr_b16 v[124:125], v24 offset:28672\n\tds_read_b64_tr_b16 v[126:127], v24 offset:28928\n\t"
      "ds_read_b64_tr_b16 v[240:241], v25 offset:24576\n\tds_read_b64_tr_b16 v[242:243], v25 offset:24832\n\tds_read_b64_tr_b16 v[244:245], v25 offset:28672\n\t"
      "ds_read_b64_tr_b16 v[246:247], v25 offset:28928\n\ts_waitcnt lgkmcnt(0)\n\ts_add_u32 s50, s59, s67\n\ts_add_u32 m0, s49, 24576\n\ts_add_u32 s48, s50, 0x0\n\t"
      "buffer_load_dwordx4 v20, s[40:43], s48 offen lds\n\ts_add_u32 m0, s49, 25600\n\ts_add_u32 s48, s50, 0x10000\n\tbuffer_load_dwordx4 v21, s[40:43], s48 offen lds\n\t"
      "s_add_u32 m0, s49, 26624\n\ts_add_u32 s48, s50, 0x20000\n\tbuffer_load_dwordx4 v20, s[40:43], s48 offen lds\n\ts_add_u32 m0, s49, 27648\n\ts_add_u32 s48, s50, 0x30000\n\t"
      "buffer_load_dwordx4 v21, s[40:43], s48 offen lds\n\ts_add_u32 m0, s49, 28672\n\ts_add_u32 s48, s50, 0x40000\n\tbuffer_load_dwordx4 v20, s[40:43], s48 offen lds\n\t"
      "s_add_u32 m0, s49, 29696\n\ts_add_u32 s48, s50, 0x50000\n\tbuffer_load_dwordx4 v21, s[40:43], s48 offen lds\n\ts_add_u32 m0, s49, 30720\n\ts_add_u32 s48, s50, 0x60000\n\t"
      "buffer_load_dwordx4 v20, s[40:43], s48 offen lds\n\ts_add_u32 m0, s49, 31744\n\ts_add_u32 s48, s50, 0x70000\n\tbuffer_load_dwordx4 v21, s[40:43], s48 offen lds\n\t"
      "s_waitcnt vmcnt(24)\n\tds_read_b64_tr_b16 a[192:193], v22 offset:0\n\tds_read_b64_tr_b16 a[194:195], v22 offset:256\n\tds_read_b64_tr_b16 a[196:197], v22 offset:4096\n\t"
      "ds_read_b64_tr_b16 a[198:199], v22 offset:4352\n\tds_read_b64_tr_b16 a[200:201], v23 offset:0\n\tds_read_b64_tr_b16 a[202:203], v23 offset:256\n\t"
      "ds_read_b64_tr_b16 a[204:205], v23 offset:4096\n\tds_read_b64_tr_b16 a[206:207], v23 offset:4352\n\tds_read_b64_tr_b16 v[128:129], v24 offset:0\n\t"
      "ds_read_b64_tr_b16 v[130:131], v24 offset:256\n\tds_read_b64_tr_b16 v[132:133], v24 offset:4096\n\tds_read_b64_tr_b16 v[134:135], v24 offset:4352\n\t"
      "ds_read_b64_tr_b16 v[248:249], v25 offset:0\n\tds_read_b64_tr_b16 v[250:251], v25 offset:256\n\tds_read_b64_tr_b16 v[252:253], v25 offset:4096\n\t"
      "ds_read_b64_tr_b16 v[254:255], v25 offset:4352\n\ts_waitcnt lgkmcnt(0)\n\tds_write_b128 v5, v[152:155] offset:0\n\tds_write_b128 v5, v[156:159] offset:1024\n\t"
      "ds_write_b128 v5, v[160:163] offset:2048\n\tds_write_b128 v5, v[164:167] offset:3072\n\tds_write_b128 v5, v[168:171] offset:4096\n\t"
      "ds_write_b128 v5, v[172:175] offset:5120\n\tds_write_b128 v5, v[176:179] offset:6144\n\tds_write_b128 v5, v[180:183] offset:7168\n\ts_waitcnt vmcnt(16)\n\t"
      "ds_read_b64_tr_b16 a[208:209], v22 offset:8192\n\tds_read_b64_tr_b16 a[210:211], v22 offset:8448\n\tds_read_b64_tr_b16 a[212:213], v22 offset:12288\n\t"
      "ds_read_b64_tr_b16 a[214:215], v22 offset:12544\n\tds_read_b64_tr_b16 a[216:217], v23 offset:8192\n\tds_read_b64_tr_b16 a[218:219], v23 offset:8448\n\t"
      "ds_read_b64_tr_b16 a[220:221], v23 offset:12288\n\tds_read_b64_tr_b16 a[222:223], v23 offset:12544\n\tds_read_b64_tr_b16 v[136:137], v24 offset:8192\n\t"
      "ds_read_b64_tr_b16 v[138:139], v24 offset:8448\n\tds_read_b64_tr_b16 v[140:141], v24 offset:12288\n\tds_read_b64_tr_b16 v[142:143], v24 offset:12544\n\t"
      "ds_read_b64_tr_b16 v[160:161], v25 offset:8192\n\tds_read_b64_tr_b16 v[162:163], v25 offset:8448\n\tds_read_b64_tr_b16 v[164:165], v25 offset:12288\n\t"
      "ds_read_b64_tr_b16 v[166:167], v25 offset:12544\n\ts_waitcnt lgkmcnt(0)\n\tds_write_b128 v5, v[184:187] offset:8192\n\tds_write_b128 v5, v[188:191] offset:9216\n\t"
      "ds_write_b128 v5, v[192:195] offset:10240\n\tds_write_b128 v5, v[196:199] offset:11264\n\tds_write_b128 v5, v[200:203] offset:12288\n\t"
      "ds_write_b128 v5, v[204:207] offset:13312\n\tds_write_b128 v5, v[208:211] offset:14336\n\tds_write_b128 v5, v[212:215] offset:15360\n\ts_waitcnt vmcnt(8)\n\t"
      "ds_read_b64_tr_b16 a[224:225], v22 offset:16384\n\tds_read_b64_tr_b16 a[226:227], v22 offset:16640\n\tds_read_b64_tr_b16 a[228:229], v22 offset:20480\n\t"
      "ds_read_b64_tr_b16 a[230:231], v22 offset:20736\n\tds_read_b64_tr_b16 a[232:233], v23 offset:16384\n\tds_read_b64_tr_b16 a[234:235], v23 offset:16640\n\t"
      "ds_read_b64_tr_b16 a[236:237], v23 offset:20480\n\tds_read_b64_tr_b16 a[238:239], v23 offset:20736\n\tds_read_b64_tr_b16 v[144:145], v24 offset:16384\n\t"
      "ds_read_b64_tr_b16 v[146:147], v24 offset:16640\n\tds_read_b64_tr_b16 v[148:149], v24 offset:20480\n\tds_read_b64_tr_b16 v[150:151], v24 offset:20736\n\t"
      "ds_read_b64_tr_b16 v[168:169], v25 offset:16384\n\tds_read_b64_tr_b16 v[170:171], v25 offset:16640\n\tds_read_b64_tr_b16 v[172:173], v25 offset:20480\n\t"
      "ds_read_b64_tr_b16 v[174:175], v25 offset:20736\n\ts_waitcnt lgkmcnt(0)\n\tds_write_b128 v5, v[216:219] offset:16384\n\tds_write_b128 v5, v[220:223] offset:17408\n\t"
      "ds_write_b128 v5, v[224:227] offset:18432\n\tds_write_b128 v5, v[228:231] offset:19456\n\tds_write_b128 v5, v[232:235] offset:20480\n\t"
      "ds_write_b128 v5, v[236:239] offset:21504\n\tds_write_b128 v5, v[240:243] offset:22528\n\tds_write_b128 v5, v[244:247] offset:23552\n\ts_waitcnt vmcnt(0)\n\t"
      "ds_read_b64_tr_b16 a[240:241], v22 offset:24576\n\tds_read_b64_tr_b16 a[242:243], v22 offset:24832\n\tds_read_b64_tr_b16 a[244:245], v22 offset:28672\n\t"
      "ds_read_b64_tr_b16 a[246:247], v22 offset:28928\n\tds_read_b64_tr_b16 a[248:249], v23 offset:24576\n\tds_read_b64_tr_b16 a[250:251], v23 offset:24832\n\t"
      "ds_read_b64_tr_b16 a[252:253], v23 offset:28672\n\tds_read_b64_tr_b16 a[254:255], v23 offset:28928\n\tds_read_b64_tr_b16 v[152:153], v24 offset:24576\n\t"
      "ds_read_b64_tr_b16 v[154:155], v24 offset:24832\n\tds_read_b64_tr_b16 v[156:157], v24 offset:28672\n\tds_read_b64_tr_b16 v[158:159], v24 offset:28928\n\t"
      "ds_read_b64_tr_b16 v[176:177], v25 offset:24576\n\tds_read_b64_tr_b16 v[178:179], v25 offset:24832\n\tds_read_b64_tr_b16 v[180:181], v25 offset:28672\n\t"
      "ds_read_b64_tr_b16 v[182:183], v25 offset:28928\n\ts_waitcnt lgkmcnt(0)\n\tds_write_b128 v5, v[248:251] offset:24576\n\tds_write_b128 v5, v[252:255] offset:25600\n\t"
      "ds_write_b128 v5, v[160:163] offset:26624\n\tds_write_b128 v5, v[164:167] offset:27648\n\tds_write_b128 v5, v[168:171] offset:28672\n\t"
      "ds_write_b128 v5, v[172:175] offset:29696\n\tds_write_b128 v5, v[176:179] offset:30720\n\tds_write_b128 v5, v[180:183] offset:31744\n\t"
      "v_mov_b64 v[30:31], 0\n\tv_mov_b32 v29, 160000\n\tds_write_b64 v29, v[30:31]\n\t"
      TKS("s_lshr_b32 s89, s62, 3\n\ts_and_b32 s89, s89, 3\n\t")
      "s_waitcnt lgkmcnt(0)\n\ts_barrier\n\t"
      STS("s_memrealtime vcc\n\ts_waitcnt lgkmcnt(0)\n\tv_mov_b32 v31, vcc_lo\n\tv_mov_b32 v30, 161024\n\tds_write_b32 v30, v31\n\t")
      "1:\n\tbuffer_load_dword v29, off, s[12:15], s63 sc1\n\ts_waitcnt vmcnt(0)\n\tv_readfirstlane_b32 s49, v29\n\ts_bitcmp1_b32 s49, 0\n\ts_cbranch_scc1 2f\n\t"
      "s_cmp_eq_u32 s18, 0\n\ts_cbranch_scc1 2f\n\ts_sub_u32 s18, s18, 1\n\ts_sleep 2\n\ts_branch 1b\n\t"
      "2:\n\ts_and_b32 s48, s49, 3\n\ts_cmp_lg_u32 s48, 3\n\ts_cbranch_scc1 4f\n\ts_cmp_eq_u32 s62, 0\n\ts_cbranch_scc1 4f\n\t"
      "s_waitcnt lgkmcnt(0)\n\ts_barrier\n\ts_mov_b64 s[0:1], s[60:61]\n\ts_mov_b32 s2, s62\n\ts_lshl_b32 s48, s16, 6\n\tv_mbcnt_lo_u32_b32 v0, -1, 0\n\t"
      "v_mbcnt_hi_u32_b32 v0, -1, v0\n\tv_add_u32 v0, s48, v0\n\ts_getpc_b64 s[4:5]\n\t"
      "3:\n\ts_sub_u32 s4, s4, 3b-mk_mega\n\ts_subb_u32 s5, s5, 0\n\ts_setpc_b64 s[4:5]\n\t"
      "4:\n\ts_memrealtime s[46:47]\n\ts_waitcnt lgkmcnt(0)\n\ts_lshl_b32 s19, s46, 2\n\ts_sub_u32 s19, s19, 0x100000\n\ts_lshr_b32 s64, s64, 9\n\ts_lshr_b32 s65, s65, 9\n\t"
      "s_lshr_b32 s66, s66, 9\n\ts_lshr_b32 s67, s67, 9\n\ts_lshl_b32 s48, s49, 10\n\ts_and_b32 s48, s48, 0x2000\n\tv_or_b32 v0, s48, v0\n\tv_or_b32 v14, s48, v14\n\t"
      "v_or_b32 v15, s48, v15\n\ts_mov_b64 s[56:57], s[8:9]\n\ts_mov_b32 s59, s11\n\ts_mov_b64 s[24:25], s[8:9]\n\ts_mov_b32 s27, s11\n\ts_add_u32 s60, s16, 1\n\t"
      "s_bitcmp1_b32 s60, 1\n\ts_cselect_b32 s60, s10, 0\n\ts_lshl_b32 s49, s49, 11\n\ts_and_b32 s49, s49, 0x2000\n\ts_xor_b32 s22, s22, s49\n\ts_xor_b32 s31, s31, s49\n\t"
      "s_cmp_eq_u32 s16, 3\n\ts_cbranch_scc1 64f\n\tv_or_b32 v10, s48, v10\n\tv_or_b32 v11, s48, v11\n\ts_branch 69f\n\t"
      "10:\n\tds_read_b128 v[160:163], v5 offset:0\n\tds_read_b128 v[164:167], v5 offset:1024\n\tds_read_b128 v[168:171], v5 offset:2048\n\t"
      "ds_read_b128 v[172:175], v5 offset:3072\n\tds_read_b128 v[176:179], v5 offset:4096\n\tds_read_b128 v[180:183], v5 offset:5120\n\t"
      "ds_read_b128 v[184:187], v5 offset:6144\n\tds_read_b128 v[188:191], v5 offset:7168\n\tds_read_b128 v[192:195], v5 offset:8192\n\t"
      "ds_read_b128 v[196:199], v5 offset:9216\n\tds_read_b128 v[200:203], v5 offset:10240\n\tds_read_b128 v[204:207], v5 offset:11264\n\t"
      "ds_read_b128 v[208:211], v5 offset:12288\n\tds_read_b128 v[212:215], v5 offset:13312\n\tds_read_b128 v[216:219], v5 offset:14336\n\t"
      "ds_read_b128 v[220:223], v5 offset:15360\n\tv_mov_b64 v[224:225], 0\n\tv_mov_b64 v[226:227], 0\n\tv_mov_b64 v[228:229], 0\n\tv_mov_b64 v[230:231], 0\n\t"
      "v_mov_b64 v[232:233], 0\n\tv_mov_b64 v[234:235], 0\n\tv_mov_b64 v[236:237], 0\n\tv_mov_b64 v[238:239], 0\n\ts_and_b32 s48, s17, 3\n\ts_lshl_b32 s40, s48, 15\n\t"
      "s_add_u32 s40, s40, 0x200000\n\ts_lshl_b32 s41, s48, 17\n\ts_add_u32 s41, s41, 0x220000\n\ts_xor_b32 s48, s48, 2\n\ts_lshl_b32 s42, s48, 17\n\t"
      "s_add_u32 s42, s42, 0x220000\n\ts_add_u32 s68, s40, s64\n\ts_add_u32 s69, s40, s65\n\ts_add_u32 s70, s40, s66\n\ts_add_u32 s71, s40, s67\n\ts_lshr_b32 s21, s20, 4\n\t"
      "s_and_b32 s21, s21, -2\n\ts_add_u32 s49, s22, 0x1000\n\ts_waitcnt lgkmcnt(0)\n\ts_cmp_lg_u32 s16, 3\n\ts_cbranch_scc1 73f\n\t"
      "71:\n\ts_memrealtime vcc\n\ts_waitcnt lgkmcnt(0)\n\ts_lshl_b32 vcc_lo, vcc_lo, 2\n\t"
      "70:\n\ts_sub_u32 s48, s19, vcc_lo\n\ts_cmp_gt_u32 s48, 4000\n\ts_cbranch_scc1 73f\n\ts_cmp_gt_u32 s48, 28\n\ts_cbranch_scc1 71b\n\ts_mul_i32 s48, s48, 45\n\t"
      "s_lshr_b32 s48, s48, 5\n\ts_lshl_b32 s48, s48, 2\n\ts_getpc_b64 vcc\n\t"
      "72:\n\ts_sub_u32 s48, 73f-72b, s48\n\ts_add_u32 vcc_lo, vcc_lo, s48\n\ts_addc_u32 vcc_hi, vcc_hi, 0\n\ts_setpc_b64 vcc\n\t.rept 40\n\ts_nop 0\n\t.endr\n\t"
      "73:\n\ts_barrier\n\t"
      TK0
      "buffer_load_dwordx4 v[240:243], v0, s[8:11], s68 offen sc1\n\ts_nop 11\n\tbuffer_load_dwordx4 v[244:247], v0, s[8:11], s69 offen sc1\n\ts_nop 11\n\t"
      "buffer_load_dwordx4 v[248:251], v0, s[8:11], s70 offen sc1\n\ts_nop 11\n\tbuffer_load_dwordx4 v[252:255], v0, s[8:11], s71 offen sc1\n\t"
      "ds_read_b32 v17, v16 offset:4\n\ts_waitcnt lgkmcnt(0)\n\tv_readfirstlane_b32 s55, v17\n\ts_cmp_gt_u32 s55, s17\n\ts_cselect_b32 s58, s60, 0\n\ts_min_u32 s48, s55, s54\n\t"
      "s_sub_u32 s48, s48, 1\n\ts_cmp_gt_i32 s48, s17\n\ts_cselect_b32 s26, s10, 0\n\ts_sub_u32 s53, s31, 0x800000\n\t"
      "11:\n\ts_cmp_lt_u32 s16, 2\n\ts_cbranch_scc1 15f\n\ts_waitcnt vmcnt(3)\n\t"
      TK1
      "v_smfmac_f32_16x16x64_bf16 v[224:227], v[240:243], a[0:7], v6\n\tv_max3_u32 v17, v240, v241, v242\n\tv_smfmac_f32_16x16x64_bf16 v[228:231], v[240:243], a[8:15], v6\n\t"
      "v_max_u32 v17, v17, v243\n\tv_smfmac_f32_16x16x64_bf16 v[232:235], v[240:243], v[32:39], v6\n\tds_write_b128 v1, v[240:243] offset:0\n\t"
      "v_smfmac_f32_16x16x64_bf16 v[236:239], v[240:243], v[160:167], v6\n\ts_waitcnt vmcnt(2)\n\tv_cmp_eq_u32 vcc, -1, v17\n\t"
      "v_smfmac_f32_16x16x64_bf16 v[224:227], v[244:247], a[16:23], v6\n\tv_max3_u32 v17, v244, v245, v246\n\tv_smfmac_f32_16x16x64_bf16 v[228:231], v[244:247], a[24:31], v6\n\t"
      "s_cbranch_vccnz 86f\n\t"
      "24:\n\tv_max_u32 v17, v17, v247\n\tv_smfmac_f32_16x16x64_bf16 v[232:235], v[244:247], v[40:47], v6\n\tds_write_b128 v1, v[244:247] offset:1024\n\t"
      "v_smfmac_f32_16x16x64_bf16 v[236:239], v[244:247], v[168:175], v6\n\ts_waitcnt lgkmcnt(1)\n\ts_barrier\n\ts_waitcnt vmcnt(1)\n\tv_cmp_eq_u32 vcc, -1, v17\n\t"
      "v_smfmac_f32_16x16x64_bf16 v[224:227], v[248:251], a[32:39], v6\n\tv_max3_u32 v17, v248, v249, v250\n\tv_smfmac_f32_16x16x64_bf16 v[228:231], v[248:251], a[40:47], v6\n\t"
      "s_cbranch_vccnz 87f\n\t"
      "25:\n\tv_max_u32 v17, v17, v251\n\tv_smfmac_f32_16x16x64_bf16 v[232:235], v[248:251], v[48:55], v6\n\tds_write_b128 v1, v[248:251] offset:2048\n\t"
      "v_smfmac_f32_16x16x64_bf16 v[236:239], v[248:251], v[176:183], v6\n\ts_waitcnt vmcnt(0)\n\tv_cmp_eq_u32 vcc, -1, v17\n\t"
      "v_smfmac_f32_16x16x64_bf16 v[224:227], v[252:255], a[48:55], v6\n\tds_read_b128 v[20:23], v2 offset:0\n\tds_read_b128 v[24:27], v3 offset:0\n\t"
      "v_max3_u32 v17, v252, v253, v254\n\tv_smfmac_f32_16x16x64_bf16 v[228:231], v[252:255], a[56:63], v6\n\ts_cbranch_vccnz 88f\n\t"
      "26:\n\tv_max_u32 v17, v17, v255\n\tv_smfmac_f32_16x16x64_bf16 v[232:235], v[252:255], v[56:63], v6\n\tds_write_b128 v1, v[252:255] offset:3072\n\t"
      "v_smfmac_f32_16x16x64_bf16 v[236:239], v[252:255], v[184:191], v6\n\ts_waitcnt lgkmcnt(2)\n\t"
      TK2
      "v_cmp_eq_u32 vcc, -1, v17\n\tv_smfmac_f32_16x16x64_bf16 v[224:227], v[20:23], a[64:71], v6\n\tds_read_b128 v[28:31], v4 offset:0\n\t"
      "v_smfmac_f32_16x16x64_bf16 v[228:231], v[20:23], a[72:79], v6\n\ts_cbranch_vccnz 89f\n\t"
      "27:\n\tv_smfmac_f32_16x16x64_bf16 v[232:235], v[20:23], v[64:71], v6\n\ts_barrier\n\tv_smfmac_f32_16x16x64_bf16 v[236:239], v[20:23], v[192:199], v6\n\t"
      "s_waitcnt lgkmcnt(1)\n\ts_barrier\n\t"
      TK3
      "v_smfmac_f32_16x16x64_bf16 v[224:227], v[24:27], a[80:87], v6\n\tds_read_b128 v[20:23], v2 offset:1024\n\t"
      "v_smfmac_f32_16x16x64_bf16 v[228:231], v[24:27], a[88:95], v6\n\tds_read_b128 v[160:163], v5 offset:16384\n\tds_read_b128 v[164:167], v5 offset:17408\n\t"
      "v_smfmac_f32_16x16x64_bf16 v[232:235], v[24:27], v[72:79], v6\n\tbuffer_load_dword v19, v11, s[24:27], s31 offen sc1\n\t"
      "v_smfmac_f32_16x16x64_bf16 v[236:239], v[24:27], v[200:207], v6\n\ts_waitcnt lgkmcnt(3)\n\tv_smfmac_f32_16x16x64_bf16 v[224:227], v[28:31], a[96:103], v6\n\t"
      "ds_read_b128 v[24:27], v3 offset:1024\n\tv_smfmac_f32_16x16x64_bf16 v[228:231], v[28:31], a[104:111], v6\n\tds_read_b128 v[168:171], v5 offset:18432\n\t"
      "ds_read_b128 v[172:175], v5 offset:19456\n\tv_smfmac_f32_16x16x64_bf16 v[232:235], v[28:31], v[80:87], v6\n\tbuffer_load_dword v19, v11, s[24:27], s53 offen sc1\n\t"
      "v_smfmac_f32_16x16x64_bf16 v[236:239], v[28:31], v[208:215], v6\n\ts_waitcnt lgkmcnt(5)\n\tv_smfmac_f32_16x16x64_bf16 v[224:227], v[20:23], a[112:119], v6\n\t"
      "ds_read_b128 v[28:31], v4 offset:1024\n\tv_smfmac_f32_16x16x64_bf16 v[228:231], v[20:23], a[120:127], v6\n\tds_read_b128 v[176:179], v5 offset:20480\n\t"
      "ds_read_b128 v[180:183], v5 offset:21504\n\tv_smfmac_f32_16x16x64_bf16 v[232:235], v[20:23], v[88:95], v6\n\t"
      "buffer_load_dwordx4 v[240:243], v12, s[56:59], s22 offen offset:0 sc1\n\tv_smfmac_f32_16x16x64_bf16 v[236:239], v[20:23], v[216:223], v6\n\ts_waitcnt lgkmcnt(5)\n\t"
      "v_smfmac_f32_16x16x64_bf16 v[224:227], v[24:27], a[128:135], v6\n\tds_read_b128 v[20:23], v2 offset:2048\n\t"
      "v_smfmac_f32_16x16x64_bf16 v[228:231], v[24:27], a[136:143], v6\n\tds_read_b128 v[184:187], v5 offset:22528\n\tds_read_b128 v[188:191], v5 offset:23552\n\t"
      "v_smfmac_f32_16x16x64_bf16 v[232:235], v[24:27], v[96:103], v6\n\tbuffer_load_dwordx4 v[244:247], v12, s[56:59], s22 offen offset:2048 sc1\n\t"
      "v_smfmac_f32_16x16x64_bf16 v[236:239], v[24:27], v[160:167], v6\n\ts_waitcnt lgkmcnt(5)\n\tv_smfmac_f32_16x16x64_bf16 v[224:227], v[28:31], a[144:151], v6\n\t"
      "ds_read_b128 v[24:27], v3 offset:2048\n\tv_smfmac_f32_16x16x64_bf16 v[228:231], v[28:31], a[152:159], v6\n\tds_read_b128 v[192:195], v5 offset:24576\n\t"
      "ds_read_b128 v[196:199], v5 offset:25600\n\tv_smfmac_f32_16x16x64_bf16 v[232:235], v[28:31], v[104:111], v6\n\t"
      "buffer_load_dwordx4 v[248:251], v12, s[56:59], s49 offen offset:0 sc1\n\tv_smfmac_f32_16x16x64_bf16 v[236:239], v[28:31], v[168:175], v6\n\ts_waitcnt lgkmcnt(5)\n\t"
      "ds_read_b128 v[160:163], v9 offset:0\n\tv_smfmac_f32_16x16x64_bf16 v[224:227], v[20:23], a[160:167], v6\n\tds_read_b128 v[28:31], v4 offset:2048\n\t"
      "ds_read_b128 v[164:167], v9 offset:2048\n\tv_smfmac_f32_16x16x64_bf16 v[228:231], v[20:23], a[168:175], v6\n\tds_read_b128 v[200:203], v5 offset:26624\n\t"
      "ds_read_b128 v[204:207], v5 offset:27648\n\tv_smfmac_f32_16x16x64_bf16 v[232:235], v[20:23], v[112:119], v6\n\t"
      "buffer_load_dwordx4 v[252:255], v12, s[56:59], s49 offen offset:2048 sc1\n\tv_smfmac_f32_16x16x64_bf16 v[236:239], v[20:23], v[176:183], v6\n\ts_waitcnt lgkmcnt(7)\n\t"
      "ds_read_b128 v[168:171], v9 offset:4096\n\tv_smfmac_f32_16x16x64_bf16 v[224:227], v[24:27], a[176:183], v6\n\tds_read_b128 v[20:23], v2 offset:3072\n\t"
      "ds_read_b128 v[172:175], v9 offset:6144\n\tv_smfmac_f32_16x16x64_bf16 v[228:231], v[24:27], a[184:191], v6\n\tds_read_b128 v[208:211], v5 offset:28672\n\t"
      "ds_read_b128 v[212:215], v5 offset:29696\n\tv_smfmac_f32_16x16x64_bf16 v[232:235], v[24:27], v[120:127], v6\n\t"
      "v_smfmac_f32_16x16x64_bf16 v[236:239], v[24:27], v[184:191], v6\n\ts_waitcnt lgkmcnt(8)\n\tds_read_b128 v[176:179], v9 offset:256\n\t"
      "v_smfmac_f32_16x16x64_bf16 v[224:227], v[28:31], a[192:199], v6\n\tds_read_b128 v[24:27], v3 offset:3072\n\tds_read_b128 v[180:183], v9 offset:2304\n\t"
      "v_smfmac_f32_16x16x64_bf16 v[228:231], v[28:31], a[200:207], v6\n\tds_read_b128 v[216:219], v5 offset:30720\n\tds_read_b128 v[220:223], v5 offset:31744\n\t"
      "v_smfmac_f32_16x16x64_bf16 v[232:235], v[28:31], v[128:135], v6\n\ts_cmp_eq_u32 s16, 2\n\tv_smfmac_f32_16x16x64_bf16 v[236:239], v[28:31], v[192:199], v6\n\t"
      "s_waitcnt lgkmcnt(8)\n\tds_read_b128 v[184:187], v9 offset:4352\n\tv_smfmac_f32_16x16x64_bf16 v[224:227], v[20:23], a[208:215], v6\n\t"
      "ds_read_b128 v[28:31], v4 offset:3072\n\tds_read_b128 v[188:191], v9 offset:6400\n\tv_smfmac_f32_16x16x64_bf16 v[228:231], v[20:23], a[216:223], v6\n\t"
      "v_smfmac_f32_16x16x64_bf16 v[232:235], v[20:23], v[136:143], v6\n\tv_smfmac_f32_16x16x64_bf16 v[236:239], v[20:23], v[200:207], v6\n\ts_waitcnt lgkmcnt(6)\n\t"
      "v_smfmac_f32_16x16x64_bf16 v[224:227], v[24:27], a[224:231], v6\n\tv_smfmac_f32_16x16x64_bf16 v[228:231], v[24:27], a[232:239], v6\n\ts_waitcnt lgkmcnt(1)\n\t"
      "v_smfmac_f32_16x16x64_bf16 v[224:227], v[28:31], a[240:247], v6\n\ts_nop 2\n\tv_smfmac_f32_16x16x64_bf16 v[228:231], v[28:31], a[248:255], v6\n\ts_nop 2\n\t"
      "v_smfmac_f32_16x16x64_bf16 v[232:235], v[24:27], v[144:151], v6\n\tv_add_f32 v224, v224, v225\n\tv_smfmac_f32_16x16x64_bf16 v[232:235], v[28:31], v[152:159], v6\n\t"
      "s_nop 0\n\tv_add_f32 v226, v226, v227\n\tv_smfmac_f32_16x16x64_bf16 v[236:239], v[24:27], v[208:215], v6\n\tv_add_f32 v228, v228, v229\n\t"
      "v_smfmac_f32_16x16x64_bf16 v[236:239], v[28:31], v[216:223], v6\n\t"
      TK4
      "v_add_f32 v230, v230, v231\n\tv_cvt_pk_bf16_f32 v224, v224, v228\n\tv_cvt_pk_bf16_f32 v226, v226, v230\n\tv_add_f32 v232, v232, v233\n\tv_add_f32 v234, v234, v235\n\t"
      "s_nop 2\n\tv_add_f32 v236, v236, v237\n\tv_add_f32 v238, v238, v239\n\tv_cvt_pk_bf16_f32 v225, v232, v236\n\tv_cvt_pk_bf16_f32 v227, v234, v238\n\t"
      "ds_write2_b64 v7, v[224:225], v[226:227] offset1:34\n\ts_waitcnt lgkmcnt(0)\n\ts_barrier\n\ts_cbranch_scc1 40f\n\ts_branch 60f\n\t"
      "15:\n\ts_waitcnt vmcnt(3)\n\t"
      TK1
      "v_smfmac_f32_16x16x64_bf16 v[224:227], v[240:243], a[0:7], v6\n\tds_write_b128 v1, v[240:243] offset:0\n\t"
      "v_smfmac_f32_16x16x64_bf16 v[228:231], v[240:243], a[8:15], v6\n\tv_max3_u32 v17, v240, v241, v242\n\tv_smfmac_f32_16x16x64_bf16 v[232:235], v[240:243], v[32:39], v6\n\t"
      "v_max_u32 v17, v17, v243\n\tv_smfmac_f32_16x16x64_bf16 v[236:239], v[240:243], v[160:167], v6\n\ts_waitcnt vmcnt(2)\n\tv_cmp_eq_u32 vcc, -1, v17\n\t"
      "v_smfmac_f32_16x16x64_bf16 v[224:227], v[244:247], a[16:23], v6\n\tds_write_b128 v1, v[244:247] offset:1024\n\t"
      "v_smfmac_f32_16x16x64_bf16 v[228:231], v[244:247], a[24:31], v6\n\ts_cbranch_vccnz 86f\n\t"
      "20:\n\tv_max3_u32 v17, v244, v245, v246\n\tv_smfmac_f32_16x16x64_bf16 v[232:235], v[244:247], v[40:47], v6\n\tv_max_u32 v17, v17, v247\n\t"
      "v_smfmac_f32_16x16x64_bf16 v[236:239], v[244:247], v[168:175], v6\n\ts_waitcnt lgkmcnt(1)\n\ts_barrier\n\ts_waitcnt vmcnt(1)\n\tv_cmp_eq_u32 vcc, -1, v17\n\t"
      "v_smfmac_f32_16x16x64_bf16 v[224:227], v[248:251], a[32:39], v6\n\tds_write_b128 v1, v[248:251] offset:2048\n\t"
      "v_smfmac_f32_16x16x64_bf16 v[228:231], v[248:251], a[40:47], v6\n\ts_cbranch_vccnz 87f\n\t"
      "21:\n\tv_max3_u32 v17, v248, v249, v250\n\tv_smfmac_f32_16x16x64_bf16 v[232:235], v[248:251], v[48:55], v6\n\tv_max_u32 v17, v17, v251\n\t"
      "v_smfmac_f32_16x16x64_bf16 v[236:239], v[248:251], v[176:183], v6\n\tds_read_b128 v[20:23], v2 offset:0\n\tds_read_b128 v[24:27], v3 offset:0\n\ts_waitcnt vmcnt(0)\n\t"
      "v_cmp_eq_u32 vcc, -1, v17\n\tv_smfmac_f32_16x16x64_bf16 v[224:227], v[252:255], a[48:55], v6\n\tds_write_b128 v1, v[252:255] offset:3072\n\t"
      "v_smfmac_f32_16x16x64_bf16 v[228:231], v[252:255], a[56:63], v6\n\ts_cbranch_vccnz 88f\n\t"
      "22:\n\tv_max3_u32 v17, v252, v253, v254\n\tv_smfmac_f32_16x16x64_bf16 v[232:235], v[252:255], v[56:63], v6\n\tv_max_u32 v17, v17, v255\n\t"
      "v_smfmac_f32_16x16x64_bf16 v[236:239], v[252:255], v[184:191], v6\n\tds_read_b128 v[28:31], v4 offset:0\n\ts_waitcnt lgkmcnt(3)\n\t"
      TK2
      "v_cmp_eq_u32 vcc, -1, v17\n\tv_smfmac_f32_16x16x64_bf16 v[224:227], v[20:23], a[64:71], v6\n\tv_smfmac_f32_16x16x64_bf16 v[228:231], v[20:23], a[72:79], v6\n\t"
      "s_cbranch_vccnz 89f\n\t"
      "23:\n\tv_smfmac_f32_16x16x64_bf16 v[232:235], v[20:23], v[64:71], v6\n\ts_barrier\n\ts_memrealtime s[46:47]\n\t"
      "v_smfmac_f32_16x16x64_bf16 v[236:239], v[20:23], v[192:199], v6\n\ts_waitcnt lgkmcnt(1)\n\ts_barrier\n\t"
      TK3
      "v_smfmac_f32_16x16x64_bf16 v[224:227], v[24:27], a[80:87], v6\n\tv_smfmac_f32_16x16x64_bf16 v[228:231], v[24:27], a[88:95], v6\n\t"
      "v_smfmac_f32_16x16x64_bf16 v[232:235], v[24:27], v[72:79], v6\n\ts_waitcnt lgkmcnt(0)\n\tds_read_b128 v[20:23], v2 offset:1024\n\ts_lshl2_add_u32 s47, s46, s21\n\t"
      "v_smfmac_f32_16x16x64_bf16 v[236:239], v[24:27], v[200:207], v6\n\tds_read_b128 v[160:163], v5 offset:16384\n\tds_read_b128 v[164:167], v5 offset:17408\n\t"
      "v_mov_b32 v18, s47\n\tv_smfmac_f32_16x16x64_bf16 v[224:227], v[28:31], a[96:103], v6\n\tbuffer_store_dword v18, v14, s[8:11], s41 offen sc1\n\t"
      "v_smfmac_f32_16x16x64_bf16 v[228:231], v[28:31], a[104:111], v6\n\tds_read_b128 v[168:171], v5 offset:18432\n\tds_read_b128 v[172:175], v5 offset:19456\n\t"
      "v_smfmac_f32_16x16x64_bf16 v[232:235], v[28:31], v[80:87], v6\n\tds_read_b128 v[24:27], v3 offset:1024\n\t"
      "v_smfmac_f32_16x16x64_bf16 v[236:239], v[28:31], v[208:215], v6\n\ts_waitcnt lgkmcnt(5)\n\tv_smfmac_f32_16x16x64_bf16 v[224:227], v[20:23], a[112:119], v6\n\t"
      "ds_read_b128 v[28:31], v4 offset:1024\n\tv_smfmac_f32_16x16x64_bf16 v[228:231], v[20:23], a[120:127], v6\n\tds_read_b128 v[176:179], v5 offset:20480\n\t"
      "ds_read_b128 v[180:183], v5 offset:21504\n\tv_smfmac_f32_16x16x64_bf16 v[232:235], v[20:23], v[88:95], v6\n\t"
      "buffer_load_dwordx4 v[240:243], v12, s[56:59], s22 offen offset:0 sc1\n\tv_smfmac_f32_16x16x64_bf16 v[236:239], v[20:23], v[216:223], v6\n\ts_waitcnt lgkmcnt(3)\n\t"
      "v_smfmac_f32_16x16x64_bf16 v[224:227], v[24:27], a[128:135], v6\n\tds_read_b128 v[20:23], v2 offset:2048\n\t"
      "v_smfmac_f32_16x16x64_bf16 v[228:231], v[24:27], a[136:143], v6\n\tds_read_b128 v[184:187], v5 offset:22528\n\tds_read_b128 v[188:191], v5 offset:23552\n\t"
      "v_smfmac_f32_16x16x64_bf16 v[232:235], v[24:27], v[96:103], v6\n\tbuffer_load_dwordx4 v[244:247], v12, s[56:59], s22 offen offset:2048 sc1\n\t"
      "v_smfmac_f32_16x16x64_bf16 v[236:239], v[24:27], v[160:167], v6\n\ts_waitcnt lgkmcnt(5)\n\tv_smfmac_f32_16x16x64_bf16 v[224:227], v[28:31], a[144:151], v6\n\t"
      "ds_read_b128 v[24:27], v3 offset:2048\n\tv_smfmac_f32_16x16x64_bf16 v[228:231], v[28:31], a[152:159], v6\n\tds_read_b128 v[192:195], v5 offset:24576\n\t"
      "ds_read_b128 v[196:199], v5 offset:25600\n\tv_smfmac_f32_16x16x64_bf16 v[232:235], v[28:31], v[104:111], v6\n\t"
      "buffer_load_dwordx4 v[248:251], v12, s[56:59], s49 offen offset:0 sc1\n\tv_smfmac_f32_16x16x64_bf16 v[236:239], v[28:31], v[168:175], v6\n\ts_waitcnt lgkmcnt(5)\n\t"
      "ds_read_b128 v[160:163], v9 offset:0\n\tv_smfmac_f32_16x16x64_bf16 v[224:227], v[20:23], a[160:167], v6\n\tds_read_b128 v[28:31], v4 offset:2048\n\t"
      "ds_read_b128 v[164:167], v9 offset:2048\n\tv_smfmac_f32_16x16x64_bf16 v[228:231], v[20:23], a[168:175], v6\n\tds_read_b128 v[200:203], v5 offset:26624\n\t"
      "ds_read_b128 v[204:207], v5 offset:27648\n\tv_smfmac_f32_16x16x64_bf16 v[232:235], v[20:23], v[112:119], v6\n\t"
      "buffer_load_dwordx4 v[252:255], v12, s[56:59], s49 offen offset:2048 sc1\n\tv_smfmac_f32_16x16x64_bf16 v[236:239], v[20:23], v[176:183], v6\n\ts_waitcnt lgkmcnt(7)\n\t"
      "ds_read_b128 v[168:171], v9 offset:4096\n\tv_smfmac_f32_16x16x64_bf16 v[224:227], v[24:27], a[176:183], v6\n\tds_read_b128 v[20:23], v2 offset:3072\n\t"
      "ds_read_b128 v[172:175], v9 offset:6144\n\tv_smfmac_f32_16x16x64_bf16 v[228:231], v[24:27], a[184:191], v6\n\tds_read_b128 v[208:211], v5 offset:28672\n\t"
      "ds_read_b128 v[212:215], v5 offset:29696\n\tv_smfmac_f32_16x16x64_bf16 v[232:235], v[24:27], v[120:127], v6\n\t"
      "v_smfmac_f32_16x16x64_bf16 v[236:239], v[24:27], v[184:191], v6\n\ts_waitcnt lgkmcnt(8)\n\tds_read_b128 v[176:179], v9 offset:256\n\t"
      "v_smfmac_f32_16x16x64_bf16 v[224:227], v[28:31], a[192:199], v6\n\tds_read_b128 v[24:27], v3 offset:3072\n\tds_read_b128 v[180:183], v9 offset:2304\n\t"
      "v_smfmac_f32_16x16x64_bf16 v[228:231], v[28:31], a[200:207], v6\n\tds_read_b128 v[216:219], v5 offset:30720\n\tds_read_b128 v[220:223], v5 offset:31744\n\t"
      "v_smfmac_f32_16x16x64_bf16 v[232:235], v[28:31], v[128:135], v6\n\ts_cmp_eq_u32 s16, 0\n\tv_smfmac_f32_16x16x64_bf16 v[236:239], v[28:31], v[192:199], v6\n\t"
      "s_waitcnt lgkmcnt(8)\n\tds_read_b128 v[184:187], v9 offset:4352\n\tv_smfmac_f32_16x16x64_bf16 v[224:227], v[20:23], a[208:215], v6\n\t"
      "ds_read_b128 v[28:31], v4 offset:3072\n\tds_read_b128 v[188:191], v9 offset:6400\n\tv_smfmac_f32_16x16x64_bf16 v[228:231], v[20:23], a[216:223], v6\n\t"
      "v_smfmac_f32_16x16x64_bf16 v[232:235], v[20:23], v[136:143], v6\n\tv_smfmac_f32_16x16x64_bf16 v[236:239], v[20:23], v[200:207], v6\n\ts_waitcnt lgkmcnt(6)\n\t"
      "v_smfmac_f32_16x16x64_bf16 v[224:227], v[24:27], a[224:231], v6\n\tv_smfmac_f32_16x16x64_bf16 v[228:231], v[24:27], a[232:239], v6\n\ts_waitcnt lgkmcnt(1)\n\t"
      "v_smfmac_f32_16x16x64_bf16 v[224:227], v[28:31], a[240:247], v6\n\ts_nop 2\n\tv_smfmac_f32_16x16x64_bf16 v[228:231], v[28:31], a[248:255], v6\n\ts_nop 2\n\t"
      "v_smfmac_f32_16x16x64_bf16 v[232:235], v[24:27], v[144:151], v6\n\tv_add_f32 v224, v224, v225\n\tv_smfmac_f32_16x16x64_bf16 v[232:235], v[28:31], v[152:159], v6\n\t"
      "s_nop 0\n\tv_add_f32 v226, v226, v227\n\tv_smfmac_f32_16x16x64_bf16 v[236:239], v[24:27], v[208:215], v6\n\tv_add_f32 v228, v228, v229\n\t"
      "v_smfmac_f32_16x16x64_bf16 v[236:239], v[28:31], v[216:223], v6\n\t"
      TK4
      "v_add_f32 v230, v230, v231\n\tv_cvt_pk_bf16_f32 v224, v224, v228\n\tv_cvt_pk_bf16_f32 v226, v226, v230\n\tv_add_f32 v232, v232, v233\n\tv_add_f32 v234, v234, v235\n\t"
      "s_nop 2\n\tv_add_f32 v236, v236, v237\n\tv_add_f32 v238, v238, v239\n\tv_cvt_pk_bf16_f32 v225, v232, v236\n\tv_cvt_pk_bf16_f32 v227, v234, v238\n\t"
      "ds_write2_b32 v7, v224, v225 offset1:4\n\tds_write2_b32 v7, v226, v227 offset0:8 offset1:12\n\ts_cbranch_scc0 39f\n\t"
      "14:\n\tv_mov_b64 v[228:229], 0\n\tv_mov_b64 v[230:231], 0\n\tv_mov_b64 v[232:233], 0\n\tv_mov_b64 v[234:235], 0\n\tv_mov_b64 v[236:237], 0\n\tv_mov_b64 v[238:239], 0\n\t"
      "s_mov_b32 vcc_lo, 0x55555555\n\ts_mov_b32 vcc_hi, 0x55555555\n\ts_waitcnt lgkmcnt(0)\n\ts_barrier\n\t"
      TK5
      "ds_read_b128 v[20:23], v1 offset:24576\n\tds_read_b128 v[24:27], v1 offset:25600\n\tds_read_b128 v[208:211], v8 offset:0\n\tds_read_b128 v[212:215], v8 offset:64\n\t"
      "ds_read_b128 v[216:219], v8 offset:128\n\tds_read_b128 v[220:223], v8 offset:192\n\ts_waitcnt lgkmcnt(5)\n\t"
      "v_smfmac_f32_16x16x64_bf16 v[232:235], v[20:23], v[160:167], v6\n\tv_smfmac_f32_16x16x64_bf16 v[236:239], v[20:23], v[176:183], v6\n\ts_waitcnt lgkmcnt(4)\n\t"
      "v_smfmac_f32_16x16x64_bf16 v[232:235], v[24:27], v[168:175], v6\n\tv_smfmac_f32_16x16x64_bf16 v[236:239], v[24:27], v[184:191], v6\n\ts_nop 1\n\ts_waitcnt lgkmcnt(2)\n\t"
      "v_smfmac_f32_16x16x64_bf16 v[228:231], v[20:23], v[208:215], v6\n\ts_nop 1\n\ts_waitcnt lgkmcnt(0)\n\tv_smfmac_f32_16x16x64_bf16 v[228:231], v[24:27], v[216:223], v6\n\t"
      "v_add_f32 v232, v232, v233\n\tv_add_f32 v234, v234, v235\n\tv_add_f32 v236, v236, v237\n\tv_add_f32 v238, v238, v239\n\t"
      "v_cndmask_b32_dpp v194, v234, v232, vcc quad_perm:[1,0,3,2] row_mask:0xf bank_mask:0xf\n\ts_not_b64 vcc, vcc\n\t"
      "v_cndmask_b32_dpp v195, v232, v234, vcc quad_perm:[1,0,3,2] row_mask:0xf bank_mask:0xf\n\t"
      "v_cndmask_b32_dpp v197, v236, v238, vcc quad_perm:[1,0,3,2] row_mask:0xf bank_mask:0xf\n\tv_add_f32 v228, v228, v229\n\tv_add_f32 v230, v230, v231\n\t"
      "v_max3_f32 v198, v194, v195, v197\n\tv_pk_mul_f32 v[194:195], v[194:195], s[28:29]\n\t"
      "v_cndmask_b32_dpp v193, v228, v230, vcc quad_perm:[1,0,3,2] row_mask:0xf bank_mask:0xf\n\ts_not_b64 vcc, vcc\n\t"
      "v_cndmask_b32_dpp v196, v238, v236, vcc quad_perm:[1,0,3,2] row_mask:0xf bank_mask:0xf\n\t"
      "v_cndmask_b32_dpp v192, v230, v228, vcc quad_perm:[1,0,3,2] row_mask:0xf bank_mask:0xf\n\tv_max3_f32 v198, v198, v196, v193\n\tv_max_f32 v198, v198, v192\n\t"
      "v_pk_mul_f32 v[196:197], v[196:197], s[28:29]\n\tv_pk_mul_f32 v[192:193], v[192:193], s[28:29]\n\tv_max_f32_dpp v198, v198, v198 row_ror:8 row_mask:0xf bank_mask:0xf\n\t"
      "s_nop 1\n\tv_max_f32_dpp v198, v198, v198 row_ror:4 row_mask:0xf bank_mask:0xf\n\ts_nop 1\n\tv_max_f32_dpp v198, v198, v198 row_ror:2 row_mask:0xf bank_mask:0xf\n\t"
      "v_mul_f32 v198, s28, v198\n\tv_pk_add_f32 v[192:193], v[192:193], v[198:199] op_sel_hi:[1,0] neg_lo:[0,1] neg_hi:[0,1]\n\t"
      "v_pk_add_f32 v[194:195], v[194:195], v[198:199] op_sel_hi:[1,0] neg_lo:[0,1] neg_hi:[0,1]\n\t"
      "v_pk_add_f32 v[196:197], v[196:197], v[198:199] op_sel_hi:[1,0] neg_lo:[0,1] neg_hi:[0,1]\n\tv_exp_f32 v192, v192\n\tv_exp_f32 v193, v193\n\tv_exp_f32 v194, v194\n\t"
      "v_exp_f32 v195, v195\n\tv_exp_f32 v196, v196\n\tv_pk_fma_f32 v[198:199], v[192:193], v[12:13], v[194:195] op_sel:[0,1,0]\n\tv_exp_f32 v197, v197\n\t"
      "v_add_f32 v198, v198, v196\n\tv_add_f32 v199, v199, v197\n\tv_add_f32 v198, v198, v199\n\ts_nop 1\n\t"
      "v_add_f32_dpp v198, v198, v198 row_ror:8 row_mask:0xf bank_mask:0xf\n\ts_nop 1\n\tv_add_f32_dpp v198, v198, v198 row_ror:4 row_mask:0xf bank_mask:0xf\n\ts_nop 1\n\t"
      "v_add_f32_dpp v198, v198, v198 row_ror:2 row_mask:0xf bank_mask:0xf\n\tv_rcp_f32 v198, v198\n\ts_nop 0\n\t"
      "v_pk_mul_f32 v[194:195], v[194:195], v[198:199] op_sel_hi:[1,0]\n\tv_pk_mul_f32 v[196:197], v[196:197], v[198:199] op_sel_hi:[1,0]\n\t"
      "v_cvt_pk_bf16_f32 v200, v194, v195\n\tv_cvt_pk_bf16_f32 v201, v196, v197\n\tv_pk_mul_f32 v[192:193], v[192:193], v[198:199] op_sel_hi:[1,0]\n\t"
      "buffer_store_dwordx2 v[200:201], v10, s[8:11], s41 offen sc1\n\tv_cvt_pk_bf16_f32 v202, v192, v193\n\tbuffer_store_dword v202, v11, s[8:11], s41 offen sc1\n\t"
      TK6
      "s_memrealtime vcc\n\ts_waitcnt lgkmcnt(0)\n\ts_sub_u32 s48, vcc_lo, s46\n\ts_lshl_b32 s48, s48, 2\n\ts_min_u32 s48, s48, 800\n\t"
      STS("s_add_u32 s51, s51, s48\n\t")
      "s_lshl_b32 s48, s48, 4\n\ts_sub_i32 s48, s48, s20\n\ts_ashr_i32 s48, s48, 2\n\ts_add_i32 s20, s20, s48\n\tv_mov_b64 v[204:205], -1\n\t"
      "buffer_store_dwordx2 v[204:205], v10, s[8:11], s42 offen sc1\n\tbuffer_store_dword v204, v11, s[8:11], s42 offen sc1\n\t"
      "buffer_store_dword v204, v14, s[8:11], s42 offen sc1\n\ts_branch 69f\n\t"
      "39:\n\ts_waitcnt lgkmcnt(0)\n\ts_barrier\n\t"
      "40:\n\t"
      TK5
      "s_sleep 2\n\tds_read_b128 v[208:211], v8 offset:0\n\tds_read_b128 v[212:215], v8 offset:64\n\tds_read_b128 v[216:219], v8 offset:128\n\t"
      "ds_read_b128 v[220:223], v8 offset:192\n\ts_and_b32 s48, s17, 7\n\ts_lshl_b32 s43, s48, 17\n\ts_add_u32 s43, s43, 0x300000\n\ts_add_u32 s48, s17, 3\n\t"
      "s_and_b32 s48, s48, 7\n\ts_lshl_b32 s44, s48, 17\n\ts_add_u32 s44, s44, 0x300000\n\ts_cmp_lg_u32 s58, 0\n\ts_cbranch_scc1 43f\n\t"
      "41:\n\ts_cmp_gt_u32 s55, s17\n\ts_cbranch_scc1 42f\n\tbuffer_load_dword v17, off, s[12:15], s45 sc1\n\ts_waitcnt vmcnt(0)\n\tv_readfirstlane_b32 s55, v17\n\t"
      "s_cmp_eq_u32 s18, 0\n\ts_cbranch_scc1 42f\n\ts_sub_u32 s18, s18, 1\n\ts_sleep 1\n\ts_branch 41b\n\t"
      "42:\n\tbuffer_load_dwordx4 v[240:243], v12, s[8:11], s22 offen offset:0 sc1\n\tbuffer_load_dwordx4 v[244:247], v12, s[8:11], s22 offen offset:2048 sc1\n\t"
      "buffer_load_dwordx4 v[248:251], v12, s[8:11], s49 offen offset:0 sc1\n\tbuffer_load_dwordx4 v[252:255], v12, s[8:11], s49 offen offset:2048 sc1\n\t"
      "43:\n\ts_waitcnt vmcnt(0)\n\tv_mov_b64 v[204:205], -1\n\tv_mov_b64 v[206:207], -1\n\tv_mov_b32 v203, s30\n\ts_cmp_eq_u32 s16, 2\n\ts_cbranch_scc1 50f\n\t"
      "v_mfma_f32_16x16x32_bf16 v[228:231], v[160:163], v[240:243], 0\n\tv_mfma_f32_16x16x32_bf16 v[228:231], v[164:167], v[244:247], v[228:231]\n\t"
      "v_mfma_f32_16x16x32_bf16 v[228:231], v[168:171], v[248:251], v[228:231]\n\tv_mfma_f32_16x16x32_bf16 v[228:231], v[172:175], v[252:255], v[228:231]\n\t"
      "s_waitcnt lgkmcnt(0)\n\tv_mfma_f32_16x16x32_bf16 v[224:227], v[208:211], v[240:243], 0\n\tv_mfma_f32_16x16x32_bf16 v[224:227], v[212:215], v[244:247], v[224:227]\n\t"
      "v_mfma_f32_16x16x32_bf16 v[224:227], v[216:219], v[248:251], v[224:227]\n\tv_mfma_f32_16x16x32_bf16 v[224:227], v[220:223], v[252:255], v[224:227]\n\ts_nop 2\n\t"
      "v_cndmask_b32 v228, v203, v228, s[32:33]\n\tv_cndmask_b32 v229, v203, v229, s[34:35]\n\tv_cndmask_b32 v230, v203, v230, s[36:37]\n\t"
      "v_cndmask_b32 v231, v203, v231, s[38:39]\n\tv_max3_f32 v192, v228, v229, v230\n\tv_max3_f32 v193, v231, v224, v225\n\tv_max3_f32 v192, v192, v226, v227\n\t"
      "v_max_f32 v192, v192, v193\n\tv_mov_b32 v193, v192\n\ts_nop 1\n\tv_permlane32_swap_b32 v192, v193\n\tv_max_f32 v192, v192, v193\n\tv_mov_b32 v193, v192\n\ts_nop 1\n\t"
      "v_permlane16_swap_b32 v192, v193\n\tv_max_f32 v192, v192, v193\n\tv_mov_b32 v193, v192\n\tv_pk_add_f32 v[224:225], v[224:225], v[192:193] neg_lo:[0,1] neg_hi:[0,1]\n\t"
      "v_pk_add_f32 v[226:227], v[226:227], v[192:193] neg_lo:[0,1] neg_hi:[0,1]\n\tv_pk_add_f32 v[228:229], v[228:229], v[192:193] neg_lo:[0,1] neg_hi:[0,1]\n\t"
      "v_pk_add_f32 v[230:231], v[230:231], v[192:193] neg_lo:[0,1] neg_hi:[0,1]\n\tv_pk_mul_f32 v[224:225], v[224:225], s[28:29]\n\t"
      "v_pk_mul_f32 v[226:227], v[226:227], s[28:29]\n\tv_pk_mul_f32 v[228:229], v[228:229], s[28:29]\n\tv_pk_mul_f32 v[230:231], v[230:231], s[28:29]\n\t"
      "v_exp_f32 v224, v224\n\tv_exp_f32 v225, v225\n\tv_exp_f32 v226, v226\n\tv_exp_f32 v227, v227\n\tv_exp_f32 v228, v228\n\tv_exp_f32 v229, v229\n\tv_exp_f32 v230, v230\n\t"
      "v_exp_f32 v231, v231\n\tv_pk_add_f32 v[192:193], v[224:225], v[226:227]\n\tv_pk_add_f32 v[194:195], v[228:229], v[230:231]\n\tv_add_f32 v192, v192, v193\n\t"
      "v_add_f32 v194, v194, v195\n\tv_fma_f32 v192, v192, v13, v194\n\tv_mov_b32 v193, v192\n\ts_nop 1\n\tv_permlane32_swap_b32 v192, v193\n\tv_add_f32 v192, v192, v193\n\t"
      "v_mov_b32 v193, v192\n\ts_nop 1\n\tv_permlane16_swap_b32 v192, v193\n\tv_add_f32 v192, v192, v193\n\tv_rcp_f32 v192, v192\n\ts_nop 0\n\tv_mov_b32 v193, v192\n\t"
      "v_pk_mul_f32 v[224:225], v[224:225], v[192:193]\n\tv_pk_mul_f32 v[226:227], v[226:227], v[192:193]\n\tv_pk_mul_f32 v[228:229], v[228:229], v[192:193]\n\t"
      "v_pk_mul_f32 v[230:231], v[230:231], v[192:193]\n\tv_cvt_pk_bf16_f32 v194, v224, v225\n\tv_cvt_pk_bf16_f32 v195, v226, v227\n\tv_cvt_pk_bf16_f32 v196, v228, v229\n\t"
      "v_cvt_pk_bf16_f32 v197, v230, v231\n\tbuffer_store_dwordx2 v[196:197], v10, s[8:11], s43 offen sc1\n\tbuffer_store_dwordx2 v[194:195], v11, s[8:11], s43 offen sc1\n\t"
      "buffer_store_dwordx2 v[204:205], v10, s[8:11], s44 offen sc1\n\tbuffer_store_dwordx2 v[204:205], v11, s[8:11], s44 offen sc1\n\t"
      TK6
      "s_branch 69f\n\t"
      "50:\n\tv_mfma_f32_16x16x32_bf16 v[228:231], v[160:163], v[240:243], 0\n\tv_mfma_f32_16x16x32_bf16 v[232:235], v[176:179], v[240:243], 0\n\t"
      "v_mfma_f32_16x16x32_bf16 v[228:231], v[164:167], v[244:247], v[228:231]\n\tv_mfma_f32_16x16x32_bf16 v[232:235], v[180:183], v[244:247], v[232:235]\n\t"
      "v_mfma_f32_16x16x32_bf16 v[228:231], v[168:171], v[248:251], v[228:231]\n\tv_mfma_f32_16x16x32_bf16 v[232:235], v[184:187], v[248:251], v[232:235]\n\t"
      "v_mfma_f32_16x16x32_bf16 v[228:231], v[172:175], v[252:255], v[228:231]\n\tv_mfma_f32_16x16x32_bf16 v[232:235], v[188:191], v[252:255], v[232:235]\n\t"
      "s_waitcnt lgkmcnt(0)\n\tv_mfma_f32_16x16x32_bf16 v[224:227], v[208:211], v[240:243], 0\n\tv_mfma_f32_16x16x32_bf16 v[224:227], v[212:215], v[244:247], v[224:227]\n\t"
      "v_mfma_f32_16x16x32_bf16 v[224:227], v[216:219], v[248:251], v[224:227]\n\tv_mfma_f32_16x16x32_bf16 v[224:227], v[220:223], v[252:255], v[224:227]\n\ts_nop 2\n\t"
      "v_cndmask_b32 v232, v203, v232, s[32:33]\n\tv_cndmask_b32 v233, v203, v233, s[34:35]\n\tv_cndmask_b32 v234, v203, v234, s[36:37]\n\t"
      "v_cndmask_b32 v235, v203, v235, s[38:39]\n\tv_max3_f32 v192, v228, v229, v230\n\tv_max3_f32 v193, v231, v224, v225\n\tv_max3_f32 v192, v192, v226, v227\n\t"
      "v_max3_f32 v193, v193, v232, v233\n\tv_max3_f32 v192, v192, v234, v235\n\tv_max_f32 v192, v192, v193\n\tv_mov_b32 v193, v192\n\ts_nop 1\n\t"
      "v_permlane32_swap_b32 v192, v193\n\tv_max_f32 v192, v192, v193\n\tv_mov_b32 v193, v192\n\ts_nop 1\n\tv_permlane16_swap_b32 v192, v193\n\tv_max_f32 v192, v192, v193\n\t"
      "v_mov_b32 v193, v192\n\tv_pk_add_f32 v[224:225], v[224:225], v[192:193] neg_lo:[0,1] neg_hi:[0,1]\n\t"
      "v_pk_add_f32 v[226:227], v[226:227], v[192:193] neg_lo:[0,1] neg_hi:[0,1]\n\tv_pk_add_f32 v[228:229], v[228:229], v[192:193] neg_lo:[0,1] neg_hi:[0,1]\n\t"
      "v_pk_add_f32 v[230:231], v[230:231], v[192:193] neg_lo:[0,1] neg_hi:[0,1]\n\tv_pk_add_f32 v[232:233], v[232:233], v[192:193] neg_lo:[0,1] neg_hi:[0,1]\n\t"
      "v_pk_add_f32 v[234:235], v[234:235], v[192:193] neg_lo:[0,1] neg_hi:[0,1]\n\tv_pk_mul_f32 v[224:225], v[224:225], s[28:29]\n\t"
      "v_pk_mul_f32 v[226:227], v[226:227], s[28:29]\n\tv_pk_mul_f32 v[228:229], v[228:229], s[28:29]\n\tv_pk_mul_f32 v[230:231], v[230:231], s[28:29]\n\t"
      "v_pk_mul_f32 v[232:233], v[232:233], s[28:29]\n\tv_pk_mul_f32 v[234:235], v[234:235], s[28:29]\n\tv_exp_f32 v224, v224\n\tv_exp_f32 v225, v225\n\tv_exp_f32 v226, v226\n\t"
      "v_exp_f32 v227, v227\n\tv_exp_f32 v228, v228\n\tv_exp_f32 v229, v229\n\tv_exp_f32 v230, v230\n\tv_exp_f32 v231, v231\n\tv_exp_f32 v232, v232\n\tv_exp_f32 v233, v233\n\t"
      "v_exp_f32 v234, v234\n\tv_exp_f32 v235, v235\n\tv_pk_add_f32 v[192:193], v[224:225], v[226:227]\n\tv_pk_add_f32 v[194:195], v[228:229], v[230:231]\n\t"
      "v_pk_add_f32 v[196:197], v[232:233], v[234:235]\n\tv_pk_add_f32 v[194:195], v[194:195], v[196:197]\n\tv_add_f32 v192, v192, v193\n\tv_add_f32 v194, v194, v195\n\t"
      "v_fma_f32 v192, v192, v13, v194\n\tv_mov_b32 v193, v192\n\ts_nop 1\n\tv_permlane32_swap_b32 v192, v193\n\tv_add_f32 v192, v192, v193\n\tv_mov_b32 v193, v192\n\t"
      "s_nop 1\n\tv_permlane16_swap_b32 v192, v193\n\tv_add_f32 v192, v192, v193\n\tv_rcp_f32 v192, v192\n\ts_nop 0\n\tv_mov_b32 v193, v192\n\t"
      "v_pk_mul_f32 v[224:225], v[224:225], v[192:193]\n\tv_pk_mul_f32 v[226:227], v[226:227], v[192:193]\n\tv_pk_mul_f32 v[228:229], v[228:229], v[192:193]\n\t"
      "v_pk_mul_f32 v[230:231], v[230:231], v[192:193]\n\tv_pk_mul_f32 v[232:233], v[232:233], v[192:193]\n\tv_pk_mul_f32 v[234:235], v[234:235], v[192:193]\n\t"
      "v_cvt_pk_bf16_f32 v194, v224, v225\n\tv_cvt_pk_bf16_f32 v195, v226, v227\n\tv_cvt_pk_bf16_f32 v196, v228, v229\n\tv_cvt_pk_bf16_f32 v197, v230, v231\n\t"
      "v_cvt_pk_bf16_f32 v198, v232, v233\n\tv_cvt_pk_bf16_f32 v199, v234, v235\n\tbuffer_store_dwordx4 v[196:199], v10, s[8:11], s43 offen sc1\n\t"
      "buffer_store_dwordx2 v[194:195], v11, s[8:11], s43 offen sc1\n\tbuffer_store_dwordx4 v[204:207], v10, s[8:11], s44 offen sc1\n\t"
      "buffer_store_dwordx2 v[204:205], v11, s[8:11], s44 offen sc1\n\t"
      TK6
      "s_branch 69f\n\t"
      "60:\n\t"
      TK5
      "s_sleep 7\n\tv_mov_b32 v17, s17\n\tbuffer_store_dword v17, v12, s[12:15], 0 offen sc1\n\tbuffer_load_dword v18, v15, s[8:11], s41 offen sc1\n\t"
      "64:\n\ts_add_u32 s49, s17, 1\n\ts_cmp_eq_u32 s49, 64\n\ts_cbranch_scc1 69f\n\ts_add_u32 s48, s45, 0x1000\n\t"
      "61:\n\ts_cmp_gt_u32 s54, s49\n\ts_cbranch_scc1 62f\n\tbuffer_load_dword v17, off, s[12:15], s48 sc1\n\ts_waitcnt vmcnt(0)\n\tv_readfirstlane_b32 s54, v17\n\t"
      "s_cmp_eq_u32 s18, 0\n\ts_cbranch_scc1 62f\n\ts_sub_u32 s18, s18, 1\n\ts_sleep 1\n\ts_branch 61b\n\t"
      "62:\n\ts_add_u32 s46, s31, 0x1000\n\ts_mov_b32 m0, 147456\n\ts_add_u32 s49, s49, 1\n\tbuffer_load_dwordx4 v10, s[8:11], s31 offen offset:0 sc1 lds\n\t"
      "buffer_load_dwordx4 v10, s[8:11], s31 offen offset:1024 sc1 lds\n\tbuffer_load_dwordx4 v10, s[8:11], s31 offen offset:2048 sc1 lds\n\t"
      "buffer_load_dwordx4 v10, s[8:11], s31 offen offset:3072 sc1 lds\n\ts_mov_b32 m0, 151552\n\ts_nop 0\n\tbuffer_load_dwordx4 v10, s[8:11], s46 offen offset:0 sc1 lds\n\t"
      "buffer_load_dwordx4 v10, s[8:11], s46 offen offset:1024 sc1 lds\n\tbuffer_load_dwordx4 v10, s[8:11], s46 offen offset:2048 sc1 lds\n\t"
      "buffer_load_dwordx4 v10, s[8:11], s46 offen offset:3072 sc1 lds\n\t"
      "s_min_u32 s46, s55, s54\n\ts_cmp_gt_u32 s46, s49\n\ts_cbranch_scc1 63f\n\tbuffer_load_dword v17, off, s[12:15], s45 sc1\n\t"
      "buffer_load_dword v19, off, s[12:15], s48 sc1\n\ts_waitcnt vmcnt(0)\n\tds_write_b32 v16, v17 offset:4\n\tv_readfirstlane_b32 s54, v19\n\t"
      "63:\n\ts_cmp_lt_i32 s17, 0\n\ts_cbranch_scc1 69f\n\t"
      TK6
      "s_waitcnt vmcnt(8)\n\ts_branch 76f\n\t"
      "74:\n\tbuffer_load_dword v18, v15, s[8:11], s41 offen sc1\n\ts_waitcnt vmcnt(0)\n\t"
      "76:\n\tv_max_u32_dpp v18, v18, v18 row_ror:8 row_mask:0xf bank_mask:0xf\n\ts_nop 1\n\tv_max_u32_dpp v18, v18, v18 row_ror:4 row_mask:0xf bank_mask:0xf\n\ts_nop 1\n\t"
      "v_max_u32_dpp v18, v18, v18 row_ror:2 row_mask:0xf bank_mask:0xf\n\ts_nop 1\n\tv_max_u32_dpp v18, v18, v18 row_ror:1 row_mask:0xf bank_mask:0xf\n\ts_nop 0\n\t"
      "v_readlane_b32 s52, v18, 0\n\tv_readlane_b32 s48, v18, 16\n\ts_max_u32 s52, s52, s48\n\ts_cmp_lg_u32 s52, -1\n\ts_cbranch_scc1 75f\n\ts_cmp_eq_u32 s18, 0\n\t"
      "s_cbranch_scc1 75f\n\ts_sub_u32 s18, s18, 1\n\ts_branch 74b\n\t"
      "75:\n\ts_add_u32 s19, s52, s23\n\ts_branch 69f\n\t"
      "86:\n\t"
      STS("s_add_u32 s50, s50, 1\n\t")
      "s_waitcnt lgkmcnt(0)\n\tbuffer_load_dwordx4 v[240:243], v0, s[8:11], s68 offen sc1\n\tbuffer_load_dwordx4 v[244:247], v0, s[8:11], s69 offen sc1\n\t"
      "buffer_load_dwordx4 v[248:251], v0, s[8:11], s70 offen sc1\n\tbuffer_load_dwordx4 v[252:255], v0, s[8:11], s71 offen sc1\n\ts_nop 3\n\tv_mov_b64 v[224:225], 0\n\t"
      "v_mov_b64 v[226:227], 0\n\tv_mov_b64 v[228:229], 0\n\tv_mov_b64 v[230:231], 0\n\tv_mov_b64 v[232:233], 0\n\tv_mov_b64 v[234:235], 0\n\tv_mov_b64 v[236:237], 0\n\t"
      "v_mov_b64 v[238:239], 0\n\ts_nop 1\n\ts_branch 84f\n\t"
      "83:\n\tbuffer_load_dwordx4 v[240:243], v0, s[8:11], s68 offen sc1\n\tbuffer_load_dwordx4 v[244:247], v0, s[8:11], s69 offen sc1\n\t"
      "buffer_load_dwordx4 v[248:251], v0, s[8:11], s70 offen sc1\n\tbuffer_load_dwordx4 v[252:255], v0, s[8:11], s71 offen sc1\n\t"
      "84:\n\ts_waitcnt vmcnt(0)\n\tv_max3_u32 v17, v240, v241, v242\n\tv_max_u32 v17, v17, v243\n\tv_max3_u32 v17, v17, v244, v245\n\tv_max3_u32 v17, v17, v246, v247\n\t"
      "v_max3_u32 v17, v17, v248, v249\n\tv_max3_u32 v17, v17, v250, v251\n\tv_max3_u32 v17, v17, v252, v253\n\tv_max3_u32 v17, v17, v254, v255\n\tv_cmp_eq_u32 vcc, -1, v17\n\t"
      "s_cbranch_vccz 85f\n\ts_cmp_eq_u32 s18, 0\n\ts_cbranch_scc1 85f\n\ts_sub_u32 s18, s18, 1\n\ts_branch 83b\n\t"
      "85:\n\tv_smfmac_f32_16x16x64_bf16 v[224:227], v[240:243], a[0:7], v6\n\tv_smfmac_f32_16x16x64_bf16 v[228:231], v[240:243], a[8:15], v6\n\t"
      "v_smfmac_f32_16x16x64_bf16 v[232:235], v[240:243], v[32:39], v6\n\tv_smfmac_f32_16x16x64_bf16 v[236:239], v[240:243], v[160:167], v6\n\t"
      "v_smfmac_f32_16x16x64_bf16 v[224:227], v[244:247], a[16:23], v6\n\tv_smfmac_f32_16x16x64_bf16 v[228:231], v[244:247], a[24:31], v6\n\t"
      "ds_write_b128 v1, v[240:243] offset:0\n\tds_write_b128 v1, v[244:247] offset:1024\n\tds_write_b128 v1, v[248:251] offset:2048\n\t"
      "ds_write_b128 v1, v[252:255] offset:3072\n\tv_mov_b32 v17, 0\n\ts_mov_b64 vcc, 0\n\ts_waitcnt lgkmcnt(0)\n\ts_cmp_lt_u32 s16, 2\n\ts_cbranch_scc1 20b\n\ts_branch 24b\n\t"
      "87:\n\t"
      STS("s_add_u32 s50, s50, 1\n\t")
      "s_waitcnt lgkmcnt(0)\n\tbuffer_load_dwordx4 v[244:247], v0, s[8:11], s69 offen sc1\n\tbuffer_load_dwordx4 v[248:251], v0, s[8:11], s70 offen sc1\n\t"
      "buffer_load_dwordx4 v[252:255], v0, s[8:11], s71 offen sc1\n\ts_nop 3\n\tv_mov_b64 v[224:225], 0\n\tv_mov_b64 v[226:227], 0\n\tv_mov_b64 v[228:229], 0\n\t"
      "v_mov_b64 v[230:231], 0\n\tv_mov_b64 v[232:233], 0\n\tv_mov_b64 v[234:235], 0\n\tv_mov_b64 v[236:237], 0\n\tv_mov_b64 v[238:239], 0\n\ts_nop 1\n\t"
      "v_smfmac_f32_16x16x64_bf16 v[224:227], v[240:243], a[0:7], v6\n\tv_smfmac_f32_16x16x64_bf16 v[228:231], v[240:243], a[8:15], v6\n\t"
      "v_smfmac_f32_16x16x64_bf16 v[232:235], v[240:243], v[32:39], v6\n\tv_smfmac_f32_16x16x64_bf16 v[236:239], v[240:243], v[160:167], v6\n\ts_branch 84f\n\t"
      "83:\n\tbuffer_load_dwordx4 v[244:247], v0, s[8:11], s69 offen sc1\n\tbuffer_load_dwordx4 v[248:251], v0, s[8:11], s70 offen sc1\n\t"
      "buffer_load_dwordx4 v[252:255], v0, s[8:11], s71 offen sc1\n\t"
      "84:\n\ts_waitcnt vmcnt(0)\n\tv_max3_u32 v17, v244, v245, v246\n\tv_max_u32 v17, v17, v247\n\tv_max3_u32 v17, v17, v248, v249\n\tv_max3_u32 v17, v17, v250, v251\n\t"
      "v_max3_u32 v17, v17, v252, v253\n\tv_max3_u32 v17, v17, v254, v255\n\tv_cmp_eq_u32 vcc, -1, v17\n\ts_cbranch_vccz 85f\n\ts_cmp_eq_u32 s18, 0\n\ts_cbranch_scc1 85f\n\t"
      "s_sub_u32 s18, s18, 1\n\ts_branch 83b\n\t"
      "85:\n\tv_smfmac_f32_16x16x64_bf16 v[224:227], v[244:247], a[16:23], v6\n\tv_smfmac_f32_16x16x64_bf16 v[228:231], v[244:247], a[24:31], v6\n\t"
      "v_smfmac_f32_16x16x64_bf16 v[232:235], v[244:247], v[40:47], v6\n\tv_smfmac_f32_16x16x64_bf16 v[236:239], v[244:247], v[168:175], v6\n\t"
      "v_smfmac_f32_16x16x64_bf16 v[224:227], v[248:251], a[32:39], v6\n\tv_smfmac_f32_16x16x64_bf16 v[228:231], v[248:251], a[40:47], v6\n\t"
      "ds_write_b128 v1, v[244:247] offset:1024\n\tds_write_b128 v1, v[248:251] offset:2048\n\tds_write_b128 v1, v[252:255] offset:3072\n\tv_mov_b32 v17, 0\n\t"
      "s_mov_b64 vcc, 0\n\ts_waitcnt lgkmcnt(0)\n\ts_cmp_lt_u32 s16, 2\n\ts_cbranch_scc1 21b\n\ts_branch 25b\n\t"
      "88:\n\t"
      STS("s_add_u32 s50, s50, 1\n\t")
      "s_waitcnt lgkmcnt(0)\n\tbuffer_load_dwordx4 v[248:251], v0, s[8:11], s70 offen sc1\n\tbuffer_load_dwordx4 v[252:255], v0, s[8:11], s71 offen sc1\n\ts_nop 3\n\t"
      "v_mov_b64 v[224:225], 0\n\tv_mov_b64 v[226:227], 0\n\tv_mov_b64 v[228:229], 0\n\tv_mov_b64 v[230:231], 0\n\tv_mov_b64 v[232:233], 0\n\tv_mov_b64 v[234:235], 0\n\t"
      "v_mov_b64 v[236:237], 0\n\tv_mov_b64 v[238:239], 0\n\ts_nop 1\n\tv_smfmac_f32_16x16x64_bf16 v[224:227], v[240:243], a[0:7], v6\n\t"
      "v_smfmac_f32_16x16x64_bf16 v[228:231], v[240:243], a[8:15], v6\n\tv_smfmac_f32_16x16x64_bf16 v[232:235], v[240:243], v[32:39], v6\n\t"
      "v_smfmac_f32_16x16x64_bf16 v[236:239], v[240:243], v[160:167], v6\n\tv_smfmac_f32_16x16x64_bf16 v[224:227], v[244:247], a[16:23], v6\n\t"
      "v_smfmac_f32_16x16x64_bf16 v[228:231], v[244:247], a[24:31], v6\n\tv_smfmac_f32_16x16x64_bf16 v[232:235], v[244:247], v[40:47], v6\n\t"
      "v_smfmac_f32_16x16x64_bf16 v[236:239], v[244:247], v[168:175], v6\n\ts_branch 84f\n\t"
      "83:\n\tbuffer_load_dwordx4 v[248:251], v0, s[8:11], s70 offen sc1\n\tbuffer_load_dwordx4 v[252:255], v0, s[8:11], s71 offen sc1\n\t"
      "84:\n\ts_waitcnt vmcnt(0)\n\tv_max3_u32 v17, v248, v249, v250\n\tv_max_u32 v17, v17, v251\n\tv_max3_u32 v17, v17, v252, v253\n\tv_max3_u32 v17, v17, v254, v255\n\t"
      "v_cmp_eq_u32 vcc, -1, v17\n\ts_cbranch_vccz 85f\n\ts_cmp_eq_u32 s18, 0\n\ts_cbranch_scc1 85f\n\ts_sub_u32 s18, s18, 1\n\ts_branch 83b\n\t"
      "85:\n\tv_smfmac_f32_16x16x64_bf16 v[224:227], v[248:251], a[32:39], v6\n\tv_smfmac_f32_16x16x64_bf16 v[228:231], v[248:251], a[40:47], v6\n\t"
      "v_smfmac_f32_16x16x64_bf16 v[232:235], v[248:251], v[48:55], v6\n\tv_smfmac_f32_16x16x64_bf16 v[236:239], v[248:251], v[176:183], v6\n\t"
      "v_smfmac_f32_16x16x64_bf16 v[224:227], v[252:255], a[48:55], v6\n\tv_smfmac_f32_16x16x64_bf16 v[228:231], v[252:255], a[56:63], v6\n\t"
      "ds_write_b128 v1, v[248:251] offset:2048\n\tds_write_b128 v1, v[252:255] offset:3072\n\tv_mov_b32 v17, 0\n\ts_mov_b64 vcc, 0\n\ts_waitcnt lgkmcnt(0)\n\t"
      "s_cmp_lt_u32 s16, 2\n\ts_cbranch_scc1 22b\n\ts_branch 26b\n\t"
      "89:\n\t"
      STS("s_add_u32 s50, s50, 1\n\t")
      "s_waitcnt lgkmcnt(0)\n\tbuffer_load_dwordx4 v[252:255], v0, s[8:11], s71 offen sc1\n\ts_nop 3\n\tv_mov_b64 v[224:225], 0\n\tv_mov_b64 v[226:227], 0\n\t"
      "v_mov_b64 v[228:229], 0\n\tv_mov_b64 v[230:231], 0\n\tv_mov_b64 v[232:233], 0\n\tv_mov_b64 v[234:235], 0\n\tv_mov_b64 v[236:237], 0\n\tv_mov_b64 v[238:239], 0\n\t"
      "s_nop 1\n\tv_smfmac_f32_16x16x64_bf16 v[224:227], v[240:243], a[0:7], v6\n\tv_smfmac_f32_16x16x64_bf16 v[228:231], v[240:243], a[8:15], v6\n\t"
      "v_smfmac_f32_16x16x64_bf16 v[232:235], v[240:243], v[32:39], v6\n\tv_smfmac_f32_16x16x64_bf16 v[236:239], v[240:243], v[160:167], v6\n\t"
      "v_smfmac_f32_16x16x64_bf16 v[224:227], v[244:247], a[16:23], v6\n\tv_smfmac_f32_16x16x64_bf16 v[228:231], v[244:247], a[24:31], v6\n\t"
      "v_smfmac_f32_16x16x64_bf16 v[232:235], v[244:247], v[40:47], v6\n\tv_smfmac_f32_16x16x64_bf16 v[236:239], v[244:247], v[168:175], v6\n\t"
      "v_smfmac_f32_16x16x64_bf16 v[224:227], v[248:251], a[32:39], v6\n\tv_smfmac_f32_16x16x64_bf16 v[228:231], v[248:251], a[40:47], v6\n\t"
      "v_smfmac_f32_16x16x64_bf16 v[232:235], v[248:251], v[48:55], v6\n\tv_smfmac_f32_16x16x64_bf16 v[236:239], v[248:251], v[176:183], v6\n\ts_branch 84f\n\t"
      "83:\n\tbuffer_load_dwordx4 v[252:255], v0, s[8:11], s71 offen sc1\n\t"
      "84:\n\ts_waitcnt vmcnt(0)\n\tv_max3_u32 v17, v252, v253, v254\n\tv_max_u32 v17, v17, v255\n\tv_cmp_eq_u32 vcc, -1, v17\n\ts_cbranch_vccz 85f\n\ts_cmp_eq_u32 s18, 0\n\t"
      "s_cbranch_scc1 85f\n\ts_sub_u32 s18, s18, 1\n\ts_branch 83b\n\t"
      "85:\n\tv_smfmac_f32_16x16x64_bf16 v[224:227], v[252:255], a[48:55], v6\n\tv_smfmac_f32_16x16x64_bf16 v[228:231], v[252:255], a[56:63], v6\n\t"
      "v_smfmac_f32_16x16x64_bf16 v[232:235], v[252:255], v[56:63], v6\n\tv_smfmac_f32_16x16x64_bf16 v[236:239], v[252:255], v[184:191], v6\n\t"
      "v_smfmac_f32_16x16x64_bf16 v[224:227], v[20:23], a[64:71], v6\n\tv_smfmac_f32_16x16x64_bf16 v[228:231], v[20:23], a[72:79], v6\n\t"
      "ds_write_b128 v1, v[252:255] offset:3072\n\tv_mov_b32 v17, 0\n\ts_mov_b64 vcc, 0\n\ts_waitcnt lgkmcnt(0)\n\ts_cmp_lt_u32 s16, 2\n\ts_cbranch_scc1 23b\n\ts_branch 27b\n\t"
      "69:\n\t"
      TK7
      STS("s_cmp_lt_i32 s17, 8\n\ts_cselect_b32 s50, 0, s50\n\ts_cselect_b32 s51, 0, s51\n\ts_cselect_b32 s61, 0, s61\n\t")
      TKS("s_waitcnt lgkmcnt(0)\n\ts_cmp_lt_i32 s17, 0\n\ts_cbranch_scc1 98f\n\ts_cmp_lg_u32 s16, s89\n\ts_cbranch_scc1 98f\n\ts_mov_b64 exec, 1\n\ts_lshl_b32 s48, s17, 4\n\ts_add_u32 s48, s48, 161056\n\tv_mov_b32 v17, s48\n\ts_sub_u32 s48, s72, s88\n\ts_min_u32 s48, s48, 0xffff\n\tv_mov_b32 v18, s48\n\tds_write_b16 v17, v18 offset:0\n\ts_sub_u32 s48, s74, s72\n\ts_min_u32 s48, s48, 0xffff\n\tv_mov_b32 v18, s48\n\tds_write_b16 v17, v18 offset:2\n\ts_sub_u32 s48, s76, s74\n\ts_min_u32 s48, s48, 0xffff\n\tv_mov_b32 v18, s48\n\tds_write_b16 v17, v18 offset:4\n\ts_sub_u32 s48, s78, s76\n\ts_min_u32 s48, s48, 0xffff\n\tv_mov_b32 v18, s48\n\tds_write_b16 v17, v18 offset:6\n\ts_sub_u32 s48, s80, s78\n\ts_min_u32 s48, s48, 0xffff\n\tv_mov_b32 v18, s48\n\tds_write_b16 v17, v18 offset:8\n\ts_sub_u32 s48, s82, s80\n\ts_min_u32 s48, s48, 0xffff\n\tv_mov_b32 v18, s48\n\tds_write_b16 v17, v18 offset:10\n\ts_sub_u32 s48, s84, s82\n\ts_min_u32 s48, s48, 0xffff\n\tv_mov_b32 v18, s48\n\tds_write_b16 v17, v18 offset:12\n\ts_sub_u32 s48, s86, s84\n\ts_min_u32 s48, s48, 0xffff\n\tv_mov_b32 v18, s48\n\tds_write_b16 v17, v18 offset:14\n\ts_mov_b64 exec, -1\n\t98:\n\ts_mov_b32 s88, s86\n\t")
      "s_add_u32 s48, s22, 0x4000\n\ts_and_b32 s48, s48, 0x3c000\n\ts_andn2_b32 s22, s22, 0x3c000\n\ts_or_b32 s22, s22, s48\n\ts_add_u32 s48, s31, 0x4000\n\t"
      "s_and_b32 s48, s48, 0x3c000\n\ts_andn2_b32 s31, s31, 0x3c000\n\ts_or_b32 s31, s31, s48\n\ts_add_u32 s17, s17, 1\n\ts_cmp_lt_u32 s17, 64\n\ts_cbranch_scc1 10b\n\t"
      "s_waitcnt vmcnt(0) lgkmcnt(0)\n\t"
      TKS("s_mul_i32 s48, s16, 72\n\ts_add_u32 s48, s48, 162080\n\tv_mov_b32 v224, s48\n\tds_read_b32 v225, v224 offset:0\n\ts_waitcnt lgkmcnt(0)\n\tv_readfirstlane_b32 s72, v225\n\tds_read_b32 v225, v224 offset:4\n\ts_waitcnt lgkmcnt(0)\n\tv_readfirstlane_b32 s73, v225\n\tds_read_b32 v225, v224 offset:8\n\ts_waitcnt lgkmcnt(0)\n\tv_readfirstlane_b32 s74, v225\n\tds_read_b32 v225, v224 offset:12\n\ts_waitcnt lgkmcnt(0)\n\tv_readfirstlane_b32 s75, v225\n\tds_read_b32 v225, v224 offset:16\n\ts_waitcnt lgkmcnt(0)\n\tv_readfirstlane_b32 s76, v225\n\tds_read_b32 v225, v224 offset:20\n\ts_waitcnt lgkmcnt(0)\n\tv_readfirstlane_b32 s77, v225\n\tds_read_b32 v225, v224 offset:24\n\ts_waitcnt lgkmcnt(0)\n\tv_readfirstlane_b32 s78, v225\n\tds_read_b32 v225, v224 offset:28\n\ts_waitcnt lgkmcnt(0)\n\tv_readfirstlane_b32 s79, v225\n\tds_read_b32 v225, v224 offset:32\n\ts_waitcnt lgkmcnt(0)\n\tv_readfirstlane_b32 s80, v225\n\tds_read_b32 v225, v224 offset:36\n\ts_waitcnt lgkmcnt(0)\n\tv_readfirstlane_b32 s81, v225\n\tds_read_b32 v225, v224 offset:40\n\ts_waitcnt lgkmcnt(0)\n\tv_readfirstlane_b32 s82, v225\n\tds_read_b32 v225, v224 offset:44\n\ts_waitcnt lgkmcnt(0)\n\tv_readfirstlane_b32 s83, v225\n\tds_read_b32 v225, v224 offset:48\n\ts_waitcnt lgkmcnt(0)\n\tv_readfirstlane_b32 s84, v225\n\tds_read_b32 v225, v224 offset:52\n\ts_waitcnt lgkmcnt(0)\n\tv_readfirstlane_b32 s85, v225\n\tds_read_b32 v225, v224 offset:56\n\ts_waitcnt lgkmcnt(0)\n\tv_readfirstlane_b32 s86, v225\n\tds_read_b32 v225, v224 offset:60\n\ts_waitcnt lgkmcnt(0)\n\tv_readfirstlane_b32 s87, v225\n\tds_read_b32 v225, v224 offset:64\n\ts_waitcnt lgkmcnt(0)\n\tv_readfirstlane_b32 s88, v225\n\tds_read_b32 v225, v224 offset:68\n\ts_waitcnt lgkmcnt(0)\n\tv_readfirstlane_b32 s89, v225\n\t")
      STS("s_lshl_b32 s48, s16, 2\n\ts_add_u32 s48, s48, 162944\n\tv_mov_b32 v0, s48\n\tv_mov_b32 v1, s50\n\tds_write_b32 v0, v1\n\ts_cmp_lg_u32 s16, 0\n\ts_cbranch_scc1 97f\n\tv_mov_b32 v1, s51\n\tds_write_b32 v0, v1 offset:16\n\t97:\n\ts_cmp_lg_u32 s16, 3\n\ts_cbranch_scc1 95f\n\tv_mov_b32 v1, s61\n\tds_write_b32 v0, v1 offset:8\n\tv_mov_b32 v1, s21\n\tds_write_b32 v0, v1 offset:12\n\tv_mov_b32 v1, s22\n\tds_write_b32 v0, v1 offset:16\n\t95:\n\t")
      "s_cmp_lg_u32 s18, 0\n\ts_cbranch_scc1 96f\n\tv_mov_b32 v1, 1\n\tbuffer_store_dword v1, off, s[12:15], 0 offset:3328 sc1\n\t"
      "96:\n\t"
      : "+{v[0:31]}"(k), "+{v[160:191]}"(s0), "+{v[192:223]}"(s1), ALL_REGS :: "memory", "vcc", "scc", S_CLOB TK_CLOB);
}

DEV void vo_role(Ctx& c, int h) {
  const KArg p = enter(c);
  constexpr int WL = 0, MEMS = 131072, VM = 147456, VROW = 272;
  c.budget = SPIN_CAP;
  const int w = c.w, lane = fresh_lane(), g = lane >> 4, cc = lane & 15, q = (lane >> 2) & 3, pp = lane & 3;
  v32u k;
  k[0] = amap(w * 4096 + lane * 16, 0);
  k[1] = MEMS + w * 4096 + lane * 16;
#pragma unroll
  for (int r = 0; r < 3; ++r) k[2 + r] = MEMS + ((w + r + 1) & 3) * 4096 + lane * 16;
  k[5] = WL + w * 32768 + lane * 16;
  k[6] = (lane & 1) ? 0xEEEE : 0x4444;
  k[7] = VM + 2 * g * VROW + (32 * w + 2 * cc) * 2;
  k[8] = VM + (cc & 7) * VROW + 16 * g;
  k[9] = R_U + (16 * w + (g >> 1)) * 16384 + h * 256 + cc * 16 + (g & 1) * 8;
  k[10] = 0;
  k[11] = amap(24576 + (h >> 3) * 4096 + (lane & 31) * 128, 0);
  k[12] = (lane & 8) ? 0xEEEE : 0x4444;
#pragma unroll
  for (int i = 0; i < 4; ++i) k[14 + i] = g * 8192 + h * 256 + 16 * (cc ^ (2 * (g & 1) + 4 * i));
#pragma unroll
  for (int i = 0; i < 2; ++i) k[18 + i] = g * 2048 + w * 512 + 16 * (cc ^ (4 * (g >> 1) + 8 * i));
#pragma unroll
  for (int i = 0; i < 8; ++i)
    k[20 + i] = WL + ((w + (i >> 1)) & 3) * 32768 + (8 * (q >> 1) + (q & 1) + krow_lane(g)) * 256 + 16 * ((4 * w + 2 * (i & 1) + (pp >> 1)) ^ (2 * (q & 1) + 4 * (g & 1) + 8 * (q >> 1))) + 8 * (pp & 1);
#pragma unroll
  for (int i = 0; i < 4; ++i) k[28 + i] = WL + w * 32768 + (16 * (q & 1) + 2 * g) * 256 + 16 * ((4 * i + 2 * (q >> 1) + (pp >> 1)) ^ (4 * (g & 1) + 8 * (q & 1))) + 8 * (pp & 1);
  k[13] = 0;
  asm volatile(
      "s_add_u32 s50, s56, s64\n\ts_add_u32 m0, s49, 0\n\ts_add_u32 s48, s50, 0x0\n\tbuffer_load_dwordx4 v14, s[40:43], s48 offen lds\n\ts_add_u32 m0, s49, 1024\n\t"
      "s_add_u32 s48, s50, 0x8000\n\tbuffer_load_dwordx4 v15, s[40:43], s48 offen lds\n\ts_add_u32 m0, s49, 2048\n\ts_add_u32 s48, s50, 0x10000\n\t"
      "buffer_load_dwordx4 v16, s[40:43], s48 offen lds\n\ts_add_u32 m0, s49, 3072\n\ts_add_u32 s48, s50, 0x18000\n\tbuffer_load_dwordx4 v17, s[40:43], s48 offen lds\n\t"
      "s_add_u32 m0, s49, 4096\n\ts_add_u32 s48, s50, 0x20000\n\tbuffer_load_dwordx4 v14, s[40:43], s48 offen lds\n\ts_add_u32 m0, s49, 5120\n\ts_add_u32 s48, s50, 0x28000\n\t"
      "buffer_load_dwordx4 v15, s[40:43], s48 offen lds\n\ts_add_u32 m0, s49, 6144\n\ts_add_u32 s48, s50, 0x30000\n\tbuffer_load_dwordx4 v16, s[40:43], s48 offen lds\n\t"
      "s_add_u32 m0, s49, 7168\n\ts_add_u32 s48, s50, 0x38000\n\tbuffer_load_dwordx4 v17, s[40:43], s48 offen lds\n\ts_add_u32 s50, s56, s64\n\ts_add_u32 m0, s49, 8192\n\t"
      "s_add_u32 s48, s50, 0x40000\n\tbuffer_load_dwordx4 v14, s[40:43], s48 offen lds\n\ts_add_u32 m0, s49, 9216\n\ts_add_u32 s48, s50, 0x48000\n\t"
      "buffer_load_dwordx4 v15, s[40:43], s48 offen lds\n\ts_add_u32 m0, s49, 10240\n\ts_add_u32 s48, s50, 0x50000\n\tbuffer_load_dwordx4 v16, s[40:43], s48 offen lds\n\t"
      "s_add_u32 m0, s49, 11264\n\ts_add_u32 s48, s50, 0x58000\n\tbuffer_load_dwordx4 v17, s[40:43], s48 offen lds\n\ts_add_u32 m0, s49, 12288\n\ts_add_u32 s48, s50, 0x60000\n\t"
      "buffer_load_dwordx4 v14, s[40:43], s48 offen lds\n\ts_add_u32 m0, s49, 13312\n\ts_add_u32 s48, s50, 0x68000\n\tbuffer_load_dwordx4 v15, s[40:43], s48 offen lds\n\t"
      "s_add_u32 m0, s49, 14336\n\ts_add_u32 s48, s50, 0x70000\n\tbuffer_load_dwordx4 v16, s[40:43], s48 offen lds\n\ts_add_u32 m0, s49, 15360\n\ts_add_u32 s48, s50, 0x78000\n\t"
      "buffer_load_dwordx4 v17, s[40:43], s48 offen lds\n\ts_add_u32 s50, s56, s65\n\ts_add_u32 m0, s49, 16384\n\ts_add_u32 s48, s50, 0x0\n\t"
      "buffer_load_dwordx4 v14, s[40:43], s48 offen lds\n\ts_add_u32 m0, s49, 17408\n\ts_add_u32 s48, s50, 0x8000\n\tbuffer_load_dwordx4 v15, s[40:43], s48 offen lds\n\t"
      "s_add_u32 m0, s49, 18432\n\ts_add_u32 s48, s50, 0x10000\n\tbuffer_load_dwordx4 v16, s[40:43], s48 offen lds\n\ts_add_u32 m0, s49, 19456\n\ts_add_u32 s48, s50, 0x18000\n\t"
      "buffer_load_dwordx4 v17, s[40:43], s48 offen lds\n\ts_add_u32 m0, s49, 20480\n\ts_add_u32 s48, s50, 0x20000\n\tbuffer_load_dwordx4 v14, s[40:43], s48 offen lds\n\t"
      "s_add_u32 m0, s49, 21504\n\ts_add_u32 s48, s50, 0x28000\n\tbuffer_load_dwordx4 v15, s[40:43], s48 offen lds\n\ts_add_u32 m0, s49, 22528\n\ts_add_u32 s48, s50, 0x30000\n\t"
      "buffer_load_dwordx4 v16, s[40:43], s48 offen lds\n\ts_add_u32 m0, s49, 23552\n\ts_add_u32 s48, s50, 0x38000\n\tbuffer_load_dwordx4 v17, s[40:43], s48 offen lds\n\t"
      "s_add_u32 s50, s56, s65\n\ts_add_u32 m0, s49, 24576\n\ts_add_u32 s48, s50, 0x40000\n\tbuffer_load_dwordx4 v14, s[40:43], s48 offen lds\n\ts_add_u32 m0, s49, 25600\n\t"
      "s_add_u32 s48, s50, 0x48000\n\tbuffer_load_dwordx4 v15, s[40:43], s48 offen lds\n\ts_add_u32 m0, s49, 26624\n\ts_add_u32 s48, s50, 0x50000\n\t"
      "buffer_load_dwordx4 v16, s[40:43], s48 offen lds\n\ts_add_u32 m0, s49, 27648\n\ts_add_u32 s48, s50, 0x58000\n\tbuffer_load_dwordx4 v17, s[40:43], s48 offen lds\n\t"
      "s_add_u32 m0, s49, 28672\n\ts_add_u32 s48, s50, 0x60000\n\tbuffer_load_dwordx4 v14, s[40:43], s48 offen lds\n\ts_add_u32 m0, s49, 29696\n\ts_add_u32 s48, s50, 0x68000\n\t"
      "buffer_load_dwordx4 v15, s[40:43], s48 offen lds\n\ts_add_u32 m0, s49, 30720\n\ts_add_u32 s48, s50, 0x70000\n\tbuffer_load_dwordx4 v16, s[40:43], s48 offen lds\n\t"
      "s_add_u32 m0, s49, 31744\n\ts_add_u32 s48, s50, 0x78000\n\tbuffer_load_dwordx4 v17, s[40:43], s48 offen lds\n\t"
      :: "{v[14:17]}"(v4u{k[14], k[15], k[16], k[17]}), "{s[40:43]}"(mkrs(p->Wv, 1024 * 4096 * 2)), "{s49}"(WL + w * 32768), "{s56}"(w << 21), "{s[64:67]}"(v4u{ROTS(h >> 3)})
      : "memory", "scc", "s48", "s50");
  claim(c, 0);
  v32u s0 = {RSRCS, (unsigned)w, 0, c.budget, 0, CV0 * 16, PACE};
  v32u s1 = {plo(p->Wv), phi(p->Wv), 1024 * 4096 * 2, 0x00020000, plo(p->Wo), phi(p->Wo), 1024 * 4096 * 2, 0x00020000, 0, (unsigned)(WL + w * 32768), 0, 0, 0, 0, 0, 0,
             (unsigned)(w << 21), (unsigned)(h << 18), 0, 0, plo((const void*)p), phi((const void*)p), c.att ? 0u : (unsigned)c.b + 256u, (unsigned)(C_VERDICT + c.att * 128), ROTS(h >> 3)};
  v32u wa[8], wv[5];
  asm volatile(
      "s_nop 0\n\tv_readfirstlane_b32 s8, v160\n\tv_readfirstlane_b32 s9, v161\n\tv_readfirstlane_b32 s10, v162\n\tv_readfirstlane_b32 s11, v163\n\t"
      "v_readfirstlane_b32 s12, v164\n\tv_readfirstlane_b32 s13, v165\n\tv_readfirstlane_b32 s14, v166\n\tv_readfirstlane_b32 s15, v167\n\tv_readfirstlane_b32 s16, v168\n\t"
      "v_readfirstlane_b32 s17, v169\n\tv_readfirstlane_b32 s18, v170\n\tv_readfirstlane_b32 s19, v171\n\tv_readfirstlane_b32 s20, v172\n\tv_readfirstlane_b32 s21, v173\n\t"
      "v_readfirstlane_b32 s22, v174\n\tv_readfirstlane_b32 s23, v175\n\tv_readfirstlane_b32 s24, v176\n\tv_readfirstlane_b32 s25, v177\n\tv_readfirstlane_b32 s26, v178\n\t"
      "v_readfirstlane_b32 s40, v192\n\tv_readfirstlane_b32 s41, v193\n\tv_readfirstlane_b32 s42, v194\n\tv_readfirstlane_b32 s43, v195\n\tv_readfirstlane_b32 s44, v196\n\t"
      "v_readfirstlane_b32 s45, v197\n\tv_readfirstlane_b32 s46, v198\n\tv_readfirstlane_b32 s47, v199\n\tv_readfirstlane_b32 s49, v201\n\tv_readfirstlane_b32 s56, v208\n\t"
      "v_readfirstlane_b32 s57, v209\n\tv_readfirstlane_b32 s60, v212\n\tv_readfirstlane_b32 s61, v213\n\tv_readfirstlane_b32 s62, v214\n\tv_readfirstlane_b32 s63, v215\n\t"
      "v_readfirstlane_b32 s64, v216\n\tv_readfirstlane_b32 s65, v217\n\tv_readfirstlane_b32 s66, v218\n\tv_readfirstlane_b32 s67, v219\n\ts_nop 4\n\t"
      TKS("s_mul_i32 s48, s16, 72\n\ts_add_u32 s48, s48, 162080\n\tv_mov_b32 v224, s48\n\tv_mov_b32 v225, s72\n\tds_write_b32 v224, v225 offset:0\n\tv_mov_b32 v225, s73\n\tds_write_b32 v224, v225 offset:4\n\tv_mov_b32 v225, s74\n\tds_write_b32 v224, v225 offset:8\n\tv_mov_b32 v225, s75\n\tds_write_b32 v224, v225 offset:12\n\tv_mov_b32 v225, s76\n\tds_write_b32 v224, v225 offset:16\n\tv_mov_b32 v225, s77\n\tds_write_b32 v224, v225 offset:20\n\tv_mov_b32 v225, s78\n\tds_write_b32 v224, v225 offset:24\n\tv_mov_b32 v225, s79\n\tds_write_b32 v224, v225 offset:28\n\tv_mov_b32 v225, s80\n\tds_write_b32 v224, v225 offset:32\n\tv_mov_b32 v225, s81\n\tds_write_b32 v224, v225 offset:36\n\tv_mov_b32 v225, s82\n\tds_write_b32 v224, v225 offset:40\n\tv_mov_b32 v225, s83\n\tds_write_b32 v224, v225 offset:44\n\tv_mov_b32 v225, s84\n\tds_write_b32 v224, v225 offset:48\n\tv_mov_b32 v225, s85\n\tds_write_b32 v224, v225 offset:52\n\tv_mov_b32 v225, s86\n\tds_write_b32 v224, v225 offset:56\n\tv_mov_b32 v225, s87\n\tds_write_b32 v224, v225 offset:60\n\tv_mov_b32 v225, s88\n\tds_write_b32 v224, v225 offset:64\n\tv_mov_b32 v225, s89\n\tds_write_b32 v224, v225 offset:68\n\t")
      "s_waitcnt vmcnt(24)\n\ts_barrier\n\tds_read_b64_tr_b16 a[0:1], v20 offset:0\n\tds_read_b64_tr_b16 a[2:3], v20 offset:512\n\tds_read_b64_tr_b16 a[8:9], v21 offset:0\n\t"
      "ds_read_b64_tr_b16 a[10:11], v21 offset:512\n\tds_read_b64_tr_b16 a[48:49], v22 offset:0\n\tds_read_b64_tr_b16 a[50:51], v22 offset:512\n\t"
      "ds_read_b64_tr_b16 a[56:57], v23 offset:0\n\tds_read_b64_tr_b16 a[58:59], v23 offset:512\n\tds_read_b64_tr_b16 a[64:65], v24 offset:0\n\t"
      "ds_read_b64_tr_b16 a[66:67], v24 offset:512\n\tds_read_b64_tr_b16 v[208:209], v25 offset:0\n\tds_read_b64_tr_b16 v[210:211], v25 offset:512\n\t"
      "ds_read_b64_tr_b16 a[72:73], v26 offset:0\n\tds_read_b64_tr_b16 a[74:75], v26 offset:512\n\tds_read_b64_tr_b16 a[80:81], v27 offset:0\n\t"
      "ds_read_b64_tr_b16 a[82:83], v27 offset:512\n\ts_waitcnt lgkmcnt(0)\n\ts_barrier\n\ts_add_u32 s50, s56, s66\n\ts_add_u32 m0, s49, 0\n\ts_add_u32 s48, s50, 0x0\n\t"
      "buffer_load_dwordx4 v14, s[40:43], s48 offen lds\n\ts_add_u32 m0, s49, 1024\n\ts_add_u32 s48, s50, 0x8000\n\tbuffer_load_dwordx4 v15, s[40:43], s48 offen lds\n\t"
      "s_add_u32 m0, s49, 2048\n\ts_add_u32 s48, s50, 0x10000\n\tbuffer_load_dwordx4 v16, s[40:43], s48 offen lds\n\ts_add_u32 m0, s49, 3072\n\ts_add_u32 s48, s50, 0x18000\n\t"
      "buffer_load_dwordx4 v17, s[40:43], s48 offen lds\n\ts_add_u32 m0, s49, 4096\n\ts_add_u32 s48, s50, 0x20000\n\tbuffer_load_dwordx4 v14, s[40:43], s48 offen lds\n\t"
      "s_add_u32 m0, s49, 5120\n\ts_add_u32 s48, s50, 0x28000\n\tbuffer_load_dwordx4 v15, s[40:43], s48 offen lds\n\ts_add_u32 m0, s49, 6144\n\ts_add_u32 s48, s50, 0x30000\n\t"
      "buffer_load_dwordx4 v16, s[40:43], s48 offen lds\n\ts_add_u32 m0, s49, 7168\n\ts_add_u32 s48, s50, 0x38000\n\tbuffer_load_dwordx4 v17, s[40:43], s48 offen lds\n\t"
      "s_waitcnt vmcnt(24)\n\ts_barrier\n\tds_read_b64_tr_b16 a[4:5], v20 offset:8192\n\tds_read_b64_tr_b16 a[6:7], v20 offset:8704\n\t"
      "ds_read_b64_tr_b16 a[12:13], v21 offset:8192\n\tds_read_b64_tr_b16 a[14:15], v21 offset:8704\n\tds_read_b64_tr_b16 a[52:53], v22 offset:8192\n\t"
      "ds_read_b64_tr_b16 a[54:55], v22 offset:8704\n\tds_read_b64_tr_b16 a[60:61], v23 offset:8192\n\tds_read_b64_tr_b16 a[62:63], v23 offset:8704\n\t"
      "ds_read_b64_tr_b16 a[68:69], v24 offset:8192\n\tds_read_b64_tr_b16 a[70:71], v24 offset:8704\n\tds_read_b64_tr_b16 v[212:213], v25 offset:8192\n\t"
      "ds_read_b64_tr_b16 v[214:215], v25 offset:8704\n\tds_read_b64_tr_b16 a[76:77], v26 offset:8192\n\tds_read_b64_tr_b16 a[78:79], v26 offset:8704\n\t"
      "ds_read_b64_tr_b16 a[84:85], v27 offset:8192\n\tds_read_b64_tr_b16 a[86:87], v27 offset:8704\n\ts_waitcnt lgkmcnt(0)\n\ts_barrier\n\ts_add_u32 s50, s56, s66\n\t"
      "s_add_u32 m0, s49, 8192\n\ts_add_u32 s48, s50, 0x40000\n\tbuffer_load_dwordx4 v14, s[40:43], s48 offen lds\n\ts_add_u32 m0, s49, 9216\n\ts_add_u32 s48, s50, 0x48000\n\t"
      "buffer_load_dwordx4 v15, s[40:43], s48 offen lds\n\ts_add_u32 m0, s49, 10240\n\ts_add_u32 s48, s50, 0x50000\n\tbuffer_load_dwordx4 v16, s[40:43], s48 offen lds\n\t"
      "s_add_u32 m0, s49, 11264\n\ts_add_u32 s48, s50, 0x58000\n\tbuffer_load_dwordx4 v17, s[40:43], s48 offen lds\n\ts_add_u32 m0, s49, 12288\n\ts_add_u32 s48, s50, 0x60000\n\t"
      "buffer_load_dwordx4 v14, s[40:43], s48 offen lds\n\ts_add_u32 m0, s49, 13312\n\ts_add_u32 s48, s50, 0x68000\n\tbuffer_load_dwordx4 v15, s[40:43], s48 offen lds\n\t"
      "s_add_u32 m0, s49, 14336\n\ts_add_u32 s48, s50, 0x70000\n\tbuffer_load_dwordx4 v16, s[40:43], s48 offen lds\n\ts_add_u32 m0, s49, 15360\n\ts_add_u32 s48, s50, 0x78000\n\t"
      "buffer_load_dwordx4 v17, s[40:43], s48 offen lds\n\ts_waitcnt vmcnt(24)\n\ts_barrier\n\tds_read_b64_tr_b16 a[16:17], v20 offset:16384\n\t"
      "ds_read_b64_tr_b16 a[18:19], v20 offset:16896\n\tds_read_b64_tr_b16 v[192:193], v21 offset:16384\n\tds_read_b64_tr_b16 v[194:195], v21 offset:16896\n\t"
      "ds_read_b64_tr_b16 a[88:89], v22 offset:16384\n\tds_read_b64_tr_b16 a[90:91], v22 offset:16896\n\tds_read_b64_tr_b16 v[216:217], v23 offset:16384\n\t"
      "ds_read_b64_tr_b16 v[218:219], v23 offset:16896\n\tds_read_b64_tr_b16 a[96:97], v24 offset:16384\n\tds_read_b64_tr_b16 a[98:99], v24 offset:16896\n\t"
      "ds_read_b64_tr_b16 a[104:105], v25 offset:16384\n\tds_read_b64_tr_b16 a[106:107], v25 offset:16896\n\tds_read_b64_tr_b16 a[112:113], v26 offset:16384\n\t"
      "ds_read_b64_tr_b16 a[114:115], v26 offset:16896\n\tds_read_b64_tr_b16 v[224:225], v27 offset:16384\n\tds_read_b64_tr_b16 v[226:227], v27 offset:16896\n\t"
      "s_waitcnt lgkmcnt(0)\n\ts_barrier\n\ts_add_u32 s50, s56, s67\n\ts_add_u32 m0, s49, 16384\n\ts_add_u32 s48, s50, 0x0\n\t"
      "buffer_load_dwordx4 v14, s[40:43], s48 offen lds\n\ts_add_u32 m0, s49, 17408\n\ts_add_u32 s48, s50, 0x8000\n\tbuffer_load_dwordx4 v15, s[40:43], s48 offen lds\n\t"
      "s_add_u32 m0, s49, 18432\n\ts_add_u32 s48, s50, 0x10000\n\tbuffer_load_dwordx4 v16, s[40:43], s48 offen lds\n\ts_add_u32 m0, s49, 19456\n\ts_add_u32 s48, s50, 0x18000\n\t"
      "buffer_load_dwordx4 v17, s[40:43], s48 offen lds\n\ts_add_u32 m0, s49, 20480\n\ts_add_u32 s48, s50, 0x20000\n\tbuffer_load_dwordx4 v14, s[40:43], s48 offen lds\n\t"
      "s_add_u32 m0, s49, 21504\n\ts_add_u32 s48, s50, 0x28000\n\tbuffer_load_dwordx4 v15, s[40:43], s48 offen lds\n\ts_add_u32 m0, s49, 22528\n\ts_add_u32 s48, s50, 0x30000\n\t"
      "buffer_load_dwordx4 v16, s[40:43], s48 offen lds\n\ts_add_u32 m0, s49, 23552\n\ts_add_u32 s48, s50, 0x38000\n\tbuffer_load_dwordx4 v17, s[40:43], s48 offen lds\n\t"
      "s_waitcnt vmcnt(24)\n\ts_barrier\n\tds_read_b64_tr_b16 a[20:21], v20 offset:24576\n\tds_read_b64_tr_b16 a[22:23], v20 offset:25088\n\t"
      "ds_read_b64_tr_b16 v[196:197], v21 offset:24576\n\tds_read_b64_tr_b16 v[198:199], v21 offset:25088\n\tds_read_b64_tr_b16 a[92:93], v22 offset:24576\n\t"
      "ds_read_b64_tr_b16 a[94:95], v22 offset:25088\n\tds_read_b64_tr_b16 v[220:221], v23 offset:24576\n\tds_read_b64_tr_b16 v[222:223], v23 offset:25088\n\t"
      "ds_read_b64_tr_b16 a[100:101], v24 offset:24576\n\tds_read_b64_tr_b16 a[102:103], v24 offset:25088\n\tds_read_b64_tr_b16 a[108:109], v25 offset:24576\n\t"
      "ds_read_b64_tr_b16 a[110:111], v25 offset:25088\n\tds_read_b64_tr_b16 a[116:117], v26 offset:24576\n\tds_read_b64_tr_b16 a[118:119], v26 offset:25088\n\t"
      "ds_read_b64_tr_b16 v[228:229], v27 offset:24576\n\tds_read_b64_tr_b16 v[230:231], v27 offset:25088\n\ts_waitcnt lgkmcnt(0)\n\ts_barrier\n\ts_add_u32 s50, s56, s67\n\t"
      "s_add_u32 m0, s49, 24576\n\ts_add_u32 s48, s50, 0x40000\n\tbuffer_load_dwordx4 v14, s[40:43], s48 offen lds\n\ts_add_u32 m0, s49, 25600\n\ts_add_u32 s48, s50, 0x48000\n\t"
      "buffer_load_dwordx4 v15, s[40:43], s48 offen lds\n\ts_add_u32 m0, s49, 26624\n\ts_add_u32 s48, s50, 0x50000\n\tbuffer_load_dwordx4 v16, s[40:43], s48 offen lds\n\t"
      "s_add_u32 m0, s49, 27648\n\ts_add_u32 s48, s50, 0x58000\n\tbuffer_load_dwordx4 v17, s[40:43], s48 offen lds\n\ts_add_u32 m0, s49, 28672\n\ts_add_u32 s48, s50, 0x60000\n\t"
      "buffer_load_dwordx4 v14, s[40:43], s48 offen lds\n\ts_add_u32 m0, s49, 29696\n\ts_add_u32 s48, s50, 0x68000\n\tbuffer_load_dwordx4 v15, s[40:43], s48 offen lds\n\t"
      "s_add_u32 m0, s49, 30720\n\ts_add_u32 s48, s50, 0x70000\n\tbuffer_load_dwordx4 v16, s[40:43], s48 offen lds\n\ts_add_u32 m0, s49, 31744\n\ts_add_u32 s48, s50, 0x78000\n\t"
      "buffer_load_dwordx4 v17, s[40:43], s48 offen lds\n\ts_waitcnt vmcnt(24)\n\ts_barrier\n\tds_read_b64_tr_b16 a[24:25], v20 offset:0\n\t"
      "ds_read_b64_tr_b16 a[26:27], v20 offset:512\n\tds_read_b64_tr_b16 a[32:33], v21 offset:0\n\tds_read_b64_tr_b16 a[34:35], v21 offset:512\n\t"
      "ds_read_b64_tr_b16 a[120:121], v22 offset:0\n\tds_read_b64_tr_b16 a[122:123], v22 offset:512\n\tds_read_b64_tr_b16 a[128:129], v23 offset:0\n\t"
      "ds_read_b64_tr_b16 a[130:131], v23 offset:512\n\tds_read_b64_tr_b16 a[136:137], v24 offset:0\n\tds_read_b64_tr_b16 a[138:139], v24 offset:512\n\t"
      "ds_read_b64_tr_b16 v[232:233], v25 offset:0\n\tds_read_b64_tr_b16 v[234:235], v25 offset:512\n\tds_read_b64_tr_b16 a[144:145], v26 offset:0\n\t"
      "ds_read_b64_tr_b16 a[146:147], v26 offset:512\n\tds_read_b64_tr_b16 a[152:153], v27 offset:0\n\tds_read_b64_tr_b16 a[154:155], v27 offset:512\n\ts_waitcnt lgkmcnt(0)\n\t"
      "s_barrier\n\ts_add_u32 m0, s49, 0\n\ts_add_u32 s48, s57, 0x100\n\tbuffer_load_dwordx4 v18, s[44:47], s48 offen lds\n\ts_add_u32 m0, s49, 1024\n\t"
      "s_add_u32 s48, s57, 0x2100\n\tbuffer_load_dwordx4 v18, s[44:47], s48 offen lds\n\ts_add_u32 m0, s49, 2048\n\ts_add_u32 s48, s57, 0x4100\n\t"
      "buffer_load_dwordx4 v18, s[44:47], s48 offen lds\n\ts_add_u32 m0, s49, 3072\n\ts_add_u32 s48, s57, 0x6100\n\tbuffer_load_dwordx4 v18, s[44:47], s48 offen lds\n\t"
      "s_add_u32 m0, s49, 4096\n\ts_add_u32 s48, s57, 0x8100\n\tbuffer_load_dwordx4 v19, s[44:47], s48 offen lds\n\ts_add_u32 m0, s49, 5120\n\ts_add_u32 s48, s57, 0xa100\n\t"
      "buffer_load_dwordx4 v19, s[44:47], s48 offen lds\n\ts_add_u32 m0, s49, 6144\n\ts_add_u32 s48, s57, 0xc100\n\tbuffer_load_dwordx4 v19, s[44:47], s48 offen lds\n\t"
      "s_add_u32 m0, s49, 7168\n\ts_add_u32 s48, s57, 0xe100\n\tbuffer_load_dwordx4 v19, s[44:47], s48 offen lds\n\ts_waitcnt vmcnt(24)\n\ts_barrier\n\t"
      "ds_read_b64_tr_b16 a[28:29], v20 offset:8192\n\tds_read_b64_tr_b16 a[30:31], v20 offset:8704\n\tds_read_b64_tr_b16 a[36:37], v21 offset:8192\n\t"
      "ds_read_b64_tr_b16 a[38:39], v21 offset:8704\n\tds_read_b64_tr_b16 a[124:125], v22 offset:8192\n\tds_read_b64_tr_b16 a[126:127], v22 offset:8704\n\t"
      "ds_read_b64_tr_b16 a[132:133], v23 offset:8192\n\tds_read_b64_tr_b16 a[134:135], v23 offset:8704\n\tds_read_b64_tr_b16 a[140:141], v24 offset:8192\n\t"
      "ds_read_b64_tr_b16 a[142:143], v24 offset:8704\n\tds_read_b64_tr_b16 v[236:237], v25 offset:8192\n\tds_read_b64_tr_b16 v[238:239], v25 offset:8704\n\t"
      "ds_read_b64_tr_b16 a[148:149], v26 offset:8192\n\tds_read_b64_tr_b16 a[150:151], v26 offset:8704\n\tds_read_b64_tr_b16 a[156:157], v27 offset:8192\n\t"
      "ds_read_b64_tr_b16 a[158:159], v27 offset:8704\n\ts_waitcnt lgkmcnt(0)\n\ts_barrier\n\ts_add_u32 m0, s49, 8192\n\ts_add_u32 s48, s57, 0x10100\n\t"
      "buffer_load_dwordx4 v18, s[44:47], s48 offen lds\n\ts_add_u32 m0, s49, 9216\n\ts_add_u32 s48, s57, 0x12100\n\tbuffer_load_dwordx4 v18, s[44:47], s48 offen lds\n\t"
      "s_add_u32 m0, s49, 10240\n\ts_add_u32 s48, s57, 0x14100\n\tbuffer_load_dwordx4 v18, s[44:47], s48 offen lds\n\ts_add_u32 m0, s49, 11264\n\ts_add_u32 s48, s57, 0x16100\n\t"
      "buffer_load_dwordx4 v18, s[44:47], s48 offen lds\n\ts_add_u32 m0, s49, 12288\n\ts_add_u32 s48, s57, 0x18100\n\tbuffer_load_dwordx4 v19, s[44:47], s48 offen lds\n\t"
      "s_add_u32 m0, s49, 13312\n\ts_add_u32 s48, s57, 0x1a100\n\tbuffer_load_dwordx4 v19, s[44:47], s48 offen lds\n\ts_add_u32 m0, s49, 14336\n\ts_add_u32 s48, s57, 0x1c100\n\t"
      "buffer_load_dwordx4 v19, s[44:47], s48 offen lds\n\ts_add_u32 m0, s49, 15360\n\ts_add_u32 s48, s57, 0x1e100\n\tbuffer_load_dwordx4 v19, s[44:47], s48 offen lds\n\t"
      "s_waitcnt vmcnt(24)\n\ts_barrier\n\tds_read_b64_tr_b16 a[40:41], v20 offset:16384\n\tds_read_b64_tr_b16 a[42:43], v20 offset:16896\n\t"
      "ds_read_b64_tr_b16 v[200:201], v21 offset:16384\n\tds_read_b64_tr_b16 v[202:203], v21 offset:16896\n\tds_read_b64_tr_b16 a[160:161], v22 offset:16384\n\t"
      "ds_read_b64_tr_b16 a[162:163], v22 offset:16896\n\tds_read_b64_tr_b16 v[240:241], v23 offset:16384\n\tds_read_b64_tr_b16 v[242:243], v23 offset:16896\n\t"
      "ds_read_b64_tr_b16 a[168:169], v24 offset:16384\n\tds_read_b64_tr_b16 a[170:171], v24 offset:16896\n\tds_read_b64_tr_b16 a[176:177], v25 offset:16384\n\t"
      "ds_read_b64_tr_b16 a[178:179], v25 offset:16896\n\tds_read_b64_tr_b16 a[184:185], v26 offset:16384\n\tds_read_b64_tr_b16 a[186:187], v26 offset:16896\n\t"
      "ds_read_b64_tr_b16 v[248:249], v27 offset:16384\n\tds_read_b64_tr_b16 v[250:251], v27 offset:16896\n\ts_waitcnt lgkmcnt(0)\n\ts_barrier\n\ts_add_u32 m0, s49, 16384\n\t"
      "s_add_u32 s48, s57, 0x20100\n\tbuffer_load_dwordx4 v18, s[44:47], s48 offen lds\n\ts_add_u32 m0, s49, 17408\n\ts_add_u32 s48, s57, 0x22100\n\t"
      "buffer_load_dwordx4 v18, s[44:47], s48 offen lds\n\ts_add_u32 m0, s49, 18432\n\ts_add_u32 s48, s57, 0x24100\n\tbuffer_load_dwordx4 v18, s[44:47], s48 offen lds\n\t"
      "s_add_u32 m0, s49, 19456\n\ts_add_u32 s48, s57, 0x26100\n\tbuffer_load_dwordx4 v18, s[44:47], s48 offen lds\n\ts_add_u32 m0, s49, 20480\n\ts_add_u32 s48, s57, 0x28100\n\t"
      "buffer_load_dwordx4 v19, s[44:47], s48 offen lds\n\ts_add_u32 m0, s49, 21504\n\ts_add_u32 s48, s57, 0x2a100\n\tbuffer_load_dwordx4 v19, s[44:47], s48 offen lds\n\t"
      "s_add_u32 m0, s49, 22528\n\ts_add_u32 s48, s57, 0x2c100\n\tbuffer_load_dwordx4 v19, s[44:47], s48 offen lds\n\ts_add_u32 m0, s49, 23552\n\ts_add_u32 s48, s57, 0x2e100\n\t"
      "buffer_load_dwordx4 v19, s[44:47], s48 offen lds\n\ts_waitcnt vmcnt(24)\n\ts_barrier\n\tds_read_b64_tr_b16 a[44:45], v20 offset:24576\n\t"
      "ds_read_b64_tr_b16 a[46:47], v20 offset:25088\n\tds_read_b64_tr_b16 v[204:205], v21 offset:24576\n\tds_read_b64_tr_b16 v[206:207], v21 offset:25088\n\t"
      "ds_read_b64_tr_b16 a[164:165], v22 offset:24576\n\tds_read_b64_tr_b16 a[166:167], v22 offset:25088\n\tds_read_b64_tr_b16 v[244:245], v23 offset:24576\n\t"
      "ds_read_b64_tr_b16 v[246:247], v23 offset:25088\n\tds_read_b64_tr_b16 a[172:173], v24 offset:24576\n\tds_read_b64_tr_b16 a[174:175], v24 offset:25088\n\t"
      "ds_read_b64_tr_b16 a[180:181], v25 offset:24576\n\tds_read_b64_tr_b16 a[182:183], v25 offset:25088\n\tds_read_b64_tr_b16 a[188:189], v26 offset:24576\n\t"
      "ds_read_b64_tr_b16 a[190:191], v26 offset:25088\n\tds_read_b64_tr_b16 v[252:253], v27 offset:24576\n\tds_read_b64_tr_b16 v[254:255], v27 offset:25088\n\t"
      "s_waitcnt lgkmcnt(0)\n\ts_barrier\n\ts_add_u32 m0, s49, 24576\n\ts_add_u32 s48, s57, 0x30100\n\tbuffer_load_dwordx4 v18, s[44:47], s48 offen lds\n\t"
      "s_add_u32 m0, s49, 25600\n\ts_add_u32 s48, s57, 0x32100\n\tbuffer_load_dwordx4 v18, s[44:47], s48 offen lds\n\ts_add_u32 m0, s49, 26624\n\ts_add_u32 s48, s57, 0x34100\n\t"
      "buffer_load_dwordx4 v18, s[44:47], s48 offen lds\n\ts_add_u32 m0, s49, 27648\n\ts_add_u32 s48, s57, 0x36100\n\tbuffer_load_dwordx4 v18, s[44:47], s48 offen lds\n\t"
      "s_add_u32 m0, s49, 28672\n\ts_add_u32 s48, s57, 0x38100\n\tbuffer_load_dwordx4 v19, s[44:47], s48 offen lds\n\ts_add_u32 m0, s49, 29696\n\ts_add_u32 s48, s57, 0x3a100\n\t"
      "buffer_load_dwordx4 v19, s[44:47], s48 offen lds\n\ts_add_u32 m0, s49, 30720\n\ts_add_u32 s48, s57, 0x3c100\n\tbuffer_load_dwordx4 v19, s[44:47], s48 offen lds\n\t"
      "s_add_u32 m0, s49, 31744\n\ts_add_u32 s48, s57, 0x3e100\n\tbuffer_load_dwordx4 v19, s[44:47], s48 offen lds\n\ts_waitcnt vmcnt(24)\n\t"
      "ds_read_b64_tr_b16 a[192:193], v28 offset:0\n\tds_read_b64_tr_b16 a[194:195], v28 offset:256\n\tds_read_b64_tr_b16 a[196:197], v28 offset:2048\n\t"
      "ds_read_b64_tr_b16 a[198:199], v28 offset:2304\n\tds_read_b64_tr_b16 a[224:225], v29 offset:0\n\tds_read_b64_tr_b16 a[226:227], v29 offset:256\n\t"
      "ds_read_b64_tr_b16 a[228:229], v29 offset:2048\n\tds_read_b64_tr_b16 a[230:231], v29 offset:2304\n\tds_read_b64_tr_b16 v[56:57], v30 offset:0\n\t"
      "ds_read_b64_tr_b16 v[58:59], v30 offset:256\n\tds_read_b64_tr_b16 v[60:61], v30 offset:2048\n\tds_read_b64_tr_b16 v[62:63], v30 offset:2304\n\t"
      "ds_read_b64_tr_b16 v[160:161], v31 offset:0\n\tds_read_b64_tr_b16 v[162:163], v31 offset:256\n\tds_read_b64_tr_b16 v[164:165], v31 offset:2048\n\t"
      "ds_read_b64_tr_b16 v[166:167], v31 offset:2304\n\ts_waitcnt lgkmcnt(0)\n\ts_add_u32 m0, s49, 0\n\ts_add_u32 s48, s57, 0x0\n\t"
      "buffer_load_dwordx4 v18, s[44:47], s48 offen lds\n\ts_add_u32 m0, s49, 1024\n\ts_add_u32 s48, s57, 0x2000\n\tbuffer_load_dwordx4 v18, s[44:47], s48 offen lds\n\t"
      "s_add_u32 m0, s49, 2048\n\ts_add_u32 s48, s57, 0x4000\n\tbuffer_load_dwordx4 v18, s[44:47], s48 offen lds\n\ts_add_u32 m0, s49, 3072\n\ts_add_u32 s48, s57, 0x6000\n\t"
      "buffer_load_dwordx4 v18, s[44:47], s48 offen lds\n\ts_add_u32 m0, s49, 4096\n\ts_add_u32 s48, s57, 0x8000\n\tbuffer_load_dwordx4 v19, s[44:47], s48 offen lds\n\t"
      "s_add_u32 m0, s49, 5120\n\ts_add_u32 s48, s57, 0xa000\n\tbuffer_load_dwordx4 v19, s[44:47], s48 offen lds\n\ts_add_u32 m0, s49, 6144\n\ts_add_u32 s48, s57, 0xc000\n\t"
      "buffer_load_dwordx4 v19, s[44:47], s48 offen lds\n\ts_add_u32 m0, s49, 7168\n\ts_add_u32 s48, s57, 0xe000\n\tbuffer_load_dwordx4 v19, s[44:47], s48 offen lds\n\t"
      "s_waitcnt vmcnt(24)\n\tds_read_b64_tr_b16 a[200:201], v28 offset:8192\n\tds_read_b64_tr_b16 a[202:203], v28 offset:8448\n\t"
      "ds_read_b64_tr_b16 a[204:205], v28 offset:10240\n\tds_read_b64_tr_b16 a[206:207], v28 offset:10496\n\tds_read_b64_tr_b16 a[232:233], v29 offset:8192\n\t"
      "ds_read_b64_tr_b16 a[234:235], v29 offset:8448\n\tds_read_b64_tr_b16 a[236:237], v29 offset:10240\n\tds_read_b64_tr_b16 a[238:239], v29 offset:10496\n\t"
      "ds_read_b64_tr_b16 v[88:89], v30 offset:8192\n\tds_read_b64_tr_b16 v[90:91], v30 offset:8448\n\tds_read_b64_tr_b16 v[92:93], v30 offset:10240\n\t"
      "ds_read_b64_tr_b16 v[94:95], v30 offset:10496\n\tds_read_b64_tr_b16 v[168:169], v31 offset:8192\n\tds_read_b64_tr_b16 v[170:171], v31 offset:8448\n\t"
      "ds_read_b64_tr_b16 v[172:173], v31 offset:10240\n\tds_read_b64_tr_b16 v[174:175], v31 offset:10496\n\ts_waitcnt lgkmcnt(0)\n\ts_add_u32 m0, s49, 8192\n\t"
      "s_add_u32 s48, s57, 0x10000\n\tbuffer_load_dwordx4 v18, s[44:47], s48 offen lds\n\ts_add_u32 m0, s49, 9216\n\ts_add_u32 s48, s57, 0x12000\n\t"
      "buffer_load_dwordx4 v18, s[44:47], s48 offen lds\n\ts_add_u32 m0, s49, 10240\n\ts_add_u32 s48, s57, 0x14000\n\tbuffer_load_dwordx4 v18, s[44:47], s48 offen lds\n\t"
      "s_add_u32 m0, s49, 11264\n\ts_add_u32 s48, s57, 0x16000\n\tbuffer_load_dwordx4 v18, s[44:47], s48 offen lds\n\ts_add_u32 m0, s49, 12288\n\ts_add_u32 s48, s57, 0x18000\n\t"
      "buffer_load_dwordx4 v19, s[44:47], s48 offen lds\n\ts_add_u32 m0, s49, 13312\n\ts_add_u32 s48, s57, 0x1a000\n\tbuffer_load_dwordx4 v19, s[44:47], s48 offen lds\n\t"
      "s_add_u32 m0, s49, 14336\n\ts_add_u32 s48, s57, 0x1c000\n\tbuffer_load_dwordx4 v19, s[44:47], s48 offen lds\n\ts_add_u32 m0, s49, 15360\n\ts_add_u32 s48, s57, 0x1e000\n\t"
      "buffer_load_dwordx4 v19, s[44:47], s48 offen lds\n\ts_waitcnt vmcnt(24)\n\tds_read_b64_tr_b16 a[208:209], v28 offset:16384\n\t"
      "ds_read_b64_tr_b16 a[210:211], v28 offset:16640\n\tds_read_b64_tr_b16 a[212:213], v28 offset:18432\n\tds_read_b64_tr_b16 a[214:215], v28 offset:18688\n\t"
      "ds_read_b64_tr_b16 a[240:241], v29 offset:16384\n\tds_read_b64_tr_b16 a[242:243], v29 offset:16640\n\tds_read_b64_tr_b16 a[244:245], v29 offset:18432\n\t"
      "ds_read_b64_tr_b16 a[246:247], v29 offset:18688\n\tds_read_b64_tr_b16 v[120:121], v30 offset:16384\n\tds_read_b64_tr_b16 v[122:123], v30 offset:16640\n\t"
      "ds_read_b64_tr_b16 v[124:125], v30 offset:18432\n\tds_read_b64_tr_b16 v[126:127], v30 offset:18688\n\tds_read_b64_tr_b16 v[176:177], v31 offset:16384\n\t"
      "ds_read_b64_tr_b16 v[178:179], v31 offset:16640\n\tds_read_b64_tr_b16 v[180:181], v31 offset:18432\n\tds_read_b64_tr_b16 v[182:183], v31 offset:18688\n\t"
      "s_waitcnt lgkmcnt(0)\n\ts_add_u32 m0, s49, 16384\n\ts_add_u32 s48, s57, 0x20000\n\tbuffer_load_dwordx4 v18, s[44:47], s48 offen lds\n\ts_add_u32 m0, s49, 17408\n\t"
      "s_add_u32 s48, s57, 0x22000\n\tbuffer_load_dwordx4 v18, s[44:47], s48 offen lds\n\ts_add_u32 m0, s49, 18432\n\ts_add_u32 s48, s57, 0x24000\n\t"
      "buffer_load_dwordx4 v18, s[44:47], s48 offen lds\n\ts_add_u32 m0, s49, 19456\n\ts_add_u32 s48, s57, 0x26000\n\tbuffer_load_dwordx4 v18, s[44:47], s48 offen lds\n\t"
      "s_add_u32 m0, s49, 20480\n\ts_add_u32 s48, s57, 0x28000\n\tbuffer_load_dwordx4 v19, s[44:47], s48 offen lds\n\ts_add_u32 m0, s49, 21504\n\ts_add_u32 s48, s57, 0x2a000\n\t"
      "buffer_load_dwordx4 v19, s[44:47], s48 offen lds\n\ts_add_u32 m0, s49, 22528\n\ts_add_u32 s48, s57, 0x2c000\n\tbuffer_load_dwordx4 v19, s[44:47], s48 offen lds\n\t"
      "s_add_u32 m0, s49, 23552\n\ts_add_u32 s48, s57, 0x2e000\n\tbuffer_load_dwordx4 v19, s[44:47], s48 offen lds\n\ts_waitcnt vmcnt(24)\n\t"
      "ds_read_b64_tr_b16 a[216:217], v28 offset:24576\n\tds_read_b64_tr_b16 a[218:219], v28 offset:24832\n\tds_read_b64_tr_b16 a[220:221], v28 offset:26624\n\t"
      "ds_read_b64_tr_b16 a[222:223], v28 offset:26880\n\tds_read_b64_tr_b16 a[248:249], v29 offset:24576\n\tds_read_b64_tr_b16 a[250:251], v29 offset:24832\n\t"
      "ds_read_b64_tr_b16 a[252:253], v29 offset:26624\n\tds_read_b64_tr_b16 a[254:255], v29 offset:26880\n\tds_read_b64_tr_b16 v[152:153], v30 offset:24576\n\t"
      "ds_read_b64_tr_b16 v[154:155], v30 offset:24832\n\tds_read_b64_tr_b16 v[156:157], v30 offset:26624\n\tds_read_b64_tr_b16 v[158:159], v30 offset:26880\n\t"
      "ds_read_b64_tr_b16 v[184:185], v31 offset:24576\n\tds_read_b64_tr_b16 v[186:187], v31 offset:24832\n\tds_read_b64_tr_b16 v[188:189], v31 offset:26624\n\t"
      "ds_read_b64_tr_b16 v[190:191], v31 offset:26880\n\ts_waitcnt lgkmcnt(0)\n\ts_add_u32 m0, s49, 24576\n\ts_add_u32 s48, s57, 0x30000\n\t"
      "buffer_load_dwordx4 v18, s[44:47], s48 offen lds\n\ts_add_u32 m0, s49, 25600\n\ts_add_u32 s48, s57, 0x32000\n\tbuffer_load_dwordx4 v18, s[44:47], s48 offen lds\n\t"
      "s_add_u32 m0, s49, 26624\n\ts_add_u32 s48, s57, 0x34000\n\tbuffer_load_dwordx4 v18, s[44:47], s48 offen lds\n\ts_add_u32 m0, s49, 27648\n\ts_add_u32 s48, s57, 0x36000\n\t"
      "buffer_load_dwordx4 v18, s[44:47], s48 offen lds\n\ts_add_u32 m0, s49, 28672\n\ts_add_u32 s48, s57, 0x38000\n\tbuffer_load_dwordx4 v19, s[44:47], s48 offen lds\n\t"
      "s_add_u32 m0, s49, 29696\n\ts_add_u32 s48, s57, 0x3a000\n\tbuffer_load_dwordx4 v19, s[44:47], s48 offen lds\n\ts_add_u32 m0, s49, 30720\n\ts_add_u32 s48, s57, 0x3c000\n\t"
      "buffer_load_dwordx4 v19, s[44:47], s48 offen lds\n\ts_add_u32 m0, s49, 31744\n\ts_add_u32 s48, s57, 0x3e000\n\tbuffer_load_dwordx4 v19, s[44:47], s48 offen lds\n\t"
      "s_waitcnt vmcnt(24)\n\tds_read_b64_tr_b16 v[32:33], v28 offset:0\n\tds_read_b64_tr_b16 v[34:35], v28 offset:256\n\tds_read_b64_tr_b16 v[36:37], v28 offset:2048\n\t"
      "ds_read_b64_tr_b16 v[38:39], v28 offset:2304\n\tds_read_b64_tr_b16 v[64:65], v29 offset:0\n\tds_read_b64_tr_b16 v[66:67], v29 offset:256\n\t"
      "ds_read_b64_tr_b16 v[68:69], v29 offset:2048\n\tds_read_b64_tr_b16 v[70:71], v29 offset:2304\n\tds_read_b64_tr_b16 v[96:97], v30 offset:0\n\t"
      "ds_read_b64_tr_b16 v[98:99], v30 offset:256\n\tds_read_b64_tr_b16 v[100:101], v30 offset:2048\n\tds_read_b64_tr_b16 v[102:103], v30 offset:2304\n\t"
      "ds_read_b64_tr_b16 v[128:129], v31 offset:0\n\tds_read_b64_tr_b16 v[130:131], v31 offset:256\n\tds_read_b64_tr_b16 v[132:133], v31 offset:2048\n\t"
      "ds_read_b64_tr_b16 v[134:135], v31 offset:2304\n\ts_waitcnt lgkmcnt(0)\n\tds_write_b128 v5, v[56:59] offset:0\n\tds_write_b128 v5, v[60:63] offset:1024\n\t"
      "ds_write_b128 v5, v[88:91] offset:2048\n\tds_write_b128 v5, v[92:95] offset:3072\n\tds_write_b128 v5, v[120:123] offset:4096\n\t"
      "ds_write_b128 v5, v[124:127] offset:5120\n\tds_write_b128 v5, v[152:155] offset:6144\n\tds_write_b128 v5, v[156:159] offset:7168\n\ts_waitcnt vmcnt(16)\n\t"
      "ds_read_b64_tr_b16 v[40:41], v28 offset:8192\n\tds_read_b64_tr_b16 v[42:43], v28 offset:8448\n\tds_read_b64_tr_b16 v[44:45], v28 offset:10240\n\t"
      "ds_read_b64_tr_b16 v[46:47], v28 offset:10496\n\tds_read_b64_tr_b16 v[72:73], v29 offset:8192\n\tds_read_b64_tr_b16 v[74:75], v29 offset:8448\n\t"
      "ds_read_b64_tr_b16 v[76:77], v29 offset:10240\n\tds_read_b64_tr_b16 v[78:79], v29 offset:10496\n\tds_read_b64_tr_b16 v[104:105], v30 offset:8192\n\t"
      "ds_read_b64_tr_b16 v[106:107], v30 offset:8448\n\tds_read_b64_tr_b16 v[108:109], v30 offset:10240\n\tds_read_b64_tr_b16 v[110:111], v30 offset:10496\n\t"
      "ds_read_b64_tr_b16 v[136:137], v31 offset:8192\n\tds_read_b64_tr_b16 v[138:139], v31 offset:8448\n\tds_read_b64_tr_b16 v[140:141], v31 offset:10240\n\t"
      "ds_read_b64_tr_b16 v[142:143], v31 offset:10496\n\ts_waitcnt lgkmcnt(0)\n\tds_write_b128 v5, v[160:163] offset:8192\n\tds_write_b128 v5, v[164:167] offset:9216\n\t"
      "ds_write_b128 v5, v[168:171] offset:10240\n\tds_write_b128 v5, v[172:175] offset:11264\n\tds_write_b128 v5, v[176:179] offset:12288\n\t"
      "ds_write_b128 v5, v[180:183] offset:13312\n\tds_write_b128 v5, v[184:187] offset:14336\n\tds_write_b128 v5, v[188:191] offset:15360\n\ts_waitcnt vmcnt(8)\n\t"
      "ds_read_b64_tr_b16 v[48:49], v28 offset:16384\n\tds_read_b64_tr_b16 v[50:51], v28 offset:16640\n\tds_read_b64_tr_b16 v[52:53], v28 offset:18432\n\t"
      "ds_read_b64_tr_b16 v[54:55], v28 offset:18688\n\tds_read_b64_tr_b16 v[80:81], v29 offset:16384\n\tds_read_b64_tr_b16 v[82:83], v29 offset:16640\n\t"
      "ds_read_b64_tr_b16 v[84:85], v29 offset:18432\n\tds_read_b64_tr_b16 v[86:87], v29 offset:18688\n\tds_read_b64_tr_b16 v[112:113], v30 offset:16384\n\t"
      "ds_read_b64_tr_b16 v[114:115], v30 offset:16640\n\tds_read_b64_tr_b16 v[116:117], v30 offset:18432\n\tds_read_b64_tr_b16 v[118:119], v30 offset:18688\n\t"
      "ds_read_b64_tr_b16 v[144:145], v31 offset:16384\n\tds_read_b64_tr_b16 v[146:147], v31 offset:16640\n\tds_read_b64_tr_b16 v[148:149], v31 offset:18432\n\t"
      "ds_read_b64_tr_b16 v[150:151], v31 offset:18688\n\ts_waitcnt lgkmcnt(0)\n\tds_write_b128 v5, v[192:195] offset:16384\n\tds_write_b128 v5, v[196:199] offset:17408\n\t"
      "ds_write_b128 v5, v[200:203] offset:18432\n\tds_write_b128 v5, v[204:207] offset:19456\n\tds_write_b128 v5, v[208:211] offset:20480\n\t"
      "ds_write_b128 v5, v[212:215] offset:21504\n\tds_write_b128 v5, v[216:219] offset:22528\n\tds_write_b128 v5, v[220:223] offset:23552\n\ts_waitcnt vmcnt(0)\n\t"
      "ds_read_b64_tr_b16 v[56:57], v28 offset:24576\n\tds_read_b64_tr_b16 v[58:59], v28 offset:24832\n\tds_read_b64_tr_b16 v[60:61], v28 offset:26624\n\t"
      "ds_read_b64_tr_b16 v[62:63], v28 offset:26880\n\tds_read_b64_tr_b16 v[88:89], v29 offset:24576\n\tds_read_b64_tr_b16 v[90:91], v29 offset:24832\n\t"
      "ds_read_b64_tr_b16 v[92:93], v29 offset:26624\n\tds_read_b64_tr_b16 v[94:95], v29 offset:26880\n\tds_read_b64_tr_b16 v[120:121], v30 offset:24576\n\t"
      "ds_read_b64_tr_b16 v[122:123], v30 offset:24832\n\tds_read_b64_tr_b16 v[124:125], v30 offset:26624\n\tds_read_b64_tr_b16 v[126:127], v30 offset:26880\n\t"
      "ds_read_b64_tr_b16 v[152:153], v31 offset:24576\n\tds_read_b64_tr_b16 v[154:155], v31 offset:24832\n\tds_read_b64_tr_b16 v[156:157], v31 offset:26624\n\t"
      "ds_read_b64_tr_b16 v[158:159], v31 offset:26880\n\ts_waitcnt lgkmcnt(0)\n\tds_write_b128 v5, v[224:227] offset:24576\n\tds_write_b128 v5, v[228:231] offset:25600\n\t"
      "ds_write_b128 v5, v[232:235] offset:26624\n\tds_write_b128 v5, v[236:239] offset:27648\n\tds_write_b128 v5, v[240:243] offset:28672\n\t"
      "ds_write_b128 v5, v[244:247] offset:29696\n\tds_write_b128 v5, v[248:251] offset:30720\n\tds_write_b128 v5, v[252:255] offset:31744\n\t"
      "v_mov_b64 v[30:31], 0\n\tv_mov_b32 v29, 160000\n\tds_write_b64 v29, v[30:31]\n\t"
      TKS("s_lshr_b32 s89, s62, 3\n\ts_and_b32 s89, s89, 3\n\t")
      "s_waitcnt lgkmcnt(0)\n\ts_barrier\n\t"
      STS("s_memrealtime vcc\n\ts_waitcnt lgkmcnt(0)\n\tv_mov_b32 v31, vcc_lo\n\tv_mov_b32 v30, 161024\n\tds_write_b32 v30, v31\n\t")
      "1:\n\tbuffer_load_dword v29, off, s[12:15], s63 sc1\n\ts_waitcnt vmcnt(0)\n\tv_readfirstlane_b32 s49, v29\n\ts_bitcmp1_b32 s49, 0\n\ts_cbranch_scc1 2f\n\t"
      "s_cmp_eq_u32 s18, 0\n\ts_cbranch_scc1 2f\n\ts_sub_u32 s18, s18, 1\n\ts_sleep 2\n\ts_branch 1b\n\t"
      "2:\n\ts_and_b32 s48, s49, 3\n\ts_cmp_lg_u32 s48, 3\n\ts_cbranch_scc1 4f\n\ts_cmp_eq_u32 s62, 0\n\ts_cbranch_scc1 4f\n\t"
      "s_waitcnt lgkmcnt(0)\n\ts_barrier\n\ts_mov_b64 s[0:1], s[60:61]\n\ts_mov_b32 s2, s62\n\ts_lshl_b32 s48, s16, 6\n\tv_mbcnt_lo_u32_b32 v0, -1, 0\n\t"
      "v_mbcnt_hi_u32_b32 v0, -1, v0\n\tv_add_u32 v0, s48, v0\n\ts_getpc_b64 s[4:5]\n\t"
      "3:\n\ts_sub_u32 s4, s4, 3b-mk_mega\n\ts_subb_u32 s5, s5, 0\n\ts_setpc_b64 s[4:5]\n\t"
      "4:\n\ts_memrealtime s[46:47]\n\ts_waitcnt lgkmcnt(0)\n\ts_lshl_b32 s19, s46, 2\n\ts_sub_u32 s19, s19, 0x100000\n\ts_lshr_b32 s64, s64, 9\n\ts_lshr_b32 s65, s65, 9\n\t"
      "s_lshr_b32 s66, s66, 9\n\ts_lshr_b32 s67, s67, 9\n\ts_lshl_b32 s48, s49, 10\n\ts_and_b32 s48, s48, 0x2000\n\tv_or_b32 v0, s48, v0\n\tv_or_b32 v11, s48, v11\n\t"
      "s_lshl_b32 s48, s49, 11\n\ts_and_b32 s48, s48, 0x2000\n\tv_or_b32 v9, s48, v9\n\t"
      "10:\n\tds_read_b128 v[160:163], v5 offset:16384\n\tds_read_b128 v[164:167], v5 offset:17408\n\tds_read_b128 v[168:171], v5 offset:18432\n\t"
      "ds_read_b128 v[172:175], v5 offset:19456\n\tds_read_b128 v[176:179], v5 offset:20480\n\tds_read_b128 v[180:183], v5 offset:21504\n\t"
      "ds_read_b128 v[184:187], v5 offset:22528\n\tds_read_b128 v[188:191], v5 offset:23552\n\tds_read_b128 v[192:195], v5 offset:24576\n\t"
      "ds_read_b128 v[196:199], v5 offset:25600\n\tds_read_b128 v[200:203], v5 offset:26624\n\tds_read_b128 v[204:207], v5 offset:27648\n\t"
      "ds_read_b128 v[208:211], v5 offset:28672\n\tds_read_b128 v[212:215], v5 offset:29696\n\tds_read_b128 v[216:219], v5 offset:30720\n\t"
      "ds_read_b128 v[220:223], v5 offset:31744\n\tv_mov_b64 v[16:17], 0\n\tv_mov_b64 v[18:19], 0\n\tv_mov_b64 v[20:21], 0\n\tv_mov_b64 v[22:23], 0\n\ts_and_b32 s48, s17, 3\n\t"
      "s_lshl_b32 s40, s48, 15\n\ts_add_u32 s40, s40, 0x200000\n\ts_lshl_b32 s41, s48, 17\n\ts_add_u32 s41, s41, 0x220000\n\ts_xor_b32 s48, s48, 2\n\ts_lshl_b32 s42, s48, 17\n\t"
      "s_add_u32 s42, s42, 0x220000\n\ts_add_u32 s68, s40, s64\n\ts_add_u32 s69, s40, s65\n\ts_add_u32 s70, s40, s66\n\ts_add_u32 s71, s40, s67\n\ts_and_b32 s43, s17, 1\n\t"
      "s_lshl_b32 s43, s43, 20\n\ts_waitcnt lgkmcnt(0)\n\ts_cmp_lg_u32 s16, 1\n\ts_cbranch_scc1 73f\n\t"
      "71:\n\ts_memrealtime vcc\n\ts_waitcnt lgkmcnt(0)\n\ts_lshl_b32 vcc_lo, vcc_lo, 2\n\t"
      "70:\n\ts_sub_u32 s48, s19, vcc_lo\n\ts_cmp_gt_u32 s48, 4000\n\ts_cbranch_scc1 73f\n\ts_cmp_gt_u32 s48, 28\n\ts_cbranch_scc1 71b\n\ts_mul_i32 s48, s48, 45\n\t"
      "s_lshr_b32 s48, s48, 5\n\ts_lshl_b32 s48, s48, 2\n\ts_getpc_b64 vcc\n\t"
      "72:\n\ts_sub_u32 s48, 73f-72b, s48\n\ts_add_u32 vcc_lo, vcc_lo, s48\n\ts_addc_u32 vcc_hi, vcc_hi, 0\n\ts_setpc_b64 vcc\n\t.rept 40\n\ts_nop 0\n\t.endr\n\t"
      "73:\n\ts_barrier\n\t"
      TK0
      "buffer_load_dwordx4 v[240:243], v0, s[8:11], s68 offen sc1\n\ts_nop 11\n\tbuffer_load_dwordx4 v[244:247], v0, s[8:11], s69 offen sc1\n\ts_nop 11\n\t"
      "buffer_load_dwordx4 v[248:251], v0, s[8:11], s70 offen sc1\n\ts_nop 11\n\tbuffer_load_dwordx4 v[252:255], v0, s[8:11], s71 offen sc1\n\t"
      "11:\n\ts_waitcnt vmcnt(3)\n\t"
      TK1
      "ds_write_b128 v1, v[240:243] offset:0\n\tv_max3_u32 v13, v240, v241, v242\n\tv_max_u32 v13, v13, v243\n\tv_cmp_eq_u32 vcc, -1, v13\n\t"
      "v_smfmac_f32_16x16x64_bf16 v[16:19], v[240:243], a[0:7], v6\n\tv_smfmac_f32_16x16x64_bf16 v[20:23], v[240:243], a[8:15], v6\n\ts_cbranch_vccnz 86f\n\t"
      "20:\n\ts_waitcnt lgkmcnt(0)\n\ts_barrier\n\tds_read_b128 v[24:27], v2 offset:0\n\tds_read_b128 v[28:31], v3 offset:0\n\tds_read_b128 v[224:227], v4 offset:0\n\t"
      "s_waitcnt vmcnt(2)\n\tv_smfmac_f32_16x16x64_bf16 v[16:19], v[244:247], a[16:23], v6\n\tds_write_b128 v1, v[244:247] offset:1024\n\tv_max3_u32 v13, v244, v245, v246\n\t"
      "v_smfmac_f32_16x16x64_bf16 v[20:23], v[244:247], v[160:167], v6\n\ts_waitcnt vmcnt(1)\n\tv_max_u32 v13, v13, v247\n\t"
      "v_smfmac_f32_16x16x64_bf16 v[16:19], v[248:251], a[24:31], v6\n\tv_cmp_eq_u32 vcc, -1, v13\n\tv_smfmac_f32_16x16x64_bf16 v[20:23], v[248:251], a[32:39], v6\n\t"
      "s_waitcnt lgkmcnt(3)\n\tv_max3_u32 v13, v248, v249, v250\n\tv_smfmac_f32_16x16x64_bf16 v[16:19], v[24:27], a[48:55], v6\n\ts_cbranch_vccnz 87f\n\t"
      "21:\n\tv_max_u32 v13, v13, v251\n\tv_smfmac_f32_16x16x64_bf16 v[20:23], v[24:27], a[56:63], v6\n\ts_waitcnt lgkmcnt(2)\n\ts_waitcnt lgkmcnt(0)\n\ts_barrier\n\t"
      "s_waitcnt vmcnt(0)\n\tv_smfmac_f32_16x16x64_bf16 v[16:19], v[252:255], a[40:47], v6\n\tds_read_b128 v[228:231], v2 offset:1024\n\t"
      "ds_read_b128 v[232:235], v3 offset:1024\n\tv_max3_u32 v13, v13, v252, v253\n\tv_smfmac_f32_16x16x64_bf16 v[20:23], v[252:255], v[168:175], v6\n\t"
      "ds_read_b128 v[236:239], v4 offset:1024\n\tv_max3_u32 v13, v13, v254, v255\n\tv_smfmac_f32_16x16x64_bf16 v[16:19], v[28:31], a[64:71], v6\n\t"
      "ds_write_b128 v1, v[248:251] offset:2048\n\tv_smfmac_f32_16x16x64_bf16 v[20:23], v[28:31], v[176:183], v6\n\tv_cmp_eq_u32 vcc, -1, v13\n\t"
      TK2
      "v_smfmac_f32_16x16x64_bf16 v[16:19], v[224:227], a[72:79], v6\n\tv_smfmac_f32_16x16x64_bf16 v[20:23], v[224:227], a[80:87], v6\n\ts_waitcnt lgkmcnt(3)\n\t"
      "ds_write_b128 v1, v[252:255] offset:3072\n\tv_smfmac_f32_16x16x64_bf16 v[16:19], v[228:231], a[88:95], v6\n\ts_cbranch_vccnz 88f\n\t"
      "22:\n\tv_smfmac_f32_16x16x64_bf16 v[20:23], v[228:231], v[184:191], v6\n\ts_waitcnt lgkmcnt(3)\n\tv_smfmac_f32_16x16x64_bf16 v[16:19], v[232:235], a[96:103], v6\n\t"
      "v_smfmac_f32_16x16x64_bf16 v[20:23], v[232:235], a[104:111], v6\n\ts_waitcnt lgkmcnt(2)\n\ts_waitcnt lgkmcnt(0)\n\ts_barrier\n\t"
      "ds_read_b128 v[240:243], v2 offset:2048\n\t"
      TK3
      "v_smfmac_f32_16x16x64_bf16 v[16:19], v[236:239], a[112:119], v6\n\tds_read_b128 v[244:247], v3 offset:2048\n\tds_read_b128 v[248:251], v4 offset:2048\n\t"
      "v_smfmac_f32_16x16x64_bf16 v[20:23], v[236:239], v[192:199], v6\n\tds_read_b128 v[24:27], v2 offset:3072\n\ts_waitcnt lgkmcnt(3)\n\t"
      "v_smfmac_f32_16x16x64_bf16 v[16:19], v[240:243], a[120:127], v6\n\tds_read_b128 v[28:31], v3 offset:3072\n\tds_read_b128 v[224:227], v4 offset:3072\n\t"
      "v_smfmac_f32_16x16x64_bf16 v[20:23], v[240:243], a[128:135], v6\n\ts_waitcnt lgkmcnt(4)\n\tds_read_b128 v[160:163], v5 offset:0\n\t"
      "v_smfmac_f32_16x16x64_bf16 v[16:19], v[244:247], a[136:143], v6\n\tds_read_b128 v[164:167], v5 offset:1024\n\tds_read_b128 v[168:171], v5 offset:2048\n\t"
      "v_smfmac_f32_16x16x64_bf16 v[20:23], v[244:247], v[200:207], v6\n\ts_waitcnt lgkmcnt(6)\n\tds_read_b128 v[172:175], v5 offset:3072\n\t"
      "v_smfmac_f32_16x16x64_bf16 v[16:19], v[248:251], a[144:151], v6\n\tds_read_b128 v[176:179], v5 offset:4096\n\tds_read_b128 v[180:183], v5 offset:5120\n\t"
      "v_smfmac_f32_16x16x64_bf16 v[20:23], v[248:251], a[152:159], v6\n\ts_waitcnt lgkmcnt(8)\n\tds_read_b128 v[184:187], v5 offset:6144\n\t"
      "v_smfmac_f32_16x16x64_bf16 v[16:19], v[24:27], a[160:167], v6\n\tds_read_b128 v[188:191], v5 offset:7168\n\tds_read_b128 v[192:195], v5 offset:8192\n\t"
      "v_smfmac_f32_16x16x64_bf16 v[20:23], v[24:27], v[208:215], v6\n\ts_waitcnt lgkmcnt(10)\n\tds_read_b128 v[196:199], v5 offset:9216\n\t"
      "v_smfmac_f32_16x16x64_bf16 v[16:19], v[28:31], a[168:175], v6\n\tds_read_b128 v[200:203], v5 offset:10240\n\tds_read_b128 v[204:207], v5 offset:11264\n\t"
      "v_smfmac_f32_16x16x64_bf16 v[20:23], v[28:31], a[176:183], v6\n\ts_waitcnt lgkmcnt(12)\n\tds_read_b128 v[208:211], v5 offset:12288\n\t"
      "v_smfmac_f32_16x16x64_bf16 v[16:19], v[224:227], a[184:191], v6\n\tds_read_b128 v[212:215], v5 offset:13312\n\t"
      "v_smfmac_f32_16x16x64_bf16 v[20:23], v[224:227], v[216:223], v6\n\tds_read_b128 v[216:219], v5 offset:14336\n\t"
      TK4
      "v_mov_b64 v[228:229], 0\n\tv_mov_b64 v[230:231], 0\n\tv_mov_b64 v[232:233], 0\n\tv_mov_b64 v[234:235], 0\n\tv_mov_b64 v[236:237], 0\n\tv_add_f32 v16, v16, v17\n\t"
      "v_add_f32 v18, v18, v19\n\tv_add_f32 v20, v20, v21\n\tv_add_f32 v22, v22, v23\n\tv_cvt_pk_bf16_f32 v16, v16, v20\n\tv_cvt_pk_bf16_f32 v18, v18, v22\n\t"
      "ds_write2_b32 v7, v16, v18 offset1:68\n\tv_mov_b64 v[238:239], 0\n\tv_mov_b64 v[224:225], 0\n\tv_mov_b64 v[226:227], 0\n\tv_mov_b64 v[24:25], 0\n\t"
      "v_mov_b64 v[26:27], 0\n\tv_mov_b64 v[28:29], 0\n\tv_mov_b64 v[30:31], 0\n\ts_waitcnt lgkmcnt(0)\n\ts_barrier\n\t"
      TK5
      "ds_read_b128 v[240:243], v8 offset:0\n\tds_read_b128 v[244:247], v8 offset:64\n\tds_read_b128 v[248:251], v8 offset:128\n\tds_read_b128 v[252:255], v8 offset:192\n\t"
      "v_mov_b64 v[16:17], 0\n\tv_mov_b64 v[18:19], 0\n\tv_mov_b64 v[20:21], 0\n\tv_mov_b64 v[22:23], 0\n\ts_waitcnt lgkmcnt(3)\n\t"
      "v_smfmac_f32_16x16x64_bf16 v[224:227], v[240:243], v[32:39], v12\n\tds_read_b128 v[220:223], v5 offset:15360\n\t"
      "v_smfmac_f32_16x16x64_bf16 v[228:231], v[240:243], v[64:71], v12\n\ts_waitcnt lgkmcnt(3)\n\tv_smfmac_f32_16x16x64_bf16 v[224:227], v[244:247], v[40:47], v12\n\t"
      "v_smfmac_f32_16x16x64_bf16 v[228:231], v[244:247], v[72:79], v12\n\ts_waitcnt lgkmcnt(2)\n\tv_smfmac_f32_16x16x64_bf16 v[224:227], v[248:251], v[48:55], v12\n\t"
      "v_smfmac_f32_16x16x64_bf16 v[228:231], v[248:251], v[80:87], v12\n\ts_waitcnt lgkmcnt(1)\n\tv_smfmac_f32_16x16x64_bf16 v[224:227], v[252:255], v[56:63], v12\n\t"
      "v_smfmac_f32_16x16x64_bf16 v[228:231], v[252:255], v[88:95], v12\n\ts_nop 1\n\tv_smfmac_f32_16x16x64_bf16 v[232:235], v[240:243], v[96:103], v12\n\ts_nop 2\n\t"
      "v_smfmac_f32_16x16x64_bf16 v[236:239], v[240:243], v[128:135], v12\n\tv_cvt_pk_bf16_f32 v224, v224, v225\n\t"
      "v_smfmac_f32_16x16x64_bf16 v[232:235], v[244:247], v[104:111], v12\n\tv_cvt_pk_bf16_f32 v225, v226, v227\n\t"
      "v_smfmac_f32_16x16x64_bf16 v[236:239], v[244:247], v[136:143], v12\n\tbuffer_store_dwordx2 v[224:225], v9, s[8:11], s43 offen sc1\n\t"
      "v_smfmac_f32_16x16x64_bf16 v[232:235], v[248:251], v[112:119], v12\n\ts_add_u32 s43, s43, 0x8000\n\tv_cvt_pk_bf16_f32 v228, v228, v229\n\t"
      "v_smfmac_f32_16x16x64_bf16 v[236:239], v[248:251], v[144:151], v12\n\tv_cvt_pk_bf16_f32 v229, v230, v231\n\t"
      "v_smfmac_f32_16x16x64_bf16 v[232:235], v[252:255], v[120:127], v12\n\tbuffer_store_dwordx2 v[228:229], v9, s[8:11], s43 offen sc1\n\t"
      "v_smfmac_f32_16x16x64_bf16 v[236:239], v[252:255], v[152:159], v12\n\ts_nop 1\n\ts_add_u32 s43, s43, 0x8000\n\t"
      "v_smfmac_f32_16x16x64_bf16 v[24:27], v[240:243], a[192:199], v12\n\ts_nop 2\n\tv_smfmac_f32_16x16x64_bf16 v[28:31], v[240:243], a[224:231], v12\n\t"
      "v_cvt_pk_bf16_f32 v232, v232, v233\n\tv_smfmac_f32_16x16x64_bf16 v[24:27], v[244:247], a[200:207], v12\n\tv_cvt_pk_bf16_f32 v233, v234, v235\n\t"
      "v_smfmac_f32_16x16x64_bf16 v[28:31], v[244:247], a[232:239], v12\n\tbuffer_store_dwordx2 v[232:233], v9, s[8:11], s43 offen sc1\n\t"
      "v_smfmac_f32_16x16x64_bf16 v[24:27], v[248:251], a[208:215], v12\n\ts_add_u32 s43, s43, 0x8000\n\tv_cvt_pk_bf16_f32 v236, v236, v237\n\t"
      "v_smfmac_f32_16x16x64_bf16 v[28:31], v[248:251], a[240:247], v12\n\tv_cvt_pk_bf16_f32 v237, v238, v239\n\t"
      "v_smfmac_f32_16x16x64_bf16 v[24:27], v[252:255], a[216:223], v12\n\ts_waitcnt lgkmcnt(0)\n\tbuffer_store_dwordx2 v[236:237], v9, s[8:11], s43 offen sc1\n\t"
      "v_smfmac_f32_16x16x64_bf16 v[28:31], v[252:255], a[248:255], v12\n\ts_nop 1\n\ts_add_u32 s43, s43, 0x8000\n\t"
      "v_smfmac_f32_16x16x64_bf16 v[16:19], v[240:243], v[160:167], v12\n\ts_nop 2\n\tv_smfmac_f32_16x16x64_bf16 v[20:23], v[240:243], v[192:199], v12\n\t"
      "v_cvt_pk_bf16_f32 v24, v24, v25\n\tv_smfmac_f32_16x16x64_bf16 v[16:19], v[244:247], v[168:175], v12\n\tv_cvt_pk_bf16_f32 v25, v26, v27\n\t"
      "v_smfmac_f32_16x16x64_bf16 v[20:23], v[244:247], v[200:207], v12\n\tbuffer_store_dwordx2 v[24:25], v9, s[8:11], s43 offen sc1\n\t"
      "v_smfmac_f32_16x16x64_bf16 v[16:19], v[248:251], v[176:183], v12\n\ts_add_u32 s43, s43, 0x8000\n\tv_cvt_pk_bf16_f32 v28, v28, v29\n\t"
      "v_smfmac_f32_16x16x64_bf16 v[20:23], v[248:251], v[208:215], v12\n\tv_cvt_pk_bf16_f32 v29, v30, v31\n\t"
      "v_smfmac_f32_16x16x64_bf16 v[16:19], v[252:255], v[184:191], v12\n\tbuffer_store_dwordx2 v[28:29], v9, s[8:11], s43 offen sc1\n\t"
      "v_smfmac_f32_16x16x64_bf16 v[20:23], v[252:255], v[216:223], v12\n\ts_add_u32 s43, s43, 0x8000\n\ts_nop 4\n\tv_cvt_pk_bf16_f32 v16, v16, v17\n\t"
      "v_cvt_pk_bf16_f32 v17, v18, v19\n\tv_cvt_pk_bf16_f32 v20, v20, v21\n\tv_cvt_pk_bf16_f32 v21, v22, v23\n\tbuffer_store_dwordx2 v[16:17], v9, s[8:11], s43 offen sc1\n\t"
      "s_add_u32 s43, s43, 0x8000\n\tbuffer_store_dwordx2 v[20:21], v9, s[8:11], s43 offen sc1\n\t"
      TK6
      "s_cmp_lg_u32 s16, 1\n\ts_cbranch_scc1 69f\n\t"
      STS("s_memrealtime s[46:47]\n\t")
      "74:\n\tbuffer_load_dword v14, v11, s[8:11], s41 offen sc1\n\ts_waitcnt vmcnt(0)\n\t"
      "76:\n\tv_max_u32_dpp v14, v14, v14 row_ror:8 row_mask:0xf bank_mask:0xf\n\ts_nop 1\n\tv_max_u32_dpp v14, v14, v14 row_ror:4 row_mask:0xf bank_mask:0xf\n\ts_nop 1\n\t"
      "v_max_u32_dpp v14, v14, v14 row_ror:2 row_mask:0xf bank_mask:0xf\n\ts_nop 1\n\tv_max_u32_dpp v14, v14, v14 row_ror:1 row_mask:0xf bank_mask:0xf\n\ts_nop 0\n\t"
      "v_readlane_b32 s52, v14, 0\n\tv_readlane_b32 s48, v14, 16\n\ts_max_u32 s52, s52, s48\n\ts_cmp_lg_u32 s52, -1\n\ts_cbranch_scc1 75f\n\ts_cmp_eq_u32 s18, 0\n\t"
      "s_cbranch_scc1 75f\n\ts_sub_u32 s18, s18, 1\n\ts_branch 74b\n\t"
      "75:\n\ts_add_u32 s19, s52, s23\n\t"
      STS("s_waitcnt lgkmcnt(0)\n\ts_lshl_b32 s46, s46, 2\n\ts_sub_u32 s46, s52, s46\n\ts_add_u32 s61, s61, s46\n\ts_min_i32 s21, s21, s46\n\ts_cmp_lt_u32 s17, 8\n\ts_cselect_b32 s21, 1000, s21\n\t")
      "s_branch 69f\n\t"
      "86:\n\t"
      STS("s_add_u32 s50, s50, 1\n\t")
      "s_waitcnt lgkmcnt(0)\n\tbuffer_load_dwordx4 v[240:243], v0, s[8:11], s68 offen sc1\n\tbuffer_load_dwordx4 v[244:247], v0, s[8:11], s69 offen sc1\n\t"
      "buffer_load_dwordx4 v[248:251], v0, s[8:11], s70 offen sc1\n\tbuffer_load_dwordx4 v[252:255], v0, s[8:11], s71 offen sc1\n\ts_nop 3\n\tv_mov_b64 v[16:17], 0\n\t"
      "v_mov_b64 v[18:19], 0\n\tv_mov_b64 v[20:21], 0\n\tv_mov_b64 v[22:23], 0\n\ts_nop 1\n\ts_branch 84f\n\t"
      "83:\n\tbuffer_load_dwordx4 v[240:243], v0, s[8:11], s68 offen sc1\n\tbuffer_load_dwordx4 v[244:247], v0, s[8:11], s69 offen sc1\n\t"
      "buffer_load_dwordx4 v[248:251], v0, s[8:11], s70 offen sc1\n\tbuffer_load_dwordx4 v[252:255], v0, s[8:11], s71 offen sc1\n\t"
      "84:\n\ts_waitcnt vmcnt(0)\n\tv_max3_u32 v13, v240, v241, v242\n\tv_max_u32 v13, v13, v243\n\tv_max3_u32 v13, v13, v244, v245\n\tv_max3_u32 v13, v13, v246, v247\n\t"
      "v_max3_u32 v13, v13, v248, v249\n\tv_max3_u32 v13, v13, v250, v251\n\tv_max3_u32 v13, v13, v252, v253\n\tv_max3_u32 v13, v13, v254, v255\n\tv_cmp_eq_u32 vcc, -1, v13\n\t"
      "s_cbranch_vccz 85f\n\ts_cmp_eq_u32 s18, 0\n\ts_cbranch_scc1 85f\n\ts_sub_u32 s18, s18, 1\n\ts_branch 83b\n\t"
      "85:\n\tv_smfmac_f32_16x16x64_bf16 v[16:19], v[240:243], a[0:7], v6\n\tv_smfmac_f32_16x16x64_bf16 v[20:23], v[240:243], a[8:15], v6\n\t"
      "ds_write_b128 v1, v[240:243] offset:0\n\tds_write_b128 v1, v[244:247] offset:1024\n\tds_write_b128 v1, v[248:251] offset:2048\n\t"
      "ds_write_b128 v1, v[252:255] offset:3072\n\tv_mov_b32 v13, 0\n\ts_mov_b64 vcc, 0\n\ts_waitcnt lgkmcnt(0)\n\ts_branch 20b\n\t"
      "87:\n\t"
      STS("s_add_u32 s50, s50, 1\n\t")
      "s_waitcnt lgkmcnt(0)\n\tbuffer_load_dwordx4 v[244:247], v0, s[8:11], s69 offen sc1\n\tbuffer_load_dwordx4 v[248:251], v0, s[8:11], s70 offen sc1\n\t"
      "buffer_load_dwordx4 v[252:255], v0, s[8:11], s71 offen sc1\n\ts_nop 3\n\tv_mov_b64 v[16:17], 0\n\tv_mov_b64 v[18:19], 0\n\tv_mov_b64 v[20:21], 0\n\t"
      "v_mov_b64 v[22:23], 0\n\ts_nop 1\n\tv_smfmac_f32_16x16x64_bf16 v[16:19], v[240:243], a[0:7], v6\n\tv_smfmac_f32_16x16x64_bf16 v[20:23], v[240:243], a[8:15], v6\n\t"
      "s_branch 84f\n\t"
      "83:\n\tbuffer_load_dwordx4 v[244:247], v0, s[8:11], s69 offen sc1\n\tbuffer_load_dwordx4 v[248:251], v0, s[8:11], s70 offen sc1\n\t"
      "buffer_load_dwordx4 v[252:255], v0, s[8:11], s71 offen sc1\n\t"
      "84:\n\ts_waitcnt vmcnt(0)\n\tv_max3_u32 v13, v244, v245, v246\n\tv_max_u32 v13, v13, v247\n\tv_max3_u32 v13, v13, v248, v249\n\tv_max3_u32 v13, v13, v250, v251\n\t"
      "v_max3_u32 v13, v13, v252, v253\n\tv_max3_u32 v13, v13, v254, v255\n\tv_cmp_eq_u32 vcc, -1, v13\n\ts_cbranch_vccz 85f\n\ts_cmp_eq_u32 s18, 0\n\ts_cbranch_scc1 85f\n\t"
      "s_sub_u32 s18, s18, 1\n\ts_branch 83b\n\t"
      "85:\n\tv_smfmac_f32_16x16x64_bf16 v[16:19], v[244:247], a[16:23], v6\n\tv_smfmac_f32_16x16x64_bf16 v[20:23], v[244:247], v[160:167], v6\n\t"
      "v_smfmac_f32_16x16x64_bf16 v[16:19], v[248:251], a[24:31], v6\n\tv_smfmac_f32_16x16x64_bf16 v[20:23], v[248:251], a[32:39], v6\n\t"
      "v_smfmac_f32_16x16x64_bf16 v[16:19], v[24:27], a[48:55], v6\n\tds_write_b128 v1, v[244:247] offset:1024\n\tds_write_b128 v1, v[248:251] offset:2048\n\t"
      "ds_write_b128 v1, v[252:255] offset:3072\n\tv_mov_b32 v13, 0\n\ts_mov_b64 vcc, 0\n\ts_waitcnt lgkmcnt(0)\n\ts_branch 21b\n\t"
      "88:\n\t"
      STS("s_add_u32 s50, s50, 1\n\t")
      "s_waitcnt lgkmcnt(0)\n\tbuffer_load_dwordx4 v[248:251], v0, s[8:11], s70 offen sc1\n\tbuffer_load_dwordx4 v[252:255], v0, s[8:11], s71 offen sc1\n\ts_nop 3\n\t"
      "v_mov_b64 v[16:17], 0\n\tv_mov_b64 v[18:19], 0\n\tv_mov_b64 v[20:21], 0\n\tv_mov_b64 v[22:23], 0\n\ts_nop 1\n\t"
      "v_smfmac_f32_16x16x64_bf16 v[16:19], v[240:243], a[0:7], v6\n\tv_smfmac_f32_16x16x64_bf16 v[20:23], v[240:243], a[8:15], v6\n\t"
      "v_smfmac_f32_16x16x64_bf16 v[16:19], v[244:247], a[16:23], v6\n\tv_smfmac_f32_16x16x64_bf16 v[20:23], v[244:247], v[160:167], v6\n\ts_branch 84f\n\t"
      "83:\n\tbuffer_load_dwordx4 v[248:251], v0, s[8:11], s70 offen sc1\n\tbuffer_load_dwordx4 v[252:255], v0, s[8:11], s71 offen sc1\n\t"
      "84:\n\ts_waitcnt vmcnt(0)\n\tv_max3_u32 v13, v248, v249, v250\n\tv_max_u32 v13, v13, v251\n\tv_max3_u32 v13, v13, v252, v253\n\tv_max3_u32 v13, v13, v254, v255\n\t"
      "v_cmp_eq_u32 vcc, -1, v13\n\ts_cbranch_vccz 85f\n\ts_cmp_eq_u32 s18, 0\n\ts_cbranch_scc1 85f\n\ts_sub_u32 s18, s18, 1\n\ts_branch 83b\n\t"
      "85:\n\tv_smfmac_f32_16x16x64_bf16 v[16:19], v[248:251], a[24:31], v6\n\tv_smfmac_f32_16x16x64_bf16 v[20:23], v[248:251], a[32:39], v6\n\t"
      "v_smfmac_f32_16x16x64_bf16 v[16:19], v[24:27], a[48:55], v6\n\tv_smfmac_f32_16x16x64_bf16 v[20:23], v[24:27], a[56:63], v6\n\t"
      "v_smfmac_f32_16x16x64_bf16 v[16:19], v[252:255], a[40:47], v6\n\tv_smfmac_f32_16x16x64_bf16 v[20:23], v[252:255], v[168:175], v6\n\t"
      "v_smfmac_f32_16x16x64_bf16 v[16:19], v[28:31], a[64:71], v6\n\tv_smfmac_f32_16x16x64_bf16 v[20:23], v[28:31], v[176:183], v6\n\t"
      "v_smfmac_f32_16x16x64_bf16 v[16:19], v[224:227], a[72:79], v6\n\tv_smfmac_f32_16x16x64_bf16 v[20:23], v[224:227], a[80:87], v6\n\t"
      "v_smfmac_f32_16x16x64_bf16 v[16:19], v[228:231], a[88:95], v6\n\tds_write_b128 v1, v[248:251] offset:2048\n\tds_write_b128 v1, v[252:255] offset:3072\n\t"
      "v_mov_b32 v13, 0\n\ts_mov_b64 vcc, 0\n\ts_waitcnt lgkmcnt(0)\n\ts_branch 22b\n\t"
      "69:\n\t"
      TK7
      STS("s_cmp_lt_i32 s17, 8\n\ts_cselect_b32 s50, 0, s50\n\ts_cselect_b32 s51, 0, s51\n\ts_cselect_b32 s61, 0, s61\n\t")
      TKS("s_waitcnt lgkmcnt(0)\n\ts_cmp_lt_i32 s17, 0\n\ts_cbranch_scc1 98f\n\ts_cmp_lg_u32 s16, s89\n\ts_cbranch_scc1 98f\n\ts_mov_b64 exec, 1\n\ts_lshl_b32 s48, s17, 4\n\ts_add_u32 s48, s48, 161056\n\tv_mov_b32 v13, s48\n\ts_sub_u32 s48, s72, s88\n\ts_min_u32 s48, s48, 0xffff\n\tv_mov_b32 v14, s48\n\tds_write_b16 v13, v14 offset:0\n\ts_sub_u32 s48, s74, s72\n\ts_min_u32 s48, s48, 0xffff\n\tv_mov_b32 v14, s48\n\tds_write_b16 v13, v14 offset:2\n\ts_sub_u32 s48, s76, s74\n\ts_min_u32 s48, s48, 0xffff\n\tv_mov_b32 v14, s48\n\tds_write_b16 v13, v14 offset:4\n\ts_sub_u32 s48, s78, s76\n\ts_min_u32 s48, s48, 0xffff\n\tv_mov_b32 v14, s48\n\tds_write_b16 v13, v14 offset:6\n\ts_sub_u32 s48, s80, s78\n\ts_min_u32 s48, s48, 0xffff\n\tv_mov_b32 v14, s48\n\tds_write_b16 v13, v14 offset:8\n\ts_sub_u32 s48, s82, s80\n\ts_min_u32 s48, s48, 0xffff\n\tv_mov_b32 v14, s48\n\tds_write_b16 v13, v14 offset:10\n\ts_sub_u32 s48, s84, s82\n\ts_min_u32 s48, s48, 0xffff\n\tv_mov_b32 v14, s48\n\tds_write_b16 v13, v14 offset:12\n\ts_sub_u32 s48, s86, s84\n\ts_min_u32 s48, s48, 0xffff\n\tv_mov_b32 v14, s48\n\tds_write_b16 v13, v14 offset:14\n\ts_mov_b64 exec, -1\n\t98:\n\ts_mov_b32 s88, s86\n\t")
      "s_add_u32 s17, s17, 1\n\ts_cmp_lt_u32 s17, 64\n\ts_cbranch_scc1 10b\n\ts_waitcnt vmcnt(0) lgkmcnt(0)\n\t"
      TKS("s_mul_i32 s48, s16, 72\n\ts_add_u32 s48, s48, 162080\n\tv_mov_b32 v224, s48\n\tds_read_b32 v225, v224 offset:0\n\ts_waitcnt lgkmcnt(0)\n\tv_readfirstlane_b32 s72, v225\n\tds_read_b32 v225, v224 offset:4\n\ts_waitcnt lgkmcnt(0)\n\tv_readfirstlane_b32 s73, v225\n\tds_read_b32 v225, v224 offset:8\n\ts_waitcnt lgkmcnt(0)\n\tv_readfirstlane_b32 s74, v225\n\tds_read_b32 v225, v224 offset:12\n\ts_waitcnt lgkmcnt(0)\n\tv_readfirstlane_b32 s75, v225\n\tds_read_b32 v225, v224 offset:16\n\ts_waitcnt lgkmcnt(0)\n\tv_readfirstlane_b32 s76, v225\n\tds_read_b32 v225, v224 offset:20\n\ts_waitcnt lgkmcnt(0)\n\tv_readfirstlane_b32 s77, v225\n\tds_read_b32 v225, v224 offset:24\n\ts_waitcnt lgkmcnt(0)\n\tv_readfirstlane_b32 s78, v225\n\tds_read_b32 v225, v224 offset:28\n\ts_waitcnt lgkmcnt(0)\n\tv_readfirstlane_b32 s79, v225\n\tds_read_b32 v225, v224 offset:32\n\ts_waitcnt lgkmcnt(0)\n\tv_readfirstlane_b32 s80, v225\n\tds_read_b32 v225, v224 offset:36\n\ts_waitcnt lgkmcnt(0)\n\tv_readfirstlane_b32 s81, v225\n\tds_read_b32 v225, v224 offset:40\n\ts_waitcnt lgkmcnt(0)\n\tv_readfirstlane_b32 s82, v225\n\tds_read_b32 v225, v224 offset:44\n\ts_waitcnt lgkmcnt(0)\n\tv_readfirstlane_b32 s83, v225\n\tds_read_b32 v225, v224 offset:48\n\ts_waitcnt lgkmcnt(0)\n\tv_readfirstlane_b32 s84, v225\n\tds_read_b32 v225, v224 offset:52\n\ts_waitcnt lgkmcnt(0)\n\tv_readfirstlane_b32 s85, v225\n\tds_read_b32 v225, v224 offset:56\n\ts_waitcnt lgkmcnt(0)\n\tv_readfirstlane_b32 s86, v225\n\tds_read_b32 v225, v224 offset:60\n\ts_waitcnt lgkmcnt(0)\n\tv_readfirstlane_b32 s87, v225\n\tds_read_b32 v225, v224 offset:64\n\ts_waitcnt lgkmcnt(0)\n\tv_readfirstlane_b32 s88, v225\n\tds_read_b32 v225, v224 offset:68\n\ts_waitcnt lgkmcnt(0)\n\tv_readfirstlane_b32 s89, v225\n\t")
      STS("s_lshl_b32 s48, s16, 2\n\ts_add_u32 s48, s48, 162944\n\tv_mov_b32 v0, s48\n\tv_mov_b32 v1, s50\n\tds_write_b32 v0, v1\n\ts_cmp_lg_u32 s16, 0\n\ts_cbranch_scc1 97f\n\tv_mov_b32 v1, s51\n\tds_write_b32 v0, v1 offset:16\n\t97:\n\ts_cmp_lg_u32 s16, 1\n\ts_cbranch_scc1 95f\n\tv_mov_b32 v1, s61\n\tds_write_b32 v0, v1 offset:16\n\tv_mov_b32 v1, s21\n\tds_write_b32 v0, v1 offset:20\n\tv_mov_b32 v1, s22\n\tds_write_b32 v0, v1 offset:24\n\t95:\n\t")
      "s_cmp_lg_u32 s18, 0\n\ts_cbranch_scc1 96f\n\tv_mov_b32 v1, 1\n\tbuffer_store_dword v1, off, s[12:15], 0 offset:3328 sc1\n\t"
      "96:\n\t"
      : "+{v[0:31]}"(k), "+{v[160:191]}"(s0), "+{v[192:223]}"(s1), ALL_REGS :: "memory", "vcc", "scc", S_CLOB TK_CLOB);
}

DEV void y_role(Ctx& c, int y) {
  const KArg p = enter(c);
  constexpr int ZR = 0, UL = 131072, CW = 155648;
  c.budget = SPIN_CAP;
  const int w = c.w, lane = c.lane = fresh_lane(), g = lane >> 4, cc = lane & 15;
  const v4u sent4 = {SENT, SENT, SENT, SENT};
  unsigned m0 = 0;
  if (w == 0) {
    unsigned best[4] = {~0u, ~0u, ~0u, ~0u};
#pragma unroll
    for (int r = 0; r < 8; ++r) {
      VMWAIT(0);
      const unsigned t0 = (unsigned)__builtin_amdgcn_s_memtime();
      LGKM(0);
      st4s(c.rs, ((r & 2) ? amap(y * 256, 0) : y * 16384) + (r & 1) * 8192 + (lane & 31) * 4, (r & 2) ? R_MEM : R_U, SENT);
      VMWAIT(0);
      const unsigned t1 = (unsigned)__builtin_amdgcn_s_memtime();
      LGKM(0);
      best[r & 3] = min(best[r & 3], t1 - t0);
    }
    if (lane < 8) *(LAS unsigned*)(c.L + CW + lane * 4) = 0u;
#pragma unroll
    for (int i = 0; i < 32; ++i) st16s(c.rs, y * 16384 + (i & 3) * 1024 + lane * 16, R_U + (i >> 4) * (64 * 16384) + ((i >> 2) & 3) * 4096, sent4);
#pragma unroll
    for (int i = 0; i < 10; ++i) st16s(c.rs, y * 10240 + lane * 16, R_MEM + i * 1024, sent4);
#pragma unroll
    for (int i = 0; i < 16; ++i) st16s(c.rs, y * 16384 + lane * 16, R_PT + i * 1024, sent4);
    m0 = *(const GAS unsigned*)(p->mem0 + (lane >> 3) * 1024 + 16 * y + 2 * (lane & 7));
    m0 = m0 < SENT - 1u ? m0 : SENT - 1u;
    VMWAIT(0);
    claim(c, (best[1] < best[0] ? 1u << 16 : 0u) | (best[3] < best[2] ? 1u << 24 : 0u));
  }
  if (w == 1 || w == 2) {
    const rsrc_t rx = mkrs(p->x, 2048 * 2048);
    unsigned t = 0;
#pragma unroll
    for (int i = 0; i < 4; ++i) t += __builtin_amdgcn_raw_buffer_load_b32(rx, (2 * y + w - 1) * 8192 + lane * 128, i * MB, 0);
    asm volatile("" :: "v"(t));
  }
  BAR();
  const unsigned vote = wait_verdict(c);
  const int par = vote_b(vote), para = vote_a(vote);
  const int mo = amap(y * 256 + lane * 4, par);
  if (w == 0) st4s(c.rs, mo, R_MEM, m0);
  STAMP(0);
  volatile LAS unsigned* const ZFULL = (volatile LAS unsigned*)(c.L + CW);
  volatile LAS unsigned* const GO = ZFULL + 1;
  volatile LAS unsigned* const TDONE = ZFULL + 2;
  volatile LAS unsigned* const PUB = ZFULL + 4;
  auto lds_wait = [&](volatile LAS unsigned* wd, unsigned need) {
#pragma unroll 1
    while (__builtin_amdgcn_readfirstlane(*wd) < need) { if (spend(c)) break; __builtin_amdgcn_s_sleep(1); }
    asm volatile("" ::: "memory");
  };
  auto lds_post = [&](volatile LAS unsigned* wd, unsigned v) { LGKM(0); *wd = v; };
  TICK_DECL;
  if (w == 0) {
    v32u k;
#pragma unroll
    for (int i = 0; i < 4; ++i) {
      const int o = (i + (y >> 4)) & 3;
      k[i] = amap(o * 4096 + lane * 16, par);
      k[4 + i] = amap(16384 + o * 1024 + 2 * g * 128 + cc * 8, par);
      k[8 + i] = R_U + y * 16384 + para * 8192 + o * 2048 + lane * 16;
      k[12 + i] = ZR + o * 8192 + lane * 16;
      k[16 + i] = UL + o * 2048 + lane * 16;
    }
    k[20] = amap(y * 256 + (2 * g + (cc & 1)) * 32 + (cc >> 1) * 4, par);
    k[21] = (cc & 1) ? 0x03020706 : 0x05040100;
    k[22] = (lane & 1) ? 0xEEEE : 0x4444;
    k[23] = amap(24576 + (y >> 4) * 4096 + (lane & 31) * 128, par);
    k[24] = CW;
#pragma unroll
    for (int i = 25; i < 32; ++i) k[i] = 0;
    v32u s0 = {RSRCS, 0, 0, c.budget, 0, 0, PACE, C1, CU - CUG * (unsigned)(3 - ((y >> 2) & 3)), KH, ~0u, 0};
    v16u x0; v32u z[4], x[3];
    asm volatile(
      "s_nop 0\n\tv_readfirstlane_b32 s8, v160\n\tv_readfirstlane_b32 s9, v161\n\tv_readfirstlane_b32 s10, v162\n\tv_readfirstlane_b32 s11, v163\n\t"
      "v_readfirstlane_b32 s12, v164\n\tv_readfirstlane_b32 s13, v165\n\tv_readfirstlane_b32 s14, v166\n\tv_readfirstlane_b32 s15, v167\n\tv_readfirstlane_b32 s16, v168\n\t"
      "v_readfirstlane_b32 s17, v169\n\tv_readfirstlane_b32 s18, v170\n\tv_readfirstlane_b32 s19, v171\n\tv_readfirstlane_b32 s20, v172\n\tv_readfirstlane_b32 s21, v173\n\t"
      "v_readfirstlane_b32 s22, v174\n\tv_readfirstlane_b32 s23, v175\n\tv_readfirstlane_b32 s24, v176\n\tv_readfirstlane_b32 s25, v177\n\tv_readfirstlane_b32 s26, v178\n\t"
      "v_readfirstlane_b32 s27, v179\n\tv_readfirstlane_b32 s28, v180\n\tv_readfirstlane_b32 s29, v181\n\tv_readfirstlane_b32 s30, v182\n\tv_readfirstlane_b32 s31, v183\n\t"
      "s_nop 4\n\t"
      TKS("s_mul_i32 s48, s16, 72\n\ts_add_u32 s48, s48, 162080\n\tv_mov_b32 v224, s48\n\tv_mov_b32 v225, s72\n\tds_write_b32 v224, v225 offset:0\n\tv_mov_b32 v225, s73\n\tds_write_b32 v224, v225 offset:4\n\tv_mov_b32 v225, s74\n\tds_write_b32 v224, v225 offset:8\n\tv_mov_b32 v225, s75\n\tds_write_b32 v224, v225 offset:12\n\tv_mov_b32 v225, s76\n\tds_write_b32 v224, v225 offset:16\n\tv_mov_b32 v225, s77\n\tds_write_b32 v224, v225 offset:20\n\tv_mov_b32 v225, s78\n\tds_write_b32 v224, v225 offset:24\n\tv_mov_b32 v225, s79\n\tds_write_b32 v224, v225 offset:28\n\tv_mov_b32 v225, s80\n\tds_write_b32 v224, v225 offset:32\n\tv_mov_b32 v225, s81\n\tds_write_b32 v224, v225 offset:36\n\tv_mov_b32 v225, s82\n\tds_write_b32 v224, v225 offset:40\n\tv_mov_b32 v225, s83\n\tds_write_b32 v224, v225 offset:44\n\tv_mov_b32 v225, s84\n\tds_write_b32 v224, v225 offset:48\n\tv_mov_b32 v225, s85\n\tds_write_b32 v224, v225 offset:52\n\tv_mov_b32 v225, s86\n\tds_write_b32 v224, v225 offset:56\n\tv_mov_b32 v225, s87\n\tds_write_b32 v224, v225 offset:60\n\tv_mov_b32 v225, s88\n\tds_write_b32 v224, v225 offset:64\n\tv_mov_b32 v225, s89\n\tds_write_b32 v224, v225 offset:68\n\t")
      TKS("s_mov_b32 s89, 0\n\t")
      "s_memrealtime s[46:47]\n\ts_waitcnt lgkmcnt(0)\n\ts_lshl_b32 s19, s46, 2\n\ts_sub_u32 s19, s19, 0x100000\n\t"
      "10:\n\t"
      "20:\n\tds_read_b32 v25, v24 offset:0\n\ts_waitcnt lgkmcnt(0)\n\tv_readfirstlane_b32 s48, v25\n\ts_cmp_gt_i32 s48, s17\n\ts_cbranch_scc1 21f\n\ts_cmp_eq_u32 s18, 0\n\t"
      "s_cbranch_scc1 21f\n\ts_sub_u32 s18, s18, 1\n\ts_sleep 1\n\ts_branch 20b\n\t"
      "21:\n\ts_and_b32 s48, s17, 3\n\ts_lshl_b32 s49, s48, 15\n\tv_add_u32 v28, s49, v12\n\tv_add_u32 v29, s49, v13\n\tv_add_u32 v30, s49, v14\n\tv_add_u32 v31, s49, v15\n\t"
      "ds_read_b128 a[0:3], v28 offset:0\n\tds_read_b128 a[4:7], v28 offset:1024\n\tds_read_b128 a[8:11], v28 offset:2048\n\tds_read_b128 a[12:15], v28 offset:3072\n\t"
      "ds_read_b128 a[16:19], v28 offset:4096\n\tds_read_b128 a[20:23], v28 offset:5120\n\tds_read_b128 a[24:27], v28 offset:6144\n\tds_read_b128 a[28:31], v28 offset:7168\n\t"
      "ds_read_b128 a[32:35], v29 offset:0\n\tds_read_b128 a[36:39], v29 offset:1024\n\tds_read_b128 a[40:43], v29 offset:2048\n\tds_read_b128 a[44:47], v29 offset:3072\n\t"
      "ds_read_b128 a[48:51], v29 offset:4096\n\tds_read_b128 a[52:55], v29 offset:5120\n\tds_read_b128 a[56:59], v29 offset:6144\n\tds_read_b128 a[60:63], v29 offset:7168\n\t"
      "ds_read_b128 a[64:67], v30 offset:0\n\tds_read_b128 a[68:71], v30 offset:1024\n\tds_read_b128 a[72:75], v30 offset:2048\n\tds_read_b128 a[76:79], v30 offset:3072\n\t"
      "ds_read_b128 a[80:83], v30 offset:4096\n\tds_read_b128 a[84:87], v30 offset:5120\n\tds_read_b128 a[88:91], v30 offset:6144\n\tds_read_b128 a[92:95], v30 offset:7168\n\t"
      "ds_read_b128 a[96:99], v31 offset:0\n\tds_read_b128 a[100:103], v31 offset:1024\n\tds_read_b128 a[104:107], v31 offset:2048\n\t"
      "ds_read_b128 a[108:111], v31 offset:3072\n\tds_read_b128 a[112:115], v31 offset:4096\n\tds_read_b128 a[116:119], v31 offset:5120\n\t"
      "ds_read_b128 a[120:123], v31 offset:6144\n\tds_read_b128 a[124:127], v31 offset:7168\n\ts_lshl_b32 s41, s48, 17\n\ts_add_u32 s41, s41, 0x220000\n\t"
      "s_add_u32 s48, s17, 1\n\ts_and_b32 s48, s48, 3\n\ts_lshl_b32 s40, s48, 15\n\ts_add_u32 s40, s40, 0x200000\n\ts_xor_b32 s42, s40, 0x10000\n\ts_and_b32 s43, s17, 1\n\t"
      "s_lshl_b32 s43, s43, 20\n\tv_mov_b64 v[120:121], 0\n\tv_mov_b64 v[122:123], 0\n\ts_add_u32 s46, s19, s29\n\t"
      TK0
      STC("s_mov_b32 s57, s18\n\t")
      "71:\n\ts_memrealtime vcc\n\ts_waitcnt lgkmcnt(0)\n\ts_lshl_b32 vcc_lo, vcc_lo, 2\n\t"
      "70:\n\ts_sub_u32 s48, s46, vcc_lo\n\ts_cmp_gt_u32 s48, 4000\n\ts_cbranch_scc1 73f\n\ts_cmp_gt_u32 s48, 28\n\ts_cbranch_scc1 71b\n\ts_mul_i32 s48, s48, 45\n\t"
      "s_lshr_b32 s48, s48, 5\n\ts_lshl_b32 s48, s48, 2\n\ts_getpc_b64 vcc\n\t"
      "72:\n\ts_sub_u32 s48, 73f-72b, s48\n\ts_add_u32 vcc_lo, vcc_lo, s48\n\ts_addc_u32 vcc_hi, vcc_hi, 0\n\ts_setpc_b64 vcc\n\t.rept 40\n\ts_nop 0\n\t.endr\n\t"
      "73:\n\t"
      "74:\n\tbuffer_load_dword v27, v23, s[8:11], s41 offen sc1\n\ts_waitcnt vmcnt(0)\n\t"
      "76:\n\ts_memrealtime vcc\n\tv_max_u32_dpp v27, v27, v27 row_ror:8 row_mask:0xf bank_mask:0xf\n\ts_nop 1\n\t"
      "v_max_u32_dpp v27, v27, v27 row_ror:4 row_mask:0xf bank_mask:0xf\n\ts_nop 1\n\tv_max_u32_dpp v27, v27, v27 row_ror:2 row_mask:0xf bank_mask:0xf\n\ts_nop 1\n\t"
      "v_max_u32_dpp v27, v27, v27 row_ror:1 row_mask:0xf bank_mask:0xf\n\ts_nop 0\n\tv_readlane_b32 s52, v27, 0\n\tv_readlane_b32 s48, v27, 16\n\ts_max_u32 s52, s52, s48\n\t"
      "s_cmp_lg_u32 s52, -1\n\ts_cbranch_scc1 75f\n\ts_cmp_eq_u32 s18, 0\n\ts_cbranch_scc1 75f\n\ts_sub_u32 s18, s18, 1\n\ts_waitcnt lgkmcnt(0)\n\ts_branch 74b\n\t"
      "75:\n\ts_add_u32 s47, s52, s27\n\t"
      STC("s_waitcnt lgkmcnt(0)\n\ts_mov_b32 s58, vcc_lo\n\ts_sub_u32 s56, s57, s18\n\ts_lshl_b32 s56, s56, 24\n\t")
      TK1
      "s_waitcnt lgkmcnt(0)\n\ts_lshl2_add_u32 vcc_lo, vcc_lo, 10\n\ts_branch 70f\n\t"
      "71:\n\ts_memrealtime vcc\n\ts_waitcnt lgkmcnt(0)\n\ts_lshl_b32 vcc_lo, vcc_lo, 2\n\t"
      "70:\n\ts_sub_u32 s48, s47, vcc_lo\n\ts_cmp_gt_u32 s48, 4000\n\ts_cbranch_scc1 73f\n\ts_cmp_gt_u32 s48, 28\n\ts_cbranch_scc1 71b\n\ts_mul_i32 s48, s48, 45\n\t"
      "s_lshr_b32 s48, s48, 5\n\ts_lshl_b32 s48, s48, 2\n\ts_getpc_b64 vcc\n\t"
      "72:\n\ts_sub_u32 s48, 73f-72b, s48\n\ts_add_u32 vcc_lo, vcc_lo, s48\n\ts_addc_u32 vcc_hi, vcc_hi, 0\n\ts_setpc_b64 vcc\n\t.rept 40\n\ts_nop 0\n\t.endr\n\t"
      "73:\n\t"
      "50:\n\t"
      TK2
      "buffer_load_dwordx4 v[192:195], v0, s[8:11], s41 offen offset:0 sc1\n\tbuffer_load_dwordx4 v[196:199], v0, s[8:11], s41 offen offset:1024 sc1\n\t"
      "buffer_load_dwordx4 v[200:203], v0, s[8:11], s41 offen offset:2048 sc1\n\tbuffer_load_dwordx4 v[204:207], v0, s[8:11], s41 offen offset:3072 sc1\n\t"
      "buffer_load_dwordx2 v[176:177], v4, s[8:11], s41 offen offset:0 sc1\n\tbuffer_load_dwordx2 v[178:179], v4, s[8:11], s41 offen offset:128 sc1\n\t"
      "buffer_load_dwordx4 v[144:147], v8, s[8:11], s43 offen offset:0 sc1\n\tbuffer_load_dwordx4 v[148:151], v8, s[8:11], s43 offen offset:1024 sc1\n\t"
      "buffer_load_dwordx4 v[208:211], v1, s[8:11], s41 offen offset:0 sc1\n\tbuffer_load_dwordx4 v[212:215], v1, s[8:11], s41 offen offset:1024 sc1\n\t"
      "buffer_load_dwordx4 v[216:219], v1, s[8:11], s41 offen offset:2048 sc1\n\tbuffer_load_dwordx4 v[220:223], v1, s[8:11], s41 offen offset:3072 sc1\n\t"
      "buffer_load_dwordx2 v[180:181], v5, s[8:11], s41 offen offset:0 sc1\n\tbuffer_load_dwordx2 v[182:183], v5, s[8:11], s41 offen offset:128 sc1\n\t"
      "buffer_load_dwordx4 v[152:155], v9, s[8:11], s43 offen offset:0 sc1\n\tbuffer_load_dwordx4 v[156:159], v9, s[8:11], s43 offen offset:1024 sc1\n\t"
      "buffer_load_dwordx4 v[224:227], v2, s[8:11], s41 offen offset:0 sc1\n\tbuffer_load_dwordx4 v[228:231], v2, s[8:11], s41 offen offset:1024 sc1\n\t"
      "buffer_load_dwordx4 v[232:235], v2, s[8:11], s41 offen offset:2048 sc1\n\tbuffer_load_dwordx4 v[236:239], v2, s[8:11], s41 offen offset:3072 sc1\n\t"
      "buffer_load_dwordx2 v[184:185], v6, s[8:11], s41 offen offset:0 sc1\n\tbuffer_load_dwordx2 v[186:187], v6, s[8:11], s41 offen offset:128 sc1\n\t"
      "buffer_load_dwordx4 v[160:163], v10, s[8:11], s43 offen offset:0 sc1\n\tbuffer_load_dwordx4 v[164:167], v10, s[8:11], s43 offen offset:1024 sc1\n\t"
      "buffer_load_dwordx4 v[240:243], v3, s[8:11], s41 offen offset:0 sc1\n\tbuffer_load_dwordx4 v[244:247], v3, s[8:11], s41 offen offset:1024 sc1\n\t"
      "buffer_load_dwordx4 v[248:251], v3, s[8:11], s41 offen offset:2048 sc1\n\tbuffer_load_dwordx4 v[252:255], v3, s[8:11], s41 offen offset:3072 sc1\n\t"
      "buffer_load_dwordx2 v[188:189], v7, s[8:11], s41 offen offset:0 sc1\n\tbuffer_load_dwordx2 v[190:191], v7, s[8:11], s41 offen offset:128 sc1\n\t"
      "buffer_load_dwordx4 v[168:171], v11, s[8:11], s43 offen offset:0 sc1\n\tbuffer_load_dwordx4 v[172:175], v11, s[8:11], s43 offen offset:1024 sc1\n\t"
      TK3
      "s_waitcnt vmcnt(31)\n\t"
      TK4
      "v_smfmac_f32_16x16x64_bf16 v[120:123], v[192:195], a[0:7], v22\n\ts_waitcnt vmcnt(30)\n\tv_smfmac_f32_16x16x64_bf16 v[120:123], v[196:199], a[8:15], v22\n\t"
      "s_waitcnt vmcnt(29)\n\tv_smfmac_f32_16x16x64_bf16 v[120:123], v[200:203], a[16:23], v22\n\ts_waitcnt vmcnt(28)\n\t"
      "v_smfmac_f32_16x16x64_bf16 v[120:123], v[204:207], a[24:31], v22\n\ts_waitcnt vmcnt(24)\n\tv_smfmac_f32_16x16x64_bf16 v[120:123], v[176:179], v[144:151], v22\n\t"
      "s_waitcnt vmcnt(23)\n\tv_smfmac_f32_16x16x64_bf16 v[120:123], v[208:211], a[32:39], v22\n\ts_waitcnt vmcnt(22)\n\t"
      "v_smfmac_f32_16x16x64_bf16 v[120:123], v[212:215], a[40:47], v22\n\ts_waitcnt vmcnt(21)\n\tv_smfmac_f32_16x16x64_bf16 v[120:123], v[216:219], a[48:55], v22\n\t"
      "s_waitcnt vmcnt(20)\n\tv_smfmac_f32_16x16x64_bf16 v[120:123], v[220:223], a[56:63], v22\n\ts_waitcnt vmcnt(16)\n\t"
      "v_smfmac_f32_16x16x64_bf16 v[120:123], v[180:183], v[152:159], v22\n\ts_waitcnt vmcnt(15)\n\tv_smfmac_f32_16x16x64_bf16 v[120:123], v[224:227], a[64:71], v22\n\t"
      "s_waitcnt vmcnt(14)\n\tv_smfmac_f32_16x16x64_bf16 v[120:123], v[228:231], a[72:79], v22\n\ts_waitcnt vmcnt(13)\n\t"
      "v_smfmac_f32_16x16x64_bf16 v[120:123], v[232:235], a[80:87], v22\n\ts_waitcnt vmcnt(12)\n\tv_smfmac_f32_16x16x64_bf16 v[120:123], v[236:239], a[88:95], v22\n\t"
      "s_waitcnt vmcnt(8)\n\tv_smfmac_f32_16x16x64_bf16 v[120:123], v[184:187], v[160:167], v22\n\ts_waitcnt vmcnt(7)\n\t"
      "v_smfmac_f32_16x16x64_bf16 v[120:123], v[240:243], a[96:103], v22\n\ts_waitcnt vmcnt(6)\n\tv_smfmac_f32_16x16x64_bf16 v[120:123], v[244:247], a[104:111], v22\n\t"
      "s_waitcnt vmcnt(5)\n\tv_smfmac_f32_16x16x64_bf16 v[120:123], v[248:251], a[112:119], v22\n\ts_waitcnt vmcnt(4)\n\t"
      "v_smfmac_f32_16x16x64_bf16 v[120:123], v[252:255], a[120:127], v22\n\ts_waitcnt vmcnt(0)\n\tv_smfmac_f32_16x16x64_bf16 v[120:123], v[188:191], v[168:175], v22\n\t"
      "52:\n\t"
      TK5
      "s_nop 7\n\tv_add_f32 v124, v120, v121\n\tv_add_f32 v125, v122, v123\n\tv_cvt_pk_bf16_f32 v126, v124, v125\n\tv_cmp_u_f32 vcc, v124, v125\n\ts_nop 0\n\t"
      "v_mov_b32_dpp v127, v126 quad_perm:[1,0,3,2] row_mask:0xf bank_mask:0xf\n\tv_perm_b32 v126, v127, v126, v21\n\ts_cbranch_vccnz 60f\n\t"
      "51:\n\tbuffer_store_dword v126, v20, s[8:11], s40 offen sc1\n\t"
      TK6
      STS("s_memrealtime s[54:55]\n\t")
      "s_add_u32 s49, s17, 1\n\tv_mov_b32 v26, s49\n\tds_write_b32 v24, v26 offset:16\n\tv_mov_b64 v[116:117], -1\n\tv_mov_b64 v[118:119], -1\n\t"
      "buffer_store_dword v116, v20, s[8:11], s42 offen sc1\n\ts_sub_u32 s49, s17, 3\n\tbuffer_store_dwordx4 v[116:119], v8, s[8:11], s43 offen offset:0 sc1\n\t"
      "buffer_store_dwordx4 v[116:119], v8, s[8:11], s43 offen offset:1024 sc1\n\tbuffer_store_dwordx4 v[116:119], v9, s[8:11], s43 offen offset:0 sc1\n\t"
      "buffer_store_dwordx4 v[116:119], v9, s[8:11], s43 offen offset:1024 sc1\n\tbuffer_store_dwordx4 v[116:119], v10, s[8:11], s43 offen offset:0 sc1\n\t"
      "buffer_store_dwordx4 v[116:119], v10, s[8:11], s43 offen offset:1024 sc1\n\tbuffer_store_dwordx4 v[116:119], v11, s[8:11], s43 offen offset:0 sc1\n\t"
      "buffer_store_dwordx4 v[116:119], v11, s[8:11], s43 offen offset:1024 sc1\n\t"
      "22:\n\tds_read_b32 v25, v24 offset:8\n\ts_waitcnt lgkmcnt(0)\n\tv_readfirstlane_b32 s48, v25\n\ts_cmp_gt_i32 s48, s49\n\ts_cbranch_scc1 23f\n\ts_cmp_eq_u32 s18, 0\n\t"
      "s_cbranch_scc1 23f\n\ts_sub_u32 s18, s18, 1\n\ts_sleep 1\n\ts_branch 22b\n\t"
      "23:\n\t"
      "24:\n\tds_read_b32 v25, v24 offset:12\n\ts_waitcnt lgkmcnt(0)\n\tv_readfirstlane_b32 s48, v25\n\ts_cmp_gt_i32 s48, s49\n\ts_cbranch_scc1 25f\n\ts_cmp_eq_u32 s18, 0\n\t"
      "s_cbranch_scc1 25f\n\ts_sub_u32 s18, s18, 1\n\ts_sleep 1\n\ts_branch 24b\n\t"
      "25:\n\tv_add_u32 v28, s31, v16\n\tv_add_u32 v29, s31, v17\n\tv_add_u32 v30, s31, v18\n\tv_add_u32 v31, s31, v19\n\tds_write_b128 v28, v[144:147] offset:0\n\t"
      "ds_write_b128 v28, v[148:151] offset:1024\n\tds_write_b128 v29, v[152:155] offset:0\n\tds_write_b128 v29, v[156:159] offset:1024\n\t"
      "ds_write_b128 v30, v[160:163] offset:0\n\tds_write_b128 v30, v[164:167] offset:1024\n\tds_write_b128 v31, v[168:171] offset:0\n\t"
      "ds_write_b128 v31, v[172:175] offset:1024\n\ts_add_u32 s31, s31, 0x2000\n\ts_cmp_eq_u32 s31, 0x6000\n\ts_cselect_b32 s31, 0, s31\n\tds_write_b32 v24, v26 offset:4\n\t"
      STC("s_lshl_b32 s48, s17, 4\n\ts_add_u32 s48, s48, 161056\n\tv_mov_b32 v112, s48\n\ts_sub_u32 s48, s52, s19\n\tv_mov_b32 v116, s48\n\tv_mov_b32 v117, s54\n\tv_mov_b32 v118, s56\n\ts_lshl_b32 s58, s58, 2\n\ts_sub_u32 s58, s52, s58\n\tv_mov_b32 v119, s58\n\tds_write_b128 v112, v[116:119]\n\ts_lshl_b32 s48, s54, 2\n\ts_sub_u32 s48, s48, s52\n\ts_add_u32 s51, s51, s48\n\t")
      "s_add_u32 s19, s52, s23\n\t"
      TK7
      STS("s_cmp_lt_i32 s17, 8\n\ts_cselect_b32 s50, 0, s50\n\ts_cselect_b32 s51, 0, s51\n\ts_cselect_b32 s61, 0, s61\n\t")
      TKS("s_waitcnt lgkmcnt(0)\n\ts_cmp_lt_i32 s17, 0\n\ts_cbranch_scc1 98f\n\ts_cmp_lg_u32 s16, s89\n\ts_cbranch_scc1 98f\n\ts_mov_b64 exec, 1\n\ts_lshl_b32 s48, s17, 4\n\ts_add_u32 s48, s48, 161056\n\tv_mov_b32 v25, s48\n\ts_sub_u32 s48, s72, s88\n\ts_min_u32 s48, s48, 0xffff\n\tv_mov_b32 v26, s48\n\tds_write_b16 v25, v26 offset:0\n\ts_sub_u32 s48, s74, s72\n\ts_min_u32 s48, s48, 0xffff\n\tv_mov_b32 v26, s48\n\tds_write_b16 v25, v26 offset:2\n\ts_sub_u32 s48, s76, s74\n\ts_min_u32 s48, s48, 0xffff\n\tv_mov_b32 v26, s48\n\tds_write_b16 v25, v26 offset:4\n\ts_sub_u32 s48, s78, s76\n\ts_min_u32 s48, s48, 0xffff\n\tv_mov_b32 v26, s48\n\tds_write_b16 v25, v26 offset:6\n\ts_sub_u32 s48, s80, s78\n\ts_min_u32 s48, s48, 0xffff\n\tv_mov_b32 v26, s48\n\tds_write_b16 v25, v26 offset:8\n\ts_sub_u32 s48, s82, s80\n\ts_min_u32 s48, s48, 0xffff\n\tv_mov_b32 v26, s48\n\tds_write_b16 v25, v26 offset:10\n\ts_sub_u32 s48, s84, s82\n\ts_min_u32 s48, s48, 0xffff\n\tv_mov_b32 v26, s48\n\tds_write_b16 v25, v26 offset:12\n\ts_sub_u32 s48, s86, s84\n\ts_min_u32 s48, s48, 0xffff\n\tv_mov_b32 v26, s48\n\tds_write_b16 v25, v26 offset:14\n\ts_mov_b64 exec, -1\n\t98:\n\ts_mov_b32 s88, s86\n\t")
      "s_add_u32 s17, s17, 1\n\ts_cmp_lt_u32 s17, 63\n\ts_cbranch_scc1 10b\n\ts_branch 40f\n\t"
      "62:\n\tbuffer_load_dwordx4 v[144:147], v8, s[8:11], s43 offen offset:0 sc1\n\tbuffer_load_dwordx4 v[148:151], v8, s[8:11], s43 offen offset:1024 sc1\n\t"
      "buffer_load_dwordx4 v[152:155], v9, s[8:11], s43 offen offset:0 sc1\n\tbuffer_load_dwordx4 v[156:159], v9, s[8:11], s43 offen offset:1024 sc1\n\t"
      "buffer_load_dwordx4 v[160:163], v10, s[8:11], s43 offen offset:0 sc1\n\tbuffer_load_dwordx4 v[164:167], v10, s[8:11], s43 offen offset:1024 sc1\n\t"
      "buffer_load_dwordx4 v[168:171], v11, s[8:11], s43 offen offset:0 sc1\n\tbuffer_load_dwordx4 v[172:175], v11, s[8:11], s43 offen offset:1024 sc1\n\t"
      STC("s_add_u32 s56, s56, 0x100\n\t")
      "s_waitcnt vmcnt(31)\n\tv_smfmac_f32_16x16x64_bf16 v[120:123], v[192:195], a[0:7], v22\n\ts_waitcnt vmcnt(30)\n\t"
      "v_smfmac_f32_16x16x64_bf16 v[120:123], v[196:199], a[8:15], v22\n\ts_waitcnt vmcnt(29)\n\tv_smfmac_f32_16x16x64_bf16 v[120:123], v[200:203], a[16:23], v22\n\t"
      "s_waitcnt vmcnt(28)\n\tv_smfmac_f32_16x16x64_bf16 v[120:123], v[204:207], a[24:31], v22\n\ts_waitcnt vmcnt(25)\n\t"
      "v_smfmac_f32_16x16x64_bf16 v[120:123], v[208:211], a[32:39], v22\n\ts_waitcnt vmcnt(24)\n\tv_smfmac_f32_16x16x64_bf16 v[120:123], v[212:215], a[40:47], v22\n\t"
      "s_waitcnt vmcnt(23)\n\tv_smfmac_f32_16x16x64_bf16 v[120:123], v[216:219], a[48:55], v22\n\ts_waitcnt vmcnt(22)\n\t"
      "v_smfmac_f32_16x16x64_bf16 v[120:123], v[220:223], a[56:63], v22\n\ts_waitcnt vmcnt(19)\n\tv_smfmac_f32_16x16x64_bf16 v[120:123], v[224:227], a[64:71], v22\n\t"
      "s_waitcnt vmcnt(18)\n\tv_smfmac_f32_16x16x64_bf16 v[120:123], v[228:231], a[72:79], v22\n\ts_waitcnt vmcnt(17)\n\t"
      "v_smfmac_f32_16x16x64_bf16 v[120:123], v[232:235], a[80:87], v22\n\ts_waitcnt vmcnt(16)\n\tv_smfmac_f32_16x16x64_bf16 v[120:123], v[236:239], a[88:95], v22\n\t"
      "s_waitcnt vmcnt(13)\n\tv_smfmac_f32_16x16x64_bf16 v[120:123], v[240:243], a[96:103], v22\n\ts_waitcnt vmcnt(12)\n\t"
      "v_smfmac_f32_16x16x64_bf16 v[120:123], v[244:247], a[104:111], v22\n\ts_waitcnt vmcnt(11)\n\tv_smfmac_f32_16x16x64_bf16 v[120:123], v[248:251], a[112:119], v22\n\t"
      "s_waitcnt vmcnt(10)\n\tv_smfmac_f32_16x16x64_bf16 v[120:123], v[252:255], a[120:127], v22\n\ts_waitcnt vmcnt(6)\n\t"
      "v_smfmac_f32_16x16x64_bf16 v[120:123], v[176:179], v[144:151], v22\n\ts_waitcnt vmcnt(4)\n\tv_smfmac_f32_16x16x64_bf16 v[120:123], v[180:183], v[152:159], v22\n\t"
      "s_waitcnt vmcnt(2)\n\tv_smfmac_f32_16x16x64_bf16 v[120:123], v[184:187], v[160:167], v22\n\ts_waitcnt vmcnt(0)\n\t"
      "v_smfmac_f32_16x16x64_bf16 v[120:123], v[188:191], v[168:175], v22\n\ts_branch 52b\n\t"
      "60:\n\ts_cmp_eq_u32 s30, s17\n\ts_mov_b32 s30, s17\n\ts_cbranch_scc0 61f\n\tv_max3_u32 v128, v144, v145, v146\n\tv_max_u32 v128, v128, v147\n\t"
      "v_max3_u32 v128, v128, v148, v149\n\tv_max3_u32 v128, v128, v150, v151\n\tv_max3_u32 v128, v128, v152, v153\n\tv_max3_u32 v128, v128, v154, v155\n\t"
      "v_max3_u32 v128, v128, v156, v157\n\tv_max3_u32 v128, v128, v158, v159\n\tv_max3_u32 v128, v128, v160, v161\n\tv_max3_u32 v128, v128, v162, v163\n\t"
      "v_max3_u32 v128, v128, v164, v165\n\tv_max3_u32 v128, v128, v166, v167\n\tv_max3_u32 v128, v128, v168, v169\n\tv_max3_u32 v128, v128, v170, v171\n\t"
      "v_max3_u32 v128, v128, v172, v173\n\tv_max3_u32 v128, v128, v174, v175\n\tv_max3_u32 v128, v128, v176, v177\n\tv_max3_u32 v128, v128, v178, v179\n\t"
      "v_max3_u32 v128, v128, v180, v181\n\tv_max3_u32 v128, v128, v182, v183\n\tv_max3_u32 v128, v128, v184, v185\n\tv_max3_u32 v128, v128, v186, v187\n\t"
      "v_max3_u32 v128, v128, v188, v189\n\tv_max3_u32 v128, v128, v190, v191\n\tv_max3_u32 v128, v128, v192, v193\n\tv_max3_u32 v128, v128, v194, v195\n\t"
      "v_max3_u32 v128, v128, v196, v197\n\tv_max3_u32 v128, v128, v198, v199\n\tv_max3_u32 v128, v128, v200, v201\n\tv_max3_u32 v128, v128, v202, v203\n\t"
      "v_max3_u32 v128, v128, v204, v205\n\tv_max3_u32 v128, v128, v206, v207\n\tv_max3_u32 v128, v128, v208, v209\n\tv_max3_u32 v128, v128, v210, v211\n\t"
      "v_max3_u32 v128, v128, v212, v213\n\tv_max3_u32 v128, v128, v214, v215\n\tv_max3_u32 v128, v128, v216, v217\n\tv_max3_u32 v128, v128, v218, v219\n\t"
      "v_max3_u32 v128, v128, v220, v221\n\tv_max3_u32 v128, v128, v222, v223\n\tv_max3_u32 v128, v128, v224, v225\n\tv_max3_u32 v128, v128, v226, v227\n\t"
      "v_max3_u32 v128, v128, v228, v229\n\tv_max3_u32 v128, v128, v230, v231\n\tv_max3_u32 v128, v128, v232, v233\n\tv_max3_u32 v128, v128, v234, v235\n\t"
      "v_max3_u32 v128, v128, v236, v237\n\tv_max3_u32 v128, v128, v238, v239\n\tv_max3_u32 v128, v128, v240, v241\n\tv_max3_u32 v128, v128, v242, v243\n\t"
      "v_max3_u32 v128, v128, v244, v245\n\tv_max3_u32 v128, v128, v246, v247\n\tv_max3_u32 v128, v128, v248, v249\n\tv_max3_u32 v128, v128, v250, v251\n\t"
      "v_max3_u32 v128, v128, v252, v253\n\tv_max3_u32 v128, v128, v254, v255\n\tv_cmp_eq_u32 vcc, -1, v128\n\ts_cbranch_vccz 51b\n\ts_cmp_eq_u32 s18, 0\n\t"
      "s_cbranch_scc1 51b\n\ts_sub_u32 s18, s18, 1\n\t"
      "61:\n\tv_mov_b64 v[120:121], 0\n\tv_mov_b64 v[122:123], 0\n\t"
      STS("s_add_u32 s50, s50, 1\n\t")
      STC("s_add_u32 s56, s56, 1\n\t")
      "s_branch 50b\n\t"
      "40:\n\ts_waitcnt vmcnt(0) lgkmcnt(0)\n\t"
      TKS("s_mul_i32 s48, s16, 72\n\ts_add_u32 s48, s48, 162080\n\tv_mov_b32 v224, s48\n\tds_read_b32 v225, v224 offset:0\n\ts_waitcnt lgkmcnt(0)\n\tv_readfirstlane_b32 s72, v225\n\tds_read_b32 v225, v224 offset:4\n\ts_waitcnt lgkmcnt(0)\n\tv_readfirstlane_b32 s73, v225\n\tds_read_b32 v225, v224 offset:8\n\ts_waitcnt lgkmcnt(0)\n\tv_readfirstlane_b32 s74, v225\n\tds_read_b32 v225, v224 offset:12\n\ts_waitcnt lgkmcnt(0)\n\tv_readfirstlane_b32 s75, v225\n\tds_read_b32 v225, v224 offset:16\n\ts_waitcnt lgkmcnt(0)\n\tv_readfirstlane_b32 s76, v225\n\tds_read_b32 v225, v224 offset:20\n\ts_waitcnt lgkmcnt(0)\n\tv_readfirstlane_b32 s77, v225\n\tds_read_b32 v225, v224 offset:24\n\ts_waitcnt lgkmcnt(0)\n\tv_readfirstlane_b32 s78, v225\n\tds_read_b32 v225, v224 offset:28\n\ts_waitcnt lgkmcnt(0)\n\tv_readfirstlane_b32 s79, v225\n\tds_read_b32 v225, v224 offset:32\n\ts_waitcnt lgkmcnt(0)\n\tv_readfirstlane_b32 s80, v225\n\tds_read_b32 v225, v224 offset:36\n\ts_waitcnt lgkmcnt(0)\n\tv_readfirstlane_b32 s81, v225\n\tds_read_b32 v225, v224 offset:40\n\ts_waitcnt lgkmcnt(0)\n\tv_readfirstlane_b32 s82, v225\n\tds_read_b32 v225, v224 offset:44\n\ts_waitcnt lgkmcnt(0)\n\tv_readfirstlane_b32 s83, v225\n\tds_read_b32 v225, v224 offset:48\n\ts_waitcnt lgkmcnt(0)\n\tv_readfirstlane_b32 s84, v225\n\tds_read_b32 v225, v224 offset:52\n\ts_waitcnt lgkmcnt(0)\n\tv_readfirstlane_b32 s85, v225\n\tds_read_b32 v225, v224 offset:56\n\ts_waitcnt lgkmcnt(0)\n\tv_readfirstlane_b32 s86, v225\n\tds_read_b32 v225, v224 offset:60\n\ts_waitcnt lgkmcnt(0)\n\tv_readfirstlane_b32 s87, v225\n\tds_read_b32 v225, v224 offset:64\n\ts_waitcnt lgkmcnt(0)\n\tv_readfirstlane_b32 s88, v225\n\tds_read_b32 v225, v224 offset:68\n\ts_waitcnt lgkmcnt(0)\n\tv_readfirstlane_b32 s89, v225\n\t")
      STS("s_lshl_b32 s48, s16, 2\n\ts_add_u32 s48, s48, 162944\n\tv_mov_b32 v0, s48\n\tv_mov_b32 v1, s50\n\tds_write_b32 v0, v1\n\ts_cmp_lg_u32 s16, 0\n\ts_cbranch_scc1 97f\n\tv_mov_b32 v1, s51\n\tds_write_b32 v0, v1 offset:16\n\t97:\n\ts_cmp_lg_u32 s16, 0\n\ts_cbranch_scc1 95f\n\tv_mov_b32 v1, s61\n\tds_write_b32 v0, v1 offset:20\n\tv_mov_b32 v1, s21\n\tds_write_b32 v0, v1 offset:24\n\tv_mov_b32 v1, s22\n\tds_write_b32 v0, v1 offset:28\n\t95:\n\t")
      "s_cmp_lg_u32 s18, 0\n\ts_cbranch_scc1 96f\n\tv_mov_b32 v1, 1\n\tbuffer_store_dword v1, off, s[12:15], 0 offset:3328 sc1\n\t"
      "96:\n\t"
      : "+{v[0:31]}"(k), "+{v[160:191]}"(s0), "=&{v[112:127]}"(x0), "=&{v[128:159]}"(x[0]), "=&{v[192:223]}"(x[1]), "=&{v[224:255]}"(x[2]),
        "=&{a[0:31]}"(z[0]), "=&{a[32:63]}"(z[1]), "=&{a[64:95]}"(z[2]), "=&{a[96:127]}"(z[3])
      :: "memory", "vcc", "scc", S_CLOB TK_CLOB);
  } else if (w < 3) {
    v16u k = {(unsigned)(par * 8192 + lane * 16) + ((HACK & 32) << 25), (unsigned)(UL + lane * 16), (unsigned)(ZR + (w == 2 ? lane * 16 : (g >> 1) * 1024 + (g & 1) * 256 + cc * 16)),
              (unsigned)((16 * (w - 1) + cc) * 4096 + (16 * y + 4 * g) * 2) | ((HACK & 1) << 30), CW};
    v32u s0 = {RSRCS, (unsigned)w, 0, c.budget, 0, plo(p->out), phi(p->out), 2048 * 2048 * 2, 0x00020000, 0, 0};
    v32u x[6];
#define TOK_REGS "+{v[0:15]}"(k), "+{v[160:191]}"(s0), "=&{v[32:63]}"(x[0]), "=&{v[64:95]}"(x[1]), "=&{v[96:127]}"(x[2]), "=&{v[128:159]}"(x[3]), "=&{v[192:223]}"(x[4]), "=&{v[224:255]}"(x[5])
    if (w == 1) asm volatile(
      "s_nop 0\n\tv_readfirstlane_b32 s8, v160\n\tv_readfirstlane_b32 s9, v161\n\tv_readfirstlane_b32 s10, v162\n\tv_readfirstlane_b32 s11, v163\n\t"
      "v_readfirstlane_b32 s12, v164\n\tv_readfirstlane_b32 s13, v165\n\tv_readfirstlane_b32 s14, v166\n\tv_readfirstlane_b32 s15, v167\n\tv_readfirstlane_b32 s16, v168\n\t"
      "v_readfirstlane_b32 s17, v169\n\tv_readfirstlane_b32 s18, v170\n\tv_readfirstlane_b32 s19, v171\n\tv_readfirstlane_b32 s20, v172\n\tv_readfirstlane_b32 s21, v173\n\t"
      "v_readfirstlane_b32 s22, v174\n\tv_readfirstlane_b32 s23, v175\n\tv_readfirstlane_b32 s24, v176\n\tv_readfirstlane_b32 s25, v177\n\ts_nop 4\n\t"
      TKS("s_mul_i32 s48, s16, 72\n\ts_add_u32 s48, s48, 162080\n\tv_mov_b32 v224, s48\n\tv_mov_b32 v225, s72\n\tds_write_b32 v224, v225 offset:0\n\tv_mov_b32 v225, s73\n\tds_write_b32 v224, v225 offset:4\n\tv_mov_b32 v225, s74\n\tds_write_b32 v224, v225 offset:8\n\tv_mov_b32 v225, s75\n\tds_write_b32 v224, v225 offset:12\n\tv_mov_b32 v225, s76\n\tds_write_b32 v224, v225 offset:16\n\tv_mov_b32 v225, s77\n\tds_write_b32 v224, v225 offset:20\n\tv_mov_b32 v225, s78\n\tds_write_b32 v224, v225 offset:24\n\tv_mov_b32 v225, s79\n\tds_write_b32 v224, v225 offset:28\n\tv_mov_b32 v225, s80\n\tds_write_b32 v224, v225 offset:32\n\tv_mov_b32 v225, s81\n\tds_write_b32 v224, v225 offset:36\n\tv_mov_b32 v225, s82\n\tds_write_b32 v224, v225 offset:40\n\tv_mov_b32 v225, s83\n\tds_write_b32 v224, v225 offset:44\n\tv_mov_b32 v225, s84\n\tds_write_b32 v224, v225 offset:48\n\tv_mov_b32 v225, s85\n\tds_write_b32 v224, v225 offset:52\n\tv_mov_b32 v225, s86\n\tds_write_b32 v224, v225 offset:56\n\tv_mov_b32 v225, s87\n\tds_write_b32 v224, v225 offset:60\n\tv_mov_b32 v225, s88\n\tds_write_b32 v224, v225 offset:64\n\tv_mov_b32 v225, s89\n\tds_write_b32 v224, v225 offset:68\n\t")
      TKS("s_mov_b32 s89, 0\n\t")
      "10:\n\ts_and_b32 s48, s17, 7\n\ts_lshl_b32 s43, s48, 17\n\ts_add_u32 s43, s43, 0x300000\n\ts_and_b32 s48, s17, 3\n\ts_lshl_b32 s48, s48, 15\n\tv_add_u32 v8, s48, v2\n\t"
      "v_add_u32 v7, s24, v1\n\ts_add_u32 s49, s17, 1\n\ts_min_u32 s49, s49, 62\n\t"
      "20:\n\tds_read_b32 v5, v4 offset:4\n\ts_waitcnt lgkmcnt(0)\n\tv_readfirstlane_b32 s48, v5\n\ts_cmp_gt_i32 s48, s17\n\ts_cbranch_scc1 21f\n\ts_cmp_eq_u32 s18, 0\n\t"
      "s_cbranch_scc1 21f\n\ts_sub_u32 s18, s18, 1\n\ts_sleep 1\n\ts_branch 20b\n\t"
      "21:\n\t"
      "22:\n\tds_read_b32 v5, v4 offset:0\n\ts_waitcnt lgkmcnt(0)\n\tv_readfirstlane_b32 s48, v5\n\ts_cmp_gt_i32 s48, s17\n\ts_cbranch_scc1 23f\n\ts_cmp_eq_u32 s18, 0\n\t"
      "s_cbranch_scc1 23f\n\ts_sub_u32 s18, s18, 1\n\ts_sleep 1\n\ts_branch 22b\n\t"
      "23:\n\t"
      "24:\n\tds_read_b32 v5, v4 offset:16\n\ts_waitcnt lgkmcnt(0)\n\tv_readfirstlane_b32 s48, v5\n\ts_cmp_gt_i32 s48, s49\n\ts_cbranch_scc1 25f\n\ts_cmp_eq_u32 s18, 0\n\t"
      "s_cbranch_scc1 25f\n\ts_sub_u32 s18, s18, 1\n\ts_sleep 1\n\ts_branch 24b\n\t"
      "25:\n\t"
      TK0
      "s_sleep 20\n\t"
      "50:\n\t"
      TK1
      "s_add_u32 s26, s43, 98304\n\tbuffer_load_dwordx4 v[64:67], v0, s[8:11], s26 offen offset:0 sc1\n\ts_nop 4\n\t"
      "buffer_load_dwordx4 v[68:71], v0, s[8:11], s26 offen offset:1024 sc1\n\ts_nop 4\n\tbuffer_load_dwordx4 v[72:75], v0, s[8:11], s26 offen offset:2048 sc1\n\ts_nop 4\n\t"
      "buffer_load_dwordx4 v[76:79], v0, s[8:11], s26 offen offset:3072 sc1\n\ts_nop 4\n\ts_add_u32 s26, s43, 102400\n\t"
      "buffer_load_dwordx4 v[80:83], v0, s[8:11], s26 offen offset:0 sc1\n\ts_nop 4\n\tbuffer_load_dwordx4 v[84:87], v0, s[8:11], s26 offen offset:1024 sc1\n\ts_nop 4\n\t"
      "buffer_load_dwordx4 v[88:91], v0, s[8:11], s26 offen offset:2048 sc1\n\ts_nop 4\n\tbuffer_load_dwordx4 v[92:95], v0, s[8:11], s26 offen offset:3072 sc1\n\ts_nop 4\n\t"
      "s_add_u32 s26, s43, 0\n\tbuffer_load_dwordx4 v[96:99], v0, s[8:11], s26 offen offset:0 sc1\n\ts_nop 4\n\t"
      "buffer_load_dwordx4 v[100:103], v0, s[8:11], s26 offen offset:1024 sc1\n\ts_nop 4\n\tbuffer_load_dwordx4 v[104:107], v0, s[8:11], s26 offen offset:2048 sc1\n\t"
      "s_nop 4\n\tbuffer_load_dwordx4 v[108:111], v0, s[8:11], s26 offen offset:3072 sc1\n\ts_nop 4\n\ts_add_u32 s26, s43, 4096\n\t"
      "buffer_load_dwordx4 v[112:115], v0, s[8:11], s26 offen offset:0 sc1\n\ts_nop 4\n\tbuffer_load_dwordx4 v[116:119], v0, s[8:11], s26 offen offset:1024 sc1\n\ts_nop 4\n\t"
      "buffer_load_dwordx4 v[120:123], v0, s[8:11], s26 offen offset:2048 sc1\n\ts_nop 4\n\tbuffer_load_dwordx4 v[124:127], v0, s[8:11], s26 offen offset:3072 sc1\n\t"
      "s_nop 4\n\ts_add_u32 s26, s43, 16384\n\tbuffer_load_dwordx4 v[128:131], v0, s[8:11], s26 offen offset:0 sc1\n\ts_nop 4\n\t"
      "buffer_load_dwordx4 v[132:135], v0, s[8:11], s26 offen offset:1024 sc1\n\ts_nop 4\n\tbuffer_load_dwordx4 v[136:139], v0, s[8:11], s26 offen offset:2048 sc1\n\t"
      "s_nop 4\n\tbuffer_load_dwordx4 v[140:143], v0, s[8:11], s26 offen offset:3072 sc1\n\ts_nop 4\n\t"
      TK2
      "ds_read_b128 v[32:35], v7 offset:0\n\tds_read_b128 v[36:39], v7 offset:1024\n\tds_read_b128 v[40:43], v7 offset:2048\n\ts_add_u32 s26, s43, 20480\n\t"
      "buffer_load_dwordx4 v[144:147], v0, s[8:11], s26 offen offset:0 sc1\n\tds_read_b128 v[44:47], v7 offset:3072\n\ts_waitcnt vmcnt(20) lgkmcnt(3)\n\t"
      TK3
      "v_mfma_f32_16x16x32_bf16 v[48:51], v[32:35], v[64:67], 0\n\tbuffer_load_dwordx4 v[148:151], v0, s[8:11], s26 offen offset:1024 sc1\n\t"
      "ds_read_b128 v[32:35], v7 offset:4096\n\ts_waitcnt vmcnt(20) lgkmcnt(3)\n\tv_mfma_f32_16x16x32_bf16 v[52:55], v[36:39], v[68:71], 0\n\t"
      "buffer_load_dwordx4 v[152:155], v0, s[8:11], s26 offen offset:2048 sc1\n\tds_read_b128 v[36:39], v7 offset:5120\n\ts_waitcnt vmcnt(20) lgkmcnt(3)\n\t"
      "v_mfma_f32_16x16x32_bf16 v[48:51], v[40:43], v[72:75], v[48:51]\n\tbuffer_load_dwordx4 v[156:159], v0, s[8:11], s26 offen offset:3072 sc1\n\t"
      "ds_read_b128 v[40:43], v7 offset:6144\n\ts_waitcnt vmcnt(20) lgkmcnt(3)\n\t"
      TK4
      "v_mfma_f32_16x16x32_bf16 v[52:55], v[44:47], v[76:79], v[52:55]\n\tds_read_b128 v[44:47], v7 offset:7168\n\ts_waitcnt vmcnt(19) lgkmcnt(3)\n\t"
      "v_mfma_f32_16x16x32_bf16 v[48:51], v[32:35], v[80:83], v[48:51]\n\tds_read2st64_b64 v[32:35], v8 offset0:0 offset1:1\n\ts_waitcnt vmcnt(18) lgkmcnt(3)\n\t"
      "v_mfma_f32_16x16x32_bf16 v[52:55], v[36:39], v[84:87], v[52:55]\n\tds_read2st64_b64 v[36:39], v8 offset0:4 offset1:5\n\ts_waitcnt vmcnt(17) lgkmcnt(3)\n\t"
      "v_mfma_f32_16x16x32_bf16 v[48:51], v[40:43], v[88:91], v[48:51]\n\tds_read2st64_b64 v[40:43], v8 offset0:8 offset1:9\n\ts_waitcnt vmcnt(16) lgkmcnt(3)\n\t"
      "v_mfma_f32_16x16x32_bf16 v[52:55], v[44:47], v[92:95], v[52:55]\n\tds_read2st64_b64 v[44:47], v8 offset0:12 offset1:13\n\ts_waitcnt vmcnt(15) lgkmcnt(3)\n\t"
      "v_mfma_f32_16x16x32_bf16 v[48:51], v[32:35], v[96:99], v[48:51]\n\tds_read2st64_b64 v[32:35], v8 offset0:16 offset1:17\n\ts_waitcnt vmcnt(14) lgkmcnt(3)\n\t"
      "v_mfma_f32_16x16x32_bf16 v[52:55], v[36:39], v[100:103], v[52:55]\n\tds_read2st64_b64 v[36:39], v8 offset0:20 offset1:21\n\ts_waitcnt vmcnt(13) lgkmcnt(3)\n\t"
      "v_mfma_f32_16x16x32_bf16 v[48:51], v[40:43], v[104:107], v[48:51]\n\tds_read2st64_b64 v[40:43], v8 offset0:24 offset1:25\n\ts_waitcnt vmcnt(12) lgkmcnt(3)\n\t"
      "v_mfma_f32_16x16x32_bf16 v[52:55], v[44:47], v[108:111], v[52:55]\n\tds_read2st64_b64 v[44:47], v8 offset0:28 offset1:29\n\ts_waitcnt vmcnt(11) lgkmcnt(3)\n\t"
      "v_mfma_f32_16x16x32_bf16 v[48:51], v[32:35], v[112:115], v[48:51]\n\tds_read2st64_b64 v[32:35], v8 offset0:32 offset1:33\n\ts_waitcnt vmcnt(10) lgkmcnt(3)\n\t"
      "v_mfma_f32_16x16x32_bf16 v[52:55], v[36:39], v[116:119], v[52:55]\n\tds_read2st64_b64 v[36:39], v8 offset0:36 offset1:37\n\ts_waitcnt vmcnt(9) lgkmcnt(3)\n\t"
      "v_mfma_f32_16x16x32_bf16 v[48:51], v[40:43], v[120:123], v[48:51]\n\tds_read2st64_b64 v[40:43], v8 offset0:40 offset1:41\n\ts_waitcnt vmcnt(8) lgkmcnt(3)\n\t"
      "v_mfma_f32_16x16x32_bf16 v[52:55], v[44:47], v[124:127], v[52:55]\n\tds_read2st64_b64 v[44:47], v8 offset0:44 offset1:45\n\ts_waitcnt vmcnt(7) lgkmcnt(3)\n\t"
      "v_mfma_f32_16x16x32_bf16 v[48:51], v[32:35], v[128:131], v[48:51]\n\tds_read2st64_b64 v[32:35], v8 offset0:48 offset1:49\n\ts_waitcnt vmcnt(6) lgkmcnt(3)\n\t"
      "v_mfma_f32_16x16x32_bf16 v[52:55], v[36:39], v[132:135], v[52:55]\n\tds_read2st64_b64 v[36:39], v8 offset0:52 offset1:53\n\ts_waitcnt vmcnt(5) lgkmcnt(3)\n\t"
      "v_mfma_f32_16x16x32_bf16 v[48:51], v[40:43], v[136:139], v[48:51]\n\tds_read2st64_b64 v[40:43], v8 offset0:56 offset1:57\n\ts_waitcnt vmcnt(4) lgkmcnt(3)\n\t"
      "v_mfma_f32_16x16x32_bf16 v[52:55], v[44:47], v[140:143], v[52:55]\n\tds_read2st64_b64 v[44:47], v8 offset0:60 offset1:61\n\ts_waitcnt vmcnt(3) lgkmcnt(3)\n\t"
      "v_mfma_f32_16x16x32_bf16 v[48:51], v[32:35], v[144:147], v[48:51]\n\ts_waitcnt vmcnt(2) lgkmcnt(2)\n\t"
      "v_mfma_f32_16x16x32_bf16 v[52:55], v[36:39], v[148:151], v[52:55]\n\ts_waitcnt vmcnt(1) lgkmcnt(1)\n\t"
      "v_mfma_f32_16x16x32_bf16 v[48:51], v[40:43], v[152:155], v[48:51]\n\ts_waitcnt vmcnt(0) lgkmcnt(0)\n\t"
      TK5
      "v_mfma_f32_16x16x32_bf16 v[52:55], v[44:47], v[156:159], v[52:55]\n\ts_add_u32 s49, s17, 1\n\tv_mov_b32 v6, s49\n\ts_nop 5\n\t"
      "v_pk_add_f32 v[48:49], v[48:49], v[52:53]\n\tv_pk_add_f32 v[50:51], v[50:51], v[54:55]\n\tv_cmp_u_f32 vcc, v48, v49\n\tv_cvt_pk_bf16_f32 v56, v48, v49\n\t"
      "v_cvt_pk_bf16_f32 v57, v50, v51\n\ts_cbranch_vccnz 60f\n\t"
      "51:\n\tds_write_b32 v4, v6 offset:8\n\tbuffer_store_dwordx2 v[56:57], v3, s[20:23], s25 offen \n\t"
      TK6
      TK7
      STS("s_cmp_lt_i32 s17, 8\n\ts_cselect_b32 s50, 0, s50\n\ts_cselect_b32 s51, 0, s51\n\ts_cselect_b32 s61, 0, s61\n\t")
      TKS("s_waitcnt lgkmcnt(0)\n\ts_cmp_lt_i32 s17, 0\n\ts_cbranch_scc1 98f\n\ts_cmp_lg_u32 s16, s89\n\ts_cbranch_scc1 98f\n\ts_mov_b64 exec, 1\n\ts_lshl_b32 s48, s17, 4\n\ts_add_u32 s48, s48, 161056\n\tv_mov_b32 v5, s48\n\ts_sub_u32 s48, s72, s88\n\ts_min_u32 s48, s48, 0xffff\n\tv_mov_b32 v6, s48\n\tds_write_b16 v5, v6 offset:0\n\ts_sub_u32 s48, s74, s72\n\ts_min_u32 s48, s48, 0xffff\n\tv_mov_b32 v6, s48\n\tds_write_b16 v5, v6 offset:2\n\ts_sub_u32 s48, s76, s74\n\ts_min_u32 s48, s48, 0xffff\n\tv_mov_b32 v6, s48\n\tds_write_b16 v5, v6 offset:4\n\ts_sub_u32 s48, s78, s76\n\ts_min_u32 s48, s48, 0xffff\n\tv_mov_b32 v6, s48\n\tds_write_b16 v5, v6 offset:6\n\ts_sub_u32 s48, s80, s78\n\ts_min_u32 s48, s48, 0xffff\n\tv_mov_b32 v6, s48\n\tds_write_b16 v5, v6 offset:8\n\ts_sub_u32 s48, s82, s80\n\ts_min_u32 s48, s48, 0xffff\n\tv_mov_b32 v6, s48\n\tds_write_b16 v5, v6 offset:10\n\ts_sub_u32 s48, s84, s82\n\ts_min_u32 s48, s48, 0xffff\n\tv_mov_b32 v6, s48\n\tds_write_b16 v5, v6 offset:12\n\ts_sub_u32 s48, s86, s84\n\ts_min_u32 s48, s48, 0xffff\n\tv_mov_b32 v6, s48\n\tds_write_b16 v5, v6 offset:14\n\ts_mov_b64 exec, -1\n\t98:\n\ts_mov_b32 s88, s86\n\t")
      "s_add_u32 s25, s25, 0x20000\n\ts_add_u32 s24, s24, 0x2000\n\ts_cmp_eq_u32 s24, 0x6000\n\ts_cselect_b32 s24, 0, s24\n\ts_add_u32 s17, s17, 1\n\ts_cmp_lt_u32 s17, 64\n\t"
      "s_cbranch_scc1 10b\n\ts_branch 40f\n\t"
      "60:\n\tv_max3_u32 v5, v64, v65, v66\n\tv_max_u32 v5, v5, v67\n\tv_max3_u32 v5, v5, v68, v69\n\tv_max3_u32 v5, v5, v70, v71\n\tv_max3_u32 v5, v5, v72, v73\n\t"
      "v_max3_u32 v5, v5, v74, v75\n\tv_max3_u32 v5, v5, v76, v77\n\tv_max3_u32 v5, v5, v78, v79\n\tv_max3_u32 v5, v5, v80, v81\n\tv_max3_u32 v5, v5, v82, v83\n\t"
      "v_max3_u32 v5, v5, v84, v85\n\tv_max3_u32 v5, v5, v86, v87\n\tv_max3_u32 v5, v5, v88, v89\n\tv_max3_u32 v5, v5, v90, v91\n\tv_max3_u32 v5, v5, v92, v93\n\t"
      "v_max3_u32 v5, v5, v94, v95\n\tv_max3_u32 v5, v5, v96, v97\n\tv_max3_u32 v5, v5, v98, v99\n\tv_max3_u32 v5, v5, v100, v101\n\tv_max3_u32 v5, v5, v102, v103\n\t"
      "v_max3_u32 v5, v5, v104, v105\n\tv_max3_u32 v5, v5, v106, v107\n\tv_max3_u32 v5, v5, v108, v109\n\tv_max3_u32 v5, v5, v110, v111\n\tv_max3_u32 v5, v5, v112, v113\n\t"
      "v_max3_u32 v5, v5, v114, v115\n\tv_max3_u32 v5, v5, v116, v117\n\tv_max3_u32 v5, v5, v118, v119\n\tv_max3_u32 v5, v5, v120, v121\n\tv_max3_u32 v5, v5, v122, v123\n\t"
      "v_max3_u32 v5, v5, v124, v125\n\tv_max3_u32 v5, v5, v126, v127\n\tv_max3_u32 v5, v5, v128, v129\n\tv_max3_u32 v5, v5, v130, v131\n\tv_max3_u32 v5, v5, v132, v133\n\t"
      "v_max3_u32 v5, v5, v134, v135\n\tv_max3_u32 v5, v5, v136, v137\n\tv_max3_u32 v5, v5, v138, v139\n\tv_max3_u32 v5, v5, v140, v141\n\tv_max3_u32 v5, v5, v142, v143\n\t"
      "v_max3_u32 v5, v5, v144, v145\n\tv_max3_u32 v5, v5, v146, v147\n\tv_max3_u32 v5, v5, v148, v149\n\tv_max3_u32 v5, v5, v150, v151\n\tv_max3_u32 v5, v5, v152, v153\n\t"
      "v_max3_u32 v5, v5, v154, v155\n\tv_max3_u32 v5, v5, v156, v157\n\tv_max3_u32 v5, v5, v158, v159\n\tv_cmp_eq_u32 vcc, -1, v5\n\ts_cbranch_vccz 51b\n\t"
      "s_cmp_eq_u32 s18, 0\n\ts_cbranch_scc1 51b\n\ts_sub_u32 s18, s18, 1\n\ts_branch 50b\n\t"
      "40:\n\ts_waitcnt vmcnt(0) lgkmcnt(0)\n\t"
      TKS("s_mul_i32 s48, s16, 72\n\ts_add_u32 s48, s48, 162080\n\tv_mov_b32 v224, s48\n\tds_read_b32 v225, v224 offset:0\n\ts_waitcnt lgkmcnt(0)\n\tv_readfirstlane_b32 s72, v225\n\tds_read_b32 v225, v224 offset:4\n\ts_waitcnt lgkmcnt(0)\n\tv_readfirstlane_b32 s73, v225\n\tds_read_b32 v225, v224 offset:8\n\ts_waitcnt lgkmcnt(0)\n\tv_readfirstlane_b32 s74, v225\n\tds_read_b32 v225, v224 offset:12\n\ts_waitcnt lgkmcnt(0)\n\tv_readfirstlane_b32 s75, v225\n\tds_read_b32 v225, v224 offset:16\n\ts_waitcnt lgkmcnt(0)\n\tv_readfirstlane_b32 s76, v225\n\tds_read_b32 v225, v224 offset:20\n\ts_waitcnt lgkmcnt(0)\n\tv_readfirstlane_b32 s77, v225\n\tds_read_b32 v225, v224 offset:24\n\ts_waitcnt lgkmcnt(0)\n\tv_readfirstlane_b32 s78, v225\n\tds_read_b32 v225, v224 offset:28\n\ts_waitcnt lgkmcnt(0)\n\tv_readfirstlane_b32 s79, v225\n\tds_read_b32 v225, v224 offset:32\n\ts_waitcnt lgkmcnt(0)\n\tv_readfirstlane_b32 s80, v225\n\tds_read_b32 v225, v224 offset:36\n\ts_waitcnt lgkmcnt(0)\n\tv_readfirstlane_b32 s81, v225\n\tds_read_b32 v225, v224 offset:40\n\ts_waitcnt lgkmcnt(0)\n\tv_readfirstlane_b32 s82, v225\n\tds_read_b32 v225, v224 offset:44\n\ts_waitcnt lgkmcnt(0)\n\tv_readfirstlane_b32 s83, v225\n\tds_read_b32 v225, v224 offset:48\n\ts_waitcnt lgkmcnt(0)\n\tv_readfirstlane_b32 s84, v225\n\tds_read_b32 v225, v224 offset:52\n\ts_waitcnt lgkmcnt(0)\n\tv_readfirstlane_b32 s85, v225\n\tds_read_b32 v225, v224 offset:56\n\ts_waitcnt lgkmcnt(0)\n\tv_readfirstlane_b32 s86, v225\n\tds_read_b32 v225, v224 offset:60\n\ts_waitcnt lgkmcnt(0)\n\tv_readfirstlane_b32 s87, v225\n\tds_read_b32 v225, v224 offset:64\n\ts_waitcnt lgkmcnt(0)\n\tv_readfirstlane_b32 s88, v225\n\tds_read_b32 v225, v224 offset:68\n\ts_waitcnt lgkmcnt(0)\n\tv_readfirstlane_b32 s89, v225\n\t")
      STS("s_lshl_b32 s48, s16, 2\n\ts_add_u32 s48, s48, 162944\n\tv_mov_b32 v0, s48\n\tv_mov_b32 v1, s50\n\tds_write_b32 v0, v1\n\ts_cmp_lg_u32 s16, 0\n\ts_cbranch_scc1 97f\n\tv_mov_b32 v1, s51\n\tds_write_b32 v0, v1 offset:16\n\t97:\n\ts_cmp_lg_u32 s16, -1\n\ts_cbranch_scc1 95f\n\tv_mov_b32 v1, s61\n\tds_write_b32 v0, v1 offset:24\n\tv_mov_b32 v1, s21\n\tds_write_b32 v0, v1 offset:28\n\tv_mov_b32 v1, s22\n\tds_write_b32 v0, v1 offset:32\n\t95:\n\t")
      "s_cmp_lg_u32 s18, 0\n\ts_cbranch_scc1 96f\n\tv_mov_b32 v1, 1\n\tbuffer_store_dword v1, off, s[12:15], 0 offset:3328 sc1\n\t"
      "96:\n\t"
      : TOK_REGS :: "memory", "vcc", "scc", S_CLOB TK_CLOB);
    else asm volatile(
      "s_nop 0\n\tv_readfirstlane_b32 s8, v160\n\tv_readfirstlane_b32 s9, v161\n\tv_readfirstlane_b32 s10, v162\n\tv_readfirstlane_b32 s11, v163\n\t"
      "v_readfirstlane_b32 s12, v164\n\tv_readfirstlane_b32 s13, v165\n\tv_readfirstlane_b32 s14, v166\n\tv_readfirstlane_b32 s15, v167\n\tv_readfirstlane_b32 s16, v168\n\t"
      "v_readfirstlane_b32 s17, v169\n\tv_readfirstlane_b32 s18, v170\n\tv_readfirstlane_b32 s19, v171\n\tv_readfirstlane_b32 s20, v172\n\tv_readfirstlane_b32 s21, v173\n\t"
      "v_readfirstlane_b32 s22, v174\n\tv_readfirstlane_b32 s23, v175\n\tv_readfirstlane_b32 s24, v176\n\tv_readfirstlane_b32 s25, v177\n\ts_nop 4\n\t"
      TKS("s_mul_i32 s48, s16, 72\n\ts_add_u32 s48, s48, 162080\n\tv_mov_b32 v224, s48\n\tv_mov_b32 v225, s72\n\tds_write_b32 v224, v225 offset:0\n\tv_mov_b32 v225, s73\n\tds_write_b32 v224, v225 offset:4\n\tv_mov_b32 v225, s74\n\tds_write_b32 v224, v225 offset:8\n\tv_mov_b32 v225, s75\n\tds_write_b32 v224, v225 offset:12\n\tv_mov_b32 v225, s76\n\tds_write_b32 v224, v225 offset:16\n\tv_mov_b32 v225, s77\n\tds_write_b32 v224, v225 offset:20\n\tv_mov_b32 v225, s78\n\tds_write_b32 v224, v225 offset:24\n\tv_mov_b32 v225, s79\n\tds_write_b32 v224, v225 offset:28\n\tv_mov_b32 v225, s80\n\tds_write_b32 v224, v225 offset:32\n\tv_mov_b32 v225, s81\n\tds_write_b32 v224, v225 offset:36\n\tv_mov_b32 v225, s82\n\tds_write_b32 v224, v225 offset:40\n\tv_mov_b32 v225, s83\n\tds_write_b32 v224, v225 offset:44\n\tv_mov_b32 v225, s84\n\tds_write_b32 v224, v225 offset:48\n\tv_mov_b32 v225, s85\n\tds_write_b32 v224, v225 offset:52\n\tv_mov_b32 v225, s86\n\tds_write_b32 v224, v225 offset:56\n\tv_mov_b32 v225, s87\n\tds_write_b32 v224, v225 offset:60\n\tv_mov_b32 v225, s88\n\tds_write_b32 v224, v225 offset:64\n\tv_mov_b32 v225, s89\n\tds_write_b32 v224, v225 offset:68\n\t")
      TKS("s_mov_b32 s89, 0\n\t")
      "10:\n\ts_and_b32 s48, s17, 7\n\ts_lshl_b32 s43, s48, 17\n\ts_add_u32 s43, s43, 0x300000\n\ts_and_b32 s48, s17, 3\n\ts_lshl_b32 s48, s48, 15\n\tv_add_u32 v8, s48, v2\n\t"
      "v_add_u32 v7, s24, v1\n\ts_add_u32 s49, s17, 1\n\ts_min_u32 s49, s49, 62\n\t"
      "20:\n\tds_read_b32 v5, v4 offset:4\n\ts_waitcnt lgkmcnt(0)\n\tv_readfirstlane_b32 s48, v5\n\ts_cmp_gt_i32 s48, s17\n\ts_cbranch_scc1 21f\n\ts_cmp_eq_u32 s18, 0\n\t"
      "s_cbranch_scc1 21f\n\ts_sub_u32 s18, s18, 1\n\ts_sleep 1\n\ts_branch 20b\n\t"
      "21:\n\t"
      "22:\n\tds_read_b32 v5, v4 offset:0\n\ts_waitcnt lgkmcnt(0)\n\tv_readfirstlane_b32 s48, v5\n\ts_cmp_gt_i32 s48, s17\n\ts_cbranch_scc1 23f\n\ts_cmp_eq_u32 s18, 0\n\t"
      "s_cbranch_scc1 23f\n\ts_sub_u32 s18, s18, 1\n\ts_sleep 1\n\ts_branch 22b\n\t"
      "23:\n\t"
      "24:\n\tds_read_b32 v5, v4 offset:16\n\ts_waitcnt lgkmcnt(0)\n\tv_readfirstlane_b32 s48, v5\n\ts_cmp_gt_i32 s48, s49\n\ts_cbranch_scc1 25f\n\ts_cmp_eq_u32 s18, 0\n\t"
      "s_cbranch_scc1 25f\n\ts_sub_u32 s18, s18, 1\n\ts_sleep 1\n\ts_branch 24b\n\t"
      "25:\n\t"
      TK0
      "50:\n\t"
      TK1
      "s_add_u32 s26, s43, 114688\n\tbuffer_load_dwordx4 v[64:67], v0, s[8:11], s26 offen offset:0 sc1\n\ts_nop 4\n\t"
      "buffer_load_dwordx4 v[68:71], v0, s[8:11], s26 offen offset:1024 sc1\n\ts_nop 4\n\tbuffer_load_dwordx4 v[72:75], v0, s[8:11], s26 offen offset:2048 sc1\n\ts_nop 4\n\t"
      "buffer_load_dwordx4 v[76:79], v0, s[8:11], s26 offen offset:3072 sc1\n\ts_nop 4\n\ts_add_u32 s26, s43, 118784\n\t"
      "buffer_load_dwordx4 v[80:83], v0, s[8:11], s26 offen offset:0 sc1\n\ts_nop 4\n\tbuffer_load_dwordx4 v[84:87], v0, s[8:11], s26 offen offset:1024 sc1\n\ts_nop 4\n\t"
      "buffer_load_dwordx4 v[88:91], v0, s[8:11], s26 offen offset:2048 sc1\n\ts_nop 4\n\tbuffer_load_dwordx4 v[92:95], v0, s[8:11], s26 offen offset:3072 sc1\n\ts_nop 4\n\t"
      "s_add_u32 s26, s43, 32768\n\tbuffer_load_dwordx4 v[96:99], v0, s[8:11], s26 offen offset:0 sc1\n\ts_nop 4\n\t"
      "buffer_load_dwordx4 v[100:103], v0, s[8:11], s26 offen offset:1024 sc1\n\ts_nop 4\n\tbuffer_load_dwordx4 v[104:107], v0, s[8:11], s26 offen offset:2048 sc1\n\t"
      "s_nop 4\n\tbuffer_load_dwordx4 v[108:111], v0, s[8:11], s26 offen offset:3072 sc1\n\ts_nop 4\n\ts_add_u32 s26, s43, 36864\n\t"
      "buffer_load_dwordx4 v[112:115], v0, s[8:11], s26 offen offset:0 sc1\n\ts_nop 4\n\tbuffer_load_dwordx4 v[116:119], v0, s[8:11], s26 offen offset:1024 sc1\n\ts_nop 4\n\t"
      "buffer_load_dwordx4 v[120:123], v0, s[8:11], s26 offen offset:2048 sc1\n\ts_nop 4\n\tbuffer_load_dwordx4 v[124:127], v0, s[8:11], s26 offen offset:3072 sc1\n\t"
      "s_nop 4\n\ts_add_u32 s26, s43, 49152\n\tbuffer_load_dwordx4 v[128:131], v0, s[8:11], s26 offen offset:0 sc1\n\ts_nop 4\n\t"
      "buffer_load_dwordx4 v[132:135], v0, s[8:11], s26 offen offset:1024 sc1\n\ts_nop 4\n\tbuffer_load_dwordx4 v[136:139], v0, s[8:11], s26 offen offset:2048 sc1\n\t"
      "s_nop 4\n\tbuffer_load_dwordx4 v[140:143], v0, s[8:11], s26 offen offset:3072 sc1\n\ts_nop 4\n\t"
      TK2
      "ds_read_b128 v[32:35], v7 offset:0\n\tds_read_b128 v[36:39], v7 offset:1024\n\tds_read_b128 v[40:43], v7 offset:2048\n\ts_add_u32 s26, s43, 53248\n\t"
      "buffer_load_dwordx4 v[144:147], v0, s[8:11], s26 offen offset:0 sc1\n\tds_read_b128 v[44:47], v7 offset:3072\n\ts_waitcnt vmcnt(20) lgkmcnt(3)\n\t"
      TK3
      "v_mfma_f32_16x16x32_bf16 v[48:51], v[32:35], v[64:67], 0\n\tbuffer_load_dwordx4 v[148:151], v0, s[8:11], s26 offen offset:1024 sc1\n\t"
      "ds_read_b128 v[32:35], v7 offset:4096\n\ts_waitcnt vmcnt(20) lgkmcnt(3)\n\tv_mfma_f32_16x16x32_bf16 v[52:55], v[36:39], v[68:71], 0\n\t"
      "buffer_load_dwordx4 v[152:155], v0, s[8:11], s26 offen offset:2048 sc1\n\tds_read_b128 v[36:39], v7 offset:5120\n\ts_waitcnt vmcnt(20) lgkmcnt(3)\n\t"
      "v_mfma_f32_16x16x32_bf16 v[48:51], v[40:43], v[72:75], v[48:51]\n\tbuffer_load_dwordx4 v[156:159], v0, s[8:11], s26 offen offset:3072 sc1\n\t"
      "ds_read_b128 v[40:43], v7 offset:6144\n\ts_waitcnt vmcnt(20) lgkmcnt(3)\n\tv_mfma_f32_16x16x32_bf16 v[52:55], v[44:47], v[76:79], v[52:55]\n\t"
      "s_add_u32 s26, s43, 65536\n\tbuffer_load_dwordx4 v[160:163], v0, s[8:11], s26 offen offset:0 sc1\n\tds_read_b128 v[44:47], v7 offset:7168\n\t"
      "s_waitcnt vmcnt(20) lgkmcnt(3)\n\tv_mfma_f32_16x16x32_bf16 v[48:51], v[32:35], v[80:83], v[48:51]\n\t"
      "buffer_load_dwordx4 v[164:167], v0, s[8:11], s26 offen offset:1024 sc1\n\tds_read_b128 v[32:35], v8 offset:0\n\ts_waitcnt vmcnt(20) lgkmcnt(3)\n\t"
      "v_mfma_f32_16x16x32_bf16 v[52:55], v[36:39], v[84:87], v[52:55]\n\tbuffer_load_dwordx4 v[168:171], v0, s[8:11], s26 offen offset:2048 sc1\n\t"
      "ds_read_b128 v[36:39], v8 offset:1024\n\ts_waitcnt vmcnt(20) lgkmcnt(3)\n\tv_mfma_f32_16x16x32_bf16 v[48:51], v[40:43], v[88:91], v[48:51]\n\t"
      "buffer_load_dwordx4 v[172:175], v0, s[8:11], s26 offen offset:3072 sc1\n\tds_read_b128 v[40:43], v8 offset:2048\n\ts_waitcnt vmcnt(20) lgkmcnt(3)\n\t"
      "v_mfma_f32_16x16x32_bf16 v[52:55], v[44:47], v[92:95], v[52:55]\n\ts_add_u32 s26, s43, 69632\n\tbuffer_load_dwordx4 v[176:179], v0, s[8:11], s26 offen offset:0 sc1\n\t"
      "ds_read_b128 v[44:47], v8 offset:3072\n\ts_waitcnt vmcnt(20) lgkmcnt(3)\n\tv_mfma_f32_16x16x32_bf16 v[48:51], v[32:35], v[96:99], v[48:51]\n\t"
      "buffer_load_dwordx4 v[180:183], v0, s[8:11], s26 offen offset:1024 sc1\n\tds_read_b128 v[32:35], v8 offset:4096\n\ts_waitcnt vmcnt(20) lgkmcnt(3)\n\t"
      "v_mfma_f32_16x16x32_bf16 v[52:55], v[36:39], v[100:103], v[52:55]\n\tbuffer_load_dwordx4 v[184:187], v0, s[8:11], s26 offen offset:2048 sc1\n\t"
      "ds_read_b128 v[36:39], v8 offset:5120\n\ts_waitcnt vmcnt(20) lgkmcnt(3)\n\tv_mfma_f32_16x16x32_bf16 v[48:51], v[40:43], v[104:107], v[48:51]\n\t"
      "buffer_load_dwordx4 v[188:191], v0, s[8:11], s26 offen offset:3072 sc1\n\tds_read_b128 v[40:43], v8 offset:6144\n\ts_waitcnt vmcnt(20) lgkmcnt(3)\n\t"
      "v_mfma_f32_16x16x32_bf16 v[52:55], v[44:47], v[108:111], v[52:55]\n\ts_add_u32 s26, s43, 81920\n\tbuffer_load_dwordx4 v[192:195], v0, s[8:11], s26 offen offset:0 sc1\n\t"
      "ds_read_b128 v[44:47], v8 offset:7168\n\ts_waitcnt vmcnt(20) lgkmcnt(3)\n\tv_mfma_f32_16x16x32_bf16 v[48:51], v[32:35], v[112:115], v[48:51]\n\t"
      "buffer_load_dwordx4 v[196:199], v0, s[8:11], s26 offen offset:1024 sc1\n\tds_read_b128 v[32:35], v8 offset:8192\n\ts_waitcnt vmcnt(20) lgkmcnt(3)\n\t"
      "v_mfma_f32_16x16x32_bf16 v[52:55], v[36:39], v[116:119], v[52:55]\n\tbuffer_load_dwordx4 v[200:203], v0, s[8:11], s26 offen offset:2048 sc1\n\t"
      "ds_read_b128 v[36:39], v8 offset:9216\n\ts_waitcnt vmcnt(20) lgkmcnt(3)\n\tv_mfma_f32_16x16x32_bf16 v[48:51], v[40:43], v[120:123], v[48:51]\n\t"
      "buffer_load_dwordx4 v[204:207], v0, s[8:11], s26 offen offset:3072 sc1\n\tds_read_b128 v[40:43], v8 offset:10240\n\ts_waitcnt vmcnt(20) lgkmcnt(3)\n\t"
      "v_mfma_f32_16x16x32_bf16 v[52:55], v[44:47], v[124:127], v[52:55]\n\ts_add_u32 s26, s43, 86016\n\tbuffer_load_dwordx4 v[208:211], v0, s[8:11], s26 offen offset:0 sc1\n\t"
      "ds_read_b128 v[44:47], v8 offset:11264\n\ts_waitcnt vmcnt(20) lgkmcnt(3)\n\tv_mfma_f32_16x16x32_bf16 v[48:51], v[32:35], v[128:131], v[48:51]\n\t"
      "buffer_load_dwordx4 v[212:215], v0, s[8:11], s26 offen offset:1024 sc1\n\tds_read_b128 v[32:35], v8 offset:12288\n\ts_waitcnt vmcnt(20) lgkmcnt(3)\n\t"
      "v_mfma_f32_16x16x32_bf16 v[52:55], v[36:39], v[132:135], v[52:55]\n\tbuffer_load_dwordx4 v[216:219], v0, s[8:11], s26 offen offset:2048 sc1\n\t"
      "ds_read_b128 v[36:39], v8 offset:13312\n\ts_waitcnt vmcnt(20) lgkmcnt(3)\n\tv_mfma_f32_16x16x32_bf16 v[48:51], v[40:43], v[136:139], v[48:51]\n\t"
      "buffer_load_dwordx4 v[220:223], v0, s[8:11], s26 offen offset:3072 sc1\n\tds_read_b128 v[40:43], v8 offset:14336\n\ts_waitcnt vmcnt(20) lgkmcnt(3)\n\t"
      TK4
      "v_mfma_f32_16x16x32_bf16 v[52:55], v[44:47], v[140:143], v[52:55]\n\tds_read_b128 v[44:47], v8 offset:15360\n\ts_waitcnt vmcnt(19) lgkmcnt(3)\n\t"
      "v_mfma_f32_16x16x32_bf16 v[48:51], v[32:35], v[144:147], v[48:51]\n\tds_read_b128 v[32:35], v8 offset:16384\n\ts_waitcnt vmcnt(18) lgkmcnt(3)\n\t"
      "v_mfma_f32_16x16x32_bf16 v[52:55], v[36:39], v[148:151], v[52:55]\n\tds_read_b128 v[36:39], v8 offset:17408\n\ts_waitcnt vmcnt(17) lgkmcnt(3)\n\t"
      "v_mfma_f32_16x16x32_bf16 v[48:51], v[40:43], v[152:155], v[48:51]\n\tds_read_b128 v[40:43], v8 offset:18432\n\ts_waitcnt vmcnt(16) lgkmcnt(3)\n\t"
      "v_mfma_f32_16x16x32_bf16 v[52:55], v[44:47], v[156:159], v[52:55]\n\tds_read_b128 v[44:47], v8 offset:19456\n\ts_waitcnt vmcnt(15) lgkmcnt(3)\n\t"
      "v_mfma_f32_16x16x32_bf16 v[48:51], v[32:35], v[160:163], v[48:51]\n\tds_read_b128 v[32:35], v8 offset:20480\n\ts_waitcnt vmcnt(14) lgkmcnt(3)\n\t"
      "v_mfma_f32_16x16x32_bf16 v[52:55], v[36:39], v[164:167], v[52:55]\n\tds_read_b128 v[36:39], v8 offset:21504\n\ts_waitcnt vmcnt(13) lgkmcnt(3)\n\t"
      "v_mfma_f32_16x16x32_bf16 v[48:51], v[40:43], v[168:171], v[48:51]\n\tds_read_b128 v[40:43], v8 offset:22528\n\ts_waitcnt vmcnt(12) lgkmcnt(3)\n\t"
      "v_mfma_f32_16x16x32_bf16 v[52:55], v[44:47], v[172:175], v[52:55]\n\tds_read_b128 v[44:47], v8 offset:23552\n\ts_waitcnt vmcnt(11) lgkmcnt(3)\n\t"
      "v_mfma_f32_16x16x32_bf16 v[48:51], v[32:35], v[176:179], v[48:51]\n\tds_read_b128 v[32:35], v8 offset:24576\n\ts_waitcnt vmcnt(10) lgkmcnt(3)\n\t"
      "v_mfma_f32_16x16x32_bf16 v[52:55], v[36:39], v[180:183], v[52:55]\n\tds_read_b128 v[36:39], v8 offset:25600\n\ts_waitcnt vmcnt(9) lgkmcnt(3)\n\t"
      "v_mfma_f32_16x16x32_bf16 v[48:51], v[40:43], v[184:187], v[48:51]\n\tds_read_b128 v[40:43], v8 offset:26624\n\ts_waitcnt vmcnt(8) lgkmcnt(3)\n\t"
      "v_mfma_f32_16x16x32_bf16 v[52:55], v[44:47], v[188:191], v[52:55]\n\tds_read_b128 v[44:47], v8 offset:27648\n\ts_waitcnt vmcnt(7) lgkmcnt(3)\n\t"
      "v_mfma_f32_16x16x32_bf16 v[48:51], v[32:35], v[192:195], v[48:51]\n\tds_read_b128 v[32:35], v8 offset:28672\n\ts_waitcnt vmcnt(6) lgkmcnt(3)\n\t"
      "v_mfma_f32_16x16x32_bf16 v[52:55], v[36:39], v[196:199], v[52:55]\n\tds_read_b128 v[36:39], v8 offset:29696\n\ts_waitcnt vmcnt(5) lgkmcnt(3)\n\t"
      "v_mfma_f32_16x16x32_bf16 v[48:51], v[40:43], v[200:203], v[48:51]\n\tds_read_b128 v[40:43], v8 offset:30720\n\ts_waitcnt vmcnt(4) lgkmcnt(3)\n\t"
      "v_mfma_f32_16x16x32_bf16 v[52:55], v[44:47], v[204:207], v[52:55]\n\tds_read_b128 v[44:47], v8 offset:31744\n\ts_waitcnt vmcnt(3) lgkmcnt(3)\n\t"
      "v_mfma_f32_16x16x32_bf16 v[48:51], v[32:35], v[208:211], v[48:51]\n\ts_waitcnt vmcnt(2) lgkmcnt(2)\n\t"
      "v_mfma_f32_16x16x32_bf16 v[52:55], v[36:39], v[212:215], v[52:55]\n\ts_waitcnt vmcnt(1) lgkmcnt(1)\n\t"
      "v_mfma_f32_16x16x32_bf16 v[48:51], v[40:43], v[216:219], v[48:51]\n\ts_waitcnt vmcnt(0) lgkmcnt(0)\n\t"
      TK5
      "v_mfma_f32_16x16x32_bf16 v[52:55], v[44:47], v[220:223], v[52:55]\n\ts_add_u32 s49, s17, 1\n\tv_mov_b32 v6, s49\n\ts_nop 5\n\t"
      "v_pk_add_f32 v[48:49], v[48:49], v[52:53]\n\tv_pk_add_f32 v[50:51], v[50:51], v[54:55]\n\tv_cmp_u_f32 vcc, v48, v49\n\tv_cvt_pk_bf16_f32 v56, v48, v49\n\t"
      "v_cvt_pk_bf16_f32 v57, v50, v51\n\ts_cbranch_vccnz 60f\n\t"
      "51:\n\tds_write_b32 v4, v6 offset:12\n\tbuffer_store_dwordx2 v[56:57], v3, s[20:23], s25 offen \n\t"
      TK6
      TK7
      STS("s_cmp_lt_i32 s17, 8\n\ts_cselect_b32 s50, 0, s50\n\ts_cselect_b32 s51, 0, s51\n\ts_cselect_b32 s61, 0, s61\n\t")
      TKS("s_waitcnt lgkmcnt(0)\n\ts_cmp_lt_i32 s17, 0\n\ts_cbranch_scc1 98f\n\ts_cmp_lg_u32 s16, s89\n\ts_cbranch_scc1 98f\n\ts_mov_b64 exec, 1\n\ts_lshl_b32 s48, s17, 4\n\ts_add_u32 s48, s48, 161056\n\tv_mov_b32 v5, s48\n\ts_sub_u32 s48, s72, s88\n\ts_min_u32 s48, s48, 0xffff\n\tv_mov_b32 v6, s48\n\tds_write_b16 v5, v6 offset:0\n\ts_sub_u32 s48, s74, s72\n\ts_min_u32 s48, s48, 0xffff\n\tv_mov_b32 v6, s48\n\tds_write_b16 v5, v6 offset:2\n\ts_sub_u32 s48, s76, s74\n\ts_min_u32 s48, s48, 0xffff\n\tv_mov_b32 v6, s48\n\tds_write_b16 v5, v6 offset:4\n\ts_sub_u32 s48, s78, s76\n\ts_min_u32 s48, s48, 0xffff\n\tv_mov_b32 v6, s48\n\tds_write_b16 v5, v6 offset:6\n\ts_sub_u32 s48, s80, s78\n\ts_min_u32 s48, s48, 0xffff\n\tv_mov_b32 v6, s48\n\tds_write_b16 v5, v6 offset:8\n\ts_sub_u32 s48, s82, s80\n\ts_min_u32 s48, s48, 0xffff\n\tv_mov_b32 v6, s48\n\tds_write_b16 v5, v6 offset:10\n\ts_sub_u32 s48, s84, s82\n\ts_min_u32 s48, s48, 0xffff\n\tv_mov_b32 v6, s48\n\tds_write_b16 v5, v6 offset:12\n\ts_sub_u32 s48, s86, s84\n\ts_min_u32 s48, s48, 0xffff\n\tv_mov_b32 v6, s48\n\tds_write_b16 v5, v6 offset:14\n\ts_mov_b64 exec, -1\n\t98:\n\ts_mov_b32 s88, s86\n\t")
      "s_add_u32 s25, s25, 0x20000\n\ts_add_u32 s24, s24, 0x2000\n\ts_cmp_eq_u32 s24, 0x6000\n\ts_cselect_b32 s24, 0, s24\n\ts_add_u32 s17, s17, 1\n\ts_cmp_lt_u32 s17, 64\n\t"
      "s_cbranch_scc1 10b\n\ts_branch 40f\n\t"
      "60:\n\tv_max3_u32 v5, v64, v65, v66\n\tv_max_u32 v5, v5, v67\n\tv_max3_u32 v5, v5, v68, v69\n\tv_max3_u32 v5, v5, v70, v71\n\tv_max3_u32 v5, v5, v72, v73\n\t"
      "v_max3_u32 v5, v5, v74, v75\n\tv_max3_u32 v5, v5, v76, v77\n\tv_max3_u32 v5, v5, v78, v79\n\tv_max3_u32 v5, v5, v80, v81\n\tv_max3_u32 v5, v5, v82, v83\n\t"
      "v_max3_u32 v5, v5, v84, v85\n\tv_max3_u32 v5, v5, v86, v87\n\tv_max3_u32 v5, v5, v88, v89\n\tv_max3_u32 v5, v5, v90, v91\n\tv_max3_u32 v5, v5, v92, v93\n\t"
      "v_max3_u32 v5, v5, v94, v95\n\tv_max3_u32 v5, v5, v96, v97\n\tv_max3_u32 v5, v5, v98, v99\n\tv_max3_u32 v5, v5, v100, v101\n\tv_max3_u32 v5, v5, v102, v103\n\t"
      "v_max3_u32 v5, v5, v104, v105\n\tv_max3_u32 v5, v5, v106, v107\n\tv_max3_u32 v5, v5, v108, v109\n\tv_max3_u32 v5, v5, v110, v111\n\tv_max3_u32 v5, v5, v112, v113\n\t"
      "v_max3_u32 v5, v5, v114, v115\n\tv_max3_u32 v5, v5, v116, v117\n\tv_max3_u32 v5, v5, v118, v119\n\tv_max3_u32 v5, v5, v120, v121\n\tv_max3_u32 v5, v5, v122, v123\n\t"
      "v_max3_u32 v5, v5, v124, v125\n\tv_max3_u32 v5, v5, v126, v127\n\tv_max3_u32 v5, v5, v128, v129\n\tv_max3_u32 v5, v5, v130, v131\n\tv_max3_u32 v5, v5, v132, v133\n\t"
      "v_max3_u32 v5, v5, v134, v135\n\tv_max3_u32 v5, v5, v136, v137\n\tv_max3_u32 v5, v5, v138, v139\n\tv_max3_u32 v5, v5, v140, v141\n\tv_max3_u32 v5, v5, v142, v143\n\t"
      "v_max3_u32 v5, v5, v144, v145\n\tv_max3_u32 v5, v5, v146, v147\n\tv_max3_u32 v5, v5, v148, v149\n\tv_max3_u32 v5, v5, v150, v151\n\tv_max3_u32 v5, v5, v152, v153\n\t"
      "v_max3_u32 v5, v5, v154, v155\n\tv_max3_u32 v5, v5, v156, v157\n\tv_max3_u32 v5, v5, v158, v159\n\tv_max3_u32 v5, v5, v160, v161\n\tv_max3_u32 v5, v5, v162, v163\n\t"
      "v_max3_u32 v5, v5, v164, v165\n\tv_max3_u32 v5, v5, v166, v167\n\tv_max3_u32 v5, v5, v168, v169\n\tv_max3_u32 v5, v5, v170, v171\n\tv_max3_u32 v5, v5, v172, v173\n\t"
      "v_max3_u32 v5, v5, v174, v175\n\tv_max3_u32 v5, v5, v176, v177\n\tv_max3_u32 v5, v5, v178, v179\n\tv_max3_u32 v5, v5, v180, v181\n\tv_max3_u32 v5, v5, v182, v183\n\t"
      "v_max3_u32 v5, v5, v184, v185\n\tv_max3_u32 v5, v5, v186, v187\n\tv_max3_u32 v5, v5, v188, v189\n\tv_max3_u32 v5, v5, v190, v191\n\tv_max3_u32 v5, v5, v192, v193\n\t"
      "v_max3_u32 v5, v5, v194, v195\n\tv_max3_u32 v5, v5, v196, v197\n\tv_max3_u32 v5, v5, v198, v199\n\tv_max3_u32 v5, v5, v200, v201\n\tv_max3_u32 v5, v5, v202, v203\n\t"
      "v_max3_u32 v5, v5, v204, v205\n\tv_max3_u32 v5, v5, v206, v207\n\tv_max3_u32 v5, v5, v208, v209\n\tv_max3_u32 v5, v5, v210, v211\n\tv_max3_u32 v5, v5, v212, v213\n\t"
      "v_max3_u32 v5, v5, v214, v215\n\tv_max3_u32 v5, v5, v216, v217\n\tv_max3_u32 v5, v5, v218, v219\n\tv_max3_u32 v5, v5, v220, v221\n\tv_max3_u32 v5, v5, v222, v223\n\t"
      "v_cmp_eq_u32 vcc, -1, v5\n\ts_cbranch_vccz 51b\n\ts_cmp_eq_u32 s18, 0\n\ts_cbranch_scc1 51b\n\ts_sub_u32 s18, s18, 1\n\ts_branch 50b\n\t"
      "40:\n\ts_waitcnt vmcnt(0) lgkmcnt(0)\n\t"
      TKS("s_mul_i32 s48, s16, 72\n\ts_add_u32 s48, s48, 162080\n\tv_mov_b32 v224, s48\n\tds_read_b32 v225, v224 offset:0\n\ts_waitcnt lgkmcnt(0)\n\tv_readfirstlane_b32 s72, v225\n\tds_read_b32 v225, v224 offset:4\n\ts_waitcnt lgkmcnt(0)\n\tv_readfirstlane_b32 s73, v225\n\tds_read_b32 v225, v224 offset:8\n\ts_waitcnt lgkmcnt(0)\n\tv_readfirstlane_b32 s74, v225\n\tds_read_b32 v225, v224 offset:12\n\ts_waitcnt lgkmcnt(0)\n\tv_readfirstlane_b32 s75, v225\n\tds_read_b32 v225, v224 offset:16\n\ts_waitcnt lgkmcnt(0)\n\tv_readfirstlane_b32 s76, v225\n\tds_read_b32 v225, v224 offset:20\n\ts_waitcnt lgkmcnt(0)\n\tv_readfirstlane_b32 s77, v225\n\tds_read_b32 v225, v224 offset:24\n\ts_waitcnt lgkmcnt(0)\n\tv_readfirstlane_b32 s78, v225\n\tds_read_b32 v225, v224 offset:28\n\ts_waitcnt lgkmcnt(0)\n\tv_readfirstlane_b32 s79, v225\n\tds_read_b32 v225, v224 offset:32\n\ts_waitcnt lgkmcnt(0)\n\tv_readfirstlane_b32 s80, v225\n\tds_read_b32 v225, v224 offset:36\n\ts_waitcnt lgkmcnt(0)\n\tv_readfirstlane_b32 s81, v225\n\tds_read_b32 v225, v224 offset:40\n\ts_waitcnt lgkmcnt(0)\n\tv_readfirstlane_b32 s82, v225\n\tds_read_b32 v225, v224 offset:44\n\ts_waitcnt lgkmcnt(0)\n\tv_readfirstlane_b32 s83, v225\n\tds_read_b32 v225, v224 offset:48\n\ts_waitcnt lgkmcnt(0)\n\tv_readfirstlane_b32 s84, v225\n\tds_read_b32 v225, v224 offset:52\n\ts_waitcnt lgkmcnt(0)\n\tv_readfirstlane_b32 s85, v225\n\tds_read_b32 v225, v224 offset:56\n\ts_waitcnt lgkmcnt(0)\n\tv_readfirstlane_b32 s86, v225\n\tds_read_b32 v225, v224 offset:60\n\ts_waitcnt lgkmcnt(0)\n\tv_readfirstlane_b32 s87, v225\n\tds_read_b32 v225, v224 offset:64\n\ts_waitcnt lgkmcnt(0)\n\tv_readfirstlane_b32 s88, v225\n\tds_read_b32 v225, v224 offset:68\n\ts_waitcnt lgkmcnt(0)\n\tv_readfirstlane_b32 s89, v225\n\t")
      STS("s_lshl_b32 s48, s16, 2\n\ts_add_u32 s48, s48, 162944\n\tv_mov_b32 v0, s48\n\tv_mov_b32 v1, s50\n\tds_write_b32 v0, v1\n\ts_cmp_lg_u32 s16, 0\n\ts_cbranch_scc1 97f\n\tv_mov_b32 v1, s51\n\tds_write_b32 v0, v1 offset:16\n\t97:\n\ts_cmp_lg_u32 s16, -1\n\ts_cbranch_scc1 95f\n\tv_mov_b32 v1, s61\n\tds_write_b32 v0, v1 offset:24\n\tv_mov_b32 v1, s21\n\tds_write_b32 v0, v1 offset:28\n\tv_mov_b32 v1, s22\n\tds_write_b32 v0, v1 offset:32\n\t95:\n\t")
      "s_cmp_lg_u32 s18, 0\n\ts_cbranch_scc1 96f\n\tv_mov_b32 v1, 1\n\tbuffer_store_dword v1, off, s[12:15], 0 offset:3328 sc1\n\t"
      "96:\n\t"
      : TOK_REGS :: "memory", "vcc", "scc", S_CLOB TK_CLOB);
  } else {
    unsigned seenZ = 0;
    gu32* f = c.ctrl + (C_FLAG + (F_Z + (y >> 4) * 32 + (lane & 31)) * 128) / 4;
    for (int n = 0; n < NSEG; ++n) {
      const int s = n; (void)s;
      if (n >= 4) { lds_wait(TDONE, n - 3); lds_wait(TDONE + 1, n - 3); }
      if (n >= 2) lds_wait(PUB, n - 1);
      const unsigned t0 = (unsigned)__builtin_amdgcn_s_memtime();
#pragma unroll 1
      while (seenZ < (unsigned)n + 1u) {
        unsigned v = __hip_atomic_load(f, RLX_AGENT);
#pragma unroll
        for (int o = 1; o < 32; o <<= 1) v = min(v, (unsigned)__shfl_xor((int)v, o));
        seenZ = __builtin_amdgcn_readfirstlane(v);
        if (seenZ >= (unsigned)n + 1u || spend(c)) break;
        __builtin_amdgcn_s_sleep(2);
      }
      if (n >= 2) {
#pragma unroll 1
        while ((unsigned)__builtin_amdgcn_s_memtime() - t0 < ZDLY * 64u) {}
      }
      unsigned so;
      asm volatile(
      "s_add_u32 m0, %[m], 0\n\ts_add_u32 %[so], %[zb], 0\n\tbuffer_load_dwordx4 %[o], %[rs], %[so] offen offset:0 sc1 lds\n\ts_nop 6\n\t"
      "buffer_load_dwordx4 %[o], %[rs], %[so] offen offset:1024 sc1 lds\n\ts_nop 6\n\tbuffer_load_dwordx4 %[o], %[rs], %[so] offen offset:2048 sc1 lds\n\ts_nop 6\n\t"
      "buffer_load_dwordx4 %[o], %[rs], %[so] offen offset:3072 sc1 lds\n\ts_nop 6\n\ts_add_u32 m0, %[m], 4096\n\ts_add_u32 %[so], %[zb], 4096\n\t"
      "buffer_load_dwordx4 %[o], %[rs], %[so] offen offset:0 sc1 lds\n\ts_nop 6\n\tbuffer_load_dwordx4 %[o], %[rs], %[so] offen offset:1024 sc1 lds\n\ts_nop 6\n\t"
      "buffer_load_dwordx4 %[o], %[rs], %[so] offen offset:2048 sc1 lds\n\ts_nop 6\n\tbuffer_load_dwordx4 %[o], %[rs], %[so] offen offset:3072 sc1 lds\n\ts_nop 6\n\t"
      "s_add_u32 m0, %[m], 8192\n\ts_add_u32 %[so], %[zb], 16384\n\tbuffer_load_dwordx4 %[o], %[rs], %[so] offen offset:0 sc1 lds\n\ts_nop 6\n\t"
      "buffer_load_dwordx4 %[o], %[rs], %[so] offen offset:1024 sc1 lds\n\ts_nop 6\n\tbuffer_load_dwordx4 %[o], %[rs], %[so] offen offset:2048 sc1 lds\n\ts_nop 6\n\t"
      "buffer_load_dwordx4 %[o], %[rs], %[so] offen offset:3072 sc1 lds\n\ts_nop 6\n\ts_add_u32 m0, %[m], 12288\n\ts_add_u32 %[so], %[zb], 20480\n\t"
      "buffer_load_dwordx4 %[o], %[rs], %[so] offen offset:0 sc1 lds\n\ts_nop 6\n\tbuffer_load_dwordx4 %[o], %[rs], %[so] offen offset:1024 sc1 lds\n\ts_nop 6\n\t"
      "buffer_load_dwordx4 %[o], %[rs], %[so] offen offset:2048 sc1 lds\n\ts_nop 6\n\tbuffer_load_dwordx4 %[o], %[rs], %[so] offen offset:3072 sc1 lds\n\ts_nop 6\n\t"
      "s_add_u32 m0, %[m], 16384\n\ts_add_u32 %[so], %[zb], 32768\n\tbuffer_load_dwordx4 %[o], %[rs], %[so] offen offset:0 sc1 lds\n\ts_nop 6\n\t"
      "buffer_load_dwordx4 %[o], %[rs], %[so] offen offset:1024 sc1 lds\n\ts_nop 6\n\tbuffer_load_dwordx4 %[o], %[rs], %[so] offen offset:2048 sc1 lds\n\ts_nop 6\n\t"
      "buffer_load_dwordx4 %[o], %[rs], %[so] offen offset:3072 sc1 lds\n\ts_nop 6\n\ts_add_u32 m0, %[m], 20480\n\ts_add_u32 %[so], %[zb], 36864\n\t"
      "buffer_load_dwordx4 %[o], %[rs], %[so] offen offset:0 sc1 lds\n\ts_nop 6\n\tbuffer_load_dwordx4 %[o], %[rs], %[so] offen offset:1024 sc1 lds\n\ts_nop 6\n\t"
      "buffer_load_dwordx4 %[o], %[rs], %[so] offen offset:2048 sc1 lds\n\ts_nop 6\n\tbuffer_load_dwordx4 %[o], %[rs], %[so] offen offset:3072 sc1 lds\n\ts_nop 6\n\t"
      "s_add_u32 m0, %[m], 24576\n\ts_add_u32 %[so], %[zb], 49152\n\tbuffer_load_dwordx4 %[o], %[rs], %[so] offen offset:0 sc1 lds\n\ts_nop 6\n\t"
      "buffer_load_dwordx4 %[o], %[rs], %[so] offen offset:1024 sc1 lds\n\ts_nop 6\n\tbuffer_load_dwordx4 %[o], %[rs], %[so] offen offset:2048 sc1 lds\n\ts_nop 6\n\t"
      "buffer_load_dwordx4 %[o], %[rs], %[so] offen offset:3072 sc1 lds\n\ts_nop 6\n\ts_add_u32 m0, %[m], 28672\n\ts_add_u32 %[so], %[zb], 53248\n\t"
      "buffer_load_dwordx4 %[o], %[rs], %[so] offen offset:0 sc1 lds\n\ts_nop 6\n\tbuffer_load_dwordx4 %[o], %[rs], %[so] offen offset:1024 sc1 lds\n\ts_nop 6\n\t"
      "buffer_load_dwordx4 %[o], %[rs], %[so] offen offset:2048 sc1 lds\n\ts_nop 6\n\tbuffer_load_dwordx4 %[o], %[rs], %[so] offen offset:3072 sc1 lds\n\ts_nop 6\n\t"
        : [so] "=&s"(so) : [o] "v"(lane * 16 + ((HACK & 8) << 27)), [rs] "s"(c.rs), [m] "s"(ZR + (n & 3) * 32768), [zb] "s"(R_Z + ((n % ZRN) * 64 + y) * 65536 + ((para ^ (y >> 5)) << 13)) : "memory", "scc");
      VMWAIT(0);
      lds_post(ZFULL, n + 1);
    }
    constexpr int s = NSEG - 1;
    const int uo = y * 16384 + para * 8192 + lane * 16, ub = R_U + (s & 1) * (64 * 16384);
    lds_wait(GO, s); lds_wait(TDONE, s - 2); lds_wait(TDONE + 1, s - 2);
    v4u u[8];
#pragma unroll 1
    for (;;) {
      asm volatile("" ::: "memory");
#pragma unroll
      for (int i = 0; i < 8; ++i) u[i] = ld16s(c.rs, uo + (i & 3) * 1024, ub + (i >> 2) * 4096);
      SCHED();
      unsigned t = 0;
#pragma unroll
      for (int i = 0; i < 8; ++i) t = umax(t, top(u[i]));
      if (allok(t) || spend(c)) break;
    }
#pragma unroll
    for (int i = 0; i < 8; ++i) sts16(c.L, UL + (s % 3) * 8192 + i * 1024 + lane * 16, u[i]);
    lds_post(GO, s + 1);
  }
  TICK_OUT();
  if (c.budget == 0u) __hip_atomic_store(c.ctrl + C_TMO / 4, 1u, RLX_AGENT);
}

#define ENTRY plo((const void*)p), phi((const void*)p), c.att ? 0u : (unsigned)c.b + 256u, (unsigned)(C_VERDICT + c.att * 128)
DEV void g_role(Ctx& c, int mat, int h, int gi) {
  const KArg p = enter(c);
  c.budget = SPIN_CAP;
  const int w = c.w, lane = fresh_lane(), g = lane >> 4, h2 = lane >> 5, r32 = lane & 31, q = (lane >> 2) & 3, pp = lane & 3;
  if (mat == 0) { claim(c, 0); __builtin_amdgcn_s_sleep(64); __builtin_amdgcn_s_sleep(127); }
  v32u k;
#pragma unroll
  for (int i = 0; i < 8; ++i) {
    k[i] = r32 * 2048 + (((2 * i + h2) ^ (r32 & 15)) << 4); k[8 + i] = k[i] + 65536;
    k[16 + i] = (8 * w + i) * 2048 + ((lane ^ ((8 * w + i) & 15)) << 4);
  }
  k[24] = (4 * w + 2 * h2) * 512 + r32 * 16 + ((HACK & 4) << 28);
  k[25] = min((gi * 4 + w) * 6 + min(lane, 5), 511) * 128;
  k[26] = 0; k[27] = (lane | w) ? 1 << 30 : 0; k[28] = (mat | (lane >> 2) | (HACK & 16)) ? 1 << 30 : h * 4096 + (4 * w + lane) * 128;
  k[29] = (16 * w + g) * 8192 + h * 256 + (((lane & 15) ^ ((g & 1) | (h2 << 3))) << 4);
  k[30] = 65536 + (8 * h2 + q) * 256 + (((8 * (w >> 1) + 4 * (g & 1) + 2 * (pp >> 1) + (w & 1)) ^ ((q & 1) | ((q >> 1) << 3))) << 4) + 8 * (pp & 1);
  k[31] = k[30] + 65536;
  const u16* W = mat == 0 ? p->Wq : mat == 1 ? p->Wk : p->Wv;
  asm volatile(
      "s_add_u32 m0, s50, 0\n\ts_nop 0\n\tbuffer_load_dwordx4 v16, s[44:47], 0 offen offset:0  lds\n\ts_add_u32 m0, s50, 0\n\ts_nop 0\n\t"
      "buffer_load_dwordx4 v16, s[44:47], 0 offen offset:1024  lds\n\ts_add_u32 m0, s50, 2048\n\ts_nop 0\n\tbuffer_load_dwordx4 v17, s[44:47], 0 offen offset:0  lds\n\t"
      "s_add_u32 m0, s50, 2048\n\ts_nop 0\n\tbuffer_load_dwordx4 v17, s[44:47], 0 offen offset:1024  lds\n\ts_add_u32 m0, s50, 4096\n\ts_nop 0\n\t"
      "buffer_load_dwordx4 v18, s[44:47], 0 offen offset:0  lds\n\ts_add_u32 m0, s50, 4096\n\ts_nop 0\n\tbuffer_load_dwordx4 v18, s[44:47], 0 offen offset:1024  lds\n\t"
      "s_add_u32 m0, s50, 6144\n\ts_nop 0\n\tbuffer_load_dwordx4 v19, s[44:47], 0 offen offset:0  lds\n\ts_add_u32 m0, s50, 6144\n\ts_nop 0\n\t"
      "buffer_load_dwordx4 v19, s[44:47], 0 offen offset:1024  lds\n\ts_add_u32 m0, s50, 8192\n\ts_nop 0\n\tbuffer_load_dwordx4 v20, s[44:47], 0 offen offset:0  lds\n\t"
      "s_add_u32 m0, s50, 8192\n\ts_nop 0\n\tbuffer_load_dwordx4 v20, s[44:47], 0 offen offset:1024  lds\n\ts_add_u32 m0, s50, 10240\n\ts_nop 0\n\t"
      "buffer_load_dwordx4 v21, s[44:47], 0 offen offset:0  lds\n\ts_add_u32 m0, s50, 10240\n\ts_nop 0\n\tbuffer_load_dwordx4 v21, s[44:47], 0 offen offset:1024  lds\n\t"
      "s_add_u32 m0, s50, 12288\n\ts_nop 0\n\tbuffer_load_dwordx4 v22, s[44:47], 0 offen offset:0  lds\n\ts_add_u32 m0, s50, 12288\n\ts_nop 0\n\t"
      "buffer_load_dwordx4 v22, s[44:47], 0 offen offset:1024  lds\n\ts_add_u32 m0, s50, 14336\n\ts_nop 0\n\tbuffer_load_dwordx4 v23, s[44:47], 0 offen offset:0  lds\n\t"
      "s_add_u32 m0, s50, 14336\n\ts_nop 0\n\tbuffer_load_dwordx4 v23, s[44:47], 0 offen offset:1024  lds\n\ts_add_u32 m0, s51, 0\n\ts_mov_b32 s48, 0x0\n\t"
      "buffer_load_dwordx4 v29, s[40:43], s48 offen lds\n\ts_add_u32 m0, s51, 1024\n\ts_mov_b32 s48, 0x8000\n\tbuffer_load_dwordx4 v29, s[40:43], s48 offen lds\n\t"
      "s_add_u32 m0, s51, 2048\n\ts_mov_b32 s48, 0x10000\n\tbuffer_load_dwordx4 v29, s[40:43], s48 offen lds\n\ts_add_u32 m0, s51, 3072\n\ts_mov_b32 s48, 0x18000\n\t"
      "buffer_load_dwordx4 v29, s[40:43], s48 offen lds\n\ts_add_u32 m0, s51, 16384\n\ts_mov_b32 s48, 0x80000\n\tbuffer_load_dwordx4 v29, s[40:43], s48 offen lds\n\t"
      "s_add_u32 m0, s51, 17408\n\ts_mov_b32 s48, 0x88000\n\tbuffer_load_dwordx4 v29, s[40:43], s48 offen lds\n\ts_add_u32 m0, s51, 18432\n\ts_mov_b32 s48, 0x90000\n\t"
      "buffer_load_dwordx4 v29, s[40:43], s48 offen lds\n\ts_add_u32 m0, s51, 19456\n\ts_mov_b32 s48, 0x98000\n\tbuffer_load_dwordx4 v29, s[40:43], s48 offen lds\n\t"
      "s_add_u32 m0, s51, 32768\n\ts_mov_b32 s48, 0x100000\n\tbuffer_load_dwordx4 v29, s[40:43], s48 offen lds\n\ts_add_u32 m0, s51, 33792\n\ts_mov_b32 s48, 0x108000\n\t"
      "buffer_load_dwordx4 v29, s[40:43], s48 offen lds\n\ts_add_u32 m0, s51, 34816\n\ts_mov_b32 s48, 0x110000\n\tbuffer_load_dwordx4 v29, s[40:43], s48 offen lds\n\t"
      "s_add_u32 m0, s51, 35840\n\ts_mov_b32 s48, 0x118000\n\tbuffer_load_dwordx4 v29, s[40:43], s48 offen lds\n\ts_add_u32 m0, s51, 49152\n\ts_mov_b32 s48, 0x180000\n\t"
      "buffer_load_dwordx4 v29, s[40:43], s48 offen lds\n\ts_add_u32 m0, s51, 50176\n\ts_mov_b32 s48, 0x188000\n\tbuffer_load_dwordx4 v29, s[40:43], s48 offen lds\n\t"
      "s_add_u32 m0, s51, 51200\n\ts_mov_b32 s48, 0x190000\n\tbuffer_load_dwordx4 v29, s[40:43], s48 offen lds\n\ts_add_u32 m0, s51, 52224\n\ts_mov_b32 s48, 0x198000\n\t"
      "buffer_load_dwordx4 v29, s[40:43], s48 offen lds\n\ts_add_u32 m0, s51, 65536\n\ts_mov_b32 s48, 0x200000\n\tbuffer_load_dwordx4 v29, s[40:43], s48 offen lds\n\t"
      "s_add_u32 m0, s51, 66560\n\ts_mov_b32 s48, 0x208000\n\tbuffer_load_dwordx4 v29, s[40:43], s48 offen lds\n\ts_add_u32 m0, s51, 67584\n\ts_mov_b32 s48, 0x210000\n\t"
      "buffer_load_dwordx4 v29, s[40:43], s48 offen lds\n\ts_add_u32 m0, s51, 68608\n\ts_mov_b32 s48, 0x218000\n\tbuffer_load_dwordx4 v29, s[40:43], s48 offen lds\n\t"
      :: "{v[16:23]}"(__builtin_shufflevector(k, k, 16, 17, 18, 19, 20, 21, 22, 23)), "{v29}"(k[29]), "{s[40:43]}"(mkrs(W, 1024 * 4096 * 2)), "{s[44:47]}"(mkrs(p->x, 2048 * 2048)),
         "{s50}"(w * 16384), "{s51}"(65536 + w * 4096) : "memory", "scc", "s48");
  if (mat != 0) claim(c, 0);
  v32u s0 = {RSRCS, (unsigned)w, 0, c.budget, (unsigned)((mat == 0 ? R_QX : R_KX) + (hblock(h) ^ (mat == 2 ? 8192 : 0)) + 15 * 16384), 0, 0, (unsigned)(C_FLAG + (mat * 32 + h) * 128), 0,
             plo(p->out), phi(p->out), 2048 * 2048 * 2, 0x00020000};
  v32u s1 = {plo(W), phi(W), 1024 * 4096 * 2, 0x00020000, plo(p->x), phi(p->x), 2048 * 2048, 0x00020000, 0, 0, (unsigned)(w * 16384), (unsigned)(65536 + w * 4096), 0, 0, 0, 0,
             0, 0, 0, 0, ENTRY};
  v32u wa[8], wv[5];
  asm volatile(
      "s_nop 0\n\tv_readfirstlane_b32 s8, v160\n\tv_readfirstlane_b32 s9, v161\n\tv_readfirstlane_b32 s10, v162\n\tv_readfirstlane_b32 s11, v163\n\t"
      "v_readfirstlane_b32 s12, v164\n\tv_readfirstlane_b32 s13, v165\n\tv_readfirstlane_b32 s14, v166\n\tv_readfirstlane_b32 s15, v167\n\tv_readfirstlane_b32 s16, v168\n\t"
      "v_readfirstlane_b32 s17, v169\n\tv_readfirstlane_b32 s18, v170\n\tv_readfirstlane_b32 s19, v171\n\tv_readfirstlane_b32 s20, v172\n\tv_readfirstlane_b32 s21, v173\n\t"
      "v_readfirstlane_b32 s22, v174\n\tv_readfirstlane_b32 s24, v176\n\tv_readfirstlane_b32 s25, v177\n\tv_readfirstlane_b32 s26, v178\n\tv_readfirstlane_b32 s27, v179\n\t"
      "v_readfirstlane_b32 s40, v192\n\tv_readfirstlane_b32 s41, v193\n\tv_readfirstlane_b32 s42, v194\n\tv_readfirstlane_b32 s43, v195\n\tv_readfirstlane_b32 s44, v196\n\t"
      "v_readfirstlane_b32 s45, v197\n\tv_readfirstlane_b32 s46, v198\n\tv_readfirstlane_b32 s47, v199\n\tv_readfirstlane_b32 s50, v202\n\tv_readfirstlane_b32 s51, v203\n\t"
      "v_readfirstlane_b32 s60, v212\n\tv_readfirstlane_b32 s61, v213\n\tv_readfirstlane_b32 s62, v214\n\tv_readfirstlane_b32 s63, v215\n\ts_nop 4\n\t"
      TKS("s_mul_i32 s48, s16, 72\n\ts_add_u32 s48, s48, 162080\n\tv_mov_b32 v224, s48\n\tv_mov_b32 v225, s72\n\tds_write_b32 v224, v225 offset:0\n\tv_mov_b32 v225, s73\n\tds_write_b32 v224, v225 offset:4\n\tv_mov_b32 v225, s74\n\tds_write_b32 v224, v225 offset:8\n\tv_mov_b32 v225, s75\n\tds_write_b32 v224, v225 offset:12\n\tv_mov_b32 v225, s76\n\tds_write_b32 v224, v225 offset:16\n\tv_mov_b32 v225, s77\n\tds_write_b32 v224, v225 offset:20\n\tv_mov_b32 v225, s78\n\tds_write_b32 v224, v225 offset:24\n\tv_mov_b32 v225, s79\n\tds_write_b32 v224, v225 offset:28\n\tv_mov_b32 v225, s80\n\tds_write_b32 v224, v225 offset:32\n\tv_mov_b32 v225, s81\n\tds_write_b32 v224, v225 offset:36\n\tv_mov_b32 v225, s82\n\tds_write_b32 v224, v225 offset:40\n\tv_mov_b32 v225, s83\n\tds_write_b32 v224, v225 offset:44\n\tv_mov_b32 v225, s84\n\tds_write_b32 v224, v225 offset:48\n\tv_mov_b32 v225, s85\n\tds_write_b32 v224, v225 offset:52\n\tv_mov_b32 v225, s86\n\tds_write_b32 v224, v225 offset:56\n\tv_mov_b32 v225, s87\n\tds_write_b32 v224, v225 offset:60\n\tv_mov_b32 v225, s88\n\tds_write_b32 v224, v225 offset:64\n\tv_mov_b32 v225, s89\n\tds_write_b32 v224, v225 offset:68\n\t")
      "v_mov_b32 v90, v28\n\tbuffer_load_dword v91, v90, s[24:27], 0 offen\n\ts_mov_b32 s48, 0x10000\n\tbuffer_load_dword v26, v25, s[44:47], s48 offen\n\t"
      "s_mov_b32 s48, 0x20000\n\tbuffer_load_dword v26, v25, s[44:47], s48 offen\n\ts_mov_b32 s48, 0x30000\n\tbuffer_load_dword v26, v25, s[44:47], s48 offen\n\t"
      "s_waitcnt vmcnt(16)\n\ts_barrier\n\tds_read_b64_tr_b16 a[0:1], v30 offset:0\n\tds_read_b64_tr_b16 a[2:3], v30 offset:1024\n\t"
      "ds_read_b64_tr_b16 a[4:5], v30 offset:4096\n\tds_read_b64_tr_b16 a[6:7], v30 offset:5120\n\tds_read_b64_tr_b16 a[8:9], v30 offset:8192\n\t"
      "ds_read_b64_tr_b16 a[10:11], v30 offset:9216\n\tds_read_b64_tr_b16 a[12:13], v30 offset:12288\n\tds_read_b64_tr_b16 a[14:15], v30 offset:13312\n\ts_waitcnt lgkmcnt(0)\n\t"
      "s_barrier\n\ts_add_u32 m0, s51, 0\n\ts_mov_b32 s48, 0x280000\n\tbuffer_load_dwordx4 v29, s[40:43], s48 offen lds\n\ts_add_u32 m0, s51, 1024\n\ts_mov_b32 s48, 0x288000\n\t"
      "buffer_load_dwordx4 v29, s[40:43], s48 offen lds\n\ts_add_u32 m0, s51, 2048\n\ts_mov_b32 s48, 0x290000\n\tbuffer_load_dwordx4 v29, s[40:43], s48 offen lds\n\t"
      "s_add_u32 m0, s51, 3072\n\ts_mov_b32 s48, 0x298000\n\tbuffer_load_dwordx4 v29, s[40:43], s48 offen lds\n\ts_waitcnt vmcnt(16)\n\ts_barrier\n\t"
      "ds_read_b64_tr_b16 a[16:17], v30 offset:16384\n\tds_read_b64_tr_b16 a[18:19], v30 offset:17408\n\tds_read_b64_tr_b16 a[20:21], v30 offset:20480\n\t"
      "ds_read_b64_tr_b16 a[22:23], v30 offset:21504\n\tds_read_b64_tr_b16 a[24:25], v30 offset:24576\n\tds_read_b64_tr_b16 a[26:27], v30 offset:25600\n\t"
      "ds_read_b64_tr_b16 a[28:29], v30 offset:28672\n\tds_read_b64_tr_b16 a[30:31], v30 offset:29696\n\ts_waitcnt lgkmcnt(0)\n\ts_barrier\n\ts_add_u32 m0, s51, 16384\n\t"
      "s_mov_b32 s48, 0x300000\n\tbuffer_load_dwordx4 v29, s[40:43], s48 offen lds\n\ts_add_u32 m0, s51, 17408\n\ts_mov_b32 s48, 0x308000\n\t"
      "buffer_load_dwordx4 v29, s[40:43], s48 offen lds\n\ts_add_u32 m0, s51, 18432\n\ts_mov_b32 s48, 0x310000\n\tbuffer_load_dwordx4 v29, s[40:43], s48 offen lds\n\t"
      "s_add_u32 m0, s51, 19456\n\ts_mov_b32 s48, 0x318000\n\tbuffer_load_dwordx4 v29, s[40:43], s48 offen lds\n\ts_waitcnt vmcnt(16)\n\ts_barrier\n\t"
      "ds_read_b64_tr_b16 a[32:33], v30 offset:32768\n\tds_read_b64_tr_b16 a[34:35], v30 offset:33792\n\tds_read_b64_tr_b16 a[36:37], v30 offset:36864\n\t"
      "ds_read_b64_tr_b16 a[38:39], v30 offset:37888\n\tds_read_b64_tr_b16 a[40:41], v30 offset:40960\n\tds_read_b64_tr_b16 a[42:43], v30 offset:41984\n\t"
      "ds_read_b64_tr_b16 a[44:45], v30 offset:45056\n\tds_read_b64_tr_b16 a[46:47], v30 offset:46080\n\ts_waitcnt lgkmcnt(0)\n\ts_barrier\n\ts_add_u32 m0, s51, 32768\n\t"
      "s_mov_b32 s48, 0x380000\n\tbuffer_load_dwordx4 v29, s[40:43], s48 offen lds\n\ts_add_u32 m0, s51, 33792\n\ts_mov_b32 s48, 0x388000\n\t"
      "buffer_load_dwordx4 v29, s[40:43], s48 offen lds\n\ts_add_u32 m0, s51, 34816\n\ts_mov_b32 s48, 0x390000\n\tbuffer_load_dwordx4 v29, s[40:43], s48 offen lds\n\t"
      "s_add_u32 m0, s51, 35840\n\ts_mov_b32 s48, 0x398000\n\tbuffer_load_dwordx4 v29, s[40:43], s48 offen lds\n\ts_waitcnt vmcnt(16)\n\ts_barrier\n\t"
      "ds_read_b64_tr_b16 a[48:49], v30 offset:49152\n\tds_read_b64_tr_b16 a[50:51], v30 offset:50176\n\tds_read_b64_tr_b16 a[52:53], v30 offset:53248\n\t"
      "ds_read_b64_tr_b16 a[54:55], v30 offset:54272\n\tds_read_b64_tr_b16 a[56:57], v30 offset:57344\n\tds_read_b64_tr_b16 a[58:59], v30 offset:58368\n\t"
      "ds_read_b64_tr_b16 a[60:61], v30 offset:61440\n\tds_read_b64_tr_b16 a[62:63], v30 offset:62464\n\ts_waitcnt lgkmcnt(0)\n\ts_barrier\n\ts_add_u32 m0, s51, 49152\n\t"
      "s_mov_b32 s48, 0x400000\n\tbuffer_load_dwordx4 v29, s[40:43], s48 offen lds\n\ts_add_u32 m0, s51, 50176\n\ts_mov_b32 s48, 0x408000\n\t"
      "buffer_load_dwordx4 v29, s[40:43], s48 offen lds\n\ts_add_u32 m0, s51, 51200\n\ts_mov_b32 s48, 0x410000\n\tbuffer_load_dwordx4 v29, s[40:43], s48 offen lds\n\t"
      "s_add_u32 m0, s51, 52224\n\ts_mov_b32 s48, 0x418000\n\tbuffer_load_dwordx4 v29, s[40:43], s48 offen lds\n\ts_waitcnt vmcnt(16)\n\ts_barrier\n\t"
      "ds_read_b64_tr_b16 a[64:65], v31 offset:0\n\tds_read_b64_tr_b16 a[66:67], v31 offset:1024\n\tds_read_b64_tr_b16 a[68:69], v31 offset:4096\n\t"
      "ds_read_b64_tr_b16 a[70:71], v31 offset:5120\n\tds_read_b64_tr_b16 a[72:73], v31 offset:8192\n\tds_read_b64_tr_b16 a[74:75], v31 offset:9216\n\t"
      "ds_read_b64_tr_b16 a[76:77], v31 offset:12288\n\tds_read_b64_tr_b16 a[78:79], v31 offset:13312\n\ts_waitcnt lgkmcnt(0)\n\ts_barrier\n\ts_add_u32 m0, s51, 65536\n\t"
      "s_mov_b32 s48, 0x480000\n\tbuffer_load_dwordx4 v29, s[40:43], s48 offen lds\n\ts_add_u32 m0, s51, 66560\n\ts_mov_b32 s48, 0x488000\n\t"
      "buffer_load_dwordx4 v29, s[40:43], s48 offen lds\n\ts_add_u32 m0, s51, 67584\n\ts_mov_b32 s48, 0x490000\n\tbuffer_load_dwordx4 v29, s[40:43], s48 offen lds\n\t"
      "s_add_u32 m0, s51, 68608\n\ts_mov_b32 s48, 0x498000\n\tbuffer_load_dwordx4 v29, s[40:43], s48 offen lds\n\ts_waitcnt vmcnt(16)\n\ts_barrier\n\t"
      "ds_read_b64_tr_b16 a[80:81], v30 offset:0\n\tds_read_b64_tr_b16 a[82:83], v30 offset:1024\n\tds_read_b64_tr_b16 a[84:85], v30 offset:4096\n\t"
      "ds_read_b64_tr_b16 a[86:87], v30 offset:5120\n\tds_read_b64_tr_b16 a[88:89], v30 offset:8192\n\tds_read_b64_tr_b16 a[90:91], v30 offset:9216\n\t"
      "ds_read_b64_tr_b16 a[92:93], v30 offset:12288\n\tds_read_b64_tr_b16 a[94:95], v30 offset:13312\n\ts_waitcnt lgkmcnt(0)\n\ts_barrier\n\ts_add_u32 m0, s51, 0\n\t"
      "s_mov_b32 s48, 0x500000\n\tbuffer_load_dwordx4 v29, s[40:43], s48 offen lds\n\ts_add_u32 m0, s51, 1024\n\ts_mov_b32 s48, 0x508000\n\t"
      "buffer_load_dwordx4 v29, s[40:43], s48 offen lds\n\ts_add_u32 m0, s51, 2048\n\ts_mov_b32 s48, 0x510000\n\tbuffer_load_dwordx4 v29, s[40:43], s48 offen lds\n\t"
      "s_add_u32 m0, s51, 3072\n\ts_mov_b32 s48, 0x518000\n\tbuffer_load_dwordx4 v29, s[40:43], s48 offen lds\n\ts_waitcnt vmcnt(16)\n\ts_barrier\n\t"
      "ds_read_b64_tr_b16 a[96:97], v30 offset:16384\n\tds_read_b64_tr_b16 a[98:99], v30 offset:17408\n\tds_read_b64_tr_b16 a[100:101], v30 offset:20480\n\t"
      "ds_read_b64_tr_b16 a[102:103], v30 offset:21504\n\tds_read_b64_tr_b16 a[104:105], v30 offset:24576\n\tds_read_b64_tr_b16 a[106:107], v30 offset:25600\n\t"
      "ds_read_b64_tr_b16 a[108:109], v30 offset:28672\n\tds_read_b64_tr_b16 a[110:111], v30 offset:29696\n\ts_waitcnt lgkmcnt(0)\n\ts_barrier\n\ts_add_u32 m0, s51, 16384\n\t"
      "s_mov_b32 s48, 0x580000\n\tbuffer_load_dwordx4 v29, s[40:43], s48 offen lds\n\ts_add_u32 m0, s51, 17408\n\ts_mov_b32 s48, 0x588000\n\t"
      "buffer_load_dwordx4 v29, s[40:43], s48 offen lds\n\ts_add_u32 m0, s51, 18432\n\ts_mov_b32 s48, 0x590000\n\tbuffer_load_dwordx4 v29, s[40:43], s48 offen lds\n\t"
      "s_add_u32 m0, s51, 19456\n\ts_mov_b32 s48, 0x598000\n\tbuffer_load_dwordx4 v29, s[40:43], s48 offen lds\n\ts_waitcnt vmcnt(16)\n\ts_barrier\n\t"
      "ds_read_b64_tr_b16 a[112:113], v30 offset:32768\n\tds_read_b64_tr_b16 a[114:115], v30 offset:33792\n\tds_read_b64_tr_b16 a[116:117], v30 offset:36864\n\t"
      "ds_read_b64_tr_b16 a[118:119], v30 offset:37888\n\tds_read_b64_tr_b16 a[120:121], v30 offset:40960\n\tds_read_b64_tr_b16 a[122:123], v30 offset:41984\n\t"
      "ds_read_b64_tr_b16 a[124:125], v30 offset:45056\n\tds_read_b64_tr_b16 a[126:127], v30 offset:46080\n\ts_waitcnt lgkmcnt(0)\n\ts_barrier\n\ts_add_u32 m0, s51, 32768\n\t"
      "s_mov_b32 s48, 0x600000\n\tbuffer_load_dwordx4 v29, s[40:43], s48 offen lds\n\ts_add_u32 m0, s51, 33792\n\ts_mov_b32 s48, 0x608000\n\t"
      "buffer_load_dwordx4 v29, s[40:43], s48 offen lds\n\ts_add_u32 m0, s51, 34816\n\ts_mov_b32 s48, 0x610000\n\tbuffer_load_dwordx4 v29, s[40:43], s48 offen lds\n\t"
      "s_add_u32 m0, s51, 35840\n\ts_mov_b32 s48, 0x618000\n\tbuffer_load_dwordx4 v29, s[40:43], s48 offen lds\n\ts_waitcnt vmcnt(16)\n\ts_barrier\n\t"
      "ds_read_b64_tr_b16 a[128:129], v30 offset:49152\n\tds_read_b64_tr_b16 a[130:131], v30 offset:50176\n\tds_read_b64_tr_b16 a[132:133], v30 offset:53248\n\t"
      "ds_read_b64_tr_b16 a[134:135], v30 offset:54272\n\tds_read_b64_tr_b16 a[136:137], v30 offset:57344\n\tds_read_b64_tr_b16 a[138:139], v30 offset:58368\n\t"
      "ds_read_b64_tr_b16 a[140:141], v30 offset:61440\n\tds_read_b64_tr_b16 a[142:143], v30 offset:62464\n\ts_waitcnt lgkmcnt(0)\n\ts_barrier\n\ts_add_u32 m0, s51, 49152\n\t"
      "s_mov_b32 s48, 0x680000\n\tbuffer_load_dwordx4 v29, s[40:43], s48 offen lds\n\ts_add_u32 m0, s51, 50176\n\ts_mov_b32 s48, 0x688000\n\t"
      "buffer_load_dwordx4 v29, s[40:43], s48 offen lds\n\ts_add_u32 m0, s51, 51200\n\ts_mov_b32 s48, 0x690000\n\tbuffer_load_dwordx4 v29, s[40:43], s48 offen lds\n\t"
      "s_add_u32 m0, s51, 52224\n\ts_mov_b32 s48, 0x698000\n\tbuffer_load_dwordx4 v29, s[40:43], s48 offen lds\n\ts_waitcnt vmcnt(16)\n\ts_barrier\n\t"
      "ds_read_b64_tr_b16 a[144:145], v31 offset:0\n\tds_read_b64_tr_b16 a[146:147], v31 offset:1024\n\tds_read_b64_tr_b16 a[148:149], v31 offset:4096\n\t"
      "ds_read_b64_tr_b16 a[150:151], v31 offset:5120\n\tds_read_b64_tr_b16 a[152:153], v31 offset:8192\n\tds_read_b64_tr_b16 a[154:155], v31 offset:9216\n\t"
      "ds_read_b64_tr_b16 a[156:157], v31 offset:12288\n\tds_read_b64_tr_b16 a[158:159], v31 offset:13312\n\ts_waitcnt lgkmcnt(0)\n\ts_barrier\n\ts_add_u32 m0, s51, 65536\n\t"
      "s_mov_b32 s48, 0x700000\n\tbuffer_load_dwordx4 v29, s[40:43], s48 offen lds\n\ts_add_u32 m0, s51, 66560\n\ts_mov_b32 s48, 0x708000\n\t"
      "buffer_load_dwordx4 v29, s[40:43], s48 offen lds\n\ts_add_u32 m0, s51, 67584\n\ts_mov_b32 s48, 0x710000\n\tbuffer_load_dwordx4 v29, s[40:43], s48 offen lds\n\t"
      "s_add_u32 m0, s51, 68608\n\ts_mov_b32 s48, 0x718000\n\tbuffer_load_dwordx4 v29, s[40:43], s48 offen lds\n\ts_waitcnt vmcnt(16)\n\ts_barrier\n\t"
      "ds_read_b64_tr_b16 a[160:161], v30 offset:0\n\tds_read_b64_tr_b16 a[162:163], v30 offset:1024\n\tds_read_b64_tr_b16 a[164:165], v30 offset:4096\n\t"
      "ds_read_b64_tr_b16 a[166:167], v30 offset:5120\n\tds_read_b64_tr_b16 a[168:169], v30 offset:8192\n\tds_read_b64_tr_b16 a[170:171], v30 offset:9216\n\t"
      "ds_read_b64_tr_b16 a[172:173], v30 offset:12288\n\tds_read_b64_tr_b16 a[174:175], v30 offset:13312\n\ts_waitcnt lgkmcnt(0)\n\ts_barrier\n\ts_add_u32 m0, s51, 0\n\t"
      "s_mov_b32 s48, 0x780000\n\tbuffer_load_dwordx4 v29, s[40:43], s48 offen lds\n\ts_add_u32 m0, s51, 1024\n\ts_mov_b32 s48, 0x788000\n\t"
      "buffer_load_dwordx4 v29, s[40:43], s48 offen lds\n\ts_add_u32 m0, s51, 2048\n\ts_mov_b32 s48, 0x790000\n\tbuffer_load_dwordx4 v29, s[40:43], s48 offen lds\n\t"
      "s_add_u32 m0, s51, 3072\n\ts_mov_b32 s48, 0x798000\n\tbuffer_load_dwordx4 v29, s[40:43], s48 offen lds\n\ts_waitcnt vmcnt(16)\n\ts_barrier\n\t"
      "ds_read_b64_tr_b16 a[176:177], v30 offset:16384\n\tds_read_b64_tr_b16 a[178:179], v30 offset:17408\n\tds_read_b64_tr_b16 a[180:181], v30 offset:20480\n\t"
      "ds_read_b64_tr_b16 a[182:183], v30 offset:21504\n\tds_read_b64_tr_b16 a[184:185], v30 offset:24576\n\tds_read_b64_tr_b16 a[186:187], v30 offset:25600\n\t"
      "ds_read_b64_tr_b16 a[188:189], v30 offset:28672\n\tds_read_b64_tr_b16 a[190:191], v30 offset:29696\n\ts_waitcnt lgkmcnt(0)\n\ts_waitcnt vmcnt(12)\n\ts_barrier\n\t"
      "ds_read_b64_tr_b16 a[192:193], v30 offset:32768\n\tds_read_b64_tr_b16 a[194:195], v30 offset:33792\n\tds_read_b64_tr_b16 a[196:197], v30 offset:36864\n\t"
      "ds_read_b64_tr_b16 a[198:199], v30 offset:37888\n\tds_read_b64_tr_b16 a[200:201], v30 offset:40960\n\tds_read_b64_tr_b16 a[202:203], v30 offset:41984\n\t"
      "ds_read_b64_tr_b16 a[204:205], v30 offset:45056\n\tds_read_b64_tr_b16 a[206:207], v30 offset:46080\n\ts_waitcnt lgkmcnt(0)\n\ts_waitcnt vmcnt(8)\n\ts_barrier\n\t"
      "ds_read_b64_tr_b16 a[208:209], v30 offset:49152\n\tds_read_b64_tr_b16 a[210:211], v30 offset:50176\n\tds_read_b64_tr_b16 a[212:213], v30 offset:53248\n\t"
      "ds_read_b64_tr_b16 a[214:215], v30 offset:54272\n\tds_read_b64_tr_b16 a[216:217], v30 offset:57344\n\tds_read_b64_tr_b16 a[218:219], v30 offset:58368\n\t"
      "ds_read_b64_tr_b16 a[220:221], v30 offset:61440\n\tds_read_b64_tr_b16 a[222:223], v30 offset:62464\n\ts_waitcnt lgkmcnt(0)\n\ts_waitcnt vmcnt(4)\n\ts_barrier\n\t"
      "ds_read_b64_tr_b16 a[224:225], v31 offset:0\n\tds_read_b64_tr_b16 a[226:227], v31 offset:1024\n\tds_read_b64_tr_b16 a[228:229], v31 offset:4096\n\t"
      "ds_read_b64_tr_b16 a[230:231], v31 offset:5120\n\tds_read_b64_tr_b16 a[232:233], v31 offset:8192\n\tds_read_b64_tr_b16 a[234:235], v31 offset:9216\n\t"
      "ds_read_b64_tr_b16 a[236:237], v31 offset:12288\n\tds_read_b64_tr_b16 a[238:239], v31 offset:13312\n\ts_waitcnt lgkmcnt(0)\n\ts_waitcnt vmcnt(0)\n\ts_barrier\n\t"
      "ds_read_b64_tr_b16 a[240:241], v30 offset:0\n\tds_read_b64_tr_b16 a[242:243], v30 offset:1024\n\tds_read_b64_tr_b16 a[244:245], v30 offset:4096\n\t"
      "ds_read_b64_tr_b16 a[246:247], v30 offset:5120\n\tds_read_b64_tr_b16 a[248:249], v30 offset:8192\n\tds_read_b64_tr_b16 a[250:251], v30 offset:9216\n\t"
      "ds_read_b64_tr_b16 a[252:253], v30 offset:12288\n\tds_read_b64_tr_b16 a[254:255], v30 offset:13312\n\ts_waitcnt lgkmcnt(0)\n\t"
      STS("s_memrealtime vcc\n\ts_waitcnt lgkmcnt(0)\n\tv_mov_b32 v95, vcc_lo\n\tv_mov_b32 v94, 161024\n\tds_write_b32 v94, v95\n\ts_memtime vcc\n\ts_waitcnt lgkmcnt(0)\n\tv_mov_b32 v95, vcc_lo\n\tds_write_b32 v94, v95 offset:400\n\ts_waitcnt lgkmcnt(0)\n\t")
      "s_mov_b32 s23, 0\n\tv_mov_b32 v29, 0x40000000\n\tv_mov_b32 v88, v27\n\tv_mov_b32 v27, 0x40000000\n\tds_read_b128 v[64:67], v0 offset:0\n\t"
      "ds_read_b128 v[68:71], v1 offset:0\n\tds_read_b128 v[72:75], v2 offset:0\n\t"
      "10:\n\ts_waitcnt lgkmcnt(2)\n\tv_mfma_f32_32x32x16_bf16 v[32:47], a[0:3], v[64:67], 0\n\tds_read_b128 v[76:79], v3 offset:0\n\t"
      "s_add_u32 s23, s23, 15\n\ts_cmp_gt_u32 s17, s23\n\ts_cbranch_scc1 24f\n\t"
      "14:\n\ts_waitcnt lgkmcnt(2)\n\tv_mfma_f32_32x32x16_bf16 v[32:47], a[4:7], v[68:71], v[32:47]\n\tds_read_b128 v[64:67], v4 offset:0\n\ts_barrier\n\t"
      "s_waitcnt lgkmcnt(2)\n\tv_mfma_f32_32x32x16_bf16 v[32:47], a[8:11], v[72:75], v[32:47]\n\tds_read_b128 v[68:71], v5 offset:0\n\ts_add_u32 s48, s17, 1\n\t"
      "s_and_b32 s48, s48, 63\n\ts_lshl_b32 s20, s48, 16\n\ts_waitcnt lgkmcnt(2)\n\tv_mfma_f32_32x32x16_bf16 v[32:47], a[12:15], v[76:79], v[32:47]\n\t"
      "ds_read_b128 v[72:75], v6 offset:0\n\ts_add_u32 s48, s17, 4\n\ts_min_u32 s48, s48, 63\n\ts_lshl_b32 s21, s48, 16\n\ts_waitcnt lgkmcnt(2)\n\t"
      "v_mfma_f32_32x32x16_bf16 v[32:47], a[16:19], v[64:67], v[32:47]\n\tds_read_b128 v[76:79], v7 offset:0\n\tv_cvt_pk_bf16_f32 v80, v48, v52\n\t"
      "v_cvt_pk_bf16_f32 v81, v56, v60\n\tv_cvt_pk_bf16_f32 v82, v49, v53\n\tv_cvt_pk_bf16_f32 v83, v57, v61\n\ts_waitcnt lgkmcnt(2)\n\t"
      "v_mfma_f32_32x32x16_bf16 v[32:47], a[20:23], v[68:71], v[32:47]\n\tds_read_b128 v[64:67], v0 offset:256\n\t"
      "buffer_store_dwordx4 v[80:83], v29, s[8:11], s19 offen offset:0 sc1\n\ts_waitcnt lgkmcnt(2)\n\tv_mfma_f32_32x32x16_bf16 v[32:47], a[24:27], v[72:75], v[32:47]\n\t"
      "ds_read_b128 v[68:71], v1 offset:256\n\tv_cvt_pk_bf16_f32 v84, v50, v54\n\tv_cvt_pk_bf16_f32 v85, v58, v62\n\tv_cvt_pk_bf16_f32 v86, v51, v55\n\t"
      "v_cvt_pk_bf16_f32 v87, v59, v63\n\ts_waitcnt lgkmcnt(2)\n\tv_mfma_f32_32x32x16_bf16 v[32:47], a[28:31], v[76:79], v[32:47]\n\tds_read_b128 v[72:75], v2 offset:256\n\t"
      "buffer_store_dwordx4 v[84:87], v29, s[8:11], s19 offen offset:512 sc1\n\ts_waitcnt lgkmcnt(2)\n\tv_mfma_f32_32x32x16_bf16 v[32:47], a[32:35], v[64:67], v[32:47]\n\t"
      "ds_read_b128 v[76:79], v3 offset:256\n\ts_add_u32 m0, s50, 65536\n\tv_mov_b32 v28, s17\n\ts_waitcnt lgkmcnt(2)\n\t"
      "v_mfma_f32_32x32x16_bf16 v[32:47], a[36:39], v[68:71], v[32:47]\n\tds_read_b128 v[64:67], v4 offset:256\n\tbuffer_load_dwordx4 v16, s[44:47], s20 offen offset:0  lds\n\t"
      "s_waitcnt lgkmcnt(2)\n\tv_mfma_f32_32x32x16_bf16 v[32:47], a[40:43], v[72:75], v[32:47]\n\tds_read_b128 v[68:71], v5 offset:256\n\ts_add_u32 m0, s50, 65536\n\t"
      "s_waitcnt lgkmcnt(2)\n\tv_mfma_f32_32x32x16_bf16 v[32:47], a[44:47], v[76:79], v[32:47]\n\tds_read_b128 v[72:75], v6 offset:256\n\t"
      "buffer_load_dwordx4 v16, s[44:47], s20 offen offset:1024  lds\n\ts_waitcnt lgkmcnt(2)\n\tv_mfma_f32_32x32x16_bf16 v[32:47], a[48:51], v[64:67], v[32:47]\n\t"
      "ds_read_b128 v[76:79], v7 offset:256\n\ts_add_u32 m0, s50, 67584\n\ts_waitcnt lgkmcnt(2)\n\tv_mfma_f32_32x32x16_bf16 v[32:47], a[52:55], v[68:71], v[32:47]\n\t"
      "ds_read_b128 v[64:67], v0 offset:512\n\tbuffer_load_dwordx4 v17, s[44:47], s20 offen offset:0  lds\n\ts_waitcnt lgkmcnt(2)\n\t"
      "v_mfma_f32_32x32x16_bf16 v[32:47], a[56:59], v[72:75], v[32:47]\n\tds_read_b128 v[68:71], v1 offset:512\n\ts_add_u32 m0, s50, 67584\n\ts_waitcnt lgkmcnt(2)\n\t"
      "v_mfma_f32_32x32x16_bf16 v[32:47], a[60:63], v[76:79], v[32:47]\n\tds_read_b128 v[72:75], v2 offset:512\n\t"
      "buffer_load_dwordx4 v17, s[44:47], s20 offen offset:1024  lds\n\ts_waitcnt lgkmcnt(2)\n\tv_mfma_f32_32x32x16_bf16 v[32:47], a[64:67], v[64:67], v[32:47]\n\t"
      "ds_read_b128 v[76:79], v3 offset:512\n\ts_add_u32 m0, s50, 69632\n\ts_waitcnt lgkmcnt(2)\n\tv_mfma_f32_32x32x16_bf16 v[32:47], a[68:71], v[68:71], v[32:47]\n\t"
      "ds_read_b128 v[64:67], v4 offset:512\n\tbuffer_load_dwordx4 v18, s[44:47], s20 offen offset:0  lds\n\ts_waitcnt lgkmcnt(2)\n\t"
      "v_mfma_f32_32x32x16_bf16 v[32:47], a[72:75], v[72:75], v[32:47]\n\tds_read_b128 v[68:71], v5 offset:512\n\ts_add_u32 m0, s50, 69632\n\ts_waitcnt lgkmcnt(2)\n\t"
      "v_mfma_f32_32x32x16_bf16 v[32:47], a[76:79], v[76:79], v[32:47]\n\tds_read_b128 v[72:75], v6 offset:512\n\t"
      "buffer_load_dwordx4 v18, s[44:47], s20 offen offset:1024  lds\n\ts_waitcnt lgkmcnt(2)\n\tv_mfma_f32_32x32x16_bf16 v[32:47], a[80:83], v[64:67], v[32:47]\n\t"
      "ds_read_b128 v[76:79], v7 offset:512\n\ts_add_u32 m0, s50, 71680\n\ts_waitcnt lgkmcnt(2)\n\tv_mfma_f32_32x32x16_bf16 v[32:47], a[84:87], v[68:71], v[32:47]\n\t"
      "ds_read_b128 v[64:67], v0 offset:768\n\tbuffer_load_dwordx4 v19, s[44:47], s20 offen offset:0  lds\n\ts_waitcnt lgkmcnt(2)\n\t"
      "v_mfma_f32_32x32x16_bf16 v[32:47], a[88:91], v[72:75], v[32:47]\n\tds_read_b128 v[68:71], v1 offset:768\n\ts_add_u32 m0, s50, 71680\n\ts_waitcnt lgkmcnt(2)\n\t"
      "v_mfma_f32_32x32x16_bf16 v[32:47], a[92:95], v[76:79], v[32:47]\n\tds_read_b128 v[72:75], v2 offset:768\n\t"
      "buffer_load_dwordx4 v19, s[44:47], s20 offen offset:1024  lds\n\ts_waitcnt lgkmcnt(2)\n\tv_mfma_f32_32x32x16_bf16 v[32:47], a[96:99], v[64:67], v[32:47]\n\t"
      "ds_read_b128 v[76:79], v3 offset:768\n\ts_add_u32 m0, s50, 73728\n\ts_waitcnt lgkmcnt(2)\n\tv_mfma_f32_32x32x16_bf16 v[32:47], a[100:103], v[68:71], v[32:47]\n\t"
      "ds_read_b128 v[64:67], v4 offset:768\n\tbuffer_load_dwordx4 v20, s[44:47], s20 offen offset:0  lds\n\ts_waitcnt lgkmcnt(2)\n\t"
      "v_mfma_f32_32x32x16_bf16 v[32:47], a[104:107], v[72:75], v[32:47]\n\tds_read_b128 v[68:71], v5 offset:768\n\ts_add_u32 m0, s50, 73728\n\ts_waitcnt lgkmcnt(2)\n\t"
      "v_mfma_f32_32x32x16_bf16 v[32:47], a[108:111], v[76:79], v[32:47]\n\tds_read_b128 v[72:75], v6 offset:768\n\t"
      "buffer_load_dwordx4 v20, s[44:47], s20 offen offset:1024  lds\n\ts_waitcnt lgkmcnt(2)\n\tv_mfma_f32_32x32x16_bf16 v[32:47], a[112:115], v[64:67], v[32:47]\n\t"
      "ds_read_b128 v[76:79], v7 offset:768\n\ts_add_u32 m0, s50, 75776\n\ts_waitcnt lgkmcnt(2)\n\tv_mfma_f32_32x32x16_bf16 v[32:47], a[116:119], v[68:71], v[32:47]\n\t"
      "ds_read_b128 v[64:67], v0 offset:1024\n\tbuffer_load_dwordx4 v21, s[44:47], s20 offen offset:0  lds\n\ts_waitcnt lgkmcnt(2)\n\t"
      "v_mfma_f32_32x32x16_bf16 v[32:47], a[120:123], v[72:75], v[32:47]\n\tds_read_b128 v[68:71], v1 offset:1024\n\ts_add_u32 m0, s50, 75776\n\ts_waitcnt lgkmcnt(2)\n\t"
      "v_mfma_f32_32x32x16_bf16 v[32:47], a[124:127], v[76:79], v[32:47]\n\tds_read_b128 v[72:75], v2 offset:1024\n\t"
      "buffer_load_dwordx4 v21, s[44:47], s20 offen offset:1024  lds\n\ts_waitcnt lgkmcnt(2)\n\tv_mfma_f32_32x32x16_bf16 v[32:47], a[128:131], v[64:67], v[32:47]\n\t"
      "ds_read_b128 v[76:79], v3 offset:1024\n\ts_add_u32 m0, s50, 77824\n\ts_waitcnt vmcnt(12)\n\ts_barrier\n\ts_waitcnt lgkmcnt(2)\n\t"
      "v_mfma_f32_32x32x16_bf16 v[32:47], a[132:135], v[68:71], v[32:47]\n\tds_read_b128 v[64:67], v4 offset:1024\n\t"
      "buffer_load_dwordx4 v22, s[44:47], s20 offen offset:0  lds\n\ts_waitcnt lgkmcnt(2)\n\tv_mfma_f32_32x32x16_bf16 v[32:47], a[136:139], v[72:75], v[32:47]\n\t"
      "ds_read_b128 v[68:71], v5 offset:1024\n\ts_add_u32 m0, s50, 77824\n\tbuffer_store_dword v28, v27, s[12:15], s22 offen sc1\n\ts_waitcnt lgkmcnt(2)\n\t"
      "v_mfma_f32_32x32x16_bf16 v[32:47], a[140:143], v[76:79], v[32:47]\n\tds_read_b128 v[72:75], v6 offset:1024\n\t"
      "buffer_load_dwordx4 v22, s[44:47], s20 offen offset:1024  lds\n\ts_waitcnt lgkmcnt(2)\n\tv_mfma_f32_32x32x16_bf16 v[32:47], a[144:147], v[64:67], v[32:47]\n\t"
      "ds_read_b128 v[76:79], v7 offset:1024\n\ts_add_u32 m0, s50, 79872\n\ts_waitcnt lgkmcnt(2)\n\tv_mfma_f32_32x32x16_bf16 v[32:47], a[148:151], v[68:71], v[32:47]\n\t"
      "ds_read_b128 v[64:67], v0 offset:1280\n\tbuffer_load_dwordx4 v23, s[44:47], s20 offen offset:0  lds\n\ts_waitcnt lgkmcnt(2)\n\t"
      "v_mfma_f32_32x32x16_bf16 v[32:47], a[152:155], v[72:75], v[32:47]\n\tds_read_b128 v[68:71], v1 offset:1280\n\ts_add_u32 m0, s50, 79872\n\ts_waitcnt lgkmcnt(2)\n\t"
      "v_mfma_f32_32x32x16_bf16 v[32:47], a[156:159], v[76:79], v[32:47]\n\tds_read_b128 v[72:75], v2 offset:1280\n\t"
      "buffer_load_dwordx4 v23, s[44:47], s20 offen offset:1024  lds\n\ts_waitcnt lgkmcnt(2)\n\tv_mfma_f32_32x32x16_bf16 v[32:47], a[160:163], v[64:67], v[32:47]\n\t"
      "ds_read_b128 v[76:79], v3 offset:1280\n\tbuffer_load_dword v89, off, s[12:15], 0 offset:3840 sc1\n\ts_waitcnt lgkmcnt(2)\n\t"
      "v_mfma_f32_32x32x16_bf16 v[32:47], a[164:167], v[68:71], v[32:47]\n\tds_read_b128 v[64:67], v4 offset:1280\n\tbuffer_load_dword v26, v25, s[44:47], s21 offen \n\t"
      "s_waitcnt lgkmcnt(2)\n\tv_mfma_f32_32x32x16_bf16 v[32:47], a[168:171], v[72:75], v[32:47]\n\tds_read_b128 v[68:71], v5 offset:1280\n\ts_add_u32 s48, s19, 0x4000\n\t"
      "s_and_b32 s48, s48, 0x3c000\n\ts_andn2_b32 s19, s19, 0x3c000\n\ts_or_b32 s19, s19, s48\n\ts_waitcnt lgkmcnt(2)\n\t"
      "v_mfma_f32_32x32x16_bf16 v[32:47], a[172:175], v[76:79], v[32:47]\n\tds_read_b128 v[72:75], v6 offset:1280\n\ts_waitcnt lgkmcnt(2)\n\t"
      "v_mfma_f32_32x32x16_bf16 v[32:47], a[176:179], v[64:67], v[32:47]\n\tds_read_b128 v[76:79], v7 offset:1280\n\t"
      "s_lshl_b32 s48, s20, 1\n\tbuffer_load_dword v91, v90, s[24:27], s48 offen\n\ts_waitcnt lgkmcnt(2)\n\tv_mfma_f32_32x32x16_bf16 v[32:47], a[180:183], v[68:71], v[32:47]\n\t"
      "ds_read_b128 v[64:67], v0 offset:1536\n\ts_waitcnt lgkmcnt(2)\n\tv_mfma_f32_32x32x16_bf16 v[32:47], a[184:187], v[72:75], v[32:47]\n\t"
      "ds_read_b128 v[68:71], v1 offset:1536\n\ts_waitcnt lgkmcnt(2)\n\tv_mfma_f32_32x32x16_bf16 v[32:47], a[188:191], v[76:79], v[32:47]\n\t"
      "ds_read_b128 v[72:75], v2 offset:1536\n\ts_waitcnt lgkmcnt(2)\n\tv_mfma_f32_32x32x16_bf16 v[32:47], a[192:195], v[64:67], v[32:47]\n\t"
      "ds_read_b128 v[76:79], v3 offset:1536\n\ts_waitcnt lgkmcnt(2)\n\tv_mfma_f32_32x32x16_bf16 v[32:47], a[196:199], v[68:71], v[32:47]\n\t"
      "ds_read_b128 v[64:67], v4 offset:1536\n\ts_waitcnt lgkmcnt(2)\n\tv_mfma_f32_32x32x16_bf16 v[32:47], a[200:203], v[72:75], v[32:47]\n\t"
      "ds_read_b128 v[68:71], v5 offset:1536\n\ts_waitcnt lgkmcnt(2)\n\tv_mfma_f32_32x32x16_bf16 v[32:47], a[204:207], v[76:79], v[32:47]\n\t"
      "ds_read_b128 v[72:75], v6 offset:1536\n\ts_waitcnt lgkmcnt(2)\n\tv_mfma_f32_32x32x16_bf16 v[32:47], a[208:211], v[64:67], v[32:47]\n\t"
      "ds_read_b128 v[76:79], v7 offset:1536\n\ts_waitcnt lgkmcnt(2)\n\tv_mfma_f32_32x32x16_bf16 v[32:47], a[212:215], v[68:71], v[32:47]\n\t"
      "ds_read_b128 v[64:67], v0 offset:1792\n\ts_waitcnt lgkmcnt(2)\n\tv_mfma_f32_32x32x16_bf16 v[32:47], a[216:219], v[72:75], v[32:47]\n\t"
      "ds_read_b128 v[68:71], v1 offset:1792\n\ts_waitcnt lgkmcnt(2)\n\tv_mfma_f32_32x32x16_bf16 v[32:47], a[220:223], v[76:79], v[32:47]\n\t"
      "ds_read_b128 v[72:75], v2 offset:1792\n\ts_waitcnt lgkmcnt(2)\n\tv_mfma_f32_32x32x16_bf16 v[32:47], a[224:227], v[64:67], v[32:47]\n\t"
      "ds_read_b128 v[76:79], v3 offset:1792\n\ts_waitcnt lgkmcnt(2)\n\tv_mfma_f32_32x32x16_bf16 v[32:47], a[228:231], v[68:71], v[32:47]\n\t"
      "ds_read_b128 v[64:67], v4 offset:1792\n\ts_waitcnt lgkmcnt(2)\n\tv_mfma_f32_32x32x16_bf16 v[32:47], a[232:235], v[72:75], v[32:47]\n\t"
      "ds_read_b128 v[68:71], v5 offset:1792\n\ts_waitcnt lgkmcnt(2)\n\tv_mfma_f32_32x32x16_bf16 v[32:47], a[236:239], v[76:79], v[32:47]\n\t"
      "ds_read_b128 v[72:75], v6 offset:1792\n\ts_waitcnt lgkmcnt(2)\n\tv_mfma_f32_32x32x16_bf16 v[32:47], a[240:243], v[64:67], v[32:47]\n\t"
      "ds_read_b128 v[76:79], v7 offset:1792\n\ts_waitcnt lgkmcnt(2)\n\tv_mfma_f32_32x32x16_bf16 v[32:47], a[244:247], v[68:71], v[32:47]\n\ts_waitcnt vmcnt(2)\n\ts_barrier\n\t"
      "ds_read_b128 v[64:67], v8 offset:0\n\ts_waitcnt lgkmcnt(2)\n\tv_mfma_f32_32x32x16_bf16 v[32:47], a[248:251], v[72:75], v[32:47]\n\tds_read_b128 v[68:71], v9 offset:0\n\t"
      "v_readfirstlane_b32 s23, v89\n\ts_waitcnt lgkmcnt(2)\n\tv_mfma_f32_32x32x16_bf16 v[32:47], a[252:255], v[76:79], v[32:47]\n\tds_read_b128 v[72:75], v10 offset:0\n\t"
      STF("s_memrealtime vcc\n\ts_waitcnt lgkmcnt(0)\n\tv_mov_b32 v95, vcc_lo\n\ts_lshl_b32 vcc_hi, s17, 2\n\ts_add_u32 vcc_hi, vcc_hi, 161056\n\tv_mov_b32 v94, vcc_hi\n\tds_write_b32 v94, v95\n\ts_memtime vcc\n\ts_waitcnt lgkmcnt(0)\n\tv_mov_b32 v95, vcc_lo\n\tds_write_b32 v94, v95 offset:400\n\ts_waitcnt lgkmcnt(0)\n\t")
      "s_add_u32 s17, s17, 1\n\ts_cmp_eq_u32 s17, 1\n\ts_cbranch_scc1 20f\n\t"
      "11:\n\ts_waitcnt lgkmcnt(2)\n\tv_mfma_f32_32x32x16_bf16 v[48:63], a[0:3], v[64:67], 0\n\tds_read_b128 v[76:79], v11 offset:0\n\t"
      "s_add_u32 s23, s23, 15\n\ts_cmp_gt_u32 s17, s23\n\ts_cbranch_scc1 25f\n\t"
      "15:\n\ts_waitcnt lgkmcnt(2)\n\tv_mfma_f32_32x32x16_bf16 v[48:63], a[4:7], v[68:71], v[48:63]\n\tds_read_b128 v[64:67], v12 offset:0\n\ts_barrier\n\t"
      "s_waitcnt lgkmcnt(2)\n\tv_mfma_f32_32x32x16_bf16 v[48:63], a[8:11], v[72:75], v[48:63]\n\tds_read_b128 v[68:71], v13 offset:0\n\ts_add_u32 s48, s17, 1\n\t"
      "s_and_b32 s48, s48, 63\n\ts_lshl_b32 s20, s48, 16\n\ts_waitcnt lgkmcnt(2)\n\tv_mfma_f32_32x32x16_bf16 v[48:63], a[12:15], v[76:79], v[48:63]\n\t"
      "ds_read_b128 v[72:75], v14 offset:0\n\ts_add_u32 s48, s17, 4\n\ts_min_u32 s48, s48, 63\n\ts_lshl_b32 s21, s48, 16\n\ts_waitcnt lgkmcnt(2)\n\t"
      "v_mfma_f32_32x32x16_bf16 v[48:63], a[16:19], v[64:67], v[48:63]\n\tds_read_b128 v[76:79], v15 offset:0\n\tv_cvt_pk_bf16_f32 v80, v32, v36\n\t"
      "v_cvt_pk_bf16_f32 v81, v40, v44\n\tv_cvt_pk_bf16_f32 v82, v33, v37\n\tv_cvt_pk_bf16_f32 v83, v41, v45\n\ts_waitcnt lgkmcnt(2)\n\t"
      "v_mfma_f32_32x32x16_bf16 v[48:63], a[20:23], v[68:71], v[48:63]\n\tds_read_b128 v[64:67], v8 offset:256\n\t"
      "buffer_store_dwordx4 v[80:83], v29, s[8:11], s19 offen offset:0 sc1\n\ts_waitcnt lgkmcnt(2)\n\tv_mfma_f32_32x32x16_bf16 v[48:63], a[24:27], v[72:75], v[48:63]\n\t"
      "ds_read_b128 v[68:71], v9 offset:256\n\tv_cvt_pk_bf16_f32 v84, v34, v38\n\tv_cvt_pk_bf16_f32 v85, v42, v46\n\tv_cvt_pk_bf16_f32 v86, v35, v39\n\t"
      "v_cvt_pk_bf16_f32 v87, v43, v47\n\ts_waitcnt lgkmcnt(2)\n\tv_mfma_f32_32x32x16_bf16 v[48:63], a[28:31], v[76:79], v[48:63]\n\tds_read_b128 v[72:75], v10 offset:256\n\t"
      "buffer_store_dwordx4 v[84:87], v29, s[8:11], s19 offen offset:512 sc1\n\ts_waitcnt lgkmcnt(2)\n\tv_mfma_f32_32x32x16_bf16 v[48:63], a[32:35], v[64:67], v[48:63]\n\t"
      "ds_read_b128 v[76:79], v11 offset:256\n\ts_add_u32 m0, s50, 0\n\tv_mov_b32 v28, s17\n\ts_waitcnt lgkmcnt(2)\n\t"
      "v_mfma_f32_32x32x16_bf16 v[48:63], a[36:39], v[68:71], v[48:63]\n\tds_read_b128 v[64:67], v12 offset:256\n\tbuffer_load_dwordx4 v16, s[44:47], s20 offen offset:0  lds\n\t"
      "s_waitcnt lgkmcnt(2)\n\tv_mfma_f32_32x32x16_bf16 v[48:63], a[40:43], v[72:75], v[48:63]\n\tds_read_b128 v[68:71], v13 offset:256\n\ts_add_u32 m0, s50, 0\n\t"
      "s_waitcnt lgkmcnt(2)\n\tv_mfma_f32_32x32x16_bf16 v[48:63], a[44:47], v[76:79], v[48:63]\n\tds_read_b128 v[72:75], v14 offset:256\n\t"
      "buffer_load_dwordx4 v16, s[44:47], s20 offen offset:1024  lds\n\ts_waitcnt lgkmcnt(2)\n\tv_mfma_f32_32x32x16_bf16 v[48:63], a[48:51], v[64:67], v[48:63]\n\t"
      "ds_read_b128 v[76:79], v15 offset:256\n\ts_add_u32 m0, s50, 2048\n\ts_waitcnt lgkmcnt(2)\n\tv_mfma_f32_32x32x16_bf16 v[48:63], a[52:55], v[68:71], v[48:63]\n\t"
      "ds_read_b128 v[64:67], v8 offset:512\n\tbuffer_load_dwordx4 v17, s[44:47], s20 offen offset:0  lds\n\ts_waitcnt lgkmcnt(2)\n\t"
      "v_mfma_f32_32x32x16_bf16 v[48:63], a[56:59], v[72:75], v[48:63]\n\tds_read_b128 v[68:71], v9 offset:512\n\ts_add_u32 m0, s50, 2048\n\ts_waitcnt lgkmcnt(2)\n\t"
      "v_mfma_f32_32x32x16_bf16 v[48:63], a[60:63], v[76:79], v[48:63]\n\tds_read_b128 v[72:75], v10 offset:512\n\t"
      "buffer_load_dwordx4 v17, s[44:47], s20 offen offset:1024  lds\n\ts_waitcnt lgkmcnt(2)\n\tv_mfma_f32_32x32x16_bf16 v[48:63], a[64:67], v[64:67], v[48:63]\n\t"
      "ds_read_b128 v[76:79], v11 offset:512\n\ts_add_u32 m0, s50, 4096\n\ts_waitcnt lgkmcnt(2)\n\tv_mfma_f32_32x32x16_bf16 v[48:63], a[68:71], v[68:71], v[48:63]\n\t"
      "ds_read_b128 v[64:67], v12 offset:512\n\tbuffer_load_dwordx4 v18, s[44:47], s20 offen offset:0  lds\n\ts_waitcnt lgkmcnt(2)\n\t"
      "v_mfma_f32_32x32x16_bf16 v[48:63], a[72:75], v[72:75], v[48:63]\n\tds_read_b128 v[68:71], v13 offset:512\n\ts_add_u32 m0, s50, 4096\n\ts_waitcnt lgkmcnt(2)\n\t"
      "v_mfma_f32_32x32x16_bf16 v[48:63], a[76:79], v[76:79], v[48:63]\n\tds_read_b128 v[72:75], v14 offset:512\n\t"
      "buffer_load_dwordx4 v18, s[44:47], s20 offen offset:1024  lds\n\ts_waitcnt lgkmcnt(2)\n\tv_mfma_f32_32x32x16_bf16 v[48:63], a[80:83], v[64:67], v[48:63]\n\t"
      "ds_read_b128 v[76:79], v15 offset:512\n\ts_add_u32 m0, s50, 6144\n\ts_waitcnt lgkmcnt(2)\n\tv_mfma_f32_32x32x16_bf16 v[48:63], a[84:87], v[68:71], v[48:63]\n\t"
      "ds_read_b128 v[64:67], v8 offset:768\n\tbuffer_load_dwordx4 v19, s[44:47], s20 offen offset:0  lds\n\ts_waitcnt lgkmcnt(2)\n\t"
      "v_mfma_f32_32x32x16_bf16 v[48:63], a[88:91], v[72:75], v[48:63]\n\tds_read_b128 v[68:71], v9 offset:768\n\ts_add_u32 m0, s50, 6144\n\ts_waitcnt lgkmcnt(2)\n\t"
      "v_mfma_f32_32x32x16_bf16 v[48:63], a[92:95], v[76:79], v[48:63]\n\tds_read_b128 v[72:75], v10 offset:768\n\t"
      "buffer_load_dwordx4 v19, s[44:47], s20 offen offset:1024  lds\n\ts_waitcnt lgkmcnt(2)\n\tv_mfma_f32_32x32x16_bf16 v[48:63], a[96:99], v[64:67], v[48:63]\n\t"
      "ds_read_b128 v[76:79], v11 offset:768\n\ts_add_u32 m0, s50, 8192\n\ts_waitcnt lgkmcnt(2)\n\tv_mfma_f32_32x32x16_bf16 v[48:63], a[100:103], v[68:71], v[48:63]\n\t"
      "ds_read_b128 v[64:67], v12 offset:768\n\tbuffer_load_dwordx4 v20, s[44:47], s20 offen offset:0  lds\n\ts_waitcnt lgkmcnt(2)\n\t"
      "v_mfma_f32_32x32x16_bf16 v[48:63], a[104:107], v[72:75], v[48:63]\n\tds_read_b128 v[68:71], v13 offset:768\n\ts_add_u32 m0, s50, 8192\n\ts_waitcnt lgkmcnt(2)\n\t"
      "v_mfma_f32_32x32x16_bf16 v[48:63], a[108:111], v[76:79], v[48:63]\n\tds_read_b128 v[72:75], v14 offset:768\n\t"
      "buffer_load_dwordx4 v20, s[44:47], s20 offen offset:1024  lds\n\ts_waitcnt lgkmcnt(2)\n\tv_mfma_f32_32x32x16_bf16 v[48:63], a[112:115], v[64:67], v[48:63]\n\t"
      "ds_read_b128 v[76:79], v15 offset:768\n\ts_add_u32 m0, s50, 10240\n\ts_waitcnt lgkmcnt(2)\n\tv_mfma_f32_32x32x16_bf16 v[48:63], a[116:119], v[68:71], v[48:63]\n\t"
      "ds_read_b128 v[64:67], v8 offset:1024\n\tbuffer_load_dwordx4 v21, s[44:47], s20 offen offset:0  lds\n\ts_waitcnt lgkmcnt(2)\n\t"
      "v_mfma_f32_32x32x16_bf16 v[48:63], a[120:123], v[72:75], v[48:63]\n\tds_read_b128 v[68:71], v9 offset:1024\n\ts_add_u32 m0, s50, 10240\n\ts_waitcnt lgkmcnt(2)\n\t"
      "v_mfma_f32_32x32x16_bf16 v[48:63], a[124:127], v[76:79], v[48:63]\n\tds_read_b128 v[72:75], v10 offset:1024\n\t"
      "buffer_load_dwordx4 v21, s[44:47], s20 offen offset:1024  lds\n\ts_waitcnt lgkmcnt(2)\n\tv_mfma_f32_32x32x16_bf16 v[48:63], a[128:131], v[64:67], v[48:63]\n\t"
      "ds_read_b128 v[76:79], v11 offset:1024\n\ts_add_u32 m0, s50, 12288\n\ts_waitcnt vmcnt(12)\n\ts_barrier\n\ts_waitcnt lgkmcnt(2)\n\t"
      "v_mfma_f32_32x32x16_bf16 v[48:63], a[132:135], v[68:71], v[48:63]\n\tds_read_b128 v[64:67], v12 offset:1024\n\t"
      "buffer_load_dwordx4 v22, s[44:47], s20 offen offset:0  lds\n\ts_waitcnt lgkmcnt(2)\n\tv_mfma_f32_32x32x16_bf16 v[48:63], a[136:139], v[72:75], v[48:63]\n\t"
      "ds_read_b128 v[68:71], v13 offset:1024\n\ts_add_u32 m0, s50, 12288\n\tbuffer_store_dword v28, v27, s[12:15], s22 offen sc1\n\ts_waitcnt lgkmcnt(2)\n\t"
      "v_mfma_f32_32x32x16_bf16 v[48:63], a[140:143], v[76:79], v[48:63]\n\tds_read_b128 v[72:75], v14 offset:1024\n\t"
      "buffer_load_dwordx4 v22, s[44:47], s20 offen offset:1024  lds\n\ts_waitcnt lgkmcnt(2)\n\tv_mfma_f32_32x32x16_bf16 v[48:63], a[144:147], v[64:67], v[48:63]\n\t"
      "ds_read_b128 v[76:79], v15 offset:1024\n\ts_add_u32 m0, s50, 14336\n\ts_waitcnt lgkmcnt(2)\n\tv_mfma_f32_32x32x16_bf16 v[48:63], a[148:151], v[68:71], v[48:63]\n\t"
      "ds_read_b128 v[64:67], v8 offset:1280\n\tbuffer_load_dwordx4 v23, s[44:47], s20 offen offset:0  lds\n\ts_waitcnt lgkmcnt(2)\n\t"
      "v_mfma_f32_32x32x16_bf16 v[48:63], a[152:155], v[72:75], v[48:63]\n\tds_read_b128 v[68:71], v9 offset:1280\n\ts_add_u32 m0, s50, 14336\n\ts_waitcnt lgkmcnt(2)\n\t"
      "v_mfma_f32_32x32x16_bf16 v[48:63], a[156:159], v[76:79], v[48:63]\n\tds_read_b128 v[72:75], v10 offset:1280\n\t"
      "buffer_load_dwordx4 v23, s[44:47], s20 offen offset:1024  lds\n\ts_waitcnt lgkmcnt(2)\n\tv_mfma_f32_32x32x16_bf16 v[48:63], a[160:163], v[64:67], v[48:63]\n\t"
      "ds_read_b128 v[76:79], v11 offset:1280\n\tbuffer_load_dword v89, off, s[12:15], 0 offset:3840 sc1\n\ts_waitcnt lgkmcnt(2)\n\t"
      "v_mfma_f32_32x32x16_bf16 v[48:63], a[164:167], v[68:71], v[48:63]\n\tds_read_b128 v[64:67], v12 offset:1280\n\tbuffer_load_dword v26, v25, s[44:47], s21 offen \n\t"
      "s_waitcnt lgkmcnt(2)\n\tv_mfma_f32_32x32x16_bf16 v[48:63], a[168:171], v[72:75], v[48:63]\n\tds_read_b128 v[68:71], v13 offset:1280\n\ts_add_u32 s48, s19, 0x4000\n\t"
      "s_and_b32 s48, s48, 0x3c000\n\ts_andn2_b32 s19, s19, 0x3c000\n\ts_or_b32 s19, s19, s48\n\ts_waitcnt lgkmcnt(2)\n\t"
      "v_mfma_f32_32x32x16_bf16 v[48:63], a[172:175], v[76:79], v[48:63]\n\tds_read_b128 v[72:75], v14 offset:1280\n\tv_mov_b32 v29, v24\n\ts_waitcnt lgkmcnt(2)\n\t"
      "v_mfma_f32_32x32x16_bf16 v[48:63], a[176:179], v[64:67], v[48:63]\n\tds_read_b128 v[76:79], v15 offset:1280\n\t"
      "s_lshl_b32 s48, s20, 1\n\tbuffer_load_dword v91, v90, s[24:27], s48 offen\n\ts_waitcnt lgkmcnt(2)\n\tv_mfma_f32_32x32x16_bf16 v[48:63], a[180:183], v[68:71], v[48:63]\n\t"
      "ds_read_b128 v[64:67], v8 offset:1536\n\ts_waitcnt lgkmcnt(2)\n\tv_mfma_f32_32x32x16_bf16 v[48:63], a[184:187], v[72:75], v[48:63]\n\t"
      "ds_read_b128 v[68:71], v9 offset:1536\n\ts_waitcnt lgkmcnt(2)\n\tv_mfma_f32_32x32x16_bf16 v[48:63], a[188:191], v[76:79], v[48:63]\n\t"
      "ds_read_b128 v[72:75], v10 offset:1536\n\ts_waitcnt lgkmcnt(2)\n\tv_mfma_f32_32x32x16_bf16 v[48:63], a[192:195], v[64:67], v[48:63]\n\t"
      "ds_read_b128 v[76:79], v11 offset:1536\n\ts_waitcnt lgkmcnt(2)\n\tv_mfma_f32_32x32x16_bf16 v[48:63], a[196:199], v[68:71], v[48:63]\n\t"
      "ds_read_b128 v[64:67], v12 offset:1536\n\ts_waitcnt lgkmcnt(2)\n\tv_mfma_f32_32x32x16_bf16 v[48:63], a[200:203], v[72:75], v[48:63]\n\t"
      "ds_read_b128 v[68:71], v13 offset:1536\n\ts_waitcnt lgkmcnt(2)\n\tv_mfma_f32_32x32x16_bf16 v[48:63], a[204:207], v[76:79], v[48:63]\n\t"
      "ds_read_b128 v[72:75], v14 offset:1536\n\ts_waitcnt lgkmcnt(2)\n\tv_mfma_f32_32x32x16_bf16 v[48:63], a[208:211], v[64:67], v[48:63]\n\t"
      "ds_read_b128 v[76:79], v15 offset:1536\n\ts_waitcnt lgkmcnt(2)\n\tv_mfma_f32_32x32x16_bf16 v[48:63], a[212:215], v[68:71], v[48:63]\n\t"
      "ds_read_b128 v[64:67], v8 offset:1792\n\ts_waitcnt lgkmcnt(2)\n\tv_mfma_f32_32x32x16_bf16 v[48:63], a[216:219], v[72:75], v[48:63]\n\t"
      "ds_read_b128 v[68:71], v9 offset:1792\n\ts_waitcnt lgkmcnt(2)\n\tv_mfma_f32_32x32x16_bf16 v[48:63], a[220:223], v[76:79], v[48:63]\n\t"
      "ds_read_b128 v[72:75], v10 offset:1792\n\ts_waitcnt lgkmcnt(2)\n\tv_mfma_f32_32x32x16_bf16 v[48:63], a[224:227], v[64:67], v[48:63]\n\t"
      "ds_read_b128 v[76:79], v11 offset:1792\n\ts_waitcnt lgkmcnt(2)\n\tv_mfma_f32_32x32x16_bf16 v[48:63], a[228:231], v[68:71], v[48:63]\n\t"
      "ds_read_b128 v[64:67], v12 offset:1792\n\ts_waitcnt lgkmcnt(2)\n\tv_mfma_f32_32x32x16_bf16 v[48:63], a[232:235], v[72:75], v[48:63]\n\t"
      "ds_read_b128 v[68:71], v13 offset:1792\n\ts_waitcnt lgkmcnt(2)\n\tv_mfma_f32_32x32x16_bf16 v[48:63], a[236:239], v[76:79], v[48:63]\n\t"
      "ds_read_b128 v[72:75], v14 offset:1792\n\ts_waitcnt lgkmcnt(2)\n\tv_mfma_f32_32x32x16_bf16 v[48:63], a[240:243], v[64:67], v[48:63]\n\t"
      "ds_read_b128 v[76:79], v15 offset:1792\n\ts_waitcnt lgkmcnt(2)\n\tv_mfma_f32_32x32x16_bf16 v[48:63], a[244:247], v[68:71], v[48:63]\n\ts_waitcnt vmcnt(2)\n\ts_barrier\n\t"
      "ds_read_b128 v[64:67], v0 offset:0\n\ts_waitcnt lgkmcnt(2)\n\tv_mfma_f32_32x32x16_bf16 v[48:63], a[248:251], v[72:75], v[48:63]\n\tds_read_b128 v[68:71], v1 offset:0\n\t"
      "v_readfirstlane_b32 s23, v89\n\ts_waitcnt lgkmcnt(2)\n\tv_mfma_f32_32x32x16_bf16 v[48:63], a[252:255], v[76:79], v[48:63]\n\tds_read_b128 v[72:75], v2 offset:0\n\t"
      STF("s_memrealtime vcc\n\ts_waitcnt lgkmcnt(0)\n\tv_mov_b32 v95, vcc_lo\n\ts_lshl_b32 vcc_hi, s17, 2\n\ts_add_u32 vcc_hi, vcc_hi, 161056\n\tv_mov_b32 v94, vcc_hi\n\tds_write_b32 v94, v95\n\ts_memtime vcc\n\ts_waitcnt lgkmcnt(0)\n\tv_mov_b32 v95, vcc_lo\n\tds_write_b32 v94, v95 offset:400\n\ts_waitcnt lgkmcnt(0)\n\t")
      "s_add_u32 s17, s17, 1\n\ts_cmp_lt_u32 s17, 64\n\ts_cbranch_scc1 10b\n\ts_branch 26f\n\t"
      "16:\n\ts_nop 11\n\tv_cvt_pk_bf16_f32 v80, v48, v52\n\tv_cvt_pk_bf16_f32 v81, v56, v60\n\tv_cvt_pk_bf16_f32 v82, v49, v53\n\tv_cvt_pk_bf16_f32 v83, v57, v61\n\t"
      "v_cvt_pk_bf16_f32 v84, v50, v54\n\tv_cvt_pk_bf16_f32 v85, v58, v62\n\tv_cvt_pk_bf16_f32 v86, v51, v55\n\tv_cvt_pk_bf16_f32 v87, v59, v63\n\t"
      "buffer_store_dwordx4 v[80:83], v24, s[8:11], s19 offen offset:0 sc1\n\tbuffer_store_dwordx4 v[84:87], v24, s[8:11], s19 offen offset:512 sc1\n\tv_mov_b32 v28, 64\n\t"
      "s_waitcnt vmcnt(0) lgkmcnt(0)\n\ts_barrier\n\tbuffer_store_dword v28, v27, s[12:15], s22 offen sc1\n\ts_branch 30f\n\t"
      "24:\n\tbuffer_load_dword v89, off, s[12:15], 0 offset:3840 sc1\n\ts_waitcnt vmcnt(0)\n\tv_readfirstlane_b32 s23, v89\n\ts_add_u32 s23, s23, 15\n\t"
      "s_cmp_le_u32 s17, s23\n\ts_cbranch_scc1 14b\n\ts_cmp_eq_u32 s18, 0\n\ts_cbranch_scc1 14b\n\ts_sub_u32 s18, s18, 1\n\ts_sleep 8\n\ts_branch 24b\n\t"
      "25:\n\tbuffer_load_dword v89, off, s[12:15], 0 offset:3840 sc1\n\ts_waitcnt vmcnt(0)\n\tv_readfirstlane_b32 s23, v89\n\ts_add_u32 s23, s23, 15\n\t"
      "s_cmp_le_u32 s17, s23\n\ts_cbranch_scc1 15b\n\ts_cmp_eq_u32 s18, 0\n\ts_cbranch_scc1 15b\n\ts_sub_u32 s18, s18, 1\n\ts_sleep 8\n\ts_branch 25b\n\t"
      "26:\n\tbuffer_load_dword v89, off, s[12:15], 0 offset:3840 sc1\n\ts_waitcnt vmcnt(0)\n\tv_readfirstlane_b32 s23, v89\n\ts_add_u32 s23, s23, 15\n\t"
      "s_cmp_le_u32 s17, s23\n\ts_cbranch_scc1 16b\n\ts_cmp_eq_u32 s18, 0\n\ts_cbranch_scc1 16b\n\ts_sub_u32 s18, s18, 1\n\ts_sleep 8\n\ts_branch 26b\n\t"
      "20:\n\t"
      "1:\n\tbuffer_load_dword v26, off, s[12:15], s63 sc1\n\ts_waitcnt vmcnt(0)\n\tv_readfirstlane_b32 s49, v26\n\ts_bitcmp1_b32 s49, 0\n\ts_cbranch_scc1 2f\n\t"
      "s_cmp_eq_u32 s18, 0\n\ts_cbranch_scc1 2f\n\ts_sub_u32 s18, s18, 1\n\ts_sleep 2\n\ts_branch 1b\n\t"
      "2:\n\ts_and_b32 s48, s49, 3\n\ts_cmp_lg_u32 s48, 3\n\ts_cbranch_scc1 4f\n\ts_cmp_eq_u32 s62, 0\n\ts_cbranch_scc1 4f\n\ts_waitcnt lgkmcnt(0)\n\ts_barrier\n\t"
      "s_mov_b64 s[0:1], s[60:61]\n\ts_mov_b32 s2, s62\n\ts_lshl_b32 s48, s16, 6\n\tv_mbcnt_lo_u32_b32 v0, -1, 0\n\tv_mbcnt_hi_u32_b32 v0, -1, v0\n\tv_add_u32 v0, s48, v0\n\t"
      "s_getpc_b64 s[4:5]\n\t"
      "3:\n\ts_sub_u32 s4, s4, 3b-mk_mega\n\ts_subb_u32 s5, s5, 0\n\ts_setpc_b64 s[4:5]\n\t"
      "4:\n\ts_lshl_b32 s49, s49, 11\n\ts_and_b32 s49, s49, 0x2000\n\ts_xor_b32 s19, s19, s49\n\ts_nop 11\n\tv_cvt_pk_bf16_f32 v80, v32, v36\n\t"
      "v_cvt_pk_bf16_f32 v81, v40, v44\n\tv_cvt_pk_bf16_f32 v82, v33, v37\n\tv_cvt_pk_bf16_f32 v83, v41, v45\n\tv_cvt_pk_bf16_f32 v84, v34, v38\n\t"
      "v_cvt_pk_bf16_f32 v85, v42, v46\n\tv_cvt_pk_bf16_f32 v86, v35, v39\n\tv_cvt_pk_bf16_f32 v87, v43, v47\n\t"
      "buffer_store_dwordx4 v[80:83], v24, s[8:11], s19 offen offset:0 sc1\n\tbuffer_store_dwordx4 v[84:87], v24, s[8:11], s19 offen offset:512 sc1\n\ts_mov_b32 s23, 0\n\t"
      "v_mov_b32 v27, v88\n\tv_mov_b32 v28, 1\n\ts_waitcnt vmcnt(0)\n\ts_barrier\n\tbuffer_store_dword v28, v27, s[12:15], s22 offen sc1\n\ts_branch 11b\n\t"
      "30:\n\ts_waitcnt vmcnt(0) lgkmcnt(0)\n\t"
      TKS("s_mul_i32 s48, s16, 72\n\ts_add_u32 s48, s48, 162080\n\tv_mov_b32 v224, s48\n\tds_read_b32 v225, v224 offset:0\n\ts_waitcnt lgkmcnt(0)\n\tv_readfirstlane_b32 s72, v225\n\tds_read_b32 v225, v224 offset:4\n\ts_waitcnt lgkmcnt(0)\n\tv_readfirstlane_b32 s73, v225\n\tds_read_b32 v225, v224 offset:8\n\ts_waitcnt lgkmcnt(0)\n\tv_readfirstlane_b32 s74, v225\n\tds_read_b32 v225, v224 offset:12\n\ts_waitcnt lgkmcnt(0)\n\tv_readfirstlane_b32 s75, v225\n\tds_read_b32 v225, v224 offset:16\n\ts_waitcnt lgkmcnt(0)\n\tv_readfirstlane_b32 s76, v225\n\tds_read_b32 v225, v224 offset:20\n\ts_waitcnt lgkmcnt(0)\n\tv_readfirstlane_b32 s77, v225\n\tds_read_b32 v225, v224 offset:24\n\ts_waitcnt lgkmcnt(0)\n\tv_readfirstlane_b32 s78, v225\n\tds_read_b32 v225, v224 offset:28\n\ts_waitcnt lgkmcnt(0)\n\tv_readfirstlane_b32 s79, v225\n\tds_read_b32 v225, v224 offset:32\n\ts_waitcnt lgkmcnt(0)\n\tv_readfirstlane_b32 s80, v225\n\tds_read_b32 v225, v224 offset:36\n\ts_waitcnt lgkmcnt(0)\n\tv_readfirstlane_b32 s81, v225\n\tds_read_b32 v225, v224 offset:40\n\ts_waitcnt lgkmcnt(0)\n\tv_readfirstlane_b32 s82, v225\n\tds_read_b32 v225, v224 offset:44\n\ts_waitcnt lgkmcnt(0)\n\tv_readfirstlane_b32 s83, v225\n\tds_read_b32 v225, v224 offset:48\n\ts_waitcnt lgkmcnt(0)\n\tv_readfirstlane_b32 s84, v225\n\tds_read_b32 v225, v224 offset:52\n\ts_waitcnt lgkmcnt(0)\n\tv_readfirstlane_b32 s85, v225\n\tds_read_b32 v225, v224 offset:56\n\ts_waitcnt lgkmcnt(0)\n\tv_readfirstlane_b32 s86, v225\n\tds_read_b32 v225, v224 offset:60\n\ts_waitcnt lgkmcnt(0)\n\tv_readfirstlane_b32 s87, v225\n\tds_read_b32 v225, v224 offset:64\n\ts_waitcnt lgkmcnt(0)\n\tv_readfirstlane_b32 s88, v225\n\tds_read_b32 v225, v224 offset:68\n\ts_waitcnt lgkmcnt(0)\n\tv_readfirstlane_b32 s89, v225\n\t")
      STS("s_lshl_b32 s48, s16, 2\n\ts_add_u32 s48, s48, 162944\n\tv_mov_b32 v0, s48\n\tv_mov_b32 v1, s50\n\tds_write_b32 v0, v1\n\ts_cmp_lg_u32 s16, 0\n\ts_cbranch_scc1 97f\n\tv_mov_b32 v1, s51\n\tds_write_b32 v0, v1 offset:16\n\t97:\n\ts_cmp_lg_u32 s16, 0\n\ts_cbranch_scc1 95f\n\tv_mov_b32 v1, s61\n\tds_write_b32 v0, v1 offset:20\n\tv_mov_b32 v1, s21\n\tds_write_b32 v0, v1 offset:24\n\tv_mov_b32 v1, s22\n\tds_write_b32 v0, v1 offset:28\n\t95:\n\t")
      "s_cmp_lg_u32 s18, 0\n\ts_cbranch_scc1 96f\n\tv_mov_b32 v1, 1\n\tbuffer_store_dword v1, off, s[12:15], 0 offset:3328 sc1\n\t"
      "96:\n\t"
      : "+{v[0:31]}"(k), "+{v[160:191]}"(s0), "+{v[192:223]}"(s1), ALL_REGS :: "memory", "vcc", "scc", S_CLOB TK_CLOB);
}

DEV void zc_role(Ctx& c, int h) {
  const KArg p = enter(c);
  c.budget = SPIN_CAP;
  const int w = c.w, lane = fresh_lane(), g = lane >> 4, h2 = lane >> 5, q = (lane >> 2) & 3, pp = lane & 3;
  v32u k;
#pragma unroll
  for (int i = 0; i < 32; ++i) k[i] = 0;
  k[0] = lane * 16;
  k[1] = (16 * w + (g & 1)) * 65536 + amap(h * 1024, w >> 1) + h2 * 256 + (lane & 15) * 16 + ((HACK & 2) << 29);
  k[6] = lane == 0 ? C_FLAG + (F_V + h) * 128 : C_PROG;
  k[3] = lane ? 1 << 30 : 0;
#pragma unroll
  for (int i = 0; i < 2; ++i) k[8 + i] = (h * 128 + 16 * h2) * 2048 + w * 512 + (((lane & 31) ^ ((2 * i + h2) << 2)) << 4);
#pragma unroll
  for (int i = 0; i < 4; ++i) k[10 + i] = w * 32768 + (8 * h2 + q) * 512 + (((4 * i + 2 * (g & 1) + (pp >> 1)) ^ (q << 2)) << 4) + 8 * (pp & 1);
  asm volatile(
      "s_add_u32 m0, s51, 0\n\ts_mov_b32 s48, 0\n\tbuffer_load_dwordx4 v8, s[40:43], s48 offen lds\n\ts_add_u32 m0, s51, 1024\n\ts_mov_b32 s48, 65536\n\t"
      "buffer_load_dwordx4 v9, s[40:43], s48 offen lds\n\ts_add_u32 m0, s51, 2048\n\ts_mov_b32 s48, 2048\n\tbuffer_load_dwordx4 v8, s[40:43], s48 offen lds\n\t"
      "s_add_u32 m0, s51, 3072\n\ts_mov_b32 s48, 67584\n\tbuffer_load_dwordx4 v9, s[40:43], s48 offen lds\n\ts_add_u32 m0, s51, 4096\n\ts_mov_b32 s48, 4096\n\t"
      "buffer_load_dwordx4 v8, s[40:43], s48 offen lds\n\ts_add_u32 m0, s51, 5120\n\ts_mov_b32 s48, 69632\n\tbuffer_load_dwordx4 v9, s[40:43], s48 offen lds\n\t"
      "s_add_u32 m0, s51, 6144\n\ts_mov_b32 s48, 6144\n\tbuffer_load_dwordx4 v8, s[40:43], s48 offen lds\n\ts_add_u32 m0, s51, 7168\n\ts_mov_b32 s48, 71680\n\t"
      "buffer_load_dwordx4 v9, s[40:43], s48 offen lds\n\ts_add_u32 m0, s51, 8192\n\ts_mov_b32 s48, 8192\n\tbuffer_load_dwordx4 v8, s[40:43], s48 offen lds\n\t"
      "s_add_u32 m0, s51, 9216\n\ts_mov_b32 s48, 73728\n\tbuffer_load_dwordx4 v9, s[40:43], s48 offen lds\n\ts_add_u32 m0, s51, 10240\n\ts_mov_b32 s48, 10240\n\t"
      "buffer_load_dwordx4 v8, s[40:43], s48 offen lds\n\ts_add_u32 m0, s51, 11264\n\ts_mov_b32 s48, 75776\n\tbuffer_load_dwordx4 v9, s[40:43], s48 offen lds\n\t"
      "s_add_u32 m0, s51, 12288\n\ts_mov_b32 s48, 12288\n\tbuffer_load_dwordx4 v8, s[40:43], s48 offen lds\n\ts_add_u32 m0, s51, 13312\n\ts_mov_b32 s48, 77824\n\t"
      "buffer_load_dwordx4 v9, s[40:43], s48 offen lds\n\ts_add_u32 m0, s51, 14336\n\ts_mov_b32 s48, 14336\n\tbuffer_load_dwordx4 v8, s[40:43], s48 offen lds\n\t"
      "s_add_u32 m0, s51, 15360\n\ts_mov_b32 s48, 79872\n\tbuffer_load_dwordx4 v9, s[40:43], s48 offen lds\n\ts_add_u32 m0, s51, 16384\n\ts_mov_b32 s48, 16384\n\t"
      "buffer_load_dwordx4 v8, s[40:43], s48 offen lds\n\ts_add_u32 m0, s51, 17408\n\ts_mov_b32 s48, 81920\n\tbuffer_load_dwordx4 v9, s[40:43], s48 offen lds\n\t"
      "s_add_u32 m0, s51, 18432\n\ts_mov_b32 s48, 18432\n\tbuffer_load_dwordx4 v8, s[40:43], s48 offen lds\n\ts_add_u32 m0, s51, 19456\n\ts_mov_b32 s48, 83968\n\t"
      "buffer_load_dwordx4 v9, s[40:43], s48 offen lds\n\ts_add_u32 m0, s51, 20480\n\ts_mov_b32 s48, 20480\n\tbuffer_load_dwordx4 v8, s[40:43], s48 offen lds\n\t"
      "s_add_u32 m0, s51, 21504\n\ts_mov_b32 s48, 86016\n\tbuffer_load_dwordx4 v9, s[40:43], s48 offen lds\n\ts_add_u32 m0, s51, 22528\n\ts_mov_b32 s48, 22528\n\t"
      "buffer_load_dwordx4 v8, s[40:43], s48 offen lds\n\ts_add_u32 m0, s51, 23552\n\ts_mov_b32 s48, 88064\n\tbuffer_load_dwordx4 v9, s[40:43], s48 offen lds\n\t"
      "s_add_u32 m0, s51, 24576\n\ts_mov_b32 s48, 24576\n\tbuffer_load_dwordx4 v8, s[40:43], s48 offen lds\n\ts_add_u32 m0, s51, 25600\n\ts_mov_b32 s48, 90112\n\t"
      "buffer_load_dwordx4 v9, s[40:43], s48 offen lds\n\ts_add_u32 m0, s51, 26624\n\ts_mov_b32 s48, 26624\n\tbuffer_load_dwordx4 v8, s[40:43], s48 offen lds\n\t"
      "s_add_u32 m0, s51, 27648\n\ts_mov_b32 s48, 92160\n\tbuffer_load_dwordx4 v9, s[40:43], s48 offen lds\n\ts_add_u32 m0, s51, 28672\n\ts_mov_b32 s48, 28672\n\t"
      "buffer_load_dwordx4 v8, s[40:43], s48 offen lds\n\ts_add_u32 m0, s51, 29696\n\ts_mov_b32 s48, 94208\n\tbuffer_load_dwordx4 v9, s[40:43], s48 offen lds\n\t"
      "s_add_u32 m0, s51, 30720\n\ts_mov_b32 s48, 30720\n\tbuffer_load_dwordx4 v8, s[40:43], s48 offen lds\n\ts_add_u32 m0, s51, 31744\n\ts_mov_b32 s48, 96256\n\t"
      "buffer_load_dwordx4 v9, s[40:43], s48 offen lds\n\t"
      :: "{v[8:9]}"(v2u{k[8], k[9]}), "{s[40:43]}"(mkrs(p->Wo, 1024 * 4096 * 2)), "{s51}"(w * 32768) : "memory", "scc", "s48");
  claim(c, 0);
  v32u s0 = {RSRCS, (unsigned)w, 0, c.budget, (unsigned)(R_KX + (hblock(h) ^ 8192)), R_Z, (unsigned)(C_FLAG + (F_V + h) * 128), (unsigned)(C_FLAG + (F_Z + w * 32 + h) * 128)};
  v32u s1 = {plo(p->Wo), phi(p->Wo), 1024 * 4096 * 2, 0x00020000, 0, 0, 0, 0, 0, 0, 0, (unsigned)(w * 32768), 0, 0, 0, 0, 0, 0, 0, 0, ENTRY};
  v32u wa[8], wv[5];
  asm volatile(
      "s_nop 0\n\tv_readfirstlane_b32 s8, v160\n\tv_readfirstlane_b32 s9, v161\n\tv_readfirstlane_b32 s10, v162\n\tv_readfirstlane_b32 s11, v163\n\t"
      "v_readfirstlane_b32 s12, v164\n\tv_readfirstlane_b32 s13, v165\n\tv_readfirstlane_b32 s14, v166\n\tv_readfirstlane_b32 s15, v167\n\tv_readfirstlane_b32 s16, v168\n\t"
      "v_readfirstlane_b32 s17, v169\n\tv_readfirstlane_b32 s18, v170\n\tv_readfirstlane_b32 s19, v171\n\tv_readfirstlane_b32 s20, v172\n\tv_readfirstlane_b32 s21, v173\n\t"
      "v_readfirstlane_b32 s22, v174\n\tv_readfirstlane_b32 s40, v192\n\tv_readfirstlane_b32 s41, v193\n\tv_readfirstlane_b32 s42, v194\n\tv_readfirstlane_b32 s43, v195\n\t"
      "v_readfirstlane_b32 s51, v203\n\tv_readfirstlane_b32 s60, v212\n\tv_readfirstlane_b32 s61, v213\n\tv_readfirstlane_b32 s62, v214\n\tv_readfirstlane_b32 s63, v215\n\t"
      "s_nop 4\n\t"
      TKS("s_mul_i32 s48, s16, 72\n\ts_add_u32 s48, s48, 162080\n\tv_mov_b32 v224, s48\n\tv_mov_b32 v225, s72\n\tds_write_b32 v224, v225 offset:0\n\tv_mov_b32 v225, s73\n\tds_write_b32 v224, v225 offset:4\n\tv_mov_b32 v225, s74\n\tds_write_b32 v224, v225 offset:8\n\tv_mov_b32 v225, s75\n\tds_write_b32 v224, v225 offset:12\n\tv_mov_b32 v225, s76\n\tds_write_b32 v224, v225 offset:16\n\tv_mov_b32 v225, s77\n\tds_write_b32 v224, v225 offset:20\n\tv_mov_b32 v225, s78\n\tds_write_b32 v224, v225 offset:24\n\tv_mov_b32 v225, s79\n\tds_write_b32 v224, v225 offset:28\n\tv_mov_b32 v225, s80\n\tds_write_b32 v224, v225 offset:32\n\tv_mov_b32 v225, s81\n\tds_write_b32 v224, v225 offset:36\n\tv_mov_b32 v225, s82\n\tds_write_b32 v224, v225 offset:40\n\tv_mov_b32 v225, s83\n\tds_write_b32 v224, v225 offset:44\n\tv_mov_b32 v225, s84\n\tds_write_b32 v224, v225 offset:48\n\tv_mov_b32 v225, s85\n\tds_write_b32 v224, v225 offset:52\n\tv_mov_b32 v225, s86\n\tds_write_b32 v224, v225 offset:56\n\tv_mov_b32 v225, s87\n\tds_write_b32 v224, v225 offset:60\n\tv_mov_b32 v225, s88\n\tds_write_b32 v224, v225 offset:64\n\tv_mov_b32 v225, s89\n\tds_write_b32 v224, v225 offset:68\n\t")
      "s_waitcnt vmcnt(24)\n\tds_read_b64_tr_b16 a[0:1], v10 offset:0\n\tds_read_b64_tr_b16 a[2:3], v10 offset:2048\n\tds_read_b64_tr_b16 a[32:33], v11 offset:0\n\t"
      "ds_read_b64_tr_b16 a[34:35], v11 offset:2048\n\tds_read_b64_tr_b16 a[64:65], v12 offset:0\n\tds_read_b64_tr_b16 a[66:67], v12 offset:2048\n\t"
      "ds_read_b64_tr_b16 a[96:97], v13 offset:0\n\tds_read_b64_tr_b16 a[98:99], v13 offset:2048\n\tds_read_b64_tr_b16 a[128:129], v10 offset:256\n\t"
      "ds_read_b64_tr_b16 a[130:131], v10 offset:2304\n\tds_read_b64_tr_b16 a[160:161], v11 offset:256\n\tds_read_b64_tr_b16 a[162:163], v11 offset:2304\n\t"
      "ds_read_b64_tr_b16 a[192:193], v12 offset:256\n\tds_read_b64_tr_b16 a[194:195], v12 offset:2304\n\tds_read_b64_tr_b16 a[224:225], v13 offset:256\n\t"
      "ds_read_b64_tr_b16 a[226:227], v13 offset:2304\n\ts_waitcnt lgkmcnt(0)\n\ts_add_u32 m0, s51, 0\n\ts_mov_b32 s48, 131072\n\t"
      "buffer_load_dwordx4 v8, s[40:43], s48 offen lds\n\ts_add_u32 m0, s51, 1024\n\ts_mov_b32 s48, 196608\n\tbuffer_load_dwordx4 v9, s[40:43], s48 offen lds\n\t"
      "s_add_u32 m0, s51, 2048\n\ts_mov_b32 s48, 133120\n\tbuffer_load_dwordx4 v8, s[40:43], s48 offen lds\n\ts_add_u32 m0, s51, 3072\n\ts_mov_b32 s48, 198656\n\t"
      "buffer_load_dwordx4 v9, s[40:43], s48 offen lds\n\ts_add_u32 m0, s51, 4096\n\ts_mov_b32 s48, 135168\n\tbuffer_load_dwordx4 v8, s[40:43], s48 offen lds\n\t"
      "s_add_u32 m0, s51, 5120\n\ts_mov_b32 s48, 200704\n\tbuffer_load_dwordx4 v9, s[40:43], s48 offen lds\n\ts_add_u32 m0, s51, 6144\n\ts_mov_b32 s48, 137216\n\t"
      "buffer_load_dwordx4 v8, s[40:43], s48 offen lds\n\ts_add_u32 m0, s51, 7168\n\ts_mov_b32 s48, 202752\n\tbuffer_load_dwordx4 v9, s[40:43], s48 offen lds\n\t"
      "s_waitcnt vmcnt(24)\n\tds_read_b64_tr_b16 a[4:5], v10 offset:8192\n\tds_read_b64_tr_b16 a[6:7], v10 offset:10240\n\tds_read_b64_tr_b16 a[36:37], v11 offset:8192\n\t"
      "ds_read_b64_tr_b16 a[38:39], v11 offset:10240\n\tds_read_b64_tr_b16 a[68:69], v12 offset:8192\n\tds_read_b64_tr_b16 a[70:71], v12 offset:10240\n\t"
      "ds_read_b64_tr_b16 a[100:101], v13 offset:8192\n\tds_read_b64_tr_b16 a[102:103], v13 offset:10240\n\tds_read_b64_tr_b16 a[132:133], v10 offset:8448\n\t"
      "ds_read_b64_tr_b16 a[134:135], v10 offset:10496\n\tds_read_b64_tr_b16 a[164:165], v11 offset:8448\n\tds_read_b64_tr_b16 a[166:167], v11 offset:10496\n\t"
      "ds_read_b64_tr_b16 a[196:197], v12 offset:8448\n\tds_read_b64_tr_b16 a[198:199], v12 offset:10496\n\tds_read_b64_tr_b16 a[228:229], v13 offset:8448\n\t"
      "ds_read_b64_tr_b16 a[230:231], v13 offset:10496\n\ts_waitcnt lgkmcnt(0)\n\ts_add_u32 m0, s51, 8192\n\ts_mov_b32 s48, 139264\n\t"
      "buffer_load_dwordx4 v8, s[40:43], s48 offen lds\n\ts_add_u32 m0, s51, 9216\n\ts_mov_b32 s48, 204800\n\tbuffer_load_dwordx4 v9, s[40:43], s48 offen lds\n\t"
      "s_add_u32 m0, s51, 10240\n\ts_mov_b32 s48, 141312\n\tbuffer_load_dwordx4 v8, s[40:43], s48 offen lds\n\ts_add_u32 m0, s51, 11264\n\ts_mov_b32 s48, 206848\n\t"
      "buffer_load_dwordx4 v9, s[40:43], s48 offen lds\n\ts_add_u32 m0, s51, 12288\n\ts_mov_b32 s48, 143360\n\tbuffer_load_dwordx4 v8, s[40:43], s48 offen lds\n\t"
      "s_add_u32 m0, s51, 13312\n\ts_mov_b32 s48, 208896\n\tbuffer_load_dwordx4 v9, s[40:43], s48 offen lds\n\ts_add_u32 m0, s51, 14336\n\ts_mov_b32 s48, 145408\n\t"
      "buffer_load_dwordx4 v8, s[40:43], s48 offen lds\n\ts_add_u32 m0, s51, 15360\n\ts_mov_b32 s48, 210944\n\tbuffer_load_dwordx4 v9, s[40:43], s48 offen lds\n\t"
      "s_waitcnt vmcnt(24)\n\tds_read_b64_tr_b16 a[8:9], v10 offset:16384\n\tds_read_b64_tr_b16 a[10:11], v10 offset:18432\n\tds_read_b64_tr_b16 a[40:41], v11 offset:16384\n\t"
      "ds_read_b64_tr_b16 a[42:43], v11 offset:18432\n\tds_read_b64_tr_b16 a[72:73], v12 offset:16384\n\tds_read_b64_tr_b16 a[74:75], v12 offset:18432\n\t"
      "ds_read_b64_tr_b16 a[104:105], v13 offset:16384\n\tds_read_b64_tr_b16 a[106:107], v13 offset:18432\n\tds_read_b64_tr_b16 a[136:137], v10 offset:16640\n\t"
      "ds_read_b64_tr_b16 a[138:139], v10 offset:18688\n\tds_read_b64_tr_b16 a[168:169], v11 offset:16640\n\tds_read_b64_tr_b16 a[170:171], v11 offset:18688\n\t"
      "ds_read_b64_tr_b16 a[200:201], v12 offset:16640\n\tds_read_b64_tr_b16 a[202:203], v12 offset:18688\n\tds_read_b64_tr_b16 a[232:233], v13 offset:16640\n\t"
      "ds_read_b64_tr_b16 a[234:235], v13 offset:18688\n\ts_waitcnt lgkmcnt(0)\n\ts_add_u32 m0, s51, 16384\n\ts_mov_b32 s48, 147456\n\t"
      "buffer_load_dwordx4 v8, s[40:43], s48 offen lds\n\ts_add_u32 m0, s51, 17408\n\ts_mov_b32 s48, 212992\n\tbuffer_load_dwordx4 v9, s[40:43], s48 offen lds\n\t"
      "s_add_u32 m0, s51, 18432\n\ts_mov_b32 s48, 149504\n\tbuffer_load_dwordx4 v8, s[40:43], s48 offen lds\n\ts_add_u32 m0, s51, 19456\n\ts_mov_b32 s48, 215040\n\t"
      "buffer_load_dwordx4 v9, s[40:43], s48 offen lds\n\ts_add_u32 m0, s51, 20480\n\ts_mov_b32 s48, 151552\n\tbuffer_load_dwordx4 v8, s[40:43], s48 offen lds\n\t"
      "s_add_u32 m0, s51, 21504\n\ts_mov_b32 s48, 217088\n\tbuffer_load_dwordx4 v9, s[40:43], s48 offen lds\n\ts_add_u32 m0, s51, 22528\n\ts_mov_b32 s48, 153600\n\t"
      "buffer_load_dwordx4 v8, s[40:43], s48 offen lds\n\ts_add_u32 m0, s51, 23552\n\ts_mov_b32 s48, 219136\n\tbuffer_load_dwordx4 v9, s[40:43], s48 offen lds\n\t"
      "s_waitcnt vmcnt(24)\n\tds_read_b64_tr_b16 a[12:13], v10 offset:24576\n\tds_read_b64_tr_b16 a[14:15], v10 offset:26624\n\tds_read_b64_tr_b16 a[44:45], v11 offset:24576\n\t"
      "ds_read_b64_tr_b16 a[46:47], v11 offset:26624\n\tds_read_b64_tr_b16 a[76:77], v12 offset:24576\n\tds_read_b64_tr_b16 a[78:79], v12 offset:26624\n\t"
      "ds_read_b64_tr_b16 a[108:109], v13 offset:24576\n\tds_read_b64_tr_b16 a[110:111], v13 offset:26624\n\tds_read_b64_tr_b16 a[140:141], v10 offset:24832\n\t"
      "ds_read_b64_tr_b16 a[142:143], v10 offset:26880\n\tds_read_b64_tr_b16 a[172:173], v11 offset:24832\n\tds_read_b64_tr_b16 a[174:175], v11 offset:26880\n\t"
      "ds_read_b64_tr_b16 a[204:205], v12 offset:24832\n\tds_read_b64_tr_b16 a[206:207], v12 offset:26880\n\tds_read_b64_tr_b16 a[236:237], v13 offset:24832\n\t"
      "ds_read_b64_tr_b16 a[238:239], v13 offset:26880\n\ts_waitcnt lgkmcnt(0)\n\ts_add_u32 m0, s51, 24576\n\ts_mov_b32 s48, 155648\n\t"
      "buffer_load_dwordx4 v8, s[40:43], s48 offen lds\n\ts_add_u32 m0, s51, 25600\n\ts_mov_b32 s48, 221184\n\tbuffer_load_dwordx4 v9, s[40:43], s48 offen lds\n\t"
      "s_add_u32 m0, s51, 26624\n\ts_mov_b32 s48, 157696\n\tbuffer_load_dwordx4 v8, s[40:43], s48 offen lds\n\ts_add_u32 m0, s51, 27648\n\ts_mov_b32 s48, 223232\n\t"
      "buffer_load_dwordx4 v9, s[40:43], s48 offen lds\n\ts_add_u32 m0, s51, 28672\n\ts_mov_b32 s48, 159744\n\tbuffer_load_dwordx4 v8, s[40:43], s48 offen lds\n\t"
      "s_add_u32 m0, s51, 29696\n\ts_mov_b32 s48, 225280\n\tbuffer_load_dwordx4 v9, s[40:43], s48 offen lds\n\ts_add_u32 m0, s51, 30720\n\ts_mov_b32 s48, 161792\n\t"
      "buffer_load_dwordx4 v8, s[40:43], s48 offen lds\n\ts_add_u32 m0, s51, 31744\n\ts_mov_b32 s48, 227328\n\tbuffer_load_dwordx4 v9, s[40:43], s48 offen lds\n\t"
      "s_waitcnt vmcnt(24)\n\tds_read_b64_tr_b16 a[16:17], v10 offset:0\n\tds_read_b64_tr_b16 a[18:19], v10 offset:2048\n\tds_read_b64_tr_b16 a[48:49], v11 offset:0\n\t"
      "ds_read_b64_tr_b16 a[50:51], v11 offset:2048\n\tds_read_b64_tr_b16 a[80:81], v12 offset:0\n\tds_read_b64_tr_b16 a[82:83], v12 offset:2048\n\t"
      "ds_read_b64_tr_b16 a[112:113], v13 offset:0\n\tds_read_b64_tr_b16 a[114:115], v13 offset:2048\n\tds_read_b64_tr_b16 a[144:145], v10 offset:256\n\t"
      "ds_read_b64_tr_b16 a[146:147], v10 offset:2304\n\tds_read_b64_tr_b16 a[176:177], v11 offset:256\n\tds_read_b64_tr_b16 a[178:179], v11 offset:2304\n\t"
      "ds_read_b64_tr_b16 a[208:209], v12 offset:256\n\tds_read_b64_tr_b16 a[210:211], v12 offset:2304\n\tds_read_b64_tr_b16 a[240:241], v13 offset:256\n\t"
      "ds_read_b64_tr_b16 a[242:243], v13 offset:2304\n\ts_waitcnt lgkmcnt(0)\n\ts_waitcnt vmcnt(16)\n\tds_read_b64_tr_b16 a[20:21], v10 offset:8192\n\t"
      "ds_read_b64_tr_b16 a[22:23], v10 offset:10240\n\tds_read_b64_tr_b16 a[52:53], v11 offset:8192\n\tds_read_b64_tr_b16 a[54:55], v11 offset:10240\n\t"
      "ds_read_b64_tr_b16 a[84:85], v12 offset:8192\n\tds_read_b64_tr_b16 a[86:87], v12 offset:10240\n\tds_read_b64_tr_b16 a[116:117], v13 offset:8192\n\t"
      "ds_read_b64_tr_b16 a[118:119], v13 offset:10240\n\tds_read_b64_tr_b16 a[148:149], v10 offset:8448\n\tds_read_b64_tr_b16 a[150:151], v10 offset:10496\n\t"
      "ds_read_b64_tr_b16 a[180:181], v11 offset:8448\n\tds_read_b64_tr_b16 a[182:183], v11 offset:10496\n\tds_read_b64_tr_b16 a[212:213], v12 offset:8448\n\t"
      "ds_read_b64_tr_b16 a[214:215], v12 offset:10496\n\tds_read_b64_tr_b16 a[244:245], v13 offset:8448\n\tds_read_b64_tr_b16 a[246:247], v13 offset:10496\n\t"
      "s_waitcnt lgkmcnt(0)\n\ts_waitcnt vmcnt(8)\n\tds_read_b64_tr_b16 a[24:25], v10 offset:16384\n\tds_read_b64_tr_b16 a[26:27], v10 offset:18432\n\t"
      "ds_read_b64_tr_b16 a[56:57], v11 offset:16384\n\tds_read_b64_tr_b16 a[58:59], v11 offset:18432\n\tds_read_b64_tr_b16 a[88:89], v12 offset:16384\n\t"
      "ds_read_b64_tr_b16 a[90:91], v12 offset:18432\n\tds_read_b64_tr_b16 a[120:121], v13 offset:16384\n\tds_read_b64_tr_b16 a[122:123], v13 offset:18432\n\t"
      "ds_read_b64_tr_b16 a[152:153], v10 offset:16640\n\tds_read_b64_tr_b16 a[154:155], v10 offset:18688\n\tds_read_b64_tr_b16 a[184:185], v11 offset:16640\n\t"
      "ds_read_b64_tr_b16 a[186:187], v11 offset:18688\n\tds_read_b64_tr_b16 a[216:217], v12 offset:16640\n\tds_read_b64_tr_b16 a[218:219], v12 offset:18688\n\t"
      "ds_read_b64_tr_b16 a[248:249], v13 offset:16640\n\tds_read_b64_tr_b16 a[250:251], v13 offset:18688\n\ts_waitcnt lgkmcnt(0)\n\ts_waitcnt vmcnt(0)\n\t"
      "ds_read_b64_tr_b16 a[28:29], v10 offset:24576\n\tds_read_b64_tr_b16 a[30:31], v10 offset:26624\n\tds_read_b64_tr_b16 a[60:61], v11 offset:24576\n\t"
      "ds_read_b64_tr_b16 a[62:63], v11 offset:26624\n\tds_read_b64_tr_b16 a[92:93], v12 offset:24576\n\tds_read_b64_tr_b16 a[94:95], v12 offset:26624\n\t"
      "ds_read_b64_tr_b16 a[124:125], v13 offset:24576\n\tds_read_b64_tr_b16 a[126:127], v13 offset:26624\n\tds_read_b64_tr_b16 a[156:157], v10 offset:24832\n\t"
      "ds_read_b64_tr_b16 a[158:159], v10 offset:26880\n\tds_read_b64_tr_b16 a[188:189], v11 offset:24832\n\tds_read_b64_tr_b16 a[190:191], v11 offset:26880\n\t"
      "ds_read_b64_tr_b16 a[220:221], v12 offset:24832\n\tds_read_b64_tr_b16 a[222:223], v12 offset:26880\n\tds_read_b64_tr_b16 a[252:253], v13 offset:24832\n\t"
      "ds_read_b64_tr_b16 a[254:255], v13 offset:26880\n\ts_waitcnt lgkmcnt(0)\n\t"
      STS("s_memrealtime vcc\n\ts_waitcnt lgkmcnt(0)\n\tv_mov_b32 v95, vcc_lo\n\tv_mov_b32 v94, 161024\n\tds_write_b32 v94, v95\n\ts_memtime vcc\n\ts_waitcnt lgkmcnt(0)\n\tv_mov_b32 v95, vcc_lo\n\tds_write_b32 v94, v95 offset:400\n\ts_waitcnt lgkmcnt(0)\n\t")
      "s_barrier\n\t"
      "1:\n\tbuffer_load_dword v5, off, s[12:15], s63 sc1\n\ts_waitcnt vmcnt(0)\n\tv_readfirstlane_b32 s49, v5\n\ts_bitcmp1_b32 s49, 0\n\ts_cbranch_scc1 2f\n\t"
      "s_cmp_eq_u32 s18, 0\n\ts_cbranch_scc1 2f\n\ts_sub_u32 s18, s18, 1\n\ts_sleep 2\n\ts_branch 1b\n\t"
      "2:\n\ts_and_b32 s48, s49, 3\n\ts_cmp_lg_u32 s48, 3\n\ts_cbranch_scc1 4f\n\ts_cmp_eq_u32 s62, 0\n\ts_cbranch_scc1 4f\n\ts_waitcnt lgkmcnt(0)\n\ts_barrier\n\t"
      "s_mov_b64 s[0:1], s[60:61]\n\ts_mov_b32 s2, s62\n\ts_lshl_b32 s48, s16, 6\n\tv_mbcnt_lo_u32_b32 v0, -1, 0\n\tv_mbcnt_hi_u32_b32 v0, -1, v0\n\tv_add_u32 v0, s48, v0\n\t"
      "s_getpc_b64 s[4:5]\n\t"
      "3:\n\ts_sub_u32 s4, s4, 3b-mk_mega\n\ts_subb_u32 s5, s5, 0\n\ts_setpc_b64 s[4:5]\n\t"
      "4:\n\ts_lshl_b32 s49, s49, 11\n\ts_and_b32 s49, s49, 0x2000\n\ts_xor_b32 s19, s19, s49\n\tv_xor_b32 v1, s49, v1\n\ts_mov_b64 s[56:57], s[8:9]\n\ts_mov_b32 s59, s11\n\t"
      "s_mov_b32 s23, 0\n\ts_mov_b32 s25, 0\n\tv_mov_b32 v2, 0x40000000\n\ts_branch 43f\n\t"
      "10:\n\tv_mfma_f32_32x32x16_bf16 v[32:47], v[96:99], a[0:3], 0\n\t"
      "s_add_u32 s48, s17, 1\n\ts_cmp_gt_u32 s23, s48\n\ts_cselect_b32 s58, s10, 0\n\ts_nop 1\n\tv_mfma_f32_32x32x16_bf16 v[32:47], v[100:103], a[4:7], v[32:47]\n\t"
      "s_add_u32 s48, s19, 0x4000\n\ts_and_b32 s48, s48, 0x3c000\n\ts_andn2_b32 s19, s19, 0x3c000\n\ts_or_b32 s19, s19, s48\n\ts_add_u32 s24, s19, 0x1000\n\t"
      "v_mfma_f32_32x32x16_bf16 v[32:47], v[104:107], a[8:11], v[32:47]\n\tv_cvt_pk_bf16_f32 v80, v48, v49\n\tv_cvt_pk_bf16_f32 v81, v50, v51\n\t"
      "v_cvt_pk_bf16_f32 v82, v56, v57\n\tv_cvt_pk_bf16_f32 v83, v58, v59\n\tv_mfma_f32_32x32x16_bf16 v[32:47], v[108:111], a[12:15], v[32:47]\n\t"
      "buffer_store_dwordx4 v[80:83], v2, s[8:11], s25 offen offset:0 sc1\n\tv_mfma_f32_32x32x16_bf16 v[32:47], v[112:115], a[16:19], v[32:47]\n\t"
      "v_cvt_pk_bf16_f32 v84, v52, v53\n\tv_cvt_pk_bf16_f32 v85, v54, v55\n\tv_cvt_pk_bf16_f32 v86, v60, v61\n\tv_cvt_pk_bf16_f32 v87, v62, v63\n\t"
      "v_mfma_f32_32x32x16_bf16 v[32:47], v[116:119], a[20:23], v[32:47]\n\tbuffer_store_dwordx4 v[84:87], v2, s[8:11], s25 offen offset:512 sc1\n\t"
      "v_mfma_f32_32x32x16_bf16 v[32:47], v[120:123], a[24:27], v[32:47]\n\ts_add_u32 s25, s20, 0x0\n\tbuffer_load_dwordx4 v[128:131], v0, s[56:59], s19 offen offset:0 sc1\n\t"
      "v_mfma_f32_32x32x16_bf16 v[32:47], v[124:127], a[28:31], v[32:47]\n\tbuffer_load_dwordx4 v[132:135], v0, s[56:59], s19 offen offset:1024 sc1\n\tv_mov_b32 v4, s17\n\t"
      "v_mov_b32 v2, v1\n\tv_mfma_f32_32x32x16_bf16 v[48:63], v[96:99], a[32:35], 0\n\ts_nop 4\n\tv_mfma_f32_32x32x16_bf16 v[48:63], v[100:103], a[36:39], v[48:63]\n\t"
      "s_nop 4\n\tv_mfma_f32_32x32x16_bf16 v[48:63], v[104:107], a[40:43], v[48:63]\n\tv_cvt_pk_bf16_f32 v80, v32, v33\n\tv_cvt_pk_bf16_f32 v81, v34, v35\n\t"
      "v_cvt_pk_bf16_f32 v82, v40, v41\n\tv_cvt_pk_bf16_f32 v83, v42, v43\n\tv_mfma_f32_32x32x16_bf16 v[48:63], v[108:111], a[44:47], v[48:63]\n\t"
      "buffer_store_dwordx4 v[80:83], v1, s[8:11], s25 offen offset:0 sc1\n\tv_mfma_f32_32x32x16_bf16 v[48:63], v[112:115], a[48:51], v[48:63]\n\t"
      "v_cvt_pk_bf16_f32 v84, v36, v37\n\tv_cvt_pk_bf16_f32 v85, v38, v39\n\tv_cvt_pk_bf16_f32 v86, v44, v45\n\tv_cvt_pk_bf16_f32 v87, v46, v47\n\t"
      "v_mfma_f32_32x32x16_bf16 v[48:63], v[116:119], a[52:55], v[48:63]\n\tbuffer_store_dwordx4 v[84:87], v1, s[8:11], s25 offen offset:512 sc1\n\t"
      "v_mfma_f32_32x32x16_bf16 v[48:63], v[120:123], a[56:59], v[48:63]\n\ts_add_u32 s25, s20, 0x20000\n\t"
      "buffer_load_dwordx4 v[136:139], v0, s[56:59], s19 offen offset:2048 sc1\n\tv_mfma_f32_32x32x16_bf16 v[48:63], v[124:127], a[60:63], v[48:63]\n\t"
      "buffer_load_dwordx4 v[140:143], v0, s[56:59], s19 offen offset:3072 sc1\n\tv_mfma_f32_32x32x16_bf16 v[32:47], v[96:99], a[64:67], 0\n\ts_nop 4\n\t"
      "v_mfma_f32_32x32x16_bf16 v[32:47], v[100:103], a[68:71], v[32:47]\n\ts_nop 4\n\tv_mfma_f32_32x32x16_bf16 v[32:47], v[104:107], a[72:75], v[32:47]\n\t"
      "v_cvt_pk_bf16_f32 v80, v48, v49\n\tv_cvt_pk_bf16_f32 v81, v50, v51\n\tv_cvt_pk_bf16_f32 v82, v56, v57\n\tv_cvt_pk_bf16_f32 v83, v58, v59\n\t"
      "v_mfma_f32_32x32x16_bf16 v[32:47], v[108:111], a[76:79], v[32:47]\n\tbuffer_store_dwordx4 v[80:83], v1, s[8:11], s25 offen offset:0 sc1\n\t"
      "v_mfma_f32_32x32x16_bf16 v[32:47], v[112:115], a[80:83], v[32:47]\n\tv_cvt_pk_bf16_f32 v84, v52, v53\n\tv_cvt_pk_bf16_f32 v85, v54, v55\n\t"
      "v_cvt_pk_bf16_f32 v86, v60, v61\n\tv_cvt_pk_bf16_f32 v87, v62, v63\n\tv_mfma_f32_32x32x16_bf16 v[32:47], v[116:119], a[84:87], v[32:47]\n\t"
      "buffer_store_dwordx4 v[84:87], v1, s[8:11], s25 offen offset:512 sc1\n\tv_mfma_f32_32x32x16_bf16 v[32:47], v[120:123], a[88:91], v[32:47]\n\t"
      "s_add_u32 s25, s20, 0x40000\n\tbuffer_load_dwordx4 v[144:147], v0, s[56:59], s24 offen offset:0 sc1\n\t"
      "v_mfma_f32_32x32x16_bf16 v[32:47], v[124:127], a[92:95], v[32:47]\n\tbuffer_load_dwordx4 v[148:151], v0, s[56:59], s24 offen offset:1024 sc1\n\t"
      "v_mfma_f32_32x32x16_bf16 v[48:63], v[96:99], a[96:99], 0\n\ts_nop 4\n\tv_mfma_f32_32x32x16_bf16 v[48:63], v[100:103], a[100:103], v[48:63]\n\ts_nop 4\n\t"
      "v_mfma_f32_32x32x16_bf16 v[48:63], v[104:107], a[104:107], v[48:63]\n\tv_cvt_pk_bf16_f32 v80, v32, v33\n\tv_cvt_pk_bf16_f32 v81, v34, v35\n\t"
      "v_cvt_pk_bf16_f32 v82, v40, v41\n\tv_cvt_pk_bf16_f32 v83, v42, v43\n\tv_mfma_f32_32x32x16_bf16 v[48:63], v[108:111], a[108:111], v[48:63]\n\t"
      "buffer_store_dwordx4 v[80:83], v1, s[8:11], s25 offen offset:0 sc1\n\tv_mfma_f32_32x32x16_bf16 v[48:63], v[112:115], a[112:115], v[48:63]\n\t"
      "v_cvt_pk_bf16_f32 v84, v36, v37\n\tv_cvt_pk_bf16_f32 v85, v38, v39\n\tv_cvt_pk_bf16_f32 v86, v44, v45\n\tv_cvt_pk_bf16_f32 v87, v46, v47\n\t"
      "v_mfma_f32_32x32x16_bf16 v[48:63], v[116:119], a[116:119], v[48:63]\n\tbuffer_store_dwordx4 v[84:87], v1, s[8:11], s25 offen offset:512 sc1\n\t"
      "v_mfma_f32_32x32x16_bf16 v[48:63], v[120:123], a[120:123], v[48:63]\n\ts_add_u32 s25, s20, 0x60000\n\t"
      "buffer_load_dwordx4 v[152:155], v0, s[56:59], s24 offen offset:2048 sc1\n\tv_mfma_f32_32x32x16_bf16 v[48:63], v[124:127], a[124:127], v[48:63]\n\t"
      "buffer_load_dwordx4 v[156:159], v0, s[56:59], s24 offen offset:3072 sc1\n\tv_mfma_f32_32x32x16_bf16 v[32:47], v[96:99], a[128:131], 0\n\ts_nop 4\n\t"
      "v_mfma_f32_32x32x16_bf16 v[32:47], v[100:103], a[132:135], v[32:47]\n\ts_nop 4\n\tv_mfma_f32_32x32x16_bf16 v[32:47], v[104:107], a[136:139], v[32:47]\n\t"
      "v_cvt_pk_bf16_f32 v80, v48, v49\n\tv_cvt_pk_bf16_f32 v81, v50, v51\n\tv_cvt_pk_bf16_f32 v82, v56, v57\n\tv_cvt_pk_bf16_f32 v83, v58, v59\n\t"
      "v_mfma_f32_32x32x16_bf16 v[32:47], v[108:111], a[140:143], v[32:47]\n\tbuffer_store_dwordx4 v[80:83], v1, s[8:11], s25 offen offset:0 sc1\n\t"
      "v_mfma_f32_32x32x16_bf16 v[32:47], v[112:115], a[144:147], v[32:47]\n\tv_cvt_pk_bf16_f32 v84, v52, v53\n\tv_cvt_pk_bf16_f32 v85, v54, v55\n\t"
      "v_cvt_pk_bf16_f32 v86, v60, v61\n\tv_cvt_pk_bf16_f32 v87, v62, v63\n\tv_mfma_f32_32x32x16_bf16 v[32:47], v[116:119], a[148:151], v[32:47]\n\t"
      "buffer_store_dwordx4 v[84:87], v1, s[8:11], s25 offen offset:512 sc1\n\tv_mfma_f32_32x32x16_bf16 v[32:47], v[120:123], a[152:155], v[32:47]\n\t"
      "s_add_u32 s25, s20, 0x80000\n\ts_waitcnt vmcnt(16)\n\tbuffer_store_dword v4, v3, s[12:15], s22 offen sc1\n\t"
      "v_mfma_f32_32x32x16_bf16 v[32:47], v[124:127], a[156:159], v[32:47]\n\tbuffer_load_dword v5, v6, s[12:15], 0 offen sc1\n\t"
      "v_mfma_f32_32x32x16_bf16 v[48:63], v[96:99], a[160:163], 0\n\ts_nop 4\n\tv_mfma_f32_32x32x16_bf16 v[48:63], v[100:103], a[164:167], v[48:63]\n\ts_nop 4\n\t"
      "v_mfma_f32_32x32x16_bf16 v[48:63], v[104:107], a[168:171], v[48:63]\n\tv_cvt_pk_bf16_f32 v80, v32, v33\n\tv_cvt_pk_bf16_f32 v81, v34, v35\n\t"
      "v_cvt_pk_bf16_f32 v82, v40, v41\n\tv_cvt_pk_bf16_f32 v83, v42, v43\n\tv_mfma_f32_32x32x16_bf16 v[48:63], v[108:111], a[172:175], v[48:63]\n\t"
      "buffer_store_dwordx4 v[80:83], v1, s[8:11], s25 offen offset:0 sc1\n\tv_mfma_f32_32x32x16_bf16 v[48:63], v[112:115], a[176:179], v[48:63]\n\t"
      "v_cvt_pk_bf16_f32 v84, v36, v37\n\tv_cvt_pk_bf16_f32 v85, v38, v39\n\tv_cvt_pk_bf16_f32 v86, v44, v45\n\tv_cvt_pk_bf16_f32 v87, v46, v47\n\t"
      "v_mfma_f32_32x32x16_bf16 v[48:63], v[116:119], a[180:183], v[48:63]\n\tbuffer_store_dwordx4 v[84:87], v1, s[8:11], s25 offen offset:512 sc1\n\t"
      "v_mfma_f32_32x32x16_bf16 v[48:63], v[120:123], a[184:187], v[48:63]\n\ts_add_u32 s25, s20, 0xa0000\n\t"
      "v_mfma_f32_32x32x16_bf16 v[48:63], v[124:127], a[188:191], v[48:63]\n\tv_mfma_f32_32x32x16_bf16 v[32:47], v[96:99], a[192:195], 0\n\ts_nop 4\n\t"
      "v_mfma_f32_32x32x16_bf16 v[32:47], v[100:103], a[196:199], v[32:47]\n\ts_nop 4\n\tv_mfma_f32_32x32x16_bf16 v[32:47], v[104:107], a[200:203], v[32:47]\n\t"
      "v_cvt_pk_bf16_f32 v80, v48, v49\n\tv_cvt_pk_bf16_f32 v81, v50, v51\n\tv_cvt_pk_bf16_f32 v82, v56, v57\n\tv_cvt_pk_bf16_f32 v83, v58, v59\n\t"
      "v_mfma_f32_32x32x16_bf16 v[32:47], v[108:111], a[204:207], v[32:47]\n\tbuffer_store_dwordx4 v[80:83], v1, s[8:11], s25 offen offset:0 sc1\n\t"
      "v_mfma_f32_32x32x16_bf16 v[32:47], v[112:115], a[208:211], v[32:47]\n\tv_cvt_pk_bf16_f32 v84, v52, v53\n\tv_cvt_pk_bf16_f32 v85, v54, v55\n\t"
      "v_cvt_pk_bf16_f32 v86, v60, v61\n\tv_cvt_pk_bf16_f32 v87, v62, v63\n\tv_mfma_f32_32x32x16_bf16 v[32:47], v[116:119], a[212:215], v[32:47]\n\t"
      "buffer_store_dwordx4 v[84:87], v1, s[8:11], s25 offen offset:512 sc1\n\tv_mfma_f32_32x32x16_bf16 v[32:47], v[120:123], a[216:219], v[32:47]\n\t"
      "s_add_u32 s25, s20, 0xc0000\n\tv_mfma_f32_32x32x16_bf16 v[32:47], v[124:127], a[220:223], v[32:47]\n\tv_mfma_f32_32x32x16_bf16 v[48:63], v[96:99], a[224:227], 0\n\t"
      "s_nop 4\n\tv_mfma_f32_32x32x16_bf16 v[48:63], v[100:103], a[228:231], v[48:63]\n\ts_nop 4\n\tv_mfma_f32_32x32x16_bf16 v[48:63], v[104:107], a[232:235], v[48:63]\n\t"
      "v_cvt_pk_bf16_f32 v80, v32, v33\n\tv_cvt_pk_bf16_f32 v81, v34, v35\n\tv_cvt_pk_bf16_f32 v82, v40, v41\n\tv_cvt_pk_bf16_f32 v83, v42, v43\n\t"
      "v_mfma_f32_32x32x16_bf16 v[48:63], v[108:111], a[236:239], v[48:63]\n\tbuffer_store_dwordx4 v[80:83], v1, s[8:11], s25 offen offset:0 sc1\n\t"
      "v_mfma_f32_32x32x16_bf16 v[48:63], v[112:115], a[240:243], v[48:63]\n\tv_cvt_pk_bf16_f32 v84, v36, v37\n\tv_cvt_pk_bf16_f32 v85, v38, v39\n\t"
      "v_cvt_pk_bf16_f32 v86, v44, v45\n\tv_cvt_pk_bf16_f32 v87, v46, v47\n\tv_mfma_f32_32x32x16_bf16 v[48:63], v[116:119], a[244:247], v[48:63]\n\t"
      "buffer_store_dwordx4 v[84:87], v1, s[8:11], s25 offen offset:512 sc1\n\tv_mfma_f32_32x32x16_bf16 v[48:63], v[120:123], a[248:251], v[48:63]\n\t"
      "s_add_u32 s25, s20, 0xe0000\n\ts_waitcnt vmcnt(6)\n\tv_readlane_b32 s23, v5, 0\n\tv_readlane_b32 s48, v5, 1\n\ts_add_u32 s48, s48, 12\n\ts_min_u32 s23, s23, s48\n\t"
      "v_mfma_f32_32x32x16_bf16 v[48:63], v[124:127], a[252:255], v[48:63]\n\ts_add_u32 s17, s17, 1\n\ts_add_u32 s20, s20, 0x400000\n\ts_cmp_eq_u32 s20, 0x4400000\n\t"
      "s_cselect_b32 s20, 0x1400000, s20\n\ts_cmp_eq_u32 s58, 0\n\ts_cbranch_scc1 40f\n\t"
      "11:\n\t"
      STF("s_memrealtime vcc\n\ts_waitcnt lgkmcnt(0)\n\tv_mov_b32 v95, vcc_lo\n\ts_lshl_b32 vcc_hi, s17, 2\n\ts_add_u32 vcc_hi, vcc_hi, 161052\n\tv_mov_b32 v94, vcc_hi\n\tds_write_b32 v94, v95\n\ts_memtime vcc\n\ts_waitcnt lgkmcnt(0)\n\tv_mov_b32 v95, vcc_lo\n\tds_write_b32 v94, v95 offset:400\n\ts_waitcnt lgkmcnt(0)\n\t")
      "v_mfma_f32_32x32x16_bf16 v[32:47], v[128:131], a[0:3], 0\n\t"
      "s_add_u32 s48, s17, 1\n\ts_cmp_gt_u32 s23, s48\n\ts_cselect_b32 s58, s10, 0\n\ts_nop 1\n\tv_mfma_f32_32x32x16_bf16 v[32:47], v[132:135], a[4:7], v[32:47]\n\t"
      "s_add_u32 s48, s19, 0x4000\n\ts_and_b32 s48, s48, 0x3c000\n\ts_andn2_b32 s19, s19, 0x3c000\n\ts_or_b32 s19, s19, s48\n\ts_add_u32 s24, s19, 0x1000\n\t"
      "v_mfma_f32_32x32x16_bf16 v[32:47], v[136:139], a[8:11], v[32:47]\n\tv_cvt_pk_bf16_f32 v80, v48, v49\n\tv_cvt_pk_bf16_f32 v81, v50, v51\n\t"
      "v_cvt_pk_bf16_f32 v82, v56, v57\n\tv_cvt_pk_bf16_f32 v83, v58, v59\n\tv_mfma_f32_32x32x16_bf16 v[32:47], v[140:143], a[12:15], v[32:47]\n\t"
      "buffer_store_dwordx4 v[80:83], v2, s[8:11], s25 offen offset:0 sc1\n\tv_mfma_f32_32x32x16_bf16 v[32:47], v[144:147], a[16:19], v[32:47]\n\t"
      "v_cvt_pk_bf16_f32 v84, v52, v53\n\tv_cvt_pk_bf16_f32 v85, v54, v55\n\tv_cvt_pk_bf16_f32 v86, v60, v61\n\tv_cvt_pk_bf16_f32 v87, v62, v63\n\t"
      "v_mfma_f32_32x32x16_bf16 v[32:47], v[148:151], a[20:23], v[32:47]\n\tbuffer_store_dwordx4 v[84:87], v2, s[8:11], s25 offen offset:512 sc1\n\t"
      "v_mfma_f32_32x32x16_bf16 v[32:47], v[152:155], a[24:27], v[32:47]\n\ts_add_u32 s25, s20, 0x0\n\tbuffer_load_dwordx4 v[96:99], v0, s[56:59], s19 offen offset:0 sc1\n\t"
      "v_mfma_f32_32x32x16_bf16 v[32:47], v[156:159], a[28:31], v[32:47]\n\tbuffer_load_dwordx4 v[100:103], v0, s[56:59], s19 offen offset:1024 sc1\n\tv_mov_b32 v4, s17\n\t"
      "v_mov_b32 v2, v1\n\tv_mfma_f32_32x32x16_bf16 v[48:63], v[128:131], a[32:35], 0\n\ts_nop 4\n\tv_mfma_f32_32x32x16_bf16 v[48:63], v[132:135], a[36:39], v[48:63]\n\t"
      "s_nop 4\n\tv_mfma_f32_32x32x16_bf16 v[48:63], v[136:139], a[40:43], v[48:63]\n\tv_cvt_pk_bf16_f32 v80, v32, v33\n\tv_cvt_pk_bf16_f32 v81, v34, v35\n\t"
      "v_cvt_pk_bf16_f32 v82, v40, v41\n\tv_cvt_pk_bf16_f32 v83, v42, v43\n\tv_mfma_f32_32x32x16_bf16 v[48:63], v[140:143], a[44:47], v[48:63]\n\t"
      "buffer_store_dwordx4 v[80:83], v1, s[8:11], s25 offen offset:0 sc1\n\tv_mfma_f32_32x32x16_bf16 v[48:63], v[144:147], a[48:51], v[48:63]\n\t"
      "v_cvt_pk_bf16_f32 v84, v36, v37\n\tv_cvt_pk_bf16_f32 v85, v38, v39\n\tv_cvt_pk_bf16_f32 v86, v44, v45\n\tv_cvt_pk_bf16_f32 v87, v46, v47\n\t"
      "v_mfma_f32_32x32x16_bf16 v[48:63], v[148:151], a[52:55], v[48:63]\n\tbuffer_store_dwordx4 v[84:87], v1, s[8:11], s25 offen offset:512 sc1\n\t"
      "v_mfma_f32_32x32x16_bf16 v[48:63], v[152:155], a[56:59], v[48:63]\n\ts_add_u32 s25, s20, 0x20000\n\t"
      "buffer_load_dwordx4 v[104:107], v0, s[56:59], s19 offen offset:2048 sc1\n\tv_mfma_f32_32x32x16_bf16 v[48:63], v[156:159], a[60:63], v[48:63]\n\t"
      "buffer_load_dwordx4 v[108:111], v0, s[56:59], s19 offen offset:3072 sc1\n\tv_mfma_f32_32x32x16_bf16 v[32:47], v[128:131], a[64:67], 0\n\ts_nop 4\n\t"
      "v_mfma_f32_32x32x16_bf16 v[32:47], v[132:135], a[68:71], v[32:47]\n\ts_nop 4\n\tv_mfma_f32_32x32x16_bf16 v[32:47], v[136:139], a[72:75], v[32:47]\n\t"
      "v_cvt_pk_bf16_f32 v80, v48, v49\n\tv_cvt_pk_bf16_f32 v81, v50, v51\n\tv_cvt_pk_bf16_f32 v82, v56, v57\n\tv_cvt_pk_bf16_f32 v83, v58, v59\n\t"
      "v_mfma_f32_32x32x16_bf16 v[32:47], v[140:143], a[76:79], v[32:47]\n\tbuffer_store_dwordx4 v[80:83], v1, s[8:11], s25 offen offset:0 sc1\n\t"
      "v_mfma_f32_32x32x16_bf16 v[32:47], v[144:147], a[80:83], v[32:47]\n\tv_cvt_pk_bf16_f32 v84, v52, v53\n\tv_cvt_pk_bf16_f32 v85, v54, v55\n\t"
      "v_cvt_pk_bf16_f32 v86, v60, v61\n\tv_cvt_pk_bf16_f32 v87, v62, v63\n\tv_mfma_f32_32x32x16_bf16 v[32:47], v[148:151], a[84:87], v[32:47]\n\t"
      "buffer_store_dwordx4 v[84:87], v1, s[8:11], s25 offen offset:512 sc1\n\tv_mfma_f32_32x32x16_bf16 v[32:47], v[152:155], a[88:91], v[32:47]\n\t"
      "s_add_u32 s25, s20, 0x40000\n\tbuffer_load_dwordx4 v[112:115], v0, s[56:59], s24 offen offset:0 sc1\n\t"
      "v_mfma_f32_32x32x16_bf16 v[32:47], v[156:159], a[92:95], v[32:47]\n\tbuffer_load_dwordx4 v[116:119], v0, s[56:59], s24 offen offset:1024 sc1\n\t"
      "v_mfma_f32_32x32x16_bf16 v[48:63], v[128:131], a[96:99], 0\n\ts_nop 4\n\tv_mfma_f32_32x32x16_bf16 v[48:63], v[132:135], a[100:103], v[48:63]\n\ts_nop 4\n\t"
      "v_mfma_f32_32x32x16_bf16 v[48:63], v[136:139], a[104:107], v[48:63]\n\tv_cvt_pk_bf16_f32 v80, v32, v33\n\tv_cvt_pk_bf16_f32 v81, v34, v35\n\t"
      "v_cvt_pk_bf16_f32 v82, v40, v41\n\tv_cvt_pk_bf16_f32 v83, v42, v43\n\tv_mfma_f32_32x32x16_bf16 v[48:63], v[140:143], a[108:111], v[48:63]\n\t"
      "buffer_store_dwordx4 v[80:83], v1, s[8:11], s25 offen offset:0 sc1\n\tv_mfma_f32_32x32x16_bf16 v[48:63], v[144:147], a[112:115], v[48:63]\n\t"
      "v_cvt_pk_bf16_f32 v84, v36, v37\n\tv_cvt_pk_bf16_f32 v85, v38, v39\n\tv_cvt_pk_bf16_f32 v86, v44, v45\n\tv_cvt_pk_bf16_f32 v87, v46, v47\n\t"
      "v_mfma_f32_32x32x16_bf16 v[48:63], v[148:151], a[116:119], v[48:63]\n\tbuffer_store_dwordx4 v[84:87], v1, s[8:11], s25 offen offset:512 sc1\n\t"
      "v_mfma_f32_32x32x16_bf16 v[48:63], v[152:155], a[120:123], v[48:63]\n\ts_add_u32 s25, s20, 0x60000\n\t"
      "buffer_load_dwordx4 v[120:123], v0, s[56:59], s24 offen offset:2048 sc1\n\tv_mfma_f32_32x32x16_bf16 v[48:63], v[156:159], a[124:127], v[48:63]\n\t"
      "buffer_load_dwordx4 v[124:127], v0, s[56:59], s24 offen offset:3072 sc1\n\tv_mfma_f32_32x32x16_bf16 v[32:47], v[128:131], a[128:131], 0\n\ts_nop 4\n\t"
      "v_mfma_f32_32x32x16_bf16 v[32:47], v[132:135], a[132:135], v[32:47]\n\ts_nop 4\n\tv_mfma_f32_32x32x16_bf16 v[32:47], v[136:139], a[136:139], v[32:47]\n\t"
      "v_cvt_pk_bf16_f32 v80, v48, v49\n\tv_cvt_pk_bf16_f32 v81, v50, v51\n\tv_cvt_pk_bf16_f32 v82, v56, v57\n\tv_cvt_pk_bf16_f32 v83, v58, v59\n\t"
      "v_mfma_f32_32x32x16_bf16 v[32:47], v[140:143], a[140:143], v[32:47]\n\tbuffer_store_dwordx4 v[80:83], v1, s[8:11], s25 offen offset:0 sc1\n\t"
      "v_mfma_f32_32x32x16_bf16 v[32:47], v[144:147], a[144:147], v[32:47]\n\tv_cvt_pk_bf16_f32 v84, v52, v53\n\tv_cvt_pk_bf16_f32 v85, v54, v55\n\t"
      "v_cvt_pk_bf16_f32 v86, v60, v61\n\tv_cvt_pk_bf16_f32 v87, v62, v63\n\tv_mfma_f32_32x32x16_bf16 v[32:47], v[148:151], a[148:151], v[32:47]\n\t"
      "buffer_store_dwordx4 v[84:87], v1, s[8:11], s25 offen offset:512 sc1\n\tv_mfma_f32_32x32x16_bf16 v[32:47], v[152:155], a[152:155], v[32:47]\n\t"
      "s_add_u32 s25, s20, 0x80000\n\ts_waitcnt vmcnt(16)\n\tbuffer_store_dword v4, v3, s[12:15], s22 offen sc1\n\t"
      "v_mfma_f32_32x32x16_bf16 v[32:47], v[156:159], a[156:159], v[32:47]\n\tbuffer_load_dword v5, v6, s[12:15], 0 offen sc1\n\t"
      "v_mfma_f32_32x32x16_bf16 v[48:63], v[128:131], a[160:163], 0\n\ts_nop 4\n\tv_mfma_f32_32x32x16_bf16 v[48:63], v[132:135], a[164:167], v[48:63]\n\ts_nop 4\n\t"
      "v_mfma_f32_32x32x16_bf16 v[48:63], v[136:139], a[168:171], v[48:63]\n\tv_cvt_pk_bf16_f32 v80, v32, v33\n\tv_cvt_pk_bf16_f32 v81, v34, v35\n\t"
      "v_cvt_pk_bf16_f32 v82, v40, v41\n\tv_cvt_pk_bf16_f32 v83, v42, v43\n\tv_mfma_f32_32x32x16_bf16 v[48:63], v[140:143], a[172:175], v[48:63]\n\t"
      "buffer_store_dwordx4 v[80:83], v1, s[8:11], s25 offen offset:0 sc1\n\tv_mfma_f32_32x32x16_bf16 v[48:63], v[144:147], a[176:179], v[48:63]\n\t"
      "v_cvt_pk_bf16_f32 v84, v36, v37\n\tv_cvt_pk_bf16_f32 v85, v38, v39\n\tv_cvt_pk_bf16_f32 v86, v44, v45\n\tv_cvt_pk_bf16_f32 v87, v46, v47\n\t"
      "v_mfma_f32_32x32x16_bf16 v[48:63], v[148:151], a[180:183], v[48:63]\n\tbuffer_store_dwordx4 v[84:87], v1, s[8:11], s25 offen offset:512 sc1\n\t"
      "v_mfma_f32_32x32x16_bf16 v[48:63], v[152:155], a[184:187], v[48:63]\n\ts_add_u32 s25, s20, 0xa0000\n\t"
      "v_mfma_f32_32x32x16_bf16 v[48:63], v[156:159], a[188:191], v[48:63]\n\tv_mfma_f32_32x32x16_bf16 v[32:47], v[128:131], a[192:195], 0\n\ts_nop 4\n\t"
      "v_mfma_f32_32x32x16_bf16 v[32:47], v[132:135], a[196:199], v[32:47]\n\ts_nop 4\n\tv_mfma_f32_32x32x16_bf16 v[32:47], v[136:139], a[200:203], v[32:47]\n\t"
      "v_cvt_pk_bf16_f32 v80, v48, v49\n\tv_cvt_pk_bf16_f32 v81, v50, v51\n\tv_cvt_pk_bf16_f32 v82, v56, v57\n\tv_cvt_pk_bf16_f32 v83, v58, v59\n\t"
      "v_mfma_f32_32x32x16_bf16 v[32:47], v[140:143], a[204:207], v[32:47]\n\tbuffer_store_dwordx4 v[80:83], v1, s[8:11], s25 offen offset:0 sc1\n\t"
      "v_mfma_f32_32x32x16_bf16 v[32:47], v[144:147], a[208:211], v[32:47]\n\tv_cvt_pk_bf16_f32 v84, v52, v53\n\tv_cvt_pk_bf16_f32 v85, v54, v55\n\t"
      "v_cvt_pk_bf16_f32 v86, v60, v61\n\tv_cvt_pk_bf16_f32 v87, v62, v63\n\tv_mfma_f32_32x32x16_bf16 v[32:47], v[148:151], a[212:215], v[32:47]\n\t"
      "buffer_store_dwordx4 v[84:87], v1, s[8:11], s25 offen offset:512 sc1\n\tv_mfma_f32_32x32x16_bf16 v[32:47], v[152:155], a[216:219], v[32:47]\n\t"
      "s_add_u32 s25, s20, 0xc0000\n\tv_mfma_f32_32x32x16_bf16 v[32:47], v[156:159], a[220:223], v[32:47]\n\tv_mfma_f32_32x32x16_bf16 v[48:63], v[128:131], a[224:227], 0\n\t"
      "s_nop 4\n\tv_mfma_f32_32x32x16_bf16 v[48:63], v[132:135], a[228:231], v[48:63]\n\ts_nop 4\n\tv_mfma_f32_32x32x16_bf16 v[48:63], v[136:139], a[232:235], v[48:63]\n\t"
      "v_cvt_pk_bf16_f32 v80, v32, v33\n\tv_cvt_pk_bf16_f32 v81, v34, v35\n\tv_cvt_pk_bf16_f32 v82, v40, v41\n\tv_cvt_pk_bf16_f32 v83, v42, v43\n\t"
      "v_mfma_f32_32x32x16_bf16 v[48:63], v[140:143], a[236:239], v[48:63]\n\tbuffer_store_dwordx4 v[80:83], v1, s[8:11], s25 offen offset:0 sc1\n\t"
      "v_mfma_f32_32x32x16_bf16 v[48:63], v[144:147], a[240:243], v[48:63]\n\tv_cvt_pk_bf16_f32 v84, v36, v37\n\tv_cvt_pk_bf16_f32 v85, v38, v39\n\t"
      "v_cvt_pk_bf16_f32 v86, v44, v45\n\tv_cvt_pk_bf16_f32 v87, v46, v47\n\tv_mfma_f32_32x32x16_bf16 v[48:63], v[148:151], a[244:247], v[48:63]\n\t"
      "buffer_store_dwordx4 v[84:87], v1, s[8:11], s25 offen offset:512 sc1\n\tv_mfma_f32_32x32x16_bf16 v[48:63], v[152:155], a[248:251], v[48:63]\n\t"
      "s_add_u32 s25, s20, 0xe0000\n\ts_waitcnt vmcnt(6)\n\tv_readlane_b32 s23, v5, 0\n\tv_readlane_b32 s48, v5, 1\n\ts_add_u32 s48, s48, 12\n\ts_min_u32 s23, s23, s48\n\t"
      "v_mfma_f32_32x32x16_bf16 v[48:63], v[156:159], a[252:255], v[48:63]\n\ts_add_u32 s17, s17, 1\n\ts_add_u32 s20, s20, 0x400000\n\ts_cmp_eq_u32 s20, 0x4400000\n\t"
      "s_cselect_b32 s20, 0x1400000, s20\n\ts_cmp_eq_u32 s58, 0\n\ts_cbranch_scc1 42f\n\t"
      "12:\n\t"
      STF("s_memrealtime vcc\n\ts_waitcnt lgkmcnt(0)\n\tv_mov_b32 v95, vcc_lo\n\ts_lshl_b32 vcc_hi, s17, 2\n\ts_add_u32 vcc_hi, vcc_hi, 161052\n\tv_mov_b32 v94, vcc_hi\n\tds_write_b32 v94, v95\n\ts_memtime vcc\n\ts_waitcnt lgkmcnt(0)\n\tv_mov_b32 v95, vcc_lo\n\tds_write_b32 v94, v95 offset:400\n\ts_waitcnt lgkmcnt(0)\n\t")
      "s_branch 10b\n\t"
      "40:\n\ts_nop 11\n\tv_cvt_pk_bf16_f32 v80, v48, v49\n\tv_cvt_pk_bf16_f32 v81, v50, v51\n\tv_cvt_pk_bf16_f32 v82, v56, v57\n\tv_cvt_pk_bf16_f32 v83, v58, v59\n\t"
      "v_cvt_pk_bf16_f32 v84, v52, v53\n\tv_cvt_pk_bf16_f32 v85, v54, v55\n\tv_cvt_pk_bf16_f32 v86, v60, v61\n\tv_cvt_pk_bf16_f32 v87, v62, v63\n\t"
      "buffer_store_dwordx4 v[80:83], v1, s[8:11], s25 offen offset:0 sc1\n\tbuffer_store_dwordx4 v[84:87], v1, s[8:11], s25 offen offset:512 sc1\n\tv_mov_b32 v4, s17\n\t"
      "s_waitcnt vmcnt(0)\n\tbuffer_store_dword v4, v3, s[12:15], s22 offen sc1\n\ts_cmp_eq_u32 s17, 64\n\ts_cbranch_scc1 50f\n\t"
      "41:\n\t"
      "45:\n\ts_cmp_gt_u32 s23, s17\n\ts_cbranch_scc1 46f\n\tbuffer_load_dword v5, v6, s[12:15], 0 offen sc1\n\ts_waitcnt vmcnt(0)\n\tv_readlane_b32 s23, v5, 0\n\t"
      "v_readlane_b32 s48, v5, 1\n\ts_add_u32 s48, s48, 12\n\ts_min_u32 s23, s23, s48\n\ts_cmp_eq_u32 s18, 0\n\ts_cbranch_scc1 46f\n\ts_sub_u32 s18, s18, 1\n\ts_sleep 1\n\t"
      "s_branch 45b\n\t"
      "46:\n\ts_add_u32 s24, s19, 0x1000\n\tbuffer_load_dwordx4 v[128:131], v0, s[8:11], s19 offen offset:0 sc1\n\t"
      "buffer_load_dwordx4 v[132:135], v0, s[8:11], s19 offen offset:1024 sc1\n\tbuffer_load_dwordx4 v[136:139], v0, s[8:11], s19 offen offset:2048 sc1\n\t"
      "buffer_load_dwordx4 v[140:143], v0, s[8:11], s19 offen offset:3072 sc1\n\tbuffer_load_dwordx4 v[144:147], v0, s[8:11], s24 offen offset:0 sc1\n\t"
      "buffer_load_dwordx4 v[148:151], v0, s[8:11], s24 offen offset:1024 sc1\n\tbuffer_load_dwordx4 v[152:155], v0, s[8:11], s24 offen offset:2048 sc1\n\t"
      "buffer_load_dwordx4 v[156:159], v0, s[8:11], s24 offen offset:3072 sc1\n\ts_waitcnt vmcnt(0)\n\ts_branch 11b\n\t"
      "42:\n\ts_nop 11\n\tv_cvt_pk_bf16_f32 v80, v48, v49\n\tv_cvt_pk_bf16_f32 v81, v50, v51\n\tv_cvt_pk_bf16_f32 v82, v56, v57\n\tv_cvt_pk_bf16_f32 v83, v58, v59\n\t"
      "v_cvt_pk_bf16_f32 v84, v52, v53\n\tv_cvt_pk_bf16_f32 v85, v54, v55\n\tv_cvt_pk_bf16_f32 v86, v60, v61\n\tv_cvt_pk_bf16_f32 v87, v62, v63\n\t"
      "buffer_store_dwordx4 v[80:83], v1, s[8:11], s25 offen offset:0 sc1\n\tbuffer_store_dwordx4 v[84:87], v1, s[8:11], s25 offen offset:512 sc1\n\tv_mov_b32 v4, s17\n\t"
      "s_waitcnt vmcnt(0)\n\tbuffer_store_dword v4, v3, s[12:15], s22 offen sc1\n\ts_cmp_eq_u32 s17, 64\n\ts_cbranch_scc1 50f\n\t"
      "43:\n\t"
      "45:\n\ts_cmp_gt_u32 s23, s17\n\ts_cbranch_scc1 46f\n\tbuffer_load_dword v5, v6, s[12:15], 0 offen sc1\n\ts_waitcnt vmcnt(0)\n\tv_readlane_b32 s23, v5, 0\n\t"
      "v_readlane_b32 s48, v5, 1\n\ts_add_u32 s48, s48, 12\n\ts_min_u32 s23, s23, s48\n\ts_cmp_eq_u32 s18, 0\n\ts_cbranch_scc1 46f\n\ts_sub_u32 s18, s18, 1\n\ts_sleep 1\n\t"
      "s_branch 45b\n\t"
      "46:\n\ts_add_u32 s24, s19, 0x1000\n\tbuffer_load_dwordx4 v[96:99], v0, s[8:11], s19 offen offset:0 sc1\n\t"
      "buffer_load_dwordx4 v[100:103], v0, s[8:11], s19 offen offset:1024 sc1\n\tbuffer_load_dwordx4 v[104:107], v0, s[8:11], s19 offen offset:2048 sc1\n\t"
      "buffer_load_dwordx4 v[108:111], v0, s[8:11], s19 offen offset:3072 sc1\n\tbuffer_load_dwordx4 v[112:115], v0, s[8:11], s24 offen offset:0 sc1\n\t"
      "buffer_load_dwordx4 v[116:119], v0, s[8:11], s24 offen offset:1024 sc1\n\tbuffer_load_dwordx4 v[120:123], v0, s[8:11], s24 offen offset:2048 sc1\n\t"
      "buffer_load_dwordx4 v[124:127], v0, s[8:11], s24 offen offset:3072 sc1\n\ts_waitcnt vmcnt(0)\n\ts_branch 12b\n\t"
      "50:\n\t"
      STF("s_memrealtime vcc\n\ts_waitcnt lgkmcnt(0)\n\tv_mov_b32 v95, vcc_lo\n\ts_lshl_b32 vcc_hi, s17, 2\n\ts_add_u32 vcc_hi, vcc_hi, 161052\n\tv_mov_b32 v94, vcc_hi\n\tds_write_b32 v94, v95\n\ts_memtime vcc\n\ts_waitcnt lgkmcnt(0)\n\tv_mov_b32 v95, vcc_lo\n\tds_write_b32 v94, v95 offset:400\n\ts_waitcnt lgkmcnt(0)\n\t")
      "s_waitcnt vmcnt(0) lgkmcnt(0)\n\t"
      TKS("s_mul_i32 s48, s16, 72\n\ts_add_u32 s48, s48, 162080\n\tv_mov_b32 v224, s48\n\tds_read_b32 v225, v224 offset:0\n\ts_waitcnt lgkmcnt(0)\n\tv_readfirstlane_b32 s72, v225\n\tds_read_b32 v225, v224 offset:4\n\ts_waitcnt lgkmcnt(0)\n\tv_readfirstlane_b32 s73, v225\n\tds_read_b32 v225, v224 offset:8\n\ts_waitcnt lgkmcnt(0)\n\tv_readfirstlane_b32 s74, v225\n\tds_read_b32 v225, v224 offset:12\n\ts_waitcnt lgkmcnt(0)\n\tv_readfirstlane_b32 s75, v225\n\tds_read_b32 v225, v224 offset:16\n\ts_waitcnt lgkmcnt(0)\n\tv_readfirstlane_b32 s76, v225\n\tds_read_b32 v225, v224 offset:20\n\ts_waitcnt lgkmcnt(0)\n\tv_readfirstlane_b32 s77, v225\n\tds_read_b32 v225, v224 offset:24\n\ts_waitcnt lgkmcnt(0)\n\tv_readfirstlane_b32 s78, v225\n\tds_read_b32 v225, v224 offset:28\n\ts_waitcnt lgkmcnt(0)\n\tv_readfirstlane_b32 s79, v225\n\tds_read_b32 v225, v224 offset:32\n\ts_waitcnt lgkmcnt(0)\n\tv_readfirstlane_b32 s80, v225\n\tds_read_b32 v225, v224 offset:36\n\ts_waitcnt lgkmcnt(0)\n\tv_readfirstlane_b32 s81, v225\n\tds_read_b32 v225, v224 offset:40\n\ts_waitcnt lgkmcnt(0)\n\tv_readfirstlane_b32 s82, v225\n\tds_read_b32 v225, v224 offset:44\n\ts_waitcnt lgkmcnt(0)\n\tv_readfirstlane_b32 s83, v225\n\tds_read_b32 v225, v224 offset:48\n\ts_waitcnt lgkmcnt(0)\n\tv_readfirstlane_b32 s84, v225\n\tds_read_b32 v225, v224 offset:52\n\ts_waitcnt lgkmcnt(0)\n\tv_readfirstlane_b32 s85, v225\n\tds_read_b32 v225, v224 offset:56\n\ts_waitcnt lgkmcnt(0)\n\tv_readfirstlane_b32 s86, v225\n\tds_read_b32 v225, v224 offset:60\n\ts_waitcnt lgkmcnt(0)\n\tv_readfirstlane_b32 s87, v225\n\tds_read_b32 v225, v224 offset:64\n\ts_waitcnt lgkmcnt(0)\n\tv_readfirstlane_b32 s88, v225\n\tds_read_b32 v225, v224 offset:68\n\ts_waitcnt lgkmcnt(0)\n\tv_readfirstlane_b32 s89, v225\n\t")
      STS("s_lshl_b32 s48, s16, 2\n\ts_add_u32 s48, s48, 162944\n\tv_mov_b32 v0, s48\n\tv_mov_b32 v1, s50\n\tds_write_b32 v0, v1\n\ts_cmp_lg_u32 s16, 0\n\ts_cbranch_scc1 97f\n\tv_mov_b32 v1, s51\n\tds_write_b32 v0, v1 offset:16\n\t97:\n\ts_cmp_lg_u32 s16, 0\n\ts_cbranch_scc1 95f\n\tv_mov_b32 v1, s61\n\tds_write_b32 v0, v1 offset:20\n\tv_mov_b32 v1, s21\n\tds_write_b32 v0, v1 offset:24\n\tv_mov_b32 v1, s22\n\tds_write_b32 v0, v1 offset:28\n\t95:\n\t")
      "s_cmp_lg_u32 s18, 0\n\ts_cbranch_scc1 96f\n\tv_mov_b32 v1, 1\n\tbuffer_store_dword v1, off, s[12:15], 0 offset:3328 sc1\n\t"
      "96:\n\t"
      : "+{v[0:31]}"(k), "+{v[160:191]}"(s0), "+{v[192:223]}"(s1), ALL_REGS :: "memory", "vcc", "scc", S_CLOB TK_CLOB);
}

DEV void conv_role(Ctx& c, int cg) {
  const KArg p = enter(c);
  constexpr int CH = 5200, U8 = 0;
  constexpr int CP = 4160, FC = 41600;
  constexpr int YS = FC + 16 * CP, RED = YS + 32768;
  static_assert(RED + 8192 <= LDS_BYTES - 16, "lds");
  c.budget = SPIN_CAP;
  const int w = c.w, lane = c.lane = fresh_lane(), tid = w * 64 + lane;
  const int d0 = 8 * cg;
  const int de = (cg >> 3) * 64 + (tid & 7) * 8, t0 = (cg & 7) * 256 + (tid >> 3) * 8;
  auto group_sync = [&](int phase) {
    gu32* cnt = c.ctrl + (C_CONV + (cg >> 3) * 16 + phase * 8) / 4;
    VMWAIT(0);
    BAR();
    if (w == 0) {
      if (lane == 0) __hip_atomic_fetch_add(cnt, 1u, RLX_AGENT);
#pragma unroll 1
      while (__builtin_amdgcn_readfirstlane(__hip_atomic_load(cnt, RLX_AGENT)) < 8u) { if (spend(c)) break; __builtin_amdgcn_s_sleep(4); }
    }
    BAR();
  };
  v4u kv[8];
  BAR();
  {
    const rsrc_t rk = mkrs(p->bk, 2048 * 2048), rv = mkrs(p->bv, 2048 * 2048);
    v4u kr[11], vr[8], fr[8];
#pragma unroll
    for (int i = 0; i < 11; ++i) kr[i] = ld16(rk, (t0 + i - 3) * 2048 + de * 2, 0);
#pragma unroll
    for (int i = 0; i < 8; ++i) vr[i] = ld16(rv, (t0 + i) * 2048 + de * 2, 0);
    v4u sh[4];
#pragma unroll
    for (int i = 0; i < 4; ++i) sh[i] = *(const GAS v4u*)(p->shift + de * 4 + i * 8);
#pragma unroll
    for (int i = 0; i < 8; ++i) fr[i] = *(const GAS v4u*)(p->ssmk + (size_t)(d0 + i) * 2048 + tid * 8);
    if (tid < 40) sts16(c.L, U8 + (tid / 5) * CH + (tid % 5) * 16, v4u{0, 0, 0, 0});
    if (tid < 64) sts16(c.L, FC + (tid >> 2) * CP + 4096 + (tid & 3) * 16, v4u{0, 0, 0, 0});
#pragma unroll
    for (int ch = 0; ch < 8; ++ch) {
      const unsigned sa = sh[ch >> 1][(ch & 1) * 2], sb = sh[ch >> 1][(ch & 1) * 2 + 1];
      const float s0 = blo(sa), s1 = bhi(sa), s2 = blo(sb), s3 = bhi(sb);
      float kk[11], o[8];
#pragma unroll
      for (int i = 0; i < 11; ++i) kk[i] = (ch & 1) ? bhi(kr[i][ch >> 1]) : blo(kr[i][ch >> 1]);
#pragma unroll
      for (int i = 0; i < 8; ++i) {
        float a = rb(kk[i] * s3);
        a = rb(a + rb(kk[i + 1] * s2));
        a = rb(a + rb(kk[i + 2] * s1));
        a = rb(a + rb(kk[i + 3] * s0));
        o[i] = a * ((ch & 1) ? bhi(vr[i][ch >> 1]) : blo(vr[i][ch >> 1]));
      }
      kv[ch] = v4u{pk(o[0], o[1]), pk(o[2], o[3]), pk(o[4], o[5]), pk(o[6], o[7])};
      st16s(c.rs, de * 4096 + t0 * 2, R_KVT + ch * 4096, kv[ch]);
    }
#pragma unroll
    for (int i = 0; i < 8; ++i) sts16(c.L, YS + i * 4096 + tid * 16, fr[i]);
    group_sync(0);
#pragma unroll
    for (int ch = 0; ch < 8; ++ch) fr[ch] = ld16s(c.rs, tid * 16, R_KVT + (d0 + ch) * 4096);
#pragma unroll
    for (int ch = 0; ch < 8; ++ch) sts16(c.L, U8 + ch * CH + 80 + (tid >> 2) * 80 + (tid & 3) * 16, fr[ch]);
  }
  BAR();
  STAMP(5);
  auto build = [&](int ch, int q0, int q1) {
    for (int q = q0; q < q1; ++q) {
      const int m = lane + 64 * q;
      const v4u hi = lds16(c.L, YS + ch * 4096 + (255 - m) * 16);
      v4u lo = lds16(c.L, YS + ch * 4096 + (m < 255 ? 254 - m : 0) * 16);
      if (m == 255) lo = v4u{0, 0, 0, 0};
      unsigned G[8];
#pragma unroll
      for (int i = 0; i < 4; ++i) { G[i] = __builtin_amdgcn_alignbyte(hi[3 - i], hi[3 - i], 2); G[4 + i] = __builtin_amdgcn_alignbyte(lo[3 - i], lo[3 - i], 2); }
#pragma unroll
      for (int sg = 0; sg < 8; ++sg) {
        v4u o;
#pragma unroll
        for (int i = 0; i < 4; ++i) o[i] = (sg & 1) ? __builtin_amdgcn_alignbyte(G[(sg >> 1) + i + 1], G[(sg >> 1) + i], 2) : G[(sg >> 1) + i];
        sts16(c.L, FC + ((ch & 1) * 8 + sg) * CP + m * 16, o);
      }
    }
  };
  build(0, w, w + 1);
  BAR();
  const int h2 = lane >> 5, r = lane & 31;
  for (int ch = 0; ch < 8; ++ch) {
    f32x16 acc;
    if (w == 3) {
      if (ch + 1 < 8) build(ch + 1, 0, 4);
    } else {
#pragma unroll
      for (int i = 0; i < 16; ++i) acc[i] = 0.f;
      const int dl0 = w == 2 ? 32 : 0, i0 = w == 0 ? 0 : 32;
      const int sg = (7 - r) & 7;
      int fo = FC + ((ch & 1) * 8 + sg) * CP + (2047 - 32 * dl0 - r + 8 * h2 - sg) * 2;
      const int ub = (i0 + r - dl0) * 80;
      const int ubase = U8 + ch * CH + 80 + h2 * 16;
      v4u fa[3][2], fb[3][2];
      fo = opq(fo - 31 * 64);
#pragma unroll
      for (int dl = -2; dl < 32; ++dl) {
        const int dn = dl + 2;
        if (dn < 32) {
          const int uo = ubase + max(ub - dn * 80, -80);
          fa[dn % 3][0] = lds16(c.L, fo + (31 - dn) * 64); fb[dn % 3][0] = lds16(c.L, uo);
          fa[dn % 3][1] = lds16(c.L, fo + (31 - dn) * 64 + 32); fb[dn % 3][1] = lds16(c.L, uo + 32);
        }
        SCHED();
        if (dl >= 0) {
          acc = mfma32(fa[dl % 3][0], fb[dl % 3][0], acc);
          acc = mfma32(fa[dl % 3][1], fb[dl % 3][1], acc);
        }
        SCHED();
      }
      if (w == 2) {
#pragma unroll
        for (int m = 0; m < 4; ++m) sts16(c.L, RED + (ch & 1) * 4096 + m * 1024 + lane * 16, v4u{fu(acc[4 * m]), fu(acc[4 * m + 1]), fu(acc[4 * m + 2]), fu(acc[4 * m + 3])});
      }
    }
    BAR();
    if (w < 2) {
#pragma unroll
      for (int m = 0; m < 4; ++m) {
        f32x4 v = {acc[4 * m], acc[4 * m + 1], acc[4 * m + 2], acc[4 * m + 3]};
        if (w == 1) v += __builtin_bit_cast(f32x4, lds16(c.L, RED + (ch & 1) * 4096 + m * 1024 + lane * 16));
        sts8(c.L, YS + ch * 4096 + (32 * (32 * w + r) + 8 * m + 4 * h2) * 2, v2u{pk(v[0], v[1]), pk(v[2], v[3])});
      }
    }
  }
  BAR();
  STAMP(6);
  {
    const rsrc_t ro = mkrs(p->out, 2048 * 2048 * 2);
    v4u qr[8], yv[8];
#pragma unroll
    for (int ch = 0; ch < 8; ++ch) st16s(c.rs, tid * 16, R_YT + (d0 + ch) * 4096, lds16(c.L, YS + ch * 4096 + tid * 16));
#pragma unroll
    for (int i = 0; i < 8; ++i) qr[i] = *(const GAS v4u*)(p->bq + (size_t)(t0 + i) * 1024 + de);
    const v4u dd = *(const GAS v4u*)(p->ssmD + de);
    group_sync(1);
#pragma unroll
    for (int ch = 0; ch < 8; ++ch) yv[ch] = ld16s(c.rs, de * 4096 + t0 * 2, R_YT + ch * 4096);
#pragma unroll
    for (int i = 0; i < 8; ++i) {
      float o[8];
#pragma unroll
      for (int ch = 0; ch < 8; ++ch) {
        const float yy = (i & 1) ? bhi(yv[ch][i >> 1]) : blo(yv[ch][i >> 1]);
        const float uu = (i & 1) ? bhi(kv[ch][i >> 1]) : blo(kv[ch][i >> 1]);
        const float dv = (ch & 1) ? bhi(dd[ch >> 1]) : blo(dd[ch >> 1]);
        const float qq = (ch & 1) ? bhi(qr[i][ch >> 1]) : blo(qr[i][ch >> 1]);
        o[ch] = qq * rb(yy + rb(uu * dv));
      }
      __builtin_amdgcn_raw_buffer_store_b128(v4u{pk(o[0], o[1]), pk(o[2], o[3]), pk(o[4], o[5]), pk(o[6], o[7])}, ro, (t0 + i) * 4096 + (1024 + de) * 2, 0, AUX_NT);
    }
  }
  if (c.budget == 0u) __hip_atomic_store(c.ctrl + C_TMO / 4, 1u, RLX_AGENT);
}

extern "C" __global__ void __launch_bounds__(256, 1) mk_mega(Params p) {
  extern __shared__ __attribute__((aligned(16))) unsigned char lds_raw[];
  Ctx c;
  c.ctrl = (gu32*)p.ctrl;
  c.L = (lds_t)lds_raw;
  c.w = __builtin_amdgcn_readfirstlane(threadIdx.x >> 6);
  c.lane = threadIdx.x & 63;
  c.b = (int)(blockIdx.x & 255u);
  c.att = (int)(blockIdx.x >> 8);
  c.ask = 0;
  if (c.w == 0 && c.att == 0) {
    c.ask = __builtin_amdgcn_readfirstlane(__hip_atomic_load(c.ctrl + C_ECHO / 4 + c.b, RLX_AGENT)) + 1u;
    if (c.lane == 0) __hip_atomic_store(c.ctrl + (C_ASK + c.b * 64) / 4, c.ask, RLX_AGENT);
  }
  c.slot = (c.att ? (c.b & 7) : (int)((unsigned)__builtin_amdgcn_s_getreg(20 | (3 << 11)) & 7u)) * 32 + (c.b >> 3);
  if (c.b == 130 && c.w == 3) serve(c);
  const int x = c.slot >> 5, t = c.slot & 31;
  if (x < 4) {
    if (t < 8) qk_role(c, x * 8 + t);
    else if (t < 16) vo_role(c, x * 8 + t - 8);
    else y_role(c, x * 16 + t - 16);
  } else {
    if (t < 24) { const int i = (x - 4) * 24 + t; g_role(c, i >> 5, i & 31, t); }
    else zc_role(c, (x - 4) * 8 + t - 24);
    STAMP(2);
    conv_role(c, (x - 4) * 32 + t);
  }
  STAMP(3);
}

}

extern "C" void kernel_launch(void* const* d_in, const int* in_sizes, int n_in, void* d_out, int out_size, void* d_ws, size_t ws_size,
                              hipStream_t stream) {
  using namespace mk;
  (void)in_sizes; (void)n_in; (void)out_size; (void)ws_size;
  Params p;
  p.x = (const u16*)d_in[0]; p.mem0 = (const u16*)d_in[1];
  p.Wq = (const u16*)d_in[2]; p.Wk = (const u16*)d_in[3]; p.Wv = (const u16*)d_in[4]; p.Wo = (const u16*)d_in[5];
  p.bq = (const u16*)d_in[7]; p.bk = (const u16*)d_in[8]; p.bv = (const u16*)d_in[9];
  p.shift = (const u16*)d_in[10]; p.ssmk = (const u16*)d_in[11]; p.ssmD = (const u16*)d_in[12];
  p.out = (u16*)d_out;
  p.ctrl = (unsigned char*)d_ws;
  p.data = (unsigned char*)(((size_t)d_ws + ws_size - R_END - MB) & ~(size_t)((2u << 20) - 1));
  (void)hipFuncSetAttribute((const void*)mk_mega, hipFuncAttributeMaxDynamicSharedMemorySize, LDS_BYTES);
  hipLaunchKernelGGL(mk_mega, dim3(256), dim3(256), LDS_BYTES, stream, p);
}
```

```cpp
#include <hip/hip_runtime.h>
#include <hip/hip_bf16.h>

namespace mk {

#define GAS __attribute__((address_space(1)))
#define LAS __attribute__((address_space(3)))
#define DEV __device__ __forceinline__
#define RLX_AGENT __ATOMIC_RELAXED, __HIP_MEMORY_SCOPE_AGENT
typedef unsigned short u16;
typedef unsigned v2u __attribute__((ext_vector_type(2)));
typedef unsigned v4u __attribute__((ext_vector_type(4)));
typedef unsigned v8u __attribute__((ext_vector_type(8)));
typedef unsigned v16u __attribute__((ext_vector_type(16)));
typedef unsigned v32u __attribute__((ext_vector_type(32)));
typedef float f32x2 __attribute__((ext_vector_type(2)));
typedef float f32x4 __attribute__((ext_vector_type(4)));
typedef float f32x16 __attribute__((ext_vector_type(16)));
typedef __bf16 bf16x2 __attribute__((ext_vector_type(2)));
typedef __bf16 bf16x8 __attribute__((ext_vector_type(8)));
typedef __bf16 bf16x16 __attribute__((ext_vector_type(16)));
typedef GAS unsigned gu32;
typedef __amdgpu_buffer_rsrc_t rsrc_t;
typedef LAS unsigned char* lds_t;

constexpr int NSEG = 64;
constexpr unsigned SENT = 0xFFFFFFFFu;
constexpr int AUX_SC1 = 16, AUX_NT = 2;
constexpr int T_LAG = 61;
constexpr int G_AHEAD = 4;
constexpr unsigned SPIN_CAP = 1u << 21;
constexpr int LDS_BYTES = 163840;

constexpr int C_ECHO = 0, C_CLAIM = 1024, C_VERDICT = 3072, C_TMO = 3328, C_CONV = 3456, C_PROG = 3840, C_FLAG = 4096, C_ASK = C_FLAG + 224 * 128;
static_assert(C_TMO == 3328, "known to the generated code");
constexpr int F_Q = 0, F_K = 32, F_V = 64, F_Z = 96;
constexpr int CTRL_BYTES = C_ASK + 256 * 64;
constexpr int MB = 1 << 20;
constexpr int R_U = 0;
constexpr int R_MEM = 2 * MB;
constexpr int R_PT = 3 * MB;
#define ZRN 12
#define GRN 16
constexpr int R_QX = 4 * MB, R_KX = R_QX + GRN * MB / 2, R_Z = R_QX + GRN * MB, R_KVT = R_Z + ZRN * 4 * MB, R_YT = R_KVT + 4 * MB, R_END = R_YT + 4 * MB;
DEV int hblock(int h) { return h * GRN * 16384 + (((h * GRN) << 6) & 8192); }

struct Params {
  const u16 *x, *mem0, *Wq, *Wk, *Wv, *Wo, *bq, *bk, *bv, *shift, *ssmk, *ssmD;
  u16* out;
  unsigned char* ctrl;
  unsigned char* data;
};

typedef const __attribute__((address_space(4))) Params* KArg;
DEV KArg args() {
  KArg a = (KArg)__builtin_amdgcn_kernarg_segment_ptr(); asm volatile("" : "+s"(a)); return a;
}
DEV rsrc_t mkrs(const void* p, unsigned bytes) { return __builtin_amdgcn_make_buffer_rsrc((void*)p, 0, bytes, 0x00020000); }
DEV v4u ld16s(rsrc_t rs, int v, int s) { return __builtin_amdgcn_raw_buffer_load_b128(rs, v, s, AUX_SC1); }
DEV v2u ld8s(rsrc_t rs, int v, int s) { return __builtin_amdgcn_raw_buffer_load_b64(rs, v, s, AUX_SC1); }
DEV v4u ld16(rsrc_t rs, int v, int s) { return __builtin_amdgcn_raw_buffer_load_b128(rs, v, s, 0); }
DEV v4u ld16n(rsrc_t rs, int v, int s) { return __builtin_amdgcn_raw_buffer_load_b128(rs, v, s, AUX_NT); }
DEV v2u ld8(rsrc_t rs, int v, int s) { return __builtin_amdgcn_raw_buffer_load_b64(rs, v, s, 0); }
DEV void st16s(rsrc_t rs, int v, int s, v4u d) { __builtin_amdgcn_raw_buffer_store_b128(d, rs, v, s, AUX_SC1); }
DEV void st8s(rsrc_t rs, int v, int s, v2u d) { __builtin_amdgcn_raw_buffer_store_b64(d, rs, v, s, AUX_SC1); }
DEV void st4s(rsrc_t rs, int v, int s, unsigned d) { __builtin_amdgcn_raw_buffer_store_b32(d, rs, v, s, AUX_SC1); }
DEV int amap(int o, int par) { return ((o >> 13) << 14) | (par << 13) | (o & 8191); }
DEV unsigned umax(unsigned a, unsigned b) { return a > b ? a : b; }
DEV unsigned top(v4u v) { return umax(umax(v.x, v.y), umax(v.z, v.w)); }
DEV unsigned pk(float a, float b) { bf16x2 v = {(__bf16)a, (__bf16)b}; return __builtin_bit_cast(unsigned, v); }
DEV unsigned pks(float a, float b) { unsigned u = pk(a, b); return u < SENT - 1u ? u : SENT - 1u; }
DEV float blo(unsigned u) { return __builtin_bit_cast(float, u << 16); }
DEV float bhi(unsigned u) { return __builtin_bit_cast(float, u & 0xFFFF0000u); }
DEV float rb(float a) { return blo(pk(a, a)); }
DEV f32x2 rb2(f32x2 a) { const unsigned u = pk(a.x, a.y); return f32x2{blo(u), bhi(u)}; }
DEV unsigned fu(float f) { return __builtin_bit_cast(unsigned, f); }
DEV float uf(unsigned u) { return __builtin_bit_cast(float, u); }
DEV f32x4 mfma16(v4u a, v4u b, f32x4 c) {
  return __builtin_amdgcn_mfma_f32_16x16x32_bf16(__builtin_bit_cast(bf16x8, a), __builtin_bit_cast(bf16x8, b), c, 0, 0, 0);
}
DEV f32x16 mfma32(v4u a, v4u b, f32x16 c) {
  return __builtin_amdgcn_mfma_f32_32x32x16_bf16(__builtin_bit_cast(bf16x8, a), __builtin_bit_cast(bf16x8, b), c, 0, 0, 0);
}
DEV f32x4 smf(v4u a, v8u b, f32x4 c, int idx) {
  return __builtin_amdgcn_smfmac_f32_16x16x64_bf16(__builtin_bit_cast(bf16x8, a), __builtin_bit_cast(bf16x16, b), c, idx, 0, 0);
}
DEV v8u cat(v4u a, v4u b) { return __builtin_shufflevector(a, b, 0, 1, 2, 3, 4, 5, 6, 7); }
DEV v4u lo4(v8u a) { return __builtin_shufflevector(a, a, 0, 1, 2, 3); }
DEV v4u hi4(v8u a) { return __builtin_shufflevector(a, a, 4, 5, 6, 7); }
DEV v4u lds16(lds_t L, int o) { return *(LAS v4u*)(L + o); }
DEV void sts16(lds_t L, int o, v4u v) { *(LAS v4u*)(L + o) = v; }
DEV void sts8(lds_t L, int o, v2u v) { *(LAS v2u*)(L + o) = v; }
#define SCHED() __builtin_amdgcn_sched_barrier(0)
#define PIN_A(x) asm volatile("" : "+a"(x))
#define PIN_V(x) asm volatile("" : "+v"(x))
DEV int opq(int v) { asm volatile("" : "+v"(v)); return v; }
DEV int fresh_lane() {
  int l; asm volatile("v_mbcnt_lo_u32_b32 %0, -1, 0\n\tv_mbcnt_hi_u32_b32 %0, -1, %0" : "=v"(l)); return l;
}
DEV f32x4 fresh_zero() {
  f32x2 a, b;
  asm volatile("v_mov_b64 %0, 0" : "=v"(a));
  asm volatile("v_mov_b64 %0, 0" : "=v"(b));
  return __builtin_shufflevector(a, b, 0, 1, 2, 3);
}
DEV unsigned fresh_sent() { unsigned z; asm volatile("v_mov_b32 %0, -1" : "=v"(z)); return z; }
#define SMF "v_smfmac_f32_16x16x64_bf16 "
#define MF16 "v_mfma_f32_16x16x32_bf16 "
#define SMF1(cls, d, a, b) asm volatile(SMF "%0, %1, %2, %3" : "+v"(d) : "v"(a), cls(b), "v"(idx))
#define SMF1F(cls, lo, hi, d, a, b) asm volatile(SMF "v[" #lo ":" #hi "], %1, %2, %3" : "+{v[" #lo ":" #hi "]}"(d) : "v"(a), cls(b), "v"(idx))
#define AT_HOME(M, j) do { const int j_ = (j); if (j_ == 0) M("{v[32:39]}"); if (j_ == 1) M("{v[40:47]}"); if (j_ == 2) M("{v[48:55]}"); if (j_ == 3) M("{v[56:63]}"); if (j_ == 4) M("{v[64:71]}"); if (j_ == 5) M("{v[72:79]}"); if (j_ == 6) M("{v[80:87]}"); if (j_ == 7) M("{v[88:95]}"); if (j_ == 8) M("{v[96:103]}"); if (j_ == 9) M("{v[104:111]}"); if (j_ == 10) M("{v[112:119]}"); if (j_ == 11) M("{v[120:127]}"); if (j_ == 12) M("{v[128:135]}"); if (j_ == 13) M("{v[136:143]}"); if (j_ == 14) M("{v[144:151]}"); if (j_ == 15) M("{v[152:159]}"); } while (0)
#define MF1(d, a, b) asm volatile(MF16 "%0, %1, %2, %0" : "+v"(d) : "v"(a), "v"(b))
#define MF1Z(d, a, b) asm volatile(MF16 "%0, %1, %2, 0" : "=&v"(d) : "v"(a), "v"(b))
#define LGKM(n) asm volatile("s_waitcnt lgkmcnt(" #n ")" ::: "memory")
#define RAWBAR() do { __builtin_amdgcn_s_barrier(); asm volatile("" ::: "memory"); } while (0)
DEV void settle3(f32x4& s0, f32x4& s1, f32x4& s2) { asm volatile("s_nop 7" : "+v"(s0), "+v"(s1), "+v"(s2)); }
DEV void settle4(f32x4 (&d)[4]) { asm volatile("s_nop 7" : "+v"(d[0]), "+v"(d[1]), "+v"(d[2]), "+v"(d[3])); }
#define BAR() do { asm volatile("s_waitcnt lgkmcnt(0)" ::: "memory"); __builtin_amdgcn_s_barrier(); asm volatile("" ::: "memory"); } while (0)
#define VMWAIT(n) asm volatile("s_waitcnt vmcnt(" #n ")" ::: "memory")
DEV float add1(float a, float b) {
  float o; asm("v_add_f32 %0, %1, %2" : "=v"(o) : "v"(a), "v"(b)); return o;
}
DEV float xmax(float v) {
  auto r = __builtin_amdgcn_permlane32_swap(fu(v), fu(v), false, false); v = fmaxf(uf(r[0]), uf(r[1]));
  auto q = __builtin_amdgcn_permlane16_swap(fu(v), fu(v), false, false); return fmaxf(uf(q[0]), uf(q[1]));
}
DEV float xsum(float v) {
  auto r = __builtin_amdgcn_permlane32_swap(fu(v), fu(v), false, false); v = uf(r[0]) + uf(r[1]);
  auto q = __builtin_amdgcn_permlane16_swap(fu(v), fu(v), false, false); return uf(q[0]) + uf(q[1]);
}

DEV float ror8(float v) { return uf(__builtin_amdgcn_update_dpp(0u, fu(v), 0x128, 0xf, 0xf, true)); }
DEV float fmax2(float a, float b) { return __builtin_amdgcn_fmed3f(a, b, __builtin_inff()); }
DEV float fmax3(float a, float b, float c) { float r; asm("v_max3_f32 %0, %1, %2, %3" : "=v"(r) : "v"(a), "v"(b), "v"(c)); return r; }
DEV bool allok(unsigned t) { return __all(t != SENT); }

template <int N> struct IC { static constexpr int value = N; };
struct Ctx {
  gu32* ctrl;
  rsrc_t rs;
  lds_t L;
  int w, lane;
  int b, slot, att;
  unsigned ask;
  unsigned budget;
};
DEV KArg enter(Ctx& c) {
  const KArg a = args();
  c.ctrl = (gu32*)a->ctrl;
  c.rs = mkrs(a->data, R_END);
  asm volatile("" : "+s"(c.w), "+s"(c.b), "+s"(c.att), "+s"(c.slot));
  return a;
}
DEV bool spend(Ctx& c) {
  if (c.budget == 0u) return true;
  c.budget -= 1u;
  return false;
}
DEV unsigned wait_flag(Ctx& c, int flag, unsigned need) {
  gu32* f = c.ctrl + (C_FLAG + flag * 128) / 4;
  unsigned v;
#pragma unroll 1
  for (;;) {
    v = __builtin_amdgcn_readfirstlane(__hip_atomic_load(f, RLX_AGENT));
    if (v >= need || spend(c)) break;
    __builtin_amdgcn_s_sleep(2);
  }
  __builtin_amdgcn_fence(__ATOMIC_ACQUIRE, "wavefront");
  return v;
}
DEV void post_flag(Ctx& c, int flag, unsigned v, int off = 0) {
  st4s(mkrs((const void*)c.ctrl, CTRL_BYTES), (c.w | off | fresh_lane()) ? 1 << 30 : 0, C_FLAG + flag * 128, v);
}
DEV void claim(Ctx& c, unsigned votes) {
  if (c.w != 0) return;
  if (c.att == 0) {
#pragma unroll 1
    while (__builtin_amdgcn_readfirstlane(__hip_atomic_load(c.ctrl + C_ECHO / 4 + c.b, RLX_AGENT)) != c.ask) {
      if (spend(c)) break;
      __builtin_amdgcn_s_sleep(2);
    }
  }
  if (fresh_lane() == 0) __hip_atomic_fetch_add(c.ctrl + (C_CLAIM + c.att * 1024) / 4 + c.slot, 1u + votes, RLX_AGENT);
}
DEV unsigned wait_verdict(Ctx& c) {
  unsigned v;
#pragma unroll 1
  for (;;) {
    v = __builtin_amdgcn_readfirstlane(__hip_atomic_load(c.ctrl + (C_VERDICT + c.att * 128) / 4, RLX_AGENT));
    if ((v & 1u) || spend(c)) break;
    __builtin_amdgcn_s_sleep(2);
  }
  __builtin_amdgcn_fence(__ATOMIC_ACQUIRE, "wavefront");
  if ((v & 3u) == 3u && c.att == 0) {
    asm volatile("s_waitcnt vmcnt(0) lgkmcnt(0)\n\ts_barrier\n\ts_mov_b64 s[0:1], %0\n\ts_mov_b32 s2, %1\n\tv_mov_b32 v0, %2\n\t"
                 "s_getpc_b64 s[4:5]\n1:\n\ts_sub_u32 s4, s4, 1b-mk_mega\n\ts_subb_u32 s5, s5, 0\n\ts_setpc_b64 s[4:5]"
                 :: "s"(__builtin_amdgcn_kernarg_segment_ptr()), "s"(c.b + 256), "v"(c.w * 64 + fresh_lane()) : "s0", "s1", "s2", "s4", "s5", "v0", "scc", "memory");
    __builtin_unreachable();
  }
  return v;
}
DEV void serve(Ctx& c) {
  c.budget = SPIN_CAP;
  const int lane = fresh_lane();
  const rsrc_t rc = mkrs((const void*)c.ctrl, CTRL_BYTES);
  if (c.att == 0) {
#pragma unroll
    for (int i = C_CLAIM / 1024; i < C_ASK / 1024; ++i) st16s(rc, lane * 16, i * 1024, v4u{0, 0, 0, 0});
    VMWAIT(0);
  }
#pragma unroll 1
  for (;;) {
    asm volatile("" ::: "memory");
    const v4u cl = ld16s(rc, lane * 16, C_CLAIM + c.att * 1024);
    if (c.att == 0) {
      v4u e;
#pragma unroll
      for (int i = 0; i < 4; ++i) e[i] = __builtin_amdgcn_raw_buffer_load_b32(rc, (lane * 4 + i) * 64, C_ASK, AUX_SC1);
      st16s(rc, lane * 16, C_ECHO, e);
    }
    unsigned n = 0, one = 1, va = 0, vb = 0;
#pragma unroll
    for (int i = 0; i < 4; ++i) { n += cl[i] & 0xFFFFu; one &= (cl[i] & 0xFFFFu) == 1u; va += (cl[i] >> 16) & 1u; vb += (cl[i] >> 24) & 1u; }
    unsigned sum = n | (va << 12) | (vb << 22);
#pragma unroll
    for (int o = 1; o < 64; o <<= 1) sum += (unsigned)__shfl_xor((int)sum, o);
    sum = __builtin_amdgcn_readfirstlane(sum);
    if ((sum & 0xFFFu) >= 256u) {
      const unsigned v = 1u | (__all(one) ? 0u : 2u) | (((sum >> 12) & 0x3FFu) > 32u ? 4u : 0u) | ((sum >> 22) > 32u ? 8u : 0u);
      if (lane == 0) __hip_atomic_store(c.ctrl + (C_VERDICT + c.att * 128) / 4, v, RLX_AGENT);
      break;
    }
    if (spend(c)) break;
  }
}
DEV int vote_a(unsigned v) { return (int)((v >> 2) & 1u); }
DEV int vote_b(unsigned v) { return (int)((v >> 3) & 1u); }

#define STAMP(slot) do {} while (0)
#define TICK_DECL
#define TICK(i) do {} while (0)
#define TICK_SUM(n) do {} while (0)
#define TICK_OUT() do {} while (0)
#define TKS(x) ""
#define TK_CLOB
#define TK0 TKS("s_memtime s[74:75]\n\t")
#define TK1 TKS("s_memtime s[76:77]\n\t")
#define TK2 TKS("s_memtime s[78:79]\n\t")
#define TK3 TKS("s_memtime s[80:81]\n\t")
#define TK4 TKS("s_memtime s[82:83]\n\t")
#define TK5 TKS("s_memtime s[84:85]\n\t")
#define TK6 TKS("s_memtime s[86:87]\n\t")
#define TK7 TKS("s_memtime s[88:89]\n\t")
#define DIAG(slot, v) do {} while (0)
#define STS(x) ""
#define STC(x) ""
#define STF(x) ""

DEV constexpr int krow(int rho8, int i) { return 32 * (rho8 >> 2) + 8 * (rho8 & 1) + 2 * ((rho8 >> 1) & 1) + i; }
DEV int krow_lane(int g) { return 16 * (g >> 1) + 4 * (g & 1); }
DEV unsigned prm(unsigned hi, unsigned lo, int odd) { return __builtin_amdgcn_perm(hi, lo, odd ? 0x07060302u : 0x05040100u); }

#define ISBAD "v_cmp_eq_u32 vcc, -1, %[t]\n\t"
#define SPEND(lbl) "s_cmp_eq_u32 %[bd], 0\n\ts_cbranch_scc1 " lbl "\n\ts_sub_u32 %[bd], %[bd], 1\n\t"
#define KNOB(i, d) (d##u)
#define CQ0 300
#define CV0 200
#define C1 KNOB(0, 30)
#define C2 KNOB(1, 292)
#define CU KNOB(2, 40)
#define KLT KNOB(4, 508)
#define KLP KNOB(3, 90)
#define KH KNOB(5, 284)
#define ZDLY KNOB(7, 2)
#define ZRING 63
#define HACK KNOB(6, 0)
#define NLK 1
#define PACE 0, 0, C2, KLT, KLP, 0
DEV unsigned uni(unsigned v) { return __builtin_amdgcn_readfirstlane(v); }
#define ALL_REGS "=&{a[0:31]}"(wa[0]), "=&{a[32:63]}"(wa[1]), "=&{a[64:95]}"(wa[2]), "=&{a[96:127]}"(wa[3]), "=&{a[128:159]}"(wa[4]), "=&{a[160:191]}"(wa[5]), "=&{a[192:223]}"(wa[6]), \
    "=&{a[224:255]}"(wa[7]), "=&{v[32:63]}"(wv[0]), "=&{v[64:95]}"(wv[1]), "=&{v[96:127]}"(wv[2]), "=&{v[128:159]}"(wv[3]), "=&{v[224:255]}"(wv[4])
#define S_CLOB "s8", "s9", "s10", "s11", "s12", "s13", "s14", "s15", "s16", "s17", "s18", "s19", "s20", "s21", "s22", "s23", "s24", "s25", "s26", "s27", "s28", "s29", "s30", "s31", "s33", "s34", "s35", "s36", "s37", "s38", "s39", "s40", "s41", "s42", "s43", "s44", "s45", "s46", "s47", "s48", "s49", "s50", "s51", "s52", "s53", "s54", "s55", "s56", "s57", "s58", "s59", "s60", "s61", "s62", "s63", "s64", "s65", "s66", "s67", "s68", "s69", "s70", "s71", "s72", "s73"
#define ROFF 1
#define ROTS(x) (unsigned)((((x) + ROFF) & 3) << 21), (unsigned)((((x) + ROFF + 1) & 3) << 21), (unsigned)((((x) + ROFF + 2) & 3) << 21), (unsigned)((((x) + ROFF + 3) & 3) << 21)
#define RSRCS plo(p->data), phi(p->data), R_END, 0x00020000, plo(p->ctrl), phi(p->ctrl), CTRL_BYTES, 0x00020000
DEV unsigned plo(const void* q) { return (unsigned)(size_t)q; }
DEV unsigned phi(const void* q) { return (unsigned)((size_t)q >> 32); }

DEV constexpr int qk_r(int j) { return j < 4 ? 0 : (j - 4) % 3 + 1; }
DEV constexpr int qk_i(int j) { return j < 4 ? j : (j - 4) / 3; }
DEV void qk_role(Ctx& c, int h) {
  const KArg p = enter(c);
  constexpr int WL = 0, MEMS = 131072, KXL = 147456, QKX = 155648, QROW = 272, CTL = 160000;
  c.budget = SPIN_CAP;
  const int w = c.w, lane = fresh_lane(), g = lane >> 4, cc = lane & 15, q = (lane >> 2) & 3, pp = lane & 3;
  const int prow = (2 * g + (cc & 1)) * 2 + ((cc >> 1) & 1);
  v32u k;
#pragma unroll
  for (int i = 0; i < 32; ++i) k[i] = 0;
  k[0] = w * 1024 + lane * 16;
  k[1] = MEMS + w * 4096 + lane * 16;
#pragma unroll
  for (int r = 0; r < 3; ++r) k[2 + r] = MEMS + ((w + r + 1) & 3) * 4096 + lane * 16;
  k[5] = WL + w * 32768 + lane * 16;
  k[6] = (lane & 1) ? 0xEEEE : 0x4444;
  k[7] = w < 2 ? QKX + (4 * w + (cc >> 2)) * 256 + g * 64 + (cc & 3) * 4 : QKX + (8 + 2 * g) * QROW + (64 * (w & 1) + 4 * cc) * 2;
  k[8] = QKX + g * 16 + (8 + (cc & 7)) * QROW;
  k[9] = KXL + g * 512 + cc * 16;
  k[10] = w == 0 ? amap((h >> 1) * 1024 + ((2 * (h & 1) + (cc >> 3)) * 16 + prow) * 16 + ((cc >> 2) & 1) * 8, 0)
        : w == 1 ? amap(h * 512 + (g & 1) * 256 + cc * 16 + (g >> 1) * 8, 0) : w == 2 ? amap(16384 + h * 1024 + lane * 16, 0) : lane * 16;
  k[11] = w == 0 ? (cc < 8 ? amap(16384 + h * 128 + prow * 8 + (cc >> 2) * 4, 0) : 1 << 30)
        : w < 3 ? (g < 2 ? amap(49152 + ((w - 1) * 32 + h) * 256 + cc * 16 + g * 8, 0) : 1 << 30) : lane * 128;
  k[12] = w == 3 ? ((lane | h) ? 1 << 30 : C_PROG) : g * 512 + (16 * ((w + 1) & 1) + cc) * 16;
  k[13] = fu((w == 0 ? cc < 8 : g < 2) ? 1.0f : 0.0f);
  k[14] = (lane >= 4 * NLK || w) ? 1 << 30 : amap(24576 + lane * 4096 + h * 128, 0);
  k[15] = amap(24576 + (h >> 3) * 4096 + (lane & 31) * 128, 0);
  k[16] = CTL;
#pragma unroll
  for (int i = 0; i < 2; ++i) k[20 + i] = (lane >> 3) * 8192 + (h * 128 + 64 * (w & 1)) * 2 + 16 * ((lane & 7) ^ (4 * i + 2 * (lane >> 5)));
#pragma unroll
  for (int t = 0; t < 4; ++t) k[22 + t] = WL + w * 32768 + (8 * (q >> 1) + (q & 1) + krow_lane(g)) * 128 + 16 * ((2 * t + (pp >> 1)) ^ (4 * (q >> 1) + 2 * (g & 1))) + 8 * (pp & 1);
  const u16* W = (w >> 1) ? p->Wk : p->Wq;
  asm volatile(
      "s_add_u32 s50, s56, s64\n\ts_add_u32 m0, s49, 0\n\ts_add_u32 s48, s50, 0x0\n\tbuffer_load_dwordx4 v20, s[40:43], s48 offen  lds\n\ts_add_u32 m0, s49, 1024\n\t"
      "s_add_u32 s48, s50, 0x10000\n\tbuffer_load_dwordx4 v21, s[40:43], s48 offen  lds\n\ts_add_u32 m0, s49, 2048\n\ts_add_u32 s48, s50, 0x20000\n\t"
      "buffer_load_dwordx4 v20, s[40:43], s48 offen  lds\n\ts_add_u32 m0, s49, 3072\n\ts_add_u32 s48, s50, 0x30000\n\tbuffer_load_dwordx4 v21, s[40:43], s48 offen  lds\n\t"
      "s_add_u32 m0, s49, 4096\n\ts_add_u32 s48, s50, 0x40000\n\tbuffer_load_dwordx4 v20, s[40:43], s48 offen  lds\n\ts_add_u32 m0, s49, 5120\n\ts_add_u32 s48, s50, 0x50000\n\t"
      "buffer_load_dwordx4 v21, s[40:43], s48 offen  lds\n\ts_add_u32 m0, s49, 6144\n\ts_add_u32 s48, s50, 0x60000\n\tbuffer_load_dwordx4 v20, s[40:43], s48 offen  lds\n\t"
      "s_add_u32 m0, s49, 7168\n\ts_add_u32 s48, s50, 0x70000\n\tbuffer_load_dwordx4 v21, s[40:43], s48 offen  lds\n\ts_add_u32 s50, s56, s65\n\ts_add_u32 m0, s49, 8192\n\t"
      "s_add_u32 s48, s50, 0x0\n\tbuffer_load_dwordx4 v20, s[40:43], s48 offen  lds\n\ts_add_u32 m0, s49, 9216\n\ts_add_u32 s48, s50, 0x10000\n\t"
      "buffer_load_dwordx4 v21, s[40:43], s48 offen  lds\n\ts_add_u32 m0, s49, 10240\n\ts_add_u32 s48, s50, 0x20000\n\tbuffer_load_dwordx4 v20, s[40:43], s48 offen  lds\n\t"
      "s_add_u32 m0, s49, 11264\n\ts_add_u32 s48, s50, 0x30000\n\tbuffer_load_dwordx4 v21, s[40:43], s48 offen  lds\n\ts_add_u32 m0, s49, 12288\n\t"
      "s_add_u32 s48, s50, 0x40000\n\tbuffer_load_dwordx4 v20, s[40:43], s48 offen  lds\n\ts_add_u32 m0, s49, 13312\n\ts_add_u32 s48, s50, 0x50000\n\t"
      "buffer_load_dwordx4 v21, s[40:43], s48 offen  lds\n\ts_add_u32 m0, s49, 14336\n\ts_add_u32 s48, s50, 0x60000\n\tbuffer_load_dwordx4 v20, s[40:43], s48 offen  lds\n\t"
      "s_add_u32 m0, s49, 15360\n\ts_add_u32 s48, s50, 0x70000\n\tbuffer_load_dwordx4 v21, s[40:43], s48 offen  lds\n\ts_add_u32 s50, s56, s66\n\ts_add_u32 m0, s49, 16384\n\t"
      "s_add_u32 s48, s50, 0x0\n\tbuffer_load_dwordx4 v20, s[40:43], s48 offen  lds\n\ts_add_u32 m0, s49, 17408\n\ts_add_u32 s48, s50, 0x10000\n\t"
      "buffer_load_dwordx4 v21, s[40:43], s48 offen  lds\n\ts_add_u32 m0, s49, 18432\n\ts_add_u32 s48, s50, 0x20000\n\tbuffer_load_dwordx4 v20, s[40:43], s48 offen  lds\n\t"
      "s_add_u32 m0, s49, 19456\n\ts_add_u32 s48, s50, 0x30000\n\tbuffer_load_dwordx4 v21, s[40:43], s48 offen  lds\n\ts_add_u32 m0, s49, 20480\n\t"
      "s_add_u32 s48, s50, 0x40000\n\tbuffer_load_dwordx4 v20, s[40:43], s48 offen  lds\n\ts_add_u32 m0, s49, 21504\n\ts_add_u32 s48, s50, 0x50000\n\t"
      "buffer_load_dwordx4 v21, s[40:43], s48 offen  lds\n\ts_add_u32 m0, s49, 22528\n\ts_add_u32 s48, s50, 0x60000\n\tbuffer_load_dwordx4 v20, s[40:43], s48 offen  lds\n\t"
      "s_add_u32 m0, s49, 23552\n\ts_add_u32 s48, s50, 0x70000\n\tbuffer_load_dwordx4 v21, s[40:43], s48 offen  lds\n\ts_add_u32 s50, s56, s67\n\ts_add_u32 m0, s49, 24576\n\t"
      "s_add_u32 s48, s50, 0x0\n\tbuffer_load_dwordx4 v20, s[40:43], s48 offen  lds\n\ts_add_u32 m0, s49, 25600\n\ts_add_u32 s48, s50, 0x10000\n\t"
      "buffer_load_dwordx4 v21, s[40:43], s48 offen  lds\n\ts_add_u32 m0, s49, 26624\n\ts_add_u32 s48, s50, 0x20000\n\tbuffer_load_dwordx4 v20, s[40:43], s48 offen  lds\n\t"
      "s_add_u32 m0, s49, 27648\n\ts_add_u32 s48, s50, 0x30000\n\tbuffer_load_dwordx4 v21, s[40:43], s48 offen  lds\n\ts_add_u32 m0, s49, 28672\n\t"
      "s_add_u32 s48, s50, 0x40000\n\tbuffer_load_dwordx4 v20, s[40:43], s48 offen  lds\n\ts_add_u32 m0, s49, 29696\n\ts_add_u32 s48, s50, 0x50000\n\t"
      "buffer_load_dwordx4 v21, s[40:43], s48 offen  lds\n\ts_add_u32 m0, s49, 30720\n\ts_add_u32 s48, s50, 0x60000\n\tbuffer_load_dwordx4 v20, s[40:43], s48 offen  lds\n\t"
      "s_add_u32 m0, s49, 31744\n\ts_add_u32 s48, s50, 0x70000\n\tbuffer_load_dwordx4 v21, s[40:43], s48 offen  lds\n\t"
      :: "{v[20:21]}"(v2u{k[20], k[21]}), "{s[40:43]}"(mkrs(W, 1024 * 4096 * 2)), "{s49}"(WL + w * 32768), "{s56}"(w << 19), "{s[64:67]}"(v4u{ROTS(h >> 3)})
      : "memory", "scc", "s48", "s50");
  claim(c, 0);
  constexpr float SC = 0.08838834764831845f, CE = 1.4426950408889634f * SC;
  v32u s0 = {RSRCS, (unsigned)w, ~0u, c.budget, 0, CQ0 * 16, 0, (unsigned)(R_QX + hblock(h) + (GRN - 1) * 16384), C2, KLT, KLP, 0, 0, fu(CE), fu(CE), fu(-1e9f / SC), (unsigned)(R_KX + hblock(h))};
#pragma unroll
  for (int i = 0; i < 4; ++i) { const unsigned long long m = __ballot(4 * g + i <= cc); s0[24 + 2 * i] = (unsigned)m; s0[25 + 2 * i] = (unsigned)(m >> 32); }
  v32u s1 = {plo(W), phi(W), 1024 * 4096 * 2, 0x00020000, 0, (unsigned)(C_FLAG + (F_Q + h) * 128), 0, 0, 0, (unsigned)(WL + w * 32768), 0, 0, 0, 0, 0, 0,
             (unsigned)(w << 19), (unsigned)(((w + 1) & 3) << 19), (unsigned)(((w + 2) & 3) << 19), (unsigned)(((w + 3) & 3) << 19), plo((const void*)p), phi((const void*)p),
             c.att ? 0u : (unsigned)c.b + 256u, (unsigned)(C_VERDICT + c.att * 128), ROTS(h >> 3)};
  v32u wa[8], wv[5];
  asm volatile(
      "s_nop 0\n\tv_readfirstlane_b32 s8, v160\n\tv_readfirstlane_b32 s9, v161\n\tv_readfirstlane_b32 s10, v162\n\tv_readfirstlane_b32 s11, v163\n\t"
      "v_readfirstlane_b32 s12, v164\n\tv_readfirstlane_b32 s13, v165\n\tv_readfirstlane_b32 s14, v166\n\tv_readfirstlane_b32 s15, v167\n\tv_readfirstlane_b32 s16, v168\n\t"
      "v_readfirstlane_b32 s17, v169\n\tv_readfirstlane_b32 s18, v170\n\tv_readfirstlane_b32 s19, v171\n\tv_readfirstlane_b32 s20, v172\n\tv_readfirstlane_b32 s21, v173\n\t"
      "v_readfirstlane_b32 s22, v174\n\tv_readfirstlane_b32 s23, v175\n\tv_readfirstlane_b32 s24, v176\n\tv_readfirstlane_b32 s25, v177\n\tv_readfirstlane_b32 s26, v178\n\t"
      "v_readfirstlane_b32 s27, v179\n\tv_readfirstlane_b32 s28, v180\n\tv_readfirstlane_b32 s29, v181\n\tv_readfirstlane_b32 s30, v182\n\tv_readfirstlane_b32 s31, v183\n\t"
      "v_readfirstlane_b32 s32, v184\n\tv_readfirstlane_b32 s33, v185\n\tv_readfirstlane_b32 s34, v186\n\tv_readfirstlane_b32 s35, v187\n\tv_readfirstlane_b32 s36, v188\n\t"
      "v_readfirstlane_b32 s37, v189\n\tv_readfirstlane_b32 s38, v190\n\tv_readfirstlane_b32 s39, v191\n\tv_readfirstlane_b32 s40, v192\n\tv_readfirstlane_b32 s41, v193\n\t"
      "v_readfirstlane_b32 s42, v194\n\tv_readfirstlane_b32 s43, v195\n\tv_readfirstlane_b32 s45, v197\n\tv_readfirstlane_b32 s49, v201\n\tv_readfirstlane_b32 s54, v206\n\t"
      "v_readfirstlane_b32 s55, v207\n\tv_readfirstlane_b32 s56, v208\n\tv_readfirstlane_b32 s57, v209\n\tv_readfirstlane_b32 s58, v210\n\tv_readfirstlane_b32 s59, v211\n\t"
      "v_readfirstlane_b32 s60, v212\n\tv_readfirstlane_b32 s61, v213\n\tv_readfirstlane_b32 s62, v214\n\tv_readfirstlane_b32 s63, v215\n\tv_readfirstlane_b32 s64, v216\n\t"
      "v_readfirstlane_b32 s65, v217\n\tv_readfirstlane_b32 s66, v218\n\tv_readfirstlane_b32 s67, v219\n\ts_nop 4\n\t"
      TKS("s_mul_i32 s48, s16, 72\n\ts_add_u32 s48, s48, 162080\n\tv_mov_b32 v224, s48\n\tv_mov_b32 v225, s74\n\tds_write_b32 v224, v225 offset:0\n\tv_mov_b32 v225, s75\n\tds_write_b32 v224, v225 offset:4\n\tv_mov_b32 v225, s76\n\tds_write_b32 v224, v225 offset:8\n\tv_mov_b32 v225, s77\n\tds_write_b32 v224, v225 offset:12\n\tv_mov_b32 v225, s78\n\tds_write_b32 v224, v225 offset:16\n\tv_mov_b32 v225, s79\n\tds_write_b32 v224, v225 offset:20\n\tv_mov_b32 v225, s80\n\tds_write_b32 v224, v225 offset:24\n\tv_mov_b32 v225, s81\n\tds_write_b32 v224, v225 offset:28\n\tv_mov_b32 v225, s82\n\tds_write_b32 v224, v225 offset:32\n\tv_mov_b32 v225, s83\n\tds_write_b32 v224, v225 offset:36\n\tv_mov_b32 v225, s84\n\tds_write_b32 v224, v225 offset:40\n\tv_mov_b32 v225, s85\n\tds_write_b32 v224, v225 offset:44\n\tv_mov_b32 v225, s86\n\tds_write_b32 v224, v225 offset:48\n\tv_mov_b32 v225, s87\n\tds_write_b32 v224, v225 offset:52\n\tv_mov_b32 v225, s88\n\tds_write_b32 v224, v225 offset:56\n\tv_mov_b32 v225, s89\n\tds_write_b32 v224, v225 offset:60\n\tv_mov_b32 v225, s90\n\tds_write_b32 v224, v225 offset:64\n\tv_mov_b32 v225, s91\n\tds_write_b32 v224, v225 offset:68\n\t")
      "s_waitcnt vmcnt(24)\n\tds_read_b64_tr_b16 a[0:1], v22 offset:0\n\tds_read_b64_tr_b16 a[2:3], v22 offset:256\n\tds_read_b64_tr_b16 a[4:5], v22 offset:4096\n\t"
      "ds_read_b64_tr_b16 a[6:7], v22 offset:4352\n\tds_read_b64_tr_b16 a[8:9], v23 offset:0\n\tds_read_b64_tr_b16 a[10:11], v23 offset:256\n\t"
      "ds_read_b64_tr_b16 a[12:13], v23 offset:4096\n\tds_read_b64_tr_b16 a[14:15], v23 offset:4352\n\tds_read_b64_tr_b16 v[32:33], v24 offset:0\n\t"
      "ds_read_b64_tr_b16 v[34:35], v24 offset:256\n\tds_read_b64_tr_b16 v[36:37], v24 offset:4096\n\tds_read_b64_tr_b16 v[38:39], v24 offset:4352\n\t"
      "ds_read_b64_tr_b16 v[152:153], v25 offset:0\n\tds_read_b64_tr_b16 v[154:155], v25 offset:256\n\tds_read_b64_tr_b16 v[156:157], v25 offset:4096\n\t"
      "ds_read_b64_tr_b16 v[158:159], v25 offset:4352\n\ts_waitcnt lgkmcnt(0)\n\ts_add_u32 s50, s57, s64\n\ts_add_u32 m0, s49, 0\n\ts_add_u32 s48, s50, 0x0\n\t"
      "buffer_load_dwordx4 v20, s[40:43], s48 offen  lds\n\ts_add_u32 m0, s49, 1024\n\ts_add_u32 s48, s50, 0x10000\n\tbuffer_load_dwordx4 v21, s[40:43], s48 offen  lds\n\t"
      "s_add_u32 m0, s49, 2048\n\ts_add_u32 s48, s50, 0x20000\n\tbuffer_load_dwordx4 v20, s[40:43], s48 offen  lds\n\ts_add_u32 m0, s49, 3072\n\ts_add_u32 s48, s50, 0x30000\n\t"
      "buffer_load_dwordx4 v21, s[40:43], s48 offen  lds\n\ts_add_u32 m0, s49, 4096\n\ts_add_u32 s48, s50, 0x40000\n\tbuffer_load_dwordx4 v20, s[40:43], s48 offen  lds\n\t"
      "s_add_u32 m0, s49, 5120\n\ts_add_u32 s48, s50, 0x50000\n\tbuffer_load_dwordx4 v21, s[40:43], s48 offen  lds\n\ts_add_u32 m0, s49, 6144\n\ts_add_u32 s48, s50, 0x60000\n\t"
      "buffer_load_dwordx4 v20, s[40:43], s48 offen  lds\n\ts_add_u32 m0, s49, 7168\n\ts_add_u32 s48, s50, 0x70000\n\tbuffer_load_dwordx4 v21, s[40:43], s48 offen  lds\n\t"
      "s_waitcnt vmcnt(24)\n\tds_read_b64_tr_b16 a[16:17], v22 offset:8192\n\tds_read_b64_tr_b16 a[18:19], v22 offset:8448\n\tds_read_b64_tr_b16 a[20:21], v22 offset:12288\n\t"
      "ds_read_b64_tr_b16 a[22:23], v22 offset:12544\n\tds_read_b64_tr_b16 a[24:25], v23 offset:8192\n\tds_read_b64_tr_b16 a[26:27], v23 offset:8448\n\t"
      "ds_read_b64_tr_b16 a[28:29], v23 offset:12288\n\tds_read_b64_tr_b16 a[30:31], v23 offset:12544\n\tds_read_b64_tr_b16 v[40:41], v24 offset:8192\n\t"
      "ds_read_b64_tr_b16 v[42:43], v24 offset:8448\n\tds_read_b64_tr_b16 v[44:45], v24 offset:12288\n\tds_read_b64_tr_b16 v[46:47], v24 offset:12544\n\t"
      "ds_read_b64_tr_b16 v[160:161], v25 offset:8192\n\tds_read_b64_tr_b16 v[162:163], v25 offset:8448\n\tds_read_b64_tr_b16 v[164:165], v25 offset:12288\n\t"
      "ds_read_b64_tr_b16 v[166:167], v25 offset:12544\n\ts_waitcnt lgkmcnt(0)\n\ts_add_u32 s50, s58, s64\n\ts_add_u32 m0, s49, 8192\n\ts_add_u32 s48, s50, 0x0\n\t"
      "buffer_load_dwordx4 v20, s[40:43], s48 offen  lds\n\ts_add_u32 m0, s49, 9216\n\ts_add_u32 s48, s50, 0x10000\n\tbuffer_load_dwordx4 v21, s[40:43], s48 offen  lds\n\t"
      "s_add_u32 m0, s49, 10240\n\ts_add_u32 s48, s50, 0x20000\n\tbuffer_load_dwordx4 v20, s[40:43], s48 offen  lds\n\ts_add_u32 m0, s49, 11264\n\t"
      "s_add_u32 s48, s50, 0x30000\n\tbuffer_load_dwordx4 v21, s[40:43], s48 offen  lds\n\ts_add_u32 m0, s49, 12288\n\ts_add_u32 s48, s50, 0x40000\n\t"
      "buffer_load_dwordx4 v20, s[40:43], s48 offen  lds\n\ts_add_u32 m0, s49, 13312\n\ts_add_u32 s48, s50, 0x50000\n\tbuffer_load_dwordx4 v21, s[40:43], s48 offen  lds\n\t"
      "s_add_u32 m0, s49, 14336\n\ts_add_u32 s48, s50, 0x60000\n\tbuffer_load_dwordx4 v20, s[40:43], s48 offen  lds\n\ts_add_u32 m0, s49, 15360\n\t"
      "s_add_u32 s48, s50, 0x70000\n\tbuffer_load_dwordx4 v21, s[40:43], s48 offen  lds\n\ts_waitcnt vmcnt(24)\n\tds_read_b64_tr_b16 a[32:33], v22 offset:16384\n\t"
      "ds_read_b64_tr_b16 a[34:35], v22 offset:16640\n\tds_read_b64_tr_b16 a[36:37], v22 offset:20480\n\tds_read_b64_tr_b16 a[38:39], v22 offset:20736\n\t"
      "ds_read_b64_tr_b16 a[40:41], v23 offset:16384\n\tds_read_b64_tr_b16 a[42:43], v23 offset:16640\n\tds_read_b64_tr_b16 a[44:45], v23 offset:20480\n\t"
      "ds_read_b64_tr_b16 a[46:47], v23 offset:20736\n\tds_read_b64_tr_b16 v[48:49], v24 offset:16384\n\tds_read_b64_tr_b16 v[50:51], v24 offset:16640\n\t"
      "ds_read_b64_tr_b16 v[52:53], v24 offset:20480\n\tds_read_b64_tr_b16 v[54:55], v24 offset:20736\n\tds_read_b64_tr_b16 v[168:169], v25 offset:16384\n\t"
      "ds_read_b64_tr_b16 v[170:171], v25 offset:16640\n\tds_read_b64_tr_b16 v[172:173], v25 offset:20480\n\tds_read_b64_tr_b16 v[174:175], v25 offset:20736\n\t"
      "s_waitcnt lgkmcnt(0)\n\ts_add_u32 s50, s59, s64\n\ts_add_u32 m0, s49, 16384\n\ts_add_u32 s48, s50, 0x0\n\tbuffer_load_dwordx4 v20, s[40:43], s48 offen  lds\n\t"
      "s_add_u32 m0, s49, 17408\n\ts_add_u32 s48, s50, 0x10000\n\tbuffer_load_dwordx4 v21, s[40:43], s48 offen  lds\n\ts_add_u32 m0, s49, 18432\n\t"
      "s_add_u32 s48, s50, 0x20000\n\tbuffer_load_dwordx4 v20, s[40:43], s48 offen  lds\n\ts_add_u32 m0, s49, 19456\n\ts_add_u32 s48, s50, 0x30000\n\t"
      "buffer_load_dwordx4 v21, s[40:43], s48 offen  lds\n\ts_add_u32 m0, s49, 20480\n\ts_add_u32 s48, s50, 0x40000\n\tbuffer_load_dwordx4 v20, s[40:43], s48 offen  lds\n\t"
      "s_add_u32 m0, s49, 21504\n\ts_add_u32 s48, s50, 0x50000\n\tbuffer_load_dwordx4 v21, s[40:43], s48 offen  lds\n\ts_add_u32 m0, s49, 22528\n\t"
      "s_add_u32 s48, s50, 0x60000\n\tbuffer_load_dwordx4 v20, s[40:43], s48 offen  lds\n\ts_add_u32 m0, s49, 23552\n\ts_add_u32 s48, s50, 0x70000\n\t"
      "buffer_load_dwordx4 v21, s[40:43], s48 offen  lds\n\ts_waitcnt vmcnt(24)\n\tds_read_b64_tr_b16 a[48:49], v22 offset:24576\n\t"
      "ds_read_b64_tr_b16 a[50:51], v22 offset:24832\n\tds_read_b64_tr_b16 a[52:53], v22 offset:28672\n\tds_read_b64_tr_b16 a[54:55], v22 offset:28928\n\t"
      "ds_read_b64_tr_b16 a[56:57], v23 offset:24576\n\tds_read_b64_tr_b16 a[58:59], v23 offset:24832\n\tds_read_b64_tr_b16 a[60:61], v23 offset:28672\n\t"
      "ds_read_b64_tr_b16 a[62:63], v23 offset:28928\n\tds_read_b64_tr_b16 v[56:57], v24 offset:24576\n\tds_read_b64_tr_b16 v[58:59], v24 offset:24832\n\t"
      "ds_read_b64_tr_b16 v[60:61], v24 offset:28672\n\tds_read_b64_tr_b16 v[62:63], v24 offset:28928\n\tds_read_b64_tr_b16 v[176:177], v25 offset:24576\n\t"
      "ds_read_b64_tr_b16 v[178:179], v25 offset:24832\n\tds_read_b64_tr_b16 v[180:181], v25 offset:28672\n\tds_read_b64_tr_b16 v[182:183], v25 offset:28928\n\t"
      "s_waitcnt lgkmcnt(0)\n\ts_add_u32 s50, s57, s65\n\ts_add_u32 m0, s49, 24576\n\ts_add_u32 s48, s50, 0x0\n\tbuffer_load_dwordx4 v20, s[40:43], s48 offen  lds\n\t"
      "s_add_u32 m0, s49, 25600\n\ts_add_u32 s48, s50, 0x10000\n\tbuffer_load_dwordx4 v21, s[40:43], s48 offen  lds\n\ts_add_u32 m0, s49, 26624\n\t"
      "s_add_u32 s48, s50, 0x20000\n\tbuffer_load_dwordx4 v20, s[40:43], s48 offen  lds\n\ts_add_u32 m0, s49, 27648\n\ts_add_u32 s48, s50, 0x30000\n\t"
      "buffer_load_dwordx4 v21, s[40:43], s48 offen  lds\n\ts_add_u32 m0, s49, 28672\n\ts_add_u32 s48, s50, 0x40000\n\tbuffer_load_dwordx4 v20, s[40:43], s48 offen  lds\n\t"
      "s_add_u32 m0, s49, 29696\n\ts_add_u32 s48, s50, 0x50000\n\tbuffer_load_dwordx4 v21, s[40:43], s48 offen  lds\n\ts_add_u32 m0, s49, 30720\n\t"
      "s_add_u32 s48, s50, 0x60000\n\tbuffer_load_dwordx4 v20, s[40:43], s48 offen  lds\n\ts_add_u32 m0, s49, 31744\n\ts_add_u32 s48, s50, 0x70000\n\t"
      "buffer_load_dwordx4 v21, s[40:43], s48 offen  lds\n\ts_waitcnt vmcnt(24)\n\tds_read_b64_tr_b16 a[64:65], v22 offset:0\n\tds_read_b64_tr_b16 a[66:67], v22 offset:256\n\t"
      "ds_read_b64_tr_b16 a[68:69], v22 offset:4096\n\tds_read_b64_tr_b16 a[70:71], v22 offset:4352\n\tds_read_b64_tr_b16 a[72:73], v23 offset:0\n\t"
      "ds_read_b64_tr_b16 a[74:75], v23 offset:256\n\tds_read_b64_tr_b16 a[76:77], v23 offset:4096\n\tds_read_b64_tr_b16 a[78:79], v23 offset:4352\n\t"
      "ds_read_b64_tr_b16 v[64:65], v24 offset:0\n\tds_read_b64_tr_b16 v[66:67], v24 offset:256\n\tds_read_b64_tr_b16 v[68:69], v24 offset:4096\n\t"
      "ds_read_b64_tr_b16 v[70:71], v24 offset:4352\n\tds_read_b64_tr_b16 v[184:185], v25 offset:0\n\tds_read_b64_tr_b16 v[186:187], v25 offset:256\n\t"
      "ds_read_b64_tr_b16 v[188:189], v25 offset:4096\n\tds_read_b64_tr_b16 v[190:191], v25 offset:4352\n\ts_waitcnt lgkmcnt(0)\n\ts_add_u32 s50, s58, s65\n\t"
      "s_add_u32 m0, s49, 0\n\ts_add_u32 s48, s50, 0x0\n\tbuffer_load_dwordx4 v20, s[40:43], s48 offen  lds\n\ts_add_u32 m0, s49, 1024\n\ts_add_u32 s48, s50, 0x10000\n\t"
      "buffer_load_dwordx4 v21, s[40:43], s48 offen  lds\n\ts_add_u32 m0, s49, 2048\n\ts_add_u32 s48, s50, 0x20000\n\tbuffer_load_dwordx4 v20, s[40:43], s48 offen  lds\n\t"
      "s_add_u32 m0, s49, 3072\n\ts_add_u32 s48, s50, 0x30000\n\tbuffer_load_dwordx4 v21, s[40:43], s48 offen  lds\n\ts_add_u32 m0, s49, 4096\n\ts_add_u32 s48, s50, 0x40000\n\t"
      "buffer_load_dwordx4 v20, s[40:43], s48 offen  lds\n\ts_add_u32 m0, s49, 5120\n\ts_add_u32 s48, s50, 0x50000\n\tbuffer_load_dwordx4 v21, s[40:43], s48 offen  lds\n\t"
      "s_add_u32 m0, s49, 6144\n\ts_add_u32 s48, s50, 0x60000\n\tbuffer_load_dwordx4 v20, s[40:43], s48 offen  lds\n\ts_add_u32 m0, s49, 7168\n\ts_add_u32 s48, s50, 0x70000\n\t"
      "buffer_load_dwordx4 v21, s[40:43], s48 offen  lds\n\ts_waitcnt vmcnt(24)\n\tds_read_b64_tr_b16 a[80:81], v22 offset:8192\n\t"
      "ds_read_b64_tr_b16 a[82:83], v22 offset:8448\n\tds_read_b64_tr_b16 a[84:85], v22 offset:12288\n\tds_read_b64_tr_b16 a[86:87], v22 offset:12544\n\t"
      "ds_read_b64_tr_b16 a[88:89], v23 offset:8192\n\tds_read_b64_tr_b16 a[90:91], v23 offset:8448\n\tds_read_b64_tr_b16 a[92:93], v23 offset:12288\n\t"
      "ds_read_b64_tr_b16 a[94:95], v23 offset:12544\n\tds_read_b64_tr_b16 v[72:73], v24 offset:8192\n\tds_read_b64_tr_b16 v[74:75], v24 offset:8448\n\t"
      "ds_read_b64_tr_b16 v[76:77], v24 offset:12288\n\tds_read_b64_tr_b16 v[78:79], v24 offset:12544\n\tds_read_b64_tr_b16 v[192:193], v25 offset:8192\n\t"
      "ds_read_b64_tr_b16 v[194:195], v25 offset:8448\n\tds_read_b64_tr_b16 v[196:197], v25 offset:12288\n\tds_read_b64_tr_b16 v[198:199], v25 offset:12544\n\t"
      "s_waitcnt lgkmcnt(0)\n\ts_add_u32 s50, s59, s65\n\ts_add_u32 m0, s49, 8192\n\ts_add_u32 s48, s50, 0x0\n\tbuffer_load_dwordx4 v20, s[40:43], s48 offen  lds\n\t"
      "s_add_u32 m0, s49, 9216\n\ts_add_u32 s48, s50, 0x10000\n\tbuffer_load_dwordx4 v21, s[40:43], s48 offen  lds\n\ts_add_u32 m0, s49, 10240\n\ts_add_u32 s48, s50, 0x20000\n\t"
      "buffer_load_dwordx4 v20, s[40:43], s48 offen  lds\n\ts_add_u32 m0, s49, 11264\n\ts_add_u32 s48, s50, 0x30000\n\tbuffer_load_dwordx4 v21, s[40:43], s48 offen  lds\n\t"
      "s_add_u32 m0, s49, 12288\n\ts_add_u32 s48, s50, 0x40000\n\tbuffer_load_dwordx4 v20, s[40:43], s48 offen  lds\n\ts_add_u32 m0, s49, 13312\n\t"
      "s_add_u32 s48, s50, 0x50000\n\tbuffer_load_dwordx4 v21, s[40:43], s48 offen  lds\n\ts_add_u32 m0, s49, 14336\n\ts_add_u32 s48, s50, 0x60000\n\t"
      "buffer_load_dwordx4 v20, s[40:43], s48 offen  lds\n\ts_add_u32 m0, s49, 15360\n\ts_add_u32 s48, s50, 0x70000\n\tbuffer_load_dwordx4 v21, s[40:43], s48 offen  lds\n\t"
      "s_waitcnt vmcnt(24)\n\tds_read_b64_tr_b16 a[96:97], v22 offset:16384\n\tds_read_b64_tr_b16 a[98:99], v22 offset:16640\n\t"
      "ds_read_b64_tr_b16 a[100:101], v22 offset:20480\n\tds_read_b64_tr_b16 a[102:103], v22 offset:20736\n\tds_read_b64_tr_b16 a[104:105], v23 offset:16384\n\t"
      "ds_read_b64_tr_b16 a[106:107], v23 offset:16640\n\tds_read_b64_tr_b16 a[108:109], v23 offset:20480\n\tds_read_b64_tr_b16 a[110:111], v23 offset:20736\n\t"
      "ds_read_b64_tr_b16 v[80:81], v24 offset:16384\n\tds_read_b64_tr_b16 v[82:83], v24 offset:16640\n\tds_read_b64_tr_b16 v[84:85], v24 offset:20480\n\t"
      "ds_read_b64_tr_b16 v[86:87], v24 offset:20736\n\tds_read_b64_tr_b16 v[200:201], v25 offset:16384\n\tds_read_b64_tr_b16 v[202:203], v25 offset:16640\n\t"
      "ds_read_b64_tr_b16 v[204:205], v25 offset:20480\n\tds_read_b64_tr_b16 v[206:207], v25 offset:20736\n\ts_waitcnt lgkmcnt(0)\n\ts_add_u32 s50, s57, s66\n\t"
      "s_add_u32 m0, s49, 16384\n\ts_add_u32 s48, s50, 0x0\n\tbuffer_load_dwordx4 v20, s[40:43], s48 offen  lds\n\ts_add_u32 m0, s49, 17408\n\ts_add_u32 s48, s50, 0x10000\n\t"
      "buffer_load_dwordx4 v21, s[40:43], s48 offen  lds\n\ts_add_u32 m0, s49, 18432\n\ts_add_u32 s48, s50, 0x20000\n\tbuffer_load_dwordx4 v20, s[40:43], s48 offen  lds\n\t"
      "s_add_u32 m0, s49, 19456\n\ts_add_u32 s48, s50, 0x30000\n\tbuffer_load_dwordx4 v21, s[40:43], s48 offen  lds\n\ts_add_u32 m0, s49, 20480\n\t"
      "s_add_u32 s48, s50, 0x40000\n\tbuffer_load_dwordx4 v20, s[40:43], s48 offen  lds\n\ts_add_u32 m0, s49, 21504\n\ts_add_u32 s48, s50, 0x50000\n\t"
      "buffer_load_dwordx4 v21, s[40:43], s48 offen  lds\n\ts_add_u32 m0, s49, 22528\n\ts_add_u32 s48, s50, 0x60000\n\tbuffer_load_dwordx4 v20, s[40:43], s48 offen  lds\n\t"
      "s_add_u32 m0, s49, 23552\n\ts_add_u32 s48, s50, 0x70000\n\tbuffer_load_dwordx4 v21, s[40:43], s48 offen  lds\n\ts_waitcnt vmcnt(24)\n\t"
      "ds_read_b64_tr_b16 a[112:113], v22 offset:24576\n\tds_read_b64_tr_b16 a[114:115], v22 offset:24832\n\tds_read_b64_tr_b16 a[116:117], v22 offset:28672\n\t"
      "ds_read_b64_tr_b16 a[118:119], v22 offset:28928\n\tds_read_b64_tr_b16 a[120:121], v23 offset:24576\n\tds_read_b64_tr_b16 a[122:123], v23 offset:24832\n\t"
      "ds_read_b64_tr_b16 a[124:125], v23 offset:28672\n\tds_read_b64_tr_b16 a[126:127], v23 offset:28928\n\tds_read_b64_tr_b16 v[88:89], v24 offset:24576\n\t"
      "ds_read_b64_tr_b16 v[90:91], v24 offset:24832\n\tds_read_b64_tr_b16 v[92:93], v24 offset:28672\n\tds_read_b64_tr_b16 v[94:95], v24 offset:28928\n\t"
      "ds_read_b64_tr_b16 v[208:209], v25 offset:24576\n\tds_read_b64_tr_b16 v[210:211], v25 offset:24832\n\tds_read_b64_tr_b16 v[212:213], v25 offset:28672\n\t"
      "ds_read_b64_tr_b16 v[214:215], v25 offset:28928\n\ts_waitcnt lgkmcnt(0)\n\ts_add_u32 s50, s58, s66\n\ts_add_u32 m0, s49, 24576\n\ts_add_u32 s48, s50, 0x0\n\t"
      "buffer_load_dwordx4 v20, s[40:43], s48 offen  lds\n\ts_add_u32 m0, s49, 25600\n\ts_add_u32 s48, s50, 0x10000\n\tbuffer_load_dwordx4 v21, s[40:43], s48 offen  lds\n\t"
      "s_add_u32 m0, s49, 26624\n\ts_add_u32 s48, s50, 0x20000\n\tbuffer_load_dwordx4 v20, s[40:43], s48 offen  lds\n\ts_add_u32 m0, s49, 27648\n\t"
      "s_add_u32 s48, s50, 0x30000\n\tbuffer_load_dwordx4 v21, s[40:43], s48 offen  lds\n\ts_add_u32 m0, s49, 28672\n\ts_add_u32 s48, s50, 0x40000\n\t"
      "buffer_load_dwordx4 v20, s[40:43], s48 offen  lds\n\ts_add_u32 m0, s49, 29696\n\ts_add_u32 s48, s50, 0x50000\n\tbuffer_load_dwordx4 v21, s[40:43], s48 offen  lds\n\t"
      "s_add_u32 m0, s49, 30720\n\ts_add_u32 s48, s50, 0x60000\n\tbuffer_load_dwordx4 v20, s[40:43], s48 offen  lds\n\ts_add_u32 m0, s49, 31744\n\t"
      "s_add_u32 s48, s50, 0x70000\n\tbuffer_load_dwordx4 v21, s[40:43], s48 offen  lds\n\ts_waitcnt vmcnt(24)\n\tds_read_b64_tr_b16 a[128:129], v22 offset:0\n\t"
      "ds_read_b64_tr_b16 a[130:131], v22 offset:256\n\tds_read_b64_tr_b16 a[132:133], v22 offset:4096\n\tds_read_b64_tr_b16 a[134:135], v22 offset:4352\n\t"
      "ds_read_b64_tr_b16 a[136:137], v23 offset:0\n\tds_read_b64_tr_b16 a[138:139], v23 offset:256\n\tds_read_b64_tr_b16 a[140:141], v23 offset:4096\n\t"
      "ds_read_b64_tr_b16 a[142:143], v23 offset:4352\n\tds_read_b64_tr_b16 v[96:97], v24 offset:0\n\tds_read_b64_tr_b16 v[98:99], v24 offset:256\n\t"
      "ds_read_b64_tr_b16 v[100:101], v24 offset:4096\n\tds_read_b64_tr_b16 v[102:103], v24 offset:4352\n\tds_read_b64_tr_b16 v[216:217], v25 offset:0\n\t"
      "ds_read_b64_tr_b16 v[218:219], v25 offset:256\n\tds_read_b64_tr_b16 v[220:221], v25 offset:4096\n\tds_read_b64_tr_b16 v[222:223], v25 offset:4352\n\t"
      "s_waitcnt lgkmcnt(0)\n\ts_add_u32 s50, s59, s66\n\ts_add_u32 m0, s49, 0\n\ts_add_u32 s48, s50, 0x0\n\tbuffer_load_dwordx4 v20, s[40:43], s48 offen  lds\n\t"
      "s_add_u32 m0, s49, 1024\n\ts_add_u32 s48, s50, 0x10000\n\tbuffer_load_dwordx4 v21, s[40:43], s48 offen  lds\n\ts_add_u32 m0, s49, 2048\n\ts_add_u32 s48, s50, 0x20000\n\t"
      "buffer_load_dwordx4 v20, s[40:43], s48 offen  lds\n\ts_add_u32 m0, s49, 3072\n\ts_add_u32 s48, s50, 0x30000\n\tbuffer_load_dwordx4 v21, s[40:43], s48 offen  lds\n\t"
      "s_add_u32 m0, s49, 4096\n\ts_add_u32 s48, s50, 0x40000\n\tbuffer_load_dwordx4 v20, s[40:43], s48 offen  lds\n\ts_add_u32 m0, s49, 5120\n\ts_add_u32 s48, s50, 0x50000\n\t"
      "buffer_load_dwordx4 v21, s[40:43], s48 offen  lds\n\ts_add_u32 m0, s49, 6144\n\ts_add_u32 s48, s50, 0x60000\n\tbuffer_load_dwordx4 v20, s[40:43], s48 offen  lds\n\t"
      "s_add_u32 m0, s49, 7168\n\ts_add_u32 s48, s50, 0x70000\n\tbuffer_load_dwordx4 v21, s[40:43], s48 offen  lds\n\ts_waitcnt vmcnt(24)\n\t"
      "ds_read_b64_tr_b16 a[144:145], v22 offset:8192\n\tds_read_b64_tr_b16 a[146:147], v22 offset:8448\n\tds_read_b64_tr_b16 a[148:149], v22 offset:12288\n\t"
      "ds_read_b64_tr_b16 a[150:151], v22 offset:12544\n\tds_read_b64_tr_b16 a[152:153], v23 offset:8192\n\tds_read_b64_tr_b16 a[154:155], v23 offset:8448\n\t"
      "ds_read_b64_tr_b16 a[156:157], v23 offset:12288\n\tds_read_b64_tr_b16 a[158:159], v23 offset:12544\n\tds_read_b64_tr_b16 v[104:105], v24 offset:8192\n\t"
      "ds_read_b64_tr_b16 v[106:107], v24 offset:8448\n\tds_read_b64_tr_b16 v[108:109], v24 offset:12288\n\tds_read_b64_tr_b16 v[110:111], v24 offset:12544\n\t"
      "ds_read_b64_tr_b16 v[224:225], v25 offset:8192\n\tds_read_b64_tr_b16 v[226:227], v25 offset:8448\n\tds_read_b64_tr_b16 v[228:229], v25 offset:12288\n\t"
      "ds_read_b64_tr_b16 v[230:231], v25 offset:12544\n\ts_waitcnt lgkmcnt(0)\n\ts_add_u32 s50, s57, s67\n\ts_add_u32 m0, s49, 8192\n\ts_add_u32 s48, s50, 0x0\n\t"
      "buffer_load_dwordx4 v20, s[40:43], s48 offen  lds\n\ts_add_u32 m0, s49, 9216\n\ts_add_u32 s48, s50, 0x10000\n\tbuffer_load_dwordx4 v21, s[40:43], s48 offen  lds\n\t"
      "s_add_u32 m0, s49, 10240\n\ts_add_u32 s48, s50, 0x20000\n\tbuffer_load_dwordx4 v20, s[40:43], s48 offen  lds\n\ts_add_u32 m0, s49, 11264\n\t"
      "s_add_u32 s48, s50, 0x30000\n\tbuffer_load_dwordx4 v21, s[40:43], s48 offen  lds\n\ts_add_u32 m0, s49, 12288\n\ts_add_u32 s48, s50, 0x40000\n\t"
      "buffer_load_dwordx4 v20, s[40:43], s48 offen  lds\n\ts_add_u32 m0, s49, 13312\n\ts_add_u32 s48, s50, 0x50000\n\tbuffer_load_dwordx4 v21, s[40:43], s48 offen  lds\n\t"
      "s_add_u32 m0, s49, 14336\n\ts_add_u32 s48, s50, 0x60000\n\tbuffer_load_dwordx4 v20, s[40:43], s48 offen  lds\n\ts_add_u32 m0, s49, 15360\n\t"
      "s_add_u32 s48, s50, 0x70000\n\tbuffer_load_dwordx4 v21, s[40:43], s48 offen  lds\n\ts_waitcnt vmcnt(24)\n\tds_read_b64_tr_b16 a[160:161], v22 offset:16384\n\t"
      "ds_read_b64_tr_b16 a[162:163], v22 offset:16640\n\tds_read_b64_tr_b16 a[164:165], v22 offset:20480\n\tds_read_b64_tr_b16 a[166:167], v22 offset:20736\n\t"
      "ds_read_b64_tr_b16 a[168:169], v23 offset:16384\n\tds_read_b64_tr_b16 a[170:171], v23 offset:16640\n\tds_read_b64_tr_b16 a[172:173], v23 offset:20480\n\t"
      "ds_read_b64_tr_b16 a[174:175], v23 offset:20736\n\tds_read_b64_tr_b16 v[112:113], v24 offset:16384\n\tds_read_b64_tr_b16 v[114:115], v24 offset:16640\n\t"
      "ds_read_b64_tr_b16 v[116:117], v24 offset:20480\n\tds_read_b64_tr_b16 v[118:119], v24 offset:20736\n\tds_read_b64_tr_b16 v[232:233], v25 offset:16384\n\t"
      "ds_read_b64_tr_b16 v[234:235], v25 offset:16640\n\tds_read_b64_tr_b16 v[236:237], v25 offset:20480\n\tds_read_b64_tr_b16 v[238:239], v25 offset:20736\n\t"
      "s_waitcnt lgkmcnt(0)\n\ts_add_u32 s50, s58, s67\n\ts_add_u32 m0, s49, 16384\n\ts_add_u32 s48, s50, 0x0\n\tbuffer_load_dwordx4 v20, s[40:43], s48 offen  lds\n\t"
      "s_add_u32 m0, s49, 17408\n\ts_add_u32 s48, s50, 0x10000\n\tbuffer_load_dwordx4 v21, s[40:43], s48 offen  lds\n\ts_add_u32 m0, s49, 18432\n\t"
      "s_add_u32 s48, s50, 0x20000\n\tbuffer_load_dwordx4 v20, s[40:43], s48 offen  lds\n\ts_add_u32 m0, s49, 19456\n\ts_add_u32 s48, s50, 0x30000\n\t"
      "buffer_load_dwordx4 v21, s[40:43], s48 offen  lds\n\ts_add_u32 m0, s49, 20480\n\ts_add_u32 s48, s50, 0x40000\n\tbuffer_load_dwordx4 v20, s[40:43], s48 offen  lds\n\t"
      "s_add_u32 m0, s49, 21504\n\ts_add_u32 s48, s50, 0x50000\n\tbuffer_load_dwordx4 v21, s[40:43], s48 offen  lds\n\ts_add_u32 m0, s49, 22528\n\t"
      "s_add_u32 s48, s50, 0x60000\n\tbuffer_load_dwordx4 v20, s[40:43], s48 offen  lds\n\ts_add_u32 m0, s49, 23552\n\ts_add_u32 s48, s50, 0x70000\n\t"
      "buffer_load_dwordx4 v21, s[40:43], s48 offen  lds\n\ts_waitcnt vmcnt(24)\n\tds_read_b64_tr_b16 a[176:177], v22 offset:24576\n\t"
      "ds_read_b64_tr_b16 a[178:179], v22 offset:24832\n\tds_read_b64_tr_b16 a[180:181], v22 offset:28672\n\tds_read_b64_tr_b16 a[182:183], v22 offset:28928\n\t"
      "ds_read_b64_tr_b16 a[184:185], v23 offset:24576\n\tds_read_b64_tr_b16 a[186:187], v23 offset:24832\n\tds_read_b64_tr_b16 a[188:189], v23 offset:28672\n\t"
      "ds_read_b64_tr_b16 a[190:191], v23 offset:28928\n\tds_read_b64_tr_b16 v[120:121], v24 offset:24576\n\tds_read_b64_tr_b16 v[122:123], v24 offset:24832\n\t"
      "ds_read_b64_tr_b16 v[124:125], v24 offset:28672\n\tds_read_b64_tr_b16 v[126:127], v24 offset:28928\n\tds_read_b64_tr_b16 v[240:241], v25 offset:24576\n\t"
      "ds_read_b64_tr_b16 v[242:243], v25 offset:24832\n\tds_read_b64_tr_b16 v[244:245], v25 offset:28672\n\tds_read_b64_tr_b16 v[246:247], v25 offset:28928\n\t"
      "s_waitcnt lgkmcnt(0)\n\ts_add_u32 s50, s59, s67\n\ts_add_u32 m0, s49, 24576\n\ts_add_u32 s48, s50, 0x0\n\tbuffer_load_dwordx4 v20, s[40:43], s48 offen  lds\n\t"
      "s_add_u32 m0, s49, 25600\n\ts_add_u32 s48, s50, 0x10000\n\tbuffer_load_dwordx4 v21, s[40:43], s48 offen  lds\n\ts_add_u32 m0, s49, 26624\n\t"
      "s_add_u32 s48, s50, 0x20000\n\tbuffer_load_dwordx4 v20, s[40:43], s48 offen  lds\n\ts_add_u32 m0, s49, 27648\n\ts_add_u32 s48, s50, 0x30000\n\t"
      "buffer_load_dwordx4 v21, s[40:43], s48 offen  lds\n\ts_add_u32 m0, s49, 28672\n\ts_add_u32 s48, s50, 0x40000\n\tbuffer_load_dwordx4 v20, s[40:43], s48 offen  lds\n\t"
      "s_add_u32 m0, s49, 29696\n\ts_add_u32 s48, s50, 0x50000\n\tbuffer_load_dwordx4 v21, s[40:43], s48 offen  lds\n\ts_add_u32 m0, s49, 30720\n\t"
      "s_add_u32 s48, s50, 0x60000\n\tbuffer_load_dwordx4 v20, s[40:43], s48 offen  lds\n\ts_add_u32 m0, s49, 31744\n\ts_add_u32 s48, s50, 0x70000\n\t"
      "buffer_load_dwordx4 v21, s[40:43], s48 offen  lds\n\ts_waitcnt vmcnt(24)\n\tds_read_b64_tr_b16 a[192:193], v22 offset:0\n\t"
      "ds_read_b64_tr_b16 a[194:195], v22 offset:256\n\tds_read_b64_tr_b16 a[196:197], v22 offset:4096\n\tds_read_b64_tr_b16 a[198:199], v22 offset:4352\n\t"
      "ds_read_b64_tr_b16 a[200:201], v23 offset:0\n\tds_read_b64_tr_b16 a[202:203], v23 offset:256\n\tds_read_b64_tr_b16 a[204:205], v23 offset:4096\n\t"
      "ds_read_b64_tr_b16 a[206:207], v23 offset:4352\n\tds_read_b64_tr_b16 v[128:129], v24 offset:0\n\tds_read_b64_tr_b16 v[130:131], v24 offset:256\n\t"
      "ds_read_b64_tr_b16 v[132:133], v24 offset:4096\n\tds_read_b64_tr_b16 v[134:135], v24 offset:4352\n\tds_read_b64_tr_b16 v[248:249], v25 offset:0\n\t"
      "ds_read_b64_tr_b16 v[250:251], v25 offset:256\n\tds_read_b64_tr_b16 v[252:253], v25 offset:4096\n\tds_read_b64_tr_b16 v[254:255], v25 offset:4352\n\t"
      "s_waitcnt lgkmcnt(0)\n\tds_write_b128 v5, v[152:155] offset:0\n\tds_write_b128 v5, v[156:159] offset:1024\n\tds_write_b128 v5, v[160:163] offset:2048\n\t"
      "ds_write_b128 v5, v[164:167] offset:3072\n\tds_write_b128 v5, v[168:171] offset:4096\n\tds_write_b128 v5, v[172:175] offset:5120\n\t"
      "ds_write_b128 v5, v[176:179] offset:6144\n\tds_write_b128 v5, v[180:183] offset:7168\n\ts_waitcnt vmcnt(16)\n\tds_read_b64_tr_b16 a[208:209], v22 offset:8192\n\t"
      "ds_read_b64_tr_b16 a[210:211], v22 offset:8448\n\tds_read_b64_tr_b16 a[212:213], v22 offset:12288\n\tds_read_b64_tr_b16 a[214:215], v22 offset:12544\n\t"
      "ds_read_b64_tr_b16 a[216:217], v23 offset:8192\n\tds_read_b64_tr_b16 a[218:219], v23 offset:8448\n\tds_read_b64_tr_b16 a[220:221], v23 offset:12288\n\t"
      "ds_read_b64_tr_b16 a[222:223], v23 offset:12544\n\tds_read_b64_tr_b16 v[136:137], v24 offset:8192\n\tds_read_b64_tr_b16 v[138:139], v24 offset:8448\n\t"
      "ds_read_b64_tr_b16 v[140:141], v24 offset:12288\n\tds_read_b64_tr_b16 v[142:143], v24 offset:12544\n\tds_read_b64_tr_b16 v[160:161], v25 offset:8192\n\t"
      "ds_read_b64_tr_b16 v[162:163], v25 offset:8448\n\tds_read_b64_tr_b16 v[164:165], v25 offset:12288\n\tds_read_b64_tr_b16 v[166:167], v25 offset:12544\n\t"
      "s_waitcnt lgkmcnt(0)\n\tds_write_b128 v5, v[184:187] offset:8192\n\tds_write_b128 v5, v[188:191] offset:9216\n\tds_write_b128 v5, v[192:195] offset:10240\n\t"
      "ds_write_b128 v5, v[196:199] offset:11264\n\tds_write_b128 v5, v[200:203] offset:12288\n\tds_write_b128 v5, v[204:207] offset:13312\n\t"
      "ds_write_b128 v5, v[208:211] offset:14336\n\tds_write_b128 v5, v[212:215] offset:15360\n\ts_waitcnt vmcnt(8)\n\tds_read_b64_tr_b16 a[224:225], v22 offset:16384\n\t"
      "ds_read_b64_tr_b16 a[226:227], v22 offset:16640\n\tds_read_b64_tr_b16 a[228:229], v22 offset:20480\n\tds_read_b64_tr_b16 a[230:231], v22 offset:20736\n\t"
      "ds_read_b64_tr_b16 a[232:233], v23 offset:16384\n\tds_read_b64_tr_b16 a[234:235], v23 offset:16640\n\tds_read_b64_tr_b16 a[236:237], v23 offset:20480\n\t"
      "ds_read_b64_tr_b16 a[238:239], v23 offset:20736\n\tds_read_b64_tr_b16 v[144:145], v24 offset:16384\n\tds_read_b64_tr_b16 v[146:147], v24 offset:16640\n\t"
      "ds_read_b64_tr_b16 v[148:149], v24 offset:20480\n\tds_read_b64_tr_b16 v[150:151], v24 offset:20736\n\tds_read_b64_tr_b16 v[168:169], v25 offset:16384\n\t"
      "ds_read_b64_tr_b16 v[170:171], v25 offset:16640\n\tds_read_b64_tr_b16 v[172:173], v25 offset:20480\n\tds_read_b64_tr_b16 v[174:175], v25 offset:20736\n\t"
      "s_waitcnt lgkmcnt(0)\n\tds_write_b128 v5, v[216:219] offset:16384\n\tds_write_b128 v5, v[220:223] offset:17408\n\tds_write_b128 v5, v[224:227] offset:18432\n\t"
      "ds_write_b128 v5, v[228:231] offset:19456\n\tds_write_b128 v5, v[232:235] offset:20480\n\tds_write_b128 v5, v[236:239] offset:21504\n\t"
      "ds_write_b128 v5, v[240:243] offset:22528\n\tds_write_b128 v5, v[244:247] offset:23552\n\ts_waitcnt vmcnt(0)\n\tds_read_b64_tr_b16 a[240:241], v22 offset:24576\n\t"
      "ds_read_b64_tr_b16 a[242:243], v22 offset:24832\n\tds_read_b64_tr_b16 a[244:245], v22 offset:28672\n\tds_read_b64_tr_b16 a[246:247], v22 offset:28928\n\t"
      "ds_read_b64_tr_b16 a[248:249], v23 offset:24576\n\tds_read_b64_tr_b16 a[250:251], v23 offset:24832\n\tds_read_b64_tr_b16 a[252:253], v23 offset:28672\n\t"
      "ds_read_b64_tr_b16 a[254:255], v23 offset:28928\n\tds_read_b64_tr_b16 v[152:153], v24 offset:24576\n\tds_read_b64_tr_b16 v[154:155], v24 offset:24832\n\t"
      "ds_read_b64_tr_b16 v[156:157], v24 offset:28672\n\tds_read_b64_tr_b16 v[158:159], v24 offset:28928\n\tds_read_b64_tr_b16 v[176:177], v25 offset:24576\n\t"
      "ds_read_b64_tr_b16 v[178:179], v25 offset:24832\n\tds_read_b64_tr_b16 v[180:181], v25 offset:28672\n\tds_read_b64_tr_b16 v[182:183], v25 offset:28928\n\t"
      "s_waitcnt lgkmcnt(0)\n\tds_write_b128 v5, v[248:251] offset:24576\n\tds_write_b128 v5, v[252:255] offset:25600\n\tds_write_b128 v5, v[160:163] offset:26624\n\t"
      "ds_write_b128 v5, v[164:167] offset:27648\n\tds_write_b128 v5, v[168:171] offset:28672\n\tds_write_b128 v5, v[172:175] offset:29696\n\t"
      "ds_write_b128 v5, v[176:179] offset:30720\n\tds_write_b128 v5, v[180:183] offset:31744\n\t"
      "v_mov_b64 v[30:31], 0\n\tv_mov_b32 v29, 160000\n\tds_write_b64 v29, v[30:31]\n\t"
      TKS("s_lshr_b32 s91, s62, 3\n\ts_and_b32 s91, s91, 3\n\t")
      "s_waitcnt lgkmcnt(0)\n\ts_barrier\n\t"
      STS("s_memrealtime vcc\n\ts_waitcnt lgkmcnt(0)\n\tv_mov_b32 v31, vcc_lo\n\tv_mov_b32 v30, 161024\n\tds_write_b32 v30, v31\n\t")
      "1:\n\tbuffer_load_dword v29, off, s[12:15], s63 sc1\n\ts_waitcnt vmcnt(0)\n\tv_readfirstlane_b32 s49, v29\n\ts_bitcmp1_b32 s49, 0\n\ts_cbranch_scc1 2f\n\t"
      "s_cmp_eq_u32 s18, 0\n\ts_cbranch_scc1 2f\n\ts_sub_u32 s18, s18, 1\n\ts_sleep 2\n\ts_branch 1b\n\t"
      "2:\n\ts_and_b32 s48, s49, 3\n\ts_cmp_lg_u32 s48, 3\n\ts_cbranch_scc1 4f\n\ts_cmp_eq_u32 s62, 0\n\ts_cbranch_scc1 4f\n\t"
      "s_waitcnt lgkmcnt(0)\n\ts_barrier\n\ts_mov_b64 s[0:1], s[60:61]\n\ts_mov_b32 s2, s62\n\ts_lshl_b32 s48, s16, 6\n\tv_mbcnt_lo_u32_b32 v0, -1, 0\n\t"
      "v_mbcnt_hi_u32_b32 v0, -1, v0\n\tv_add_u32 v0, s48, v0\n\ts_getpc_b64 s[4:5]\n\t"
      "3:\n\ts_sub_u32 s4, s4, 3b-mk_mega\n\ts_subb_u32 s5, s5, 0\n\ts_setpc_b64 s[4:5]\n\t"
      "4:\n\ts_memrealtime s[46:47]\n\ts_waitcnt lgkmcnt(0)\n\ts_lshl_b32 s19, s46, 2\n\ts_sub_u32 s19, s19, 0x100000\n\ts_lshr_b32 s64, s64, 9\n\ts_and_b32 s48, s64, 0x2000\n\t"
      "s_add_u32 s64, s64, s48\n\ts_lshr_b32 s65, s65, 9\n\ts_and_b32 s48, s65, 0x2000\n\ts_add_u32 s65, s65, s48\n\ts_lshr_b32 s66, s66, 9\n\ts_and_b32 s48, s66, 0x2000\n\t"
      "s_add_u32 s66, s66, s48\n\ts_lshr_b32 s67, s67, 9\n\ts_and_b32 s48, s67, 0x2000\n\ts_add_u32 s67, s67, s48\n\ts_lshl_b32 s48, s49, 10\n\ts_and_b32 s48, s48, 0x2000\n\t"
      "v_or_b32 v0, s48, v0\n\tv_or_b32 v14, s48, v14\n\tv_or_b32 v15, s48, v15\n\ts_mov_b64 s[72:73], s[24:25]\n\ts_mov_b64 s[56:57], s[8:9]\n\ts_mov_b32 s59, s11\n\t"
      "s_mov_b64 s[24:25], s[8:9]\n\ts_mov_b32 s27, s11\n\ts_add_u32 s60, s16, 1\n\ts_bitcmp1_b32 s60, 1\n\ts_cselect_b32 s60, s10, 0\n\ts_lshl_b32 s49, s49, 11\n\t"
      "s_and_b32 s49, s49, 0x2000\n\ts_xor_b32 s22, s22, s49\n\ts_xor_b32 s31, s31, s49\n\ts_cmp_eq_u32 s16, 3\n\ts_cbranch_scc1 64f\n\tv_or_b32 v10, s48, v10\n\t"
      "v_or_b32 v11, s48, v11\n\ts_branch 69f\n\t"
      "10:\n\tds_read_b128 v[160:163], v5 offset:0\n\tds_read_b128 v[164:167], v5 offset:1024\n\tds_read_b128 v[168:171], v5 offset:2048\n\t"
      "ds_read_b128 v[172:175], v5 offset:3072\n\tds_read_b128 v[176:179], v5 offset:4096\n\tds_read_b128 v[180:183], v5 offset:5120\n\t"
      "ds_read_b128 v[184:187], v5 offset:6144\n\tds_read_b128 v[188:191], v5 offset:7168\n\tds_read_b128 v[192:195], v5 offset:8192\n\t"
      "ds_read_b128 v[196:199], v5 offset:9216\n\tds_read_b128 v[200:203], v5 offset:10240\n\tds_read_b128 v[204:207], v5 offset:11264\n\t"
      "ds_read_b128 v[208:211], v5 offset:12288\n\tds_read_b128 v[212:215], v5 offset:13312\n\tds_read_b128 v[216:219], v5 offset:14336\n\t"
      "ds_read_b128 v[220:223], v5 offset:15360\n\tv_mov_b64 v[224:225], 0\n\tv_mov_b64 v[226:227], 0\n\tv_mov_b64 v[228:229], 0\n\tv_mov_b64 v[230:231], 0\n\t"
      "v_mov_b64 v[232:233], 0\n\tv_mov_b64 v[234:235], 0\n\tv_mov_b64 v[236:237], 0\n\tv_mov_b64 v[238:239], 0\n\ts_and_b32 s48, s17, 3\n\ts_lshl_b32 s40, s48, 18\n\t"
      "s_add_u32 s40, s40, 0x200000\n\ts_lshl_b32 s41, s48, 18\n\ts_add_u32 s41, s41, 0x208000\n\ts_xor_b32 s48, s48, 2\n\ts_lshl_b32 s42, s48, 18\n\t"
      "s_add_u32 s42, s42, 0x208000\n\ts_add_u32 s68, s40, s64\n\ts_add_u32 s69, s40, s65\n\ts_add_u32 s70, s40, s66\n\ts_add_u32 s71, s40, s67\n\ts_lshr_b32 s21, s20, 4\n\t"
      "s_and_b32 s21, s21, -2\n\ts_add_u32 s49, s22, 0x1000\n\ts_waitcnt lgkmcnt(0)\n\ts_cmp_lg_u32 s16, 3\n\ts_cbranch_scc1 73f\n\t"
      "71:\n\ts_memrealtime vcc\n\ts_waitcnt lgkmcnt(0)\n\ts_lshl_b32 vcc_lo, vcc_lo, 2\n\t"
      "70:\n\ts_sub_u32 s48, s19, vcc_lo\n\ts_cmp_gt_u32 s48, 4000\n\ts_cbranch_scc1 73f\n\ts_cmp_gt_u32 s48, 28\n\ts_cbranch_scc1 71b\n\ts_mul_i32 s48, s48, 45\n\t"
      "s_lshr_b32 s48, s48, 5\n\ts_lshl_b32 s48, s48, 2\n\ts_getpc_b64 vcc\n\t"
      "72:\n\ts_sub_u32 s48, 73f-72b, s48\n\ts_add_u32 vcc_lo, vcc_lo, s48\n\ts_addc_u32 vcc_hi, vcc_hi, 0\n\ts_setpc_b64 vcc\n\t.rept 40\n\ts_nop 0\n\t.endr\n\t"
      "73:\n\ts_barrier\n\t"
      TK0
      "buffer_load_dwordx4 v[240:243], v0, s[8:11], s68 offen sc1\n\ts_nop 11\n\tbuffer_load_dwordx4 v[244:247], v0, s[8:11], s69 offen sc1\n\ts_nop 11\n\t"
      "buffer_load_dwordx4 v[248:251], v0, s[8:11], s70 offen sc1\n\ts_nop 11\n\tbuffer_load_dwordx4 v[252:255], v0, s[8:11], s71 offen sc1\n\t"
      "ds_read_b32 v17, v16 offset:4\n\ts_waitcnt lgkmcnt(0)\n\tv_readfirstlane_b32 s55, v17\n\ts_cmp_gt_u32 s55, s17\n\ts_cselect_b32 s58, s60, 0\n\ts_min_u32 s48, s55, s54\n\t"
      "s_sub_u32 s48, s48, 1\n\ts_cmp_gt_i32 s48, s17\n\ts_cselect_b32 s26, s10, 0\n\ts_sub_u32 s53, s31, 0x800000\n\t"
      "11:\n\ts_cmp_lt_u32 s16, 2\n\ts_cbranch_scc1 15f\n\ts_waitcnt vmcnt(3)\n\t"
      TK1
      "v_smfmac_f32_16x16x64_bf16 v[224:227], v[240:243], a[0:7], v6\n\tv_max3_u32 v17, v240, v241, v242\n\tv_smfmac_f32_16x16x64_bf16 v[228:231], v[240:243], a[8:15], v6\n\t"
      "v_max_u32 v17, v17, v243\n\tv_smfmac_f32_16x16x64_bf16 v[232:235], v[240:243], v[32:39], v6\n\tds_write_b128 v1, v[240:243] offset:0\n\t"
      "v_smfmac_f32_16x16x64_bf16 v[236:239], v[240:243], v[160:167], v6\n\ts_waitcnt vmcnt(2)\n\tv_cmp_eq_u32 vcc, -1, v17\n\t"
      "v_smfmac_f32_16x16x64_bf16 v[224:227], v[244:247], a[16:23], v6\n\tv_max3_u32 v17, v244, v245, v246\n\tv_smfmac_f32_16x16x64_bf16 v[228:231], v[244:247], a[24:31], v6\n\t"
      "s_cbranch_vccnz 86f\n\t"
      "24:\n\tv_max_u32 v17, v17, v247\n\tv_smfmac_f32_16x16x64_bf16 v[232:235], v[244:247], v[40:47], v6\n\tds_write_b128 v1, v[244:247] offset:1024\n\t"
      "v_smfmac_f32_16x16x64_bf16 v[236:239], v[244:247], v[168:175], v6\n\ts_waitcnt lgkmcnt(1)\n\ts_barrier\n\ts_waitcnt vmcnt(1)\n\tv_cmp_eq_u32 vcc, -1, v17\n\t"
      "v_smfmac_f32_16x16x64_bf16 v[224:227], v[248:251], a[32:39], v6\n\tv_max3_u32 v17, v248, v249, v250\n\tv_smfmac_f32_16x16x64_bf16 v[228:231], v[248:251], a[40:47], v6\n\t"
      "s_cbranch_vccnz 87f\n\t"
      "25:\n\tv_max_u32 v17, v17, v251\n\tv_smfmac_f32_16x16x64_bf16 v[232:235], v[248:251], v[48:55], v6\n\tds_write_b128 v1, v[248:251] offset:2048\n\t"
      "v_smfmac_f32_16x16x64_bf16 v[236:239], v[248:251], v[176:183], v6\n\ts_waitcnt vmcnt(0)\n\tv_cmp_eq_u32 vcc, -1, v17\n\t"
      "v_smfmac_f32_16x16x64_bf16 v[224:227], v[252:255], a[48:55], v6\n\tds_read_b128 v[20:23], v2 offset:0\n\tds_read_b128 v[24:27], v3 offset:0\n\t"
      "v_max3_u32 v17, v252, v253, v254\n\tv_smfmac_f32_16x16x64_bf16 v[228:231], v[252:255], a[56:63], v6\n\ts_cbranch_vccnz 88f\n\t"
      "26:\n\tv_max_u32 v17, v17, v255\n\tv_smfmac_f32_16x16x64_bf16 v[232:235], v[252:255], v[56:63], v6\n\tds_write_b128 v1, v[252:255] offset:3072\n\t"
      "v_smfmac_f32_16x16x64_bf16 v[236:239], v[252:255], v[184:191], v6\n\ts_waitcnt lgkmcnt(2)\n\t"
      TK2
      "v_cmp_eq_u32 vcc, -1, v17\n\tv_smfmac_f32_16x16x64_bf16 v[224:227], v[20:23], a[64:71], v6\n\tds_read_b128 v[28:31], v4 offset:0\n\t"
      "v_smfmac_f32_16x16x64_bf16 v[228:231], v[20:23], a[72:79], v6\n\ts_cbranch_vccnz 89f\n\t"
      "27:\n\tv_smfmac_f32_16x16x64_bf16 v[232:235], v[20:23], v[64:71], v6\n\ts_barrier\n\tv_smfmac_f32_16x16x64_bf16 v[236:239], v[20:23], v[192:199], v6\n\t"
      "s_waitcnt lgkmcnt(1)\n\ts_barrier\n\t"
      TK3
      "v_smfmac_f32_16x16x64_bf16 v[224:227], v[24:27], a[80:87], v6\n\tds_read_b128 v[20:23], v2 offset:1024\n\t"
      "v_smfmac_f32_16x16x64_bf16 v[228:231], v[24:27], a[88:95], v6\n\tds_read_b128 v[160:163], v5 offset:16384\n\tds_read_b128 v[164:167], v5 offset:17408\n\t"
      "v_smfmac_f32_16x16x64_bf16 v[232:235], v[24:27], v[72:79], v6\n\tbuffer_load_dword v19, v11, s[24:27], s31 offen sc1\n\t"
      "v_smfmac_f32_16x16x64_bf16 v[236:239], v[24:27], v[200:207], v6\n\ts_waitcnt lgkmcnt(3)\n\tv_smfmac_f32_16x16x64_bf16 v[224:227], v[28:31], a[96:103], v6\n\t"
      "ds_read_b128 v[24:27], v3 offset:1024\n\tv_smfmac_f32_16x16x64_bf16 v[228:231], v[28:31], a[104:111], v6\n\tds_read_b128 v[168:171], v5 offset:18432\n\t"
      "ds_read_b128 v[172:175], v5 offset:19456\n\tv_smfmac_f32_16x16x64_bf16 v[232:235], v[28:31], v[80:87], v6\n\tbuffer_load_dword v19, v11, s[24:27], s53 offen sc1\n\t"
      "v_smfmac_f32_16x16x64_bf16 v[236:239], v[28:31], v[208:215], v6\n\ts_waitcnt lgkmcnt(5)\n\tv_smfmac_f32_16x16x64_bf16 v[224:227], v[20:23], a[112:119], v6\n\t"
      "ds_read_b128 v[28:31], v4 offset:1024\n\tv_smfmac_f32_16x16x64_bf16 v[228:231], v[20:23], a[120:127], v6\n\tds_read_b128 v[176:179], v5 offset:20480\n\t"
      "ds_read_b128 v[180:183], v5 offset:21504\n\tv_smfmac_f32_16x16x64_bf16 v[232:235], v[20:23], v[88:95], v6\n\t"
      "buffer_load_dwordx4 v[240:243], v12, s[56:59], s22 offen offset:0 sc1\n\tv_smfmac_f32_16x16x64_bf16 v[236:239], v[20:23], v[216:223], v6\n\ts_waitcnt lgkmcnt(5)\n\t"
      "v_smfmac_f32_16x16x64_bf16 v[224:227], v[24:27], a[128:135], v6\n\tds_read_b128 v[20:23], v2 offset:2048\n\t"
      "v_smfmac_f32_16x16x64_bf16 v[228:231], v[24:27], a[136:143], v6\n\tds_read_b128 v[184:187], v5 offset:22528\n\tds_read_b128 v[188:191], v5 offset:23552\n\t"
      "v_smfmac_f32_16x16x64_bf16 v[232:235], v[24:27], v[96:103], v6\n\tbuffer_load_dwordx4 v[244:247], v12, s[56:59], s22 offen offset:2048 sc1\n\t"
      "v_smfmac_f32_16x16x64_bf16 v[236:239], v[24:27], v[160:167], v6\n\ts_waitcnt lgkmcnt(5)\n\tv_smfmac_f32_16x16x64_bf16 v[224:227], v[28:31], a[144:151], v6\n\t"
      "ds_read_b128 v[24:27], v3 offset:2048\n\tv_smfmac_f32_16x16x64_bf16 v[228:231], v[28:31], a[152:159], v6\n\tds_read_b128 v[192:195], v5 offset:24576\n\t"
      "ds_read_b128 v[196:199], v5 offset:25600\n\tv_smfmac_f32_16x16x64_bf16 v[232:235], v[28:31], v[104:111], v6\n\t"
      "buffer_load_dwordx4 v[248:251], v12, s[56:59], s49 offen offset:0 sc1\n\tv_smfmac_f32_16x16x64_bf16 v[236:239], v[28:31], v[168:175], v6\n\ts_waitcnt lgkmcnt(5)\n\t"
      "ds_read_b128 v[160:163], v9 offset:0\n\tv_smfmac_f32_16x16x64_bf16 v[224:227], v[20:23], a[160:167], v6\n\tds_read_b128 v[28:31], v4 offset:2048\n\t"
      "ds_read_b128 v[164:167], v9 offset:2048\n\tv_smfmac_f32_16x16x64_bf16 v[228:231], v[20:23], a[168:175], v6\n\tds_read_b128 v[200:203], v5 offset:26624\n\t"
      "ds_read_b128 v[204:207], v5 offset:27648\n\tv_smfmac_f32_16x16x64_bf16 v[232:235], v[20:23], v[112:119], v6\n\t"
      "buffer_load_dwordx4 v[252:255], v12, s[56:59], s49 offen offset:2048 sc1\n\tv_smfmac_f32_16x16x64_bf16 v[236:239], v[20:23], v[176:183], v6\n\ts_waitcnt lgkmcnt(7)\n\t"
      "ds_read_b128 v[168:171], v9 offset:4096\n\tv_smfmac_f32_16x16x64_bf16 v[224:227], v[24:27], a[176:183], v6\n\tds_read_b128 v[20:23], v2 offset:3072\n\t"
      "ds_read_b128 v[172:175], v9 offset:6144\n\tv_smfmac_f32_16x16x64_bf16 v[228:231], v[24:27], a[184:191], v6\n\tds_read_b128 v[208:211], v5 offset:28672\n\t"
      "ds_read_b128 v[212:215], v5 offset:29696\n\tv_smfmac_f32_16x16x64_bf16 v[232:235], v[24:27], v[120:127], v6\n\t"
      "v_smfmac_f32_16x16x64_bf16 v[236:239], v[24:27], v[184:191], v6\n\ts_waitcnt lgkmcnt(8)\n\tds_read_b128 v[176:179], v9 offset:256\n\t"
      "v_smfmac_f32_16x16x64_bf16 v[224:227], v[28:31], a[192:199], v6\n\tds_read_b128 v[24:27], v3 offset:3072\n\tds_read_b128 v[180:183], v9 offset:2304\n\t"
      "v_smfmac_f32_16x16x64_bf16 v[228:231], v[28:31], a[200:207], v6\n\tds_read_b128 v[216:219], v5 offset:30720\n\tds_read_b128 v[220:223], v5 offset:31744\n\t"
      "v_smfmac_f32_16x16x64_bf16 v[232:235], v[28:31], v[128:135], v6\n\ts_cmp_eq_u32 s16, 2\n\tv_smfmac_f32_16x16x64_bf16 v[236:239], v[28:31], v[192:199], v6\n\t"
      "s_waitcnt lgkmcnt(8)\n\tds_read_b128 v[184:187], v9 offset:4352\n\tv_smfmac_f32_16x16x64_bf16 v[224:227], v[20:23], a[208:215], v6\n\t"
      "ds_read_b128 v[28:31], v4 offset:3072\n\tds_read_b128 v[188:191], v9 offset:6400\n\tv_smfmac_f32_16x16x64_bf16 v[228:231], v[20:23], a[216:223], v6\n\t"
      "v_smfmac_f32_16x16x64_bf16 v[232:235], v[20:23], v[136:143], v6\n\tv_smfmac_f32_16x16x64_bf16 v[236:239], v[20:23], v[200:207], v6\n\ts_waitcnt lgkmcnt(6)\n\t"
      "v_smfmac_f32_16x16x64_bf16 v[224:227], v[24:27], a[224:231], v6\n\tv_smfmac_f32_16x16x64_bf16 v[228:231], v[24:27], a[232:239], v6\n\ts_waitcnt lgkmcnt(1)\n\t"
      "v_smfmac_f32_16x16x64_bf16 v[224:227], v[28:31], a[240:247], v6\n\ts_nop 2\n\tv_smfmac_f32_16x16x64_bf16 v[228:231], v[28:31], a[248:255], v6\n\ts_nop 2\n\t"
      "v_smfmac_f32_16x16x64_bf16 v[232:235], v[24:27], v[144:151], v6\n\tv_add_f32 v224, v224, v225\n\tv_smfmac_f32_16x16x64_bf16 v[232:235], v[28:31], v[152:159], v6\n\t"
      "s_nop 0\n\tv_add_f32 v226, v226, v227\n\tv_smfmac_f32_16x16x64_bf16 v[236:239], v[24:27], v[208:215], v6\n\tv_add_f32 v228, v228, v229\n\t"
      "v_smfmac_f32_16x16x64_bf16 v[236:239], v[28:31], v[216:223], v6\n\t"
      TK4
      "v_add_f32 v230, v230, v231\n\tv_cvt_pk_bf16_f32 v224, v224, v228\n\tv_cvt_pk_bf16_f32 v226, v226, v230\n\tv_add_f32 v232, v232, v233\n\tv_add_f32 v234, v234, v235\n\t"
      "s_nop 2\n\tv_add_f32 v236, v236, v237\n\tv_add_f32 v238, v238, v239\n\tv_cvt_pk_bf16_f32 v225, v232, v236\n\tv_cvt_pk_bf16_f32 v227, v234, v238\n\t"
      "ds_write2_b64 v7, v[224:225], v[226:227] offset1:34\n\ts_waitcnt lgkmcnt(0)\n\ts_barrier\n\ts_cbranch_scc1 40f\n\ts_branch 60f\n\t"
      "15:\n\ts_waitcnt vmcnt(3)\n\t"
      TK1
      "v_smfmac_f32_16x16x64_bf16 v[224:227], v[240:243], a[0:7], v6\n\tds_write_b128 v1, v[240:243] offset:0\n\t"
      "v_smfmac_f32_16x16x64_bf16 v[228:231], v[240:243], a[8:15], v6\n\tv_max3_u32 v17, v240, v241, v242\n\tv_smfmac_f32_16x16x64_bf16 v[232:235], v[240:243], v[32:39], v6\n\t"
      "v_max_u32 v17, v17, v243\n\tv_smfmac_f32_16x16x64_bf16 v[236:239], v[240:243], v[160:167], v6\n\ts_waitcnt vmcnt(2)\n\tv_cmp_eq_u32 vcc, -1, v17\n\t"
      "v_smfmac_f32_16x16x64_bf16 v[224:227], v[244:247], a[16:23], v6\n\tds_write_b128 v1, v[244:247] offset:1024\n\t"
      "v_smfmac_f32_16x16x64_bf16 v[228:231], v[244:247], a[24:31], v6\n\ts_cbranch_vccnz 86f\n\t"
      "20:\n\tv_max3_u32 v17, v244, v245, v246\n\tv_smfmac_f32_16x16x64_bf16 v[232:235], v[244:247], v[40:47], v6\n\tv_max_u32 v17, v17, v247\n\t"
      "v_smfmac_f32_16x16x64_bf16 v[236:239], v[244:247], v[168:175], v6\n\ts_waitcnt lgkmcnt(1)\n\ts_barrier\n\ts_waitcnt vmcnt(1)\n\tv_cmp_eq_u32 vcc, -1, v17\n\t"
      "v_smfmac_f32_16x16x64_bf16 v[224:227], v[248:251], a[32:39], v6\n\tds_write_b128 v1, v[248:251] offset:2048\n\t"
      "v_smfmac_f32_16x16x64_bf16 v[228:231], v[248:251], a[40:47], v6\n\ts_cbranch_vccnz 87f\n\t"
      "21:\n\tv_max3_u32 v17, v248, v249, v250\n\tv_smfmac_f32_16x16x64_bf16 v[232:235], v[248:251], v[48:55], v6\n\tv_max_u32 v17, v17, v251\n\t"
      "v_smfmac_f32_16x16x64_bf16 v[236:239], v[248:251], v[176:183], v6\n\tds_read_b128 v[20:23], v2 offset:0\n\tds_read_b128 v[24:27], v3 offset:0\n\ts_waitcnt vmcnt(0)\n\t"
      "v_cmp_eq_u32 vcc, -1, v17\n\tv_smfmac_f32_16x16x64_bf16 v[224:227], v[252:255], a[48:55], v6\n\tds_write_b128 v1, v[252:255] offset:3072\n\t"
      "v_smfmac_f32_16x16x64_bf16 v[228:231], v[252:255], a[56:63], v6\n\ts_cbranch_vccnz 88f\n\t"
      "22:\n\tv_max3_u32 v17, v252, v253, v254\n\tv_smfmac_f32_16x16x64_bf16 v[232:235], v[252:255], v[56:63], v6\n\tv_max_u32 v17, v17, v255\n\t"
      "v_smfmac_f32_16x16x64_bf16 v[236:239], v[252:255], v[184:191], v6\n\tds_read_b128 v[28:31], v4 offset:0\n\ts_waitcnt lgkmcnt(3)\n\t"
      TK2
      "v_cmp_eq_u32 vcc, -1, v17\n\tv_smfmac_f32_16x16x64_bf16 v[224:227], v[20:23], a[64:71], v6\n\tv_smfmac_f32_16x16x64_bf16 v[228:231], v[20:23], a[72:79], v6\n\t"
      "s_cbranch_vccnz 89f\n\t"
      "23:\n\tv_smfmac_f32_16x16x64_bf16 v[232:235], v[20:23], v[64:71], v6\n\ts_barrier\n\ts_memrealtime s[46:47]\n\t"
      "v_smfmac_f32_16x16x64_bf16 v[236:239], v[20:23], v[192:199], v6\n\ts_waitcnt lgkmcnt(1)\n\ts_barrier\n\t"
      TK3
      "v_smfmac_f32_16x16x64_bf16 v[224:227], v[24:27], a[80:87], v6\n\tv_smfmac_f32_16x16x64_bf16 v[228:231], v[24:27], a[88:95], v6\n\t"
      "v_smfmac_f32_16x16x64_bf16 v[232:235], v[24:27], v[72:79], v6\n\ts_waitcnt lgkmcnt(0)\n\tds_read_b128 v[20:23], v2 offset:1024\n\ts_lshl2_add_u32 s47, s46, s21\n\t"
      "v_smfmac_f32_16x16x64_bf16 v[236:239], v[24:27], v[200:207], v6\n\tds_read_b128 v[160:163], v5 offset:16384\n\tds_read_b128 v[164:167], v5 offset:17408\n\t"
      "v_mov_b32 v18, s47\n\tv_smfmac_f32_16x16x64_bf16 v[224:227], v[28:31], a[96:103], v6\n\tbuffer_store_dword v18, v14, s[8:11], s41 offen sc1\n\t"
      "v_smfmac_f32_16x16x64_bf16 v[228:231], v[28:31], a[104:111], v6\n\tds_read_b128 v[168:171], v5 offset:18432\n\tds_read_b128 v[172:175], v5 offset:19456\n\t"
      "v_smfmac_f32_16x16x64_bf16 v[232:235], v[28:31], v[80:87], v6\n\tds_read_b128 v[24:27], v3 offset:1024\n\t"
      "v_smfmac_f32_16x16x64_bf16 v[236:239], v[28:31], v[208:215], v6\n\ts_waitcnt lgkmcnt(5)\n\tv_smfmac_f32_16x16x64_bf16 v[224:227], v[20:23], a[112:119], v6\n\t"
      "ds_read_b128 v[28:31], v4 offset:1024\n\tv_smfmac_f32_16x16x64_bf16 v[228:231], v[20:23], a[120:127], v6\n\tds_read_b128 v[176:179], v5 offset:20480\n\t"
      "ds_read_b128 v[180:183], v5 offset:21504\n\tv_smfmac_f32_16x16x64_bf16 v[232:235], v[20:23], v[88:95], v6\n\t"
      "buffer_load_dwordx4 v[240:243], v12, s[56:59], s22 offen offset:0 sc1\n\tv_smfmac_f32_16x16x64_bf16 v[236:239], v[20:23], v[216:223], v6\n\ts_waitcnt lgkmcnt(3)\n\t"
      "v_smfmac_f32_16x16x64_bf16 v[224:227], v[24:27], a[128:135], v6\n\tds_read_b128 v[20:23], v2 offset:2048\n\t"
      "v_smfmac_f32_16x16x64_bf16 v[228:231], v[24:27], a[136:143], v6\n\tds_read_b128 v[184:187], v5 offset:22528\n\tds_read_b128 v[188:191], v5 offset:23552\n\t"
      "v_smfmac_f32_16x16x64_bf16 v[232:235], v[24:27], v[96:103], v6\n\tbuffer_load_dwordx4 v[244:247], v12, s[56:59], s22 offen offset:2048 sc1\n\t"
      "v_smfmac_f32_16x16x64_bf16 v[236:239], v[24:27], v[160:167], v6\n\ts_waitcnt lgkmcnt(5)\n\tv_smfmac_f32_16x16x64_bf16 v[224:227], v[28:31], a[144:151], v6\n\t"
      "ds_read_b128 v[24:27], v3 offset:2048\n\tv_smfmac_f32_16x16x64_bf16 v[228:231], v[28:31], a[152:159], v6\n\tds_read_b128 v[192:195], v5 offset:24576\n\t"
      "ds_read_b128 v[196:199], v5 offset:25600\n\tv_smfmac_f32_16x16x64_bf16 v[232:235], v[28:31], v[104:111], v6\n\t"
      "buffer_load_dwordx4 v[248:251], v12, s[56:59], s49 offen offset:0 sc1\n\tv_smfmac_f32_16x16x64_bf16 v[236:239], v[28:31], v[168:175], v6\n\ts_waitcnt lgkmcnt(5)\n\t"
      "ds_read_b128 v[160:163], v9 offset:0\n\tv_smfmac_f32_16x16x64_bf16 v[224:227], v[20:23], a[160:167], v6\n\tds_read_b128 v[28:31], v4 offset:2048\n\t"
      "ds_read_b128 v[164:167], v9 offset:2048\n\tv_smfmac_f32_16x16x64_bf16 v[228:231], v[20:23], a[168:175], v6\n\tds_read_b128 v[200:203], v5 offset:26624\n\t"
      "ds_read_b128 v[204:207], v5 offset:27648\n\tv_smfmac_f32_16x16x64_bf16 v[232:235], v[20:23], v[112:119], v6\n\t"
      "buffer_load_dwordx4 v[252:255], v12, s[56:59], s49 offen offset:2048 sc1\n\tv_smfmac_f32_16x16x64_bf16 v[236:239], v[20:23], v[176:183], v6\n\ts_waitcnt lgkmcnt(7)\n\t"
      "ds_read_b128 v[168:171], v9 offset:4096\n\tv_smfmac_f32_16x16x64_bf16 v[224:227], v[24:27], a[176:183], v6\n\tds_read_b128 v[20:23], v2 offset:3072\n\t"
      "ds_read_b128 v[172:175], v9 offset:6144\n\tv_smfmac_f32_16x16x64_bf16 v[228:231], v[24:27], a[184:191], v6\n\tds_read_b128 v[208:211], v5 offset:28672\n\t"
      "ds_read_b128 v[212:215], v5 offset:29696\n\tv_smfmac_f32_16x16x64_bf16 v[232:235], v[24:27], v[120:127], v6\n\t"
      "v_smfmac_f32_16x16x64_bf16 v[236:239], v[24:27], v[184:191], v6\n\ts_waitcnt lgkmcnt(8)\n\tds_read_b128 v[176:179], v9 offset:256\n\t"
      "v_smfmac_f32_16x16x64_bf16 v[224:227], v[28:31], a[192:199], v6\n\tds_read_b128 v[24:27], v3 offset:3072\n\tds_read_b128 v[180:183], v9 offset:2304\n\t"
      "v_smfmac_f32_16x16x64_bf16 v[228:231], v[28:31], a[200:207], v6\n\tds_read_b128 v[216:219], v5 offset:30720\n\tds_read_b128 v[220:223], v5 offset:31744\n\t"
      "v_smfmac_f32_16x16x64_bf16 v[232:235], v[28:31], v[128:135], v6\n\ts_cmp_eq_u32 s16, 0\n\tv_smfmac_f32_16x16x64_bf16 v[236:239], v[28:31], v[192:199], v6\n\t"
      "s_waitcnt lgkmcnt(8)\n\tds_read_b128 v[184:187], v9 offset:4352\n\tv_smfmac_f32_16x16x64_bf16 v[224:227], v[20:23], a[208:215], v6\n\t"
      "ds_read_b128 v[28:31], v4 offset:3072\n\tds_read_b128 v[188:191], v9 offset:6400\n\tv_smfmac_f32_16x16x64_bf16 v[228:231], v[20:23], a[216:223], v6\n\t"
      "v_smfmac_f32_16x16x64_bf16 v[232:235], v[20:23], v[136:143], v6\n\tv_smfmac_f32_16x16x64_bf16 v[236:239], v[20:23], v[200:207], v6\n\ts_waitcnt lgkmcnt(6)\n\t"
      "v_smfmac_f32_16x16x64_bf16 v[224:227], v[24:27], a[224:231], v6\n\tv_smfmac_f32_16x16x64_bf16 v[228:231], v[24:27], a[232:239], v6\n\ts_waitcnt lgkmcnt(1)\n\t"
      "v_smfmac_f32_16x16x64_bf16 v[224:227], v[28:31], a[240:247], v6\n\ts_nop 2\n\tv_smfmac_f32_16x16x64_bf16 v[228:231], v[28:31], a[248:255], v6\n\ts_nop 2\n\t"
      "v_smfmac_f32_16x16x64_bf16 v[232:235], v[24:27], v[144:151], v6\n\tv_add_f32 v224, v224, v225\n\tv_smfmac_f32_16x16x64_bf16 v[232:235], v[28:31], v[152:159], v6\n\t"
      "s_nop 0\n\tv_add_f32 v226, v226, v227\n\tv_smfmac_f32_16x16x64_bf16 v[236:239], v[24:27], v[208:215], v6\n\tv_add_f32 v228, v228, v229\n\t"
      "v_smfmac_f32_16x16x64_bf16 v[236:239], v[28:31], v[216:223], v6\n\t"
      TK4
      "v_add_f32 v230, v230, v231\n\tv_cvt_pk_bf16_f32 v224, v224, v228\n\tv_cvt_pk_bf16_f32 v226, v226, v230\n\tv_add_f32 v232, v232, v233\n\tv_add_f32 v234, v234, v235\n\t"
      "s_nop 2\n\tv_add_f32 v236, v236, v237\n\tv_add_f32 v238, v238, v239\n\tv_cvt_pk_bf16_f32 v225, v232, v236\n\tv_cvt_pk_bf16_f32 v227, v234, v238\n\t"
      "ds_write2_b32 v7, v224, v225 offset1:4\n\tds_write2_b32 v7, v226, v227 offset0:8 offset1:12\n\ts_cbranch_scc0 39f\n\t"
      "14:\n\tv_mov_b64 v[228:229], 0\n\tv_mov_b64 v[230:231], 0\n\tv_mov_b64 v[232:233], 0\n\tv_mov_b64 v[234:235], 0\n\tv_mov_b64 v[236:237], 0\n\tv_mov_b64 v[238:239], 0\n\t"
      "s_mov_b32 vcc_lo, 0x55555555\n\ts_mov_b32 vcc_hi, 0x55555555\n\ts_waitcnt lgkmcnt(0)\n\ts_barrier\n\t"
      TK5
      "ds_read_b128 v[20:23], v1 offset:24576\n\tds_read_b128 v[24:27], v1 offset:25600\n\tds_read_b128 v[208:211], v8 offset:0\n\tds_read_b128 v[212:215], v8 offset:64\n\t"
      "ds_read_b128 v[216:219], v8 offset:128\n\tds_read_b128 v[220:223], v8 offset:192\n\ts_waitcnt lgkmcnt(5)\n\t"
      "v_smfmac_f32_16x16x64_bf16 v[232:235], v[20:23], v[160:167], v6\n\tv_smfmac_f32_16x16x64_bf16 v[236:239], v[20:23], v[176:183], v6\n\ts_waitcnt lgkmcnt(4)\n\t"
      "v_smfmac_f32_16x16x64_bf16 v[232:235], v[24:27], v[168:175], v6\n\tv_smfmac_f32_16x16x64_bf16 v[236:239], v[24:27], v[184:191], v6\n\ts_nop 1\n\ts_waitcnt lgkmcnt(2)\n\t"
      "v_smfmac_f32_16x16x64_bf16 v[228:231], v[20:23], v[208:215], v6\n\ts_nop 1\n\ts_waitcnt lgkmcnt(0)\n\tv_smfmac_f32_16x16x64_bf16 v[228:231], v[24:27], v[216:223], v6\n\t"
      "v_add_f32 v232, v232, v233\n\tv_add_f32 v234, v234, v235\n\tv_add_f32 v236, v236, v237\n\tv_add_f32 v238, v238, v239\n\t"
      "v_cndmask_b32_dpp v194, v234, v232, vcc quad_perm:[1,0,3,2] row_mask:0xf bank_mask:0xf\n\ts_not_b64 vcc, vcc\n\t"
      "v_cndmask_b32_dpp v195, v232, v234, vcc quad_perm:[1,0,3,2] row_mask:0xf bank_mask:0xf\n\t"
      "v_cndmask_b32_dpp v197, v236, v238, vcc quad_perm:[1,0,3,2] row_mask:0xf bank_mask:0xf\n\tv_add_f32 v228, v228, v229\n\tv_add_f32 v230, v230, v231\n\t"
      "v_max3_f32 v198, v194, v195, v197\n\tv_pk_mul_f32 v[194:195], v[194:195], s[28:29]\n\t"
      "v_cndmask_b32_dpp v193, v228, v230, vcc quad_perm:[1,0,3,2] row_mask:0xf bank_mask:0xf\n\ts_not_b64 vcc, vcc\n\t"
      "v_cndmask_b32_dpp v196, v238, v236, vcc quad_perm:[1,0,3,2] row_mask:0xf bank_mask:0xf\n\t"
      "v_cndmask_b32_dpp v192, v230, v228, vcc quad_perm:[1,0,3,2] row_mask:0xf bank_mask:0xf\n\tv_max3_f32 v198, v198, v196, v193\n\tv_max_f32 v198, v198, v192\n\t"
      "v_pk_mul_f32 v[196:197], v[196:197], s[28:29]\n\tv_pk_mul_f32 v[192:193], v[192:193], s[28:29]\n\tv_max_f32_dpp v198, v198, v198 row_ror:8 row_mask:0xf bank_mask:0xf\n\t"
      "s_nop 1\n\tv_max_f32_dpp v198, v198, v198 row_ror:4 row_mask:0xf bank_mask:0xf\n\ts_nop 1\n\tv_max_f32_dpp v198, v198, v198 row_ror:2 row_mask:0xf bank_mask:0xf\n\t"
      "v_mul_f32 v198, s28, v198\n\tv_pk_add_f32 v[192:193], v[192:193], v[198:199] op_sel_hi:[1,0] neg_lo:[0,1] neg_hi:[0,1]\n\t"
      "v_pk_add_f32 v[194:195], v[194:195], v[198:199] op_sel_hi:[1,0] neg_lo:[0,1] neg_hi:[0,1]\n\t"
      "v_pk_add_f32 v[196:197], v[196:197], v[198:199] op_sel_hi:[1,0] neg_lo:[0,1] neg_hi:[0,1]\n\tv_exp_f32 v192, v192\n\tv_exp_f32 v193, v193\n\tv_exp_f32 v194, v194\n\t"
      "v_exp_f32 v195, v195\n\tv_exp_f32 v196, v196\n\tv_pk_fma_f32 v[198:199], v[192:193], v[12:13], v[194:195] op_sel:[0,1,0]\n\tv_exp_f32 v197, v197\n\t"
      "v_add_f32 v198, v198, v196\n\tv_add_f32 v199, v199, v197\n\tv_add_f32 v198, v198, v199\n\ts_nop 1\n\t"
      "v_add_f32_dpp v198, v198, v198 row_ror:8 row_mask:0xf bank_mask:0xf\n\ts_nop 1\n\tv_add_f32_dpp v198, v198, v198 row_ror:4 row_mask:0xf bank_mask:0xf\n\ts_nop 1\n\t"
      "v_add_f32_dpp v198, v198, v198 row_ror:2 row_mask:0xf bank_mask:0xf\n\tv_rcp_f32 v198, v198\n\ts_nop 0\n\t"
      "v_pk_mul_f32 v[194:195], v[194:195], v[198:199] op_sel_hi:[1,0]\n\tv_pk_mul_f32 v[196:197], v[196:197], v[198:199] op_sel_hi:[1,0]\n\t"
      "v_cvt_pk_bf16_f32 v200, v194, v195\n\tv_cvt_pk_bf16_f32 v201, v196, v197\n\tv_pk_mul_f32 v[192:193], v[192:193], v[198:199] op_sel_hi:[1,0]\n\t"
      "buffer_store_dwordx2 v[200:201], v10, s[8:11], s41 offen sc1\n\tv_cvt_pk_bf16_f32 v202, v192, v193\n\tbuffer_store_dword v202, v11, s[8:11], s41 offen sc1\n\t"
      TK6
      "s_memrealtime vcc\n\ts_waitcnt lgkmcnt(0)\n\ts_sub_u32 s48, vcc_lo, s46\n\ts_lshl_b32 s48, s48, 2\n\ts_min_u32 s48, s48, 800\n\t"
      STS("s_add_u32 s51, s51, s48\n\t")
      "s_lshl_b32 s48, s48, 4\n\ts_sub_i32 s48, s48, s20\n\ts_ashr_i32 s48, s48, 2\n\ts_add_i32 s20, s20, s48\n\tv_mov_b64 v[204:205], -1\n\t"
      "buffer_store_dwordx2 v[204:205], v10, s[8:11], s42 offen sc1\n\tbuffer_store_dword v204, v11, s[8:11], s42 offen sc1\n\t"
      "buffer_store_dword v204, v14, s[8:11], s42 offen sc1\n\ts_branch 69f\n\t"
      "39:\n\ts_waitcnt lgkmcnt(0)\n\ts_barrier\n\t"
      "40:\n\t"
      TK5
      "s_sleep 2\n\tds_read_b128 v[208:211], v8 offset:0\n\tds_read_b128 v[212:215], v8 offset:64\n\tds_read_b128 v[216:219], v8 offset:128\n\t"
      "ds_read_b128 v[220:223], v8 offset:192\n\ts_and_b32 s48, s17, 7\n\ts_lshl_b32 s43, s48, 17\n\ts_add_u32 s43, s43, 0x300000\n\ts_add_u32 s48, s17, 3\n\t"
      "s_and_b32 s48, s48, 7\n\ts_lshl_b32 s44, s48, 17\n\ts_add_u32 s44, s44, 0x300000\n\ts_cmp_lg_u32 s58, 0\n\ts_cbranch_scc1 43f\n\t"
      "41:\n\ts_cmp_gt_u32 s55, s17\n\ts_cbranch_scc1 42f\n\tbuffer_load_dword v17, off, s[12:15], s45 sc1\n\ts_waitcnt vmcnt(0)\n\tv_readfirstlane_b32 s55, v17\n\t"
      "s_cmp_eq_u32 s18, 0\n\ts_cbranch_scc1 42f\n\ts_sub_u32 s18, s18, 1\n\ts_sleep 1\n\ts_branch 41b\n\t"
      "42:\n\tbuffer_load_dwordx4 v[240:243], v12, s[8:11], s22 offen offset:0 sc1\n\tbuffer_load_dwordx4 v[244:247], v12, s[8:11], s22 offen offset:2048 sc1\n\t"
      "buffer_load_dwordx4 v[248:251], v12, s[8:11], s49 offen offset:0 sc1\n\tbuffer_load_dwordx4 v[252:255], v12, s[8:11], s49 offen offset:2048 sc1\n\t"
      "43:\n\ts_waitcnt vmcnt(0)\n\tv_mov_b64 v[204:205], -1\n\tv_mov_b64 v[206:207], -1\n\tv_mov_b32 v203, s30\n\ts_cmp_eq_u32 s16, 2\n\ts_cbranch_scc1 50f\n\t"
      "v_mfma_f32_16x16x32_bf16 v[228:231], v[160:163], v[240:243], 0\n\tv_mfma_f32_16x16x32_bf16 v[228:231], v[164:167], v[244:247], v[228:231]\n\t"
      "v_mfma_f32_16x16x32_bf16 v[228:231], v[168:171], v[248:251], v[228:231]\n\tv_mfma_f32_16x16x32_bf16 v[228:231], v[172:175], v[252:255], v[228:231]\n\t"
      "s_waitcnt lgkmcnt(0)\n\tv_mfma_f32_16x16x32_bf16 v[224:227], v[208:211], v[240:243], 0\n\tv_mfma_f32_16x16x32_bf16 v[224:227], v[212:215], v[244:247], v[224:227]\n\t"
      "v_mfma_f32_16x16x32_bf16 v[224:227], v[216:219], v[248:251], v[224:227]\n\tv_mfma_f32_16x16x32_bf16 v[224:227], v[220:223], v[252:255], v[224:227]\n\ts_nop 2\n\t"
      "v_cndmask_b32 v228, v203, v228, s[32:33]\n\tv_cndmask_b32 v229, v203, v229, s[34:35]\n\tv_cndmask_b32 v230, v203, v230, s[36:37]\n\t"
      "v_cndmask_b32 v231, v203, v231, s[38:39]\n\tv_max3_f32 v192, v228, v229, v230\n\tv_max3_f32 v193, v231, v224, v225\n\tv_max3_f32 v192, v192, v226, v227\n\t"
      "v_max_f32 v192, v192, v193\n\tv_mov_b32 v193, v192\n\ts_nop 1\n\tv_permlane32_swap_b32 v192, v193\n\tv_max_f32 v192, v192, v193\n\tv_mov_b32 v193, v192\n\ts_nop 1\n\t"
      "v_permlane16_swap_b32 v192, v193\n\tv_max_f32 v192, v192, v193\n\tv_mov_b32 v193, v192\n\tv_pk_add_f32 v[224:225], v[224:225], v[192:193] neg_lo:[0,1] neg_hi:[0,1]\n\t"
      "v_pk_add_f32 v[226:227], v[226:227], v[192:193] neg_lo:[0,1] neg_hi:[0,1]\n\tv_pk_add_f32 v[228:229], v[228:229], v[192:193] neg_lo:[0,1] neg_hi:[0,1]\n\t"
      "v_pk_add_f32 v[230:231], v[230:231], v[192:193] neg_lo:[0,1] neg_hi:[0,1]\n\tv_pk_mul_f32 v[224:225], v[224:225], s[28:29]\n\t"
      "v_pk_mul_f32 v[226:227], v[226:227], s[28:29]\n\tv_pk_mul_f32 v[228:229], v[228:229], s[28:29]\n\tv_pk_mul_f32 v[230:231], v[230:231], s[28:29]\n\t"
      "v_exp_f32 v224, v224\n\tv_exp_f32 v225, v225\n\tv_exp_f32 v226, v226\n\tv_exp_f32 v227, v227\n\tv_exp_f32 v228, v228\n\tv_exp_f32 v229, v229\n\tv_exp_f32 v230, v230\n\t"
      "v_exp_f32 v231, v231\n\tv_pk_add_f32 v[192:193], v[224:225], v[226:227]\n\tv_pk_add_f32 v[194:195], v[228:229], v[230:231]\n\tv_add_f32 v192, v192, v193\n\t"
      "v_add_f32 v194, v194, v195\n\tv_fma_f32 v192, v192, v13, v194\n\tv_mov_b32 v193, v192\n\ts_nop 1\n\tv_permlane32_swap_b32 v192, v193\n\tv_add_f32 v192, v192, v193\n\t"
      "v_mov_b32 v193, v192\n\ts_nop 1\n\tv_permlane16_swap_b32 v192, v193\n\tv_add_f32 v192, v192, v193\n\tv_rcp_f32 v192, v192\n\ts_nop 0\n\tv_mov_b32 v193, v192\n\t"
      "v_pk_mul_f32 v[224:225], v[224:225], v[192:193]\n\tv_pk_mul_f32 v[226:227], v[226:227], v[192:193]\n\tv_pk_mul_f32 v[228:229], v[228:229], v[192:193]\n\t"
      "v_pk_mul_f32 v[230:231], v[230:231], v[192:193]\n\tv_cvt_pk_bf16_f32 v194, v224, v225\n\tv_cvt_pk_bf16_f32 v195, v226, v227\n\tv_cvt_pk_bf16_f32 v196, v228, v229\n\t"
      "v_cvt_pk_bf16_f32 v197, v230, v231\n\tbuffer_store_dwordx2 v[196:197], v10, s[8:11], s43 offen sc1\n\tbuffer_store_dwordx2 v[194:195], v11, s[8:11], s43 offen sc1\n\t"
      "buffer_store_dwordx2 v[204:205], v10, s[8:11], s44 offen sc1\n\tbuffer_store_dwordx2 v[204:205], v11, s[8:11], s44 offen sc1\n\t"
      TK6
      "s_branch 69f\n\t"
      "50:\n\tv_mfma_f32_16x16x32_bf16 v[228:231], v[160:163], v[240:243], 0\n\tv_mfma_f32_16x16x32_bf16 v[232:235], v[176:179], v[240:243], 0\n\t"
      "v_mfma_f32_16x16x32_bf16 v[228:231], v[164:167], v[244:247], v[228:231]\n\tv_mfma_f32_16x16x32_bf16 v[232:235], v[180:183], v[244:247], v[232:235]\n\t"
      "v_mfma_f32_16x16x32_bf16 v[228:231], v[168:171], v[248:251], v[228:231]\n\tv_mfma_f32_16x16x32_bf16 v[232:235], v[184:187], v[248:251], v[232:235]\n\t"
      "v_mfma_f32_16x16x32_bf16 v[228:231], v[172:175], v[252:255], v[228:231]\n\tv_mfma_f32_16x16x32_bf16 v[232:235], v[188:191], v[252:255], v[232:235]\n\t"
      "s_waitcnt lgkmcnt(0)\n\tv_mfma_f32_16x16x32_bf16 v[224:227], v[208:211], v[240:243], 0\n\tv_mfma_f32_16x16x32_bf16 v[224:227], v[212:215], v[244:247], v[224:227]\n\t"
      "v_mfma_f32_16x16x32_bf16 v[224:227], v[216:219], v[248:251], v[224:227]\n\tv_mfma_f32_16x16x32_bf16 v[224:227], v[220:223], v[252:255], v[224:227]\n\ts_nop 2\n\t"
      "v_cndmask_b32 v232, v203, v232, s[32:33]\n\tv_cndmask_b32 v233, v203, v233, s[34:35]\n\tv_cndmask_b32 v234, v203, v234, s[36:37]\n\t"
      "v_cndmask_b32 v235, v203, v235, s[38:39]\n\tv_max3_f32 v192, v228, v229, v230\n\tv_max3_f32 v193, v231, v224, v225\n\tv_max3_f32 v192, v192, v226, v227\n\t"
      "v_max3_f32 v193, v193, v232, v233\n\tv_max3_f32 v192, v192, v234, v235\n\tv_max_f32 v192, v192, v193\n\tv_mov_b32 v193, v192\n\ts_nop 1\n\t"
      "v_permlane32_swap_b32 v192, v193\n\tv_max_f32 v192, v192, v193\n\tv_mov_b32 v193, v192\n\ts_nop 1\n\tv_permlane16_swap_b32 v192, v193\n\tv_max_f32 v192, v192, v193\n\t"
      "v_mov_b32 v193, v192\n\tv_pk_add_f32 v[224:225], v[224:225], v[192:193] neg_lo:[0,1] neg_hi:[0,1]\n\t"
      "v_pk_add_f32 v[226:227], v[226:227], v[192:193] neg_lo:[0,1] neg_hi:[0,1]\n\tv_pk_add_f32 v[228:229], v[228:229], v[192:193] neg_lo:[0,1] neg_hi:[0,1]\n\t"
      "v_pk_add_f32 v[230:231], v[230:231], v[192:193] neg_lo:[0,1] neg_hi:[0,1]\n\tv_pk_add_f32 v[232:233], v[232:233], v[192:193] neg_lo:[0,1] neg_hi:[0,1]\n\t"
      "v_pk_add_f32 v[234:235], v[234:235], v[192:193] neg_lo:[0,1] neg_hi:[0,1]\n\tv_pk_mul_f32 v[224:225], v[224:225], s[28:29]\n\t"
      "v_pk_mul_f32 v[226:227], v[226:227], s[28:29]\n\tv_pk_mul_f32 v[228:229], v[228:229], s[28:29]\n\tv_pk_mul_f32 v[230:231], v[230:231], s[28:29]\n\t"
      "v_pk_mul_f32 v[232:233], v[232:233], s[28:29]\n\tv_pk_mul_f32 v[234:235], v[234:235], s[28:29]\n\tv_exp_f32 v224, v224\n\tv_exp_f32 v225, v225\n\tv_exp_f32 v226, v226\n\t"
      "v_exp_f32 v227, v227\n\tv_exp_f32 v228, v228\n\tv_exp_f32 v229, v229\n\tv_exp_f32 v230, v230\n\tv_exp_f32 v231, v231\n\tv_exp_f32 v232, v232\n\tv_exp_f32 v233, v233\n\t"
      "v_exp_f32 v234, v234\n\tv_exp_f32 v235, v235\n\tv_pk_add_f32 v[192:193], v[224:225], v[226:227]\n\tv_pk_add_f32 v[194:195], v[228:229], v[230:231]\n\t"
      "v_pk_add_f32 v[196:197], v[232:233], v[234:235]\n\tv_pk_add_f32 v[194:195], v[194:195], v[196:197]\n\tv_add_f32 v192, v192, v193\n\tv_add_f32 v194, v194, v195\n\t"
      "v_fma_f32 v192, v192, v13, v194\n\tv_mov_b32 v193, v192\n\ts_nop 1\n\tv_permlane32_swap_b32 v192, v193\n\tv_add_f32 v192, v192, v193\n\tv_mov_b32 v193, v192\n\t"
      "s_nop 1\n\tv_permlane16_swap_b32 v192, v193\n\tv_add_f32 v192, v192, v193\n\tv_rcp_f32 v192, v192\n\ts_nop 0\n\tv_mov_b32 v193, v192\n\t"
      "v_pk_mul_f32 v[224:225], v[224:225], v[192:193]\n\tv_pk_mul_f32 v[226:227], v[226:227], v[192:193]\n\tv_pk_mul_f32 v[228:229], v[228:229], v[192:193]\n\t"
      "v_pk_mul_f32 v[230:231], v[230:231], v[192:193]\n\tv_pk_mul_f32 v[232:233], v[232:233], v[192:193]\n\tv_pk_mul_f32 v[234:235], v[234:235], v[192:193]\n\t"
      "v_cvt_pk_bf16_f32 v194, v224, v225\n\tv_cvt_pk_bf16_f32 v195, v226, v227\n\tv_cvt_pk_bf16_f32 v196, v228, v229\n\tv_cvt_pk_bf16_f32 v197, v230, v231\n\t"
      "v_cvt_pk_bf16_f32 v198, v232, v233\n\tv_cvt_pk_bf16_f32 v199, v234, v235\n\tbuffer_store_dwordx4 v[196:199], v10, s[8:11], s43 offen sc1\n\t"
      "buffer_store_dwordx2 v[194:195], v11, s[8:11], s43 offen sc1\n\tbuffer_store_dwordx4 v[204:207], v10, s[8:11], s44 offen sc1\n\t"
      "buffer_store_dwordx2 v[204:205], v11, s[8:11], s44 offen sc1\n\t"
      TK6
      "s_branch 69f\n\t"
      "60:\n\t"
      TK5
      "s_sleep 7\n\tv_mov_b32 v17, s17\n\tbuffer_store_dword v17, v12, s[12:15], 0 offen sc1\n\tbuffer_load_dword v18, v15, s[8:11], s41 offen sc1\n\t"
      "64:\n\ts_add_u32 s49, s17, 1\n\ts_cmp_eq_u32 s49, 64\n\ts_cbranch_scc1 69f\n\ts_add_u32 s48, s45, 0x1000\n\t"
      "61:\n\ts_cmp_gt_u32 s54, s49\n\ts_cbranch_scc1 62f\n\tbuffer_load_dword v17, off, s[12:15], s48 sc1\n\ts_waitcnt vmcnt(0)\n\tv_readfirstlane_b32 s54, v17\n\t"
      "s_cmp_eq_u32 s18, 0\n\ts_cbranch_scc1 62f\n\ts_sub_u32 s18, s18, 1\n\ts_sleep 1\n\ts_branch 61b\n\t"
      "62:\n\ts_add_u32 s46, s31, 0x1000\n\ts_mov_b32 m0, 147456\n\ts_add_u32 s49, s49, 1\n\tbuffer_load_dwordx4 v10, s[8:11], s31 offen offset:0 sc1 lds\n\t"
      "buffer_load_dwordx4 v10, s[8:11], s31 offen offset:1024 sc1 lds\n\tbuffer_load_dwordx4 v10, s[8:11], s31 offen offset:2048 sc1 lds\n\t"
      "buffer_load_dwordx4 v10, s[8:11], s31 offen offset:3072 sc1 lds\n\ts_mov_b32 m0, 151552\n\ts_nop 0\n\tbuffer_load_dwordx4 v10, s[8:11], s46 offen offset:0 sc1 lds\n\t"
      "buffer_load_dwordx4 v10, s[8:11], s46 offen offset:1024 sc1 lds\n\tbuffer_load_dwordx4 v10, s[8:11], s46 offen offset:2048 sc1 lds\n\t"
      "buffer_load_dwordx4 v10, s[8:11], s46 offen offset:3072 sc1 lds\n\t"
      "s_min_u32 s46, s55, s54\n\ts_cmp_gt_u32 s46, s49\n\ts_cbranch_scc1 63f\n\tbuffer_load_dword v17, off, s[12:15], s45 sc1\n\t"
      "buffer_load_dword v19, off, s[12:15], s48 sc1\n\ts_waitcnt vmcnt(0)\n\tds_write_b32 v16, v17 offset:4\n\tv_readfirstlane_b32 s54, v19\n\t"
      "63:\n\ts_cmp_lt_i32 s17, 0\n\ts_cbranch_scc1 69f\n\t"
      TK6
      "s_waitcnt vmcnt(8)\n\ts_branch 76f\n\t"
      "74:\n\tbuffer_load_dword v18, v15, s[8:11], s41 offen sc1\n\ts_waitcnt vmcnt(0)\n\t"
      "76:\n\tv_max_u32_dpp v18, v18, v18 row_ror:8 row_mask:0xf bank_mask:0xf\n\ts_nop 1\n\tv_max_u32_dpp v18, v18, v18 row_ror:4 row_mask:0xf bank_mask:0xf\n\ts_nop 1\n\t"
      "v_max_u32_dpp v18, v18, v18 row_ror:2 row_mask:0xf bank_mask:0xf\n\ts_nop 1\n\tv_max_u32_dpp v18, v18, v18 row_ror:1 row_mask:0xf bank_mask:0xf\n\ts_nop 0\n\t"
      "v_readlane_b32 s52, v18, 0\n\tv_readlane_b32 s48, v18, 16\n\ts_max_u32 s52, s52, s48\n\ts_cmp_lg_u32 s52, -1\n\ts_cbranch_scc1 75f\n\ts_cmp_eq_u32 s18, 0\n\t"
      "s_cbranch_scc1 75f\n\ts_sub_u32 s18, s18, 1\n\ts_branch 74b\n\t"
      "75:\n\ts_sub_u32 s48, s52, s19\n\ts_cmp_gt_u32 s48, s72\n\ts_cselect_b32 s48, s73, 0\n\ts_add_u32 s19, s52, s23\n\ts_add_u32 s19, s19, s48\n\ts_branch 69f\n\t"
      "86:\n\t"
      STS("s_add_u32 s50, s50, 1\n\t")
      "s_waitcnt lgkmcnt(0)\n\tbuffer_load_dwordx4 v[240:243], v0, s[8:11], s68 offen sc1\n\tbuffer_load_dwordx4 v[244:247], v0, s[8:11], s69 offen sc1\n\t"
      "buffer_load_dwordx4 v[248:251], v0, s[8:11], s70 offen sc1\n\tbuffer_load_dwordx4 v[252:255], v0, s[8:11], s71 offen sc1\n\ts_nop 3\n\tv_mov_b64 v[224:225], 0\n\t"
      "v_mov_b64 v[226:227], 0\n\tv_mov_b64 v[228:229], 0\n\tv_mov_b64 v[230:231], 0\n\tv_mov_b64 v[232:233], 0\n\tv_mov_b64 v[234:235], 0\n\tv_mov_b64 v[236:237], 0\n\t"
      "v_mov_b64 v[238:239], 0\n\ts_nop 1\n\ts_branch 84f\n\t"
      "83:\n\tbuffer_load_dwordx4 v[240:243], v0, s[8:11], s68 offen sc1\n\tbuffer_load_dwordx4 v[244:247], v0, s[8:11], s69 offen sc1\n\t"
      "buffer_load_dwordx4 v[248:251], v0, s[8:11], s70 offen sc1\n\tbuffer_load_dwordx4 v[252:255], v0, s[8:11], s71 offen sc1\n\t"
      "84:\n\ts_waitcnt vmcnt(0)\n\tv_max3_u32 v17, v240, v241, v242\n\tv_max_u32 v17, v17, v243\n\tv_max3_u32 v17, v17, v244, v245\n\tv_max3_u32 v17, v17, v246, v247\n\t"
      "v_max3_u32 v17, v17, v248, v249\n\tv_max3_u32 v17, v17, v250, v251\n\tv_max3_u32 v17, v17, v252, v253\n\tv_max3_u32 v17, v17, v254, v255\n\tv_cmp_eq_u32 vcc, -1, v17\n\t"
      "s_cbranch_vccz 85f\n\ts_cmp_eq_u32 s18, 0\n\ts_cbranch_scc1 85f\n\ts_sub_u32 s18, s18, 1\n\ts_branch 83b\n\t"
      "85:\n\tv_smfmac_f32_16x16x64_bf16 v[224:227], v[240:243], a[0:7], v6\n\tv_smfmac_f32_16x16x64_bf16 v[228:231], v[240:243], a[8:15], v6\n\t"
      "v_smfmac_f32_16x16x64_bf16 v[232:235], v[240:243], v[32:39], v6\n\tv_smfmac_f32_16x16x64_bf16 v[236:239], v[240:243], v[160:167], v6\n\t"
      "v_smfmac_f32_16x16x64_bf16 v[224:227], v[244:247], a[16:23], v6\n\tv_smfmac_f32_16x16x64_bf16 v[228:231], v[244:247], a[24:31], v6\n\t"
      "ds_write_b128 v1, v[240:243] offset:0\n\tds_write_b128 v1, v[244:247] offset:1024\n\tds_write_b128 v1, v[248:251] offset:2048\n\t"
      "ds_write_b128 v1, v[252:255] offset:3072\n\tv_mov_b32 v17, 0\n\ts_mov_b64 vcc, 0\n\ts_waitcnt lgkmcnt(0)\n\ts_cmp_lt_u32 s16, 2\n\ts_cbranch_scc1 20b\n\ts_branch 24b\n\t"
      "87:\n\t"
      STS("s_add_u32 s50, s50, 1\n\t")
      "s_waitcnt lgkmcnt(0)\n\tbuffer_load_dwordx4 v[244:247], v0, s[8:11], s69 offen sc1\n\tbuffer_load_dwordx4 v[248:251], v0, s[8:11], s70 offen sc1\n\t"
      "buffer_load_dwordx4 v[252:255], v0, s[8:11], s71 offen sc1\n\ts_nop 3\n\tv_mov_b64 v[224:225], 0\n\tv_mov_b64 v[226:227], 0\n\tv_mov_b64 v[228:229], 0\n\t"
      "v_mov_b64 v[230:231], 0\n\tv_mov_b64 v[232:233], 0\n\tv_mov_b64 v[234:235], 0\n\tv_mov_b64 v[236:237], 0\n\tv_mov_b64 v[238:239], 0\n\ts_nop 1\n\t"
      "v_smfmac_f32_16x16x64_bf16 v[224:227], v[240:243], a[0:7], v6\n\tv_smfmac_f32_16x16x64_bf16 v[228:231], v[240:243], a[8:15], v6\n\t"
      "v_smfmac_f32_16x16x64_bf16 v[232:235], v[240:243], v[32:39], v6\n\tv_smfmac_f32_16x16x64_bf16 v[236:239], v[240:243], v[160:167], v6\n\ts_branch 84f\n\t"
      "83:\n\tbuffer_load_dwordx4 v[244:247], v0, s[8:11], s69 offen sc1\n\tbuffer_load_dwordx4 v[248:251], v0, s[8:11], s70 offen sc1\n\t"
      "buffer_load_dwordx4 v[252:255], v0, s[8:11], s71 offen sc1\n\t"
      "84:\n\ts_waitcnt vmcnt(0)\n\tv_max3_u32 v17, v244, v245, v246\n\tv_max_u32 v17, v17, v247\n\tv_max3_u32 v17, v17, v248, v249\n\tv_max3_u32 v17, v17, v250, v251\n\t"
      "v_max3_u32 v17, v17, v252, v253\n\tv_max3_u32 v17, v17, v254, v255\n\tv_cmp_eq_u32 vcc, -1, v17\n\ts_cbranch_vccz 85f\n\ts_cmp_eq_u32 s18, 0\n\ts_cbranch_scc1 85f\n\t"
      "s_sub_u32 s18, s18, 1\n\ts_branch 83b\n\t"
      "85:\n\tv_smfmac_f32_16x16x64_bf16 v[224:227], v[244:247], a[16:23], v6\n\tv_smfmac_f32_16x16x64_bf16 v[228:231], v[244:247], a[24:31], v6\n\t"
      "v_smfmac_f32_16x16x64_bf16 v[232:235], v[244:247], v[40:47], v6\n\tv_smfmac_f32_16x16x64_bf16 v[236:239], v[244:247], v[168:175], v6\n\t"
      "v_smfmac_f32_16x16x64_bf16 v[224:227], v[248:251], a[32:39], v6\n\tv_smfmac_f32_16x16x64_bf16 v[228:231], v[248:251], a[40:47], v6\n\t"
      "ds_write_b128 v1, v[244:247] offset:1024\n\tds_write_b128 v1, v[248:251] offset:2048\n\tds_write_b128 v1, v[252:255] offset:3072\n\tv_mov_b32 v17, 0\n\t"
      "s_mov_b64 vcc, 0\n\ts_waitcnt lgkmcnt(0)\n\ts_cmp_lt_u32 s16, 2\n\ts_cbranch_scc1 21b\n\ts_branch 25b\n\t"
      "88:\n\t"
      STS("s_add_u32 s50, s50, 1\n\t")
      "s_waitcnt lgkmcnt(0)\n\tbuffer_load_dwordx4 v[248:251], v0, s[8:11], s70 offen sc1\n\tbuffer_load_dwordx4 v[252:255], v0, s[8:11], s71 offen sc1\n\ts_nop 3\n\t"
      "v_mov_b64 v[224:225], 0\n\tv_mov_b64 v[226:227], 0\n\tv_mov_b64 v[228:229], 0\n\tv_mov_b64 v[230:231], 0\n\tv_mov_b64 v[232:233], 0\n\tv_mov_b64 v[234:235], 0\n\t"
      "v_mov_b64 v[236:237], 0\n\tv_mov_b64 v[238:239], 0\n\ts_nop 1\n\tv_smfmac_f32_16x16x64_bf16 v[224:227], v[240:243], a[0:7], v6\n\t"
      "v_smfmac_f32_16x16x64_bf16 v[228:231], v[240:243], a[8:15], v6\n\tv_smfmac_f32_16x16x64_bf16 v[232:235], v[240:243], v[32:39], v6\n\t"
      "v_smfmac_f32_16x16x64_bf16 v[236:239], v[240:243], v[160:167], v6\n\tv_smfmac_f32_16x16x64_bf16 v[224:227], v[244:247], a[16:23], v6\n\t"
      "v_smfmac_f32_16x16x64_bf16 v[228:231], v[244:247], a[24:31], v6\n\tv_smfmac_f32_16x16x64_bf16 v[232:235], v[244:247], v[40:47], v6\n\t"
      "v_smfmac_f32_16x16x64_bf16 v[236:239], v[244:247], v[168:175], v6\n\ts_branch 84f\n\t"
      "83:\n\tbuffer_load_dwordx4 v[248:251], v0, s[8:11], s70 offen sc1\n\tbuffer_load_dwordx4 v[252:255], v0, s[8:11], s71 offen sc1\n\t"
      "84:\n\ts_waitcnt vmcnt(0)\n\tv_max3_u32 v17, v248, v249, v250\n\tv_max_u32 v17, v17, v251\n\tv_max3_u32 v17, v17, v252, v253\n\tv_max3_u32 v17, v17, v254, v255\n\t"
      "v_cmp_eq_u32 vcc, -1, v17\n\ts_cbranch_vccz 85f\n\ts_cmp_eq_u32 s18, 0\n\ts_cbranch_scc1 85f\n\ts_sub_u32 s18, s18, 1\n\ts_branch 83b\n\t"
      "85:\n\tv_smfmac_f32_16x16x64_bf16 v[224:227], v[248:251], a[32:39], v6\n\tv_smfmac_f32_16x16x64_bf16 v[228:231], v[248:251], a[40:47], v6\n\t"
      "v_smfmac_f32_16x16x64_bf16 v[232:235], v[248:251], v[48:55], v6\n\tv_smfmac_f32_16x16x64_bf16 v[236:239], v[248:251], v[176:183], v6\n\t"
      "v_smfmac_f32_16x16x64_bf16 v[224:227], v[252:255], a[48:55], v6\n\tv_smfmac_f32_16x16x64_bf16 v[228:231], v[252:255], a[56:63], v6\n\t"
      "ds_write_b128 v1, v[248:251] offset:2048\n\tds_write_b128 v1, v[252:255] offset:3072\n\tv_mov_b32 v17, 0\n\ts_mov_b64 vcc, 0\n\ts_waitcnt lgkmcnt(0)\n\t"
      "s_cmp_lt_u32 s16, 2\n\ts_cbranch_scc1 22b\n\ts_branch 26b\n\t"
      "89:\n\t"
      STS("s_add_u32 s50, s50, 1\n\t")
      "s_waitcnt lgkmcnt(0)\n\tbuffer_load_dwordx4 v[252:255], v0, s[8:11], s71 offen sc1\n\ts_nop 3\n\tv_mov_b64 v[224:225], 0\n\tv_mov_b64 v[226:227], 0\n\t"
      "v_mov_b64 v[228:229], 0\n\tv_mov_b64 v[230:231], 0\n\tv_mov_b64 v[232:233], 0\n\tv_mov_b64 v[234:235], 0\n\tv_mov_b64 v[236:237], 0\n\tv_mov_b64 v[238:239], 0\n\t"
      "s_nop 1\n\tv_smfmac_f32_16x16x64_bf16 v[224:227], v[240:243], a[0:7], v6\n\tv_smfmac_f32_16x16x64_bf16 v[228:231], v[240:243], a[8:15], v6\n\t"
      "v_smfmac_f32_16x16x64_bf16 v[232:235], v[240:243], v[32:39], v6\n\tv_smfmac_f32_16x16x64_bf16 v[236:239], v[240:243], v[160:167], v6\n\t"
      "v_smfmac_f32_16x16x64_bf16 v[224:227], v[244:247], a[16:23], v6\n\tv_smfmac_f32_16x16x64_bf16 v[228:231], v[244:247], a[24:31], v6\n\t"
      "v_smfmac_f32_16x16x64_bf16 v[232:235], v[244:247], v[40:47], v6\n\tv_smfmac_f32_16x16x64_bf16 v[236:239], v[244:247], v[168:175], v6\n\t"
      "v_smfmac_f32_16x16x64_bf16 v[224:227], v[248:251], a[32:39], v6\n\tv_smfmac_f32_16x16x64_bf16 v[228:231], v[248:251], a[40:47], v6\n\t"
      "v_smfmac_f32_16x16x64_bf16 v[232:235], v[248:251], v[48:55], v6\n\tv_smfmac_f32_16x16x64_bf16 v[236:239], v[248:251], v[176:183], v6\n\ts_branch 84f\n\t"
      "83:\n\tbuffer_load_dwordx4 v[252:255], v0, s[8:11], s71 offen sc1\n\t"
      "84:\n\ts_waitcnt vmcnt(0)\n\tv_max3_u32 v17, v252, v253, v254\n\tv_max_u32 v17, v17, v255\n\tv_cmp_eq_u32 vcc, -1, v17\n\ts_cbranch_vccz 85f\n\ts_cmp_eq_u32 s18, 0\n\t"
      "s_cbranch_scc1 85f\n\ts_sub_u32 s18, s18, 1\n\ts_branch 83b\n\t"
      "85:\n\tv_smfmac_f32_16x16x64_bf16 v[224:227], v[252:255], a[48:55], v6\n\tv_smfmac_f32_16x16x64_bf16 v[228:231], v[252:255], a[56:63], v6\n\t"
      "v_smfmac_f32_16x16x64_bf16 v[232:235], v[252:255], v[56:63], v6\n\tv_smfmac_f32_16x16x64_bf16 v[236:239], v[252:255], v[184:191], v6\n\t"
      "v_smfmac_f32_16x16x64_bf16 v[224:227], v[20:23], a[64:71], v6\n\tv_smfmac_f32_16x16x64_bf16 v[228:231], v[20:23], a[72:79], v6\n\t"
      "ds_write_b128 v1, v[252:255] offset:3072\n\tv_mov_b32 v17, 0\n\ts_mov_b64 vcc, 0\n\ts_waitcnt lgkmcnt(0)\n\ts_cmp_lt_u32 s16, 2\n\ts_cbranch_scc1 23b\n\ts_branch 27b\n\t"
      "69:\n\t"
      TK7
      STS("s_cmp_lt_i32 s17, 8\n\ts_cselect_b32 s50, 0, s50\n\ts_cselect_b32 s51, 0, s51\n\ts_cselect_b32 s61, 0, s61\n\t")
      TKS("s_waitcnt lgkmcnt(0)\n\ts_cmp_lt_i32 s17, 0\n\ts_cbranch_scc1 98f\n\ts_cmp_lg_u32 s16, s91\n\ts_cbranch_scc1 98f\n\ts_mov_b64 exec, 1\n\ts_lshl_b32 s48, s17, 4\n\ts_add_u32 s48, s48, 161056\n\tv_mov_b32 v17, s48\n\ts_sub_u32 s48, s74, s90\n\ts_min_u32 s48, s48, 0xffff\n\tv_mov_b32 v18, s48\n\tds_write_b16 v17, v18 offset:0\n\ts_sub_u32 s48, s76, s74\n\ts_min_u32 s48, s48, 0xffff\n\tv_mov_b32 v18, s48\n\tds_write_b16 v17, v18 offset:2\n\ts_sub_u32 s48, s78, s76\n\ts_min_u32 s48, s48, 0xffff\n\tv_mov_b32 v18, s48\n\tds_write_b16 v17, v18 offset:4\n\ts_sub_u32 s48, s80, s78\n\ts_min_u32 s48, s48, 0xffff\n\tv_mov_b32 v18, s48\n\tds_write_b16 v17, v18 offset:6\n\ts_sub_u32 s48, s82, s80\n\ts_min_u32 s48, s48, 0xffff\n\tv_mov_b32 v18, s48\n\tds_write_b16 v17, v18 offset:8\n\ts_sub_u32 s48, s84, s82\n\ts_min_u32 s48, s48, 0xffff\n\tv_mov_b32 v18, s48\n\tds_write_b16 v17, v18 offset:10\n\ts_sub_u32 s48, s86, s84\n\ts_min_u32 s48, s48, 0xffff\n\tv_mov_b32 v18, s48\n\tds_write_b16 v17, v18 offset:12\n\ts_sub_u32 s48, s88, s86\n\ts_min_u32 s48, s48, 0xffff\n\tv_mov_b32 v18, s48\n\tds_write_b16 v17, v18 offset:14\n\ts_mov_b64 exec, -1\n\t98:\n\ts_mov_b32 s90, s88\n\t")
      "s_add_u32 s48, s22, 0x4000\n\ts_and_b32 s48, s48, 0x3c000\n\ts_andn2_b32 s22, s22, 0x3c000\n\ts_or_b32 s22, s22, s48\n\ts_add_u32 s48, s31, 0x4000\n\t"
      "s_and_b32 s48, s48, 0x3c000\n\ts_andn2_b32 s31, s31, 0x3c000\n\ts_or_b32 s31, s31, s48\n\ts_add_u32 s17, s17, 1\n\ts_cmp_lt_u32 s17, 64\n\ts_cbranch_scc1 10b\n\t"
      "s_waitcnt vmcnt(0) lgkmcnt(0)\n\t"
      TKS("s_mul_i32 s48, s16, 72\n\ts_add_u32 s48, s48, 162080\n\tv_mov_b32 v224, s48\n\tds_read_b32 v225, v224 offset:0\n\ts_waitcnt lgkmcnt(0)\n\tv_readfirstlane_b32 s74, v225\n\tds_read_b32 v225, v224 offset:4\n\ts_waitcnt lgkmcnt(0)\n\tv_readfirstlane_b32 s75, v225\n\tds_read_b32 v225, v224 offset:8\n\ts_waitcnt lgkmcnt(0)\n\tv_readfirstlane_b32 s76, v225\n\tds_read_b32 v225, v224 offset:12\n\ts_waitcnt lgkmcnt(0)\n\tv_readfirstlane_b32 s77, v225\n\tds_read_b32 v225, v224 offset:16\n\ts_waitcnt lgkmcnt(0)\n\tv_readfirstlane_b32 s78, v225\n\tds_read_b32 v225, v224 offset:20\n\ts_waitcnt lgkmcnt(0)\n\tv_readfirstlane_b32 s79, v225\n\tds_read_b32 v225, v224 offset:24\n\ts_waitcnt lgkmcnt(0)\n\tv_readfirstlane_b32 s80, v225\n\tds_read_b32 v225, v224 offset:28\n\ts_waitcnt lgkmcnt(0)\n\tv_readfirstlane_b32 s81, v225\n\tds_read_b32 v225, v224 offset:32\n\ts_waitcnt lgkmcnt(0)\n\tv_readfirstlane_b32 s82, v225\n\tds_read_b32 v225, v224 offset:36\n\ts_waitcnt lgkmcnt(0)\n\tv_readfirstlane_b32 s83, v225\n\tds_read_b32 v225, v224 offset:40\n\ts_waitcnt lgkmcnt(0)\n\tv_readfirstlane_b32 s84, v225\n\tds_read_b32 v225, v224 offset:44\n\ts_waitcnt lgkmcnt(0)\n\tv_readfirstlane_b32 s85, v225\n\tds_read_b32 v225, v224 offset:48\n\ts_waitcnt lgkmcnt(0)\n\tv_readfirstlane_b32 s86, v225\n\tds_read_b32 v225, v224 offset:52\n\ts_waitcnt lgkmcnt(0)\n\tv_readfirstlane_b32 s87, v225\n\tds_read_b32 v225, v224 offset:56\n\ts_waitcnt lgkmcnt(0)\n\tv_readfirstlane_b32 s88, v225\n\tds_read_b32 v225, v224 offset:60\n\ts_waitcnt lgkmcnt(0)\n\tv_readfirstlane_b32 s89, v225\n\tds_read_b32 v225, v224 offset:64\n\ts_waitcnt lgkmcnt(0)\n\tv_readfirstlane_b32 s90, v225\n\tds_read_b32 v225, v224 offset:68\n\ts_waitcnt lgkmcnt(0)\n\tv_readfirstlane_b32 s91, v225\n\t")
      STS("s_lshl_b32 s48, s16, 2\n\ts_add_u32 s48, s48, 162944\n\tv_mov_b32 v0, s48\n\tv_mov_b32 v1, s50\n\tds_write_b32 v0, v1\n\ts_cmp_lg_u32 s16, 0\n\ts_cbranch_scc1 97f\n\tv_mov_b32 v1, s51\n\tds_write_b32 v0, v1 offset:16\n\t97:\n\ts_cmp_lg_u32 s16, 3\n\ts_cbranch_scc1 95f\n\tv_mov_b32 v1, s61\n\tds_write_b32 v0, v1 offset:8\n\tv_mov_b32 v1, s21\n\tds_write_b32 v0, v1 offset:12\n\tv_mov_b32 v1, s22\n\tds_write_b32 v0, v1 offset:16\n\t95:\n\t")
      "s_cmp_lg_u32 s18, 0\n\ts_cbranch_scc1 96f\n\tv_mov_b32 v1, 1\n\tbuffer_store_dword v1, off, s[12:15], 0 offset:3328 sc1\n\t"
      "96:\n\t"
      : "+{v[0:31]}"(k), "+{v[160:191]}"(s0), "+{v[192:223]}"(s1), ALL_REGS :: "memory", "vcc", "scc", S_CLOB TK_CLOB);
}

DEV void vo_role(Ctx& c, int h) {
  const KArg p = enter(c);
  constexpr int WL = 0, MEMS = 131072, VM = 147456, VROW = 272;
  c.budget = SPIN_CAP;
  const int w = c.w, lane = fresh_lane(), g = lane >> 4, cc = lane & 15, q = (lane >> 2) & 3, pp = lane & 3;
  v32u k;
  k[0] = w * 1024 + lane * 16;
  k[1] = MEMS + w * 4096 + lane * 16;
#pragma unroll
  for (int r = 0; r < 3; ++r) k[2 + r] = MEMS + ((w + r + 1) & 3) * 4096 + lane * 16;
  k[5] = WL + w * 32768 + lane * 16;
  k[6] = (lane & 1) ? 0xEEEE : 0x4444;
  k[7] = VM + 2 * g * VROW + (32 * w + 2 * cc) * 2;
  k[8] = VM + (cc & 7) * VROW + 16 * g;
  k[9] = R_U + (16 * w + (g >> 1)) * 16384 + h * 256 + cc * 16 + (g & 1) * 8;
  k[10] = 0;
  k[11] = amap(24576 + (h >> 3) * 4096 + (lane & 31) * 128, 0);
  k[12] = (lane & 8) ? 0xEEEE : 0x4444;
#pragma unroll
  for (int i = 0; i < 4; ++i) k[14 + i] = g * 8192 + h * 256 + 16 * (cc ^ (2 * (g & 1) + 4 * i));
#pragma unroll
  for (int i = 0; i < 2; ++i) k[18 + i] = g * 2048 + w * 512 + 16 * (cc ^ (4 * (g >> 1) + 8 * i));
#pragma unroll
  for (int i = 0; i < 8; ++i)
    k[20 + i] = WL + ((w + (i >> 1)) & 3) * 32768 + (8 * (q >> 1) + (q & 1) + krow_lane(g)) * 256 + 16 * ((4 * w + 2 * (i & 1) + (pp >> 1)) ^ (2 * (q & 1) + 4 * (g & 1) + 8 * (q >> 1))) + 8 * (pp & 1);
#pragma unroll
  for (int i = 0; i < 4; ++i) k[28 + i] = WL + w * 32768 + (16 * (q & 1) + 2 * g) * 256 + 16 * ((4 * i + 2 * (q >> 1) + (pp >> 1)) ^ (4 * (g & 1) + 8 * (q & 1))) + 8 * (pp & 1);
  k[13] = 0;
  asm volatile(
      "s_add_u32 s50, s56, s64\n\ts_add_u32 m0, s49, 0\n\ts_add_u32 s48, s50, 0x0\n\tbuffer_load_dwordx4 v14, s[40:43], s48 offen  lds\n\ts_add_u32 m0, s49, 1024\n\t"
      "s_add_u32 s48, s50, 0x8000\n\tbuffer_load_dwordx4 v15, s[40:43], s48 offen  lds\n\ts_add_u32 m0, s49, 2048\n\ts_add_u32 s48, s50, 0x10000\n\t"
      "buffer_load_dwordx4 v16, s[40:43], s48 offen  lds\n\ts_add_u32 m0, s49, 3072\n\ts_add_u32 s48, s50, 0x18000\n\tbuffer_load_dwordx4 v17, s[40:43], s48 offen  lds\n\t"
      "s_add_u32 m0, s49, 4096\n\ts_add_u32 s48, s50, 0x20000\n\tbuffer_load_dwordx4 v14, s[40:43], s48 offen  lds\n\ts_add_u32 m0, s49, 5120\n\ts_add_u32 s48, s50, 0x28000\n\t"
      "buffer_load_dwordx4 v15, s[40:43], s48 offen  lds\n\ts_add_u32 m0, s49, 6144\n\ts_add_u32 s48, s50, 0x30000\n\tbuffer_load_dwordx4 v16, s[40:43], s48 offen  lds\n\t"
      "s_add_u32 m0, s49, 7168\n\ts_add_u32 s48, s50, 0x38000\n\tbuffer_load_dwordx4 v17, s[40:43], s48 offen  lds\n\ts_add_u32 s50, s56, s64\n\ts_add_u32 m0, s49, 8192\n\t"
      "s_add_u32 s48, s50, 0x40000\n\tbuffer_load_dwordx4 v14, s[40:43], s48 offen  lds\n\ts_add_u32 m0, s49, 9216\n\ts_add_u32 s48, s50, 0x48000\n\t"
      "buffer_load_dwordx4 v15, s[40:43], s48 offen  lds\n\ts_add_u32 m0, s49, 10240\n\ts_add_u32 s48, s50, 0x50000\n\tbuffer_load_dwordx4 v16, s[40:43], s48 offen  lds\n\t"
      "s_add_u32 m0, s49, 11264\n\ts_add_u32 s48, s50, 0x58000\n\tbuffer_load_dwordx4 v17, s[40:43], s48 offen  lds\n\ts_add_u32 m0, s49, 12288\n\t"
      "s_add_u32 s48, s50, 0x60000\n\tbuffer_load_dwordx4 v14, s[40:43], s48 offen  lds\n\ts_add_u32 m0, s49, 13312\n\ts_add_u32 s48, s50, 0x68000\n\t"
      "buffer_load_dwordx4 v15, s[40:43], s48 offen  lds\n\ts_add_u32 m0, s49, 14336\n\ts_add_u32 s48, s50, 0x70000\n\tbuffer_load_dwordx4 v16, s[40:43], s48 offen  lds\n\t"
      "s_add_u32 m0, s49, 15360\n\ts_add_u32 s48, s50, 0x78000\n\tbuffer_load_dwordx4 v17, s[40:43], s48 offen  lds\n\ts_add_u32 s50, s56, s65\n\ts_add_u32 m0, s49, 16384\n\t"
      "s_add_u32 s48, s50, 0x0\n\tbuffer_load_dwordx4 v14, s[40:43], s48 offen  lds\n\ts_add_u32 m0, s49, 17408\n\ts_add_u32 s48, s50, 0x8000\n\t"
      "buffer_load_dwordx4 v15, s[40:43], s48 offen  lds\n\ts_add_u32 m0, s49, 18432\n\ts_add_u32 s48, s50, 0x10000\n\tbuffer_load_dwordx4 v16, s[40:43], s48 offen  lds\n\t"
      "s_add_u32 m0, s49, 19456\n\ts_add_u32 s48, s50, 0x18000\n\tbuffer_load_dwordx4 v17, s[40:43], s48 offen  lds\n\ts_add_u32 m0, s49, 20480\n\t"
      "s_add_u32 s48, s50, 0x20000\n\tbuffer_load_dwordx4 v14, s[40:43], s48 offen  lds\n\ts_add_u32 m0, s49, 21504\n\ts_add_u32 s48, s50, 0x28000\n\t"
      "buffer_load_dwordx4 v15, s[40:43], s48 offen  lds\n\ts_add_u32 m0, s49, 22528\n\ts_add_u32 s48, s50, 0x30000\n\tbuffer_load_dwordx4 v16, s[40:43], s48 offen  lds\n\t"
      "s_add_u32 m0, s49, 23552\n\ts_add_u32 s48, s50, 0x38000\n\tbuffer_load_dwordx4 v17, s[40:43], s48 offen  lds\n\ts_add_u32 s50, s56, s65\n\ts_add_u32 m0, s49, 24576\n\t"
      "s_add_u32 s48, s50, 0x40000\n\tbuffer_load_dwordx4 v14, s[40:43], s48 offen  lds\n\ts_add_u32 m0, s49, 25600\n\ts_add_u32 s48, s50, 0x48000\n\t"
      "buffer_load_dwordx4 v15, s[40:43], s48 offen  lds\n\ts_add_u32 m0, s49, 26624\n\ts_add_u32 s48, s50, 0x50000\n\tbuffer_load_dwordx4 v16, s[40:43], s48 offen  lds\n\t"
      "s_add_u32 m0, s49, 27648\n\ts_add_u32 s48, s50, 0x58000\n\tbuffer_load_dwordx4 v17, s[40:43], s48 offen  lds\n\ts_add_u32 m0, s49, 28672\n\t"
      "s_add_u32 s48, s50, 0x60000\n\tbuffer_load_dwordx4 v14, s[40:43], s48 offen  lds\n\ts_add_u32 m0, s49, 29696\n\ts_add_u32 s48, s50, 0x68000\n\t"
      "buffer_load_dwordx4 v15, s[40:43], s48 offen  lds\n\ts_add_u32 m0, s49, 30720\n\ts_add_u32 s48, s50, 0x70000\n\tbuffer_load_dwordx4 v16, s[40:43], s48 offen  lds\n\t"
      "s_add_u32 m0, s49, 31744\n\ts_add_u32 s48, s50, 0x78000\n\tbuffer_load_dwordx4 v17, s[40:43], s48 offen  lds\n\t"
      :: "{v[14:17]}"(v4u{k[14], k[15], k[16], k[17]}), "{s[40:43]}"(mkrs(p->Wv, 1024 * 4096 * 2)), "{s49}"(WL + w * 32768), "{s56}"(w << 19), "{s[64:67]}"(v4u{ROTS(h >> 3)})
      : "memory", "scc", "s48", "s50");
  claim(c, 0);
  v32u s0 = {RSRCS, (unsigned)w, 0, c.budget, 0, CV0 * 16, PACE};
  v32u s1 = {plo(p->Wv), phi(p->Wv), 1024 * 4096 * 2, 0x00020000, plo(p->Wo), phi(p->Wo), 1024 * 4096 * 2, 0x00020000, 0, (unsigned)(WL + w * 32768), 0, 0, 0, 0, 0, 0,
             (unsigned)(w << 19), (unsigned)(h << 18), 0, 0, plo((const void*)p), phi((const void*)p), c.att ? 0u : (unsigned)c.b + 256u, (unsigned)(C_VERDICT + c.att * 128), ROTS(h >> 3)};
  v32u wa[8], wv[5];
  asm volatile(
      "s_nop 0\n\tv_readfirstlane_b32 s8, v160\n\tv_readfirstlane_b32 s9, v161\n\tv_readfirstlane_b32 s10, v162\n\tv_readfirstlane_b32 s11, v163\n\t"
      "v_readfirstlane_b32 s12, v164\n\tv_readfirstlane_b32 s13, v165\n\tv_readfirstlane_b32 s14, v166\n\tv_readfirstlane_b32 s15, v167\n\tv_readfirstlane_b32 s16, v168\n\t"
      "v_readfirstlane_b32 s17, v169\n\tv_readfirstlane_b32 s18, v170\n\tv_readfirstlane_b32 s19, v171\n\tv_readfirstlane_b32 s20, v172\n\tv_readfirstlane_b32 s21, v173\n\t"
      "v_readfirstlane_b32 s22, v174\n\tv_readfirstlane_b32 s23, v175\n\tv_readfirstlane_b32 s24, v176\n\tv_readfirstlane_b32 s25, v177\n\tv_readfirstlane_b32 s26, v178\n\t"
      "v_readfirstlane_b32 s40, v192\n\tv_readfirstlane_b32 s41, v193\n\tv_readfirstlane_b32 s42, v194\n\tv_readfirstlane_b32 s43, v195\n\tv_readfirstlane_b32 s44, v196\n\t"
      "v_readfirstlane_b32 s45, v197\n\tv_readfirstlane_b32 s46, v198\n\tv_readfirstlane_b32 s47, v199\n\tv_readfirstlane_b32 s49, v201\n\tv_readfirstlane_b32 s56, v208\n\t"
      "v_readfirstlane_b32 s57, v209\n\tv_readfirstlane_b32 s60, v212\n\tv_readfirstlane_b32 s61, v213\n\tv_readfirstlane_b32 s62, v214\n\tv_readfirstlane_b32 s63, v215\n\t"
      "v_readfirstlane_b32 s64, v216\n\tv_readfirstlane_b32 s65, v217\n\tv_readfirstlane_b32 s66, v218\n\tv_readfirstlane_b32 s67, v219\n\ts_nop 4\n\t"
      TKS("s_mul_i32 s48, s16, 72\n\ts_add_u32 s48, s48, 162080\n\tv_mov_b32 v224, s48\n\tv_mov_b32 v225, s74\n\tds_write_b32 v224, v225 offset:0\n\tv_mov_b32 v225, s75\n\tds_write_b32 v224, v225 offset:4\n\tv_mov_b32 v225, s76\n\tds_write_b32 v224, v225 offset:8\n\tv_mov_b32 v225, s77\n\tds_write_b32 v224, v225 offset:12\n\tv_mov_b32 v225, s78\n\tds_write_b32 v224, v225 offset:16\n\tv_mov_b32 v225, s79\n\tds_write_b32 v224, v225 offset:20\n\tv_mov_b32 v225, s80\n\tds_write_b32 v224, v225 offset:24\n\tv_mov_b32 v225, s81\n\tds_write_b32 v224, v225 offset:28\n\tv_mov_b32 v225, s82\n\tds_write_b32 v224, v225 offset:32\n\tv_mov_b32 v225, s83\n\tds_write_b32 v224, v225 offset:36\n\tv_mov_b32 v225, s84\n\tds_write_b32 v224, v225 offset:40\n\tv_mov_b32 v225, s85\n\tds_write_b32 v224, v225 offset:44\n\tv_mov_b32 v225, s86\n\tds_write_b32 v224, v225 offset:48\n\tv_mov_b32 v225, s87\n\tds_write_b32 v224, v225 offset:52\n\tv_mov_b32 v225, s88\n\tds_write_b32 v224, v225 offset:56\n\tv_mov_b32 v225, s89\n\tds_write_b32 v224, v225 offset:60\n\tv_mov_b32 v225, s90\n\tds_write_b32 v224, v225 offset:64\n\tv_mov_b32 v225, s91\n\tds_write_b32 v224, v225 offset:68\n\t")
      "s_waitcnt vmcnt(24)\n\ts_barrier\n\tds_read_b64_tr_b16 a[0:1], v20 offset:0\n\tds_read_b64_tr_b16 a[2:3], v20 offset:512\n\tds_read_b64_tr_b16 a[8:9], v21 offset:0\n\t"
      "ds_read_b64_tr_b16 a[10:11], v21 offset:512\n\tds_read_b64_tr_b16 a[48:49], v22 offset:0\n\tds_read_b64_tr_b16 a[50:51], v22 offset:512\n\t"
      "ds_read_b64_tr_b16 a[56:57], v23 offset:0\n\tds_read_b64_tr_b16 a[58:59], v23 offset:512\n\tds_read_b64_tr_b16 a[64:65], v24 offset:0\n\t"
      "ds_read_b64_tr_b16 a[66:67], v24 offset:512\n\tds_read_b64_tr_b16 v[208:209], v25 offset:0\n\tds_read_b64_tr_b16 v[210:211], v25 offset:512\n\t"
      "ds_read_b64_tr_b16 a[72:73], v26 offset:0\n\tds_read_b64_tr_b16 a[74:75], v26 offset:512\n\tds_read_b64_tr_b16 a[80:81], v27 offset:0\n\t"
      "ds_read_b64_tr_b16 a[82:83], v27 offset:512\n\ts_waitcnt lgkmcnt(0)\n\ts_barrier\n\ts_add_u32 s50, s56, s66\n\ts_add_u32 m0, s49, 0\n\ts_add_u32 s48, s50, 0x0\n\t"
      "buffer_load_dwordx4 v14, s[40:43], s48 offen  lds\n\ts_add_u32 m0, s49, 1024\n\ts_add_u32 s48, s50, 0x8000\n\tbuffer_load_dwordx4 v15, s[40:43], s48 offen  lds\n\t"
      "s_add_u32 m0, s49, 2048\n\ts_add_u32 s48, s50, 0x10000\n\tbuffer_load_dwordx4 v16, s[40:43], s48 offen  lds\n\ts_add_u32 m0, s49, 3072\n\ts_add_u32 s48, s50, 0x18000\n\t"
      "buffer_load_dwordx4 v17, s[40:43], s48 offen  lds\n\ts_add_u32 m0, s49, 4096\n\ts_add_u32 s48, s50, 0x20000\n\tbuffer_load_dwordx4 v14, s[40:43], s48 offen  lds\n\t"
      "s_add_u32 m0, s49, 5120\n\ts_add_u32 s48, s50, 0x28000\n\tbuffer_load_dwordx4 v15, s[40:43], s48 offen  lds\n\ts_add_u32 m0, s49, 6144\n\ts_add_u32 s48, s50, 0x30000\n\t"
      "buffer_load_dwordx4 v16, s[40:43], s48 offen  lds\n\ts_add_u32 m0, s49, 7168\n\ts_add_u32 s48, s50, 0x38000\n\tbuffer_load_dwordx4 v17, s[40:43], s48 offen  lds\n\t"
      "s_waitcnt vmcnt(24)\n\ts_barrier\n\tds_read_b64_tr_b16 a[4:5], v20 offset:8192\n\tds_read_b64_tr_b16 a[6:7], v20 offset:8704\n\t"
      "ds_read_b64_tr_b16 a[12:13], v21 offset:8192\n\tds_read_b64_tr_b16 a[14:15], v21 offset:8704\n\tds_read_b64_tr_b16 a[52:53], v22 offset:8192\n\t"
      "ds_read_b64_tr_b16 a[54:55], v22 offset:8704\n\tds_read_b64_tr_b16 a[60:61], v23 offset:8192\n\tds_read_b64_tr_b16 a[62:63], v23 offset:8704\n\t"
      "ds_read_b64_tr_b16 a[68:69], v24 offset:8192\n\tds_read_b64_tr_b16 a[70:71], v24 offset:8704\n\tds_read_b64_tr_b16 v[212:213], v25 offset:8192\n\t"
      "ds_read_b64_tr_b16 v[214:215], v25 offset:8704\n\tds_read_b64_tr_b16 a[76:77], v26 offset:8192\n\tds_read_b64_tr_b16 a[78:79], v26 offset:8704\n\t"
      "ds_read_b64_tr_b16 a[84:85], v27 offset:8192\n\tds_read_b64_tr_b16 a[86:87], v27 offset:8704\n\ts_waitcnt lgkmcnt(0)\n\ts_barrier\n\ts_add_u32 s50, s56, s66\n\t"
      "s_add_u32 m0, s49, 8192\n\ts_add_u32 s48, s50, 0x40000\n\tbuffer_load_dwordx4 v14, s[40:43], s48 offen  lds\n\ts_add_u32 m0, s49, 9216\n\ts_add_u32 s48, s50, 0x48000\n\t"
      "buffer_load_dwordx4 v15, s[40:43], s48 offen  lds\n\ts_add_u32 m0, s49, 10240\n\ts_add_u32 s48, s50, 0x50000\n\tbuffer_load_dwordx4 v16, s[40:43], s48 offen  lds\n\t"
      "s_add_u32 m0, s49, 11264\n\ts_add_u32 s48, s50, 0x58000\n\tbuffer_load_dwordx4 v17, s[40:43], s48 offen  lds\n\ts_add_u32 m0, s49, 12288\n\t"
      "s_add_u32 s48, s50, 0x60000\n\tbuffer_load_dwordx4 v14, s[40:43], s48 offen  lds\n\ts_add_u32 m0, s49, 13312\n\ts_add_u32 s48, s50, 0x68000\n\t"
      "buffer_load_dwordx4 v15, s[40:43], s48 offen  lds\n\ts_add_u32 m0, s49, 14336\n\ts_add_u32 s48, s50, 0x70000\n\tbuffer_load_dwordx4 v16, s[40:43], s48 offen  lds\n\t"
      "s_add_u32 m0, s49, 15360\n\ts_add_u32 s48, s50, 0x78000\n\tbuffer_load_dwordx4 v17, s[40:43], s48 offen  lds\n\ts_waitcnt vmcnt(24)\n\ts_barrier\n\t"
      "ds_read_b64_tr_b16 a[16:17], v20 offset:16384\n\tds_read_b64_tr_b16 a[18:19], v20 offset:16896\n\tds_read_b64_tr_b16 v[192:193], v21 offset:16384\n\t"
      "ds_read_b64_tr_b16 v[194:195], v21 offset:16896\n\tds_read_b64_tr_b16 a[88:89], v22 offset:16384\n\tds_read_b64_tr_b16 a[90:91], v22 offset:16896\n\t"
      "ds_read_b64_tr_b16 v[216:217], v23 offset:16384\n\tds_read_b64_tr_b16 v[218:219], v23 offset:16896\n\tds_read_b64_tr_b16 a[96:97], v24 offset:16384\n\t"
      "ds_read_b64_tr_b16 a[98:99], v24 offset:16896\n\tds_read_b64_tr_b16 a[104:105], v25 offset:16384\n\tds_read_b64_tr_b16 a[106:107], v25 offset:16896\n\t"
      "ds_read_b64_tr_b16 a[112:113], v26 offset:16384\n\tds_read_b64_tr_b16 a[114:115], v26 offset:16896\n\tds_read_b64_tr_b16 v[224:225], v27 offset:16384\n\t"
      "ds_read_b64_tr_b16 v[226:227], v27 offset:16896\n\ts_waitcnt lgkmcnt(0)\n\ts_barrier\n\ts_add_u32 s50, s56, s67\n\ts_add_u32 m0, s49, 16384\n\ts_add_u32 s48, s50, 0x0\n\t"
      "buffer_load_dwordx4 v14, s[40:43], s48 offen  lds\n\ts_add_u32 m0, s49, 17408\n\ts_add_u32 s48, s50, 0x8000\n\tbuffer_load_dwordx4 v15, s[40:43], s48 offen  lds\n\t"
      "s_add_u32 m0, s49, 18432\n\ts_add_u32 s48, s50, 0x10000\n\tbuffer_load_dwordx4 v16, s[40:43], s48 offen  lds\n\ts_add_u32 m0, s49, 19456\n\t"
      "s_add_u32 s48, s50, 0x18000\n\tbuffer_load_dwordx4 v17, s[40:43], s48 offen  lds\n\ts_add_u32 m0, s49, 20480\n\ts_add_u32 s48, s50, 0x20000\n\t"
      "buffer_load_dwordx4 v14, s[40:43], s48 offen  lds\n\ts_add_u32 m0, s49, 21504\n\ts_add_u32 s48, s50, 0x28000\n\tbuffer_load_dwordx4 v15, s[40:43], s48 offen  lds\n\t"
      "s_add_u32 m0, s49, 22528\n\ts_add_u32 s48, s50, 0x30000\n\tbuffer_load_dwordx4 v16, s[40:43], s48 offen  lds\n\ts_add_u32 m0, s49, 23552\n\t"
      "s_add_u32 s48, s50, 0x38000\n\tbuffer_load_dwordx4 v17, s[40:43], s48 offen  lds\n\ts_waitcnt vmcnt(24)\n\ts_barrier\n\tds_read_b64_tr_b16 a[20:21], v20 offset:24576\n\t"
      "ds_read_b64_tr_b16 a[22:23], v20 offset:25088\n\tds_read_b64_tr_b16 v[196:197], v21 offset:24576\n\tds_read_b64_tr_b16 v[198:199], v21 offset:25088\n\t"
      "ds_read_b64_tr_b16 a[92:93], v22 offset:24576\n\tds_read_b64_tr_b16 a[94:95], v22 offset:25088\n\tds_read_b64_tr_b16 v[220:221], v23 offset:24576\n\t"
      "ds_read_b64_tr_b16 v[222:223], v23 offset:25088\n\tds_read_b64_tr_b16 a[100:101], v24 offset:24576\n\tds_read_b64_tr_b16 a[102:103], v24 offset:25088\n\t"
      "ds_read_b64_tr_b16 a[108:109], v25 offset:24576\n\tds_read_b64_tr_b16 a[110:111], v25 offset:25088\n\tds_read_b64_tr_b16 a[116:117], v26 offset:24576\n\t"
      "ds_read_b64_tr_b16 a[118:119], v26 offset:25088\n\tds_read_b64_tr_b16 v[228:229], v27 offset:24576\n\tds_read_b64_tr_b16 v[230:231], v27 offset:25088\n\t"
      "s_waitcnt lgkmcnt(0)\n\ts_barrier\n\ts_add_u32 s50, s56, s67\n\ts_add_u32 m0, s49, 24576\n\ts_add_u32 s48, s50, 0x40000\n\t"
      "buffer_load_dwordx4 v14, s[40:43], s48 offen  lds\n\ts_add_u32 m0, s49, 25600\n\ts_add_u32 s48, s50, 0x48000\n\tbuffer_load_dwordx4 v15, s[40:43], s48 offen  lds\n\t"
      "s_add_u32 m0, s49, 26624\n\ts_add_u32 s48, s50, 0x50000\n\tbuffer_load_dwordx4 v16, s[40:43], s48 offen  lds\n\ts_add_u32 m0, s49, 27648\n\t"
      "s_add_u32 s48, s50, 0x58000\n\tbuffer_load_dwordx4 v17, s[40:43], s48 offen  lds\n\ts_add_u32 m0, s49, 28672\n\ts_add_u32 s48, s50, 0x60000\n\t"
      "buffer_load_dwordx4 v14, s[40:43], s48 offen  lds\n\ts_add_u32 m0, s49, 29696\n\ts_add_u32 s48, s50, 0x68000\n\tbuffer_load_dwordx4 v15, s[40:43], s48 offen  lds\n\t"
      "s_add_u32 m0, s49, 30720\n\ts_add_u32 s48, s50, 0x70000\n\tbuffer_load_dwordx4 v16, s[40:43], s48 offen  lds\n\ts_add_u32 m0, s49, 31744\n\t"
      "s_add_u32 s48, s50, 0x78000\n\tbuffer_load_dwordx4 v17, s[40:43], s48 offen  lds\n\ts_waitcnt vmcnt(24)\n\ts_barrier\n\tds_read_b64_tr_b16 a[24:25], v20 offset:0\n\t"
      "ds_read_b64_tr_b16 a[26:27], v20 offset:512\n\tds_read_b64_tr_b16 a[32:33], v21 offset:0\n\tds_read_b64_tr_b16 a[34:35], v21 offset:512\n\t"
      "ds_read_b64_tr_b16 a[120:121], v22 offset:0\n\tds_read_b64_tr_b16 a[122:123], v22 offset:512\n\tds_read_b64_tr_b16 a[128:129], v23 offset:0\n\t"
      "ds_read_b64_tr_b16 a[130:131], v23 offset:512\n\tds_read_b64_tr_b16 a[136:137], v24 offset:0\n\tds_read_b64_tr_b16 a[138:139], v24 offset:512\n\t"
      "ds_read_b64_tr_b16 v[232:233], v25 offset:0\n\tds_read_b64_tr_b16 v[234:235], v25 offset:512\n\tds_read_b64_tr_b16 a[144:145], v26 offset:0\n\t"
      "ds_read_b64_tr_b16 a[146:147], v26 offset:512\n\tds_read_b64_tr_b16 a[152:153], v27 offset:0\n\tds_read_b64_tr_b16 a[154:155], v27 offset:512\n\ts_waitcnt lgkmcnt(0)\n\t"
      "s_barrier\n\ts_add_u32 m0, s49, 0\n\ts_add_u32 s48, s57, 0x100\n\tbuffer_load_dwordx4 v18, s[44:47], s48 offen  lds\n\ts_add_u32 m0, s49, 1024\n\t"
      "s_add_u32 s48, s57, 0x2100\n\tbuffer_load_dwordx4 v18, s[44:47], s48 offen  lds\n\ts_add_u32 m0, s49, 2048\n\ts_add_u32 s48, s57, 0x4100\n\t"
      "buffer_load_dwordx4 v18, s[44:47], s48 offen  lds\n\ts_add_u32 m0, s49, 3072\n\ts_add_u32 s48, s57, 0x6100\n\tbuffer_load_dwordx4 v18, s[44:47], s48 offen  lds\n\t"
      "s_add_u32 m0, s49, 4096\n\ts_add_u32 s48, s57, 0x8100\n\tbuffer_load_dwordx4 v19, s[44:47], s48 offen  lds\n\ts_add_u32 m0, s49, 5120\n\ts_add_u32 s48, s57, 0xa100\n\t"
      "buffer_load_dwordx4 v19, s[44:47], s48 offen  lds\n\ts_add_u32 m0, s49, 6144\n\ts_add_u32 s48, s57, 0xc100\n\tbuffer_load_dwordx4 v19, s[44:47], s48 offen  lds\n\t"
      "s_add_u32 m0, s49, 7168\n\ts_add_u32 s48, s57, 0xe100\n\tbuffer_load_dwordx4 v19, s[44:47], s48 offen  lds\n\ts_waitcnt vmcnt(24)\n\ts_barrier\n\t"
      "ds_read_b64_tr_b16 a[28:29], v20 offset:8192\n\tds_read_b64_tr_b16 a[30:31], v20 offset:8704\n\tds_read_b64_tr_b16 a[36:37], v21 offset:8192\n\t"
      "ds_read_b64_tr_b16 a[38:39], v21 offset:8704\n\tds_read_b64_tr_b16 a[124:125], v22 offset:8192\n\tds_read_b64_tr_b16 a[126:127], v22 offset:8704\n\t"
      "ds_read_b64_tr_b16 a[132:133], v23 offset:8192\n\tds_read_b64_tr_b16 a[134:135], v23 offset:8704\n\tds_read_b64_tr_b16 a[140:141], v24 offset:8192\n\t"
      "ds_read_b64_tr_b16 a[142:143], v24 offset:8704\n\tds_read_b64_tr_b16 v[236:237], v25 offset:8192\n\tds_read_b64_tr_b16 v[238:239], v25 offset:8704\n\t"
      "ds_read_b64_tr_b16 a[148:149], v26 offset:8192\n\tds_read_b64_tr_b16 a[150:151], v26 offset:8704\n\tds_read_b64_tr_b16 a[156:157], v27 offset:8192\n\t"
      "ds_read_b64_tr_b16 a[158:159], v27 offset:8704\n\ts_waitcnt lgkmcnt(0)\n\ts_barrier\n\ts_add_u32 m0, s49, 8192\n\ts_add_u32 s48, s57, 0x10100\n\t"
      "buffer_load_dwordx4 v18, s[44:47], s48 offen  lds\n\ts_add_u32 m0, s49, 9216\n\ts_add_u32 s48, s57, 0x12100\n\tbuffer_load_dwordx4 v18, s[44:47], s48 offen  lds\n\t"
      "s_add_u32 m0, s49, 10240\n\ts_add_u32 s48, s57, 0x14100\n\tbuffer_load_dwordx4 v18, s[44:47], s48 offen  lds\n\ts_add_u32 m0, s49, 11264\n\t"
      "s_add_u32 s48, s57, 0x16100\n\tbuffer_load_dwordx4 v18, s[44:47], s48 offen  lds\n\ts_add_u32 m0, s49, 12288\n\ts_add_u32 s48, s57, 0x18100\n\t"
      "buffer_load_dwordx4 v19, s[44:47], s48 offen  lds\n\ts_add_u32 m0, s49, 13312\n\ts_add_u32 s48, s57, 0x1a100\n\tbuffer_load_dwordx4 v19, s[44:47], s48 offen  lds\n\t"
      "s_add_u32 m0, s49, 14336\n\ts_add_u32 s48, s57, 0x1c100\n\tbuffer_load_dwordx4 v19, s[44:47], s48 offen  lds\n\ts_add_u32 m0, s49, 15360\n\t"
      "s_add_u32 s48, s57, 0x1e100\n\tbuffer_load_dwordx4 v19, s[44:47], s48 offen  lds\n\ts_waitcnt vmcnt(24)\n\ts_barrier\n\tds_read_b64_tr_b16 a[40:41], v20 offset:16384\n\t"
      "ds_read_b64_tr_b16 a[42:43], v20 offset:16896\n\tds_read_b64_tr_b16 v[200:201], v21 offset:16384\n\tds_read_b64_tr_b16 v[202:203], v21 offset:16896\n\t"
      "ds_read_b64_tr_b16 a[160:161], v22 offset:16384\n\tds_read_b64_tr_b16 a[162:163], v22 offset:16896\n\tds_read_b64_tr_b16 v[240:241], v23 offset:16384\n\t"
      "ds_read_b64_tr_b16 v[242:243], v23 offset:16896\n\tds_read_b64_tr_b16 a[168:169], v24 offset:16384\n\tds_read_b64_tr_b16 a[170:171], v24 offset:16896\n\t"
      "ds_read_b64_tr_b16 a[176:177], v25 offset:16384\n\tds_read_b64_tr_b16 a[178:179], v25 offset:16896\n\tds_read_b64_tr_b16 a[184:185], v26 offset:16384\n\t"
      "ds_read_b64_tr_b16 a[186:187], v26 offset:16896\n\tds_read_b64_tr_b16 v[248:249], v27 offset:16384\n\tds_read_b64_tr_b16 v[250:251], v27 offset:16896\n\t"
      "s_waitcnt lgkmcnt(0)\n\ts_barrier\n\ts_add_u32 m0, s49, 16384\n\ts_add_u32 s48, s57, 0x20100\n\tbuffer_load_dwordx4 v18, s[44:47], s48 offen  lds\n\t"
      "s_add_u32 m0, s49, 17408\n\ts_add_u32 s48, s57, 0x22100\n\tbuffer_load_dwordx4 v18, s[44:47], s48 offen  lds\n\ts_add_u32 m0, s49, 18432\n\t"
      "s_add_u32 s48, s57, 0x24100\n\tbuffer_load_dwordx4 v18, s[44:47], s48 offen  lds\n\ts_add_u32 m0, s49, 19456\n\ts_add_u32 s48, s57, 0x26100\n\t"
      "buffer_load_dwordx4 v18, s[44:47], s48 offen  lds\n\ts_add_u32 m0, s49, 20480\n\ts_add_u32 s48, s57, 0x28100\n\tbuffer_load_dwordx4 v19, s[44:47], s48 offen  lds\n\t"
      "s_add_u32 m0, s49, 21504\n\ts_add_u32 s48, s57, 0x2a100\n\tbuffer_load_dwordx4 v19, s[44:47], s48 offen  lds\n\ts_add_u32 m0, s49, 22528\n\t"
      "s_add_u32 s48, s57, 0x2c100\n\tbuffer_load_dwordx4 v19, s[44:47], s48 offen  lds\n\ts_add_u32 m0, s49, 23552\n\ts_add_u32 s48, s57, 0x2e100\n\t"
      "buffer_load_dwordx4 v19, s[44:47], s48 offen  lds\n\ts_waitcnt vmcnt(24)\n\ts_barrier\n\tds_read_b64_tr_b16 a[44:45], v20 offset:24576\n\t"
      "ds_read_b64_tr_b16 a[46:47], v20 offset:25088\n\tds_read_b64_tr_b16 v[204:205], v21 offset:24576\n\tds_read_b64_tr_b16 v[206:207], v21 offset:25088\n\t"
      "ds_read_b64_tr_b16 a[164:165], v22 offset:24576\n\tds_read_b64_tr_b16 a[166:167], v22 offset:25088\n\tds_read_b64_tr_b16 v[244:245], v23 offset:24576\n\t"
      "ds_read_b64_tr_b16 v[246:247], v23 offset:25088\n\tds_read_b64_tr_b16 a[172:173], v24 offset:24576\n\tds_read_b64_tr_b16 a[174:175], v24 offset:25088\n\t"
      "ds_read_b64_tr_b16 a[180:181], v25 offset:24576\n\tds_read_b64_tr_b16 a[182:183], v25 offset:25088\n\tds_read_b64_tr_b16 a[188:189], v26 offset:24576\n\t"
      "ds_read_b64_tr_b16 a[190:191], v26 offset:25088\n\tds_read_b64_tr_b16 v[252:253], v27 offset:24576\n\tds_read_b64_tr_b16 v[254:255], v27 offset:25088\n\t"
      "s_waitcnt lgkmcnt(0)\n\ts_barrier\n\ts_add_u32 m0, s49, 24576\n\ts_add_u32 s48, s57, 0x30100\n\tbuffer_load_dwordx4 v18, s[44:47], s48 offen  lds\n\t"
      "s_add_u32 m0, s49, 25600\n\ts_add_u32 s48, s57, 0x32100\n\tbuffer_load_dwordx4 v18, s[44:47], s48 offen  lds\n\ts_add_u32 m0, s49, 26624\n\t"
      "s_add_u32 s48, s57, 0x34100\n\tbuffer_load_dwordx4 v18, s[44:47], s48 offen  lds\n\ts_add_u32 m0, s49, 27648\n\ts_add_u32 s48, s57, 0x36100\n\t"
      "buffer_load_dwordx4 v18, s[44:47], s48 offen  lds\n\ts_add_u32 m0, s49, 28672\n\ts_add_u32 s48, s57, 0x38100\n\tbuffer_load_dwordx4 v19, s[44:47], s48 offen  lds\n\t"
      "s_add_u32 m0, s49, 29696\n\ts_add_u32 s48, s57, 0x3a100\n\tbuffer_load_dwordx4 v19, s[44:47], s48 offen  lds\n\ts_add_u32 m0, s49, 30720\n\t"
      "s_add_u32 s48, s57, 0x3c100\n\tbuffer_load_dwordx4 v19, s[44:47], s48 offen  lds\n\ts_add_u32 m0, s49, 31744\n\ts_add_u32 s48, s57, 0x3e100\n\t"
      "buffer_load_dwordx4 v19, s[44:47], s48 offen  lds\n\ts_waitcnt vmcnt(24)\n\tds_read_b64_tr_b16 a[192:193], v28 offset:0\n\t"
      "ds_read_b64_tr_b16 a[194:195], v28 offset:256\n\tds_read_b64_tr_b16 a[196:197], v28 offset:2048\n\tds_read_b64_tr_b16 a[198:199], v28 offset:2304\n\t"
      "ds_read_b64_tr_b16 a[224:225], v29 offset:0\n\tds_read_b64_tr_b16 a[226:227], v29 offset:256\n\tds_read_b64_tr_b16 a[228:229], v29 offset:2048\n\t"
      "ds_read_b64_tr_b16 a[230:231], v29 offset:2304\n\tds_read_b64_tr_b16 v[56:57], v30 offset:0\n\tds_read_b64_tr_b16 v[58:59], v30 offset:256\n\t"
      "ds_read_b64_tr_b16 v[60:61], v30 offset:2048\n\tds_read_b64_tr_b16 v[62:63], v30 offset:2304\n\tds_read_b64_tr_b16 v[160:161], v31 offset:0\n\t"
      "ds_read_b64_tr_b16 v[162:163], v31 offset:256\n\tds_read_b64_tr_b16 v[164:165], v31 offset:2048\n\tds_read_b64_tr_b16 v[166:167], v31 offset:2304\n\t"
      "s_waitcnt lgkmcnt(0)\n\ts_add_u32 m0, s49, 0\n\ts_add_u32 s48, s57, 0x0\n\tbuffer_load_dwordx4 v18, s[44:47], s48 offen  lds\n\ts_add_u32 m0, s49, 1024\n\t"
      "s_add_u32 s48, s57, 0x2000\n\tbuffer_load_dwordx4 v18, s[44:47], s48 offen  lds\n\ts_add_u32 m0, s49, 2048\n\ts_add_u32 s48, s57, 0x4000\n\t"
      "buffer_load_dwordx4 v18, s[44:47], s48 offen  lds\n\ts_add_u32 m0, s49, 3072\n\ts_add_u32 s48, s57, 0x6000\n\tbuffer_load_dwordx4 v18, s[44:47], s48 offen  lds\n\t"
      "s_add_u32 m0, s49, 4096\n\ts_add_u32 s48, s57, 0x8000\n\tbuffer_load_dwordx4 v19, s[44:47], s48 offen  lds\n\ts_add_u32 m0, s49, 5120\n\ts_add_u32 s48, s57, 0xa000\n\t"
      "buffer_load_dwordx4 v19, s[44:47], s48 offen  lds\n\ts_add_u32 m0, s49, 6144\n\ts_add_u32 s48, s57, 0xc000\n\tbuffer_load_dwordx4 v19, s[44:47], s48 offen  lds\n\t"
      "s_add_u32 m0, s49, 7168\n\ts_add_u32 s48, s57, 0xe000\n\tbuffer_load_dwordx4 v19, s[44:47], s48 offen  lds\n\ts_waitcnt vmcnt(24)\n\t"
      "ds_read_b64_tr_b16 a[200:201], v28 offset:8192\n\tds_read_b64_tr_b16 a[202:203], v28 offset:8448\n\tds_read_b64_tr_b16 a[204:205], v28 offset:10240\n\t"
      "ds_read_b64_tr_b16 a[206:207], v28 offset:10496\n\tds_read_b64_tr_b16 a[232:233], v29 offset:8192\n\tds_read_b64_tr_b16 a[234:235], v29 offset:8448\n\t"
      "ds_read_b64_tr_b16 a[236:237], v29 offset:10240\n\tds_read_b64_tr_b16 a[238:239], v29 offset:10496\n\tds_read_b64_tr_b16 v[88:89], v30 offset:8192\n\t"
      "ds_read_b64_tr_b16 v[90:91], v30 offset:8448\n\tds_read_b64_tr_b16 v[92:93], v30 offset:10240\n\tds_read_b64_tr_b16 v[94:95], v30 offset:10496\n\t"
      "ds_read_b64_tr_b16 v[168:169], v31 offset:8192\n\tds_read_b64_tr_b16 v[170:171], v31 offset:8448\n\tds_read_b64_tr_b16 v[172:173], v31 offset:10240\n\t"
      "ds_read_b64_tr_b16 v[174:175], v31 offset:10496\n\ts_waitcnt lgkmcnt(0)\n\ts_add_u32 m0, s49, 8192\n\ts_add_u32 s48, s57, 0x10000\n\t"
      "buffer_load_dwordx4 v18, s[44:47], s48 offen  lds\n\ts_add_u32 m0, s49, 9216\n\ts_add_u32 s48, s57, 0x12000\n\tbuffer_load_dwordx4 v18, s[44:47], s48 offen  lds\n\t"
      "s_add_u32 m0, s49, 10240\n\ts_add_u32 s48, s57, 0x14000\n\tbuffer_load_dwordx4 v18, s[44:47], s48 offen  lds\n\ts_add_u32 m0, s49, 11264\n\t"
      "s_add_u32 s48, s57, 0x16000\n\tbuffer_load_dwordx4 v18, s[44:47], s48 offen  lds\n\ts_add_u32 m0, s49, 12288\n\ts_add_u32 s48, s57, 0x18000\n\t"
      "buffer_load_dwordx4 v19, s[44:47], s48 offen  lds\n\ts_add_u32 m0, s49, 13312\n\ts_add_u32 s48, s57, 0x1a000\n\tbuffer_load_dwordx4 v19, s[44:47], s48 offen  lds\n\t"
      "s_add_u32 m0, s49, 14336\n\ts_add_u32 s48, s57, 0x1c000\n\tbuffer_load_dwordx4 v19, s[44:47], s48 offen  lds\n\ts_add_u32 m0, s49, 15360\n\t"
      "s_add_u32 s48, s57, 0x1e000\n\tbuffer_load_dwordx4 v19, s[44:47], s48 offen  lds\n\ts_waitcnt vmcnt(24)\n\tds_read_b64_tr_b16 a[208:209], v28 offset:16384\n\t"
      "ds_read_b64_tr_b16 a[210:211], v28 offset:16640\n\tds_read_b64_tr_b16 a[212:213], v28 offset:18432\n\tds_read_b64_tr_b16 a[214:215], v28 offset:18688\n\t"
      "ds_read_b64_tr_b16 a[240:241], v29 offset:16384\n\tds_read_b64_tr_b16 a[242:243], v29 offset:16640\n\tds_read_b64_tr_b16 a[244:245], v29 offset:18432\n\t"
      "ds_read_b64_tr_b16 a[246:247], v29 offset:18688\n\tds_read_b64_tr_b16 v[120:121], v30 offset:16384\n\tds_read_b64_tr_b16 v[122:123], v30 offset:16640\n\t"
      "ds_read_b64_tr_b16 v[124:125], v30 offset:18432\n\tds_read_b64_tr_b16 v[126:127], v30 offset:18688\n\tds_read_b64_tr_b16 v[176:177], v31 offset:16384\n\t"
      "ds_read_b64_tr_b16 v[178:179], v31 offset:16640\n\tds_read_b64_tr_b16 v[180:181], v31 offset:18432\n\tds_read_b64_tr_b16 v[182:183], v31 offset:18688\n\t"
      "s_waitcnt lgkmcnt(0)\n\ts_add_u32 m0, s49, 16384\n\ts_add_u32 s48, s57, 0x20000\n\tbuffer_load_dwordx4 v18, s[44:47], s48 offen  lds\n\ts_add_u32 m0, s49, 17408\n\t"
      "s_add_u32 s48, s57, 0x22000\n\tbuffer_load_dwordx4 v18, s[44:47], s48 offen  lds\n\ts_add_u32 m0, s49, 18432\n\ts_add_u32 s48, s57, 0x24000\n\t"
      "buffer_load_dwordx4 v18, s[44:47], s48 offen  lds\n\ts_add_u32 m0, s49, 19456\n\ts_add_u32 s48, s57, 0x26000\n\tbuffer_load_dwordx4 v18, s[44:47], s48 offen  lds\n\t"
      "s_add_u32 m0, s49, 20480\n\ts_add_u32 s48, s57, 0x28000\n\tbuffer_load_dwordx4 v19, s[44:47], s48 offen  lds\n\ts_add_u32 m0, s49, 21504\n\t"
      "s_add_u32 s48, s57, 0x2a000\n\tbuffer_load_dwordx4 v19, s[44:47], s48 offen  lds\n\ts_add_u32 m0, s49, 22528\n\ts_add_u32 s48, s57, 0x2c000\n\t"
      "buffer_load_dwordx4 v19, s[44:47], s48 offen  lds\n\ts_add_u32 m0, s49, 23552\n\ts_add_u32 s48, s57, 0x2e000\n\tbuffer_load_dwordx4 v19, s[44:47], s48 offen  lds\n\t"
      "s_waitcnt vmcnt(24)\n\tds_read_b64_tr_b16 a[216:217], v28 offset:24576\n\tds_read_b64_tr_b16 a[218:219], v28 offset:24832\n\t"
      "ds_read_b64_tr_b16 a[220:221], v28 offset:26624\n\tds_read_b64_tr_b16 a[222:223], v28 offset:26880\n\tds_read_b64_tr_b16 a[248:249], v29 offset:24576\n\t"
      "ds_read_b64_tr_b16 a[250:251], v29 offset:24832\n\tds_read_b64_tr_b16 a[252:253], v29 offset:26624\n\tds_read_b64_tr_b16 a[254:255], v29 offset:26880\n\t"
      "ds_read_b64_tr_b16 v[152:153], v30 offset:24576\n\tds_read_b64_tr_b16 v[154:155], v30 offset:24832\n\tds_read_b64_tr_b16 v[156:157], v30 offset:26624\n\t"
      "ds_read_b64_tr_b16 v[158:159], v30 offset:26880\n\tds_read_b64_tr_b16 v[184:185], v31 offset:24576\n\tds_read_b64_tr_b16 v[186:187], v31 offset:24832\n\t"
      "ds_read_b64_tr_b16 v[188:189], v31 offset:26624\n\tds_read_b64_tr_b16 v[190:191], v31 offset:26880\n\ts_waitcnt lgkmcnt(0)\n\ts_add_u32 m0, s49, 24576\n\t"
      "s_add_u32 s48, s57, 0x30000\n\tbuffer_load_dwordx4 v18, s[44:47], s48 offen  lds\n\ts_add_u32 m0, s49, 25600\n\ts_add_u32 s48, s57, 0x32000\n\t"
      "buffer_load_dwordx4 v18, s[44:47], s48 offen  lds\n\ts_add_u32 m0, s49, 26624\n\ts_add_u32 s48, s57, 0x34000\n\tbuffer_load_dwordx4 v18, s[44:47], s48 offen  lds\n\t"
      "s_add_u32 m0, s49, 27648\n\ts_add_u32 s48, s57, 0x36000\n\tbuffer_load_dwordx4 v18, s[44:47], s48 offen  lds\n\ts_add_u32 m0, s49, 28672\n\t"
      "s_add_u32 s48, s57, 0x38000\n\tbuffer_load_dwordx4 v19, s[44:47], s48 offen  lds\n\ts_add_u32 m0, s49, 29696\n\ts_add_u32 s48, s57, 0x3a000\n\t"
      "buffer_load_dwordx4 v19, s[44:47], s48 offen  lds\n\ts_add_u32 m0, s49, 30720\n\ts_add_u32 s48, s57, 0x3c000\n\tbuffer_load_dwordx4 v19, s[44:47], s48 offen  lds\n\t"
      "s_add_u32 m0, s49, 31744\n\ts_add_u32 s48, s57, 0x3e000\n\tbuffer_load_dwordx4 v19, s[44:47], s48 offen  lds\n\ts_waitcnt vmcnt(24)\n\t"
      "ds_read_b64_tr_b16 v[32:33], v28 offset:0\n\tds_read_b64_tr_b16 v[34:35], v28 offset:256\n\tds_read_b64_tr_b16 v[36:37], v28 offset:2048\n\t"
      "ds_read_b64_tr_b16 v[38:39], v28 offset:2304\n\tds_read_b64_tr_b16 v[64:65], v29 offset:0\n\tds_read_b64_tr_b16 v[66:67], v29 offset:256\n\t"
      "ds_read_b64_tr_b16 v[68:69], v29 offset:2048\n\tds_read_b64_tr_b16 v[70:71], v29 offset:2304\n\tds_read_b64_tr_b16 v[96:97], v30 offset:0\n\t"
      "ds_read_b64_tr_b16 v[98:99], v30 offset:256\n\tds_read_b64_tr_b16 v[100:101], v30 offset:2048\n\tds_read_b64_tr_b16 v[102:103], v30 offset:2304\n\t"
      "ds_read_b64_tr_b16 v[128:129], v31 offset:0\n\tds_read_b64_tr_b16 v[130:131], v31 offset:256\n\tds_read_b64_tr_b16 v[132:133], v31 offset:2048\n\t"
      "ds_read_b64_tr_b16 v[134:135], v31 offset:2304\n\ts_waitcnt lgkmcnt(0)\n\tds_write_b128 v5, v[56:59] offset:0\n\tds_write_b128 v5, v[60:63] offset:1024\n\t"
      "ds_write_b128 v5, v[88:91] offset:2048\n\tds_write_b128 v5, v[92:95] offset:3072\n\tds_write_b128 v5, v[120:123] offset:4096\n\t"
      "ds_write_b128 v5, v[124:127] offset:5120\n\tds_write_b128 v5, v[152:155] offset:6144\n\tds_write_b128 v5, v[156:159] offset:7168\n\ts_waitcnt vmcnt(16)\n\t"
      "ds_read_b64_tr_b16 v[40:41], v28 offset:8192\n\tds_read_b64_tr_b16 v[42:43], v28 offset:8448\n\tds_read_b64_tr_b16 v[44:45], v28 offset:10240\n\t"
      "ds_read_b64_tr_b16 v[46:47], v28 offset:10496\n\tds_read_b64_tr_b16 v[72:73], v29 offset:8192\n\tds_read_b64_tr_b16 v[74:75], v29 offset:8448\n\t"
      "ds_read_b64_tr_b16 v[76:77], v29 offset:10240\n\tds_read_b64_tr_b16 v[78:79], v29 offset:10496\n\tds_read_b64_tr_b16 v[104:105], v30 offset:8192\n\t"
      "ds_read_b64_tr_b16 v[106:107], v30 offset:8448\n\tds_read_b64_tr_b16 v[108:109], v30 offset:10240\n\tds_read_b64_tr_b16 v[110:111], v30 offset:10496\n\t"
      "ds_read_b64_tr_b16 v[136:137], v31 offset:8192\n\tds_read_b64_tr_b16 v[138:139], v31 offset:8448\n\tds_read_b64_tr_b16 v[140:141], v31 offset:10240\n\t"
      "ds_read_b64_tr_b16 v[142:143], v31 offset:10496\n\ts_waitcnt lgkmcnt(0)\n\tds_write_b128 v5, v[160:163] offset:8192\n\tds_write_b128 v5, v[164:167] offset:9216\n\t"
      "ds_write_b128 v5, v[168:171] offset:10240\n\tds_write_b128 v5, v[172:175] offset:11264\n\tds_write_b128 v5, v[176:179] offset:12288\n\t"
      "ds_write_b128 v5, v[180:183] offset:13312\n\tds_write_b128 v5, v[184:187] offset:14336\n\tds_write_b128 v5, v[188:191] offset:15360\n\ts_waitcnt vmcnt(8)\n\t"
      "ds_read_b64_tr_b16 v[48:49], v28 offset:16384\n\tds_read_b64_tr_b16 v[50:51], v28 offset:16640\n\tds_read_b64_tr_b16 v[52:53], v28 offset:18432\n\t"
      "ds_read_b64_tr_b16 v[54:55], v28 offset:18688\n\tds_read_b64_tr_b16 v[80:81], v29 offset:16384\n\tds_read_b64_tr_b16 v[82:83], v29 offset:16640\n\t"
      "ds_read_b64_tr_b16 v[84:85], v29 offset:18432\n\tds_read_b64_tr_b16 v[86:87], v29 offset:18688\n\tds_read_b64_tr_b16 v[112:113], v30 offset:16384\n\t"
      "ds_read_b64_tr_b16 v[114:115], v30 offset:16640\n\tds_read_b64_tr_b16 v[116:117], v30 offset:18432\n\tds_read_b64_tr_b16 v[118:119], v30 offset:18688\n\t"
      "ds_read_b64_tr_b16 v[144:145], v31 offset:16384\n\tds_read_b64_tr_b16 v[146:147], v31 offset:16640\n\tds_read_b64_tr_b16 v[148:149], v31 offset:18432\n\t"
      "ds_read_b64_tr_b16 v[150:151], v31 offset:18688\n\ts_waitcnt lgkmcnt(0)\n\tds_write_b128 v5, v[192:195] offset:16384\n\tds_write_b128 v5, v[196:199] offset:17408\n\t"
      "ds_write_b128 v5, v[200:203] offset:18432\n\tds_write_b128 v5, v[204:207] offset:19456\n\tds_write_b128 v5, v[208:211] offset:20480\n\t"
      "ds_write_b128 v5, v[212:215] offset:21504\n\tds_write_b128 v5, v[216:219] offset:22528\n\tds_write_b128 v5, v[220:223] offset:23552\n\ts_waitcnt vmcnt(0)\n\t"
      "ds_read_b64_tr_b16 v[56:57], v28 offset:24576\n\tds_read_b64_tr_b16 v[58:59], v28 offset:24832\n\tds_read_b64_tr_b16 v[60:61], v28 offset:26624\n\t"
      "ds_read_b64_tr_b16 v[62:63], v28 offset:26880\n\tds_read_b64_tr_b16 v[88:89], v29 offset:24576\n\tds_read_b64_tr_b16 v[90:91], v29 offset:24832\n\t"
      "ds_read_b64_tr_b16 v[92:93], v29 offset:26624\n\tds_read_b64_tr_b16 v[94:95], v29 offset:26880\n\tds_read_b64_tr_b16 v[120:121], v30 offset:24576\n\t"
      "ds_read_b64_tr_b16 v[122:123], v30 offset:24832\n\tds_read_b64_tr_b16 v[124:125], v30 offset:26624\n\tds_read_b64_tr_b16 v[126:127], v30 offset:26880\n\t"
      "ds_read_b64_tr_b16 v[152:153], v31 offset:24576\n\tds_read_b64_tr_b16 v[154:155], v31 offset:24832\n\tds_read_b64_tr_b16 v[156:157], v31 offset:26624\n\t"
      "ds_read_b64_tr_b16 v[158:159], v31 offset:26880\n\ts_waitcnt lgkmcnt(0)\n\tds_write_b128 v5, v[224:227] offset:24576\n\tds_write_b128 v5, v[228:231] offset:25600\n\t"
      "ds_write_b128 v5, v[232:235] offset:26624\n\tds_write_b128 v5, v[236:239] offset:27648\n\tds_write_b128 v5, v[240:243] offset:28672\n\t"
      "ds_write_b128 v5, v[244:247] offset:29696\n\tds_write_b128 v5, v[248:251] offset:30720\n\tds_write_b128 v5, v[252:255] offset:31744\n\t"
      "v_mov_b64 v[30:31], 0\n\tv_mov_b32 v29, 160000\n\tds_write_b64 v29, v[30:31]\n\t"
      TKS("s_lshr_b32 s91, s62, 3\n\ts_and_b32 s91, s91, 3\n\t")
      "s_waitcnt lgkmcnt(0)\n\ts_barrier\n\t"
      STS("s_memrealtime vcc\n\ts_waitcnt lgkmcnt(0)\n\tv_mov_b32 v31, vcc_lo\n\tv_mov_b32 v30, 161024\n\tds_write_b32 v30, v31\n\t")
      "1:\n\tbuffer_load_dword v29, off, s[12:15], s63 sc1\n\ts_waitcnt vmcnt(0)\n\tv_readfirstlane_b32 s49, v29\n\ts_bitcmp1_b32 s49, 0\n\ts_cbranch_scc1 2f\n\t"
      "s_cmp_eq_u32 s18, 0\n\ts_cbranch_scc1 2f\n\ts_sub_u32 s18, s18, 1\n\ts_sleep 2\n\ts_branch 1b\n\t"
      "2:\n\ts_and_b32 s48, s49, 3\n\ts_cmp_lg_u32 s48, 3\n\ts_cbranch_scc1 4f\n\ts_cmp_eq_u32 s62, 0\n\ts_cbranch_scc1 4f\n\t"
      "s_waitcnt lgkmcnt(0)\n\ts_barrier\n\ts_mov_b64 s[0:1], s[60:61]\n\ts_mov_b32 s2, s62\n\ts_lshl_b32 s48, s16, 6\n\tv_mbcnt_lo_u32_b32 v0, -1, 0\n\t"
      "v_mbcnt_hi_u32_b32 v0, -1, v0\n\tv_add_u32 v0, s48, v0\n\ts_getpc_b64 s[4:5]\n\t"
      "3:\n\ts_sub_u32 s4, s4, 3b-mk_mega\n\ts_subb_u32 s5, s5, 0\n\ts_setpc_b64 s[4:5]\n\t"
      "4:\n\ts_memrealtime s[46:47]\n\ts_waitcnt lgkmcnt(0)\n\ts_lshl_b32 s19, s46, 2\n\ts_sub_u32 s19, s19, 0x100000\n\ts_lshr_b32 s64, s64, 9\n\ts_and_b32 s48, s64, 0x2000\n\t"
      "s_add_u32 s64, s64, s48\n\ts_lshr_b32 s65, s65, 9\n\ts_and_b32 s48, s65, 0x2000\n\ts_add_u32 s65, s65, s48\n\ts_lshr_b32 s66, s66, 9\n\ts_and_b32 s48, s66, 0x2000\n\t"
      "s_add_u32 s66, s66, s48\n\ts_lshr_b32 s67, s67, 9\n\ts_and_b32 s48, s67, 0x2000\n\ts_add_u32 s67, s67, s48\n\ts_lshl_b32 s48, s49, 10\n\ts_and_b32 s48, s48, 0x2000\n\t"
      "s_mov_b64 s[72:73], s[24:25]\n\tv_or_b32 v0, s48, v0\n\tv_or_b32 v11, s48, v11\n\ts_lshl_b32 s48, s49, 11\n\ts_and_b32 s48, s48, 0x2000\n\tv_or_b32 v9, s48, v9\n\t"
      "10:\n\tds_read_b128 v[160:163], v5 offset:16384\n\tds_read_b128 v[164:167], v5 offset:17408\n\tds_read_b128 v[168:171], v5 offset:18432\n\t"
      "ds_read_b128 v[172:175], v5 offset:19456\n\tds_read_b128 v[176:179], v5 offset:20480\n\tds_read_b128 v[180:183], v5 offset:21504\n\t"
      "ds_read_b128 v[184:187], v5 offset:22528\n\tds_read_b128 v[188:191], v5 offset:23552\n\tds_read_b128 v[192:195], v5 offset:24576\n\t"
      "ds_read_b128 v[196:199], v5 offset:25600\n\tds_read_b128 v[200:203], v5 offset:26624\n\tds_read_b128 v[204:207], v5 offset:27648\n\t"
      "ds_read_b128 v[208:211], v5 offset:28672\n\tds_read_b128 v[212:215], v5 offset:29696\n\tds_read_b128 v[216:219], v5 offset:30720\n\t"
      "ds_read_b128 v[220:223], v5 offset:31744\n\tv_mov_b64 v[16:17], 0\n\tv_mov_b64 v[18:19], 0\n\tv_mov_b64 v[20:21], 0\n\tv_mov_b64 v[22:23], 0\n\ts_and_b32 s48, s17, 3\n\t"
      "s_lshl_b32 s40, s48, 18\n\ts_add_u32 s40, s40, 0x200000\n\ts_lshl_b32 s41, s48, 18\n\ts_add_u32 s41, s41, 0x208000\n\ts_xor_b32 s48, s48, 2\n\ts_lshl_b32 s42, s48, 18\n\t"
      "s_add_u32 s42, s42, 0x208000\n\ts_add_u32 s68, s40, s64\n\ts_add_u32 s69, s40, s65\n\ts_add_u32 s70, s40, s66\n\ts_add_u32 s71, s40, s67\n\ts_and_b32 s43, s17, 1\n\t"
      "s_lshl_b32 s43, s43, 20\n\ts_waitcnt lgkmcnt(0)\n\ts_cmp_lg_u32 s16, 1\n\ts_cbranch_scc1 73f\n\t"
      "71:\n\ts_memrealtime vcc\n\ts_waitcnt lgkmcnt(0)\n\ts_lshl_b32 vcc_lo, vcc_lo, 2\n\t"
      "70:\n\ts_sub_u32 s48, s19, vcc_lo\n\ts_cmp_gt_u32 s48, 4000\n\ts_cbranch_scc1 73f\n\ts_cmp_gt_u32 s48, 28\n\ts_cbranch_scc1 71b\n\ts_mul_i32 s48, s48, 45\n\t"
      "s_lshr_b32 s48, s48, 5\n\ts_lshl_b32 s48, s48, 2\n\ts_getpc_b64 vcc\n\t"
      "72:\n\ts_sub_u32 s48, 73f-72b, s48\n\ts_add_u32 vcc_lo, vcc_lo, s48\n\ts_addc_u32 vcc_hi, vcc_hi, 0\n\ts_setpc_b64 vcc\n\t.rept 40\n\ts_nop 0\n\t.endr\n\t"
      "73:\n\ts_barrier\n\t"
      TK0
      "buffer_load_dwordx4 v[240:243], v0, s[8:11], s68 offen sc1\n\ts_nop 11\n\tbuffer_load_dwordx4 v[244:247], v0, s[8:11], s69 offen sc1\n\ts_nop 11\n\t"
      "buffer_load_dwordx4 v[248:251], v0, s[8:11], s70 offen sc1\n\ts_nop 11\n\tbuffer_load_dwordx4 v[252:255], v0, s[8:11], s71 offen sc1\n\t"
      "11:\n\ts_waitcnt vmcnt(3)\n\t"
      TK1
      "ds_write_b128 v1, v[240:243] offset:0\n\tv_max3_u32 v13, v240, v241, v242\n\tv_max_u32 v13, v13, v243\n\tv_cmp_eq_u32 vcc, -1, v13\n\t"
      "v_smfmac_f32_16x16x64_bf16 v[16:19], v[240:243], a[0:7], v6\n\tv_smfmac_f32_16x16x64_bf16 v[20:23], v[240:243], a[8:15], v6\n\ts_cbranch_vccnz 86f\n\t"
      "20:\n\ts_waitcnt lgkmcnt(0)\n\ts_barrier\n\tds_read_b128 v[24:27], v2 offset:0\n\tds_read_b128 v[28:31], v3 offset:0\n\tds_read_b128 v[224:227], v4 offset:0\n\t"
      "s_waitcnt vmcnt(2)\n\tv_smfmac_f32_16x16x64_bf16 v[16:19], v[244:247], a[16:23], v6\n\tds_write_b128 v1, v[244:247] offset:1024\n\tv_max3_u32 v13, v244, v245, v246\n\t"
      "v_smfmac_f32_16x16x64_bf16 v[20:23], v[244:247], v[160:167], v6\n\ts_waitcnt vmcnt(1)\n\tv_max_u32 v13, v13, v247\n\t"
      "v_smfmac_f32_16x16x64_bf16 v[16:19], v[248:251], a[24:31], v6\n\tv_cmp_eq_u32 vcc, -1, v13\n\tv_smfmac_f32_16x16x64_bf16 v[20:23], v[248:251], a[32:39], v6\n\t"
      "s_waitcnt lgkmcnt(3)\n\tv_max3_u32 v13, v248, v249, v250\n\tv_smfmac_f32_16x16x64_bf16 v[16:19], v[24:27], a[48:55], v6\n\ts_cbranch_vccnz 87f\n\t"
      "21:\n\tv_max_u32 v13, v13, v251\n\tv_smfmac_f32_16x16x64_bf16 v[20:23], v[24:27], a[56:63], v6\n\ts_waitcnt lgkmcnt(2)\n\ts_waitcnt lgkmcnt(0)\n\ts_barrier\n\t"
      "s_waitcnt vmcnt(0)\n\tv_smfmac_f32_16x16x64_bf16 v[16:19], v[252:255], a[40:47], v6\n\tds_read_b128 v[228:231], v2 offset:1024\n\t"
      "ds_read_b128 v[232:235], v3 offset:1024\n\tv_max3_u32 v13, v13, v252, v253\n\tv_smfmac_f32_16x16x64_bf16 v[20:23], v[252:255], v[168:175], v6\n\t"
      "ds_read_b128 v[236:239], v4 offset:1024\n\tv_max3_u32 v13, v13, v254, v255\n\tv_smfmac_f32_16x16x64_bf16 v[16:19], v[28:31], a[64:71], v6\n\t"
      "ds_write_b128 v1, v[248:251] offset:2048\n\tv_smfmac_f32_16x16x64_bf16 v[20:23], v[28:31], v[176:183], v6\n\tv_cmp_eq_u32 vcc, -1, v13\n\t"
      TK2
      "v_smfmac_f32_16x16x64_bf16 v[16:19], v[224:227], a[72:79], v6\n\tv_smfmac_f32_16x16x64_bf16 v[20:23], v[224:227], a[80:87], v6\n\ts_waitcnt lgkmcnt(3)\n\t"
      "ds_write_b128 v1, v[252:255] offset:3072\n\tv_smfmac_f32_16x16x64_bf16 v[16:19], v[228:231], a[88:95], v6\n\ts_cbranch_vccnz 88f\n\t"
      "22:\n\tv_smfmac_f32_16x16x64_bf16 v[20:23], v[228:231], v[184:191], v6\n\ts_waitcnt lgkmcnt(3)\n\tv_smfmac_f32_16x16x64_bf16 v[16:19], v[232:235], a[96:103], v6\n\t"
      "v_smfmac_f32_16x16x64_bf16 v[20:23], v[232:235], a[104:111], v6\n\ts_waitcnt lgkmcnt(2)\n\ts_waitcnt lgkmcnt(0)\n\ts_barrier\n\t"
      "ds_read_b128 v[240:243], v2 offset:2048\n\t"
      TK3
      "v_smfmac_f32_16x16x64_bf16 v[16:19], v[236:239], a[112:119], v6\n\tds_read_b128 v[244:247], v3 offset:2048\n\tds_read_b128 v[248:251], v4 offset:2048\n\t"
      "v_smfmac_f32_16x16x64_bf16 v[20:23], v[236:239], v[192:199], v6\n\tds_read_b128 v[24:27], v2 offset:3072\n\ts_waitcnt lgkmcnt(3)\n\t"
      "v_smfmac_f32_16x16x64_bf16 v[16:19], v[240:243], a[120:127], v6\n\tds_read_b128 v[28:31], v3 offset:3072\n\tds_read_b128 v[224:227], v4 offset:3072\n\t"
      "v_smfmac_f32_16x16x64_bf16 v[20:23], v[240:243], a[128:135], v6\n\ts_waitcnt lgkmcnt(4)\n\tds_read_b128 v[160:163], v5 offset:0\n\t"
      "v_smfmac_f32_16x16x64_bf16 v[16:19], v[244:247], a[136:143], v6\n\tds_read_b128 v[164:167], v5 offset:1024\n\tds_read_b128 v[168:171], v5 offset:2048\n\t"
      "v_smfmac_f32_16x16x64_bf16 v[20:23], v[244:247], v[200:207], v6\n\ts_waitcnt lgkmcnt(6)\n\tds_read_b128 v[172:175], v5 offset:3072\n\t"
      "v_smfmac_f32_16x16x64_bf16 v[16:19], v[248:251], a[144:151], v6\n\tds_read_b128 v[176:179], v5 offset:4096\n\tds_read_b128 v[180:183], v5 offset:5120\n\t"
      "v_smfmac_f32_16x16x64_bf16 v[20:23], v[248:251], a[152:159], v6\n\ts_waitcnt lgkmcnt(8)\n\tds_read_b128 v[184:187], v5 offset:6144\n\t"
      "v_smfmac_f32_16x16x64_bf16 v[16:19], v[24:27], a[160:167], v6\n\tds_read_b128 v[188:191], v5 offset:7168\n\tds_read_b128 v[192:195], v5 offset:8192\n\t"
      "v_smfmac_f32_16x16x64_bf16 v[20:23], v[24:27], v[208:215], v6\n\ts_waitcnt lgkmcnt(10)\n\tds_read_b128 v[196:199], v5 offset:9216\n\t"
      "v_smfmac_f32_16x16x64_bf16 v[16:19], v[28:31], a[168:175], v6\n\tds_read_b128 v[200:203], v5 offset:10240\n\tds_read_b128 v[204:207], v5 offset:11264\n\t"
      "v_smfmac_f32_16x16x64_bf16 v[20:23], v[28:31], a[176:183], v6\n\ts_waitcnt lgkmcnt(12)\n\tds_read_b128 v[208:211], v5 offset:12288\n\t"
      "v_smfmac_f32_16x16x64_bf16 v[16:19], v[224:227], a[184:191], v6\n\tds_read_b128 v[212:215], v5 offset:13312\n\t"
      "v_smfmac_f32_16x16x64_bf16 v[20:23], v[224:227], v[216:223], v6\n\tds_read_b128 v[216:219], v5 offset:14336\n\t"
      TK4
      "v_mov_b64 v[228:229], 0\n\tv_mov_b64 v[230:231], 0\n\tv_mov_b64 v[232:233], 0\n\tv_mov_b64 v[234:235], 0\n\tv_mov_b64 v[236:237], 0\n\tv_add_f32 v16, v16, v17\n\t"
      "v_add_f32 v18, v18, v19\n\tv_add_f32 v20, v20, v21\n\tv_add_f32 v22, v22, v23\n\tv_cvt_pk_bf16_f32 v16, v16, v20\n\tv_cvt_pk_bf16_f32 v18, v18, v22\n\t"
      "ds_write2_b32 v7, v16, v18 offset1:68\n\tv_mov_b64 v[238:239], 0\n\tv_mov_b64 v[224:225], 0\n\tv_mov_b64 v[226:227], 0\n\tv_mov_b64 v[24:25], 0\n\t"
      "v_mov_b64 v[26:27], 0\n\tv_mov_b64 v[28:29], 0\n\tv_mov_b64 v[30:31], 0\n\ts_waitcnt lgkmcnt(0)\n\ts_barrier\n\t"
      TK5
      "ds_read_b128 v[240:243], v8 offset:0\n\tds_read_b128 v[244:247], v8 offset:64\n\tds_read_b128 v[248:251], v8 offset:128\n\tds_read_b128 v[252:255], v8 offset:192\n\t"
      "v_mov_b64 v[16:17], 0\n\tv_mov_b64 v[18:19], 0\n\tv_mov_b64 v[20:21], 0\n\tv_mov_b64 v[22:23], 0\n\ts_waitcnt lgkmcnt(3)\n\t"
      "v_smfmac_f32_16x16x64_bf16 v[224:227], v[240:243], v[32:39], v12\n\tds_read_b128 v[220:223], v5 offset:15360\n\t"
      "v_smfmac_f32_16x16x64_bf16 v[228:231], v[240:243], v[64:71], v12\n\ts_waitcnt lgkmcnt(3)\n\tv_smfmac_f32_16x16x64_bf16 v[224:227], v[244:247], v[40:47], v12\n\t"
      "v_smfmac_f32_16x16x64_bf16 v[228:231], v[244:247], v[72:79], v12\n\ts_waitcnt lgkmcnt(2)\n\tv_smfmac_f32_16x16x64_bf16 v[224:227], v[248:251], v[48:55], v12\n\t"
      "v_smfmac_f32_16x16x64_bf16 v[228:231], v[248:251], v[80:87], v12\n\ts_waitcnt lgkmcnt(1)\n\tv_smfmac_f32_16x16x64_bf16 v[224:227], v[252:255], v[56:63], v12\n\t"
      "v_smfmac_f32_16x16x64_bf16 v[228:231], v[252:255], v[88:95], v12\n\ts_nop 1\n\tv_smfmac_f32_16x16x64_bf16 v[232:235], v[240:243], v[96:103], v12\n\ts_nop 2\n\t"
      "v_smfmac_f32_16x16x64_bf16 v[236:239], v[240:243], v[128:135], v12\n\tv_cvt_pk_bf16_f32 v224, v224, v225\n\t"
      "v_smfmac_f32_16x16x64_bf16 v[232:235], v[244:247], v[104:111], v12\n\tv_cvt_pk_bf16_f32 v225, v226, v227\n\t"
      "v_smfmac_f32_16x16x64_bf16 v[236:239], v[244:247], v[136:143], v12\n\tbuffer_store_dwordx2 v[224:225], v9, s[8:11], s43 offen sc1\n\t"
      "v_smfmac_f32_16x16x64_bf16 v[232:235], v[248:251], v[112:119], v12\n\ts_add_u32 s43, s43, 0x8000\n\tv_cvt_pk_bf16_f32 v228, v228, v229\n\t"
      "v_smfmac_f32_16x16x64_bf16 v[236:239], v[248:251], v[144:151], v12\n\tv_cvt_pk_bf16_f32 v229, v230, v231\n\t"
      "v_smfmac_f32_16x16x64_bf16 v[232:235], v[252:255], v[120:127], v12\n\tbuffer_store_dwordx2 v[228:229], v9, s[8:11], s43 offen sc1\n\t"
      "v_smfmac_f32_16x16x64_bf16 v[236:239], v[252:255], v[152:159], v12\n\ts_nop 1\n\ts_add_u32 s43, s43, 0x8000\n\t"
      "v_smfmac_f32_16x16x64_bf16 v[24:27], v[240:243], a[192:199], v12\n\ts_nop 2\n\tv_smfmac_f32_16x16x64_bf16 v[28:31], v[240:243], a[224:231], v12\n\t"
      "v_cvt_pk_bf16_f32 v232, v232, v233\n\tv_smfmac_f32_16x16x64_bf16 v[24:27], v[244:247], a[200:207], v12\n\tv_cvt_pk_bf16_f32 v233, v234, v235\n\t"
      "v_smfmac_f32_16x16x64_bf16 v[28:31], v[244:247], a[232:239], v12\n\tbuffer_store_dwordx2 v[232:233], v9, s[8:11], s43 offen sc1\n\t"
      "v_smfmac_f32_16x16x64_bf16 v[24:27], v[248:251], a[208:215], v12\n\ts_add_u32 s43, s43, 0x8000\n\tv_cvt_pk_bf16_f32 v236, v236, v237\n\t"
      "v_smfmac_f32_16x16x64_bf16 v[28:31], v[248:251], a[240:247], v12\n\tv_cvt_pk_bf16_f32 v237, v238, v239\n\t"
      "v_smfmac_f32_16x16x64_bf16 v[24:27], v[252:255], a[216:223], v12\n\ts_waitcnt lgkmcnt(0)\n\tbuffer_store_dwordx2 v[236:237], v9, s[8:11], s43 offen sc1\n\t"
      "v_smfmac_f32_16x16x64_bf16 v[28:31], v[252:255], a[248:255], v12\n\ts_nop 1\n\ts_add_u32 s43, s43, 0x8000\n\t"
      "v_smfmac_f32_16x16x64_bf16 v[16:19], v[240:243], v[160:167], v12\n\ts_nop 2\n\tv_smfmac_f32_16x16x64_bf16 v[20:23], v[240:243], v[192:199], v12\n\t"
      "v_cvt_pk_bf16_f32 v24, v24, v25\n\tv_smfmac_f32_16x16x64_bf16 v[16:19], v[244:247], v[168:175], v12\n\tv_cvt_pk_bf16_f32 v25, v26, v27\n\t"
      "v_smfmac_f32_16x16x64_bf16 v[20:23], v[244:247], v[200:207], v12\n\tbuffer_store_dwordx2 v[24:25], v9, s[8:11], s43 offen sc1\n\t"
      "v_smfmac_f32_16x16x64_bf16 v[16:19], v[248:251], v[176:183], v12\n\ts_add_u32 s43, s43, 0x8000\n\tv_cvt_pk_bf16_f32 v28, v28, v29\n\t"
      "v_smfmac_f32_16x16x64_bf16 v[20:23], v[248:251], v[208:215], v12\n\tv_cvt_pk_bf16_f32 v29, v30, v31\n\t"
      "v_smfmac_f32_16x16x64_bf16 v[16:19], v[252:255], v[184:191], v12\n\tbuffer_store_dwordx2 v[28:29], v9, s[8:11], s43 offen sc1\n\t"
      "v_smfmac_f32_16x16x64_bf16 v[20:23], v[252:255], v[216:223], v12\n\ts_add_u32 s43, s43, 0x8000\n\ts_nop 4\n\tv_cvt_pk_bf16_f32 v16, v16, v17\n\t"
      "v_cvt_pk_bf16_f32 v17, v18, v19\n\tv_cvt_pk_bf16_f32 v20, v20, v21\n\tv_cvt_pk_bf16_f32 v21, v22, v23\n\tbuffer_store_dwordx2 v[16:17], v9, s[8:11], s43 offen sc1\n\t"
      "s_add_u32 s43, s43, 0x8000\n\tbuffer_store_dwordx2 v[20:21], v9, s[8:11], s43 offen sc1\n\t"
      TK6
      "s_cmp_lg_u32 s16, 1\n\ts_cbranch_scc1 69f\n\t"
      STS("s_memrealtime s[46:47]\n\t")
      "74:\n\tbuffer_load_dword v14, v11, s[8:11], s41 offen sc1\n\ts_waitcnt vmcnt(0)\n\t"
      "76:\n\tv_max_u32_dpp v14, v14, v14 row_ror:8 row_mask:0xf bank_mask:0xf\n\ts_nop 1\n\tv_max_u32_dpp v14, v14, v14 row_ror:4 row_mask:0xf bank_mask:0xf\n\ts_nop 1\n\t"
      "v_max_u32_dpp v14, v14, v14 row_ror:2 row_mask:0xf bank_mask:0xf\n\ts_nop 1\n\tv_max_u32_dpp v14, v14, v14 row_ror:1 row_mask:0xf bank_mask:0xf\n\ts_nop 0\n\t"
      "v_readlane_b32 s52, v14, 0\n\tv_readlane_b32 s48, v14, 16\n\ts_max_u32 s52, s52, s48\n\ts_cmp_lg_u32 s52, -1\n\ts_cbranch_scc1 75f\n\ts_cmp_eq_u32 s18, 0\n\t"
      "s_cbranch_scc1 75f\n\ts_sub_u32 s18, s18, 1\n\ts_branch 74b\n\t"
      "75:\n\ts_sub_u32 s48, s52, s19\n\ts_cmp_gt_u32 s48, s72\n\ts_cselect_b32 s48, s73, 0\n\ts_add_u32 s19, s52, s23\n\ts_add_u32 s19, s19, s48\n\t"
      STS("s_waitcnt lgkmcnt(0)\n\ts_lshl_b32 s46, s46, 2\n\ts_sub_u32 s46, s52, s46\n\ts_add_u32 s61, s61, s46\n\ts_min_i32 s21, s21, s46\n\ts_cmp_lt_u32 s17, 8\n\ts_cselect_b32 s21, 1000, s21\n\t")
      "s_branch 69f\n\t"
      "86:\n\t"
      STS("s_add_u32 s50, s50, 1\n\t")
      "s_waitcnt lgkmcnt(0)\n\tbuffer_load_dwordx4 v[240:243], v0, s[8:11], s68 offen sc1\n\tbuffer_load_dwordx4 v[244:247], v0, s[8:11], s69 offen sc1\n\t"
      "buffer_load_dwordx4 v[248:251], v0, s[8:11], s70 offen sc1\n\tbuffer_load_dwordx4 v[252:255], v0, s[8:11], s71 offen sc1\n\ts_nop 3\n\tv_mov_b64 v[16:17], 0\n\t"
      "v_mov_b64 v[18:19], 0\n\tv_mov_b64 v[20:21], 0\n\tv_mov_b64 v[22:23], 0\n\ts_nop 1\n\ts_branch 84f\n\t"
      "83:\n\tbuffer_load_dwordx4 v[240:243], v0, s[8:11], s68 offen sc1\n\tbuffer_load_dwordx4 v[244:247], v0, s[8:11], s69 offen sc1\n\t"
      "buffer_load_dwordx4 v[248:251], v0, s[8:11], s70 offen sc1\n\tbuffer_load_dwordx4 v[252:255], v0, s[8:11], s71 offen sc1\n\t"
      "84:\n\ts_waitcnt vmcnt(0)\n\tv_max3_u32 v13, v240, v241, v242\n\tv_max_u32 v13, v13, v243\n\tv_max3_u32 v13, v13, v244, v245\n\tv_max3_u32 v13, v13, v246, v247\n\t"
      "v_max3_u32 v13, v13, v248, v249\n\tv_max3_u32 v13, v13, v250, v251\n\tv_max3_u32 v13, v13, v252, v253\n\tv_max3_u32 v13, v13, v254, v255\n\tv_cmp_eq_u32 vcc, -1, v13\n\t"
      "s_cbranch_vccz 85f\n\ts_cmp_eq_u32 s18, 0\n\ts_cbranch_scc1 85f\n\ts_sub_u32 s18, s18, 1\n\ts_branch 83b\n\t"
      "85:\n\tv_smfmac_f32_16x16x64_bf16 v[16:19], v[240:243], a[0:7], v6\n\tv_smfmac_f32_16x16x64_bf16 v[20:23], v[240:243], a[8:15], v6\n\t"
      "ds_write_b128 v1, v[240:243] offset:0\n\tds_write_b128 v1, v[244:247] offset:1024\n\tds_write_b128 v1, v[248:251] offset:2048\n\t"
      "ds_write_b128 v1, v[252:255] offset:3072\n\tv_mov_b32 v13, 0\n\ts_mov_b64 vcc, 0\n\ts_waitcnt lgkmcnt(0)\n\ts_branch 20b\n\t"
      "87:\n\t"
      STS("s_add_u32 s50, s50, 1\n\t")
      "s_waitcnt lgkmcnt(0)\n\tbuffer_load_dwordx4 v[244:247], v0, s[8:11], s69 offen sc1\n\tbuffer_load_dwordx4 v[248:251], v0, s[8:11], s70 offen sc1\n\t"
      "buffer_load_dwordx4 v[252:255], v0, s[8:11], s71 offen sc1\n\ts_nop 3\n\tv_mov_b64 v[16:17], 0\n\tv_mov_b64 v[18:19], 0\n\tv_mov_b64 v[20:21], 0\n\t"
      "v_mov_b64 v[22:23], 0\n\ts_nop 1\n\tv_smfmac_f32_16x16x64_bf16 v[16:19], v[240:243], a[0:7], v6\n\tv_smfmac_f32_16x16x64_bf16 v[20:23], v[240:243], a[8:15], v6\n\t"
      "s_branch 84f\n\t"
      "83:\n\tbuffer_load_dwordx4 v[244:247], v0, s[8:11], s69 offen sc1\n\tbuffer_load_dwordx4 v[248:251], v0, s[8:11], s70 offen sc1\n\t"
      "buffer_load_dwordx4 v[252:255], v0, s[8:11], s71 offen sc1\n\t"
      "84:\n\ts_waitcnt vmcnt(0)\n\tv_max3_u32 v13, v244, v245, v246\n\tv_max_u32 v13, v13, v247\n\tv_max3_u32 v13, v13, v248, v249\n\tv_max3_u32 v13, v13, v250, v251\n\t"
      "v_max3_u32 v13, v13, v252, v253\n\tv_max3_u32 v13, v13, v254, v255\n\tv_cmp_eq_u32 vcc, -1, v13\n\ts_cbranch_vccz 85f\n\ts_cmp_eq_u32 s18, 0\n\ts_cbranch_scc1 85f\n\t"
      "s_sub_u32 s18, s18, 1\n\ts_branch 83b\n\t"
      "85:\n\tv_smfmac_f32_16x16x64_bf16 v[16:19], v[244:247], a[16:23], v6\n\tv_smfmac_f32_16x16x64_bf16 v[20:23], v[244:247], v[160:167], v6\n\t"
      "v_smfmac_f32_16x16x64_bf16 v[16:19], v[248:251], a[24:31], v6\n\tv_smfmac_f32_16x16x64_bf16 v[20:23], v[248:251], a[32:39], v6\n\t"
      "v_smfmac_f32_16x16x64_bf16 v[16:19], v[24:27], a[48:55], v6\n\tds_write_b128 v1, v[244:247] offset:1024\n\tds_write_b128 v1, v[248:251] offset:2048\n\t"
      "ds_write_b128 v1, v[252:255] offset:3072\n\tv_mov_b32 v13, 0\n\ts_mov_b64 vcc, 0\n\ts_waitcnt lgkmcnt(0)\n\ts_branch 21b\n\t"
      "88:\n\t"
      STS("s_add_u32 s50, s50, 1\n\t")
      "s_waitcnt lgkmcnt(0)\n\tbuffer_load_dwordx4 v[248:251], v0, s[8:11], s70 offen sc1\n\tbuffer_load_dwordx4 v[252:255], v0, s[8:11], s71 offen sc1\n\ts_nop 3\n\t"
      "v_mov_b64 v[16:17], 0\n\tv_mov_b64 v[18:19], 0\n\tv_mov_b64 v[20:21], 0\n\tv_mov_b64 v[22:23], 0\n\ts_nop 1\n\t"
      "v_smfmac_f32_16x16x64_bf16 v[16:19], v[240:243], a[0:7], v6\n\tv_smfmac_f32_16x16x64_bf16 v[20:23], v[240:243], a[8:15], v6\n\t"
      "v_smfmac_f32_16x16x64_bf16 v[16:19], v[244:247], a[16:23], v6\n\tv_smfmac_f32_16x16x64_bf16 v[20:23], v[244:247], v[160:167], v6\n\ts_branch 84f\n\t"
      "83:\n\tbuffer_load_dwordx4 v[248:251], v0, s[8:11], s70 offen sc1\n\tbuffer_load_dwordx4 v[252:255], v0, s[8:11], s71 offen sc1\n\t"
      "84:\n\ts_waitcnt vmcnt(0)\n\tv_max3_u32 v13, v248, v249, v250\n\tv_max_u32 v13, v13, v251\n\tv_max3_u32 v13, v13, v252, v253\n\tv_max3_u32 v13, v13, v254, v255\n\t"
      "v_cmp_eq_u32 vcc, -1, v13\n\ts_cbranch_vccz 85f\n\ts_cmp_eq_u32 s18, 0\n\ts_cbranch_scc1 85f\n\ts_sub_u32 s18, s18, 1\n\ts_branch 83b\n\t"
      "85:\n\tv_smfmac_f32_16x16x64_bf16 v[16:19], v[248:251], a[24:31], v6\n\tv_smfmac_f32_16x16x64_bf16 v[20:23], v[248:251], a[32:39], v6\n\t"
      "v_smfmac_f32_16x16x64_bf16 v[16:19], v[24:27], a[48:55], v6\n\tv_smfmac_f32_16x16x64_bf16 v[20:23], v[24:27], a[56:63], v6\n\t"
      "v_smfmac_f32_16x16x64_bf16 v[16:19], v[252:255], a[40:47], v6\n\tv_smfmac_f32_16x16x64_bf16 v[20:23], v[252:255], v[168:175], v6\n\t"
      "v_smfmac_f32_16x16x64_bf16 v[16:19], v[28:31], a[64:71], v6\n\tv_smfmac_f32_16x16x64_bf16 v[20:23], v[28:31], v[176:183], v6\n\t"
      "v_smfmac_f32_16x16x64_bf16 v[16:19], v[224:227], a[72:79], v6\n\tv_smfmac_f32_16x16x64_bf16 v[20:23], v[224:227], a[80:87], v6\n\t"
      "v_smfmac_f32_16x16x64_bf16 v[16:19], v[228:231], a[88:95], v6\n\tds_write_b128 v1, v[248:251] offset:2048\n\tds_write_b128 v1, v[252:255] offset:3072\n\t"
      "v_mov_b32 v13, 0\n\ts_mov_b64 vcc, 0\n\ts_waitcnt lgkmcnt(0)\n\ts_branch 22b\n\t"
      "69:\n\t"
      TK7
      STS("s_cmp_lt_i32 s17, 8\n\ts_cselect_b32 s50, 0, s50\n\ts_cselect_b32 s51, 0, s51\n\ts_cselect_b32 s61, 0, s61\n\t")
      TKS("s_waitcnt lgkmcnt(0)\n\ts_cmp_lt_i32 s17, 0\n\ts_cbranch_scc1 98f\n\ts_cmp_lg_u32 s16, s91\n\ts_cbranch_scc1 98f\n\ts_mov_b64 exec, 1\n\ts_lshl_b32 s48, s17, 4\n\ts_add_u32 s48, s48, 161056\n\tv_mov_b32 v13, s48\n\ts_sub_u32 s48, s74, s90\n\ts_min_u32 s48, s48, 0xffff\n\tv_mov_b32 v14, s48\n\tds_write_b16 v13, v14 offset:0\n\ts_sub_u32 s48, s76, s74\n\ts_min_u32 s48, s48, 0xffff\n\tv_mov_b32 v14, s48\n\tds_write_b16 v13, v14 offset:2\n\ts_sub_u32 s48, s78, s76\n\ts_min_u32 s48, s48, 0xffff\n\tv_mov_b32 v14, s48\n\tds_write_b16 v13, v14 offset:4\n\ts_sub_u32 s48, s80, s78\n\ts_min_u32 s48, s48, 0xffff\n\tv_mov_b32 v14, s48\n\tds_write_b16 v13, v14 offset:6\n\ts_sub_u32 s48, s82, s80\n\ts_min_u32 s48, s48, 0xffff\n\tv_mov_b32 v14, s48\n\tds_write_b16 v13, v14 offset:8\n\ts_sub_u32 s48, s84, s82\n\ts_min_u32 s48, s48, 0xffff\n\tv_mov_b32 v14, s48\n\tds_write_b16 v13, v14 offset:10\n\ts_sub_u32 s48, s86, s84\n\ts_min_u32 s48, s48, 0xffff\n\tv_mov_b32 v14, s48\n\tds_write_b16 v13, v14 offset:12\n\ts_sub_u32 s48, s88, s86\n\ts_min_u32 s48, s48, 0xffff\n\tv_mov_b32 v14, s48\n\tds_write_b16 v13, v14 offset:14\n\ts_mov_b64 exec, -1\n\t98:\n\ts_mov_b32 s90, s88\n\t")
      "s_add_u32 s17, s17, 1\n\ts_cmp_lt_u32 s17, 64\n\ts_cbranch_scc1 10b\n\ts_waitcnt vmcnt(0) lgkmcnt(0)\n\t"
      TKS("s_mul_i32 s48, s16, 72\n\ts_add_u32 s48, s48, 162080\n\tv_mov_b32 v224, s48\n\tds_read_b32 v225, v224 offset:0\n\ts_waitcnt lgkmcnt(0)\n\tv_readfirstlane_b32 s74, v225\n\tds_read_b32 v225, v224 offset:4\n\ts_waitcnt lgkmcnt(0)\n\tv_readfirstlane_b32 s75, v225\n\tds_read_b32 v225, v224 offset:8\n\ts_waitcnt lgkmcnt(0)\n\tv_readfirstlane_b32 s76, v225\n\tds_read_b32 v225, v224 offset:12\n\ts_waitcnt lgkmcnt(0)\n\tv_readfirstlane_b32 s77, v225\n\tds_read_b32 v225, v224 offset:16\n\ts_waitcnt lgkmcnt(0)\n\tv_readfirstlane_b32 s78, v225\n\tds_read_b32 v225, v224 offset:20\n\ts_waitcnt lgkmcnt(0)\n\tv_readfirstlane_b32 s79, v225\n\tds_read_b32 v225, v224 offset:24\n\ts_waitcnt lgkmcnt(0)\n\tv_readfirstlane_b32 s80, v225\n\tds_read_b32 v225, v224 offset:28\n\ts_waitcnt lgkmcnt(0)\n\tv_readfirstlane_b32 s81, v225\n\tds_read_b32 v225, v224 offset:32\n\ts_waitcnt lgkmcnt(0)\n\tv_readfirstlane_b32 s82, v225\n\tds_read_b32 v225, v224 offset:36\n\ts_waitcnt lgkmcnt(0)\n\tv_readfirstlane_b32 s83, v225\n\tds_read_b32 v225, v224 offset:40\n\ts_waitcnt lgkmcnt(0)\n\tv_readfirstlane_b32 s84, v225\n\tds_read_b32 v225, v224 offset:44\n\ts_waitcnt lgkmcnt(0)\n\tv_readfirstlane_b32 s85, v225\n\tds_read_b32 v225, v224 offset:48\n\ts_waitcnt lgkmcnt(0)\n\tv_readfirstlane_b32 s86, v225\n\tds_read_b32 v225, v224 offset:52\n\ts_waitcnt lgkmcnt(0)\n\tv_readfirstlane_b32 s87, v225\n\tds_read_b32 v225, v224 offset:56\n\ts_waitcnt lgkmcnt(0)\n\tv_readfirstlane_b32 s88, v225\n\tds_read_b32 v225, v224 offset:60\n\ts_waitcnt lgkmcnt(0)\n\tv_readfirstlane_b32 s89, v225\n\tds_read_b32 v225, v224 offset:64\n\ts_waitcnt lgkmcnt(0)\n\tv_readfirstlane_b32 s90, v225\n\tds_read_b32 v225, v224 offset:68\n\ts_waitcnt lgkmcnt(0)\n\tv_readfirstlane_b32 s91, v225\n\t")
      STS("s_lshl_b32 s48, s16, 2\n\ts_add_u32 s48, s48, 162944\n\tv_mov_b32 v0, s48\n\tv_mov_b32 v1, s50\n\tds_write_b32 v0, v1\n\ts_cmp_lg_u32 s16, 0\n\ts_cbranch_scc1 97f\n\tv_mov_b32 v1, s51\n\tds_write_b32 v0, v1 offset:16\n\t97:\n\ts_cmp_lg_u32 s16, 1\n\ts_cbranch_scc1 95f\n\tv_mov_b32 v1, s61\n\tds_write_b32 v0, v1 offset:16\n\tv_mov_b32 v1, s21\n\tds_write_b32 v0, v1 offset:20\n\tv_mov_b32 v1, s22\n\tds_write_b32 v0, v1 offset:24\n\t95:\n\t")
      "s_cmp_lg_u32 s18, 0\n\ts_cbranch_scc1 96f\n\tv_mov_b32 v1, 1\n\tbuffer_store_dword v1, off, s[12:15], 0 offset:3328 sc1\n\t"
      "96:\n\t"
      : "+{v[0:31]}"(k), "+{v[160:191]}"(s0), "+{v[192:223]}"(s1), ALL_REGS :: "memory", "vcc", "scc", S_CLOB TK_CLOB);
}

DEV void y_role(Ctx& c, int y) {
  const KArg p = enter(c);
  constexpr int ZR = 0, UL = 131072, CW = 155648;
  c.budget = SPIN_CAP;
  const int w = c.w, lane = c.lane = fresh_lane(), g = lane >> 4, cc = lane & 15;
  const v4u sent4 = {SENT, SENT, SENT, SENT};
  unsigned m0 = 0;
  if (w == 0) {
    unsigned best[4] = {~0u, ~0u, ~0u, ~0u};
#pragma unroll
    for (int r = 0; r < 8; ++r) {
      VMWAIT(0);
      const unsigned t0 = (unsigned)__builtin_amdgcn_s_memtime();
      LGKM(0);
      st4s(c.rs, ((r & 2) ? amap(y * 256, 0) : y * 16384 + (y & 31) * 256) + (r & 1) * 8192 + (lane & 31) * 4, (r & 2) ? R_MEM : R_U, SENT);
      VMWAIT(0);
      const unsigned t1 = (unsigned)__builtin_amdgcn_s_memtime();
      LGKM(0);
      best[r & 3] = min(best[r & 3], t1 - t0);
    }
    if (lane < 8) *(LAS unsigned*)(c.L + CW + lane * 4) = 0u;
#pragma unroll
    for (int i = 0; i < 32; ++i) st16s(c.rs, y * 16384 + (i & 3) * 1024 + lane * 16, R_U + (i >> 4) * (64 * 16384) + ((i >> 2) & 3) * 4096, sent4);
#pragma unroll
    for (int i = 0; i < 11; ++i) st16s(c.rs, (y >> 4) * 262144 + (y & 15) * 11264 + lane * 16, R_MEM + i * 1024, sent4);
#pragma unroll
    for (int i = 0; i < 16; ++i) st16s(c.rs, y * 16384 + lane * 16, R_PT + i * 1024, sent4);
    m0 = *(const GAS unsigned*)(p->mem0 + (lane >> 3) * 1024 + 16 * y + 2 * (lane & 7));
    m0 = m0 < SENT - 1u ? m0 : SENT - 1u;
    VMWAIT(0);
    claim(c, (best[1] < best[0] ? 1u << 16 : 0u) | (best[3] < best[2] ? 1u << 24 : 0u));
  }
  if (w == 1 || w == 2) {
    const rsrc_t rx = mkrs(p->x, 2048 * 2048);
    unsigned t = 0;
#pragma unroll
    for (int i = 0; i < 4; ++i) t += __builtin_amdgcn_raw_buffer_load_b32(rx, (2 * y + w - 1) * 8192 + lane * 128, i * MB, 0);
    asm volatile("" :: "v"(t));
  }
  BAR();
  const unsigned vote = wait_verdict(c);
  const int par = vote_b(vote), para = vote_a(vote);
  const int mo = amap(y * 256 + lane * 4, par);
  if (w == 0) st4s(c.rs, mo, R_MEM, m0);
  STAMP(0);
  volatile LAS unsigned* const ZFULL = (volatile LAS unsigned*)(c.L + CW);
  volatile LAS unsigned* const GO = ZFULL + 1;
  volatile LAS unsigned* const TDONE = ZFULL + 2;
  volatile LAS unsigned* const PUB = ZFULL + 4;
  auto lds_wait = [&](volatile LAS unsigned* wd, unsigned need) {
#pragma unroll 1
    while (__builtin_amdgcn_readfirstlane(*wd) < need) { if (spend(c)) break; __builtin_amdgcn_s_sleep(1); }
    asm volatile("" ::: "memory");
  };
  auto lds_post = [&](volatile LAS unsigned* wd, unsigned v) { LGKM(0); *wd = v; };
  TICK_DECL;
  if (w == 0) {
    v32u k;
#pragma unroll
    for (int i = 0; i < 4; ++i) {
      const int o = (i + (y >> 4) + ROFF) & 3;
      k[i] = amap(o * 4096 + lane * 16, par);
      k[4 + i] = amap(16384 + o * 1024 + 2 * g * 128 + cc * 8, par);
      k[8 + i] = R_U + y * 16384 + para * 8192 + o * 2048 + lane * 16;
      k[12 + i] = ZR + o * 8192 + lane * 16;
      k[16 + i] = UL + o * 2048 + lane * 16;
    }
    k[20] = amap(y * 256 + (2 * g + (cc & 1)) * 32 + (cc >> 1) * 4, par);
    k[21] = (cc & 1) ? 0x03020706 : 0x05040100;
    k[22] = (lane & 1) ? 0xEEEE : 0x4444;
    k[23] = amap(24576 + (y >> 4) * 4096 + (lane & 31) * 128, par);
    k[24] = CW;
#pragma unroll
    for (int i = 25; i < 32; ++i) k[i] = 0;
    v32u s0 = {RSRCS, 0, 0, c.budget, 0, 0, PACE, C1, CU, KH, ~0u, 0};
    v16u x0; v8u x3; v32u z[4], x[3];
    asm volatile(
      "v_mov_b32 v32, v23\n\tv_add_u32 v33, 0x8000, v23\n\tv_add_u32 v34, 0x10000, v23\n\ts_nop 0\n\tv_readfirstlane_b32 s8, v160\n\tv_readfirstlane_b32 s9, v161\n\t"
      "v_readfirstlane_b32 s10, v162\n\tv_readfirstlane_b32 s11, v163\n\tv_readfirstlane_b32 s12, v164\n\tv_readfirstlane_b32 s13, v165\n\tv_readfirstlane_b32 s14, v166\n\t"
      "v_readfirstlane_b32 s15, v167\n\tv_readfirstlane_b32 s16, v168\n\tv_readfirstlane_b32 s17, v169\n\tv_readfirstlane_b32 s18, v170\n\tv_readfirstlane_b32 s19, v171\n\t"
      "v_readfirstlane_b32 s20, v172\n\tv_readfirstlane_b32 s21, v173\n\tv_readfirstlane_b32 s22, v174\n\tv_readfirstlane_b32 s23, v175\n\tv_readfirstlane_b32 s24, v176\n\t"
      "v_readfirstlane_b32 s25, v177\n\tv_readfirstlane_b32 s26, v178\n\tv_readfirstlane_b32 s27, v179\n\tv_readfirstlane_b32 s28, v180\n\tv_readfirstlane_b32 s29, v181\n\t"
      "v_readfirstlane_b32 s30, v182\n\tv_readfirstlane_b32 s31, v183\n\ts_nop 4\n\t"
      TKS("s_mul_i32 s48, s16, 72\n\ts_add_u32 s48, s48, 162080\n\tv_mov_b32 v224, s48\n\tv_mov_b32 v225, s74\n\tds_write_b32 v224, v225 offset:0\n\tv_mov_b32 v225, s75\n\tds_write_b32 v224, v225 offset:4\n\tv_mov_b32 v225, s76\n\tds_write_b32 v224, v225 offset:8\n\tv_mov_b32 v225, s77\n\tds_write_b32 v224, v225 offset:12\n\tv_mov_b32 v225, s78\n\tds_write_b32 v224, v225 offset:16\n\tv_mov_b32 v225, s79\n\tds_write_b32 v224, v225 offset:20\n\tv_mov_b32 v225, s80\n\tds_write_b32 v224, v225 offset:24\n\tv_mov_b32 v225, s81\n\tds_write_b32 v224, v225 offset:28\n\tv_mov_b32 v225, s82\n\tds_write_b32 v224, v225 offset:32\n\tv_mov_b32 v225, s83\n\tds_write_b32 v224, v225 offset:36\n\tv_mov_b32 v225, s84\n\tds_write_b32 v224, v225 offset:40\n\tv_mov_b32 v225, s85\n\tds_write_b32 v224, v225 offset:44\n\tv_mov_b32 v225, s86\n\tds_write_b32 v224, v225 offset:48\n\tv_mov_b32 v225, s87\n\tds_write_b32 v224, v225 offset:52\n\tv_mov_b32 v225, s88\n\tds_write_b32 v224, v225 offset:56\n\tv_mov_b32 v225, s89\n\tds_write_b32 v224, v225 offset:60\n\tv_mov_b32 v225, s90\n\tds_write_b32 v224, v225 offset:64\n\tv_mov_b32 v225, s91\n\tds_write_b32 v224, v225 offset:68\n\t")
      "s_mov_b64 s[72:73], s[24:25]\n\t"
      TKS("s_mov_b32 s91, 0\n\t")
      "v_mov_b64 v[36:37], -1\n\tv_mov_b64 v[38:39], -1\n\ts_mov_b32 s42, 0x280000\n\ts_mov_b32 s43, 0x100000\n\ts_memrealtime s[46:47]\n\ts_waitcnt lgkmcnt(0)\n\t"
      "s_lshl_b32 s19, s46, 2\n\ts_sub_u32 s19, s19, 0x100000\n\t"
      "10:\n\t"
      "20:\n\tds_read_b32 v25, v24 offset:0\n\ts_waitcnt lgkmcnt(0)\n\tv_readfirstlane_b32 s48, v25\n\ts_cmp_gt_i32 s48, s17\n\ts_cbranch_scc1 21f\n\ts_cmp_eq_u32 s18, 0\n\t"
      "s_cbranch_scc1 21f\n\ts_sub_u32 s18, s18, 1\n\ts_sleep 1\n\ts_branch 20b\n\t"
      "21:\n\ts_and_b32 s48, s17, 3\n\ts_lshl_b32 s49, s48, 15\n\tv_add_u32 v28, s49, v12\n\tv_add_u32 v29, s49, v13\n\tv_add_u32 v30, s49, v14\n\tv_add_u32 v31, s49, v15\n\t"
      "ds_read_b128 a[0:3], v28 offset:0\n\tds_read_b128 a[4:7], v28 offset:1024\n\tds_read_b128 a[8:11], v28 offset:2048\n\tds_read_b128 a[12:15], v28 offset:3072\n\t"
      "ds_read_b128 a[16:19], v28 offset:4096\n\tds_read_b128 a[20:23], v28 offset:5120\n\tds_read_b128 a[24:27], v28 offset:6144\n\tds_read_b128 a[28:31], v28 offset:7168\n\t"
      "ds_read_b128 a[32:35], v29 offset:0\n\tds_read_b128 a[36:39], v29 offset:1024\n\tds_read_b128 a[40:43], v29 offset:2048\n\tds_read_b128 a[44:47], v29 offset:3072\n\t"
      "ds_read_b128 a[48:51], v29 offset:4096\n\tds_read_b128 a[52:55], v29 offset:5120\n\tds_read_b128 a[56:59], v29 offset:6144\n\t"
      "buffer_store_dword v36, v20, s[8:11], s42 offen sc1\n\tds_read_b128 a[60:63], v29 offset:7168\n\tds_read_b128 a[64:67], v30 offset:0\n\t"
      "buffer_store_dwordx4 v[36:39], v8, s[8:11], s43 offen offset:0 sc1\n\tds_read_b128 a[68:71], v30 offset:1024\n\tds_read_b128 a[72:75], v30 offset:2048\n\t"
      "buffer_store_dwordx4 v[36:39], v8, s[8:11], s43 offen offset:1024 sc1\n\tds_read_b128 a[76:79], v30 offset:3072\n\tds_read_b128 a[80:83], v30 offset:4096\n\t"
      "buffer_store_dwordx4 v[36:39], v9, s[8:11], s43 offen offset:0 sc1\n\tds_read_b128 a[84:87], v30 offset:5120\n\tds_read_b128 a[88:91], v30 offset:6144\n\t"
      "buffer_store_dwordx4 v[36:39], v9, s[8:11], s43 offen offset:1024 sc1\n\tds_read_b128 a[92:95], v30 offset:7168\n\tds_read_b128 a[96:99], v31 offset:0\n\t"
      "buffer_store_dwordx4 v[36:39], v10, s[8:11], s43 offen offset:0 sc1\n\tds_read_b128 a[100:103], v31 offset:1024\n\tds_read_b128 a[104:107], v31 offset:2048\n\t"
      "buffer_store_dwordx4 v[36:39], v10, s[8:11], s43 offen offset:1024 sc1\n\tds_read_b128 a[108:111], v31 offset:3072\n\tds_read_b128 a[112:115], v31 offset:4096\n\t"
      "buffer_store_dwordx4 v[36:39], v11, s[8:11], s43 offen offset:0 sc1\n\tds_read_b128 a[116:119], v31 offset:5120\n\tds_read_b128 a[120:123], v31 offset:6144\n\t"
      "buffer_store_dwordx4 v[36:39], v11, s[8:11], s43 offen offset:1024 sc1\n\tds_read_b128 a[124:127], v31 offset:7168\n\ts_lshl_b32 s41, s48, 18\n\t"
      "s_add_u32 s41, s41, 0x208000\n\ts_add_u32 s48, s17, 1\n\ts_and_b32 s48, s48, 3\n\ts_lshl_b32 s40, s48, 18\n\ts_add_u32 s40, s40, 0x200000\n\t"
      "s_xor_b32 s42, s40, 0x80000\n\ts_and_b32 s43, s17, 1\n\ts_lshl_b32 s43, s43, 20\n\tv_mov_b64 v[120:121], 0\n\tv_mov_b64 v[122:123], 0\n\ts_add_u32 s46, s19, s29\n\t"
      TK0
      STC("s_mov_b32 s57, s18\n\t")
      "71:\n\ts_memrealtime vcc\n\ts_waitcnt lgkmcnt(0)\n\ts_lshl_b32 vcc_lo, vcc_lo, 2\n\t"
      "70:\n\ts_sub_u32 s48, s46, vcc_lo\n\ts_cmp_gt_u32 s48, 4000\n\ts_cbranch_scc1 73f\n\ts_cmp_gt_u32 s48, 28\n\ts_cbranch_scc1 71b\n\ts_mul_i32 s48, s48, 45\n\t"
      "s_lshr_b32 s48, s48, 5\n\ts_lshl_b32 s48, s48, 2\n\ts_getpc_b64 vcc\n\t"
      "72:\n\ts_sub_u32 s48, 73f-72b, s48\n\ts_add_u32 vcc_lo, vcc_lo, s48\n\ts_addc_u32 vcc_hi, vcc_hi, 0\n\ts_setpc_b64 vcc\n\t.rept 40\n\ts_nop 0\n\t.endr\n\t"
      "73:\n\tbuffer_load_dword v112, v32, s[8:11], s41 offen sc1\n\ts_waitcnt vmcnt(0)\n\ts_branch 76f\n\t"
      "74:\n\tbuffer_load_dword v112, v32, s[8:11], s41 offen sc1\n\ts_waitcnt vmcnt(0)\n\t"
      "76:\n\ts_memrealtime vcc\n\tv_max_u32_dpp v112, v112, v112 row_ror:8 row_mask:0xf bank_mask:0xf\n\ts_nop 1\n\t"
      "v_max_u32_dpp v112, v112, v112 row_ror:4 row_mask:0xf bank_mask:0xf\n\ts_nop 1\n\tv_max_u32_dpp v112, v112, v112 row_ror:2 row_mask:0xf bank_mask:0xf\n\ts_nop 1\n\t"
      "v_max_u32_dpp v112, v112, v112 row_ror:1 row_mask:0xf bank_mask:0xf\n\ts_nop 0\n\tv_readlane_b32 s52, v112, 0\n\tv_readlane_b32 s48, v112, 16\n\t"
      "s_max_u32 s52, s52, s48\n\ts_cmp_lg_u32 s52, -1\n\ts_cbranch_scc1 75f\n\ts_waitcnt lgkmcnt(0)\n\ts_cmp_eq_u32 s18, 0\n\ts_cbranch_scc1 75f\n\ts_sub_u32 s18, s18, 1\n\t"
      "s_branch 74b\n\t"
      "75:\n\ts_add_u32 s47, s52, s27\n\t"
      STC("s_waitcnt lgkmcnt(0)\n\ts_mov_b32 s58, vcc_lo\n\ts_sub_u32 s56, s57, s18\n\ts_lshl_b32 s56, s56, 24\n\t")
      TK1
      "s_waitcnt lgkmcnt(0)\n\ts_lshl2_add_u32 vcc_lo, vcc_lo, 10\n\ts_branch 70f\n\t"
      "71:\n\ts_memrealtime vcc\n\ts_waitcnt lgkmcnt(0)\n\ts_lshl_b32 vcc_lo, vcc_lo, 2\n\t"
      "70:\n\ts_sub_u32 s48, s47, vcc_lo\n\ts_cmp_gt_u32 s48, 4000\n\ts_cbranch_scc1 73f\n\ts_cmp_gt_u32 s48, 28\n\ts_cbranch_scc1 71b\n\ts_mul_i32 s48, s48, 45\n\t"
      "s_lshr_b32 s48, s48, 5\n\ts_lshl_b32 s48, s48, 2\n\ts_getpc_b64 vcc\n\t"
      "72:\n\ts_sub_u32 s48, 73f-72b, s48\n\ts_add_u32 vcc_lo, vcc_lo, s48\n\ts_addc_u32 vcc_hi, vcc_hi, 0\n\ts_setpc_b64 vcc\n\t.rept 40\n\ts_nop 0\n\t.endr\n\t"
      "73:\n\t"
      "50:\n\t"
      TK2
      "buffer_load_dwordx4 v[192:195], v0, s[8:11], s41 offen offset:0 sc1\n\tbuffer_load_dwordx4 v[196:199], v0, s[8:11], s41 offen offset:1024 sc1\n\t"
      "buffer_load_dwordx4 v[200:203], v0, s[8:11], s41 offen offset:2048 sc1\n\tbuffer_load_dwordx4 v[204:207], v0, s[8:11], s41 offen offset:3072 sc1\n\t"
      "buffer_load_dwordx2 v[176:177], v4, s[8:11], s41 offen offset:0 sc1\n\tbuffer_load_dwordx2 v[178:179], v4, s[8:11], s41 offen offset:128 sc1\n\t"
      "buffer_load_dwordx4 v[144:147], v8, s[8:11], s43 offen offset:0 sc1\n\tbuffer_load_dwordx4 v[148:151], v8, s[8:11], s43 offen offset:1024 sc1\n\t"
      "buffer_load_dwordx4 v[208:211], v1, s[8:11], s41 offen offset:0 sc1\n\tbuffer_load_dwordx4 v[212:215], v1, s[8:11], s41 offen offset:1024 sc1\n\t"
      "buffer_load_dwordx4 v[216:219], v1, s[8:11], s41 offen offset:2048 sc1\n\tbuffer_load_dwordx4 v[220:223], v1, s[8:11], s41 offen offset:3072 sc1\n\t"
      "buffer_load_dwordx2 v[180:181], v5, s[8:11], s41 offen offset:0 sc1\n\tbuffer_load_dwordx2 v[182:183], v5, s[8:11], s41 offen offset:128 sc1\n\t"
      "buffer_load_dwordx4 v[152:155], v9, s[8:11], s43 offen offset:0 sc1\n\tbuffer_load_dwordx4 v[156:159], v9, s[8:11], s43 offen offset:1024 sc1\n\t"
      "buffer_load_dwordx4 v[224:227], v2, s[8:11], s41 offen offset:0 sc1\n\tbuffer_load_dwordx4 v[228:231], v2, s[8:11], s41 offen offset:1024 sc1\n\t"
      "buffer_load_dwordx4 v[232:235], v2, s[8:11], s41 offen offset:2048 sc1\n\tbuffer_load_dwordx4 v[236:239], v2, s[8:11], s41 offen offset:3072 sc1\n\t"
      "buffer_load_dwordx2 v[184:185], v6, s[8:11], s41 offen offset:0 sc1\n\tbuffer_load_dwordx2 v[186:187], v6, s[8:11], s41 offen offset:128 sc1\n\t"
      "buffer_load_dwordx4 v[160:163], v10, s[8:11], s43 offen offset:0 sc1\n\tbuffer_load_dwordx4 v[164:167], v10, s[8:11], s43 offen offset:1024 sc1\n\t"
      "buffer_load_dwordx4 v[240:243], v3, s[8:11], s41 offen offset:0 sc1\n\tbuffer_load_dwordx4 v[244:247], v3, s[8:11], s41 offen offset:1024 sc1\n\t"
      "buffer_load_dwordx4 v[248:251], v3, s[8:11], s41 offen offset:2048 sc1\n\tbuffer_load_dwordx4 v[252:255], v3, s[8:11], s41 offen offset:3072 sc1\n\t"
      "buffer_load_dwordx2 v[188:189], v7, s[8:11], s41 offen offset:0 sc1\n\tbuffer_load_dwordx2 v[190:191], v7, s[8:11], s41 offen offset:128 sc1\n\t"
      "buffer_load_dwordx4 v[168:171], v11, s[8:11], s43 offen offset:0 sc1\n\tbuffer_load_dwordx4 v[172:175], v11, s[8:11], s43 offen offset:1024 sc1\n\t"
      TK3
      "s_waitcnt vmcnt(31)\n\t"
      TK4
      "v_smfmac_f32_16x16x64_bf16 v[120:123], v[192:195], a[0:7], v22\n\ts_waitcnt vmcnt(30)\n\tv_smfmac_f32_16x16x64_bf16 v[120:123], v[196:199], a[8:15], v22\n\t"
      "s_waitcnt vmcnt(29)\n\tv_smfmac_f32_16x16x64_bf16 v[120:123], v[200:203], a[16:23], v22\n\ts_waitcnt vmcnt(28)\n\t"
      "v_smfmac_f32_16x16x64_bf16 v[120:123], v[204:207], a[24:31], v22\n\ts_waitcnt vmcnt(24)\n\tv_smfmac_f32_16x16x64_bf16 v[120:123], v[176:179], v[144:151], v22\n\t"
      "s_waitcnt vmcnt(23)\n\tv_smfmac_f32_16x16x64_bf16 v[120:123], v[208:211], a[32:39], v22\n\ts_waitcnt vmcnt(22)\n\t"
      "v_smfmac_f32_16x16x64_bf16 v[120:123], v[212:215], a[40:47], v22\n\ts_waitcnt vmcnt(21)\n\tv_smfmac_f32_16x16x64_bf16 v[120:123], v[216:219], a[48:55], v22\n\t"
      "s_waitcnt vmcnt(20)\n\tv_smfmac_f32_16x16x64_bf16 v[120:123], v[220:223], a[56:63], v22\n\ts_waitcnt vmcnt(16)\n\t"
      "v_smfmac_f32_16x16x64_bf16 v[120:123], v[180:183], v[152:159], v22\n\ts_waitcnt vmcnt(15)\n\tv_smfmac_f32_16x16x64_bf16 v[120:123], v[224:227], a[64:71], v22\n\t"
      "s_waitcnt vmcnt(14)\n\tv_smfmac_f32_16x16x64_bf16 v[120:123], v[228:231], a[72:79], v22\n\ts_waitcnt vmcnt(13)\n\t"
      "v_smfmac_f32_16x16x64_bf16 v[120:123], v[232:235], a[80:87], v22\n\ts_waitcnt vmcnt(12)\n\tv_smfmac_f32_16x16x64_bf16 v[120:123], v[236:239], a[88:95], v22\n\t"
      "s_waitcnt vmcnt(8)\n\tv_smfmac_f32_16x16x64_bf16 v[120:123], v[184:187], v[160:167], v22\n\ts_waitcnt vmcnt(7)\n\t"
      "v_smfmac_f32_16x16x64_bf16 v[120:123], v[240:243], a[96:103], v22\n\ts_waitcnt vmcnt(6)\n\tv_smfmac_f32_16x16x64_bf16 v[120:123], v[244:247], a[104:111], v22\n\t"
      "s_waitcnt vmcnt(5)\n\tv_smfmac_f32_16x16x64_bf16 v[120:123], v[248:251], a[112:119], v22\n\ts_waitcnt vmcnt(4)\n\t"
      "v_smfmac_f32_16x16x64_bf16 v[120:123], v[252:255], a[120:127], v22\n\ts_waitcnt vmcnt(0)\n\tv_smfmac_f32_16x16x64_bf16 v[120:123], v[188:191], v[168:175], v22\n\t"
      "52:\n\t"
      TK5
      "s_nop 7\n\tv_add_f32 v124, v120, v121\n\tv_add_f32 v125, v122, v123\n\tv_cvt_pk_bf16_f32 v126, v124, v125\n\tv_cmp_u_f32 vcc, v124, v125\n\ts_nop 0\n\t"
      "v_mov_b32_dpp v127, v126 quad_perm:[1,0,3,2] row_mask:0xf bank_mask:0xf\n\tv_perm_b32 v126, v127, v126, v21\n\ts_cbranch_vccnz 60f\n\t"
      "51:\n\tbuffer_store_dword v126, v20, s[8:11], s40 offen sc1\n\t"
      TK6
      STS("s_memrealtime s[54:55]\n\t")
      "s_add_u32 s49, s17, 1\n\tv_mov_b32 v26, s49\n\tds_write_b32 v24, v26 offset:16\n\ts_sub_u32 s49, s17, 3\n\t"
      "22:\n\tds_read_b32 v25, v24 offset:8\n\ts_waitcnt lgkmcnt(0)\n\tv_readfirstlane_b32 s48, v25\n\ts_cmp_gt_i32 s48, s49\n\ts_cbranch_scc1 23f\n\ts_cmp_eq_u32 s18, 0\n\t"
      "s_cbranch_scc1 23f\n\ts_sub_u32 s18, s18, 1\n\ts_sleep 1\n\ts_branch 22b\n\t"
      "23:\n\t"
      "24:\n\tds_read_b32 v25, v24 offset:12\n\ts_waitcnt lgkmcnt(0)\n\tv_readfirstlane_b32 s48, v25\n\ts_cmp_gt_i32 s48, s49\n\ts_cbranch_scc1 25f\n\ts_cmp_eq_u32 s18, 0\n\t"
      "s_cbranch_scc1 25f\n\ts_sub_u32 s18, s18, 1\n\ts_sleep 1\n\ts_branch 24b\n\t"
      "25:\n\tv_add_u32 v28, s31, v16\n\tv_add_u32 v29, s31, v17\n\tv_add_u32 v30, s31, v18\n\tv_add_u32 v31, s31, v19\n\tds_write_b128 v28, v[144:147] offset:0\n\t"
      "ds_write_b128 v28, v[148:151] offset:1024\n\tds_write_b128 v29, v[152:155] offset:0\n\tds_write_b128 v29, v[156:159] offset:1024\n\t"
      "ds_write_b128 v30, v[160:163] offset:0\n\tds_write_b128 v30, v[164:167] offset:1024\n\tds_write_b128 v31, v[168:171] offset:0\n\t"
      "ds_write_b128 v31, v[172:175] offset:1024\n\ts_add_u32 s31, s31, 0x2000\n\ts_cmp_eq_u32 s31, 0x6000\n\ts_cselect_b32 s31, 0, s31\n\tds_write_b32 v24, v26 offset:4\n\t"
      STC("s_lshl_b32 s48, s17, 4\n\ts_add_u32 s48, s48, 161056\n\tv_mov_b32 v112, s48\n\ts_sub_u32 s48, s52, s19\n\tv_mov_b32 v116, s48\n\tv_mov_b32 v117, s54\n\tv_mov_b32 v118, s56\n\ts_lshl_b32 s58, s58, 2\n\ts_sub_u32 s58, s52, s58\n\tv_mov_b32 v119, s58\n\tds_write_b128 v112, v[116:119]\n\ts_lshl_b32 s48, s54, 2\n\ts_sub_u32 s48, s48, s52\n\ts_add_u32 s51, s51, s48\n\t")
      "s_sub_u32 s48, s52, s19\n\ts_cmp_gt_u32 s48, s72\n\ts_cselect_b32 s48, s73, 0\n\ts_add_u32 s19, s52, s23\n\ts_add_u32 s19, s19, s48\n\t"
      TK7
      STS("s_cmp_lt_i32 s17, 8\n\ts_cselect_b32 s50, 0, s50\n\ts_cselect_b32 s51, 0, s51\n\ts_cselect_b32 s61, 0, s61\n\t")
      TKS("s_waitcnt lgkmcnt(0)\n\ts_cmp_lt_i32 s17, 0\n\ts_cbranch_scc1 98f\n\ts_cmp_lg_u32 s16, s91\n\ts_cbranch_scc1 98f\n\ts_mov_b64 exec, 1\n\ts_lshl_b32 s48, s17, 4\n\ts_add_u32 s48, s48, 161056\n\tv_mov_b32 v25, s48\n\ts_sub_u32 s48, s74, s90\n\ts_min_u32 s48, s48, 0xffff\n\tv_mov_b32 v26, s48\n\tds_write_b16 v25, v26 offset:0\n\ts_sub_u32 s48, s76, s74\n\ts_min_u32 s48, s48, 0xffff\n\tv_mov_b32 v26, s48\n\tds_write_b16 v25, v26 offset:2\n\ts_sub_u32 s48, s78, s76\n\ts_min_u32 s48, s48, 0xffff\n\tv_mov_b32 v26, s48\n\tds_write_b16 v25, v26 offset:4\n\ts_sub_u32 s48, s80, s78\n\ts_min_u32 s48, s48, 0xffff\n\tv_mov_b32 v26, s48\n\tds_write_b16 v25, v26 offset:6\n\ts_sub_u32 s48, s82, s80\n\ts_min_u32 s48, s48, 0xffff\n\tv_mov_b32 v26, s48\n\tds_write_b16 v25, v26 offset:8\n\ts_sub_u32 s48, s84, s82\n\ts_min_u32 s48, s48, 0xffff\n\tv_mov_b32 v26, s48\n\tds_write_b16 v25, v26 offset:10\n\ts_sub_u32 s48, s86, s84\n\ts_min_u32 s48, s48, 0xffff\n\tv_mov_b32 v26, s48\n\tds_write_b16 v25, v26 offset:12\n\ts_sub_u32 s48, s88, s86\n\ts_min_u32 s48, s48, 0xffff\n\tv_mov_b32 v26, s48\n\tds_write_b16 v25, v26 offset:14\n\ts_mov_b64 exec, -1\n\t98:\n\ts_mov_b32 s90, s88\n\t")
      "s_add_u32 s17, s17, 1\n\ts_cmp_lt_u32 s17, 63\n\ts_cbranch_scc1 10b\n\ts_branch 40f\n\t"
      "62:\n\tbuffer_load_dwordx4 v[144:147], v8, s[8:11], s43 offen offset:0 sc1\n\tbuffer_load_dwordx4 v[148:151], v8, s[8:11], s43 offen offset:1024 sc1\n\t"
      "buffer_load_dwordx4 v[152:155], v9, s[8:11], s43 offen offset:0 sc1\n\tbuffer_load_dwordx4 v[156:159], v9, s[8:11], s43 offen offset:1024 sc1\n\t"
      "buffer_load_dwordx4 v[160:163], v10, s[8:11], s43 offen offset:0 sc1\n\tbuffer_load_dwordx4 v[164:167], v10, s[8:11], s43 offen offset:1024 sc1\n\t"
      "buffer_load_dwordx4 v[168:171], v11, s[8:11], s43 offen offset:0 sc1\n\tbuffer_load_dwordx4 v[172:175], v11, s[8:11], s43 offen offset:1024 sc1\n\t"
      STC("s_add_u32 s56, s56, 0x100\n\t")
      "s_waitcnt vmcnt(31)\n\tv_smfmac_f32_16x16x64_bf16 v[120:123], v[192:195], a[0:7], v22\n\ts_waitcnt vmcnt(30)\n\t"
      "v_smfmac_f32_16x16x64_bf16 v[120:123], v[196:199], a[8:15], v22\n\ts_waitcnt vmcnt(29)\n\tv_smfmac_f32_16x16x64_bf16 v[120:123], v[200:203], a[16:23], v22\n\t"
      "s_waitcnt vmcnt(28)\n\tv_smfmac_f32_16x16x64_bf16 v[120:123], v[204:207], a[24:31], v22\n\ts_waitcnt vmcnt(25)\n\t"
      "v_smfmac_f32_16x16x64_bf16 v[120:123], v[208:211], a[32:39], v22\n\ts_waitcnt vmcnt(24)\n\tv_smfmac_f32_16x16x64_bf16 v[120:123], v[212:215], a[40:47], v22\n\t"
      "s_waitcnt vmcnt(23)\n\tv_smfmac_f32_16x16x64_bf16 v[120:123], v[216:219], a[48:55], v22\n\ts_waitcnt vmcnt(22)\n\t"
      "v_smfmac_f32_16x16x64_bf16 v[120:123], v[220:223], a[56:63], v22\n\ts_waitcnt vmcnt(19)\n\tv_smfmac_f32_16x16x64_bf16 v[120:123], v[224:227], a[64:71], v22\n\t"
      "s_waitcnt vmcnt(18)\n\tv_smfmac_f32_16x16x64_bf16 v[120:123], v[228:231], a[72:79], v22\n\ts_waitcnt vmcnt(17)\n\t"
      "v_smfmac_f32_16x16x64_bf16 v[120:123], v[232:235], a[80:87], v22\n\ts_waitcnt vmcnt(16)\n\tv_smfmac_f32_16x16x64_bf16 v[120:123], v[236:239], a[88:95], v22\n\t"
      "s_waitcnt vmcnt(13)\n\tv_smfmac_f32_16x16x64_bf16 v[120:123], v[240:243], a[96:103], v22\n\ts_waitcnt vmcnt(12)\n\t"
      "v_smfmac_f32_16x16x64_bf16 v[120:123], v[244:247], a[104:111], v22\n\ts_waitcnt vmcnt(11)\n\tv_smfmac_f32_16x16x64_bf16 v[120:123], v[248:251], a[112:119], v22\n\t"
      "s_waitcnt vmcnt(10)\n\tv_smfmac_f32_16x16x64_bf16 v[120:123], v[252:255], a[120:127], v22\n\ts_waitcnt vmcnt(6)\n\t"
      "v_smfmac_f32_16x16x64_bf16 v[120:123], v[176:179], v[144:151], v22\n\ts_waitcnt vmcnt(4)\n\tv_smfmac_f32_16x16x64_bf16 v[120:123], v[180:183], v[152:159], v22\n\t"
      "s_waitcnt vmcnt(2)\n\tv_smfmac_f32_16x16x64_bf16 v[120:123], v[184:187], v[160:167], v22\n\ts_waitcnt vmcnt(0)\n\t"
      "v_smfmac_f32_16x16x64_bf16 v[120:123], v[188:191], v[168:175], v22\n\ts_branch 52b\n\t"
      "60:\n\ts_cmp_eq_u32 s30, s17\n\ts_mov_b32 s30, s17\n\ts_cbranch_scc0 61f\n\tv_max3_u32 v128, v144, v145, v146\n\tv_max_u32 v128, v128, v147\n\t"
      "v_max3_u32 v128, v128, v148, v149\n\tv_max3_u32 v128, v128, v150, v151\n\tv_max3_u32 v128, v128, v152, v153\n\tv_max3_u32 v128, v128, v154, v155\n\t"
      "v_max3_u32 v128, v128, v156, v157\n\tv_max3_u32 v128, v128, v158, v159\n\tv_max3_u32 v128, v128, v160, v161\n\tv_max3_u32 v128, v128, v162, v163\n\t"
      "v_max3_u32 v128, v128, v164, v165\n\tv_max3_u32 v128, v128, v166, v167\n\tv_max3_u32 v128, v128, v168, v169\n\tv_max3_u32 v128, v128, v170, v171\n\t"
      "v_max3_u32 v128, v128, v172, v173\n\tv_max3_u32 v128, v128, v174, v175\n\tv_max3_u32 v128, v128, v176, v177\n\tv_max3_u32 v128, v128, v178, v179\n\t"
      "v_max3_u32 v128, v128, v180, v181\n\tv_max3_u32 v128, v128, v182, v183\n\tv_max3_u32 v128, v128, v184, v185\n\tv_max3_u32 v128, v128, v186, v187\n\t"
      "v_max3_u32 v128, v128, v188, v189\n\tv_max3_u32 v128, v128, v190, v191\n\tv_max3_u32 v128, v128, v192, v193\n\tv_max3_u32 v128, v128, v194, v195\n\t"
      "v_max3_u32 v128, v128, v196, v197\n\tv_max3_u32 v128, v128, v198, v199\n\tv_max3_u32 v128, v128, v200, v201\n\tv_max3_u32 v128, v128, v202, v203\n\t"
      "v_max3_u32 v128, v128, v204, v205\n\tv_max3_u32 v128, v128, v206, v207\n\tv_max3_u32 v128, v128, v208, v209\n\tv_max3_u32 v128, v128, v210, v211\n\t"
      "v_max3_u32 v128, v128, v212, v213\n\tv_max3_u32 v128, v128, v214, v215\n\tv_max3_u32 v128, v128, v216, v217\n\tv_max3_u32 v128, v128, v218, v219\n\t"
      "v_max3_u32 v128, v128, v220, v221\n\tv_max3_u32 v128, v128, v222, v223\n\tv_max3_u32 v128, v128, v224, v225\n\tv_max3_u32 v128, v128, v226, v227\n\t"
      "v_max3_u32 v128, v128, v228, v229\n\tv_max3_u32 v128, v128, v230, v231\n\tv_max3_u32 v128, v128, v232, v233\n\tv_max3_u32 v128, v128, v234, v235\n\t"
      "v_max3_u32 v128, v128, v236, v237\n\tv_max3_u32 v128, v128, v238, v239\n\tv_max3_u32 v128, v128, v240, v241\n\tv_max3_u32 v128, v128, v242, v243\n\t"
      "v_max3_u32 v128, v128, v244, v245\n\tv_max3_u32 v128, v128, v246, v247\n\tv_max3_u32 v128, v128, v248, v249\n\tv_max3_u32 v128, v128, v250, v251\n\t"
      "v_max3_u32 v128, v128, v252, v253\n\tv_max3_u32 v128, v128, v254, v255\n\tv_cmp_eq_u32 vcc, -1, v128\n\ts_cbranch_vccz 51b\n\ts_cmp_eq_u32 s18, 0\n\t"
      "s_cbranch_scc1 51b\n\ts_sub_u32 s18, s18, 1\n\t"
      "61:\n\tv_mov_b64 v[120:121], 0\n\tv_mov_b64 v[122:123], 0\n\t"
      STS("s_add_u32 s50, s50, 1\n\t")
      STC("s_add_u32 s56, s56, 1\n\t")
      "s_branch 50b\n\t"
      "40:\n\ts_waitcnt vmcnt(0) lgkmcnt(0)\n\t"
      TKS("s_mul_i32 s48, s16, 72\n\ts_add_u32 s48, s48, 162080\n\tv_mov_b32 v224, s48\n\tds_read_b32 v225, v224 offset:0\n\ts_waitcnt lgkmcnt(0)\n\tv_readfirstlane_b32 s74, v225\n\tds_read_b32 v225, v224 offset:4\n\ts_waitcnt lgkmcnt(0)\n\tv_readfirstlane_b32 s75, v225\n\tds_read_b32 v225, v224 offset:8\n\ts_waitcnt lgkmcnt(0)\n\tv_readfirstlane_b32 s76, v225\n\tds_read_b32 v225, v224 offset:12\n\ts_waitcnt lgkmcnt(0)\n\tv_readfirstlane_b32 s77, v225\n\tds_read_b32 v225, v224 offset:16\n\ts_waitcnt lgkmcnt(0)\n\tv_readfirstlane_b32 s78, v225\n\tds_read_b32 v225, v224 offset:20\n\ts_waitcnt lgkmcnt(0)\n\tv_readfirstlane_b32 s79, v225\n\tds_read_b32 v225, v224 offset:24\n\ts_waitcnt lgkmcnt(0)\n\tv_readfirstlane_b32 s80, v225\n\tds_read_b32 v225, v224 offset:28\n\ts_waitcnt lgkmcnt(0)\n\tv_readfirstlane_b32 s81, v225\n\tds_read_b32 v225, v224 offset:32\n\ts_waitcnt lgkmcnt(0)\n\tv_readfirstlane_b32 s82, v225\n\tds_read_b32 v225, v224 offset:36\n\ts_waitcnt lgkmcnt(0)\n\tv_readfirstlane_b32 s83, v225\n\tds_read_b32 v225, v224 offset:40\n\ts_waitcnt lgkmcnt(0)\n\tv_readfirstlane_b32 s84, v225\n\tds_read_b32 v225, v224 offset:44\n\ts_waitcnt lgkmcnt(0)\n\tv_readfirstlane_b32 s85, v225\n\tds_read_b32 v225, v224 offset:48\n\ts_waitcnt lgkmcnt(0)\n\tv_readfirstlane_b32 s86, v225\n\tds_read_b32 v225, v224 offset:52\n\ts_waitcnt lgkmcnt(0)\n\tv_readfirstlane_b32 s87, v225\n\tds_read_b32 v225, v224 offset:56\n\ts_waitcnt lgkmcnt(0)\n\tv_readfirstlane_b32 s88, v225\n\tds_read_b32 v225, v224 offset:60\n\ts_waitcnt lgkmcnt(0)\n\tv_readfirstlane_b32 s89, v225\n\tds_read_b32 v225, v224 offset:64\n\ts_waitcnt lgkmcnt(0)\n\tv_readfirstlane_b32 s90, v225\n\tds_read_b32 v225, v224 offset:68\n\ts_waitcnt lgkmcnt(0)\n\tv_readfirstlane_b32 s91, v225\n\t")
      STS("s_lshl_b32 s48, s16, 2\n\ts_add_u32 s48, s48, 162944\n\tv_mov_b32 v0, s48\n\tv_mov_b32 v1, s50\n\tds_write_b32 v0, v1\n\ts_cmp_lg_u32 s16, 0\n\ts_cbranch_scc1 97f\n\tv_mov_b32 v1, s51\n\tds_write_b32 v0, v1 offset:16\n\t97:\n\ts_cmp_lg_u32 s16, 0\n\ts_cbranch_scc1 95f\n\tv_mov_b32 v1, s61\n\tds_write_b32 v0, v1 offset:20\n\tv_mov_b32 v1, s21\n\tds_write_b32 v0, v1 offset:24\n\tv_mov_b32 v1, s22\n\tds_write_b32 v0, v1 offset:28\n\t95:\n\t")
      "s_cmp_lg_u32 s18, 0\n\ts_cbranch_scc1 96f\n\tv_mov_b32 v1, 1\n\tbuffer_store_dword v1, off, s[12:15], 0 offset:3328 sc1\n\t"
      "96:\n\t"
      : "+{v[0:31]}"(k), "+{v[160:191]}"(s0), "=&{v[32:39]}"(x3), "=&{v[112:127]}"(x0), "=&{v[128:159]}"(x[0]), "=&{v[192:223]}"(x[1]), "=&{v[224:255]}"(x[2]),
        "=&{a[0:31]}"(z[0]), "=&{a[32:63]}"(z[1]), "=&{a[64:95]}"(z[2]), "=&{a[96:127]}"(z[3])
      :: "memory", "vcc", "scc", S_CLOB TK_CLOB);
  } else if (w < 3) {
    v16u k = {(unsigned)(par * 8192 + lane * 16) + ((HACK & 32) << 25), (unsigned)(UL + lane * 16), (unsigned)(ZR + (w == 2 ? lane * 16 : (g >> 1) * 1024 + (g & 1) * 256 + cc * 16)),
              (unsigned)((16 * (w - 1) + cc) * 4096 + (16 * y + 4 * g) * 2) | ((HACK & 1) << 30), CW};
    v32u s0 = {RSRCS, (unsigned)w, 0, c.budget, 0, plo(p->out), phi(p->out), 2048 * 2048 * 2, 0x00020000, 0, 0};
    v32u x[6];
#define TOK_REGS "+{v[0:15]}"(k), "+{v[160:191]}"(s0), "=&{v[32:63]}"(x[0]), "=&{v[64:95]}"(x[1]), "=&{v[96:127]}"(x[2]), "=&{v[128:159]}"(x[3]), "=&{v[192:223]}"(x[4]), "=&{v[224:255]}"(x[5])
    if (w == 1) asm volatile(
      "s_nop 0\n\tv_readfirstlane_b32 s8, v160\n\tv_readfirstlane_b32 s9, v161\n\tv_readfirstlane_b32 s10, v162\n\tv_readfirstlane_b32 s11, v163\n\t"
      "v_readfirstlane_b32 s12, v164\n\tv_readfirstlane_b32 s13, v165\n\tv_readfirstlane_b32 s14, v166\n\tv_readfirstlane_b32 s15, v167\n\tv_readfirstlane_b32 s16, v168\n\t"
      "v_readfirstlane_b32 s17, v169\n\tv_readfirstlane_b32 s18, v170\n\tv_readfirstlane_b32 s19, v171\n\tv_readfirstlane_b32 s20, v172\n\tv_readfirstlane_b32 s21, v173\n\t"
      "v_readfirstlane_b32 s22, v174\n\tv_readfirstlane_b32 s23, v175\n\tv_readfirstlane_b32 s24, v176\n\tv_readfirstlane_b32 s25, v177\n\ts_nop 4\n\t"
      TKS("s_mul_i32 s48, s16, 72\n\ts_add_u32 s48, s48, 162080\n\tv_mov_b32 v224, s48\n\tv_mov_b32 v225, s74\n\tds_write_b32 v224, v225 offset:0\n\tv_mov_b32 v225, s75\n\tds_write_b32 v224, v225 offset:4\n\tv_mov_b32 v225, s76\n\tds_write_b32 v224, v225 offset:8\n\tv_mov_b32 v225, s77\n\tds_write_b32 v224, v225 offset:12\n\tv_mov_b32 v225, s78\n\tds_write_b32 v224, v225 offset:16\n\tv_mov_b32 v225, s79\n\tds_write_b32 v224, v225 offset:20\n\tv_mov_b32 v225, s80\n\tds_write_b32 v224, v225 offset:24\n\tv_mov_b32 v225, s81\n\tds_write_b32 v224, v225 offset:28\n\tv_mov_b32 v225, s82\n\tds_write_b32 v224, v225 offset:32\n\tv_mov_b32 v225, s83\n\tds_write_b32 v224, v225 offset:36\n\tv_mov_b32 v225, s84\n\tds_write_b32 v224, v225 offset:40\n\tv_mov_b32 v225, s85\n\tds_write_b32 v224, v225 offset:44\n\tv_mov_b32 v225, s86\n\tds_write_b32 v224, v225 offset:48\n\tv_mov_b32 v225, s87\n\tds_write_b32 v224, v225 offset:52\n\tv_mov_b32 v225, s88\n\tds_write_b32 v224, v225 offset:56\n\tv_mov_b32 v225, s89\n\tds_write_b32 v224, v225 offset:60\n\tv_mov_b32 v225, s90\n\tds_write_b32 v224, v225 offset:64\n\tv_mov_b32 v225, s91\n\tds_write_b32 v224, v225 offset:68\n\t")
      TKS("s_mov_b32 s91, 0\n\t")
      "10:\n\ts_and_b32 s48, s17, 7\n\ts_lshl_b32 s43, s48, 17\n\ts_add_u32 s43, s43, 0x300000\n\ts_and_b32 s48, s17, 3\n\ts_lshl_b32 s48, s48, 15\n\tv_add_u32 v8, s48, v2\n\t"
      "v_add_u32 v7, s24, v1\n\ts_add_u32 s49, s17, 1\n\ts_min_u32 s49, s49, 62\n\t"
      "20:\n\tds_read_b32 v5, v4 offset:4\n\ts_waitcnt lgkmcnt(0)\n\tv_readfirstlane_b32 s48, v5\n\ts_cmp_gt_i32 s48, s17\n\ts_cbranch_scc1 21f\n\ts_cmp_eq_u32 s18, 0\n\t"
      "s_cbranch_scc1 21f\n\ts_sub_u32 s18, s18, 1\n\ts_sleep 1\n\ts_branch 20b\n\t"
      "21:\n\t"
      "22:\n\tds_read_b32 v5, v4 offset:0\n\ts_waitcnt lgkmcnt(0)\n\tv_readfirstlane_b32 s48, v5\n\ts_cmp_gt_i32 s48, s17\n\ts_cbranch_scc1 23f\n\ts_cmp_eq_u32 s18, 0\n\t"
      "s_cbranch_scc1 23f\n\ts_sub_u32 s18, s18, 1\n\ts_sleep 1\n\ts_branch 22b\n\t"
      "23:\n\t"
      "24:\n\tds_read_b32 v5, v4 offset:16\n\ts_waitcnt lgkmcnt(0)\n\tv_readfirstlane_b32 s48, v5\n\ts_cmp_gt_i32 s48, s49\n\ts_cbranch_scc1 25f\n\ts_cmp_eq_u32 s18, 0\n\t"
      "s_cbranch_scc1 25f\n\ts_sub_u32 s18, s18, 1\n\ts_sleep 1\n\ts_branch 24b\n\t"
      "25:\n\t"
      TK0
      "s_sleep 27\n\t"
      "50:\n\t"
      TK1
      "s_add_u32 s26, s43, 98304\n\tbuffer_load_dwordx4 v[64:67], v0, s[8:11], s26 offen offset:0 sc1\n\ts_nop 2\n\t"
      "buffer_load_dwordx4 v[68:71], v0, s[8:11], s26 offen offset:1024 sc1\n\ts_nop 2\n\tbuffer_load_dwordx4 v[72:75], v0, s[8:11], s26 offen offset:2048 sc1\n\ts_nop 2\n\t"
      "buffer_load_dwordx4 v[76:79], v0, s[8:11], s26 offen offset:3072 sc1\n\ts_nop 2\n\ts_add_u32 s26, s43, 102400\n\t"
      "buffer_load_dwordx4 v[80:83], v0, s[8:11], s26 offen offset:0 sc1\n\ts_nop 2\n\tbuffer_load_dwordx4 v[84:87], v0, s[8:11], s26 offen offset:1024 sc1\n\ts_nop 2\n\t"
      "buffer_load_dwordx4 v[88:91], v0, s[8:11], s26 offen offset:2048 sc1\n\ts_nop 2\n\tbuffer_load_dwordx4 v[92:95], v0, s[8:11], s26 offen offset:3072 sc1\n\ts_nop 2\n\t"
      "s_add_u32 s26, s43, 0\n\tbuffer_load_dwordx4 v[96:99], v0, s[8:11], s26 offen offset:0 sc1\n\ts_nop 2\n\t"
      "buffer_load_dwordx4 v[100:103], v0, s[8:11], s26 offen offset:1024 sc1\n\ts_nop 2\n\tbuffer_load_dwordx4 v[104:107], v0, s[8:11], s26 offen offset:2048 sc1\n\t"
      "s_nop 2\n\tbuffer_load_dwordx4 v[108:111], v0, s[8:11], s26 offen offset:3072 sc1\n\ts_nop 2\n\ts_add_u32 s26, s43, 4096\n\t"
      "buffer_load_dwordx4 v[112:115], v0, s[8:11], s26 offen offset:0 sc1\n\ts_nop 2\n\tbuffer_load_dwordx4 v[116:119], v0, s[8:11], s26 offen offset:1024 sc1\n\ts_nop 2\n\t"
      "buffer_load_dwordx4 v[120:123], v0, s[8:11], s26 offen offset:2048 sc1\n\ts_nop 2\n\tbuffer_load_dwordx4 v[124:127], v0, s[8:11], s26 offen offset:3072 sc1\n\t"
      "s_nop 2\n\ts_add_u32 s26, s43, 16384\n\tbuffer_load_dwordx4 v[128:131], v0, s[8:11], s26 offen offset:0 sc1\n\ts_nop 2\n\t"
      "buffer_load_dwordx4 v[132:135], v0, s[8:11], s26 offen offset:1024 sc1\n\ts_nop 2\n\tbuffer_load_dwordx4 v[136:139], v0, s[8:11], s26 offen offset:2048 sc1\n\t"
      "s_nop 2\n\tbuffer_load_dwordx4 v[140:143], v0, s[8:11], s26 offen offset:3072 sc1\n\ts_nop 2\n\t"
      TK2
      "ds_read_b128 v[32:35], v7 offset:0\n\tds_read_b128 v[36:39], v7 offset:1024\n\tds_read_b128 v[40:43], v7 offset:2048\n\ts_add_u32 s26, s43, 20480\n\t"
      "buffer_load_dwordx4 v[144:147], v0, s[8:11], s26 offen offset:0 sc1\n\tds_read_b128 v[44:47], v7 offset:3072\n\ts_waitcnt vmcnt(20) lgkmcnt(3)\n\t"
      TK3
      "v_mfma_f32_16x16x32_bf16 v[48:51], v[32:35], v[64:67], 0\n\tbuffer_load_dwordx4 v[148:151], v0, s[8:11], s26 offen offset:1024 sc1\n\t"
      "ds_read_b128 v[32:35], v7 offset:4096\n\ts_waitcnt vmcnt(20) lgkmcnt(3)\n\tv_mfma_f32_16x16x32_bf16 v[52:55], v[36:39], v[68:71], 0\n\t"
      "buffer_load_dwordx4 v[152:155], v0, s[8:11], s26 offen offset:2048 sc1\n\tds_read_b128 v[36:39], v7 offset:5120\n\ts_waitcnt vmcnt(20) lgkmcnt(3)\n\t"
      "v_mfma_f32_16x16x32_bf16 v[48:51], v[40:43], v[72:75], v[48:51]\n\tbuffer_load_dwordx4 v[156:159], v0, s[8:11], s26 offen offset:3072 sc1\n\t"
      "ds_read_b128 v[40:43], v7 offset:6144\n\ts_waitcnt vmcnt(20) lgkmcnt(3)\n\t"
      TK4
      "v_mfma_f32_16x16x32_bf16 v[52:55], v[44:47], v[76:79], v[52:55]\n\tds_read_b128 v[44:47], v7 offset:7168\n\ts_waitcnt vmcnt(19) lgkmcnt(3)\n\t"
      "v_mfma_f32_16x16x32_bf16 v[48:51], v[32:35], v[80:83], v[48:51]\n\tds_read2st64_b64 v[32:35], v8 offset0:0 offset1:1\n\ts_waitcnt vmcnt(18) lgkmcnt(3)\n\t"
      "v_mfma_f32_16x16x32_bf16 v[52:55], v[36:39], v[84:87], v[52:55]\n\tds_read2st64_b64 v[36:39], v8 offset0:4 offset1:5\n\ts_waitcnt vmcnt(17) lgkmcnt(3)\n\t"
      "v_mfma_f32_16x16x32_bf16 v[48:51], v[40:43], v[88:91], v[48:51]\n\tds_read2st64_b64 v[40:43], v8 offset0:8 offset1:9\n\ts_waitcnt vmcnt(16) lgkmcnt(3)\n\t"
      "v_mfma_f32_16x16x32_bf16 v[52:55], v[44:47], v[92:95], v[52:55]\n\tds_read2st64_b64 v[44:47], v8 offset0:12 offset1:13\n\ts_waitcnt vmcnt(15) lgkmcnt(3)\n\t"
      "v_mfma_f32_16x16x32_bf16 v[48:51], v[32:35], v[96:99], v[48:51]\n\tds_read2st64_b64 v[32:35], v8 offset0:16 offset1:17\n\ts_waitcnt vmcnt(14) lgkmcnt(3)\n\t"
      "v_mfma_f32_16x16x32_bf16 v[52:55], v[36:39], v[100:103], v[52:55]\n\tds_read2st64_b64 v[36:39], v8 offset0:20 offset1:21\n\ts_waitcnt vmcnt(13) lgkmcnt(3)\n\t"
      "v_mfma_f32_16x16x32_bf16 v[48:51], v[40:43], v[104:107], v[48:51]\n\tds_read2st64_b64 v[40:43], v8 offset0:24 offset1:25\n\ts_waitcnt vmcnt(12) lgkmcnt(3)\n\t"
      "v_mfma_f32_16x16x32_bf16 v[52:55], v[44:47], v[108:111], v[52:55]\n\tds_read2st64_b64 v[44:47], v8 offset0:28 offset1:29\n\ts_waitcnt vmcnt(11) lgkmcnt(3)\n\t"
      "v_mfma_f32_16x16x32_bf16 v[48:51], v[32:35], v[112:115], v[48:51]\n\tds_read2st64_b64 v[32:35], v8 offset0:32 offset1:33\n\ts_waitcnt vmcnt(10) lgkmcnt(3)\n\t"
      "v_mfma_f32_16x16x32_bf16 v[52:55], v[36:39], v[116:119], v[52:55]\n\tds_read2st64_b64 v[36:39], v8 offset0:36 offset1:37\n\ts_waitcnt vmcnt(9) lgkmcnt(3)\n\t"
      "v_mfma_f32_16x16x32_bf16 v[48:51], v[40:43], v[120:123], v[48:51]\n\tds_read2st64_b64 v[40:43], v8 offset0:40 offset1:41\n\ts_waitcnt vmcnt(8) lgkmcnt(3)\n\t"
      "v_mfma_f32_16x16x32_bf16 v[52:55], v[44:47], v[124:127], v[52:55]\n\tds_read2st64_b64 v[44:47], v8 offset0:44 offset1:45\n\ts_waitcnt vmcnt(7) lgkmcnt(3)\n\t"
      "v_mfma_f32_16x16x32_bf16 v[48:51], v[32:35], v[128:131], v[48:51]\n\tds_read2st64_b64 v[32:35], v8 offset0:48 offset1:49\n\ts_waitcnt vmcnt(6) lgkmcnt(3)\n\t"
      "v_mfma_f32_16x16x32_bf16 v[52:55], v[36:39], v[132:135], v[52:55]\n\tds_read2st64_b64 v[36:39], v8 offset0:52 offset1:53\n\ts_waitcnt vmcnt(5) lgkmcnt(3)\n\t"
      "v_mfma_f32_16x16x32_bf16 v[48:51], v[40:43], v[136:139], v[48:51]\n\tds_read2st64_b64 v[40:43], v8 offset0:56 offset1:57\n\ts_waitcnt vmcnt(4) lgkmcnt(3)\n\t"
      "v_mfma_f32_16x16x32_bf16 v[52:55], v[44:47], v[140:143], v[52:55]\n\tds_read2st64_b64 v[44:47], v8 offset0:60 offset1:61\n\ts_waitcnt vmcnt(3) lgkmcnt(3)\n\t"
      "v_mfma_f32_16x16x32_bf16 v[48:51], v[32:35], v[144:147], v[48:51]\n\ts_waitcnt vmcnt(2) lgkmcnt(2)\n\t"
      "v_mfma_f32_16x16x32_bf16 v[52:55], v[36:39], v[148:151], v[52:55]\n\ts_waitcnt vmcnt(1) lgkmcnt(1)\n\t"
      "v_mfma_f32_16x16x32_bf16 v[48:51], v[40:43], v[152:155], v[48:51]\n\ts_waitcnt vmcnt(0) lgkmcnt(0)\n\t"
      TK5
      "v_mfma_f32_16x16x32_bf16 v[52:55], v[44:47], v[156:159], v[52:55]\n\ts_add_u32 s49, s17, 1\n\tv_mov_b32 v6, s49\n\ts_nop 5\n\t"
      "v_pk_add_f32 v[48:49], v[48:49], v[52:53]\n\tv_pk_add_f32 v[50:51], v[50:51], v[54:55]\n\tv_cmp_u_f32 vcc, v48, v49\n\tv_cvt_pk_bf16_f32 v56, v48, v49\n\t"
      "v_cvt_pk_bf16_f32 v57, v50, v51\n\ts_cbranch_vccnz 60f\n\t"
      "51:\n\tds_write_b32 v4, v6 offset:8\n\tbuffer_store_dwordx2 v[56:57], v3, s[20:23], s25 offen \n\t"
      TK6
      TK7
      STS("s_cmp_lt_i32 s17, 8\n\ts_cselect_b32 s50, 0, s50\n\ts_cselect_b32 s51, 0, s51\n\ts_cselect_b32 s61, 0, s61\n\t")
      TKS("s_waitcnt lgkmcnt(0)\n\ts_cmp_lt_i32 s17, 0\n\ts_cbranch_scc1 98f\n\ts_cmp_lg_u32 s16, s91\n\ts_cbranch_scc1 98f\n\ts_mov_b64 exec, 1\n\ts_lshl_b32 s48, s17, 4\n\ts_add_u32 s48, s48, 161056\n\tv_mov_b32 v5, s48\n\ts_sub_u32 s48, s74, s90\n\ts_min_u32 s48, s48, 0xffff\n\tv_mov_b32 v6, s48\n\tds_write_b16 v5, v6 offset:0\n\ts_sub_u32 s48, s76, s74\n\ts_min_u32 s48, s48, 0xffff\n\tv_mov_b32 v6, s48\n\tds_write_b16 v5, v6 offset:2\n\ts_sub_u32 s48, s78, s76\n\ts_min_u32 s48, s48, 0xffff\n\tv_mov_b32 v6, s48\n\tds_write_b16 v5, v6 offset:4\n\ts_sub_u32 s48, s80, s78\n\ts_min_u32 s48, s48, 0xffff\n\tv_mov_b32 v6, s48\n\tds_write_b16 v5, v6 offset:6\n\ts_sub_u32 s48, s82, s80\n\ts_min_u32 s48, s48, 0xffff\n\tv_mov_b32 v6, s48\n\tds_write_b16 v5, v6 offset:8\n\ts_sub_u32 s48, s84, s82\n\ts_min_u32 s48, s48, 0xffff\n\tv_mov_b32 v6, s48\n\tds_write_b16 v5, v6 offset:10\n\ts_sub_u32 s48, s86, s84\n\ts_min_u32 s48, s48, 0xffff\n\tv_mov_b32 v6, s48\n\tds_write_b16 v5, v6 offset:12\n\ts_sub_u32 s48, s88, s86\n\ts_min_u32 s48, s48, 0xffff\n\tv_mov_b32 v6, s48\n\tds_write_b16 v5, v6 offset:14\n\ts_mov_b64 exec, -1\n\t98:\n\ts_mov_b32 s90, s88\n\t")
      "s_add_u32 s25, s25, 0x20000\n\ts_add_u32 s24, s24, 0x2000\n\ts_cmp_eq_u32 s24, 0x6000\n\ts_cselect_b32 s24, 0, s24\n\ts_add_u32 s17, s17, 1\n\ts_cmp_lt_u32 s17, 64\n\t"
      "s_cbranch_scc1 10b\n\ts_branch 40f\n\t"
      "60:\n\tv_max3_u32 v5, v64, v65, v66\n\tv_max_u32 v5, v5, v67\n\tv_max3_u32 v5, v5, v68, v69\n\tv_max3_u32 v5, v5, v70, v71\n\tv_max3_u32 v5, v5, v72, v73\n\t"
      "v_max3_u32 v5, v5, v74, v75\n\tv_max3_u32 v5, v5, v76, v77\n\tv_max3_u32 v5, v5, v78, v79\n\tv_max3_u32 v5, v5, v80, v81\n\tv_max3_u32 v5, v5, v82, v83\n\t"
      "v_max3_u32 v5, v5, v84, v85\n\tv_max3_u32 v5, v5, v86, v87\n\tv_max3_u32 v5, v5, v88, v89\n\tv_max3_u32 v5, v5, v90, v91\n\tv_max3_u32 v5, v5, v92, v93\n\t"
      "v_max3_u32 v5, v5, v94, v95\n\tv_max3_u32 v5, v5, v96, v97\n\tv_max3_u32 v5, v5, v98, v99\n\tv_max3_u32 v5, v5, v100, v101\n\tv_max3_u32 v5, v5, v102, v103\n\t"
      "v_max3_u32 v5, v5, v104, v105\n\tv_max3_u32 v5, v5, v106, v107\n\tv_max3_u32 v5, v5, v108, v109\n\tv_max3_u32 v5, v5, v110, v111\n\tv_max3_u32 v5, v5, v112, v113\n\t"
      "v_max3_u32 v5, v5, v114, v115\n\tv_max3_u32 v5, v5, v116, v117\n\tv_max3_u32 v5, v5, v118, v119\n\tv_max3_u32 v5, v5, v120, v121\n\tv_max3_u32 v5, v5, v122, v123\n\t"
      "v_max3_u32 v5, v5, v124, v125\n\tv_max3_u32 v5, v5, v126, v127\n\tv_max3_u32 v5, v5, v128, v129\n\tv_max3_u32 v5, v5, v130, v131\n\tv_max3_u32 v5, v5, v132, v133\n\t"
      "v_max3_u32 v5, v5, v134, v135\n\tv_max3_u32 v5, v5, v136, v137\n\tv_max3_u32 v5, v5, v138, v139\n\tv_max3_u32 v5, v5, v140, v141\n\tv_max3_u32 v5, v5, v142, v143\n\t"
      "v_max3_u32 v5, v5, v144, v145\n\tv_max3_u32 v5, v5, v146, v147\n\tv_max3_u32 v5, v5, v148, v149\n\tv_max3_u32 v5, v5, v150, v151\n\tv_max3_u32 v5, v5, v152, v153\n\t"
      "v_max3_u32 v5, v5, v154, v155\n\tv_max3_u32 v5, v5, v156, v157\n\tv_max3_u32 v5, v5, v158, v159\n\tv_cmp_eq_u32 vcc, -1, v5\n\ts_cbranch_vccz 51b\n\t"
      "s_cmp_eq_u32 s18, 0\n\ts_cbranch_scc1 51b\n\ts_sub_u32 s18, s18, 1\n\ts_branch 50b\n\t"
      "40:\n\ts_waitcnt vmcnt(0) lgkmcnt(0)\n\t"
      TKS("s_mul_i32 s48, s16, 72\n\ts_add_u32 s48, s48, 162080\n\tv_mov_b32 v224, s48\n\tds_read_b32 v225, v224 offset:0\n\ts_waitcnt lgkmcnt(0)\n\tv_readfirstlane_b32 s74, v225\n\tds_read_b32 v225, v224 offset:4\n\ts_waitcnt lgkmcnt(0)\n\tv_readfirstlane_b32 s75, v225\n\tds_read_b32 v225, v224 offset:8\n\ts_waitcnt lgkmcnt(0)\n\tv_readfirstlane_b32 s76, v225\n\tds_read_b32 v225, v224 offset:12\n\ts_waitcnt lgkmcnt(0)\n\tv_readfirstlane_b32 s77, v225\n\tds_read_b32 v225, v224 offset:16\n\ts_waitcnt lgkmcnt(0)\n\tv_readfirstlane_b32 s78, v225\n\tds_read_b32 v225, v224 offset:20\n\ts_waitcnt lgkmcnt(0)\n\tv_readfirstlane_b32 s79, v225\n\tds_read_b32 v225, v224 offset:24\n\ts_waitcnt lgkmcnt(0)\n\tv_readfirstlane_b32 s80, v225\n\tds_read_b32 v225, v224 offset:28\n\ts_waitcnt lgkmcnt(0)\n\tv_readfirstlane_b32 s81, v225\n\tds_read_b32 v225, v224 offset:32\n\ts_waitcnt lgkmcnt(0)\n\tv_readfirstlane_b32 s82, v225\n\tds_read_b32 v225, v224 offset:36\n\ts_waitcnt lgkmcnt(0)\n\tv_readfirstlane_b32 s83, v225\n\tds_read_b32 v225, v224 offset:40\n\ts_waitcnt lgkmcnt(0)\n\tv_readfirstlane_b32 s84, v225\n\tds_read_b32 v225, v224 offset:44\n\ts_waitcnt lgkmcnt(0)\n\tv_readfirstlane_b32 s85, v225\n\tds_read_b32 v225, v224 offset:48\n\ts_waitcnt lgkmcnt(0)\n\tv_readfirstlane_b32 s86, v225\n\tds_read_b32 v225, v224 offset:52\n\ts_waitcnt lgkmcnt(0)\n\tv_readfirstlane_b32 s87, v225\n\tds_read_b32 v225, v224 offset:56\n\ts_waitcnt lgkmcnt(0)\n\tv_readfirstlane_b32 s88, v225\n\tds_read_b32 v225, v224 offset:60\n\ts_waitcnt lgkmcnt(0)\n\tv_readfirstlane_b32 s89, v225\n\tds_read_b32 v225, v224 offset:64\n\ts_waitcnt lgkmcnt(0)\n\tv_readfirstlane_b32 s90, v225\n\tds_read_b32 v225, v224 offset:68\n\ts_waitcnt lgkmcnt(0)\n\tv_readfirstlane_b32 s91, v225\n\t")
      STS("s_lshl_b32 s48, s16, 2\n\ts_add_u32 s48, s48, 162944\n\tv_mov_b32 v0, s48\n\tv_mov_b32 v1, s50\n\tds_write_b32 v0, v1\n\ts_cmp_lg_u32 s16, 0\n\ts_cbranch_scc1 97f\n\tv_mov_b32 v1, s51\n\tds_write_b32 v0, v1 offset:16\n\t97:\n\ts_cmp_lg_u32 s16, -1\n\ts_cbranch_scc1 95f\n\tv_mov_b32 v1, s61\n\tds_write_b32 v0, v1 offset:24\n\tv_mov_b32 v1, s21\n\tds_write_b32 v0, v1 offset:28\n\tv_mov_b32 v1, s22\n\tds_write_b32 v0, v1 offset:32\n\t95:\n\t")
      "s_cmp_lg_u32 s18, 0\n\ts_cbranch_scc1 96f\n\tv_mov_b32 v1, 1\n\tbuffer_store_dword v1, off, s[12:15], 0 offset:3328 sc1\n\t"
      "96:\n\t"
      : TOK_REGS :: "memory", "vcc", "scc", S_CLOB TK_CLOB);
    else asm volatile(
      "s_nop 0\n\tv_readfirstlane_b32 s8, v160\n\tv_readfirstlane_b32 s9, v161\n\tv_readfirstlane_b32 s10, v162\n\tv_readfirstlane_b32 s11, v163\n\t"
      "v_readfirstlane_b32 s12, v164\n\tv_readfirstlane_b32 s13, v165\n\tv_readfirstlane_b32 s14, v166\n\tv_readfirstlane_b32 s15, v167\n\tv_readfirstlane_b32 s16, v168\n\t"
      "v_readfirstlane_b32 s17, v169\n\tv_readfirstlane_b32 s18, v170\n\tv_readfirstlane_b32 s19, v171\n\tv_readfirstlane_b32 s20, v172\n\tv_readfirstlane_b32 s21, v173\n\t"
      "v_readfirstlane_b32 s22, v174\n\tv_readfirstlane_b32 s23, v175\n\tv_readfirstlane_b32 s24, v176\n\tv_readfirstlane_b32 s25, v177\n\ts_nop 4\n\t"
      TKS("s_mul_i32 s48, s16, 72\n\ts_add_u32 s48, s48, 162080\n\tv_mov_b32 v224, s48\n\tv_mov_b32 v225, s74\n\tds_write_b32 v224, v225 offset:0\n\tv_mov_b32 v225, s75\n\tds_write_b32 v224, v225 offset:4\n\tv_mov_b32 v225, s76\n\tds_write_b32 v224, v225 offset:8\n\tv_mov_b32 v225, s77\n\tds_write_b32 v224, v225 offset:12\n\tv_mov_b32 v225, s78\n\tds_write_b32 v224, v225 offset:16\n\tv_mov_b32 v225, s79\n\tds_write_b32 v224, v225 offset:20\n\tv_mov_b32 v225, s80\n\tds_write_b32 v224, v225 offset:24\n\tv_mov_b32 v225, s81\n\tds_write_b32 v224, v225 offset:28\n\tv_mov_b32 v225, s82\n\tds_write_b32 v224, v225 offset:32\n\tv_mov_b32 v225, s83\n\tds_write_b32 v224, v225 offset:36\n\tv_mov_b32 v225, s84\n\tds_write_b32 v224, v225 offset:40\n\tv_mov_b32 v225, s85\n\tds_write_b32 v224, v225 offset:44\n\tv_mov_b32 v225, s86\n\tds_write_b32 v224, v225 offset:48\n\tv_mov_b32 v225, s87\n\tds_write_b32 v224, v225 offset:52\n\tv_mov_b32 v225, s88\n\tds_write_b32 v224, v225 offset:56\n\tv_mov_b32 v225, s89\n\tds_write_b32 v224, v225 offset:60\n\tv_mov_b32 v225, s90\n\tds_write_b32 v224, v225 offset:64\n\tv_mov_b32 v225, s91\n\tds_write_b32 v224, v225 offset:68\n\t")
      TKS("s_mov_b32 s91, 0\n\t")
      "10:\n\ts_and_b32 s48, s17, 7\n\ts_lshl_b32 s43, s48, 17\n\ts_add_u32 s43, s43, 0x300000\n\ts_and_b32 s48, s17, 3\n\ts_lshl_b32 s48, s48, 15\n\tv_add_u32 v8, s48, v2\n\t"
      "v_add_u32 v7, s24, v1\n\ts_add_u32 s49, s17, 1\n\ts_min_u32 s49, s49, 62\n\t"
      "20:\n\tds_read_b32 v5, v4 offset:4\n\ts_waitcnt lgkmcnt(0)\n\tv_readfirstlane_b32 s48, v5\n\ts_cmp_gt_i32 s48, s17\n\ts_cbranch_scc1 21f\n\ts_cmp_eq_u32 s18, 0\n\t"
      "s_cbranch_scc1 21f\n\ts_sub_u32 s18, s18, 1\n\ts_sleep 1\n\ts_branch 20b\n\t"
      "21:\n\t"
      "22:\n\tds_read_b32 v5, v4 offset:0\n\ts_waitcnt lgkmcnt(0)\n\tv_readfirstlane_b32 s48, v5\n\ts_cmp_gt_i32 s48, s17\n\ts_cbranch_scc1 23f\n\ts_cmp_eq_u32 s18, 0\n\t"
      "s_cbranch_scc1 23f\n\ts_sub_u32 s18, s18, 1\n\ts_sleep 1\n\ts_branch 22b\n\t"
      "23:\n\t"
      "24:\n\tds_read_b32 v5, v4 offset:16\n\ts_waitcnt lgkmcnt(0)\n\tv_readfirstlane_b32 s48, v5\n\ts_cmp_gt_i32 s48, s49\n\ts_cbranch_scc1 25f\n\ts_cmp_eq_u32 s18, 0\n\t"
      "s_cbranch_scc1 25f\n\ts_sub_u32 s18, s18, 1\n\ts_sleep 1\n\ts_branch 24b\n\t"
      "25:\n\t"
      TK0
      "50:\n\t"
      TK1
      "s_add_u32 s26, s43, 114688\n\tbuffer_load_dwordx4 v[64:67], v0, s[8:11], s26 offen offset:0 sc1\n\ts_nop 4\n\t"
      "buffer_load_dwordx4 v[68:71], v0, s[8:11], s26 offen offset:1024 sc1\n\ts_nop 4\n\tbuffer_load_dwordx4 v[72:75], v0, s[8:11], s26 offen offset:2048 sc1\n\ts_nop 4\n\t"
      "buffer_load_dwordx4 v[76:79], v0, s[8:11], s26 offen offset:3072 sc1\n\ts_nop 4\n\ts_add_u32 s26, s43, 118784\n\t"
      "buffer_load_dwordx4 v[80:83], v0, s[8:11], s26 offen offset:0 sc1\n\ts_nop 4\n\tbuffer_load_dwordx4 v[84:87], v0, s[8:11], s26 offen offset:1024 sc1\n\ts_nop 4\n\t"
      "buffer_load_dwordx4 v[88:91], v0, s[8:11], s26 offen offset:2048 sc1\n\ts_nop 4\n\tbuffer_load_dwordx4 v[92:95], v0, s[8:11], s26 offen offset:3072 sc1\n\ts_nop 4\n\t"
      "s_add_u32 s26, s43, 32768\n\tbuffer_load_dwordx4 v[96:99], v0, s[8:11], s26 offen offset:0 sc1\n\ts_nop 4\n\t"
      "buffer_load_dwordx4 v[100:103], v0, s[8:11], s26 offen offset:1024 sc1\n\ts_nop 4\n\tbuffer_load_dwordx4 v[104:107], v0, s[8:11], s26 offen offset:2048 sc1\n\t"
      "s_nop 4\n\tbuffer_load_dwordx4 v[108:111], v0, s[8:11], s26 offen offset:3072 sc1\n\ts_nop 4\n\ts_add_u32 s26, s43, 36864\n\t"
      "buffer_load_dwordx4 v[112:115], v0, s[8:11], s26 offen offset:0 sc1\n\ts_nop 4\n\tbuffer_load_dwordx4 v[116:119], v0, s[8:11], s26 offen offset:1024 sc1\n\ts_nop 4\n\t"
      "buffer_load_dwordx4 v[120:123], v0, s[8:11], s26 offen offset:2048 sc1\n\ts_nop 4\n\tbuffer_load_dwordx4 v[124:127], v0, s[8:11], s26 offen offset:3072 sc1\n\t"
      "s_nop 4\n\ts_add_u32 s26, s43, 49152\n\tbuffer_load_dwordx4 v[128:131], v0, s[8:11], s26 offen offset:0 sc1\n\ts_nop 4\n\t"
      "buffer_load_dwordx4 v[132:135], v0, s[8:11], s26 offen offset:1024 sc1\n\ts_nop 4\n\tbuffer_load_dwordx4 v[136:139], v0, s[8:11], s26 offen offset:2048 sc1\n\t"
      "s_nop 4\n\tbuffer_load_dwordx4 v[140:143], v0, s[8:11], s26 offen offset:3072 sc1\n\ts_nop 4\n\t"
      TK2
      "ds_read_b128 v[32:35], v7 offset:0\n\tds_read_b128 v[36:39], v7 offset:1024\n\tds_read_b128 v[40:43], v7 offset:2048\n\ts_add_u32 s26, s43, 53248\n\t"
      "buffer_load_dwordx4 v[144:147], v0, s[8:11], s26 offen offset:0 sc1\n\tds_read_b128 v[44:47], v7 offset:3072\n\ts_waitcnt vmcnt(20) lgkmcnt(3)\n\t"
      TK3
      "v_mfma_f32_16x16x32_bf16 v[48:51], v[32:35], v[64:67], 0\n\tbuffer_load_dwordx4 v[148:151], v0, s[8:11], s26 offen offset:1024 sc1\n\t"
      "ds_read_b128 v[32:35], v7 offset:4096\n\ts_waitcnt vmcnt(20) lgkmcnt(3)\n\tv_mfma_f32_16x16x32_bf16 v[52:55], v[36:39], v[68:71], 0\n\t"
      "buffer_load_dwordx4 v[152:155], v0, s[8:11], s26 offen offset:2048 sc1\n\tds_read_b128 v[36:39], v7 offset:5120\n\ts_waitcnt vmcnt(20) lgkmcnt(3)\n\t"
      "v_mfma_f32_16x16x32_bf16 v[48:51], v[40:43], v[72:75], v[48:51]\n\tbuffer_load_dwordx4 v[156:159], v0, s[8:11], s26 offen offset:3072 sc1\n\t"
      "ds_read_b128 v[40:43], v7 offset:6144\n\ts_waitcnt vmcnt(20) lgkmcnt(3)\n\tv_mfma_f32_16x16x32_bf16 v[52:55], v[44:47], v[76:79], v[52:55]\n\t"
      "s_add_u32 s26, s43, 65536\n\tbuffer_load_dwordx4 v[160:163], v0, s[8:11], s26 offen offset:0 sc1\n\tds_read_b128 v[44:47], v7 offset:7168\n\t"
      "s_waitcnt vmcnt(20) lgkmcnt(3)\n\tv_mfma_f32_16x16x32_bf16 v[48:51], v[32:35], v[80:83], v[48:51]\n\t"
      "buffer_load_dwordx4 v[164:167], v0, s[8:11], s26 offen offset:1024 sc1\n\tds_read_b128 v[32:35], v8 offset:0\n\ts_waitcnt vmcnt(20) lgkmcnt(3)\n\t"
      "v_mfma_f32_16x16x32_bf16 v[52:55], v[36:39], v[84:87], v[52:55]\n\tbuffer_load_dwordx4 v[168:171], v0, s[8:11], s26 offen offset:2048 sc1\n\t"
      "ds_read_b128 v[36:39], v8 offset:1024\n\ts_waitcnt vmcnt(20) lgkmcnt(3)\n\tv_mfma_f32_16x16x32_bf16 v[48:51], v[40:43], v[88:91], v[48:51]\n\t"
      "buffer_load_dwordx4 v[172:175], v0, s[8:11], s26 offen offset:3072 sc1\n\tds_read_b128 v[40:43], v8 offset:2048\n\ts_waitcnt vmcnt(20) lgkmcnt(3)\n\t"
      "v_mfma_f32_16x16x32_bf16 v[52:55], v[44:47], v[92:95], v[52:55]\n\ts_add_u32 s26, s43, 69632\n\tbuffer_load_dwordx4 v[176:179], v0, s[8:11], s26 offen offset:0 sc1\n\t"
      "ds_read_b128 v[44:47], v8 offset:3072\n\ts_waitcnt vmcnt(20) lgkmcnt(3)\n\tv_mfma_f32_16x16x32_bf16 v[48:51], v[32:35], v[96:99], v[48:51]\n\t"
      "buffer_load_dwordx4 v[180:183], v0, s[8:11], s26 offen offset:1024 sc1\n\tds_read_b128 v[32:35], v8 offset:4096\n\ts_waitcnt vmcnt(20) lgkmcnt(3)\n\t"
      "v_mfma_f32_16x16x32_bf16 v[52:55], v[36:39], v[100:103], v[52:55]\n\tbuffer_load_dwordx4 v[184:187], v0, s[8:11], s26 offen offset:2048 sc1\n\t"
      "ds_read_b128 v[36:39], v8 offset:5120\n\ts_waitcnt vmcnt(20) lgkmcnt(3)\n\tv_mfma_f32_16x16x32_bf16 v[48:51], v[40:43], v[104:107], v[48:51]\n\t"
      "buffer_load_dwordx4 v[188:191], v0, s[8:11], s26 offen offset:3072 sc1\n\tds_read_b128 v[40:43], v8 offset:6144\n\ts_waitcnt vmcnt(20) lgkmcnt(3)\n\t"
      "v_mfma_f32_16x16x32_bf16 v[52:55], v[44:47], v[108:111], v[52:55]\n\ts_add_u32 s26, s43, 81920\n\tbuffer_load_dwordx4 v[192:195], v0, s[8:11], s26 offen offset:0 sc1\n\t"
      "ds_read_b128 v[44:47], v8 offset:7168\n\ts_waitcnt vmcnt(20) lgkmcnt(3)\n\tv_mfma_f32_16x16x32_bf16 v[48:51], v[32:35], v[112:115], v[48:51]\n\t"
      "buffer_load_dwordx4 v[196:199], v0, s[8:11], s26 offen offset:1024 sc1\n\tds_read_b128 v[32:35], v8 offset:8192\n\ts_waitcnt vmcnt(20) lgkmcnt(3)\n\t"
      "v_mfma_f32_16x16x32_bf16 v[52:55], v[36:39], v[116:119], v[52:55]\n\tbuffer_load_dwordx4 v[200:203], v0, s[8:11], s26 offen offset:2048 sc1\n\t"
      "ds_read_b128 v[36:39], v8 offset:9216\n\ts_waitcnt vmcnt(20) lgkmcnt(3)\n\tv_mfma_f32_16x16x32_bf16 v[48:51], v[40:43], v[120:123], v[48:51]\n\t"
      "buffer_load_dwordx4 v[204:207], v0, s[8:11], s26 offen offset:3072 sc1\n\tds_read_b128 v[40:43], v8 offset:10240\n\ts_waitcnt vmcnt(20) lgkmcnt(3)\n\t"
      "v_mfma_f32_16x16x32_bf16 v[52:55], v[44:47], v[124:127], v[52:55]\n\ts_add_u32 s26, s43, 86016\n\tbuffer_load_dwordx4 v[208:211], v0, s[8:11], s26 offen offset:0 sc1\n\t"
      "ds_read_b128 v[44:47], v8 offset:11264\n\ts_waitcnt vmcnt(20) lgkmcnt(3)\n\tv_mfma_f32_16x16x32_bf16 v[48:51], v[32:35], v[128:131], v[48:51]\n\t"
      "buffer_load_dwordx4 v[212:215], v0, s[8:11], s26 offen offset:1024 sc1\n\tds_read_b128 v[32:35], v8 offset:12288\n\ts_waitcnt vmcnt(20) lgkmcnt(3)\n\t"
      "v_mfma_f32_16x16x32_bf16 v[52:55], v[36:39], v[132:135], v[52:55]\n\tbuffer_load_dwordx4 v[216:219], v0, s[8:11], s26 offen offset:2048 sc1\n\t"
      "ds_read_b128 v[36:39], v8 offset:13312\n\ts_waitcnt vmcnt(20) lgkmcnt(3)\n\tv_mfma_f32_16x16x32_bf16 v[48:51], v[40:43], v[136:139], v[48:51]\n\t"
      "buffer_load_dwordx4 v[220:223], v0, s[8:11], s26 offen offset:3072 sc1\n\tds_read_b128 v[40:43], v8 offset:14336\n\ts_waitcnt vmcnt(20) lgkmcnt(3)\n\t"
      TK4
      "v_mfma_f32_16x16x32_bf16 v[52:55], v[44:47], v[140:143], v[52:55]\n\tds_read_b128 v[44:47], v8 offset:15360\n\ts_waitcnt vmcnt(19) lgkmcnt(3)\n\t"
      "v_mfma_f32_16x16x32_bf16 v[48:51], v[32:35], v[144:147], v[48:51]\n\tds_read_b128 v[32:35], v8 offset:16384\n\ts_waitcnt vmcnt(18) lgkmcnt(3)\n\t"
      "v_mfma_f32_16x16x32_bf16 v[52:55], v[36:39], v[148:151], v[52:55]\n\tds_read_b128 v[36:39], v8 offset:17408\n\ts_waitcnt vmcnt(17) lgkmcnt(3)\n\t"
      "v_mfma_f32_16x16x32_bf16 v[48:51], v[40:43], v[152:155], v[48:51]\n\tds_read_b128 v[40:43], v8 offset:18432\n\ts_waitcnt vmcnt(16) lgkmcnt(3)\n\t"
      "v_mfma_f32_16x16x32_bf16 v[52:55], v[44:47], v[156:159], v[52:55]\n\tds_read_b128 v[44:47], v8 offset:19456\n\ts_waitcnt vmcnt(15) lgkmcnt(3)\n\t"
      "v_mfma_f32_16x16x32_bf16 v[48:51], v[32:35], v[160:163], v[48:51]\n\tds_read_b128 v[32:35], v8 offset:20480\n\ts_waitcnt vmcnt(14) lgkmcnt(3)\n\t"
      "v_mfma_f32_16x16x32_bf16 v[52:55], v[36:39], v[164:167], v[52:55]\n\tds_read_b128 v[36:39], v8 offset:21504\n\ts_waitcnt vmcnt(13) lgkmcnt(3)\n\t"
      "v_mfma_f32_16x16x32_bf16 v[48:51], v[40:43], v[168:171], v[48:51]\n\tds_read_b128 v[40:43], v8 offset:22528\n\ts_waitcnt vmcnt(12) lgkmcnt(3)\n\t"
      "v_mfma_f32_16x16x32_bf16 v[52:55], v[44:47], v[172:175], v[52:55]\n\tds_read_b128 v[44:47], v8 offset:23552\n\ts_waitcnt vmcnt(11) lgkmcnt(3)\n\t"
      "v_mfma_f32_16x16x32_bf16 v[48:51], v[32:35], v[176:179], v[48:51]\n\tds_read_b128 v[32:35], v8 offset:24576\n\ts_waitcnt vmcnt(10) lgkmcnt(3)\n\t"
      "v_mfma_f32_16x16x32_bf16 v[52:55], v[36:39], v[180:183], v[52:55]\n\tds_read_b128 v[36:39], v8 offset:25600\n\ts_waitcnt vmcnt(9) lgkmcnt(3)\n\t"
      "v_mfma_f32_16x16x32_bf16 v[48:51], v[40:43], v[184:187], v[48:51]\n\tds_read_b128 v[40:43], v8 offset:26624\n\ts_waitcnt vmcnt(8) lgkmcnt(3)\n\t"
      "v_mfma_f32_16x16x32_bf16 v[52:55], v[44:47], v[188:191], v[52:55]\n\tds_read_b128 v[44:47], v8 offset:27648\n\ts_waitcnt vmcnt(7) lgkmcnt(3)\n\t"
      "v_mfma_f32_16x16x32_bf16 v[48:51], v[32:35], v[192:195], v[48:51]\n\tds_read_b128 v[32:35], v8 offset:28672\n\ts_waitcnt vmcnt(6) lgkmcnt(3)\n\t"
      "v_mfma_f32_16x16x32_bf16 v[52:55], v[36:39], v[196:199], v[52:55]\n\tds_read_b128 v[36:39], v8 offset:29696\n\ts_waitcnt vmcnt(5) lgkmcnt(3)\n\t"
      "v_mfma_f32_16x16x32_bf16 v[48:51], v[40:43], v[200:203], v[48:51]\n\tds_read_b128 v[40:43], v8 offset:30720\n\ts_waitcnt vmcnt(4) lgkmcnt(3)\n\t"
      "v_mfma_f32_16x16x32_bf16 v[52:55], v[44:47], v[204:207], v[52:55]\n\tds_read_b128 v[44:47], v8 offset:31744\n\ts_waitcnt vmcnt(3) lgkmcnt(3)\n\t"
      "v_mfma_f32_16x16x32_bf16 v[48:51], v[32:35], v[208:211], v[48:51]\n\ts_waitcnt vmcnt(2) lgkmcnt(2)\n\t"
      "v_mfma_f32_16x16x32_bf16 v[52:55], v[36:39], v[212:215], v[52:55]\n\ts_waitcnt vmcnt(1) lgkmcnt(1)\n\t"
      "v_mfma_f32_16x16x32_bf16 v[48:51], v[40:43], v[216:219], v[48:51]\n\ts_waitcnt vmcnt(0) lgkmcnt(0)\n\t"
      TK5
      "v_mfma_f32_16x16x32_bf16 v[52:55], v[44:47], v[220:223], v[52:55]\n\ts_add_u32 s49, s17, 1\n\tv_mov_b32 v6, s49\n\ts_nop 5\n\t"
      "v_pk_add_f32 v[48:49], v[48:49], v[52:53]\n\tv_pk_add_f32 v[50:51], v[50:51], v[54:55]\n\tv_cmp_u_f32 vcc, v48, v49\n\tv_cvt_pk_bf16_f32 v56, v48, v49\n\t"
      "v_cvt_pk_bf16_f32 v57, v50, v51\n\ts_cbranch_vccnz 60f\n\t"
      "51:\n\tds_write_b32 v4, v6 offset:12\n\tbuffer_store_dwordx2 v[56:57], v3, s[20:23], s25 offen \n\t"
      TK6
      TK7
      STS("s_cmp_lt_i32 s17, 8\n\ts_cselect_b32 s50, 0, s50\n\ts_cselect_b32 s51, 0, s51\n\ts_cselect_b32 s61, 0, s61\n\t")
      TKS("s_waitcnt lgkmcnt(0)\n\ts_cmp_lt_i32 s17, 0\n\ts_cbranch_scc1 98f\n\ts_cmp_lg_u32 s16, s91\n\ts_cbranch_scc1 98f\n\ts_mov_b64 exec, 1\n\ts_lshl_b32 s48, s17, 4\n\ts_add_u32 s48, s48, 161056\n\tv_mov_b32 v5, s48\n\ts_sub_u32 s48, s74, s90\n\ts_min_u32 s48, s48, 0xffff\n\tv_mov_b32 v6, s48\n\tds_write_b16 v5, v6 offset:0\n\ts_sub_u32 s48, s76, s74\n\ts_min_u32 s48, s48, 0xffff\n\tv_mov_b32 v6, s48\n\tds_write_b16 v5, v6 offset:2\n\ts_sub_u32 s48, s78, s76\n\ts_min_u32 s48, s48, 0xffff\n\tv_mov_b32 v6, s48\n\tds_write_b16 v5, v6 offset:4\n\ts_sub_u32 s48, s80, s78\n\ts_min_u32 s48, s48, 0xffff\n\tv_mov_b32 v6, s48\n\tds_write_b16 v5, v6 offset:6\n\ts_sub_u32 s48, s82, s80\n\ts_min_u32 s48, s48, 0xffff\n\tv_mov_b32 v6, s48\n\tds_write_b16 v5, v6 offset:8\n\ts_sub_u32 s48, s84, s82\n\ts_min_u32 s48, s48, 0xffff\n\tv_mov_b32 v6, s48\n\tds_write_b16 v5, v6 offset:10\n\ts_sub_u32 s48, s86, s84\n\ts_min_u32 s48, s48, 0xffff\n\tv_mov_b32 v6, s48\n\tds_write_b16 v5, v6 offset:12\n\ts_sub_u32 s48, s88, s86\n\ts_min_u32 s48, s48, 0xffff\n\tv_mov_b32 v6, s48\n\tds_write_b16 v5, v6 offset:14\n\ts_mov_b64 exec, -1\n\t98:\n\ts_mov_b32 s90, s88\n\t")
      "s_add_u32 s25, s25, 0x20000\n\ts_add_u32 s24, s24, 0x2000\n\ts_cmp_eq_u32 s24, 0x6000\n\ts_cselect_b32 s24, 0, s24\n\ts_add_u32 s17, s17, 1\n\ts_cmp_lt_u32 s17, 64\n\t"
      "s_cbranch_scc1 10b\n\ts_branch 40f\n\t"
      "60:\n\tv_max3_u32 v5, v64, v65, v66\n\tv_max_u32 v5, v5, v67\n\tv_max3_u32 v5, v5, v68, v69\n\tv_max3_u32 v5, v5, v70, v71\n\tv_max3_u32 v5, v5, v72, v73\n\t"
      "v_max3_u32 v5, v5, v74, v75\n\tv_max3_u32 v5, v5, v76, v77\n\tv_max3_u32 v5, v5, v78, v79\n\tv_max3_u32 v5, v5, v80, v81\n\tv_max3_u32 v5, v5, v82, v83\n\t"
      "v_max3_u32 v5, v5, v84, v85\n\tv_max3_u32 v5, v5, v86, v87\n\tv_max3_u32 v5, v5, v88, v89\n\tv_max3_u32 v5, v5, v90, v91\n\tv_max3_u32 v5, v5, v92, v93\n\t"
      "v_max3_u32 v5, v5, v94, v95\n\tv_max3_u32 v5, v5, v96, v97\n\tv_max3_u32 v5, v5, v98, v99\n\tv_max3_u32 v5, v5, v100, v101\n\tv_max3_u32 v5, v5, v102, v103\n\t"
      "v_max3_u32 v5, v5, v104, v105\n\tv_max3_u32 v5, v5, v106, v107\n\tv_max3_u32 v5, v5, v108, v109\n\tv_max3_u32 v5, v5, v110, v111\n\tv_max3_u32 v5, v5, v112, v113\n\t"
      "v_max3_u32 v5, v5, v114, v115\n\tv_max3_u32 v5, v5, v116, v117\n\tv_max3_u32 v5, v5, v118, v119\n\tv_max3_u32 v5, v5, v120, v121\n\tv_max3_u32 v5, v5, v122, v123\n\t"
      "v_max3_u32 v5, v5, v124, v125\n\tv_max3_u32 v5, v5, v126, v127\n\tv_max3_u32 v5, v5, v128, v129\n\tv_max3_u32 v5, v5, v130, v131\n\tv_max3_u32 v5, v5, v132, v133\n\t"
      "v_max3_u32 v5, v5, v134, v135\n\tv_max3_u32 v5, v5, v136, v137\n\tv_max3_u32 v5, v5, v138, v139\n\tv_max3_u32 v5, v5, v140, v141\n\tv_max3_u32 v5, v5, v142, v143\n\t"
      "v_max3_u32 v5, v5, v144, v145\n\tv_max3_u32 v5, v5, v146, v147\n\tv_max3_u32 v5, v5, v148, v149\n\tv_max3_u32 v5, v5, v150, v151\n\tv_max3_u32 v5, v5, v152, v153\n\t"
      "v_max3_u32 v5, v5, v154, v155\n\tv_max3_u32 v5, v5, v156, v157\n\tv_max3_u32 v5, v5, v158, v159\n\tv_max3_u32 v5, v5, v160, v161\n\tv_max3_u32 v5, v5, v162, v163\n\t"
      "v_max3_u32 v5, v5, v164, v165\n\tv_max3_u32 v5, v5, v166, v167\n\tv_max3_u32 v5, v5, v168, v169\n\tv_max3_u32 v5, v5, v170, v171\n\tv_max3_u32 v5, v5, v172, v173\n\t"
      "v_max3_u32 v5, v5, v174, v175\n\tv_max3_u32 v5, v5, v176, v177\n\tv_max3_u32 v5, v5, v178, v179\n\tv_max3_u32 v5, v5, v180, v181\n\tv_max3_u32 v5, v5, v182, v183\n\t"
      "v_max3_u32 v5, v5, v184, v185\n\tv_max3_u32 v5, v5, v186, v187\n\tv_max3_u32 v5, v5, v188, v189\n\tv_max3_u32 v5, v5, v190, v191\n\tv_max3_u32 v5, v5, v192, v193\n\t"
      "v_max3_u32 v5, v5, v194, v195\n\tv_max3_u32 v5, v5, v196, v197\n\tv_max3_u32 v5, v5, v198, v199\n\tv_max3_u32 v5, v5, v200, v201\n\tv_max3_u32 v5, v5, v202, v203\n\t"
      "v_max3_u32 v5, v5, v204, v205\n\tv_max3_u32 v5, v5, v206, v207\n\tv_max3_u32 v5, v5, v208, v209\n\tv_max3_u32 v5, v5, v210, v211\n\tv_max3_u32 v5, v5, v212, v213\n\t"
      "v_max3_u32 v5, v5, v214, v215\n\tv_max3_u32 v5, v5, v216, v217\n\tv_max3_u32 v5, v5, v218, v219\n\tv_max3_u32 v5, v5, v220, v221\n\tv_max3_u32 v5, v5, v222, v223\n\t"
      "v_cmp_eq_u32 vcc, -1, v5\n\ts_cbranch_vccz 51b\n\ts_cmp_eq_u32 s18, 0\n\ts_cbranch_scc1 51b\n\ts_sub_u32 s18, s18, 1\n\ts_branch 50b\n\t"
      "40:\n\ts_waitcnt vmcnt(0) lgkmcnt(0)\n\t"
      TKS("s_mul_i32 s48, s16, 72\n\ts_add_u32 s48, s48, 162080\n\tv_mov_b32 v224, s48\n\tds_read_b32 v225, v224 offset:0\n\ts_waitcnt lgkmcnt(0)\n\tv_readfirstlane_b32 s74, v225\n\tds_read_b32 v225, v224 offset:4\n\ts_waitcnt lgkmcnt(0)\n\tv_readfirstlane_b32 s75, v225\n\tds_read_b32 v225, v224 offset:8\n\ts_waitcnt lgkmcnt(0)\n\tv_readfirstlane_b32 s76, v225\n\tds_read_b32 v225, v224 offset:12\n\ts_waitcnt lgkmcnt(0)\n\tv_readfirstlane_b32 s77, v225\n\tds_read_b32 v225, v224 offset:16\n\ts_waitcnt lgkmcnt(0)\n\tv_readfirstlane_b32 s78, v225\n\tds_read_b32 v225, v224 offset:20\n\ts_waitcnt lgkmcnt(0)\n\tv_readfirstlane_b32 s79, v225\n\tds_read_b32 v225, v224 offset:24\n\ts_waitcnt lgkmcnt(0)\n\tv_readfirstlane_b32 s80, v225\n\tds_read_b32 v225, v224 offset:28\n\ts_waitcnt lgkmcnt(0)\n\tv_readfirstlane_b32 s81, v225\n\tds_read_b32 v225, v224 offset:32\n\ts_waitcnt lgkmcnt(0)\n\tv_readfirstlane_b32 s82, v225\n\tds_read_b32 v225, v224 offset:36\n\ts_waitcnt lgkmcnt(0)\n\tv_readfirstlane_b32 s83, v225\n\tds_read_b32 v225, v224 offset:40\n\ts_waitcnt lgkmcnt(0)\n\tv_readfirstlane_b32 s84, v225\n\tds_read_b32 v225, v224 offset:44\n\ts_waitcnt lgkmcnt(0)\n\tv_readfirstlane_b32 s85, v225\n\tds_read_b32 v225, v224 offset:48\n\ts_waitcnt lgkmcnt(0)\n\tv_readfirstlane_b32 s86, v225\n\tds_read_b32 v225, v224 offset:52\n\ts_waitcnt lgkmcnt(0)\n\tv_readfirstlane_b32 s87, v225\n\tds_read_b32 v225, v224 offset:56\n\ts_waitcnt lgkmcnt(0)\n\tv_readfirstlane_b32 s88, v225\n\tds_read_b32 v225, v224 offset:60\n\ts_waitcnt lgkmcnt(0)\n\tv_readfirstlane_b32 s89, v225\n\tds_read_b32 v225, v224 offset:64\n\ts_waitcnt lgkmcnt(0)\n\tv_readfirstlane_b32 s90, v225\n\tds_read_b32 v225, v224 offset:68\n\ts_waitcnt lgkmcnt(0)\n\tv_readfirstlane_b32 s91, v225\n\t")
      STS("s_lshl_b32 s48, s16, 2\n\ts_add_u32 s48, s48, 162944\n\tv_mov_b32 v0, s48\n\tv_mov_b32 v1, s50\n\tds_write_b32 v0, v1\n\ts_cmp_lg_u32 s16, 0\n\ts_cbranch_scc1 97f\n\tv_mov_b32 v1, s51\n\tds_write_b32 v0, v1 offset:16\n\t97:\n\ts_cmp_lg_u32 s16, -1\n\ts_cbranch_scc1 95f\n\tv_mov_b32 v1, s61\n\tds_write_b32 v0, v1 offset:24\n\tv_mov_b32 v1, s21\n\tds_write_b32 v0, v1 offset:28\n\tv_mov_b32 v1, s22\n\tds_write_b32 v0, v1 offset:32\n\t95:\n\t")
      "s_cmp_lg_u32 s18, 0\n\ts_cbranch_scc1 96f\n\tv_mov_b32 v1, 1\n\tbuffer_store_dword v1, off, s[12:15], 0 offset:3328 sc1\n\t"
      "96:\n\t"
      : TOK_REGS :: "memory", "vcc", "scc", S_CLOB TK_CLOB);
  } else {
    unsigned seenZ = 0;
    gu32* f = c.ctrl + (C_FLAG + (F_Z + (y >> 4) * 32 + (lane & 31)) * 128) / 4;
    for (int n = 0; n < NSEG; ++n) {
      const int s = n; (void)s;
      if (n >= 4) { lds_wait(TDONE, n - 3); lds_wait(TDONE + 1, n - 3); }
      if (n >= 2) lds_wait(PUB, n - 1);
      const unsigned t0 = (unsigned)__builtin_amdgcn_s_memtime();
#pragma unroll 1
      while (seenZ < (unsigned)n + 1u) {
        unsigned v = __hip_atomic_load(f, RLX_AGENT);
#pragma unroll
        for (int o = 1; o < 32; o <<= 1) v = min(v, (unsigned)__shfl_xor((int)v, o));
        seenZ = __builtin_amdgcn_readfirstlane(v);
        if (seenZ >= (unsigned)n + 1u || spend(c)) break;
        __builtin_amdgcn_s_sleep(2);
      }
      if (n >= 2) {
#pragma unroll 1
        while ((unsigned)__builtin_amdgcn_s_memtime() - t0 < ZDLY * 64u) {}
      }
      unsigned so;
      asm volatile(
      "s_add_u32 m0, %[m], 0\n\ts_add_u32 %[so], %[zb], 0\n\tbuffer_load_dwordx4 %[o], %[rs], %[so] offen offset:0 sc1 lds\n\ts_nop 4\n\t"
      "buffer_load_dwordx4 %[o], %[rs], %[so] offen offset:1024 sc1 lds\n\ts_nop 4\n\tbuffer_load_dwordx4 %[o], %[rs], %[so] offen offset:2048 sc1 lds\n\ts_nop 4\n\t"
      "buffer_load_dwordx4 %[o], %[rs], %[so] offen offset:3072 sc1 lds\n\ts_nop 4\n\ts_add_u32 m0, %[m], 4096\n\ts_add_u32 %[so], %[zb], 4096\n\t"
      "buffer_load_dwordx4 %[o], %[rs], %[so] offen offset:0 sc1 lds\n\ts_nop 4\n\tbuffer_load_dwordx4 %[o], %[rs], %[so] offen offset:1024 sc1 lds\n\ts_nop 4\n\t"
      "buffer_load_dwordx4 %[o], %[rs], %[so] offen offset:2048 sc1 lds\n\ts_nop 4\n\tbuffer_load_dwordx4 %[o], %[rs], %[so] offen offset:3072 sc1 lds\n\ts_nop 4\n\t"
      "s_add_u32 m0, %[m], 8192\n\ts_add_u32 %[so], %[zb], 16384\n\tbuffer_load_dwordx4 %[o], %[rs], %[so] offen offset:0 sc1 lds\n\ts_nop 4\n\t"
      "buffer_load_dwordx4 %[o], %[rs], %[so] offen offset:1024 sc1 lds\n\ts_nop 4\n\tbuffer_load_dwordx4 %[o], %[rs], %[so] offen offset:2048 sc1 lds\n\ts_nop 4\n\t"
      "buffer_load_dwordx4 %[o], %[rs], %[so] offen offset:3072 sc1 lds\n\ts_nop 4\n\ts_add_u32 m0, %[m], 12288\n\ts_add_u32 %[so], %[zb], 20480\n\t"
      "buffer_load_dwordx4 %[o], %[rs], %[so] offen offset:0 sc1 lds\n\ts_nop 4\n\tbuffer_load_dwordx4 %[o], %[rs], %[so] offen offset:1024 sc1 lds\n\ts_nop 4\n\t"
      "buffer_load_dwordx4 %[o], %[rs], %[so] offen offset:2048 sc1 lds\n\ts_nop 4\n\tbuffer_load_dwordx4 %[o], %[rs], %[so] offen offset:3072 sc1 lds\n\ts_nop 4\n\t"
      "s_add_u32 m0, %[m], 16384\n\ts_add_u32 %[so], %[zb], 32768\n\tbuffer_load_dwordx4 %[o], %[rs], %[so] offen offset:0 sc1 lds\n\ts_nop 4\n\t"
      "buffer_load_dwordx4 %[o], %[rs], %[so] offen offset:1024 sc1 lds\n\ts_nop 4\n\tbuffer_load_dwordx4 %[o], %[rs], %[so] offen offset:2048 sc1 lds\n\ts_nop 4\n\t"
      "buffer_load_dwordx4 %[o], %[rs], %[so] offen offset:3072 sc1 lds\n\ts_nop 4\n\ts_add_u32 m0, %[m], 20480\n\ts_add_u32 %[so], %[zb], 36864\n\t"
      "buffer_load_dwordx4 %[o], %[rs], %[so] offen offset:0 sc1 lds\n\ts_nop 4\n\tbuffer_load_dwordx4 %[o], %[rs], %[so] offen offset:1024 sc1 lds\n\ts_nop 4\n\t"
      "buffer_load_dwordx4 %[o], %[rs], %[so] offen offset:2048 sc1 lds\n\ts_nop 4\n\tbuffer_load_dwordx4 %[o], %[rs], %[so] offen offset:3072 sc1 lds\n\ts_nop 4\n\t"
      "s_add_u32 m0, %[m], 24576\n\ts_add_u32 %[so], %[zb], 49152\n\tbuffer_load_dwordx4 %[o], %[rs], %[so] offen offset:0 sc1 lds\n\ts_nop 4\n\t"
      "buffer_load_dwordx4 %[o], %[rs], %[so] offen offset:1024 sc1 lds\n\ts_nop 4\n\tbuffer_load_dwordx4 %[o], %[rs], %[so] offen offset:2048 sc1 lds\n\ts_nop 4\n\t"
      "buffer_load_dwordx4 %[o], %[rs], %[so] offen offset:3072 sc1 lds\n\ts_nop 4\n\ts_add_u32 m0, %[m], 28672\n\ts_add_u32 %[so], %[zb], 53248\n\t"
      "buffer_load_dwordx4 %[o], %[rs], %[so] offen offset:0 sc1 lds\n\ts_nop 4\n\tbuffer_load_dwordx4 %[o], %[rs], %[so] offen offset:1024 sc1 lds\n\ts_nop 4\n\t"
      "buffer_load_dwordx4 %[o], %[rs], %[so] offen offset:2048 sc1 lds\n\ts_nop 4\n\tbuffer_load_dwordx4 %[o], %[rs], %[so] offen offset:3072 sc1 lds\n\ts_nop 4\n\t"
        : [so] "=&s"(so) : [o] "v"(lane * 16 + ((HACK & 8) << 27)), [rs] "s"(c.rs), [m] "s"(ZR + (n & 3) * 32768), [zb] "s"(R_Z + ((n % ZRN) * 64 + y) * 65536 + ((para ^ (y >> 5)) << 13)) : "memory", "scc");
      VMWAIT(0);
      lds_post(ZFULL, n + 1);
    }
    constexpr int s = NSEG - 1;
    const int uo = y * 16384 + para * 8192 + lane * 16, ub = R_U + (s & 1) * (64 * 16384);
    lds_wait(GO, s); lds_wait(TDONE, s - 2); lds_wait(TDONE + 1, s - 2);
    v4u u[8];
#pragma unroll 1
    for (;;) {
      asm volatile("" ::: "memory");
#pragma unroll
      for (int i = 0; i < 8; ++i) u[i] = ld16s(c.rs, uo + (i & 3) * 1024, ub + (i >> 2) * 4096);
      SCHED();
      unsigned t = 0;
#pragma unroll
      for (int i = 0; i < 8; ++i) t = umax(t, top(u[i]));
      if (allok(t) || spend(c)) break;
    }
#pragma unroll
    for (int i = 0; i < 8; ++i) sts16(c.L, UL + (s % 3) * 8192 + i * 1024 + lane * 16, u[i]);
    lds_post(GO, s + 1);
  }
  TICK_OUT();
  if (c.budget == 0u) __hip_atomic_store(c.ctrl + C_TMO / 4, 1u, RLX_AGENT);
}

#define ENTRY plo((const void*)p), phi((const void*)p), c.att ? 0u : (unsigned)c.b + 256u, (unsigned)(C_VERDICT + c.att * 128)
DEV void g_role(Ctx& c, int mat, int h, int gi) {
  const KArg p = enter(c);
  c.budget = SPIN_CAP;
  const int w = c.w, lane = fresh_lane(), g = lane >> 4, h2 = lane >> 5, r32 = lane & 31, q = (lane >> 2) & 3, pp = lane & 3;
  if (mat == 0) { claim(c, 0); __builtin_amdgcn_s_sleep(64); __builtin_amdgcn_s_sleep(127); }
  v32u k;
#pragma unroll
  for (int i = 0; i < 8; ++i) {
    k[i] = r32 * 2048 + (((2 * i + h2) ^ (r32 & 15)) << 4); k[8 + i] = k[i] + 65536;
    k[16 + i] = (8 * w + i) * 2048 + ((lane ^ ((8 * w + i) & 15)) << 4);
  }
  k[24] = (4 * w + 2 * h2) * 512 + r32 * 16 + ((HACK & 4) << 28);
  k[25] = min((gi * 4 + w) * 6 + min(lane, 5), 511) * 128;
  k[26] = 0; k[27] = (lane | w) ? 1 << 30 : 0; k[28] = 0;
  k[29] = (16 * w + g) * 8192 + h * 256 + (((lane & 15) ^ ((g & 1) | (h2 << 3))) << 4);
  k[30] = 65536 + (8 * h2 + q) * 256 + (((8 * (w >> 1) + 4 * (g & 1) + 2 * (pp >> 1) + (w & 1)) ^ ((q & 1) | ((q >> 1) << 3))) << 4) + 8 * (pp & 1);
  k[31] = k[30] + 65536;
  const u16* W = mat == 0 ? p->Wq : mat == 1 ? p->Wk : p->Wv;
  asm volatile(
      "s_add_u32 m0, s50, 0\n\ts_nop 0\n\tbuffer_load_dwordx4 v16, s[44:47], 0 offen offset:0  lds\n\ts_add_u32 m0, s50, 0\n\ts_nop 0\n\t"
      "buffer_load_dwordx4 v16, s[44:47], 0 offen offset:1024  lds\n\ts_add_u32 m0, s50, 2048\n\ts_nop 0\n\tbuffer_load_dwordx4 v17, s[44:47], 0 offen offset:0  lds\n\t"
      "s_add_u32 m0, s50, 2048\n\ts_nop 0\n\tbuffer_load_dwordx4 v17, s[44:47], 0 offen offset:1024  lds\n\ts_add_u32 m0, s50, 4096\n\ts_nop 0\n\t"
      "buffer_load_dwordx4 v18, s[44:47], 0 offen offset:0  lds\n\ts_add_u32 m0, s50, 4096\n\ts_nop 0\n\tbuffer_load_dwordx4 v18, s[44:47], 0 offen offset:1024  lds\n\t"
      "s_add_u32 m0, s50, 6144\n\ts_nop 0\n\tbuffer_load_dwordx4 v19, s[44:47], 0 offen offset:0  lds\n\ts_add_u32 m0, s50, 6144\n\ts_nop 0\n\t"
      "buffer_load_dwordx4 v19, s[44:47], 0 offen offset:1024  lds\n\ts_add_u32 m0, s50, 8192\n\ts_nop 0\n\tbuffer_load_dwordx4 v20, s[44:47], 0 offen offset:0  lds\n\t"
      "s_add_u32 m0, s50, 8192\n\ts_nop 0\n\tbuffer_load_dwordx4 v20, s[44:47], 0 offen offset:1024  lds\n\ts_add_u32 m0, s50, 10240\n\ts_nop 0\n\t"
      "buffer_load_dwordx4 v21, s[44:47], 0 offen offset:0  lds\n\ts_add_u32 m0, s50, 10240\n\ts_nop 0\n\tbuffer_load_dwordx4 v21, s[44:47], 0 offen offset:1024  lds\n\t"
      "s_add_u32 m0, s50, 12288\n\ts_nop 0\n\tbuffer_load_dwordx4 v22, s[44:47], 0 offen offset:0  lds\n\ts_add_u32 m0, s50, 12288\n\ts_nop 0\n\t"
      "buffer_load_dwordx4 v22, s[44:47], 0 offen offset:1024  lds\n\ts_add_u32 m0, s50, 14336\n\ts_nop 0\n\tbuffer_load_dwordx4 v23, s[44:47], 0 offen offset:0  lds\n\t"
      "s_add_u32 m0, s50, 14336\n\ts_nop 0\n\tbuffer_load_dwordx4 v23, s[44:47], 0 offen offset:1024  lds\n\ts_add_u32 m0, s51, 0\n\ts_mov_b32 s48, 0x0\n\t"
      "buffer_load_dwordx4 v29, s[40:43], s48 offen  lds\n\ts_add_u32 m0, s51, 1024\n\ts_mov_b32 s48, 0x8000\n\tbuffer_load_dwordx4 v29, s[40:43], s48 offen  lds\n\t"
      "s_add_u32 m0, s51, 2048\n\ts_mov_b32 s48, 0x10000\n\tbuffer_load_dwordx4 v29, s[40:43], s48 offen  lds\n\ts_add_u32 m0, s51, 3072\n\ts_mov_b32 s48, 0x18000\n\t"
      "buffer_load_dwordx4 v29, s[40:43], s48 offen  lds\n\ts_add_u32 m0, s51, 16384\n\ts_mov_b32 s48, 0x80000\n\tbuffer_load_dwordx4 v29, s[40:43], s48 offen  lds\n\t"
      "s_add_u32 m0, s51, 17408\n\ts_mov_b32 s48, 0x88000\n\tbuffer_load_dwordx4 v29, s[40:43], s48 offen  lds\n\ts_add_u32 m0, s51, 18432\n\ts_mov_b32 s48, 0x90000\n\t"
      "buffer_load_dwordx4 v29, s[40:43], s48 offen  lds\n\ts_add_u32 m0, s51, 19456\n\ts_mov_b32 s48, 0x98000\n\tbuffer_load_dwordx4 v29, s[40:43], s48 offen  lds\n\t"
      "s_add_u32 m0, s51, 32768\n\ts_mov_b32 s48, 0x100000\n\tbuffer_load_dwordx4 v29, s[40:43], s48 offen  lds\n\ts_add_u32 m0, s51, 33792\n\ts_mov_b32 s48, 0x108000\n\t"
      "buffer_load_dwordx4 v29, s[40:43], s48 offen  lds\n\ts_add_u32 m0, s51, 34816\n\ts_mov_b32 s48, 0x110000\n\tbuffer_load_dwordx4 v29, s[40:43], s48 offen  lds\n\t"
      "s_add_u32 m0, s51, 35840\n\ts_mov_b32 s48, 0x118000\n\tbuffer_load_dwordx4 v29, s[40:43], s48 offen  lds\n\ts_add_u32 m0, s51, 49152\n\ts_mov_b32 s48, 0x180000\n\t"
      "buffer_load_dwordx4 v29, s[40:43], s48 offen  lds\n\ts_add_u32 m0, s51, 50176\n\ts_mov_b32 s48, 0x188000\n\tbuffer_load_dwordx4 v29, s[40:43], s48 offen  lds\n\t"
      "s_add_u32 m0, s51, 51200\n\ts_mov_b32 s48, 0x190000\n\tbuffer_load_dwordx4 v29, s[40:43], s48 offen  lds\n\ts_add_u32 m0, s51, 52224\n\ts_mov_b32 s48, 0x198000\n\t"
      "buffer_load_dwordx4 v29, s[40:43], s48 offen  lds\n\ts_add_u32 m0, s51, 65536\n\ts_mov_b32 s48, 0x200000\n\tbuffer_load_dwordx4 v29, s[40:43], s48 offen  lds\n\t"
      "s_add_u32 m0, s51, 66560\n\ts_mov_b32 s48, 0x208000\n\tbuffer_load_dwordx4 v29, s[40:43], s48 offen  lds\n\ts_add_u32 m0, s51, 67584\n\ts_mov_b32 s48, 0x210000\n\t"
      "buffer_load_dwordx4 v29, s[40:43], s48 offen  lds\n\ts_add_u32 m0, s51, 68608\n\ts_mov_b32 s48, 0x218000\n\tbuffer_load_dwordx4 v29, s[40:43], s48 offen  lds\n\t"
      :: "{v[16:23]}"(__builtin_shufflevector(k, k, 16, 17, 18, 19, 20, 21, 22, 23)), "{v29}"(k[29]), "{s[40:43]}"(mkrs(W, 1024 * 4096 * 2)), "{s[44:47]}"(mkrs(p->x, 2048 * 2048)),
         "{s50}"(w * 16384), "{s51}"(65536 + w * 4096) : "memory", "scc", "s48");
  if (mat != 0) claim(c, 0);
  v32u s0 = {RSRCS, (unsigned)w, 0, c.budget, (unsigned)((mat == 0 ? R_QX : R_KX) + (hblock(h) ^ (mat == 2 ? 8192 : 0)) + (GRN - 1) * 16384), 0, 0, (unsigned)(C_FLAG + (mat * 32 + h) * 128)};
  v32u s1 = {plo(W), phi(W), 1024 * 4096 * 2, 0x00020000, plo(p->x), phi(p->x), 2048 * 2048, 0x00020000, 0, 0, (unsigned)(w * 16384), (unsigned)(65536 + w * 4096), 0, 0, 0, 0,
             0, 0, 0, 0, ENTRY};
  v32u wa[8], wv[5];
  asm volatile(
      "s_nop 0\n\tv_readfirstlane_b32 s8, v160\n\tv_readfirstlane_b32 s9, v161\n\tv_readfirstlane_b32 s10, v162\n\tv_readfirstlane_b32 s11, v163\n\t"
      "v_readfirstlane_b32 s12, v164\n\tv_readfirstlane_b32 s13, v165\n\tv_readfirstlane_b32 s14, v166\n\tv_readfirstlane_b32 s15, v167\n\tv_readfirstlane_b32 s16, v168\n\t"
      "v_readfirstlane_b32 s17, v169\n\tv_readfirstlane_b32 s18, v170\n\tv_readfirstlane_b32 s19, v171\n\tv_readfirstlane_b32 s20, v172\n\tv_readfirstlane_b32 s21, v173\n\t"
      "v_readfirstlane_b32 s22, v174\n\tv_readfirstlane_b32 s40, v192\n\tv_readfirstlane_b32 s41, v193\n\tv_readfirstlane_b32 s42, v194\n\tv_readfirstlane_b32 s43, v195\n\t"
      "v_readfirstlane_b32 s44, v196\n\tv_readfirstlane_b32 s45, v197\n\tv_readfirstlane_b32 s46, v198\n\tv_readfirstlane_b32 s47, v199\n\tv_readfirstlane_b32 s50, v202\n\t"
      "v_readfirstlane_b32 s51, v203\n\tv_readfirstlane_b32 s60, v212\n\tv_readfirstlane_b32 s61, v213\n\tv_readfirstlane_b32 s62, v214\n\tv_readfirstlane_b32 s63, v215\n\t"
      "s_nop 4\n\t"
      TKS("s_mul_i32 s48, s16, 72\n\ts_add_u32 s48, s48, 162080\n\tv_mov_b32 v224, s48\n\tv_mov_b32 v225, s74\n\tds_write_b32 v224, v225 offset:0\n\tv_mov_b32 v225, s75\n\tds_write_b32 v224, v225 offset:4\n\tv_mov_b32 v225, s76\n\tds_write_b32 v224, v225 offset:8\n\tv_mov_b32 v225, s77\n\tds_write_b32 v224, v225 offset:12\n\tv_mov_b32 v225, s78\n\tds_write_b32 v224, v225 offset:16\n\tv_mov_b32 v225, s79\n\tds_write_b32 v224, v225 offset:20\n\tv_mov_b32 v225, s80\n\tds_write_b32 v224, v225 offset:24\n\tv_mov_b32 v225, s81\n\tds_write_b32 v224, v225 offset:28\n\tv_mov_b32 v225, s82\n\tds_write_b32 v224, v225 offset:32\n\tv_mov_b32 v225, s83\n\tds_write_b32 v224, v225 offset:36\n\tv_mov_b32 v225, s84\n\tds_write_b32 v224, v225 offset:40\n\tv_mov_b32 v225, s85\n\tds_write_b32 v224, v225 offset:44\n\tv_mov_b32 v225, s86\n\tds_write_b32 v224, v225 offset:48\n\tv_mov_b32 v225, s87\n\tds_write_b32 v224, v225 offset:52\n\tv_mov_b32 v225, s88\n\tds_write_b32 v224, v225 offset:56\n\tv_mov_b32 v225, s89\n\tds_write_b32 v224, v225 offset:60\n\tv_mov_b32 v225, s90\n\tds_write_b32 v224, v225 offset:64\n\tv_mov_b32 v225, s91\n\tds_write_b32 v224, v225 offset:68\n\t")
      "s_mov_b32 s48, 0x10000\n\tbuffer_load_dword v26, v25, s[44:47], s48 offen\n\ts_mov_b32 s48, 0x20000\n\tbuffer_load_dword v26, v25, s[44:47], s48 offen\n\t"
      "s_mov_b32 s48, 0x30000\n\tbuffer_load_dword v26, v25, s[44:47], s48 offen\n\ts_waitcnt vmcnt(16)\n\ts_barrier\n\tds_read_b64_tr_b16 a[0:1], v30 offset:0\n\t"
      "ds_read_b64_tr_b16 a[2:3], v30 offset:1024\n\tds_read_b64_tr_b16 a[4:5], v30 offset:4096\n\tds_read_b64_tr_b16 a[6:7], v30 offset:5120\n\t"
      "ds_read_b64_tr_b16 a[8:9], v30 offset:8192\n\tds_read_b64_tr_b16 a[10:11], v30 offset:9216\n\tds_read_b64_tr_b16 a[12:13], v30 offset:12288\n\t"
      "ds_read_b64_tr_b16 a[14:15], v30 offset:13312\n\ts_waitcnt lgkmcnt(0)\n\ts_barrier\n\ts_add_u32 m0, s51, 0\n\ts_mov_b32 s48, 0x280000\n\t"
      "buffer_load_dwordx4 v29, s[40:43], s48 offen  lds\n\ts_add_u32 m0, s51, 1024\n\ts_mov_b32 s48, 0x288000\n\tbuffer_load_dwordx4 v29, s[40:43], s48 offen  lds\n\t"
      "s_add_u32 m0, s51, 2048\n\ts_mov_b32 s48, 0x290000\n\tbuffer_load_dwordx4 v29, s[40:43], s48 offen  lds\n\ts_add_u32 m0, s51, 3072\n\ts_mov_b32 s48, 0x298000\n\t"
      "buffer_load_dwordx4 v29, s[40:43], s48 offen  lds\n\ts_waitcnt vmcnt(16)\n\ts_barrier\n\tds_read_b64_tr_b16 a[16:17], v30 offset:16384\n\t"
      "ds_read_b64_tr_b16 a[18:19], v30 offset:17408\n\tds_read_b64_tr_b16 a[20:21], v30 offset:20480\n\tds_read_b64_tr_b16 a[22:23], v30 offset:21504\n\t"
      "ds_read_b64_tr_b16 a[24:25], v30 offset:24576\n\tds_read_b64_tr_b16 a[26:27], v30 offset:25600\n\tds_read_b64_tr_b16 a[28:29], v30 offset:28672\n\t"
      "ds_read_b64_tr_b16 a[30:31], v30 offset:29696\n\ts_waitcnt lgkmcnt(0)\n\ts_barrier\n\ts_add_u32 m0, s51, 16384\n\ts_mov_b32 s48, 0x300000\n\t"
      "buffer_load_dwordx4 v29, s[40:43], s48 offen  lds\n\ts_add_u32 m0, s51, 17408\n\ts_mov_b32 s48, 0x308000\n\tbuffer_load_dwordx4 v29, s[40:43], s48 offen  lds\n\t"
      "s_add_u32 m0, s51, 18432\n\ts_mov_b32 s48, 0x310000\n\tbuffer_load_dwordx4 v29, s[40:43], s48 offen  lds\n\ts_add_u32 m0, s51, 19456\n\ts_mov_b32 s48, 0x318000\n\t"
      "buffer_load_dwordx4 v29, s[40:43], s48 offen  lds\n\ts_waitcnt vmcnt(16)\n\ts_barrier\n\tds_read_b64_tr_b16 a[32:33], v30 offset:32768\n\t"
      "ds_read_b64_tr_b16 a[34:35], v30 offset:33792\n\tds_read_b64_tr_b16 a[36:37], v30 offset:36864\n\tds_read_b64_tr_b16 a[38:39], v30 offset:37888\n\t"
      "ds_read_b64_tr_b16 a[40:41], v30 offset:40960\n\tds_read_b64_tr_b16 a[42:43], v30 offset:41984\n\tds_read_b64_tr_b16 a[44:45], v30 offset:45056\n\t"
      "ds_read_b64_tr_b16 a[46:47], v30 offset:46080\n\ts_waitcnt lgkmcnt(0)\n\ts_barrier\n\ts_add_u32 m0, s51, 32768\n\ts_mov_b32 s48, 0x380000\n\t"
      "buffer_load_dwordx4 v29, s[40:43], s48 offen  lds\n\ts_add_u32 m0, s51, 33792\n\ts_mov_b32 s48, 0x388000\n\tbuffer_load_dwordx4 v29, s[40:43], s48 offen  lds\n\t"
      "s_add_u32 m0, s51, 34816\n\ts_mov_b32 s48, 0x390000\n\tbuffer_load_dwordx4 v29, s[40:43], s48 offen  lds\n\ts_add_u32 m0, s51, 35840\n\ts_mov_b32 s48, 0x398000\n\t"
      "buffer_load_dwordx4 v29, s[40:43], s48 offen  lds\n\ts_waitcnt vmcnt(16)\n\ts_barrier\n\tds_read_b64_tr_b16 a[48:49], v30 offset:49152\n\t"
      "ds_read_b64_tr_b16 a[50:51], v30 offset:50176\n\tds_read_b64_tr_b16 a[52:53], v30 offset:53248\n\tds_read_b64_tr_b16 a[54:55], v30 offset:54272\n\t"
      "ds_read_b64_tr_b16 a[56:57], v30 offset:57344\n\tds_read_b64_tr_b16 a[58:59], v30 offset:58368\n\tds_read_b64_tr_b16 a[60:61], v30 offset:61440\n\t"
      "ds_read_b64_tr_b16 a[62:63], v30 offset:62464\n\ts_waitcnt lgkmcnt(0)\n\ts_barrier\n\ts_add_u32 m0, s51, 49152\n\ts_mov_b32 s48, 0x400000\n\t"
      "buffer_load_dwordx4 v29, s[40:43], s48 offen  lds\n\ts_add_u32 m0, s51, 50176\n\ts_mov_b32 s48, 0x408000\n\tbuffer_load_dwordx4 v29, s[40:43], s48 offen  lds\n\t"
      "s_add_u32 m0, s51, 51200\n\ts_mov_b32 s48, 0x410000\n\tbuffer_load_dwordx4 v29, s[40:43], s48 offen  lds\n\ts_add_u32 m0, s51, 52224\n\ts_mov_b32 s48, 0x418000\n\t"
      "buffer_load_dwordx4 v29, s[40:43], s48 offen  lds\n\ts_waitcnt vmcnt(16)\n\ts_barrier\n\tds_read_b64_tr_b16 a[64:65], v31 offset:0\n\t"
      "ds_read_b64_tr_b16 a[66:67], v31 offset:1024\n\tds_read_b64_tr_b16 a[68:69], v31 offset:4096\n\tds_read_b64_tr_b16 a[70:71], v31 offset:5120\n\t"
      "ds_read_b64_tr_b16 a[72:73], v31 offset:8192\n\tds_read_b64_tr_b16 a[74:75], v31 offset:9216\n\tds_read_b64_tr_b16 a[76:77], v31 offset:12288\n\t"
      "ds_read_b64_tr_b16 a[78:79], v31 offset:13312\n\ts_waitcnt lgkmcnt(0)\n\ts_barrier\n\ts_add_u32 m0, s51, 65536\n\ts_mov_b32 s48, 0x480000\n\t"
      "buffer_load_dwordx4 v29, s[40:43], s48 offen  lds\n\ts_add_u32 m0, s51, 66560\n\ts_mov_b32 s48, 0x488000\n\tbuffer_load_dwordx4 v29, s[40:43], s48 offen  lds\n\t"
      "s_add_u32 m0, s51, 67584\n\ts_mov_b32 s48, 0x490000\n\tbuffer_load_dwordx4 v29, s[40:43], s48 offen  lds\n\ts_add_u32 m0, s51, 68608\n\ts_mov_b32 s48, 0x498000\n\t"
      "buffer_load_dwordx4 v29, s[40:43], s48 offen  lds\n\ts_waitcnt vmcnt(16)\n\ts_barrier\n\tds_read_b64_tr_b16 a[80:81], v30 offset:0\n\t"
      "ds_read_b64_tr_b16 a[82:83], v30 offset:1024\n\tds_read_b64_tr_b16 a[84:85], v30 offset:4096\n\tds_read_b64_tr_b16 a[86:87], v30 offset:5120\n\t"
      "ds_read_b64_tr_b16 a[88:89], v30 offset:8192\n\tds_read_b64_tr_b16 a[90:91], v30 offset:9216\n\tds_read_b64_tr_b16 a[92:93], v30 offset:12288\n\t"
      "ds_read_b64_tr_b16 a[94:95], v30 offset:13312\n\ts_waitcnt lgkmcnt(0)\n\ts_barrier\n\ts_add_u32 m0, s51, 0\n\ts_mov_b32 s48, 0x500000\n\t"
      "buffer_load_dwordx4 v29, s[40:43], s48 offen  lds\n\ts_add_u32 m0, s51, 1024\n\ts_mov_b32 s48, 0x508000\n\tbuffer_load_dwordx4 v29, s[40:43], s48 offen  lds\n\t"
      "s_add_u32 m0, s51, 2048\n\ts_mov_b32 s48, 0x510000\n\tbuffer_load_dwordx4 v29, s[40:43], s48 offen  lds\n\ts_add_u32 m0, s51, 3072\n\ts_mov_b32 s48, 0x518000\n\t"
      "buffer_load_dwordx4 v29, s[40:43], s48 offen  lds\n\ts_waitcnt vmcnt(16)\n\ts_barrier\n\tds_read_b64_tr_b16 a[96:97], v30 offset:16384\n\t"
      "ds_read_b64_tr_b16 a[98:99], v30 offset:17408\n\tds_read_b64_tr_b16 a[100:101], v30 offset:20480\n\tds_read_b64_tr_b16 a[102:103], v30 offset:21504\n\t"
      "ds_read_b64_tr_b16 a[104:105], v30 offset:24576\n\tds_read_b64_tr_b16 a[106:107], v30 offset:25600\n\tds_read_b64_tr_b16 a[108:109], v30 offset:28672\n\t"
      "ds_read_b64_tr_b16 a[110:111], v30 offset:29696\n\ts_waitcnt lgkmcnt(0)\n\ts_barrier\n\ts_add_u32 m0, s51, 16384\n\ts_mov_b32 s48, 0x580000\n\t"
      "buffer_load_dwordx4 v29, s[40:43], s48 offen  lds\n\ts_add_u32 m0, s51, 17408\n\ts_mov_b32 s48, 0x588000\n\tbuffer_load_dwordx4 v29, s[40:43], s48 offen  lds\n\t"
      "s_add_u32 m0, s51, 18432\n\ts_mov_b32 s48, 0x590000\n\tbuffer_load_dwordx4 v29, s[40:43], s48 offen  lds\n\ts_add_u32 m0, s51, 19456\n\ts_mov_b32 s48, 0x598000\n\t"
      "buffer_load_dwordx4 v29, s[40:43], s48 offen  lds\n\ts_waitcnt vmcnt(16)\n\ts_barrier\n\tds_read_b64_tr_b16 a[112:113], v30 offset:32768\n\t"
      "ds_read_b64_tr_b16 a[114:115], v30 offset:33792\n\tds_read_b64_tr_b16 a[116:117], v30 offset:36864\n\tds_read_b64_tr_b16 a[118:119], v30 offset:37888\n\t"
      "ds_read_b64_tr_b16 a[120:121], v30 offset:40960\n\tds_read_b64_tr_b16 a[122:123], v30 offset:41984\n\tds_read_b64_tr_b16 a[124:125], v30 offset:45056\n\t"
      "ds_read_b64_tr_b16 a[126:127], v30 offset:46080\n\ts_waitcnt lgkmcnt(0)\n\ts_barrier\n\ts_add_u32 m0, s51, 32768\n\ts_mov_b32 s48, 0x600000\n\t"
      "buffer_load_dwordx4 v29, s[40:43], s48 offen  lds\n\ts_add_u32 m0, s51, 33792\n\ts_mov_b32 s48, 0x608000\n\tbuffer_load_dwordx4 v29, s[40:43], s48 offen  lds\n\t"
      "s_add_u32 m0, s51, 34816\n\ts_mov_b32 s48, 0x610000\n\tbuffer_load_dwordx4 v29, s[40:43], s48 offen  lds\n\ts_add_u32 m0, s51, 35840\n\ts_mov_b32 s48, 0x618000\n\t"
      "buffer_load_dwordx4 v29, s[40:43], s48 offen  lds\n\ts_waitcnt vmcnt(16)\n\ts_barrier\n\tds_read_b64_tr_b16 a[128:129], v30 offset:49152\n\t"
      "ds_read_b64_tr_b16 a[130:131], v30 offset:50176\n\tds_read_b64_tr_b16 a[132:133], v30 offset:53248\n\tds_read_b64_tr_b16 a[134:135], v30 offset:54272\n\t"
      "ds_read_b64_tr_b16 a[136:137], v30 offset:57344\n\tds_read_b64_tr_b16 a[138:139], v30 offset:58368\n\tds_read_b64_tr_b16 a[140:141], v30 offset:61440\n\t"
      "ds_read_b64_tr_b16 a[142:143], v30 offset:62464\n\ts_waitcnt lgkmcnt(0)\n\ts_barrier\n\ts_add_u32 m0, s51, 49152\n\ts_mov_b32 s48, 0x680000\n\t"
      "buffer_load_dwordx4 v29, s[40:43], s48 offen  lds\n\ts_add_u32 m0, s51, 50176\n\ts_mov_b32 s48, 0x688000\n\tbuffer_load_dwordx4 v29, s[40:43], s48 offen  lds\n\t"
      "s_add_u32 m0, s51, 51200\n\ts_mov_b32 s48, 0x690000\n\tbuffer_load_dwordx4 v29, s[40:43], s48 offen  lds\n\ts_add_u32 m0, s51, 52224\n\ts_mov_b32 s48, 0x698000\n\t"
      "buffer_load_dwordx4 v29, s[40:43], s48 offen  lds\n\ts_waitcnt vmcnt(16)\n\ts_barrier\n\tds_read_b64_tr_b16 a[144:145], v31 offset:0\n\t"
      "ds_read_b64_tr_b16 a[146:147], v31 offset:1024\n\tds_read_b64_tr_b16 a[148:149], v31 offset:4096\n\tds_read_b64_tr_b16 a[150:151], v31 offset:5120\n\t"
      "ds_read_b64_tr_b16 a[152:153], v31 offset:8192\n\tds_read_b64_tr_b16 a[154:155], v31 offset:9216\n\tds_read_b64_tr_b16 a[156:157], v31 offset:12288\n\t"
      "ds_read_b64_tr_b16 a[158:159], v31 offset:13312\n\ts_waitcnt lgkmcnt(0)\n\ts_barrier\n\ts_add_u32 m0, s51, 65536\n\ts_mov_b32 s48, 0x700000\n\t"
      "buffer_load_dwordx4 v29, s[40:43], s48 offen  lds\n\ts_add_u32 m0, s51, 66560\n\ts_mov_b32 s48, 0x708000\n\tbuffer_load_dwordx4 v29, s[40:43], s48 offen  lds\n\t"
      "s_add_u32 m0, s51, 67584\n\ts_mov_b32 s48, 0x710000\n\tbuffer_load_dwordx4 v29, s[40:43], s48 offen  lds\n\ts_add_u32 m0, s51, 68608\n\ts_mov_b32 s48, 0x718000\n\t"
      "buffer_load_dwordx4 v29, s[40:43], s48 offen  lds\n\ts_waitcnt vmcnt(16)\n\ts_barrier\n\tds_read_b64_tr_b16 a[160:161], v30 offset:0\n\t"
      "ds_read_b64_tr_b16 a[162:163], v30 offset:1024\n\tds_read_b64_tr_b16 a[164:165], v30 offset:4096\n\tds_read_b64_tr_b16 a[166:167], v30 offset:5120\n\t"
      "ds_read_b64_tr_b16 a[168:169], v30 offset:8192\n\tds_read_b64_tr_b16 a[170:171], v30 offset:9216\n\tds_read_b64_tr_b16 a[172:173], v30 offset:12288\n\t"
      "ds_read_b64_tr_b16 a[174:175], v30 offset:13312\n\ts_waitcnt lgkmcnt(0)\n\ts_barrier\n\ts_add_u32 m0, s51, 0\n\ts_mov_b32 s48, 0x780000\n\t"
      "buffer_load_dwordx4 v29, s[40:43], s48 offen  lds\n\ts_add_u32 m0, s51, 1024\n\ts_mov_b32 s48, 0x788000\n\tbuffer_load_dwordx4 v29, s[40:43], s48 offen  lds\n\t"
      "s_add_u32 m0, s51, 2048\n\ts_mov_b32 s48, 0x790000\n\tbuffer_load_dwordx4 v29, s[40:43], s48 offen  lds\n\ts_add_u32 m0, s51, 3072\n\ts_mov_b32 s48, 0x798000\n\t"
      "buffer_load_dwordx4 v29, s[40:43], s48 offen  lds\n\ts_waitcnt vmcnt(16)\n\ts_barrier\n\tds_read_b64_tr_b16 a[176:177], v30 offset:16384\n\t"
      "ds_read_b64_tr_b16 a[178:179], v30 offset:17408\n\tds_read_b64_tr_b16 a[180:181], v30 offset:20480\n\tds_read_b64_tr_b16 a[182:183], v30 offset:21504\n\t"
      "ds_read_b64_tr_b16 a[184:185], v30 offset:24576\n\tds_read_b64_tr_b16 a[186:187], v30 offset:25600\n\tds_read_b64_tr_b16 a[188:189], v30 offset:28672\n\t"
      "ds_read_b64_tr_b16 a[190:191], v30 offset:29696\n\ts_waitcnt lgkmcnt(0)\n\ts_waitcnt vmcnt(12)\n\ts_barrier\n\tds_read_b64_tr_b16 a[192:193], v30 offset:32768\n\t"
      "ds_read_b64_tr_b16 a[194:195], v30 offset:33792\n\tds_read_b64_tr_b16 a[196:197], v30 offset:36864\n\tds_read_b64_tr_b16 a[198:199], v30 offset:37888\n\t"
      "ds_read_b64_tr_b16 a[200:201], v30 offset:40960\n\tds_read_b64_tr_b16 a[202:203], v30 offset:41984\n\tds_read_b64_tr_b16 a[204:205], v30 offset:45056\n\t"
      "ds_read_b64_tr_b16 a[206:207], v30 offset:46080\n\ts_waitcnt lgkmcnt(0)\n\ts_waitcnt vmcnt(8)\n\ts_barrier\n\tds_read_b64_tr_b16 a[208:209], v30 offset:49152\n\t"
      "ds_read_b64_tr_b16 a[210:211], v30 offset:50176\n\tds_read_b64_tr_b16 a[212:213], v30 offset:53248\n\tds_read_b64_tr_b16 a[214:215], v30 offset:54272\n\t"
      "ds_read_b64_tr_b16 a[216:217], v30 offset:57344\n\tds_read_b64_tr_b16 a[218:219], v30 offset:58368\n\tds_read_b64_tr_b16 a[220:221], v30 offset:61440\n\t"
      "ds_read_b64_tr_b16 a[222:223], v30 offset:62464\n\ts_waitcnt lgkmcnt(0)\n\ts_waitcnt vmcnt(4)\n\ts_barrier\n\tds_read_b64_tr_b16 a[224:225], v31 offset:0\n\t"
      "ds_read_b64_tr_b16 a[226:227], v31 offset:1024\n\tds_read_b64_tr_b16 a[228:229], v31 offset:4096\n\tds_read_b64_tr_b16 a[230:231], v31 offset:5120\n\t"
      "ds_read_b64_tr_b16 a[232:233], v31 offset:8192\n\tds_read_b64_tr_b16 a[234:235], v31 offset:9216\n\tds_read_b64_tr_b16 a[236:237], v31 offset:12288\n\t"
      "ds_read_b64_tr_b16 a[238:239], v31 offset:13312\n\ts_waitcnt lgkmcnt(0)\n\ts_waitcnt vmcnt(0)\n\ts_barrier\n\tds_read_b64_tr_b16 a[240:241], v30 offset:0\n\t"
      "ds_read_b64_tr_b16 a[242:243], v30 offset:1024\n\tds_read_b64_tr_b16 a[244:245], v30 offset:4096\n\tds_read_b64_tr_b16 a[246:247], v30 offset:5120\n\t"
      "ds_read_b64_tr_b16 a[248:249], v30 offset:8192\n\tds_read_b64_tr_b16 a[250:251], v30 offset:9216\n\tds_read_b64_tr_b16 a[252:253], v30 offset:12288\n\t"
      "ds_read_b64_tr_b16 a[254:255], v30 offset:13312\n\ts_waitcnt lgkmcnt(0)\n\t"
      STS("s_memrealtime vcc\n\ts_waitcnt lgkmcnt(0)\n\tv_mov_b32 v95, vcc_lo\n\tv_mov_b32 v94, 161024\n\tds_write_b32 v94, v95\n\ts_memtime vcc\n\ts_waitcnt lgkmcnt(0)\n\tv_mov_b32 v95, vcc_lo\n\tds_write_b32 v94, v95 offset:400\n\ts_waitcnt lgkmcnt(0)\n\t")
      "s_mov_b32 s23, 0\n\tv_mov_b32 v29, 0x40000000\n\tv_mov_b32 v88, v27\n\tv_mov_b32 v27, 0x40000000\n\tds_read_b128 v[64:67], v0 offset:0\n\t"
      "ds_read_b128 v[68:71], v1 offset:0\n\tds_read_b128 v[72:75], v2 offset:0\n\t"
      "10:\n\ts_waitcnt lgkmcnt(2)\n\tv_mfma_f32_32x32x16_bf16 v[32:47], a[0:3], v[64:67], 0\n\tds_read_b128 v[76:79], v3 offset:0\n\t"
      "s_add_u32 s23, s23, 15\n\ts_cmp_gt_u32 s17, s23\n\ts_cbranch_scc1 24f\n\t"
      "14:\n\ts_waitcnt lgkmcnt(2)\n\tv_mfma_f32_32x32x16_bf16 v[32:47], a[4:7], v[68:71], v[32:47]\n\tds_read_b128 v[64:67], v4 offset:0\n\ts_barrier\n\t"
      "s_waitcnt lgkmcnt(2)\n\tv_mfma_f32_32x32x16_bf16 v[32:47], a[8:11], v[72:75], v[32:47]\n\tds_read_b128 v[68:71], v5 offset:0\n\ts_add_u32 s48, s17, 1\n\t"
      "s_and_b32 s48, s48, 63\n\ts_lshl_b32 s20, s48, 16\n\ts_waitcnt lgkmcnt(2)\n\tv_mfma_f32_32x32x16_bf16 v[32:47], a[12:15], v[76:79], v[32:47]\n\t"
      "ds_read_b128 v[72:75], v6 offset:0\n\ts_add_u32 s48, s17, 4\n\ts_min_u32 s48, s48, 63\n\ts_lshl_b32 s21, s48, 16\n\ts_waitcnt lgkmcnt(2)\n\t"
      "v_mfma_f32_32x32x16_bf16 v[32:47], a[16:19], v[64:67], v[32:47]\n\tds_read_b128 v[76:79], v7 offset:0\n\tv_cvt_pk_bf16_f32 v80, v48, v52\n\t"
      "v_cvt_pk_bf16_f32 v81, v56, v60\n\tv_cvt_pk_bf16_f32 v82, v49, v53\n\tv_cvt_pk_bf16_f32 v83, v57, v61\n\ts_waitcnt lgkmcnt(2)\n\t"
      "v_mfma_f32_32x32x16_bf16 v[32:47], a[20:23], v[68:71], v[32:47]\n\tds_read_b128 v[64:67], v0 offset:256\n\t"
      "buffer_store_dwordx4 v[80:83], v29, s[8:11], s19 offen offset:0 sc1\n\ts_waitcnt lgkmcnt(2)\n\tv_mfma_f32_32x32x16_bf16 v[32:47], a[24:27], v[72:75], v[32:47]\n\t"
      "ds_read_b128 v[68:71], v1 offset:256\n\tv_cvt_pk_bf16_f32 v84, v50, v54\n\tv_cvt_pk_bf16_f32 v85, v58, v62\n\tv_cvt_pk_bf16_f32 v86, v51, v55\n\t"
      "v_cvt_pk_bf16_f32 v87, v59, v63\n\ts_waitcnt lgkmcnt(2)\n\tv_mfma_f32_32x32x16_bf16 v[32:47], a[28:31], v[76:79], v[32:47]\n\tds_read_b128 v[72:75], v2 offset:256\n\t"
      "buffer_store_dwordx4 v[84:87], v29, s[8:11], s19 offen offset:512 sc1\n\ts_waitcnt lgkmcnt(2)\n\tv_mfma_f32_32x32x16_bf16 v[32:47], a[32:35], v[64:67], v[32:47]\n\t"
      "ds_read_b128 v[76:79], v3 offset:256\n\ts_add_u32 m0, s50, 65536\n\tv_mov_b32 v28, s17\n\ts_waitcnt lgkmcnt(2)\n\t"
      "v_mfma_f32_32x32x16_bf16 v[32:47], a[36:39], v[68:71], v[32:47]\n\tds_read_b128 v[64:67], v4 offset:256\n\tbuffer_load_dwordx4 v16, s[44:47], s20 offen offset:0  lds\n\t"
      "s_waitcnt lgkmcnt(2)\n\tv_mfma_f32_32x32x16_bf16 v[32:47], a[40:43], v[72:75], v[32:47]\n\tds_read_b128 v[68:71], v5 offset:256\n\ts_add_u32 m0, s50, 65536\n\t"
      "s_waitcnt lgkmcnt(2)\n\tv_mfma_f32_32x32x16_bf16 v[32:47], a[44:47], v[76:79], v[32:47]\n\tds_read_b128 v[72:75], v6 offset:256\n\t"
      "buffer_load_dwordx4 v16, s[44:47], s20 offen offset:1024  lds\n\ts_waitcnt lgkmcnt(2)\n\tv_mfma_f32_32x32x16_bf16 v[32:47], a[48:51], v[64:67], v[32:47]\n\t"
      "ds_read_b128 v[76:79], v7 offset:256\n\ts_add_u32 m0, s50, 67584\n\ts_waitcnt lgkmcnt(2)\n\tv_mfma_f32_32x32x16_bf16 v[32:47], a[52:55], v[68:71], v[32:47]\n\t"
      "ds_read_b128 v[64:67], v0 offset:512\n\tbuffer_load_dwordx4 v17, s[44:47], s20 offen offset:0  lds\n\ts_waitcnt lgkmcnt(2)\n\t"
      "v_mfma_f32_32x32x16_bf16 v[32:47], a[56:59], v[72:75], v[32:47]\n\tds_read_b128 v[68:71], v1 offset:512\n\ts_add_u32 m0, s50, 67584\n\ts_waitcnt lgkmcnt(2)\n\t"
      "v_mfma_f32_32x32x16_bf16 v[32:47], a[60:63], v[76:79], v[32:47]\n\tds_read_b128 v[72:75], v2 offset:512\n\t"
      "buffer_load_dwordx4 v17, s[44:47], s20 offen offset:1024  lds\n\ts_waitcnt lgkmcnt(2)\n\tv_mfma_f32_32x32x16_bf16 v[32:47], a[64:67], v[64:67], v[32:47]\n\t"
      "ds_read_b128 v[76:79], v3 offset:512\n\ts_add_u32 m0, s50, 69632\n\ts_waitcnt lgkmcnt(2)\n\tv_mfma_f32_32x32x16_bf16 v[32:47], a[68:71], v[68:71], v[32:47]\n\t"
      "ds_read_b128 v[64:67], v4 offset:512\n\tbuffer_load_dwordx4 v18, s[44:47], s20 offen offset:0  lds\n\ts_waitcnt lgkmcnt(2)\n\t"
      "v_mfma_f32_32x32x16_bf16 v[32:47], a[72:75], v[72:75], v[32:47]\n\tds_read_b128 v[68:71], v5 offset:512\n\ts_add_u32 m0, s50, 69632\n\ts_waitcnt lgkmcnt(2)\n\t"
      "v_mfma_f32_32x32x16_bf16 v[32:47], a[76:79], v[76:79], v[32:47]\n\tds_read_b128 v[72:75], v6 offset:512\n\t"
      "buffer_load_dwordx4 v18, s[44:47], s20 offen offset:1024  lds\n\ts_waitcnt lgkmcnt(2)\n\tv_mfma_f32_32x32x16_bf16 v[32:47], a[80:83], v[64:67], v[32:47]\n\t"
      "ds_read_b128 v[76:79], v7 offset:512\n\ts_add_u32 m0, s50, 71680\n\ts_waitcnt lgkmcnt(2)\n\tv_mfma_f32_32x32x16_bf16 v[32:47], a[84:87], v[68:71], v[32:47]\n\t"
      "ds_read_b128 v[64:67], v0 offset:768\n\tbuffer_load_dwordx4 v19, s[44:47], s20 offen offset:0  lds\n\ts_waitcnt lgkmcnt(2)\n\t"
      "v_mfma_f32_32x32x16_bf16 v[32:47], a[88:91], v[72:75], v[32:47]\n\tds_read_b128 v[68:71], v1 offset:768\n\ts_add_u32 m0, s50, 71680\n\ts_waitcnt lgkmcnt(2)\n\t"
      "v_mfma_f32_32x32x16_bf16 v[32:47], a[92:95], v[76:79], v[32:47]\n\tds_read_b128 v[72:75], v2 offset:768\n\t"
      "buffer_load_dwordx4 v19, s[44:47], s20 offen offset:1024  lds\n\ts_waitcnt lgkmcnt(2)\n\tv_mfma_f32_32x32x16_bf16 v[32:47], a[96:99], v[64:67], v[32:47]\n\t"
      "ds_read_b128 v[76:79], v3 offset:768\n\ts_add_u32 m0, s50, 73728\n\ts_waitcnt lgkmcnt(2)\n\tv_mfma_f32_32x32x16_bf16 v[32:47], a[100:103], v[68:71], v[32:47]\n\t"
      "ds_read_b128 v[64:67], v4 offset:768\n\tbuffer_load_dwordx4 v20, s[44:47], s20 offen offset:0  lds\n\ts_waitcnt lgkmcnt(2)\n\t"
      "v_mfma_f32_32x32x16_bf16 v[32:47], a[104:107], v[72:75], v[32:47]\n\tds_read_b128 v[68:71], v5 offset:768\n\ts_add_u32 m0, s50, 73728\n\ts_waitcnt vmcnt(9)\n\t"
      "s_barrier\n\ts_waitcnt lgkmcnt(2)\n\tv_mfma_f32_32x32x16_bf16 v[32:47], a[108:111], v[76:79], v[32:47]\n\tds_read_b128 v[72:75], v6 offset:768\n\t"
      "buffer_load_dwordx4 v20, s[44:47], s20 offen offset:1024  lds\n\ts_waitcnt lgkmcnt(2)\n\tv_mfma_f32_32x32x16_bf16 v[32:47], a[112:115], v[64:67], v[32:47]\n\t"
      "ds_read_b128 v[76:79], v7 offset:768\n\ts_add_u32 m0, s50, 75776\n\tbuffer_store_dword v28, v27, s[12:15], s22 offen sc1\n\ts_waitcnt lgkmcnt(2)\n\t"
      "v_mfma_f32_32x32x16_bf16 v[32:47], a[116:119], v[68:71], v[32:47]\n\tds_read_b128 v[64:67], v0 offset:1024\n\t"
      "buffer_load_dwordx4 v21, s[44:47], s20 offen offset:0  lds\n\ts_waitcnt lgkmcnt(2)\n\tv_mfma_f32_32x32x16_bf16 v[32:47], a[120:123], v[72:75], v[32:47]\n\t"
      "ds_read_b128 v[68:71], v1 offset:1024\n\ts_add_u32 m0, s50, 75776\n\ts_waitcnt lgkmcnt(2)\n\tv_mfma_f32_32x32x16_bf16 v[32:47], a[124:127], v[76:79], v[32:47]\n\t"
      "ds_read_b128 v[72:75], v2 offset:1024\n\tbuffer_load_dwordx4 v21, s[44:47], s20 offen offset:1024  lds\n\ts_waitcnt lgkmcnt(2)\n\t"
      "v_mfma_f32_32x32x16_bf16 v[32:47], a[128:131], v[64:67], v[32:47]\n\tds_read_b128 v[76:79], v3 offset:1024\n\ts_add_u32 m0, s50, 77824\n\ts_waitcnt lgkmcnt(2)\n\t"
      "v_mfma_f32_32x32x16_bf16 v[32:47], a[132:135], v[68:71], v[32:47]\n\tds_read_b128 v[64:67], v4 offset:1024\n\t"
      "buffer_load_dwordx4 v22, s[44:47], s20 offen offset:0  lds\n\ts_waitcnt lgkmcnt(2)\n\tv_mfma_f32_32x32x16_bf16 v[32:47], a[136:139], v[72:75], v[32:47]\n\t"
      "ds_read_b128 v[68:71], v5 offset:1024\n\ts_add_u32 m0, s50, 77824\n\ts_waitcnt lgkmcnt(2)\n\tv_mfma_f32_32x32x16_bf16 v[32:47], a[140:143], v[76:79], v[32:47]\n\t"
      "ds_read_b128 v[72:75], v6 offset:1024\n\tbuffer_load_dwordx4 v22, s[44:47], s20 offen offset:1024  lds\n\ts_waitcnt lgkmcnt(2)\n\t"
      "v_mfma_f32_32x32x16_bf16 v[32:47], a[144:147], v[64:67], v[32:47]\n\tds_read_b128 v[76:79], v7 offset:1024\n\ts_add_u32 m0, s50, 79872\n\ts_waitcnt lgkmcnt(2)\n\t"
      "v_mfma_f32_32x32x16_bf16 v[32:47], a[148:151], v[68:71], v[32:47]\n\tds_read_b128 v[64:67], v0 offset:1280\n\t"
      "buffer_load_dwordx4 v23, s[44:47], s20 offen offset:0  lds\n\ts_waitcnt lgkmcnt(2)\n\tv_mfma_f32_32x32x16_bf16 v[32:47], a[152:155], v[72:75], v[32:47]\n\t"
      "ds_read_b128 v[68:71], v1 offset:1280\n\ts_add_u32 m0, s50, 79872\n\ts_waitcnt lgkmcnt(2)\n\tv_mfma_f32_32x32x16_bf16 v[32:47], a[156:159], v[76:79], v[32:47]\n\t"
      "ds_read_b128 v[72:75], v2 offset:1280\n\tbuffer_load_dwordx4 v23, s[44:47], s20 offen offset:1024  lds\n\ts_waitcnt lgkmcnt(2)\n\t"
      "v_mfma_f32_32x32x16_bf16 v[32:47], a[160:163], v[64:67], v[32:47]\n\tds_read_b128 v[76:79], v3 offset:1280\n\tbuffer_load_dword v89, off, s[12:15], 0 offset:3840 sc1\n\t"
      "s_waitcnt lgkmcnt(2)\n\tv_mfma_f32_32x32x16_bf16 v[32:47], a[164:167], v[68:71], v[32:47]\n\tds_read_b128 v[64:67], v4 offset:1280\n\t"
      "buffer_load_dword v26, v25, s[44:47], s21 offen \n\ts_waitcnt lgkmcnt(2)\n\tv_mfma_f32_32x32x16_bf16 v[32:47], a[168:171], v[72:75], v[32:47]\n\t"
      "ds_read_b128 v[68:71], v5 offset:1280\n\ts_add_u32 s48, s19, 0x4000\n\ts_and_b32 s48, s48, 0x3c000\n\ts_andn2_b32 s19, s19, 0x3c000\n\ts_or_b32 s19, s19, s48\n\t"
      "s_waitcnt lgkmcnt(2)\n\tv_mfma_f32_32x32x16_bf16 v[32:47], a[172:175], v[76:79], v[32:47]\n\tds_read_b128 v[72:75], v6 offset:1280\n\ts_waitcnt lgkmcnt(2)\n\t"
      "v_mfma_f32_32x32x16_bf16 v[32:47], a[176:179], v[64:67], v[32:47]\n\tds_read_b128 v[76:79], v7 offset:1280\n\ts_waitcnt lgkmcnt(2)\n\t"
      "v_mfma_f32_32x32x16_bf16 v[32:47], a[180:183], v[68:71], v[32:47]\n\tds_read_b128 v[64:67], v0 offset:1536\n\ts_waitcnt lgkmcnt(2)\n\t"
      "v_mfma_f32_32x32x16_bf16 v[32:47], a[184:187], v[72:75], v[32:47]\n\tds_read_b128 v[68:71], v1 offset:1536\n\ts_waitcnt lgkmcnt(2)\n\t"
      "v_mfma_f32_32x32x16_bf16 v[32:47], a[188:191], v[76:79], v[32:47]\n\tds_read_b128 v[72:75], v2 offset:1536\n\ts_waitcnt lgkmcnt(2)\n\t"
      "v_mfma_f32_32x32x16_bf16 v[32:47], a[192:195], v[64:67], v[32:47]\n\tds_read_b128 v[76:79], v3 offset:1536\n\ts_waitcnt lgkmcnt(2)\n\t"
      "v_mfma_f32_32x32x16_bf16 v[32:47], a[196:199], v[68:71], v[32:47]\n\tds_read_b128 v[64:67], v4 offset:1536\n\ts_waitcnt lgkmcnt(2)\n\t"
      "v_mfma_f32_32x32x16_bf16 v[32:47], a[200:203], v[72:75], v[32:47]\n\tds_read_b128 v[68:71], v5 offset:1536\n\ts_waitcnt lgkmcnt(2)\n\t"
      "v_mfma_f32_32x32x16_bf16 v[32:47], a[204:207], v[76:79], v[32:47]\n\tds_read_b128 v[72:75], v6 offset:1536\n\ts_waitcnt lgkmcnt(2)\n\t"
      "v_mfma_f32_32x32x16_bf16 v[32:47], a[208:211], v[64:67], v[32:47]\n\tds_read_b128 v[76:79], v7 offset:1536\n\ts_waitcnt lgkmcnt(2)\n\t"
      "v_mfma_f32_32x32x16_bf16 v[32:47], a[212:215], v[68:71], v[32:47]\n\tds_read_b128 v[64:67], v0 offset:1792\n\ts_waitcnt lgkmcnt(2)\n\t"
      "v_mfma_f32_32x32x16_bf16 v[32:47], a[216:219], v[72:75], v[32:47]\n\tds_read_b128 v[68:71], v1 offset:1792\n\ts_waitcnt lgkmcnt(2)\n\t"
      "v_mfma_f32_32x32x16_bf16 v[32:47], a[220:223], v[76:79], v[32:47]\n\tds_read_b128 v[72:75], v2 offset:1792\n\ts_waitcnt lgkmcnt(2)\n\t"
      "v_mfma_f32_32x32x16_bf16 v[32:47], a[224:227], v[64:67], v[32:47]\n\tds_read_b128 v[76:79], v3 offset:1792\n\ts_waitcnt lgkmcnt(2)\n\t"
      "v_mfma_f32_32x32x16_bf16 v[32:47], a[228:231], v[68:71], v[32:47]\n\tds_read_b128 v[64:67], v4 offset:1792\n\ts_waitcnt lgkmcnt(2)\n\t"
      "v_mfma_f32_32x32x16_bf16 v[32:47], a[232:235], v[72:75], v[32:47]\n\tds_read_b128 v[68:71], v5 offset:1792\n\ts_waitcnt lgkmcnt(2)\n\t"
      "v_mfma_f32_32x32x16_bf16 v[32:47], a[236:239], v[76:79], v[32:47]\n\tds_read_b128 v[72:75], v6 offset:1792\n\ts_waitcnt lgkmcnt(2)\n\t"
      "v_mfma_f32_32x32x16_bf16 v[32:47], a[240:243], v[64:67], v[32:47]\n\tds_read_b128 v[76:79], v7 offset:1792\n\ts_waitcnt lgkmcnt(2)\n\t"
      "v_mfma_f32_32x32x16_bf16 v[32:47], a[244:247], v[68:71], v[32:47]\n\ts_waitcnt vmcnt(1)\n\ts_barrier\n\tds_read_b128 v[64:67], v8 offset:0\n\ts_waitcnt lgkmcnt(2)\n\t"
      "v_mfma_f32_32x32x16_bf16 v[32:47], a[248:251], v[72:75], v[32:47]\n\tds_read_b128 v[68:71], v9 offset:0\n\tv_readfirstlane_b32 s23, v89\n\ts_waitcnt lgkmcnt(2)\n\t"
      "v_mfma_f32_32x32x16_bf16 v[32:47], a[252:255], v[76:79], v[32:47]\n\tds_read_b128 v[72:75], v10 offset:0\n\t"
      STF("s_memrealtime vcc\n\ts_waitcnt lgkmcnt(0)\n\tv_mov_b32 v95, vcc_lo\n\ts_lshl_b32 vcc_hi, s17, 2\n\ts_add_u32 vcc_hi, vcc_hi, 161056\n\tv_mov_b32 v94, vcc_hi\n\tds_write_b32 v94, v95\n\ts_memtime vcc\n\ts_waitcnt lgkmcnt(0)\n\tv_mov_b32 v95, vcc_lo\n\tds_write_b32 v94, v95 offset:400\n\ts_waitcnt lgkmcnt(0)\n\t")
      "s_add_u32 s17, s17, 1\n\ts_cmp_eq_u32 s17, 1\n\ts_cbranch_scc1 20f\n\t"
      "11:\n\ts_waitcnt lgkmcnt(2)\n\tv_mfma_f32_32x32x16_bf16 v[48:63], a[0:3], v[64:67], 0\n\tds_read_b128 v[76:79], v11 offset:0\n\t"
      "s_add_u32 s23, s23, 15\n\ts_cmp_gt_u32 s17, s23\n\ts_cbranch_scc1 25f\n\t"
      "15:\n\ts_waitcnt lgkmcnt(2)\n\tv_mfma_f32_32x32x16_bf16 v[48:63], a[4:7], v[68:71], v[48:63]\n\tds_read_b128 v[64:67], v12 offset:0\n\ts_barrier\n\t"
      "s_waitcnt lgkmcnt(2)\n\tv_mfma_f32_32x32x16_bf16 v[48:63], a[8:11], v[72:75], v[48:63]\n\tds_read_b128 v[68:71], v13 offset:0\n\ts_add_u32 s48, s17, 1\n\t"
      "s_and_b32 s48, s48, 63\n\ts_lshl_b32 s20, s48, 16\n\ts_waitcnt lgkmcnt(2)\n\tv_mfma_f32_32x32x16_bf16 v[48:63], a[12:15], v[76:79], v[48:63]\n\t"
      "ds_read_b128 v[72:75], v14 offset:0\n\ts_add_u32 s48, s17, 4\n\ts_min_u32 s48, s48, 63\n\ts_lshl_b32 s21, s48, 16\n\ts_waitcnt lgkmcnt(2)\n\t"
      "v_mfma_f32_32x32x16_bf16 v[48:63], a[16:19], v[64:67], v[48:63]\n\tds_read_b128 v[76:79], v15 offset:0\n\tv_cvt_pk_bf16_f32 v80, v32, v36\n\t"
      "v_cvt_pk_bf16_f32 v81, v40, v44\n\tv_cvt_pk_bf16_f32 v82, v33, v37\n\tv_cvt_pk_bf16_f32 v83, v41, v45\n\ts_waitcnt lgkmcnt(2)\n\t"
      "v_mfma_f32_32x32x16_bf16 v[48:63], a[20:23], v[68:71], v[48:63]\n\tds_read_b128 v[64:67], v8 offset:256\n\t"
      "buffer_store_dwordx4 v[80:83], v29, s[8:11], s19 offen offset:0 sc1\n\ts_waitcnt lgkmcnt(2)\n\tv_mfma_f32_32x32x16_bf16 v[48:63], a[24:27], v[72:75], v[48:63]\n\t"
      "ds_read_b128 v[68:71], v9 offset:256\n\tv_cvt_pk_bf16_f32 v84, v34, v38\n\tv_cvt_pk_bf16_f32 v85, v42, v46\n\tv_cvt_pk_bf16_f32 v86, v35, v39\n\t"
      "v_cvt_pk_bf16_f32 v87, v43, v47\n\ts_waitcnt lgkmcnt(2)\n\tv_mfma_f32_32x32x16_bf16 v[48:63], a[28:31], v[76:79], v[48:63]\n\tds_read_b128 v[72:75], v10 offset:256\n\t"
      "buffer_store_dwordx4 v[84:87], v29, s[8:11], s19 offen offset:512 sc1\n\ts_waitcnt lgkmcnt(2)\n\tv_mfma_f32_32x32x16_bf16 v[48:63], a[32:35], v[64:67], v[48:63]\n\t"
      "ds_read_b128 v[76:79], v11 offset:256\n\ts_add_u32 m0, s50, 0\n\tv_mov_b32 v28, s17\n\ts_waitcnt lgkmcnt(2)\n\t"
      "v_mfma_f32_32x32x16_bf16 v[48:63], a[36:39], v[68:71], v[48:63]\n\tds_read_b128 v[64:67], v12 offset:256\n\tbuffer_load_dwordx4 v16, s[44:47], s20 offen offset:0  lds\n\t"
      "s_waitcnt lgkmcnt(2)\n\tv_mfma_f32_32x32x16_bf16 v[48:63], a[40:43], v[72:75], v[48:63]\n\tds_read_b128 v[68:71], v13 offset:256\n\ts_add_u32 m0, s50, 0\n\t"
      "s_waitcnt lgkmcnt(2)\n\tv_mfma_f32_32x32x16_bf16 v[48:63], a[44:47], v[76:79], v[48:63]\n\tds_read_b128 v[72:75], v14 offset:256\n\t"
      "buffer_load_dwordx4 v16, s[44:47], s20 offen offset:1024  lds\n\ts_waitcnt lgkmcnt(2)\n\tv_mfma_f32_32x32x16_bf16 v[48:63], a[48:51], v[64:67], v[48:63]\n\t"
      "ds_read_b128 v[76:79], v15 offset:256\n\ts_add_u32 m0, s50, 2048\n\ts_waitcnt lgkmcnt(2)\n\tv_mfma_f32_32x32x16_bf16 v[48:63], a[52:55], v[68:71], v[48:63]\n\t"
      "ds_read_b128 v[64:67], v8 offset:512\n\tbuffer_load_dwordx4 v17, s[44:47], s20 offen offset:0  lds\n\ts_waitcnt lgkmcnt(2)\n\t"
      "v_mfma_f32_32x32x16_bf16 v[48:63], a[56:59], v[72:75], v[48:63]\n\tds_read_b128 v[68:71], v9 offset:512\n\ts_add_u32 m0, s50, 2048\n\ts_waitcnt lgkmcnt(2)\n\t"
      "v_mfma_f32_32x32x16_bf16 v[48:63], a[60:63], v[76:79], v[48:63]\n\tds_read_b128 v[72:75], v10 offset:512\n\t"
      "buffer_load_dwordx4 v17, s[44:47], s20 offen offset:1024  lds\n\ts_waitcnt lgkmcnt(2)\n\tv_mfma_f32_32x32x16_bf16 v[48:63], a[64:67], v[64:67], v[48:63]\n\t"
      "ds_read_b128 v[76:79], v11 offset:512\n\ts_add_u32 m0, s50, 4096\n\ts_waitcnt lgkmcnt(2)\n\tv_mfma_f32_32x32x16_bf16 v[48:63], a[68:71], v[68:71], v[48:63]\n\t"
      "ds_read_b128 v[64:67], v12 offset:512\n\tbuffer_load_dwordx4 v18, s[44:47], s20 offen offset:0  lds\n\ts_waitcnt lgkmcnt(2)\n\t"
      "v_mfma_f32_32x32x16_bf16 v[48:63], a[72:75], v[72:75], v[48:63]\n\tds_read_b128 v[68:71], v13 offset:512\n\ts_add_u32 m0, s50, 4096\n\ts_waitcnt lgkmcnt(2)\n\t"
      "v_mfma_f32_32x32x16_bf16 v[48:63], a[76:79], v[76:79], v[48:63]\n\tds_read_b128 v[72:75], v14 offset:512\n\t"
      "buffer_load_dwordx4 v18, s[44:47], s20 offen offset:1024  lds\n\ts_waitcnt lgkmcnt(2)\n\tv_mfma_f32_32x32x16_bf16 v[48:63], a[80:83], v[64:67], v[48:63]\n\t"
      "ds_read_b128 v[76:79], v15 offset:512\n\ts_add_u32 m0, s50, 6144\n\ts_waitcnt lgkmcnt(2)\n\tv_mfma_f32_32x32x16_bf16 v[48:63], a[84:87], v[68:71], v[48:63]\n\t"
      "ds_read_b128 v[64:67], v8 offset:768\n\tbuffer_load_dwordx4 v19, s[44:47], s20 offen offset:0  lds\n\ts_waitcnt lgkmcnt(2)\n\t"
      "v_mfma_f32_32x32x16_bf16 v[48:63], a[88:91], v[72:75], v[48:63]\n\tds_read_b128 v[68:71], v9 offset:768\n\ts_add_u32 m0, s50, 6144\n\ts_waitcnt lgkmcnt(2)\n\t"
      "v_mfma_f32_32x32x16_bf16 v[48:63], a[92:95], v[76:79], v[48:63]\n\tds_read_b128 v[72:75], v10 offset:768\n\t"
      "buffer_load_dwordx4 v19, s[44:47], s20 offen offset:1024  lds\n\ts_waitcnt lgkmcnt(2)\n\tv_mfma_f32_32x32x16_bf16 v[48:63], a[96:99], v[64:67], v[48:63]\n\t"
      "ds_read_b128 v[76:79], v11 offset:768\n\ts_add_u32 m0, s50, 8192\n\ts_waitcnt lgkmcnt(2)\n\tv_mfma_f32_32x32x16_bf16 v[48:63], a[100:103], v[68:71], v[48:63]\n\t"
      "ds_read_b128 v[64:67], v12 offset:768\n\tbuffer_load_dwordx4 v20, s[44:47], s20 offen offset:0  lds\n\ts_waitcnt lgkmcnt(2)\n\t"
      "v_mfma_f32_32x32x16_bf16 v[48:63], a[104:107], v[72:75], v[48:63]\n\tds_read_b128 v[68:71], v13 offset:768\n\ts_add_u32 m0, s50, 8192\n\ts_waitcnt vmcnt(9)\n\t"
      "s_barrier\n\ts_waitcnt lgkmcnt(2)\n\tv_mfma_f32_32x32x16_bf16 v[48:63], a[108:111], v[76:79], v[48:63]\n\tds_read_b128 v[72:75], v14 offset:768\n\t"
      "buffer_load_dwordx4 v20, s[44:47], s20 offen offset:1024  lds\n\ts_waitcnt lgkmcnt(2)\n\tv_mfma_f32_32x32x16_bf16 v[48:63], a[112:115], v[64:67], v[48:63]\n\t"
      "ds_read_b128 v[76:79], v15 offset:768\n\ts_add_u32 m0, s50, 10240\n\tbuffer_store_dword v28, v27, s[12:15], s22 offen sc1\n\ts_waitcnt lgkmcnt(2)\n\t"
      "v_mfma_f32_32x32x16_bf16 v[48:63], a[116:119], v[68:71], v[48:63]\n\tds_read_b128 v[64:67], v8 offset:1024\n\t"
      "buffer_load_dwordx4 v21, s[44:47], s20 offen offset:0  lds\n\ts_waitcnt lgkmcnt(2)\n\tv_mfma_f32_32x32x16_bf16 v[48:63], a[120:123], v[72:75], v[48:63]\n\t"
      "ds_read_b128 v[68:71], v9 offset:1024\n\ts_add_u32 m0, s50, 10240\n\ts_waitcnt lgkmcnt(2)\n\tv_mfma_f32_32x32x16_bf16 v[48:63], a[124:127], v[76:79], v[48:63]\n\t"
      "ds_read_b128 v[72:75], v10 offset:1024\n\tbuffer_load_dwordx4 v21, s[44:47], s20 offen offset:1024  lds\n\ts_waitcnt lgkmcnt(2)\n\t"
      "v_mfma_f32_32x32x16_bf16 v[48:63], a[128:131], v[64:67], v[48:63]\n\tds_read_b128 v[76:79], v11 offset:1024\n\ts_add_u32 m0, s50, 12288\n\ts_waitcnt lgkmcnt(2)\n\t"
      "v_mfma_f32_32x32x16_bf16 v[48:63], a[132:135], v[68:71], v[48:63]\n\tds_read_b128 v[64:67], v12 offset:1024\n\t"
      "buffer_load_dwordx4 v22, s[44:47], s20 offen offset:0  lds\n\ts_waitcnt lgkmcnt(2)\n\tv_mfma_f32_32x32x16_bf16 v[48:63], a[136:139], v[72:75], v[48:63]\n\t"
      "ds_read_b128 v[68:71], v13 offset:1024\n\ts_add_u32 m0, s50, 12288\n\ts_waitcnt lgkmcnt(2)\n\tv_mfma_f32_32x32x16_bf16 v[48:63], a[140:143], v[76:79], v[48:63]\n\t"
      "ds_read_b128 v[72:75], v14 offset:1024\n\tbuffer_load_dwordx4 v22, s[44:47], s20 offen offset:1024  lds\n\ts_waitcnt lgkmcnt(2)\n\t"
      "v_mfma_f32_32x32x16_bf16 v[48:63], a[144:147], v[64:67], v[48:63]\n\tds_read_b128 v[76:79], v15 offset:1024\n\ts_add_u32 m0, s50, 14336\n\ts_waitcnt lgkmcnt(2)\n\t"
      "v_mfma_f32_32x32x16_bf16 v[48:63], a[148:151], v[68:71], v[48:63]\n\tds_read_b128 v[64:67], v8 offset:1280\n\t"
      "buffer_load_dwordx4 v23, s[44:47], s20 offen offset:0  lds\n\ts_waitcnt lgkmcnt(2)\n\tv_mfma_f32_32x32x16_bf16 v[48:63], a[152:155], v[72:75], v[48:63]\n\t"
      "ds_read_b128 v[68:71], v9 offset:1280\n\ts_add_u32 m0, s50, 14336\n\ts_waitcnt lgkmcnt(2)\n\tv_mfma_f32_32x32x16_bf16 v[48:63], a[156:159], v[76:79], v[48:63]\n\t"
      "ds_read_b128 v[72:75], v10 offset:1280\n\tbuffer_load_dwordx4 v23, s[44:47], s20 offen offset:1024  lds\n\ts_waitcnt lgkmcnt(2)\n\t"
      "v_mfma_f32_32x32x16_bf16 v[48:63], a[160:163], v[64:67], v[48:63]\n\tds_read_b128 v[76:79], v11 offset:1280\n\tbuffer_load_dword v89, off, s[12:15], 0 offset:3840 sc1\n\t"
      "s_waitcnt lgkmcnt(2)\n\tv_mfma_f32_32x32x16_bf16 v[48:63], a[164:167], v[68:71], v[48:63]\n\tds_read_b128 v[64:67], v12 offset:1280\n\t"
      "buffer_load_dword v26, v25, s[44:47], s21 offen \n\ts_waitcnt lgkmcnt(2)\n\tv_mfma_f32_32x32x16_bf16 v[48:63], a[168:171], v[72:75], v[48:63]\n\t"
      "ds_read_b128 v[68:71], v13 offset:1280\n\ts_add_u32 s48, s19, 0x4000\n\ts_and_b32 s48, s48, 0x3c000\n\ts_andn2_b32 s19, s19, 0x3c000\n\ts_or_b32 s19, s19, s48\n\t"
      "s_waitcnt lgkmcnt(2)\n\tv_mfma_f32_32x32x16_bf16 v[48:63], a[172:175], v[76:79], v[48:63]\n\tds_read_b128 v[72:75], v14 offset:1280\n\tv_mov_b32 v29, v24\n\t"
      "s_waitcnt lgkmcnt(2)\n\tv_mfma_f32_32x32x16_bf16 v[48:63], a[176:179], v[64:67], v[48:63]\n\tds_read_b128 v[76:79], v15 offset:1280\n\ts_waitcnt lgkmcnt(2)\n\t"
      "v_mfma_f32_32x32x16_bf16 v[48:63], a[180:183], v[68:71], v[48:63]\n\tds_read_b128 v[64:67], v8 offset:1536\n\ts_waitcnt lgkmcnt(2)\n\t"
      "v_mfma_f32_32x32x16_bf16 v[48:63], a[184:187], v[72:75], v[48:63]\n\tds_read_b128 v[68:71], v9 offset:1536\n\ts_waitcnt lgkmcnt(2)\n\t"
      "v_mfma_f32_32x32x16_bf16 v[48:63], a[188:191], v[76:79], v[48:63]\n\tds_read_b128 v[72:75], v10 offset:1536\n\ts_waitcnt lgkmcnt(2)\n\t"
      "v_mfma_f32_32x32x16_bf16 v[48:63], a[192:195], v[64:67], v[48:63]\n\tds_read_b128 v[76:79], v11 offset:1536\n\ts_waitcnt lgkmcnt(2)\n\t"
      "v_mfma_f32_32x32x16_bf16 v[48:63], a[196:199], v[68:71], v[48:63]\n\tds_read_b128 v[64:67], v12 offset:1536\n\ts_waitcnt lgkmcnt(2)\n\t"
      "v_mfma_f32_32x32x16_bf16 v[48:63], a[200:203], v[72:75], v[48:63]\n\tds_read_b128 v[68:71], v13 offset:1536\n\ts_waitcnt lgkmcnt(2)\n\t"
      "v_mfma_f32_32x32x16_bf16 v[48:63], a[204:207], v[76:79], v[48:63]\n\tds_read_b128 v[72:75], v14 offset:1536\n\ts_waitcnt lgkmcnt(2)\n\t"
      "v_mfma_f32_32x32x16_bf16 v[48:63], a[208:211], v[64:67], v[48:63]\n\tds_read_b128 v[76:79], v15 offset:1536\n\ts_waitcnt lgkmcnt(2)\n\t"
      "v_mfma_f32_32x32x16_bf16 v[48:63], a[212:215], v[68:71], v[48:63]\n\tds_read_b128 v[64:67], v8 offset:1792\n\ts_waitcnt lgkmcnt(2)\n\t"
      "v_mfma_f32_32x32x16_bf16 v[48:63], a[216:219], v[72:75], v[48:63]\n\tds_read_b128 v[68:71], v9 offset:1792\n\ts_waitcnt lgkmcnt(2)\n\t"
      "v_mfma_f32_32x32x16_bf16 v[48:63], a[220:223], v[76:79], v[48:63]\n\tds_read_b128 v[72:75], v10 offset:1792\n\ts_waitcnt lgkmcnt(2)\n\t"
      "v_mfma_f32_32x32x16_bf16 v[48:63], a[224:227], v[64:67], v[48:63]\n\tds_read_b128 v[76:79], v11 offset:1792\n\ts_waitcnt lgkmcnt(2)\n\t"
      "v_mfma_f32_32x32x16_bf16 v[48:63], a[228:231], v[68:71], v[48:63]\n\tds_read_b128 v[64:67], v12 offset:1792\n\ts_waitcnt lgkmcnt(2)\n\t"
      "v_mfma_f32_32x32x16_bf16 v[48:63], a[232:235], v[72:75], v[48:63]\n\tds_read_b128 v[68:71], v13 offset:1792\n\ts_waitcnt lgkmcnt(2)\n\t"
      "v_mfma_f32_32x32x16_bf16 v[48:63], a[236:239], v[76:79], v[48:63]\n\tds_read_b128 v[72:75], v14 offset:1792\n\ts_waitcnt lgkmcnt(2)\n\t"
      "v_mfma_f32_32x32x16_bf16 v[48:63], a[240:243], v[64:67], v[48:63]\n\tds_read_b128 v[76:79], v15 offset:1792\n\ts_waitcnt lgkmcnt(2)\n\t"
      "v_mfma_f32_32x32x16_bf16 v[48:63], a[244:247], v[68:71], v[48:63]\n\ts_waitcnt vmcnt(1)\n\ts_barrier\n\tds_read_b128 v[64:67], v0 offset:0\n\ts_waitcnt lgkmcnt(2)\n\t"
      "v_mfma_f32_32x32x16_bf16 v[48:63], a[248:251], v[72:75], v[48:63]\n\tds_read_b128 v[68:71], v1 offset:0\n\tv_readfirstlane_b32 s23, v89\n\ts_waitcnt lgkmcnt(2)\n\t"
      "v_mfma_f32_32x32x16_bf16 v[48:63], a[252:255], v[76:79], v[48:63]\n\tds_read_b128 v[72:75], v2 offset:0\n\t"
      STF("s_memrealtime vcc\n\ts_waitcnt lgkmcnt(0)\n\tv_mov_b32 v95, vcc_lo\n\ts_lshl_b32 vcc_hi, s17, 2\n\ts_add_u32 vcc_hi, vcc_hi, 161056\n\tv_mov_b32 v94, vcc_hi\n\tds_write_b32 v94, v95\n\ts_memtime vcc\n\ts_waitcnt lgkmcnt(0)\n\tv_mov_b32 v95, vcc_lo\n\tds_write_b32 v94, v95 offset:400\n\ts_waitcnt lgkmcnt(0)\n\t")
      "s_add_u32 s17, s17, 1\n\ts_cmp_lt_u32 s17, 64\n\ts_cbranch_scc1 10b\n\ts_branch 26f\n\t"
      "16:\n\ts_nop 11\n\tv_cvt_pk_bf16_f32 v80, v48, v52\n\tv_cvt_pk_bf16_f32 v81, v56, v60\n\tv_cvt_pk_bf16_f32 v82, v49, v53\n\tv_cvt_pk_bf16_f32 v83, v57, v61\n\t"
      "v_cvt_pk_bf16_f32 v84, v50, v54\n\tv_cvt_pk_bf16_f32 v85, v58, v62\n\tv_cvt_pk_bf16_f32 v86, v51, v55\n\tv_cvt_pk_bf16_f32 v87, v59, v63\n\t"
      "buffer_store_dwordx4 v[80:83], v24, s[8:11], s19 offen offset:0 sc1\n\tbuffer_store_dwordx4 v[84:87], v24, s[8:11], s19 offen offset:512 sc1\n\tv_mov_b32 v28, 64\n\t"
      "s_waitcnt vmcnt(0) lgkmcnt(0)\n\ts_barrier\n\tbuffer_store_dword v28, v27, s[12:15], s22 offen sc1\n\ts_branch 30f\n\t"
      "24:\n\tbuffer_load_dword v89, off, s[12:15], 0 offset:3840 sc1\n\ts_waitcnt vmcnt(0)\n\tv_readfirstlane_b32 s23, v89\n\ts_add_u32 s23, s23, 15\n\t"
      "s_cmp_le_u32 s17, s23\n\ts_cbranch_scc1 14b\n\ts_cmp_eq_u32 s18, 0\n\ts_cbranch_scc1 14b\n\ts_sub_u32 s18, s18, 1\n\ts_sleep 8\n\ts_branch 24b\n\t"
      "25:\n\tbuffer_load_dword v89, off, s[12:15], 0 offset:3840 sc1\n\ts_waitcnt vmcnt(0)\n\tv_readfirstlane_b32 s23, v89\n\ts_add_u32 s23, s23, 15\n\t"
      "s_cmp_le_u32 s17, s23\n\ts_cbranch_scc1 15b\n\ts_cmp_eq_u32 s18, 0\n\ts_cbranch_scc1 15b\n\ts_sub_u32 s18, s18, 1\n\ts_sleep 8\n\ts_branch 25b\n\t"
      "26:\n\tbuffer_load_dword v89, off, s[12:15], 0 offset:3840 sc1\n\ts_waitcnt vmcnt(0)\n\tv_readfirstlane_b32 s23, v89\n\ts_add_u32 s23, s23, 15\n\t"
      "s_cmp_le_u32 s17, s23\n\ts_cbranch_scc1 16b\n\ts_cmp_eq_u32 s18, 0\n\ts_cbranch_scc1 16b\n\ts_sub_u32 s18, s18, 1\n\ts_sleep 8\n\ts_branch 26b\n\t"
      "20:\n\t"
      "1:\n\tbuffer_load_dword v26, off, s[12:15], s63 sc1\n\ts_waitcnt vmcnt(0)\n\tv_readfirstlane_b32 s49, v26\n\ts_bitcmp1_b32 s49, 0\n\ts_cbranch_scc1 2f\n\t"
      "s_cmp_eq_u32 s18, 0\n\ts_cbranch_scc1 2f\n\ts_sub_u32 s18, s18, 1\n\ts_sleep 2\n\ts_branch 1b\n\t"
      "2:\n\ts_and_b32 s48, s49, 3\n\ts_cmp_lg_u32 s48, 3\n\ts_cbranch_scc1 4f\n\ts_cmp_eq_u32 s62, 0\n\ts_cbranch_scc1 4f\n\ts_waitcnt lgkmcnt(0)\n\ts_barrier\n\t"
      "s_mov_b64 s[0:1], s[60:61]\n\ts_mov_b32 s2, s62\n\ts_lshl_b32 s48, s16, 6\n\tv_mbcnt_lo_u32_b32 v0, -1, 0\n\tv_mbcnt_hi_u32_b32 v0, -1, v0\n\tv_add_u32 v0, s48, v0\n\t"
      "s_getpc_b64 s[4:5]\n\t"
      "3:\n\ts_sub_u32 s4, s4, 3b-mk_mega\n\ts_subb_u32 s5, s5, 0\n\ts_setpc_b64 s[4:5]\n\t"
      "4:\n\ts_lshl_b32 s49, s49, 11\n\ts_and_b32 s49, s49, 0x2000\n\ts_xor_b32 s19, s19, s49\n\ts_nop 11\n\tv_cvt_pk_bf16_f32 v80, v32, v36\n\t"
      "v_cvt_pk_bf16_f32 v81, v40, v44\n\tv_cvt_pk_bf16_f32 v82, v33, v37\n\tv_cvt_pk_bf16_f32 v83, v41, v45\n\tv_cvt_pk_bf16_f32 v84, v34, v38\n\t"
      "v_cvt_pk_bf16_f32 v85, v42, v46\n\tv_cvt_pk_bf16_f32 v86, v35, v39\n\tv_cvt_pk_bf16_f32 v87, v43, v47\n\t"
      "buffer_store_dwordx4 v[80:83], v24, s[8:11], s19 offen offset:0 sc1\n\tbuffer_store_dwordx4 v[84:87], v24, s[8:11], s19 offen offset:512 sc1\n\ts_mov_b32 s23, 0\n\t"
      "v_mov_b32 v27, v88\n\tv_mov_b32 v28, 1\n\ts_waitcnt vmcnt(0)\n\ts_barrier\n\tbuffer_store_dword v28, v27, s[12:15], s22 offen sc1\n\ts_branch 11b\n\t"
      "30:\n\ts_waitcnt vmcnt(0) lgkmcnt(0)\n\t"
      TKS("s_mul_i32 s48, s16, 72\n\ts_add_u32 s48, s48, 162080\n\tv_mov_b32 v224, s48\n\tds_read_b32 v225, v224 offset:0\n\ts_waitcnt lgkmcnt(0)\n\tv_readfirstlane_b32 s74, v225\n\tds_read_b32 v225, v224 offset:4\n\ts_waitcnt lgkmcnt(0)\n\tv_readfirstlane_b32 s75, v225\n\tds_read_b32 v225, v224 offset:8\n\ts_waitcnt lgkmcnt(0)\n\tv_readfirstlane_b32 s76, v225\n\tds_read_b32 v225, v224 offset:12\n\ts_waitcnt lgkmcnt(0)\n\tv_readfirstlane_b32 s77, v225\n\tds_read_b32 v225, v224 offset:16\n\ts_waitcnt lgkmcnt(0)\n\tv_readfirstlane_b32 s78, v225\n\tds_read_b32 v225, v224 offset:20\n\ts_waitcnt lgkmcnt(0)\n\tv_readfirstlane_b32 s79, v225\n\tds_read_b32 v225, v224 offset:24\n\ts_waitcnt lgkmcnt(0)\n\tv_readfirstlane_b32 s80, v225\n\tds_read_b32 v225, v224 offset:28\n\ts_waitcnt lgkmcnt(0)\n\tv_readfirstlane_b32 s81, v225\n\tds_read_b32 v225, v224 offset:32\n\ts_waitcnt lgkmcnt(0)\n\tv_readfirstlane_b32 s82, v225\n\tds_read_b32 v225, v224 offset:36\n\ts_waitcnt lgkmcnt(0)\n\tv_readfirstlane_b32 s83, v225\n\tds_read_b32 v225, v224 offset:40\n\ts_waitcnt lgkmcnt(0)\n\tv_readfirstlane_b32 s84, v225\n\tds_read_b32 v225, v224 offset:44\n\ts_waitcnt lgkmcnt(0)\n\tv_readfirstlane_b32 s85, v225\n\tds_read_b32 v225, v224 offset:48\n\ts_waitcnt lgkmcnt(0)\n\tv_readfirstlane_b32 s86, v225\n\tds_read_b32 v225, v224 offset:52\n\ts_waitcnt lgkmcnt(0)\n\tv_readfirstlane_b32 s87, v225\n\tds_read_b32 v225, v224 offset:56\n\ts_waitcnt lgkmcnt(0)\n\tv_readfirstlane_b32 s88, v225\n\tds_read_b32 v225, v224 offset:60\n\ts_waitcnt lgkmcnt(0)\n\tv_readfirstlane_b32 s89, v225\n\tds_read_b32 v225, v224 offset:64\n\ts_waitcnt lgkmcnt(0)\n\tv_readfirstlane_b32 s90, v225\n\tds_read_b32 v225, v224 offset:68\n\ts_waitcnt lgkmcnt(0)\n\tv_readfirstlane_b32 s91, v225\n\t")
      STS("s_lshl_b32 s48, s16, 2\n\ts_add_u32 s48, s48, 162944\n\tv_mov_b32 v0, s48\n\tv_mov_b32 v1, s50\n\tds_write_b32 v0, v1\n\ts_cmp_lg_u32 s16, 0\n\ts_cbranch_scc1 97f\n\tv_mov_b32 v1, s51\n\tds_write_b32 v0, v1 offset:16\n\t97:\n\ts_cmp_lg_u32 s16, 0\n\ts_cbranch_scc1 95f\n\tv_mov_b32 v1, s61\n\tds_write_b32 v0, v1 offset:20\n\tv_mov_b32 v1, s21\n\tds_write_b32 v0, v1 offset:24\n\tv_mov_b32 v1, s22\n\tds_write_b32 v0, v1 offset:28\n\t95:\n\t")
      "s_cmp_lg_u32 s18, 0\n\ts_cbranch_scc1 96f\n\tv_mov_b32 v1, 1\n\tbuffer_store_dword v1, off, s[12:15], 0 offset:3328 sc1\n\t"
      "96:\n\t"
      : "+{v[0:31]}"(k), "+{v[160:191]}"(s0), "+{v[192:223]}"(s1), ALL_REGS :: "memory", "vcc", "scc", S_CLOB TK_CLOB);
}

DEV void zc_role(Ctx& c, int h) {
  const KArg p = enter(c);
  c.budget = SPIN_CAP;
  const int w = c.w, lane = fresh_lane(), g = lane >> 4, h2 = lane >> 5, q = (lane >> 2) & 3, pp = lane & 3;
  v32u k;
#pragma unroll
  for (int i = 0; i < 32; ++i) k[i] = 0;
  k[0] = lane * 16;
  k[1] = (16 * w + (g & 1)) * 65536 + amap(h * 1024, w >> 1) + h2 * 256 + (lane & 15) * 16 + ((HACK & 2) << 29);
  k[6] = lane == 0 ? C_FLAG + (F_V + h) * 128 : C_PROG;
  k[3] = lane ? 1 << 30 : 0;
#pragma unroll
  for (int i = 0; i < 2; ++i) k[8 + i] = (h * 128 + 16 * h2) * 2048 + w * 512 + (((lane & 31) ^ ((2 * i + h2) << 2)) << 4);
#pragma unroll
  for (int i = 0; i < 4; ++i) k[10 + i] = w * 32768 + (8 * h2 + q) * 512 + (((4 * i + 2 * (g & 1) + (pp >> 1)) ^ (q << 2)) << 4) + 8 * (pp & 1);
  asm volatile(
      "s_add_u32 m0, s51, 0\n\ts_mov_b32 s48, 0\n\tbuffer_load_dwordx4 v8, s[40:43], s48 offen  lds\n\ts_add_u32 m0, s51, 1024\n\ts_mov_b32 s48, 65536\n\t"
      "buffer_load_dwordx4 v9, s[40:43], s48 offen  lds\n\ts_add_u32 m0, s51, 2048\n\ts_mov_b32 s48, 2048\n\tbuffer_load_dwordx4 v8, s[40:43], s48 offen  lds\n\t"
      "s_add_u32 m0, s51, 3072\n\ts_mov_b32 s48, 67584\n\tbuffer_load_dwordx4 v9, s[40:43], s48 offen  lds\n\ts_add_u32 m0, s51, 4096\n\ts_mov_b32 s48, 4096\n\t"
      "buffer_load_dwordx4 v8, s[40:43], s48 offen  lds\n\ts_add_u32 m0, s51, 5120\n\ts_mov_b32 s48, 69632\n\tbuffer_load_dwordx4 v9, s[40:43], s48 offen  lds\n\t"
      "s_add_u32 m0, s51, 6144\n\ts_mov_b32 s48, 6144\n\tbuffer_load_dwordx4 v8, s[40:43], s48 offen  lds\n\ts_add_u32 m0, s51, 7168\n\ts_mov_b32 s48, 71680\n\t"
      "buffer_load_dwordx4 v9, s[40:43], s48 offen  lds\n\ts_add_u32 m0, s51, 8192\n\ts_mov_b32 s48, 8192\n\tbuffer_load_dwordx4 v8, s[40:43], s48 offen  lds\n\t"
      "s_add_u32 m0, s51, 9216\n\ts_mov_b32 s48, 73728\n\tbuffer_load_dwordx4 v9, s[40:43], s48 offen  lds\n\ts_add_u32 m0, s51, 10240\n\ts_mov_b32 s48, 10240\n\t"
      "buffer_load_dwordx4 v8, s[40:43], s48 offen  lds\n\ts_add_u32 m0, s51, 11264\n\ts_mov_b32 s48, 75776\n\tbuffer_load_dwordx4 v9, s[40:43], s48 offen  lds\n\t"
      "s_add_u32 m0, s51, 12288\n\ts_mov_b32 s48, 12288\n\tbuffer_load_dwordx4 v8, s[40:43], s48 offen  lds\n\ts_add_u32 m0, s51, 13312\n\ts_mov_b32 s48, 77824\n\t"
      "buffer_load_dwordx4 v9, s[40:43], s48 offen  lds\n\ts_add_u32 m0, s51, 14336\n\ts_mov_b32 s48, 14336\n\tbuffer_load_dwordx4 v8, s[40:43], s48 offen  lds\n\t"
      "s_add_u32 m0, s51, 15360\n\ts_mov_b32 s48, 79872\n\tbuffer_load_dwordx4 v9, s[40:43], s48 offen  lds\n\ts_add_u32 m0, s51, 16384\n\ts_mov_b32 s48, 16384\n\t"
      "buffer_load_dwordx4 v8, s[40:43], s48 offen  lds\n\ts_add_u32 m0, s51, 17408\n\ts_mov_b32 s48, 81920\n\tbuffer_load_dwordx4 v9, s[40:43], s48 offen  lds\n\t"
      "s_add_u32 m0, s51, 18432\n\ts_mov_b32 s48, 18432\n\tbuffer_load_dwordx4 v8, s[40:43], s48 offen  lds\n\ts_add_u32 m0, s51, 19456\n\ts_mov_b32 s48, 83968\n\t"
      "buffer_load_dwordx4 v9, s[40:43], s48 offen  lds\n\ts_add_u32 m0, s51, 20480\n\ts_mov_b32 s48, 20480\n\tbuffer_load_dwordx4 v8, s[40:43], s48 offen  lds\n\t"
      "s_add_u32 m0, s51, 21504\n\ts_mov_b32 s48, 86016\n\tbuffer_load_dwordx4 v9, s[40:43], s48 offen  lds\n\ts_add_u32 m0, s51, 22528\n\ts_mov_b32 s48, 22528\n\t"
      "buffer_load_dwordx4 v8, s[40:43], s48 offen  lds\n\ts_add_u32 m0, s51, 23552\n\ts_mov_b32 s48, 88064\n\tbuffer_load_dwordx4 v9, s[40:43], s48 offen  lds\n\t"
      "s_add_u32 m0, s51, 24576\n\ts_mov_b32 s48, 24576\n\tbuffer_load_dwordx4 v8, s[40:43], s48 offen  lds\n\ts_add_u32 m0, s51, 25600\n\ts_mov_b32 s48, 90112\n\t"
      "buffer_load_dwordx4 v9, s[40:43], s48 offen  lds\n\ts_add_u32 m0, s51, 26624\n\ts_mov_b32 s48, 26624\n\tbuffer_load_dwordx4 v8, s[40:43], s48 offen  lds\n\t"
      "s_add_u32 m0, s51, 27648\n\ts_mov_b32 s48, 92160\n\tbuffer_load_dwordx4 v9, s[40:43], s48 offen  lds\n\ts_add_u32 m0, s51, 28672\n\ts_mov_b32 s48, 28672\n\t"
      "buffer_load_dwordx4 v8, s[40:43], s48 offen  lds\n\ts_add_u32 m0, s51, 29696\n\ts_mov_b32 s48, 94208\n\tbuffer_load_dwordx4 v9, s[40:43], s48 offen  lds\n\t"
      "s_add_u32 m0, s51, 30720\n\ts_mov_b32 s48, 30720\n\tbuffer_load_dwordx4 v8, s[40:43], s48 offen  lds\n\ts_add_u32 m0, s51, 31744\n\ts_mov_b32 s48, 96256\n\t"
      "buffer_load_dwordx4 v9, s[40:43], s48 offen  lds\n\t"
      :: "{v[8:9]}"(v2u{k[8], k[9]}), "{s[40:43]}"(mkrs(p->Wo, 1024 * 4096 * 2)), "{s51}"(w * 32768) : "memory", "scc", "s48");
  claim(c, 0);
  v32u s0 = {RSRCS, (unsigned)w, 0, c.budget, (unsigned)(R_KX + (hblock(h) ^ 8192)), R_Z, (unsigned)(C_FLAG + (F_V + h) * 128), (unsigned)(C_FLAG + (F_Z + w * 32 + h) * 128)};
  v32u s1 = {plo(p->Wo), phi(p->Wo), 1024 * 4096 * 2, 0x00020000, 0, 0, 0, 0, 0, 0, 0, (unsigned)(w * 32768), 0, 0, 0, 0, 0, 0, 0, 0, ENTRY};
  v32u wa[8], wv[5];
  asm volatile(
      "s_nop 0\n\tv_readfirstlane_b32 s8, v160\n\tv_readfirstlane_b32 s9, v161\n\tv_readfirstlane_b32 s10, v162\n\tv_readfirstlane_b32 s11, v163\n\t"
      "v_readfirstlane_b32 s12, v164\n\tv_readfirstlane_b32 s13, v165\n\tv_readfirstlane_b32 s14, v166\n\tv_readfirstlane_b32 s15, v167\n\tv_readfirstlane_b32 s16, v168\n\t"
      "v_readfirstlane_b32 s17, v169\n\tv_readfirstlane_b32 s18, v170\n\tv_readfirstlane_b32 s19, v171\n\tv_readfirstlane_b32 s20, v172\n\tv_readfirstlane_b32 s21, v173\n\t"
      "v_readfirstlane_b32 s22, v174\n\tv_readfirstlane_b32 s40, v192\n\tv_readfirstlane_b32 s41, v193\n\tv_readfirstlane_b32 s42, v194\n\tv_readfirstlane_b32 s43, v195\n\t"
      "v_readfirstlane_b32 s51, v203\n\tv_readfirstlane_b32 s60, v212\n\tv_readfirstlane_b32 s61, v213\n\tv_readfirstlane_b32 s62, v214\n\tv_readfirstlane_b32 s63, v215\n\t"
      "s_nop 4\n\t"
      TKS("s_mul_i32 s48, s16, 72\n\ts_add_u32 s48, s48, 162080\n\tv_mov_b32 v224, s48\n\tv_mov_b32 v225, s74\n\tds_write_b32 v224, v225 offset:0\n\tv_mov_b32 v225, s75\n\tds_write_b32 v224, v225 offset:4\n\tv_mov_b32 v225, s76\n\tds_write_b32 v224, v225 offset:8\n\tv_mov_b32 v225, s77\n\tds_write_b32 v224, v225 offset:12\n\tv_mov_b32 v225, s78\n\tds_write_b32 v224, v225 offset:16\n\tv_mov_b32 v225, s79\n\tds_write_b32 v224, v225 offset:20\n\tv_mov_b32 v225, s80\n\tds_write_b32 v224, v225 offset:24\n\tv_mov_b32 v225, s81\n\tds_write_b32 v224, v225 offset:28\n\tv_mov_b32 v225, s82\n\tds_write_b32 v224, v225 offset:32\n\tv_mov_b32 v225, s83\n\tds_write_b32 v224, v225 offset:36\n\tv_mov_b32 v225, s84\n\tds_write_b32 v224, v225 offset:40\n\tv_mov_b32 v225, s85\n\tds_write_b32 v224, v225 offset:44\n\tv_mov_b32 v225, s86\n\tds_write_b32 v224, v225 offset:48\n\tv_mov_b32 v225, s87\n\tds_write_b32 v224, v225 offset:52\n\tv_mov_b32 v225, s88\n\tds_write_b32 v224, v225 offset:56\n\tv_mov_b32 v225, s89\n\tds_write_b32 v224, v225 offset:60\n\tv_mov_b32 v225, s90\n\tds_write_b32 v224, v225 offset:64\n\tv_mov_b32 v225, s91\n\tds_write_b32 v224, v225 offset:68\n\t")
      "s_waitcnt vmcnt(24)\n\tds_read_b64_tr_b16 a[0:1], v10 offset:0\n\tds_read_b64_tr_b16 a[2:3], v10 offset:2048\n\tds_read_b64_tr_b16 a[32:33], v11 offset:0\n\t"
      "ds_read_b64_tr_b16 a[34:35], v11 offset:2048\n\tds_read_b64_tr_b16 a[64:65], v12 offset:0\n\tds_read_b64_tr_b16 a[66:67], v12 offset:2048\n\t"
      "ds_read_b64_tr_b16 a[96:97], v13 offset:0\n\tds_read_b64_tr_b16 a[98:99], v13 offset:2048\n\tds_read_b64_tr_b16 a[128:129], v10 offset:256\n\t"
      "ds_read_b64_tr_b16 a[130:131], v10 offset:2304\n\tds_read_b64_tr_b16 a[160:161], v11 offset:256\n\tds_read_b64_tr_b16 a[162:163], v11 offset:2304\n\t"
      "ds_read_b64_tr_b16 a[192:193], v12 offset:256\n\tds_read_b64_tr_b16 a[194:195], v12 offset:2304\n\tds_read_b64_tr_b16 a[224:225], v13 offset:256\n\t"
      "ds_read_b64_tr_b16 a[226:227], v13 offset:2304\n\ts_waitcnt lgkmcnt(0)\n\ts_add_u32 m0, s51, 0\n\ts_mov_b32 s48, 131072\n\t"
      "buffer_load_dwordx4 v8, s[40:43], s48 offen  lds\n\ts_add_u32 m0, s51, 1024\n\ts_mov_b32 s48, 196608\n\tbuffer_load_dwordx4 v9, s[40:43], s48 offen  lds\n\t"
      "s_add_u32 m0, s51, 2048\n\ts_mov_b32 s48, 133120\n\tbuffer_load_dwordx4 v8, s[40:43], s48 offen  lds\n\ts_add_u32 m0, s51, 3072\n\ts_mov_b32 s48, 198656\n\t"
      "buffer_load_dwordx4 v9, s[40:43], s48 offen  lds\n\ts_add_u32 m0, s51, 4096\n\ts_mov_b32 s48, 135168\n\tbuffer_load_dwordx4 v8, s[40:43], s48 offen  lds\n\t"
      "s_add_u32 m0, s51, 5120\n\ts_mov_b32 s48, 200704\n\tbuffer_load_dwordx4 v9, s[40:43], s48 offen  lds\n\ts_add_u32 m0, s51, 6144\n\ts_mov_b32 s48, 137216\n\t"
      "buffer_load_dwordx4 v8, s[40:43], s48 offen  lds\n\ts_add_u32 m0, s51, 7168\n\ts_mov_b32 s48, 202752\n\tbuffer_load_dwordx4 v9, s[40:43], s48 offen  lds\n\t"
      "s_waitcnt vmcnt(24)\n\tds_read_b64_tr_b16 a[4:5], v10 offset:8192\n\tds_read_b64_tr_b16 a[6:7], v10 offset:10240\n\tds_read_b64_tr_b16 a[36:37], v11 offset:8192\n\t"
      "ds_read_b64_tr_b16 a[38:39], v11 offset:10240\n\tds_read_b64_tr_b16 a[68:69], v12 offset:8192\n\tds_read_b64_tr_b16 a[70:71], v12 offset:10240\n\t"
      "ds_read_b64_tr_b16 a[100:101], v13 offset:8192\n\tds_read_b64_tr_b16 a[102:103], v13 offset:10240\n\tds_read_b64_tr_b16 a[132:133], v10 offset:8448\n\t"
      "ds_read_b64_tr_b16 a[134:135], v10 offset:10496\n\tds_read_b64_tr_b16 a[164:165], v11 offset:8448\n\tds_read_b64_tr_b16 a[166:167], v11 offset:10496\n\t"
      "ds_read_b64_tr_b16 a[196:197], v12 offset:8448\n\tds_read_b64_tr_b16 a[198:199], v12 offset:10496\n\tds_read_b64_tr_b16 a[228:229], v13 offset:8448\n\t"
      "ds_read_b64_tr_b16 a[230:231], v13 offset:10496\n\ts_waitcnt lgkmcnt(0)\n\ts_add_u32 m0, s51, 8192\n\ts_mov_b32 s48, 139264\n\t"
      "buffer_load_dwordx4 v8, s[40:43], s48 offen  lds\n\ts_add_u32 m0, s51, 9216\n\ts_mov_b32 s48, 204800\n\tbuffer_load_dwordx4 v9, s[40:43], s48 offen  lds\n\t"
      "s_add_u32 m0, s51, 10240\n\ts_mov_b32 s48, 141312\n\tbuffer_load_dwordx4 v8, s[40:43], s48 offen  lds\n\ts_add_u32 m0, s51, 11264\n\ts_mov_b32 s48, 206848\n\t"
      "buffer_load_dwordx4 v9, s[40:43], s48 offen  lds\n\ts_add_u32 m0, s51, 12288\n\ts_mov_b32 s48, 143360\n\tbuffer_load_dwordx4 v8, s[40:43], s48 offen  lds\n\t"
      "s_add_u32 m0, s51, 13312\n\ts_mov_b32 s48, 208896\n\tbuffer_load_dwordx4 v9, s[40:43], s48 offen  lds\n\ts_add_u32 m0, s51, 14336\n\ts_mov_b32 s48, 145408\n\t"
      "buffer_load_dwordx4 v8, s[40:43], s48 offen  lds\n\ts_add_u32 m0, s51, 15360\n\ts_mov_b32 s48, 210944\n\tbuffer_load_dwordx4 v9, s[40:43], s48 offen  lds\n\t"
      "s_waitcnt vmcnt(24)\n\tds_read_b64_tr_b16 a[8:9], v10 offset:16384\n\tds_read_b64_tr_b16 a[10:11], v10 offset:18432\n\tds_read_b64_tr_b16 a[40:41], v11 offset:16384\n\t"
      "ds_read_b64_tr_b16 a[42:43], v11 offset:18432\n\tds_read_b64_tr_b16 a[72:73], v12 offset:16384\n\tds_read_b64_tr_b16 a[74:75], v12 offset:18432\n\t"
      "ds_read_b64_tr_b16 a[104:105], v13 offset:16384\n\tds_read_b64_tr_b16 a[106:107], v13 offset:18432\n\tds_read_b64_tr_b16 a[136:137], v10 offset:16640\n\t"
      "ds_read_b64_tr_b16 a[138:139], v10 offset:18688\n\tds_read_b64_tr_b16 a[168:169], v11 offset:16640\n\tds_read_b64_tr_b16 a[170:171], v11 offset:18688\n\t"
      "ds_read_b64_tr_b16 a[200:201], v12 offset:16640\n\tds_read_b64_tr_b16 a[202:203], v12 offset:18688\n\tds_read_b64_tr_b16 a[232:233], v13 offset:16640\n\t"
      "ds_read_b64_tr_b16 a[234:235], v13 offset:18688\n\ts_waitcnt lgkmcnt(0)\n\ts_add_u32 m0, s51, 16384\n\ts_mov_b32 s48, 147456\n\t"
      "buffer_load_dwordx4 v8, s[40:43], s48 offen  lds\n\ts_add_u32 m0, s51, 17408\n\ts_mov_b32 s48, 212992\n\tbuffer_load_dwordx4 v9, s[40:43], s48 offen  lds\n\t"
      "s_add_u32 m0, s51, 18432\n\ts_mov_b32 s48, 149504\n\tbuffer_load_dwordx4 v8, s[40:43], s48 offen  lds\n\ts_add_u32 m0, s51, 19456\n\ts_mov_b32 s48, 215040\n\t"
      "buffer_load_dwordx4 v9, s[40:43], s48 offen  lds\n\ts_add_u32 m0, s51, 20480\n\ts_mov_b32 s48, 151552\n\tbuffer_load_dwordx4 v8, s[40:43], s48 offen  lds\n\t"
      "s_add_u32 m0, s51, 21504\n\ts_mov_b32 s48, 217088\n\tbuffer_load_dwordx4 v9, s[40:43], s48 offen  lds\n\ts_add_u32 m0, s51, 22528\n\ts_mov_b32 s48, 153600\n\t"
      "buffer_load_dwordx4 v8, s[40:43], s48 offen  lds\n\ts_add_u32 m0, s51, 23552\n\ts_mov_b32 s48, 219136\n\tbuffer_load_dwordx4 v9, s[40:43], s48 offen  lds\n\t"
      "s_waitcnt vmcnt(24)\n\tds_read_b64_tr_b16 a[12:13], v10 offset:24576\n\tds_read_b64_tr_b16 a[14:15], v10 offset:26624\n\tds_read_b64_tr_b16 a[44:45], v11 offset:24576\n\t"
      "ds_read_b64_tr_b16 a[46:47], v11 offset:26624\n\tds_read_b64_tr_b16 a[76:77], v12 offset:24576\n\tds_read_b64_tr_b16 a[78:79], v12 offset:26624\n\t"
      "ds_read_b64_tr_b16 a[108:109], v13 offset:24576\n\tds_read_b64_tr_b16 a[110:111], v13 offset:26624\n\tds_read_b64_tr_b16 a[140:141], v10 offset:24832\n\t"
      "ds_read_b64_tr_b16 a[142:143], v10 offset:26880\n\tds_read_b64_tr_b16 a[172:173], v11 offset:24832\n\tds_read_b64_tr_b16 a[174:175], v11 offset:26880\n\t"
      "ds_read_b64_tr_b16 a[204:205], v12 offset:24832\n\tds_read_b64_tr_b16 a[206:207], v12 offset:26880\n\tds_read_b64_tr_b16 a[236:237], v13 offset:24832\n\t"
      "ds_read_b64_tr_b16 a[238:239], v13 offset:26880\n\ts_waitcnt lgkmcnt(0)\n\ts_add_u32 m0, s51, 24576\n\ts_mov_b32 s48, 155648\n\t"
      "buffer_load_dwordx4 v8, s[40:43], s48 offen  lds\n\ts_add_u32 m0, s51, 25600\n\ts_mov_b32 s48, 221184\n\tbuffer_load_dwordx4 v9, s[40:43], s48 offen  lds\n\t"
      "s_add_u32 m0, s51, 26624\n\ts_mov_b32 s48, 157696\n\tbuffer_load_dwordx4 v8, s[40:43], s48 offen  lds\n\ts_add_u32 m0, s51, 27648\n\ts_mov_b32 s48, 223232\n\t"
      "buffer_load_dwordx4 v9, s[40:43], s48 offen  lds\n\ts_add_u32 m0, s51, 28672\n\ts_mov_b32 s48, 159744\n\tbuffer_load_dwordx4 v8, s[40:43], s48 offen  lds\n\t"
      "s_add_u32 m0, s51, 29696\n\ts_mov_b32 s48, 225280\n\tbuffer_load_dwordx4 v9, s[40:43], s48 offen  lds\n\ts_add_u32 m0, s51, 30720\n\ts_mov_b32 s48, 161792\n\t"
      "buffer_load_dwordx4 v8, s[40:43], s48 offen  lds\n\ts_add_u32 m0, s51, 31744\n\ts_mov_b32 s48, 227328\n\tbuffer_load_dwordx4 v9, s[40:43], s48 offen  lds\n\t"
      "s_waitcnt vmcnt(24)\n\tds_read_b64_tr_b16 a[16:17], v10 offset:0\n\tds_read_b64_tr_b16 a[18:19], v10 offset:2048\n\tds_read_b64_tr_b16 a[48:49], v11 offset:0\n\t"
      "ds_read_b64_tr_b16 a[50:51], v11 offset:2048\n\tds_read_b64_tr_b16 a[80:81], v12 offset:0\n\tds_read_b64_tr_b16 a[82:83], v12 offset:2048\n\t"
      "ds_read_b64_tr_b16 a[112:113], v13 offset:0\n\tds_read_b64_tr_b16 a[114:115], v13 offset:2048\n\tds_read_b64_tr_b16 a[144:145], v10 offset:256\n\t"
      "ds_read_b64_tr_b16 a[146:147], v10 offset:2304\n\tds_read_b64_tr_b16 a[176:177], v11 offset:256\n\tds_read_b64_tr_b16 a[178:179], v11 offset:2304\n\t"
      "ds_read_b64_tr_b16 a[208:209], v12 offset:256\n\tds_read_b64_tr_b16 a[210:211], v12 offset:2304\n\tds_read_b64_tr_b16 a[240:241], v13 offset:256\n\t"
      "ds_read_b64_tr_b16 a[242:243], v13 offset:2304\n\ts_waitcnt lgkmcnt(0)\n\ts_waitcnt vmcnt(16)\n\tds_read_b64_tr_b16 a[20:21], v10 offset:8192\n\t"
      "ds_read_b64_tr_b16 a[22:23], v10 offset:10240\n\tds_read_b64_tr_b16 a[52:53], v11 offset:8192\n\tds_read_b64_tr_b16 a[54:55], v11 offset:10240\n\t"
      "ds_read_b64_tr_b16 a[84:85], v12 offset:8192\n\tds_read_b64_tr_b16 a[86:87], v12 offset:10240\n\tds_read_b64_tr_b16 a[116:117], v13 offset:8192\n\t"
      "ds_read_b64_tr_b16 a[118:119], v13 offset:10240\n\tds_read_b64_tr_b16 a[148:149], v10 offset:8448\n\tds_read_b64_tr_b16 a[150:151], v10 offset:10496\n\t"
      "ds_read_b64_tr_b16 a[180:181], v11 offset:8448\n\tds_read_b64_tr_b16 a[182:183], v11 offset:10496\n\tds_read_b64_tr_b16 a[212:213], v12 offset:8448\n\t"
      "ds_read_b64_tr_b16 a[214:215], v12 offset:10496\n\tds_read_b64_tr_b16 a[244:245], v13 offset:8448\n\tds_read_b64_tr_b16 a[246:247], v13 offset:10496\n\t"
      "s_waitcnt lgkmcnt(0)\n\ts_waitcnt vmcnt(8)\n\tds_read_b64_tr_b16 a[24:25], v10 offset:16384\n\tds_read_b64_tr_b16 a[26:27], v10 offset:18432\n\t"
      "ds_read_b64_tr_b16 a[56:57], v11 offset:16384\n\tds_read_b64_tr_b16 a[58:59], v11 offset:18432\n\tds_read_b64_tr_b16 a[88:89], v12 offset:16384\n\t"
      "ds_read_b64_tr_b16 a[90:91], v12 offset:18432\n\tds_read_b64_tr_b16 a[120:121], v13 offset:16384\n\tds_read_b64_tr_b16 a[122:123], v13 offset:18432\n\t"
      "ds_read_b64_tr_b16 a[152:153], v10 offset:16640\n\tds_read_b64_tr_b16 a[154:155], v10 offset:18688\n\tds_read_b64_tr_b16 a[184:185], v11 offset:16640\n\t"
      "ds_read_b64_tr_b16 a[186:187], v11 offset:18688\n\tds_read_b64_tr_b16 a[216:217], v12 offset:16640\n\tds_read_b64_tr_b16 a[218:219], v12 offset:18688\n\t"
      "ds_read_b64_tr_b16 a[248:249], v13 offset:16640\n\tds_read_b64_tr_b16 a[250:251], v13 offset:18688\n\ts_waitcnt lgkmcnt(0)\n\ts_waitcnt vmcnt(0)\n\t"
      "ds_read_b64_tr_b16 a[28:29], v10 offset:24576\n\tds_read_b64_tr_b16 a[30:31], v10 offset:26624\n\tds_read_b64_tr_b16 a[60:61], v11 offset:24576\n\t"
      "ds_read_b64_tr_b16 a[62:63], v11 offset:26624\n\tds_read_b64_tr_b16 a[92:93], v12 offset:24576\n\tds_read_b64_tr_b16 a[94:95], v12 offset:26624\n\t"
      "ds_read_b64_tr_b16 a[124:125], v13 offset:24576\n\tds_read_b64_tr_b16 a[126:127], v13 offset:26624\n\tds_read_b64_tr_b16 a[156:157], v10 offset:24832\n\t"
      "ds_read_b64_tr_b16 a[158:159], v10 offset:26880\n\tds_read_b64_tr_b16 a[188:189], v11 offset:24832\n\tds_read_b64_tr_b16 a[190:191], v11 offset:26880\n\t"
      "ds_read_b64_tr_b16 a[220:221], v12 offset:24832\n\tds_read_b64_tr_b16 a[222:223], v12 offset:26880\n\tds_read_b64_tr_b16 a[252:253], v13 offset:24832\n\t"
      "ds_read_b64_tr_b16 a[254:255], v13 offset:26880\n\ts_waitcnt lgkmcnt(0)\n\t"
      STS("s_memrealtime vcc\n\ts_waitcnt lgkmcnt(0)\n\tv_mov_b32 v95, vcc_lo\n\tv_mov_b32 v94, 161024\n\tds_write_b32 v94, v95\n\ts_memtime vcc\n\ts_waitcnt lgkmcnt(0)\n\tv_mov_b32 v95, vcc_lo\n\tds_write_b32 v94, v95 offset:400\n\ts_waitcnt lgkmcnt(0)\n\t")
      "s_barrier\n\t"
      "1:\n\tbuffer_load_dword v5, off, s[12:15], s63 sc1\n\ts_waitcnt vmcnt(0)\n\tv_readfirstlane_b32 s49, v5\n\ts_bitcmp1_b32 s49, 0\n\ts_cbranch_scc1 2f\n\t"
      "s_cmp_eq_u32 s18, 0\n\ts_cbranch_scc1 2f\n\ts_sub_u32 s18, s18, 1\n\ts_sleep 2\n\ts_branch 1b\n\t"
      "2:\n\ts_and_b32 s48, s49, 3\n\ts_cmp_lg_u32 s48, 3\n\ts_cbranch_scc1 4f\n\ts_cmp_eq_u32 s62, 0\n\ts_cbranch_scc1 4f\n\ts_waitcnt lgkmcnt(0)\n\ts_barrier\n\t"
      "s_mov_b64 s[0:1], s[60:61]\n\ts_mov_b32 s2, s62\n\ts_lshl_b32 s48, s16, 6\n\tv_mbcnt_lo_u32_b32 v0, -1, 0\n\tv_mbcnt_hi_u32_b32 v0, -1, v0\n\tv_add_u32 v0, s48, v0\n\t"
      "s_getpc_b64 s[4:5]\n\t"
      "3:\n\ts_sub_u32 s4, s4, 3b-mk_mega\n\ts_subb_u32 s5, s5, 0\n\ts_setpc_b64 s[4:5]\n\t"
      "4:\n\ts_lshl_b32 s49, s49, 11\n\ts_and_b32 s49, s49, 0x2000\n\ts_xor_b32 s19, s19, s49\n\tv_xor_b32 v1, s49, v1\n\ts_mov_b64 s[56:57], s[8:9]\n\ts_mov_b32 s59, s11\n\t"
      "s_mov_b32 s23, 0\n\ts_mov_b32 s25, 0\n\tv_mov_b32 v2, 0x40000000\n\ts_branch 43f\n\t"
      "10:\n\tv_mfma_f32_32x32x16_bf16 v[32:47], v[96:99], a[0:3], 0\n\ts_nop 4\n\tv_mfma_f32_32x32x16_bf16 v[32:47], v[100:103], a[4:7], v[32:47]\n\t"
      "s_add_u32 s48, s19, 0x4000\n\ts_and_b32 s48, s48, 0x3c000\n\ts_andn2_b32 s19, s19, 0x3c000\n\ts_or_b32 s19, s19, s48\n\ts_add_u32 s24, s19, 0x1000\n\t"
      "v_mfma_f32_32x32x16_bf16 v[32:47], v[104:107], a[8:11], v[32:47]\n\tv_cvt_pk_bf16_f32 v80, v48, v49\n\tv_cvt_pk_bf16_f32 v81, v50, v51\n\t"
      "v_cvt_pk_bf16_f32 v82, v56, v57\n\tv_cvt_pk_bf16_f32 v83, v58, v59\n\tv_mfma_f32_32x32x16_bf16 v[32:47], v[108:111], a[12:15], v[32:47]\n\t"
      "buffer_store_dwordx4 v[80:83], v2, s[8:11], s25 offen offset:0 sc1\n\tv_mfma_f32_32x32x16_bf16 v[32:47], v[112:115], a[16:19], v[32:47]\n\t"
      "v_cvt_pk_bf16_f32 v84, v52, v53\n\tv_cvt_pk_bf16_f32 v85, v54, v55\n\tv_cvt_pk_bf16_f32 v86, v60, v61\n\tv_cvt_pk_bf16_f32 v87, v62, v63\n\t"
      "v_mfma_f32_32x32x16_bf16 v[32:47], v[116:119], a[20:23], v[32:47]\n\tbuffer_store_dwordx4 v[84:87], v2, s[8:11], s25 offen offset:512 sc1\n\t"
      "v_mfma_f32_32x32x16_bf16 v[32:47], v[120:123], a[24:27], v[32:47]\n\ts_add_u32 s25, s20, 0x0\n\tv_mfma_f32_32x32x16_bf16 v[32:47], v[124:127], a[28:31], v[32:47]\n\t"
      "v_mov_b32 v4, s17\n\tv_mov_b32 v2, v1\n\tv_mfma_f32_32x32x16_bf16 v[48:63], v[96:99], a[32:35], 0\n\ts_nop 4\n\t"
      "v_mfma_f32_32x32x16_bf16 v[48:63], v[100:103], a[36:39], v[48:63]\n\ts_nop 4\n\tv_mfma_f32_32x32x16_bf16 v[48:63], v[104:107], a[40:43], v[48:63]\n\t"
      "v_cvt_pk_bf16_f32 v80, v32, v33\n\tv_cvt_pk_bf16_f32 v81, v34, v35\n\tv_cvt_pk_bf16_f32 v82, v40, v41\n\tv_cvt_pk_bf16_f32 v83, v42, v43\n\t"
      "v_mfma_f32_32x32x16_bf16 v[48:63], v[108:111], a[44:47], v[48:63]\n\tbuffer_store_dwordx4 v[80:83], v1, s[8:11], s25 offen offset:0 sc1\n\t"
      "v_mfma_f32_32x32x16_bf16 v[48:63], v[112:115], a[48:51], v[48:63]\n\tv_cvt_pk_bf16_f32 v84, v36, v37\n\tv_cvt_pk_bf16_f32 v85, v38, v39\n\t"
      "v_cvt_pk_bf16_f32 v86, v44, v45\n\tv_cvt_pk_bf16_f32 v87, v46, v47\n\tv_mfma_f32_32x32x16_bf16 v[48:63], v[116:119], a[52:55], v[48:63]\n\t"
      "buffer_store_dwordx4 v[84:87], v1, s[8:11], s25 offen offset:512 sc1\n\tv_mfma_f32_32x32x16_bf16 v[48:63], v[120:123], a[56:59], v[48:63]\n\t"
      "s_add_u32 s25, s20, 0x20000\n\tv_mfma_f32_32x32x16_bf16 v[48:63], v[124:127], a[60:63], v[48:63]\n\tbuffer_load_dword v5, v6, s[12:15], 0 offen sc1\n\t"
      "v_mfma_f32_32x32x16_bf16 v[32:47], v[96:99], a[64:67], 0\n\ts_nop 4\n\tv_mfma_f32_32x32x16_bf16 v[32:47], v[100:103], a[68:71], v[32:47]\n\ts_nop 4\n\t"
      "v_mfma_f32_32x32x16_bf16 v[32:47], v[104:107], a[72:75], v[32:47]\n\tv_cvt_pk_bf16_f32 v80, v48, v49\n\tv_cvt_pk_bf16_f32 v81, v50, v51\n\t"
      "v_cvt_pk_bf16_f32 v82, v56, v57\n\tv_cvt_pk_bf16_f32 v83, v58, v59\n\tv_mfma_f32_32x32x16_bf16 v[32:47], v[108:111], a[76:79], v[32:47]\n\t"
      "buffer_store_dwordx4 v[80:83], v1, s[8:11], s25 offen offset:0 sc1\n\tv_mfma_f32_32x32x16_bf16 v[32:47], v[112:115], a[80:83], v[32:47]\n\t"
      "v_cvt_pk_bf16_f32 v84, v52, v53\n\tv_cvt_pk_bf16_f32 v85, v54, v55\n\tv_cvt_pk_bf16_f32 v86, v60, v61\n\tv_cvt_pk_bf16_f32 v87, v62, v63\n\t"
      "v_mfma_f32_32x32x16_bf16 v[32:47], v[116:119], a[84:87], v[32:47]\n\tbuffer_store_dwordx4 v[84:87], v1, s[8:11], s25 offen offset:512 sc1\n\t"
      "v_mfma_f32_32x32x16_bf16 v[32:47], v[120:123], a[88:91], v[32:47]\n\ts_add_u32 s25, s20, 0x40000\n\tv_mfma_f32_32x32x16_bf16 v[32:47], v[124:127], a[92:95], v[32:47]\n\t"
      "v_mfma_f32_32x32x16_bf16 v[48:63], v[96:99], a[96:99], 0\n\ts_nop 4\n\tv_mfma_f32_32x32x16_bf16 v[48:63], v[100:103], a[100:103], v[48:63]\n\ts_nop 4\n\t"
      "v_mfma_f32_32x32x16_bf16 v[48:63], v[104:107], a[104:107], v[48:63]\n\tv_cvt_pk_bf16_f32 v80, v32, v33\n\tv_cvt_pk_bf16_f32 v81, v34, v35\n\t"
      "v_cvt_pk_bf16_f32 v82, v40, v41\n\tv_cvt_pk_bf16_f32 v83, v42, v43\n\tv_mfma_f32_32x32x16_bf16 v[48:63], v[108:111], a[108:111], v[48:63]\n\t"
      "buffer_store_dwordx4 v[80:83], v1, s[8:11], s25 offen offset:0 sc1\n\tv_mfma_f32_32x32x16_bf16 v[48:63], v[112:115], a[112:115], v[48:63]\n\t"
      "v_cvt_pk_bf16_f32 v84, v36, v37\n\tv_cvt_pk_bf16_f32 v85, v38, v39\n\tv_cvt_pk_bf16_f32 v86, v44, v45\n\tv_cvt_pk_bf16_f32 v87, v46, v47\n\t"
      "v_mfma_f32_32x32x16_bf16 v[48:63], v[116:119], a[116:119], v[48:63]\n\tbuffer_store_dwordx4 v[84:87], v1, s[8:11], s25 offen offset:512 sc1\n\t"
      "v_mfma_f32_32x32x16_bf16 v[48:63], v[120:123], a[120:123], v[48:63]\n\ts_add_u32 s25, s20, 0x60000\n\ts_waitcnt vmcnt(4)\n\t"
      "buffer_store_dword v4, v3, s[12:15], s22 offen sc1\n\tv_mfma_f32_32x32x16_bf16 v[48:63], v[124:127], a[124:127], v[48:63]\n\tv_readlane_b32 s23, v5, 0\n\t"
      "v_readlane_b32 s48, v5, 1\n\ts_add_u32 s48, s48, 12\n\ts_min_u32 s23, s23, s48\n\tv_mfma_f32_32x32x16_bf16 v[32:47], v[96:99], a[128:131], 0\n\t"
      "s_add_u32 s48, s17, 1\n\ts_cmp_gt_u32 s23, s48\n\ts_cselect_b32 s58, s10, 0\n\ts_nop 1\n\tv_mfma_f32_32x32x16_bf16 v[32:47], v[100:103], a[132:135], v[32:47]\n\t"
      "s_nop 4\n\tv_mfma_f32_32x32x16_bf16 v[32:47], v[104:107], a[136:139], v[32:47]\n\tv_cvt_pk_bf16_f32 v80, v48, v49\n\tv_cvt_pk_bf16_f32 v81, v50, v51\n\t"
      "v_cvt_pk_bf16_f32 v82, v56, v57\n\tv_cvt_pk_bf16_f32 v83, v58, v59\n\tbuffer_load_dwordx4 v[128:131], v0, s[56:59], s19 offen offset:0 sc1\n\t"
      "v_mfma_f32_32x32x16_bf16 v[32:47], v[108:111], a[140:143], v[32:47]\n\tbuffer_store_dwordx4 v[80:83], v1, s[8:11], s25 offen offset:0 sc1\n\t"
      "v_mfma_f32_32x32x16_bf16 v[32:47], v[112:115], a[144:147], v[32:47]\n\tv_cvt_pk_bf16_f32 v84, v52, v53\n\tv_cvt_pk_bf16_f32 v85, v54, v55\n\t"
      "v_cvt_pk_bf16_f32 v86, v60, v61\n\tv_cvt_pk_bf16_f32 v87, v62, v63\n\tbuffer_load_dwordx4 v[132:135], v0, s[56:59], s19 offen offset:1024 sc1\n\t"
      "v_mfma_f32_32x32x16_bf16 v[32:47], v[116:119], a[148:151], v[32:47]\n\tbuffer_store_dwordx4 v[84:87], v1, s[8:11], s25 offen offset:512 sc1\n\t"
      "v_mfma_f32_32x32x16_bf16 v[32:47], v[120:123], a[152:155], v[32:47]\n\ts_add_u32 s25, s20, 0x80000\n\t"
      "buffer_load_dwordx4 v[136:139], v0, s[56:59], s19 offen offset:2048 sc1\n\tv_mfma_f32_32x32x16_bf16 v[32:47], v[124:127], a[156:159], v[32:47]\n\t"
      "buffer_load_dwordx4 v[140:143], v0, s[56:59], s19 offen offset:3072 sc1\n\tv_mfma_f32_32x32x16_bf16 v[48:63], v[96:99], a[160:163], 0\n\ts_nop 4\n\t"
      "v_mfma_f32_32x32x16_bf16 v[48:63], v[100:103], a[164:167], v[48:63]\n\ts_nop 4\n\tv_mfma_f32_32x32x16_bf16 v[48:63], v[104:107], a[168:171], v[48:63]\n\t"
      "v_cvt_pk_bf16_f32 v80, v32, v33\n\tv_cvt_pk_bf16_f32 v81, v34, v35\n\tv_cvt_pk_bf16_f32 v82, v40, v41\n\tv_cvt_pk_bf16_f32 v83, v42, v43\n\t"
      "buffer_load_dwordx4 v[144:147], v0, s[56:59], s24 offen offset:0 sc1\n\tv_mfma_f32_32x32x16_bf16 v[48:63], v[108:111], a[172:175], v[48:63]\n\t"
      "buffer_store_dwordx4 v[80:83], v1, s[8:11], s25 offen offset:0 sc1\n\tv_mfma_f32_32x32x16_bf16 v[48:63], v[112:115], a[176:179], v[48:63]\n\t"
      "v_cvt_pk_bf16_f32 v84, v36, v37\n\tv_cvt_pk_bf16_f32 v85, v38, v39\n\tv_cvt_pk_bf16_f32 v86, v44, v45\n\tv_cvt_pk_bf16_f32 v87, v46, v47\n\t"
      "buffer_load_dwordx4 v[148:151], v0, s[56:59], s24 offen offset:1024 sc1\n\tv_mfma_f32_32x32x16_bf16 v[48:63], v[116:119], a[180:183], v[48:63]\n\t"
      "buffer_store_dwordx4 v[84:87], v1, s[8:11], s25 offen offset:512 sc1\n\tv_mfma_f32_32x32x16_bf16 v[48:63], v[120:123], a[184:187], v[48:63]\n\t"
      "s_add_u32 s25, s20, 0xa0000\n\tbuffer_load_dwordx4 v[152:155], v0, s[56:59], s24 offen offset:2048 sc1\n\t"
      "v_mfma_f32_32x32x16_bf16 v[48:63], v[124:127], a[188:191], v[48:63]\n\tbuffer_load_dwordx4 v[156:159], v0, s[56:59], s24 offen offset:3072 sc1\n\t"
      "v_mfma_f32_32x32x16_bf16 v[32:47], v[96:99], a[192:195], 0\n\ts_nop 4\n\tv_mfma_f32_32x32x16_bf16 v[32:47], v[100:103], a[196:199], v[32:47]\n\ts_nop 4\n\t"
      "v_mfma_f32_32x32x16_bf16 v[32:47], v[104:107], a[200:203], v[32:47]\n\tv_cvt_pk_bf16_f32 v80, v48, v49\n\tv_cvt_pk_bf16_f32 v81, v50, v51\n\t"
      "v_cvt_pk_bf16_f32 v82, v56, v57\n\tv_cvt_pk_bf16_f32 v83, v58, v59\n\tv_mfma_f32_32x32x16_bf16 v[32:47], v[108:111], a[204:207], v[32:47]\n\t"
      "buffer_store_dwordx4 v[80:83], v1, s[8:11], s25 offen offset:0 sc1\n\tv_mfma_f32_32x32x16_bf16 v[32:47], v[112:115], a[208:211], v[32:47]\n\t"
      "v_cvt_pk_bf16_f32 v84, v52, v53\n\tv_cvt_pk_bf16_f32 v85, v54, v55\n\tv_cvt_pk_bf16_f32 v86, v60, v61\n\tv_cvt_pk_bf16_f32 v87, v62, v63\n\t"
      "v_mfma_f32_32x32x16_bf16 v[32:47], v[116:119], a[212:215], v[32:47]\n\tbuffer_store_dwordx4 v[84:87], v1, s[8:11], s25 offen offset:512 sc1\n\t"
      "v_mfma_f32_32x32x16_bf16 v[32:47], v[120:123], a[216:219], v[32:47]\n\ts_add_u32 s25, s20, 0xc0000\n\t"
      "v_mfma_f32_32x32x16_bf16 v[32:47], v[124:127], a[220:223], v[32:47]\n\tv_mfma_f32_32x32x16_bf16 v[48:63], v[96:99], a[224:227], 0\n\ts_nop 4\n\t"
      "v_mfma_f32_32x32x16_bf16 v[48:63], v[100:103], a[228:231], v[48:63]\n\ts_nop 4\n\tv_mfma_f32_32x32x16_bf16 v[48:63], v[104:107], a[232:235], v[48:63]\n\t"
      "v_cvt_pk_bf16_f32 v80, v32, v33\n\tv_cvt_pk_bf16_f32 v81, v34, v35\n\tv_cvt_pk_bf16_f32 v82, v40, v41\n\tv_cvt_pk_bf16_f32 v83, v42, v43\n\t"
      "v_mfma_f32_32x32x16_bf16 v[48:63], v[108:111], a[236:239], v[48:63]\n\tbuffer_store_dwordx4 v[80:83], v1, s[8:11], s25 offen offset:0 sc1\n\t"
      "v_mfma_f32_32x32x16_bf16 v[48:63], v[112:115], a[240:243], v[48:63]\n\tv_cvt_pk_bf16_f32 v84, v36, v37\n\tv_cvt_pk_bf16_f32 v85, v38, v39\n\t"
      "v_cvt_pk_bf16_f32 v86, v44, v45\n\tv_cvt_pk_bf16_f32 v87, v46, v47\n\tv_mfma_f32_32x32x16_bf16 v[48:63], v[116:119], a[244:247], v[48:63]\n\t"
      "buffer_store_dwordx4 v[84:87], v1, s[8:11], s25 offen offset:512 sc1\n\tv_mfma_f32_32x32x16_bf16 v[48:63], v[120:123], a[248:251], v[48:63]\n\t"
      "s_add_u32 s25, s20, 0xe0000\n\tv_mfma_f32_32x32x16_bf16 v[48:63], v[124:127], a[252:255], v[48:63]\n\ts_waitcnt vmcnt(4)\n\ts_add_u32 s17, s17, 1\n\t"
      "s_add_u32 s20, s20, 0x400000\n\ts_cmp_eq_u32 s20, 0x4400000\n\ts_cselect_b32 s20, 0x1400000, s20\n\ts_cmp_eq_u32 s58, 0\n\ts_cbranch_scc1 40f\n\t"
      "11:\n\t"
      STF("s_memrealtime vcc\n\ts_waitcnt lgkmcnt(0)\n\tv_mov_b32 v95, vcc_lo\n\ts_lshl_b32 vcc_hi, s17, 2\n\ts_add_u32 vcc_hi, vcc_hi, 161052\n\tv_mov_b32 v94, vcc_hi\n\tds_write_b32 v94, v95\n\ts_memtime vcc\n\ts_waitcnt lgkmcnt(0)\n\tv_mov_b32 v95, vcc_lo\n\tds_write_b32 v94, v95 offset:400\n\ts_waitcnt lgkmcnt(0)\n\t")
      "v_mfma_f32_32x32x16_bf16 v[32:47], v[128:131], a[0:3], 0\n\ts_nop 4\n\tv_mfma_f32_32x32x16_bf16 v[32:47], v[132:135], a[4:7], v[32:47]\n\ts_add_u32 s48, s19, 0x4000\n\t"
      "s_and_b32 s48, s48, 0x3c000\n\ts_andn2_b32 s19, s19, 0x3c000\n\ts_or_b32 s19, s19, s48\n\ts_add_u32 s24, s19, 0x1000\n\t"
      "v_mfma_f32_32x32x16_bf16 v[32:47], v[136:139], a[8:11], v[32:47]\n\tv_cvt_pk_bf16_f32 v80, v48, v49\n\tv_cvt_pk_bf16_f32 v81, v50, v51\n\t"
      "v_cvt_pk_bf16_f32 v82, v56, v57\n\tv_cvt_pk_bf16_f32 v83, v58, v59\n\tv_mfma_f32_32x32x16_bf16 v[32:47], v[140:143], a[12:15], v[32:47]\n\t"
      "buffer_store_dwordx4 v[80:83], v2, s[8:11], s25 offen offset:0 sc1\n\tv_mfma_f32_32x32x16_bf16 v[32:47], v[144:147], a[16:19], v[32:47]\n\t"
      "v_cvt_pk_bf16_f32 v84, v52, v53\n\tv_cvt_pk_bf16_f32 v85, v54, v55\n\tv_cvt_pk_bf16_f32 v86, v60, v61\n\tv_cvt_pk_bf16_f32 v87, v62, v63\n\t"
      "v_mfma_f32_32x32x16_bf16 v[32:47], v[148:151], a[20:23], v[32:47]\n\tbuffer_store_dwordx4 v[84:87], v2, s[8:11], s25 offen offset:512 sc1\n\t"
      "v_mfma_f32_32x32x16_bf16 v[32:47], v[152:155], a[24:27], v[32:47]\n\ts_add_u32 s25, s20, 0x0\n\tv_mfma_f32_32x32x16_bf16 v[32:47], v[156:159], a[28:31], v[32:47]\n\t"
      "v_mov_b32 v4, s17\n\tv_mov_b32 v2, v1\n\tv_mfma_f32_32x32x16_bf16 v[48:63], v[128:131], a[32:35], 0\n\ts_nop 4\n\t"
      "v_mfma_f32_32x32x16_bf16 v[48:63], v[132:135], a[36:39], v[48:63]\n\ts_nop 4\n\tv_mfma_f32_32x32x16_bf16 v[48:63], v[136:139], a[40:43], v[48:63]\n\t"
      "v_cvt_pk_bf16_f32 v80, v32, v33\n\tv_cvt_pk_bf16_f32 v81, v34, v35\n\tv_cvt_pk_bf16_f32 v82, v40, v41\n\tv_cvt_pk_bf16_f32 v83, v42, v43\n\t"
      "v_mfma_f32_32x32x16_bf16 v[48:63], v[140:143], a[44:47], v[48:63]\n\tbuffer_store_dwordx4 v[80:83], v1, s[8:11], s25 offen offset:0 sc1\n\t"
      "v_mfma_f32_32x32x16_bf16 v[48:63], v[144:147], a[48:51], v[48:63]\n\tv_cvt_pk_bf16_f32 v84, v36, v37\n\tv_cvt_pk_bf16_f32 v85, v38, v39\n\t"
      "v_cvt_pk_bf16_f32 v86, v44, v45\n\tv_cvt_pk_bf16_f32 v87, v46, v47\n\tv_mfma_f32_32x32x16_bf16 v[48:63], v[148:151], a[52:55], v[48:63]\n\t"
      "buffer_store_dwordx4 v[84:87], v1, s[8:11], s25 offen offset:512 sc1\n\tv_mfma_f32_32x32x16_bf16 v[48:63], v[152:155], a[56:59], v[48:63]\n\t"
      "s_add_u32 s25, s20, 0x20000\n\tv_mfma_f32_32x32x16_bf16 v[48:63], v[156:159], a[60:63], v[48:63]\n\tbuffer_load_dword v5, v6, s[12:15], 0 offen sc1\n\t"
      "v_mfma_f32_32x32x16_bf16 v[32:47], v[128:131], a[64:67], 0\n\ts_nop 4\n\tv_mfma_f32_32x32x16_bf16 v[32:47], v[132:135], a[68:71], v[32:47]\n\ts_nop 4\n\t"
      "v_mfma_f32_32x32x16_bf16 v[32:47], v[136:139], a[72:75], v[32:47]\n\tv_cvt_pk_bf16_f32 v80, v48, v49\n\tv_cvt_pk_bf16_f32 v81, v50, v51\n\t"
      "v_cvt_pk_bf16_f32 v82, v56, v57\n\tv_cvt_pk_bf16_f32 v83, v58, v59\n\tv_mfma_f32_32x32x16_bf16 v[32:47], v[140:143], a[76:79], v[32:47]\n\t"
      "buffer_store_dwordx4 v[80:83], v1, s[8:11], s25 offen offset:0 sc1\n\tv_mfma_f32_32x32x16_bf16 v[32:47], v[144:147], a[80:83], v[32:47]\n\t"
      "v_cvt_pk_bf16_f32 v84, v52, v53\n\tv_cvt_pk_bf16_f32 v85, v54, v55\n\tv_cvt_pk_bf16_f32 v86, v60, v61\n\tv_cvt_pk_bf16_f32 v87, v62, v63\n\t"
      "v_mfma_f32_32x32x16_bf16 v[32:47], v[148:151], a[84:87], v[32:47]\n\tbuffer_store_dwordx4 v[84:87], v1, s[8:11], s25 offen offset:512 sc1\n\t"
      "v_mfma_f32_32x32x16_bf16 v[32:47], v[152:155], a[88:91], v[32:47]\n\ts_add_u32 s25, s20, 0x40000\n\tv_mfma_f32_32x32x16_bf16 v[32:47], v[156:159], a[92:95], v[32:47]\n\t"
      "v_mfma_f32_32x32x16_bf16 v[48:63], v[128:131], a[96:99], 0\n\ts_nop 4\n\tv_mfma_f32_32x32x16_bf16 v[48:63], v[132:135], a[100:103], v[48:63]\n\ts_nop 4\n\t"
      "v_mfma_f32_32x32x16_bf16 v[48:63], v[136:139], a[104:107], v[48:63]\n\tv_cvt_pk_bf16_f32 v80, v32, v33\n\tv_cvt_pk_bf16_f32 v81, v34, v35\n\t"
      "v_cvt_pk_bf16_f32 v82, v40, v41\n\tv_cvt_pk_bf16_f32 v83, v42, v43\n\tv_mfma_f32_32x32x16_bf16 v[48:63], v[140:143], a[108:111], v[48:63]\n\t"
      "buffer_store_dwordx4 v[80:83], v1, s[8:11], s25 offen offset:0 sc1\n\tv_mfma_f32_32x32x16_bf16 v[48:63], v[144:147], a[112:115], v[48:63]\n\t"
      "v_cvt_pk_bf16_f32 v84, v36, v37\n\tv_cvt_pk_bf16_f32 v85, v38, v39\n\tv_cvt_pk_bf16_f32 v86, v44, v45\n\tv_cvt_pk_bf16_f32 v87, v46, v47\n\t"
      "v_mfma_f32_32x32x16_bf16 v[48:63], v[148:151], a[116:119], v[48:63]\n\tbuffer_store_dwordx4 v[84:87], v1, s[8:11], s25 offen offset:512 sc1\n\t"
      "v_mfma_f32_32x32x16_bf16 v[48:63], v[152:155], a[120:123], v[48:63]\n\ts_add_u32 s25, s20, 0x60000\n\ts_waitcnt vmcnt(4)\n\t"
      "buffer_store_dword v4, v3, s[12:15], s22 offen sc1\n\tv_mfma_f32_32x32x16_bf16 v[48:63], v[156:159], a[124:127], v[48:63]\n\tv_readlane_b32 s23, v5, 0\n\t"
      "v_readlane_b32 s48, v5, 1\n\ts_add_u32 s48, s48, 12\n\ts_min_u32 s23, s23, s48\n\tv_mfma_f32_32x32x16_bf16 v[32:47], v[128:131], a[128:131], 0\n\t"
      "s_add_u32 s48, s17, 1\n\ts_cmp_gt_u32 s23, s48\n\ts_cselect_b32 s58, s10, 0\n\ts_nop 1\n\tv_mfma_f32_32x32x16_bf16 v[32:47], v[132:135], a[132:135], v[32:47]\n\t"
      "s_nop 4\n\tv_mfma_f32_32x32x16_bf16 v[32:47], v[136:139], a[136:139], v[32:47]\n\tv_cvt_pk_bf16_f32 v80, v48, v49\n\tv_cvt_pk_bf16_f32 v81, v50, v51\n\t"
      "v_cvt_pk_bf16_f32 v82, v56, v57\n\tv_cvt_pk_bf16_f32 v83, v58, v59\n\tbuffer_load_dwordx4 v[96:99], v0, s[56:59], s19 offen offset:0 sc1\n\t"
      "v_mfma_f32_32x32x16_bf16 v[32:47], v[140:143], a[140:143], v[32:47]\n\tbuffer_store_dwordx4 v[80:83], v1, s[8:11], s25 offen offset:0 sc1\n\t"
      "v_mfma_f32_32x32x16_bf16 v[32:47], v[144:147], a[144:147], v[32:47]\n\tv_cvt_pk_bf16_f32 v84, v52, v53\n\tv_cvt_pk_bf16_f32 v85, v54, v55\n\t"
      "v_cvt_pk_bf16_f32 v86, v60, v61\n\tv_cvt_pk_bf16_f32 v87, v62, v63\n\tbuffer_load_dwordx4 v[100:103], v0, s[56:59], s19 offen offset:1024 sc1\n\t"
      "v_mfma_f32_32x32x16_bf16 v[32:47], v[148:151], a[148:151], v[32:47]\n\tbuffer_store_dwordx4 v[84:87], v1, s[8:11], s25 offen offset:512 sc1\n\t"
      "v_mfma_f32_32x32x16_bf16 v[32:47], v[152:155], a[152:155], v[32:47]\n\ts_add_u32 s25, s20, 0x80000\n\t"
      "buffer_load_dwordx4 v[104:107], v0, s[56:59], s19 offen offset:2048 sc1\n\tv_mfma_f32_32x32x16_bf16 v[32:47], v[156:159], a[156:159], v[32:47]\n\t"
      "buffer_load_dwordx4 v[108:111], v0, s[56:59], s19 offen offset:3072 sc1\n\tv_mfma_f32_32x32x16_bf16 v[48:63], v[128:131], a[160:163], 0\n\ts_nop 4\n\t"
      "v_mfma_f32_32x32x16_bf16 v[48:63], v[132:135], a[164:167], v[48:63]\n\ts_nop 4\n\tv_mfma_f32_32x32x16_bf16 v[48:63], v[136:139], a[168:171], v[48:63]\n\t"
      "v_cvt_pk_bf16_f32 v80, v32, v33\n\tv_cvt_pk_bf16_f32 v81, v34, v35\n\tv_cvt_pk_bf16_f32 v82, v40, v41\n\tv_cvt_pk_bf16_f32 v83, v42, v43\n\t"
      "buffer_load_dwordx4 v[112:115], v0, s[56:59], s24 offen offset:0 sc1\n\tv_mfma_f32_32x32x16_bf16 v[48:63], v[140:143], a[172:175], v[48:63]\n\t"
      "buffer_store_dwordx4 v[80:83], v1, s[8:11], s25 offen offset:0 sc1\n\tv_mfma_f32_32x32x16_bf16 v[48:63], v[144:147], a[176:179], v[48:63]\n\t"
      "v_cvt_pk_bf16_f32 v84, v36, v37\n\tv_cvt_pk_bf16_f32 v85, v38, v39\n\tv_cvt_pk_bf16_f32 v86, v44, v45\n\tv_cvt_pk_bf16_f32 v87, v46, v47\n\t"
      "buffer_load_dwordx4 v[116:119], v0, s[56:59], s24 offen offset:1024 sc1\n\tv_mfma_f32_32x32x16_bf16 v[48:63], v[148:151], a[180:183], v[48:63]\n\t"
      "buffer_store_dwordx4 v[84:87], v1, s[8:11], s25 offen offset:512 sc1\n\tv_mfma_f32_32x32x16_bf16 v[48:63], v[152:155], a[184:187], v[48:63]\n\t"
      "s_add_u32 s25, s20, 0xa0000\n\tbuffer_load_dwordx4 v[120:123], v0, s[56:59], s24 offen offset:2048 sc1\n\t"
      "v_mfma_f32_32x32x16_bf16 v[48:63], v[156:159], a[188:191], v[48:63]\n\tbuffer_load_dwordx4 v[124:127], v0, s[56:59], s24 offen offset:3072 sc1\n\t"
      "v_mfma_f32_32x32x16_bf16 v[32:47], v[128:131], a[192:195], 0\n\ts_nop 4\n\tv_mfma_f32_32x32x16_bf16 v[32:47], v[132:135], a[196:199], v[32:47]\n\ts_nop 4\n\t"
      "v_mfma_f32_32x32x16_bf16 v[32:47], v[136:139], a[200:203], v[32:47]\n\tv_cvt_pk_bf16_f32 v80, v48, v49\n\tv_cvt_pk_bf16_f32 v81, v50, v51\n\t"
      "v_cvt_pk_bf16_f32 v82, v56, v57\n\tv_cvt_pk_bf16_f32 v83, v58, v59\n\tv_mfma_f32_32x32x16_bf16 v[32:47], v[140:143], a[204:207], v[32:47]\n\t"
      "buffer_store_dwordx4 v[80:83], v1, s[8:11], s25 offen offset:0 sc1\n\tv_mfma_f32_32x32x16_bf16 v[32:47], v[144:147], a[208:211], v[32:47]\n\t"
      "v_cvt_pk_bf16_f32 v84, v52, v53\n\tv_cvt_pk_bf16_f32 v85, v54, v55\n\tv_cvt_pk_bf16_f32 v86, v60, v61\n\tv_cvt_pk_bf16_f32 v87, v62, v63\n\t"
      "v_mfma_f32_32x32x16_bf16 v[32:47], v[148:151], a[212:215], v[32:47]\n\tbuffer_store_dwordx4 v[84:87], v1, s[8:11], s25 offen offset:512 sc1\n\t"
      "v_mfma_f32_32x32x16_bf16 v[32:47], v[152:155], a[216:219], v[32:47]\n\ts_add_u32 s25, s20, 0xc0000\n\t"
      "v_mfma_f32_32x32x16_bf16 v[32:47], v[156:159], a[220:223], v[32:47]\n\tv_mfma_f32_32x32x16_bf16 v[48:63], v[128:131], a[224:227], 0\n\ts_nop 4\n\t"
      "v_mfma_f32_32x32x16_bf16 v[48:63], v[132:135], a[228:231], v[48:63]\n\ts_nop 4\n\tv_mfma_f32_32x32x16_bf16 v[48:63], v[136:139], a[232:235], v[48:63]\n\t"
      "v_cvt_pk_bf16_f32 v80, v32, v33\n\tv_cvt_pk_bf16_f32 v81, v34, v35\n\tv_cvt_pk_bf16_f32 v82, v40, v41\n\tv_cvt_pk_bf16_f32 v83, v42, v43\n\t"
      "v_mfma_f32_32x32x16_bf16 v[48:63], v[140:143], a[236:239], v[48:63]\n\tbuffer_store_dwordx4 v[80:83], v1, s[8:11], s25 offen offset:0 sc1\n\t"
      "v_mfma_f32_32x32x16_bf16 v[48:63], v[144:147], a[240:243], v[48:63]\n\tv_cvt_pk_bf16_f32 v84, v36, v37\n\tv_cvt_pk_bf16_f32 v85, v38, v39\n\t"
      "v_cvt_pk_bf16_f32 v86, v44, v45\n\tv_cvt_pk_bf16_f32 v87, v46, v47\n\tv_mfma_f32_32x32x16_bf16 v[48:63], v[148:151], a[244:247], v[48:63]\n\t"
      "buffer_store_dwordx4 v[84:87], v1, s[8:11], s25 offen offset:512 sc1\n\tv_mfma_f32_32x32x16_bf16 v[48:63], v[152:155], a[248:251], v[48:63]\n\t"
      "s_add_u32 s25, s20, 0xe0000\n\tv_mfma_f32_32x32x16_bf16 v[48:63], v[156:159], a[252:255], v[48:63]\n\ts_waitcnt vmcnt(4)\n\ts_add_u32 s17, s17, 1\n\t"
      "s_add_u32 s20, s20, 0x400000\n\ts_cmp_eq_u32 s20, 0x4400000\n\ts_cselect_b32 s20, 0x1400000, s20\n\ts_cmp_eq_u32 s58, 0\n\ts_cbranch_scc1 42f\n\t"
      "12:\n\t"
      STF("s_memrealtime vcc\n\ts_waitcnt lgkmcnt(0)\n\tv_mov_b32 v95, vcc_lo\n\ts_lshl_b32 vcc_hi, s17, 2\n\ts_add_u32 vcc_hi, vcc_hi, 161052\n\tv_mov_b32 v94, vcc_hi\n\tds_write_b32 v94, v95\n\ts_memtime vcc\n\ts_waitcnt lgkmcnt(0)\n\tv_mov_b32 v95, vcc_lo\n\tds_write_b32 v94, v95 offset:400\n\ts_waitcnt lgkmcnt(0)\n\t")
      "s_branch 10b\n\t"
      "40:\n\tbuffer_load_dword v5, v6, s[12:15], 0 offen sc1\n\ts_nop 10\n\tv_cvt_pk_bf16_f32 v80, v48, v49\n\tv_cvt_pk_bf16_f32 v81, v50, v51\n\t"
      "v_cvt_pk_bf16_f32 v82, v56, v57\n\tv_cvt_pk_bf16_f32 v83, v58, v59\n\tv_cvt_pk_bf16_f32 v84, v52, v53\n\tv_cvt_pk_bf16_f32 v85, v54, v55\n\t"
      "v_cvt_pk_bf16_f32 v86, v60, v61\n\tv_cvt_pk_bf16_f32 v87, v62, v63\n\tbuffer_store_dwordx4 v[80:83], v1, s[8:11], s25 offen offset:0 sc1\n\t"
      "buffer_store_dwordx4 v[84:87], v1, s[8:11], s25 offen offset:512 sc1\n\tv_mov_b32 v4, s17\n\ts_waitcnt vmcnt(2)\n\tv_readlane_b32 s23, v5, 0\n\t"
      "v_readlane_b32 s48, v5, 1\n\ts_add_u32 s48, s48, 12\n\ts_min_u32 s23, s23, s48\n\ts_cmp_gt_u32 s23, s17\n\ts_cbranch_scc0 47f\n\t"
      "buffer_load_dwordx4 v[128:131], v0, s[8:11], s19 offen offset:0 sc1\n\tbuffer_load_dwordx4 v[132:135], v0, s[8:11], s19 offen offset:1024 sc1\n\t"
      "buffer_load_dwordx4 v[136:139], v0, s[8:11], s19 offen offset:2048 sc1\n\tbuffer_load_dwordx4 v[140:143], v0, s[8:11], s19 offen offset:3072 sc1\n\t"
      "buffer_load_dwordx4 v[144:147], v0, s[8:11], s24 offen offset:0 sc1\n\tbuffer_load_dwordx4 v[148:151], v0, s[8:11], s24 offen offset:1024 sc1\n\t"
      "buffer_load_dwordx4 v[152:155], v0, s[8:11], s24 offen offset:2048 sc1\n\tbuffer_load_dwordx4 v[156:159], v0, s[8:11], s24 offen offset:3072 sc1\n\t"
      "s_waitcnt vmcnt(8)\n\tbuffer_store_dword v4, v3, s[12:15], s22 offen sc1\n\ts_waitcnt vmcnt(1)\n\ts_branch 11b\n\t"
      "47:\n\ts_waitcnt vmcnt(0)\n\tbuffer_store_dword v4, v3, s[12:15], s22 offen sc1\n\ts_cmp_eq_u32 s17, 64\n\ts_cbranch_scc1 50f\n\t"
      "41:\n\t"
      "45:\n\ts_cmp_gt_u32 s23, s17\n\ts_cbranch_scc1 46f\n\tbuffer_load_dword v5, v6, s[12:15], 0 offen sc1\n\ts_waitcnt vmcnt(0)\n\tv_readlane_b32 s23, v5, 0\n\t"
      "v_readlane_b32 s48, v5, 1\n\ts_add_u32 s48, s48, 12\n\ts_min_u32 s23, s23, s48\n\ts_cmp_eq_u32 s18, 0\n\ts_cbranch_scc1 46f\n\ts_sub_u32 s18, s18, 1\n\ts_sleep 1\n\t"
      "s_branch 45b\n\t"
      "46:\n\ts_add_u32 s24, s19, 0x1000\n\tbuffer_load_dwordx4 v[128:131], v0, s[8:11], s19 offen offset:0 sc1\n\t"
      "buffer_load_dwordx4 v[132:135], v0, s[8:11], s19 offen offset:1024 sc1\n\tbuffer_load_dwordx4 v[136:139], v0, s[8:11], s19 offen offset:2048 sc1\n\t"
      "buffer_load_dwordx4 v[140:143], v0, s[8:11], s19 offen offset:3072 sc1\n\tbuffer_load_dwordx4 v[144:147], v0, s[8:11], s24 offen offset:0 sc1\n\t"
      "buffer_load_dwordx4 v[148:151], v0, s[8:11], s24 offen offset:1024 sc1\n\tbuffer_load_dwordx4 v[152:155], v0, s[8:11], s24 offen offset:2048 sc1\n\t"
      "buffer_load_dwordx4 v[156:159], v0, s[8:11], s24 offen offset:3072 sc1\n\ts_waitcnt vmcnt(0)\n\ts_branch 11b\n\t"
      "42:\n\tbuffer_load_dword v5, v6, s[12:15], 0 offen sc1\n\ts_nop 10\n\tv_cvt_pk_bf16_f32 v80, v48, v49\n\tv_cvt_pk_bf16_f32 v81, v50, v51\n\t"
      "v_cvt_pk_bf16_f32 v82, v56, v57\n\tv_cvt_pk_bf16_f32 v83, v58, v59\n\tv_cvt_pk_bf16_f32 v84, v52, v53\n\tv_cvt_pk_bf16_f32 v85, v54, v55\n\t"
      "v_cvt_pk_bf16_f32 v86, v60, v61\n\tv_cvt_pk_bf16_f32 v87, v62, v63\n\tbuffer_store_dwordx4 v[80:83], v1, s[8:11], s25 offen offset:0 sc1\n\t"
      "buffer_store_dwordx4 v[84:87], v1, s[8:11], s25 offen offset:512 sc1\n\tv_mov_b32 v4, s17\n\ts_waitcnt vmcnt(2)\n\tv_readlane_b32 s23, v5, 0\n\t"
      "v_readlane_b32 s48, v5, 1\n\ts_add_u32 s48, s48, 12\n\ts_min_u32 s23, s23, s48\n\ts_cmp_gt_u32 s23, s17\n\ts_cbranch_scc0 47f\n\t"
      "buffer_load_dwordx4 v[96:99], v0, s[8:11], s19 offen offset:0 sc1\n\tbuffer_load_dwordx4 v[100:103], v0, s[8:11], s19 offen offset:1024 sc1\n\t"
      "buffer_load_dwordx4 v[104:107], v0, s[8:11], s19 offen offset:2048 sc1\n\tbuffer_load_dwordx4 v[108:111], v0, s[8:11], s19 offen offset:3072 sc1\n\t"
      "buffer_load_dwordx4 v[112:115], v0, s[8:11], s24 offen offset:0 sc1\n\tbuffer_load_dwordx4 v[116:119], v0, s[8:11], s24 offen offset:1024 sc1\n\t"
      "buffer_load_dwordx4 v[120:123], v0, s[8:11], s24 offen offset:2048 sc1\n\tbuffer_load_dwordx4 v[124:127], v0, s[8:11], s24 offen offset:3072 sc1\n\t"
      "s_waitcnt vmcnt(8)\n\tbuffer_store_dword v4, v3, s[12:15], s22 offen sc1\n\ts_waitcnt vmcnt(1)\n\ts_branch 12b\n\t"
      "47:\n\ts_waitcnt vmcnt(0)\n\tbuffer_store_dword v4, v3, s[12:15], s22 offen sc1\n\ts_cmp_eq_u32 s17, 64\n\ts_cbranch_scc1 50f\n\t"
      "43:\n\t"
      "45:\n\ts_cmp_gt_u32 s23, s17\n\ts_cbranch_scc1 46f\n\tbuffer_load_dword v5, v6, s[12:15], 0 offen sc1\n\ts_waitcnt vmcnt(0)\n\tv_readlane_b32 s23, v5, 0\n\t"
      "v_readlane_b32 s48, v5, 1\n\ts_add_u32 s48, s48, 12\n\ts_min_u32 s23, s23, s48\n\ts_cmp_eq_u32 s18, 0\n\ts_cbranch_scc1 46f\n\ts_sub_u32 s18, s18, 1\n\ts_sleep 1\n\t"
      "s_branch 45b\n\t"
      "46:\n\ts_add_u32 s24, s19, 0x1000\n\tbuffer_load_dwordx4 v[96:99], v0, s[8:11], s19 offen offset:0 sc1\n\t"
      "buffer_load_dwordx4 v[100:103], v0, s[8:11], s19 offen offset:1024 sc1\n\tbuffer_load_dwordx4 v[104:107], v0, s[8:11], s19 offen offset:2048 sc1\n\t"
      "buffer_load_dwordx4 v[108:111], v0, s[8:11], s19 offen offset:3072 sc1\n\tbuffer_load_dwordx4 v[112:115], v0, s[8:11], s24 offen offset:0 sc1\n\t"
      "buffer_load_dwordx4 v[116:119], v0, s[8:11], s24 offen offset:1024 sc1\n\tbuffer_load_dwordx4 v[120:123], v0, s[8:11], s24 offen offset:2048 sc1\n\t"
      "buffer_load_dwordx4 v[124:127], v0, s[8:11], s24 offen offset:3072 sc1\n\ts_waitcnt vmcnt(0)\n\ts_branch 12b\n\t"
      "50:\n\t"
      STF("s_memrealtime vcc\n\ts_waitcnt lgkmcnt(0)\n\tv_mov_b32 v95, vcc_lo\n\ts_lshl_b32 vcc_hi, s17, 2\n\ts_add_u32 vcc_hi, vcc_hi, 161052\n\tv_mov_b32 v94, vcc_hi\n\tds_write_b32 v94, v95\n\ts_memtime vcc\n\ts_waitcnt lgkmcnt(0)\n\tv_mov_b32 v95, vcc_lo\n\tds_write_b32 v94, v95 offset:400\n\ts_waitcnt lgkmcnt(0)\n\t")
      "s_waitcnt vmcnt(0) lgkmcnt(0)\n\t"
      TKS("s_mul_i32 s48, s16, 72\n\ts_add_u32 s48, s48, 162080\n\tv_mov_b32 v224, s48\n\tds_read_b32 v225, v224 offset:0\n\ts_waitcnt lgkmcnt(0)\n\tv_readfirstlane_b32 s74, v225\n\tds_read_b32 v225, v224 offset:4\n\ts_waitcnt lgkmcnt(0)\n\tv_readfirstlane_b32 s75, v225\n\tds_read_b32 v225, v224 offset:8\n\ts_waitcnt lgkmcnt(0)\n\tv_readfirstlane_b32 s76, v225\n\tds_read_b32 v225, v224 offset:12\n\ts_waitcnt lgkmcnt(0)\n\tv_readfirstlane_b32 s77, v225\n\tds_read_b32 v225, v224 offset:16\n\ts_waitcnt lgkmcnt(0)\n\tv_readfirstlane_b32 s78, v225\n\tds_read_b32 v225, v224 offset:20\n\ts_waitcnt lgkmcnt(0)\n\tv_readfirstlane_b32 s79, v225\n\tds_read_b32 v225, v224 offset:24\n\ts_waitcnt lgkmcnt(0)\n\tv_readfirstlane_b32 s80, v225\n\tds_read_b32 v225, v224 offset:28\n\ts_waitcnt lgkmcnt(0)\n\tv_readfirstlane_b32 s81, v225\n\tds_read_b32 v225, v224 offset:32\n\ts_waitcnt lgkmcnt(0)\n\tv_readfirstlane_b32 s82, v225\n\tds_read_b32 v225, v224 offset:36\n\ts_waitcnt lgkmcnt(0)\n\tv_readfirstlane_b32 s83, v225\n\tds_read_b32 v225, v224 offset:40\n\ts_waitcnt lgkmcnt(0)\n\tv_readfirstlane_b32 s84, v225\n\tds_read_b32 v225, v224 offset:44\n\ts_waitcnt lgkmcnt(0)\n\tv_readfirstlane_b32 s85, v225\n\tds_read_b32 v225, v224 offset:48\n\ts_waitcnt lgkmcnt(0)\n\tv_readfirstlane_b32 s86, v225\n\tds_read_b32 v225, v224 offset:52\n\ts_waitcnt lgkmcnt(0)\n\tv_readfirstlane_b32 s87, v225\n\tds_read_b32 v225, v224 offset:56\n\ts_waitcnt lgkmcnt(0)\n\tv_readfirstlane_b32 s88, v225\n\tds_read_b32 v225, v224 offset:60\n\ts_waitcnt lgkmcnt(0)\n\tv_readfirstlane_b32 s89, v225\n\tds_read_b32 v225, v224 offset:64\n\ts_waitcnt lgkmcnt(0)\n\tv_readfirstlane_b32 s90, v225\n\tds_read_b32 v225, v224 offset:68\n\ts_waitcnt lgkmcnt(0)\n\tv_readfirstlane_b32 s91, v225\n\t")
      STS("s_lshl_b32 s48, s16, 2\n\ts_add_u32 s48, s48, 162944\n\tv_mov_b32 v0, s48\n\tv_mov_b32 v1, s50\n\tds_write_b32 v0, v1\n\ts_cmp_lg_u32 s16, 0\n\ts_cbranch_scc1 97f\n\tv_mov_b32 v1, s51\n\tds_write_b32 v0, v1 offset:16\n\t97:\n\ts_cmp_lg_u32 s16, 0\n\ts_cbranch_scc1 95f\n\tv_mov_b32 v1, s61\n\tds_write_b32 v0, v1 offset:20\n\tv_mov_b32 v1, s21\n\tds_write_b32 v0, v1 offset:24\n\tv_mov_b32 v1, s22\n\tds_write_b32 v0, v1 offset:28\n\t95:\n\t")
      "s_cmp_lg_u32 s18, 0\n\ts_cbranch_scc1 96f\n\tv_mov_b32 v1, 1\n\tbuffer_store_dword v1, off, s[12:15], 0 offset:3328 sc1\n\t"
      "96:\n\t"
      : "+{v[0:31]}"(k), "+{v[160:191]}"(s0), "+{v[192:223]}"(s1), ALL_REGS :: "memory", "vcc", "scc", S_CLOB TK_CLOB);
}

DEV void conv_role(Ctx& c, int cg) {
  const KArg p = enter(c);
  constexpr int CH = 96 * 80, U8 = 0;
  constexpr int CP = 4160, FC = 8 * CH;
  static_assert(FC + 16 * CP <= 160000, "lds");
  c.budget = SPIN_CAP;
  const int w = c.w, lane = c.lane = fresh_lane(), tid = w * 64 + lane;
  const int d0 = 8 * cg + 2 * w;
  const int de = (cg >> 3) * 64 + (tid & 7) * 8, t0 = (cg & 7) * 256 + (tid >> 3) * 8;
  auto group_sync = [&](int phase) {
    gu32* cnt = c.ctrl + (C_CONV + (cg >> 3) * 16 + phase * 8) / 4;
    VMWAIT(0);
    BAR();
    if (w == 0) {
      if (lane == 0) __hip_atomic_fetch_add(cnt, 1u, RLX_AGENT);
#pragma unroll 1
      while (__builtin_amdgcn_readfirstlane(__hip_atomic_load(cnt, RLX_AGENT)) < 8u) { if (spend(c)) break; __builtin_amdgcn_s_sleep(4); }
    }
    BAR();
  };
  BAR();
  v4u fd[2][4], qr[8]; v2u fp[2][4];
  auto build = [&](int k) {
#pragma unroll
    for (int i = 0; i < 4; ++i) {
      const v4u d = fd[k][i]; const v2u q = fp[k][i];
      const unsigned o0 = __builtin_amdgcn_alignbyte(q.y, q.x, 2), o1 = __builtin_amdgcn_alignbyte(d.x, q.y, 2), o2 = __builtin_amdgcn_alignbyte(d.y, d.x, 2),
                     o3 = __builtin_amdgcn_alignbyte(d.z, d.y, 2), o4 = __builtin_amdgcn_alignbyte(d.w, d.z, 2);
      const int at = FC + 4 * w * CP + 64 + i * 1024 + lane * 16;
      sts16(c.L, at, d); sts16(c.L, at + CP, v4u{o1, o2, o3, o4}); sts16(c.L, at + 2 * CP, v4u{q.y, d.x, d.y, d.z}); sts16(c.L, at + 3 * CP, v4u{o0, o1, o2, o3});
    }
  };
  {
    const rsrc_t rk = mkrs(p->bk, 2048 * 2048), rv = mkrs(p->bv, 2048 * 2048);
    const rsrc_t rq = mkrs(p->bq, 2048 * 2048);
    v4u kr[11], vr[8];
#pragma unroll
    for (int i = 0; i < 11; ++i) kr[i] = ld16(rk, (t0 + i - 3) * 2048 + de * 2, 0);
#pragma unroll
    for (int i = 0; i < 8; ++i) vr[i] = ld16n(rv, (t0 + i) * 2048 + de * 2, 0);
#pragma unroll
    for (int i = 0; i < 8; ++i) qr[i] = ld16n(rq, (t0 + i) * 2048 + de * 2, 0);
    unsigned warm = 0;
    if ((cg & 31) < 16) {
      const int n = (cg & 15) * 128 + lane, zc = (cg >> 5) * 256 + 192;
      const rsrc_t rz = mkrs(w == 0 ? p->bk : w == 1 ? p->bv : w == 2 ? p->bq : p->ssmk, 2048 * 2048);
#pragma unroll
      for (int i = 0; i < 2; ++i) warm += __builtin_amdgcn_raw_buffer_load_b32(rz, w < 3 ? (n + 64 * i) * 2048 + zc * 2 : zc * 4096 + (n + 64 * i) * 128, 0, 0);
    }
    v4u sh[4];
#pragma unroll
    for (int i = 0; i < 4; ++i) sh[i] = *(const GAS v4u*)(p->shift + de * 4 + i * 8);
#pragma unroll
    for (int k = 0; k < 2; ++k) {
      const rsrc_t rf = mkrs(p->ssmk + (d0 + k) * 2048, 4096);
#pragma unroll
      for (int i = 0; i < 4; ++i) { fd[k][i] = ld16(rf, i * 1024 + lane * 16, 0); fp[k][i] = ld8(rf, i * 1024 + lane * 16 - 8, 0); }
    }
#pragma unroll
    for (int i = 0; i < 5; ++i) { const int z = i * 64 + lane; sts16(c.L, U8 + (2 * w + z / 160) * CH + (z % 160) * 16, v4u{0, 0, 0, 0}); }
    if (lane < 16) sts16(c.L, FC + (4 * w + (lane >> 2)) * CP + (lane & 3) * 16, v4u{0, 0, 0, 0});
#pragma unroll
    for (int ch = 0; ch < 8; ++ch) {
      const unsigned sa = sh[ch >> 1][(ch & 1) * 2], sb = sh[ch >> 1][(ch & 1) * 2 + 1];
      const float s0 = blo(sa), s1 = bhi(sa), s2 = blo(sb), s3 = bhi(sb);
      float kk[11];
      v4u o;
#pragma unroll
      for (int i = 0; i < 11; ++i) kk[i] = (ch & 1) ? bhi(kr[i][ch >> 1]) : blo(kr[i][ch >> 1]);
#pragma unroll
      for (int i = 0; i < 8; i += 2) {
        f32x2 a = rb2(f32x2{kk[i], kk[i + 1]} * s3);
        a = rb2(a + rb2(f32x2{kk[i + 1], kk[i + 2]} * s2));
        a = rb2(a + rb2(f32x2{kk[i + 2], kk[i + 3]} * s1));
        a = rb2(a + rb2(f32x2{kk[i + 3], kk[i + 4 < 11 ? i + 4 : 10]} * s0));
        a *= (ch & 1) ? f32x2{bhi(vr[i][ch >> 1]), bhi(vr[i + 1][ch >> 1])} : f32x2{blo(vr[i][ch >> 1]), blo(vr[i + 1][ch >> 1])};
        o[i >> 1] = pk(a.x, a.y);
      }
      st16s(c.rs, de * 4096 + t0 * 2, R_KVT + ch * 4096, o);
    }
    asm volatile("" :: "v"(warm));
    build(0);
    group_sync(0);
    v4u u[8];
#pragma unroll
    for (int i = 0; i < 8; ++i) u[i] = ld16s(c.rs, (i & 3) * 1024 + lane * 16, R_KVT + (d0 + (i >> 2)) * 4096);
#pragma unroll
    for (int i = 0; i < 8; ++i) sts16(c.L, U8 + (2 * w + (i >> 2)) * CH + (32 + (i & 3) * 16 + (lane >> 2)) * 80 + (lane & 3) * 16, u[i]);
  }
  STAMP(5);
  const int h2 = lane >> 5, r = lane & 31;
  const unsigned dd = *(const GAS unsigned*)(p->ssmD + d0);
#pragma unroll
  for (int k = 0; k < 2; ++k) {
    if (k == 1) build(1);
    f32x16 acc[2]; v32u x0, x1;
    const int ub = U8 + (2 * w + k) * CH;
    asm volatile("s_waitcnt lgkmcnt(0)\n\t"
      "ds_read_b64_tr_b16 v[160:161], %[fa] offset:40\n\tds_read_b64_tr_b16 v[162:163], %[fa] offset:32\n\tds_read_b128 v[168:171], %[ua] offset:2480\n\t"
      "ds_read_b128 v[172:175], %[ua] offset:5040\n\tds_read_b64_tr_b16 v[164:165], %[fa] offset:2088\n\tds_read_b64_tr_b16 v[166:167], %[fa] offset:2080\n\t"
      "ds_read_b64_tr_b16 v[176:177], %[fa] offset:8\n\tds_read_b64_tr_b16 v[178:179], %[fa] offset:0\n\tds_read_b128 v[184:187], %[ua] offset:2512\n\t"
      "ds_read_b128 v[188:191], %[ua] offset:5072\n\tds_read_b64_tr_b16 v[180:181], %[fa] offset:2056\n\tds_read_b64_tr_b16 v[182:183], %[fa] offset:2048\n\t"
      "s_waitcnt lgkmcnt(6)\n\tv_mfma_f32_32x32x16_bf16 v[128:143], v[160:163], v[168:171], 0\n\tds_read_b64_tr_b16 v[192:193], %[fa] offset:104\n\t"
      "ds_read_b64_tr_b16 v[194:195], %[fa] offset:96\n\tv_mfma_f32_32x32x16_bf16 v[144:159], v[160:163], v[172:175], 0\n\tds_read_b128 v[200:203], %[ua] offset:2400\n\t"
      "ds_read_b128 v[204:207], %[ua] offset:4960\n\tv_mfma_f32_32x32x16_bf16 v[144:159], v[164:167], v[168:171], v[144:159]\n\t"
      "ds_read_b64_tr_b16 v[196:197], %[fa] offset:2152\n\tds_read_b64_tr_b16 v[198:199], %[fa] offset:2144\n\ts_waitcnt lgkmcnt(6)\n\t"
      "v_mfma_f32_32x32x16_bf16 v[128:143], v[176:179], v[184:187], v[128:143]\n\tds_read_b64_tr_b16 v[208:209], %[fa] offset:72\n\t"
      "ds_read_b64_tr_b16 v[210:211], %[fa] offset:64\n\tv_mfma_f32_32x32x16_bf16 v[144:159], v[176:179], v[188:191], v[144:159]\n\t"
      "ds_read_b128 v[216:219], %[ua] offset:2432\n\tds_read_b128 v[220:223], %[ua] offset:4992\n\tv_mfma_f32_32x32x16_bf16 v[144:159], v[180:183], v[184:187], v[144:159]\n\t"
      "ds_read_b64_tr_b16 v[212:213], %[fa] offset:2120\n\tds_read_b64_tr_b16 v[214:215], %[fa] offset:2112\n\ts_waitcnt lgkmcnt(6)\n\t"
      "v_mfma_f32_32x32x16_bf16 v[128:143], v[192:195], v[200:203], v[128:143]\n\tds_read_b64_tr_b16 v[160:161], %[fa] offset:168\n\t"
      "ds_read_b64_tr_b16 v[162:163], %[fa] offset:160\n\tv_mfma_f32_32x32x16_bf16 v[144:159], v[192:195], v[204:207], v[144:159]\n\t"
      "ds_read_b128 v[168:171], %[ua] offset:2320\n\tds_read_b128 v[172:175], %[ua] offset:4880\n\tv_mfma_f32_32x32x16_bf16 v[144:159], v[196:199], v[200:203], v[144:159]\n\t"
      "ds_read_b64_tr_b16 v[164:165], %[fa] offset:2216\n\tds_read_b64_tr_b16 v[166:167], %[fa] offset:2208\n\ts_waitcnt lgkmcnt(6)\n\t"
      "v_mfma_f32_32x32x16_bf16 v[128:143], v[208:211], v[216:219], v[128:143]\n\tds_read_b64_tr_b16 v[176:177], %[fa] offset:136\n\t"
      "ds_read_b64_tr_b16 v[178:179], %[fa] offset:128\n\tv_mfma_f32_32x32x16_bf16 v[144:159], v[208:211], v[220:223], v[144:159]\n\t"
      "ds_read_b128 v[184:187], %[ua] offset:2352\n\tds_read_b128 v[188:191], %[ua] offset:4912\n\tv_mfma_f32_32x32x16_bf16 v[144:159], v[212:215], v[216:219], v[144:159]\n\t"
      "ds_read_b64_tr_b16 v[180:181], %[fa] offset:2184\n\tds_read_b64_tr_b16 v[182:183], %[fa] offset:2176\n\ts_waitcnt lgkmcnt(6)\n\t"
      "v_mfma_f32_32x32x16_bf16 v[128:143], v[160:163], v[168:171], v[128:143]\n\tds_read_b64_tr_b16 v[192:193], %[fa] offset:232\n\t"
      "ds_read_b64_tr_b16 v[194:195], %[fa] offset:224\n\tv_mfma_f32_32x32x16_bf16 v[144:159], v[160:163], v[172:175], v[144:159]\n\t"
      "ds_read_b128 v[200:203], %[ua] offset:2240\n\tds_read_b128 v[204:207], %[ua] offset:4800\n\tv_mfma_f32_32x32x16_bf16 v[144:159], v[164:167], v[168:171], v[144:159]\n\t"
      "ds_read_b64_tr_b16 v[196:197], %[fa] offset:2280\n\tds_read_b64_tr_b16 v[198:199], %[fa] offset:2272\n\ts_waitcnt lgkmcnt(6)\n\t"
      "v_mfma_f32_32x32x16_bf16 v[128:143], v[176:179], v[184:187], v[128:143]\n\tds_read_b64_tr_b16 v[208:209], %[fa] offset:200\n\t"
      "ds_read_b64_tr_b16 v[210:211], %[fa] offset:192\n\tv_mfma_f32_32x32x16_bf16 v[144:159], v[176:179], v[188:191], v[144:159]\n\t"
      "ds_read_b128 v[216:219], %[ua] offset:2272\n\tds_read_b128 v[220:223], %[ua] offset:4832\n\tv_mfma_f32_32x32x16_bf16 v[144:159], v[180:183], v[184:187], v[144:159]\n\t"
      "ds_read_b64_tr_b16 v[212:213], %[fa] offset:2248\n\tds_read_b64_tr_b16 v[214:215], %[fa] offset:2240\n\ts_waitcnt lgkmcnt(6)\n\t"
      "v_mfma_f32_32x32x16_bf16 v[128:143], v[192:195], v[200:203], v[128:143]\n\tds_read_b64_tr_b16 v[160:161], %[fa] offset:296\n\t"
      "ds_read_b64_tr_b16 v[162:163], %[fa] offset:288\n\tv_mfma_f32_32x32x16_bf16 v[144:159], v[192:195], v[204:207], v[144:159]\n\t"
      "ds_read_b128 v[168:171], %[ua] offset:2160\n\tds_read_b128 v[172:175], %[ua] offset:4720\n\tv_mfma_f32_32x32x16_bf16 v[144:159], v[196:199], v[200:203], v[144:159]\n\t"
      "ds_read_b64_tr_b16 v[164:165], %[fa] offset:2344\n\tds_read_b64_tr_b16 v[166:167], %[fa] offset:2336\n\ts_waitcnt lgkmcnt(6)\n\t"
      "v_mfma_f32_32x32x16_bf16 v[128:143], v[208:211], v[216:219], v[128:143]\n\tds_read_b64_tr_b16 v[176:177], %[fa] offset:264\n\t"
      "ds_read_b64_tr_b16 v[178:179], %[fa] offset:256\n\tv_mfma_f32_32x32x16_bf16 v[144:159], v[208:211], v[220:223], v[144:159]\n\t"
      "ds_read_b128 v[184:187], %[ua] offset:2192\n\tds_read_b128 v[188:191], %[ua] offset:4752\n\tv_mfma_f32_32x32x16_bf16 v[144:159], v[212:215], v[216:219], v[144:159]\n\t"
      "ds_read_b64_tr_b16 v[180:181], %[fa] offset:2312\n\tds_read_b64_tr_b16 v[182:183], %[fa] offset:2304\n\ts_waitcnt lgkmcnt(6)\n\t"
      "v_mfma_f32_32x32x16_bf16 v[128:143], v[160:163], v[168:171], v[128:143]\n\tds_read_b64_tr_b16 v[192:193], %[fa] offset:360\n\t"
      "ds_read_b64_tr_b16 v[194:195], %[fa] offset:352\n\tv_mfma_f32_32x32x16_bf16 v[144:159], v[160:163], v[172:175], v[144:159]\n\t"
      "ds_read_b128 v[200:203], %[ua] offset:2080\n\tds_read_b128 v[204:207], %[ua] offset:4640\n\tv_mfma_f32_32x32x16_bf16 v[144:159], v[164:167], v[168:171], v[144:159]\n\t"
      "ds_read_b64_tr_b16 v[196:197], %[fa] offset:2408\n\tds_read_b64_tr_b16 v[198:199], %[fa] offset:2400\n\ts_waitcnt lgkmcnt(6)\n\t"
      "v_mfma_f32_32x32x16_bf16 v[128:143], v[176:179], v[184:187], v[128:143]\n\tds_read_b64_tr_b16 v[208:209], %[fa] offset:328\n\t"
      "ds_read_b64_tr_b16 v[210:211], %[fa] offset:320\n\tv_mfma_f32_32x32x16_bf16 v[144:159], v[176:179], v[188:191], v[144:159]\n\t"
      "ds_read_b128 v[216:219], %[ua] offset:2112\n\tds_read_b128 v[220:223], %[ua] offset:4672\n\tv_mfma_f32_32x32x16_bf16 v[144:159], v[180:183], v[184:187], v[144:159]\n\t"
      "ds_read_b64_tr_b16 v[212:213], %[fa] offset:2376\n\tds_read_b64_tr_b16 v[214:215], %[fa] offset:2368\n\ts_waitcnt lgkmcnt(6)\n\t"
      "v_mfma_f32_32x32x16_bf16 v[128:143], v[192:195], v[200:203], v[128:143]\n\tds_read_b64_tr_b16 v[160:161], %[fa] offset:424\n\t"
      "ds_read_b64_tr_b16 v[162:163], %[fa] offset:416\n\tv_mfma_f32_32x32x16_bf16 v[144:159], v[192:195], v[204:207], v[144:159]\n\t"
      "ds_read_b128 v[168:171], %[ua] offset:2000\n\tds_read_b128 v[172:175], %[ua] offset:4560\n\tv_mfma_f32_32x32x16_bf16 v[144:159], v[196:199], v[200:203], v[144:159]\n\t"
      "ds_read_b64_tr_b16 v[164:165], %[fa] offset:2472\n\tds_read_b64_tr_b16 v[166:167], %[fa] offset:2464\n\ts_waitcnt lgkmcnt(6)\n\t"
      "v_mfma_f32_32x32x16_bf16 v[128:143], v[208:211], v[216:219], v[128:143]\n\tds_read_b64_tr_b16 v[176:177], %[fa] offset:392\n\t"
      "ds_read_b64_tr_b16 v[178:179], %[fa] offset:384\n\tv_mfma_f32_32x32x16_bf16 v[144:159], v[208:211], v[220:223], v[144:159]\n\t"
      "ds_read_b128 v[184:187], %[ua] offset:2032\n\tds_read_b128 v[188:191], %[ua] offset:4592\n\tv_mfma_f32_32x32x16_bf16 v[144:159], v[212:215], v[216:219], v[144:159]\n\t"
      "ds_read_b64_tr_b16 v[180:181], %[fa] offset:2440\n\tds_read_b64_tr_b16 v[182:183], %[fa] offset:2432\n\ts_waitcnt lgkmcnt(6)\n\t"
      "v_mfma_f32_32x32x16_bf16 v[128:143], v[160:163], v[168:171], v[128:143]\n\tds_read_b64_tr_b16 v[192:193], %[fa] offset:488\n\t"
      "ds_read_b64_tr_b16 v[194:195], %[fa] offset:480\n\tv_mfma_f32_32x32x16_bf16 v[144:159], v[160:163], v[172:175], v[144:159]\n\t"
      "ds_read_b128 v[200:203], %[ua] offset:1920\n\tds_read_b128 v[204:207], %[ua] offset:4480\n\tv_mfma_f32_32x32x16_bf16 v[144:159], v[164:167], v[168:171], v[144:159]\n\t"
      "ds_read_b64_tr_b16 v[196:197], %[fa] offset:2536\n\tds_read_b64_tr_b16 v[198:199], %[fa] offset:2528\n\ts_waitcnt lgkmcnt(6)\n\t"
      "v_mfma_f32_32x32x16_bf16 v[128:143], v[176:179], v[184:187], v[128:143]\n\tds_read_b64_tr_b16 v[208:209], %[fa] offset:456\n\t"
      "ds_read_b64_tr_b16 v[210:211], %[fa] offset:448\n\tv_mfma_f32_32x32x16_bf16 v[144:159], v[176:179], v[188:191], v[144:159]\n\t"
      "ds_read_b128 v[216:219], %[ua] offset:1952\n\tds_read_b128 v[220:223], %[ua] offset:4512\n\tv_mfma_f32_32x32x16_bf16 v[144:159], v[180:183], v[184:187], v[144:159]\n\t"
      "ds_read_b64_tr_b16 v[212:213], %[fa] offset:2504\n\tds_read_b64_tr_b16 v[214:215], %[fa] offset:2496\n\ts_waitcnt lgkmcnt(6)\n\t"
      "v_mfma_f32_32x32x16_bf16 v[128:143], v[192:195], v[200:203], v[128:143]\n\tds_read_b64_tr_b16 v[160:161], %[fa] offset:552\n\t"
      "ds_read_b64_tr_b16 v[162:163], %[fa] offset:544\n\tv_mfma_f32_32x32x16_bf16 v[144:159], v[192:195], v[204:207], v[144:159]\n\t"
      "ds_read_b128 v[168:171], %[ua] offset:1840\n\tds_read_b128 v[172:175], %[ua] offset:4400\n\tv_mfma_f32_32x32x16_bf16 v[144:159], v[196:199], v[200:203], v[144:159]\n\t"
      "ds_read_b64_tr_b16 v[164:165], %[fa] offset:2600\n\tds_read_b64_tr_b16 v[166:167], %[fa] offset:2592\n\ts_waitcnt lgkmcnt(6)\n\t"
      "v_mfma_f32_32x32x16_bf16 v[128:143], v[208:211], v[216:219], v[128:143]\n\tds_read_b64_tr_b16 v[176:177], %[fa] offset:520\n\t"
      "ds_read_b64_tr_b16 v[178:179], %[fa] offset:512\n\tv_mfma_f32_32x32x16_bf16 v[144:159], v[208:211], v[220:223], v[144:159]\n\t"
      "ds_read_b128 v[184:187], %[ua] offset:1872\n\tds_read_b128 v[188:191], %[ua] offset:4432\n\tv_mfma_f32_32x32x16_bf16 v[144:159], v[212:215], v[216:219], v[144:159]\n\t"
      "ds_read_b64_tr_b16 v[180:181], %[fa] offset:2568\n\tds_read_b64_tr_b16 v[182:183], %[fa] offset:2560\n\ts_waitcnt lgkmcnt(6)\n\t"
      "v_mfma_f32_32x32x16_bf16 v[128:143], v[160:163], v[168:171], v[128:143]\n\tds_read_b64_tr_b16 v[192:193], %[fa] offset:616\n\t"
      "ds_read_b64_tr_b16 v[194:195], %[fa] offset:608\n\tv_mfma_f32_32x32x16_bf16 v[144:159], v[160:163], v[172:175], v[144:159]\n\t"
      "ds_read_b128 v[200:203], %[ua] offset:1760\n\tds_read_b128 v[204:207], %[ua] offset:4320\n\tv_mfma_f32_32x32x16_bf16 v[144:159], v[164:167], v[168:171], v[144:159]\n\t"
      "ds_read_b64_tr_b16 v[196:197], %[fa] offset:2664\n\tds_read_b64_tr_b16 v[198:199], %[fa] offset:2656\n\ts_waitcnt lgkmcnt(6)\n\t"
      "v_mfma_f32_32x32x16_bf16 v[128:143], v[176:179], v[184:187], v[128:143]\n\tds_read_b64_tr_b16 v[208:209], %[fa] offset:584\n\t"
      "ds_read_b64_tr_b16 v[210:211], %[fa] offset:576\n\tv_mfma_f32_32x32x16_bf16 v[144:159], v[176:179], v[188:191], v[144:159]\n\t"
      "ds_read_b128 v[216:219], %[ua] offset:1792\n\tds_read_b128 v[220:223], %[ua] offset:4352\n\tv_mfma_f32_32x32x16_bf16 v[144:159], v[180:183], v[184:187], v[144:159]\n\t"
      "ds_read_b64_tr_b16 v[212:213], %[fa] offset:2632\n\tds_read_b64_tr_b16 v[214:215], %[fa] offset:2624\n\ts_waitcnt lgkmcnt(6)\n\t"
      "v_mfma_f32_32x32x16_bf16 v[128:143], v[192:195], v[200:203], v[128:143]\n\tds_read_b64_tr_b16 v[160:161], %[fa] offset:680\n\t"
      "ds_read_b64_tr_b16 v[162:163], %[fa] offset:672\n\tv_mfma_f32_32x32x16_bf16 v[144:159], v[192:195], v[204:207], v[144:159]\n\t"
      "ds_read_b128 v[168:171], %[ua] offset:1680\n\tds_read_b128 v[172:175], %[ua] offset:4240\n\tv_mfma_f32_32x32x16_bf16 v[144:159], v[196:199], v[200:203], v[144:159]\n\t"
      "ds_read_b64_tr_b16 v[164:165], %[fa] offset:2728\n\tds_read_b64_tr_b16 v[166:167], %[fa] offset:2720\n\ts_waitcnt lgkmcnt(6)\n\t"
      "v_mfma_f32_32x32x16_bf16 v[128:143], v[208:211], v[216:219], v[128:143]\n\tds_read_b64_tr_b16 v[176:177], %[fa] offset:648\n\t"
      "ds_read_b64_tr_b16 v[178:179], %[fa] offset:640\n\tv_mfma_f32_32x32x16_bf16 v[144:159], v[208:211], v[220:223], v[144:159]\n\t"
      "ds_read_b128 v[184:187], %[ua] offset:1712\n\tds_read_b128 v[188:191], %[ua] offset:4272\n\tv_mfma_f32_32x32x16_bf16 v[144:159], v[212:215], v[216:219], v[144:159]\n\t"
      "ds_read_b64_tr_b16 v[180:181], %[fa] offset:2696\n\tds_read_b64_tr_b16 v[182:183], %[fa] offset:2688\n\ts_waitcnt lgkmcnt(6)\n\t"
      "v_mfma_f32_32x32x16_bf16 v[128:143], v[160:163], v[168:171], v[128:143]\n\tds_read_b64_tr_b16 v[192:193], %[fa] offset:744\n\t"
      "ds_read_b64_tr_b16 v[194:195], %[fa] offset:736\n\tv_mfma_f32_32x32x16_bf16 v[144:159], v[160:163], v[172:175], v[144:159]\n\t"
      "ds_read_b128 v[200:203], %[ua] offset:1600\n\tds_read_b128 v[204:207], %[ua] offset:4160\n\tv_mfma_f32_32x32x16_bf16 v[144:159], v[164:167], v[168:171], v[144:159]\n\t"
      "ds_read_b64_tr_b16 v[196:197], %[fa] offset:2792\n\tds_read_b64_tr_b16 v[198:199], %[fa] offset:2784\n\ts_waitcnt lgkmcnt(6)\n\t"
      "v_mfma_f32_32x32x16_bf16 v[128:143], v[176:179], v[184:187], v[128:143]\n\tds_read_b64_tr_b16 v[208:209], %[fa] offset:712\n\t"
      "ds_read_b64_tr_b16 v[210:211], %[fa] offset:704\n\tv_mfma_f32_32x32x16_bf16 v[144:159], v[176:179], v[188:191], v[144:159]\n\t"
      "ds_read_b128 v[216:219], %[ua] offset:1632\n\tds_read_b128 v[220:223], %[ua] offset:4192\n\tv_mfma_f32_32x32x16_bf16 v[144:159], v[180:183], v[184:187], v[144:159]\n\t"
      "ds_read_b64_tr_b16 v[212:213], %[fa] offset:2760\n\tds_read_b64_tr_b16 v[214:215], %[fa] offset:2752\n\ts_waitcnt lgkmcnt(6)\n\t"
      "v_mfma_f32_32x32x16_bf16 v[128:143], v[192:195], v[200:203], v[128:143]\n\tds_read_b64_tr_b16 v[160:161], %[fa] offset:808\n\t"
      "ds_read_b64_tr_b16 v[162:163], %[fa] offset:800\n\tv_mfma_f32_32x32x16_bf16 v[144:159], v[192:195], v[204:207], v[144:159]\n\t"
      "ds_read_b128 v[168:171], %[ua] offset:1520\n\tds_read_b128 v[172:175], %[ua] offset:4080\n\tv_mfma_f32_32x32x16_bf16 v[144:159], v[196:199], v[200:203], v[144:159]\n\t"
      "ds_read_b64_tr_b16 v[164:165], %[fa] offset:2856\n\tds_read_b64_tr_b16 v[166:167], %[fa] offset:2848\n\ts_waitcnt lgkmcnt(6)\n\t"
      "v_mfma_f32_32x32x16_bf16 v[128:143], v[208:211], v[216:219], v[128:143]\n\tds_read_b64_tr_b16 v[176:177], %[fa] offset:776\n\t"
      "ds_read_b64_tr_b16 v[178:179], %[fa] offset:768\n\tv_mfma_f32_32x32x16_bf16 v[144:159], v[208:211], v[220:223], v[144:159]\n\t"
      "ds_read_b128 v[184:187], %[ua] offset:1552\n\tds_read_b128 v[188:191], %[ua] offset:4112\n\tv_mfma_f32_32x32x16_bf16 v[144:159], v[212:215], v[216:219], v[144:159]\n\t"
      "ds_read_b64_tr_b16 v[180:181], %[fa] offset:2824\n\tds_read_b64_tr_b16 v[182:183], %[fa] offset:2816\n\ts_waitcnt lgkmcnt(6)\n\t"
      "v_mfma_f32_32x32x16_bf16 v[128:143], v[160:163], v[168:171], v[128:143]\n\tds_read_b64_tr_b16 v[192:193], %[fa] offset:872\n\t"
      "ds_read_b64_tr_b16 v[194:195], %[fa] offset:864\n\tv_mfma_f32_32x32x16_bf16 v[144:159], v[160:163], v[172:175], v[144:159]\n\t"
      "ds_read_b128 v[200:203], %[ua] offset:1440\n\tds_read_b128 v[204:207], %[ua] offset:4000\n\tv_mfma_f32_32x32x16_bf16 v[144:159], v[164:167], v[168:171], v[144:159]\n\t"
      "ds_read_b64_tr_b16 v[196:197], %[fa] offset:2920\n\tds_read_b64_tr_b16 v[198:199], %[fa] offset:2912\n\ts_waitcnt lgkmcnt(6)\n\t"
      "v_mfma_f32_32x32x16_bf16 v[128:143], v[176:179], v[184:187], v[128:143]\n\tds_read_b64_tr_b16 v[208:209], %[fa] offset:840\n\t"
      "ds_read_b64_tr_b16 v[210:211], %[fa] offset:832\n\tv_mfma_f32_32x32x16_bf16 v[144:159], v[176:179], v[188:191], v[144:159]\n\t"
      "ds_read_b128 v[216:219], %[ua] offset:1472\n\tds_read_b128 v[220:223], %[ua] offset:4032\n\tv_mfma_f32_32x32x16_bf16 v[144:159], v[180:183], v[184:187], v[144:159]\n\t"
      "ds_read_b64_tr_b16 v[212:213], %[fa] offset:2888\n\tds_read_b64_tr_b16 v[214:215], %[fa] offset:2880\n\ts_waitcnt lgkmcnt(6)\n\t"
      "v_mfma_f32_32x32x16_bf16 v[128:143], v[192:195], v[200:203], v[128:143]\n\tds_read_b64_tr_b16 v[160:161], %[fa] offset:936\n\t"
      "ds_read_b64_tr_b16 v[162:163], %[fa] offset:928\n\tv_mfma_f32_32x32x16_bf16 v[144:159], v[192:195], v[204:207], v[144:159]\n\t"
      "ds_read_b128 v[168:171], %[ua] offset:1360\n\tds_read_b128 v[172:175], %[ua] offset:3920\n\tv_mfma_f32_32x32x16_bf16 v[144:159], v[196:199], v[200:203], v[144:159]\n\t"
      "ds_read_b64_tr_b16 v[164:165], %[fa] offset:2984\n\tds_read_b64_tr_b16 v[166:167], %[fa] offset:2976\n\ts_waitcnt lgkmcnt(6)\n\t"
      "v_mfma_f32_32x32x16_bf16 v[128:143], v[208:211], v[216:219], v[128:143]\n\tds_read_b64_tr_b16 v[176:177], %[fa] offset:904\n\t"
      "ds_read_b64_tr_b16 v[178:179], %[fa] offset:896\n\tv_mfma_f32_32x32x16_bf16 v[144:159], v[208:211], v[220:223], v[144:159]\n\t"
      "ds_read_b128 v[184:187], %[ua] offset:1392\n\tds_read_b128 v[188:191], %[ua] offset:3952\n\tv_mfma_f32_32x32x16_bf16 v[144:159], v[212:215], v[216:219], v[144:159]\n\t"
      "ds_read_b64_tr_b16 v[180:181], %[fa] offset:2952\n\tds_read_b64_tr_b16 v[182:183], %[fa] offset:2944\n\ts_waitcnt lgkmcnt(6)\n\t"
      "v_mfma_f32_32x32x16_bf16 v[128:143], v[160:163], v[168:171], v[128:143]\n\tds_read_b64_tr_b16 v[192:193], %[fa] offset:1000\n\t"
      "ds_read_b64_tr_b16 v[194:195], %[fa] offset:992\n\tv_mfma_f32_32x32x16_bf16 v[144:159], v[160:163], v[172:175], v[144:159]\n\t"
      "ds_read_b128 v[200:203], %[ua] offset:1280\n\tds_read_b128 v[204:207], %[ua] offset:3840\n\tv_mfma_f32_32x32x16_bf16 v[144:159], v[164:167], v[168:171], v[144:159]\n\t"
      "ds_read_b64_tr_b16 v[196:197], %[fa] offset:3048\n\tds_read_b64_tr_b16 v[198:199], %[fa] offset:3040\n\ts_waitcnt lgkmcnt(6)\n\t"
      "v_mfma_f32_32x32x16_bf16 v[128:143], v[176:179], v[184:187], v[128:143]\n\tds_read_b64_tr_b16 v[208:209], %[fa] offset:968\n\t"
      "ds_read_b64_tr_b16 v[210:211], %[fa] offset:960\n\tv_mfma_f32_32x32x16_bf16 v[144:159], v[176:179], v[188:191], v[144:159]\n\t"
      "ds_read_b128 v[216:219], %[ua] offset:1312\n\tds_read_b128 v[220:223], %[ua] offset:3872\n\tv_mfma_f32_32x32x16_bf16 v[144:159], v[180:183], v[184:187], v[144:159]\n\t"
      "ds_read_b64_tr_b16 v[212:213], %[fa] offset:3016\n\tds_read_b64_tr_b16 v[214:215], %[fa] offset:3008\n\ts_waitcnt lgkmcnt(6)\n\t"
      "v_mfma_f32_32x32x16_bf16 v[128:143], v[192:195], v[200:203], v[128:143]\n\tds_read_b64_tr_b16 v[160:161], %[fa] offset:1064\n\t"
      "ds_read_b64_tr_b16 v[162:163], %[fa] offset:1056\n\tv_mfma_f32_32x32x16_bf16 v[144:159], v[192:195], v[204:207], v[144:159]\n\t"
      "ds_read_b128 v[168:171], %[ua] offset:1200\n\tds_read_b128 v[172:175], %[ua] offset:3760\n\tv_mfma_f32_32x32x16_bf16 v[144:159], v[196:199], v[200:203], v[144:159]\n\t"
      "ds_read_b64_tr_b16 v[164:165], %[fa] offset:3112\n\tds_read_b64_tr_b16 v[166:167], %[fa] offset:3104\n\ts_waitcnt lgkmcnt(6)\n\t"
      "v_mfma_f32_32x32x16_bf16 v[128:143], v[208:211], v[216:219], v[128:143]\n\tds_read_b64_tr_b16 v[176:177], %[fa] offset:1032\n\t"
      "ds_read_b64_tr_b16 v[178:179], %[fa] offset:1024\n\tv_mfma_f32_32x32x16_bf16 v[144:159], v[208:211], v[220:223], v[144:159]\n\t"
      "ds_read_b128 v[184:187], %[ua] offset:1232\n\tds_read_b128 v[188:191], %[ua] offset:3792\n\tv_mfma_f32_32x32x16_bf16 v[144:159], v[212:215], v[216:219], v[144:159]\n\t"
      "ds_read_b64_tr_b16 v[180:181], %[fa] offset:3080\n\tds_read_b64_tr_b16 v[182:183], %[fa] offset:3072\n\ts_waitcnt lgkmcnt(6)\n\t"
      "v_mfma_f32_32x32x16_bf16 v[128:143], v[160:163], v[168:171], v[128:143]\n\tds_read_b64_tr_b16 v[192:193], %[fa] offset:1128\n\t"
      "ds_read_b64_tr_b16 v[194:195], %[fa] offset:1120\n\tv_mfma_f32_32x32x16_bf16 v[144:159], v[160:163], v[172:175], v[144:159]\n\t"
      "ds_read_b128 v[200:203], %[ua] offset:1120\n\tds_read_b128 v[204:207], %[ua] offset:3680\n\tv_mfma_f32_32x32x16_bf16 v[144:159], v[164:167], v[168:171], v[144:159]\n\t"
      "ds_read_b64_tr_b16 v[196:197], %[fa] offset:3176\n\tds_read_b64_tr_b16 v[198:199], %[fa] offset:3168\n\ts_waitcnt lgkmcnt(6)\n\t"
      "v_mfma_f32_32x32x16_bf16 v[128:143], v[176:179], v[184:187], v[128:143]\n\tds_read_b64_tr_b16 v[208:209], %[fa] offset:1096\n\t"
      "ds_read_b64_tr_b16 v[210:211], %[fa] offset:1088\n\tv_mfma_f32_32x32x16_bf16 v[144:159], v[176:179], v[188:191], v[144:159]\n\t"
      "ds_read_b128 v[216:219], %[ua] offset:1152\n\tds_read_b128 v[220:223], %[ua] offset:3712\n\tv_mfma_f32_32x32x16_bf16 v[144:159], v[180:183], v[184:187], v[144:159]\n\t"
      "ds_read_b64_tr_b16 v[212:213], %[fa] offset:3144\n\tds_read_b64_tr_b16 v[214:215], %[fa] offset:3136\n\ts_waitcnt lgkmcnt(6)\n\t"
      "v_mfma_f32_32x32x16_bf16 v[128:143], v[192:195], v[200:203], v[128:143]\n\tds_read_b64_tr_b16 v[160:161], %[fa] offset:1192\n\t"
      "ds_read_b64_tr_b16 v[162:163], %[fa] offset:1184\n\tv_mfma_f32_32x32x16_bf16 v[144:159], v[192:195], v[204:207], v[144:159]\n\t"
      "ds_read_b128 v[168:171], %[ua] offset:1040\n\tds_read_b128 v[172:175], %[ua] offset:3600\n\tv_mfma_f32_32x32x16_bf16 v[144:159], v[196:199], v[200:203], v[144:159]\n\t"
      "ds_read_b64_tr_b16 v[164:165], %[fa] offset:3240\n\tds_read_b64_tr_b16 v[166:167], %[fa] offset:3232\n\ts_waitcnt lgkmcnt(6)\n\t"
      "v_mfma_f32_32x32x16_bf16 v[128:143], v[208:211], v[216:219], v[128:143]\n\tds_read_b64_tr_b16 v[176:177], %[fa] offset:1160\n\t"
      "ds_read_b64_tr_b16 v[178:179], %[fa] offset:1152\n\tv_mfma_f32_32x32x16_bf16 v[144:159], v[208:211], v[220:223], v[144:159]\n\t"
      "ds_read_b128 v[184:187], %[ua] offset:1072\n\tds_read_b128 v[188:191], %[ua] offset:3632\n\tv_mfma_f32_32x32x16_bf16 v[144:159], v[212:215], v[216:219], v[144:159]\n\t"
      "ds_read_b64_tr_b16 v[180:181], %[fa] offset:3208\n\tds_read_b64_tr_b16 v[182:183], %[fa] offset:3200\n\ts_waitcnt lgkmcnt(6)\n\t"
      "v_mfma_f32_32x32x16_bf16 v[128:143], v[160:163], v[168:171], v[128:143]\n\tds_read_b64_tr_b16 v[192:193], %[fa] offset:1256\n\t"
      "ds_read_b64_tr_b16 v[194:195], %[fa] offset:1248\n\tv_mfma_f32_32x32x16_bf16 v[144:159], v[160:163], v[172:175], v[144:159]\n\t"
      "ds_read_b128 v[200:203], %[ua] offset:960\n\tds_read_b128 v[204:207], %[ua] offset:3520\n\tv_mfma_f32_32x32x16_bf16 v[144:159], v[164:167], v[168:171], v[144:159]\n\t"
      "ds_read_b64_tr_b16 v[196:197], %[fa] offset:3304\n\tds_read_b64_tr_b16 v[198:199], %[fa] offset:3296\n\ts_waitcnt lgkmcnt(6)\n\t"
      "v_mfma_f32_32x32x16_bf16 v[128:143], v[176:179], v[184:187], v[128:143]\n\tds_read_b64_tr_b16 v[208:209], %[fa] offset:1224\n\t"
      "ds_read_b64_tr_b16 v[210:211], %[fa] offset:1216\n\tv_mfma_f32_32x32x16_bf16 v[144:159], v[176:179], v[188:191], v[144:159]\n\t"
      "ds_read_b128 v[216:219], %[ua] offset:992\n\tds_read_b128 v[220:223], %[ua] offset:3552\n\tv_mfma_f32_32x32x16_bf16 v[144:159], v[180:183], v[184:187], v[144:159]\n\t"
      "ds_read_b64_tr_b16 v[212:213], %[fa] offset:3272\n\tds_read_b64_tr_b16 v[214:215], %[fa] offset:3264\n\ts_waitcnt lgkmcnt(6)\n\t"
      "v_mfma_f32_32x32x16_bf16 v[128:143], v[192:195], v[200:203], v[128:143]\n\tds_read_b64_tr_b16 v[160:161], %[fa] offset:1320\n\t"
      "ds_read_b64_tr_b16 v[162:163], %[fa] offset:1312\n\tv_mfma_f32_32x32x16_bf16 v[144:159], v[192:195], v[204:207], v[144:159]\n\t"
      "ds_read_b128 v[168:171], %[ua] offset:880\n\tds_read_b128 v[172:175], %[ua] offset:3440\n\tv_mfma_f32_32x32x16_bf16 v[144:159], v[196:199], v[200:203], v[144:159]\n\t"
      "ds_read_b64_tr_b16 v[164:165], %[fa] offset:3368\n\tds_read_b64_tr_b16 v[166:167], %[fa] offset:3360\n\ts_waitcnt lgkmcnt(6)\n\t"
      "v_mfma_f32_32x32x16_bf16 v[128:143], v[208:211], v[216:219], v[128:143]\n\tds_read_b64_tr_b16 v[176:177], %[fa] offset:1288\n\t"
      "ds_read_b64_tr_b16 v[178:179], %[fa] offset:1280\n\tv_mfma_f32_32x32x16_bf16 v[144:159], v[208:211], v[220:223], v[144:159]\n\t"
      "ds_read_b128 v[184:187], %[ua] offset:912\n\tds_read_b128 v[188:191], %[ua] offset:3472\n\tv_mfma_f32_32x32x16_bf16 v[144:159], v[212:215], v[216:219], v[144:159]\n\t"
      "ds_read_b64_tr_b16 v[180:181], %[fa] offset:3336\n\tds_read_b64_tr_b16 v[182:183], %[fa] offset:3328\n\ts_waitcnt lgkmcnt(6)\n\t"
      "v_mfma_f32_32x32x16_bf16 v[128:143], v[160:163], v[168:171], v[128:143]\n\tds_read_b64_tr_b16 v[192:193], %[fa] offset:1384\n\t"
      "ds_read_b64_tr_b16 v[194:195], %[fa] offset:1376\n\tv_mfma_f32_32x32x16_bf16 v[144:159], v[160:163], v[172:175], v[144:159]\n\t"
      "ds_read_b128 v[200:203], %[ua] offset:800\n\tds_read_b128 v[204:207], %[ua] offset:3360\n\tv_mfma_f32_32x32x16_bf16 v[144:159], v[164:167], v[168:171], v[144:159]\n\t"
      "ds_read_b64_tr_b16 v[196:197], %[fa] offset:3432\n\tds_read_b64_tr_b16 v[198:199], %[fa] offset:3424\n\ts_waitcnt lgkmcnt(6)\n\t"
      "v_mfma_f32_32x32x16_bf16 v[128:143], v[176:179], v[184:187], v[128:143]\n\tds_read_b64_tr_b16 v[208:209], %[fa] offset:1352\n\t"
      "ds_read_b64_tr_b16 v[210:211], %[fa] offset:1344\n\tv_mfma_f32_32x32x16_bf16 v[144:159], v[176:179], v[188:191], v[144:159]\n\t"
      "ds_read_b128 v[216:219], %[ua] offset:832\n\tds_read_b128 v[220:223], %[ua] offset:3392\n\tv_mfma_f32_32x32x16_bf16 v[144:159], v[180:183], v[184:187], v[144:159]\n\t"
      "ds_read_b64_tr_b16 v[212:213], %[fa] offset:3400\n\tds_read_b64_tr_b16 v[214:215], %[fa] offset:3392\n\ts_waitcnt lgkmcnt(6)\n\t"
      "v_mfma_f32_32x32x16_bf16 v[128:143], v[192:195], v[200:203], v[128:143]\n\tds_read_b64_tr_b16 v[160:161], %[fa] offset:1448\n\t"
      "ds_read_b64_tr_b16 v[162:163], %[fa] offset:1440\n\tv_mfma_f32_32x32x16_bf16 v[144:159], v[192:195], v[204:207], v[144:159]\n\t"
      "ds_read_b128 v[168:171], %[ua] offset:720\n\tds_read_b128 v[172:175], %[ua] offset:3280\n\tv_mfma_f32_32x32x16_bf16 v[144:159], v[196:199], v[200:203], v[144:159]\n\t"
      "ds_read_b64_tr_b16 v[164:165], %[fa] offset:3496\n\tds_read_b64_tr_b16 v[166:167], %[fa] offset:3488\n\ts_waitcnt lgkmcnt(6)\n\t"
      "v_mfma_f32_32x32x16_bf16 v[128:143], v[208:211], v[216:219], v[128:143]\n\tds_read_b64_tr_b16 v[176:177], %[fa] offset:1416\n\t"
      "ds_read_b64_tr_b16 v[178:179], %[fa] offset:1408\n\tv_mfma_f32_32x32x16_bf16 v[144:159], v[208:211], v[220:223], v[144:159]\n\t"
      "ds_read_b128 v[184:187], %[ua] offset:752\n\tds_read_b128 v[188:191], %[ua] offset:3312\n\tv_mfma_f32_32x32x16_bf16 v[144:159], v[212:215], v[216:219], v[144:159]\n\t"
      "ds_read_b64_tr_b16 v[180:181], %[fa] offset:3464\n\tds_read_b64_tr_b16 v[182:183], %[fa] offset:3456\n\ts_waitcnt lgkmcnt(6)\n\t"
      "v_mfma_f32_32x32x16_bf16 v[128:143], v[160:163], v[168:171], v[128:143]\n\tds_read_b64_tr_b16 v[192:193], %[fa] offset:1512\n\t"
      "ds_read_b64_tr_b16 v[194:195], %[fa] offset:1504\n\tv_mfma_f32_32x32x16_bf16 v[144:159], v[160:163], v[172:175], v[144:159]\n\t"
      "ds_read_b128 v[200:203], %[ua] offset:640\n\tds_read_b128 v[204:207], %[ua] offset:3200\n\tv_mfma_f32_32x32x16_bf16 v[144:159], v[164:167], v[168:171], v[144:159]\n\t"
      "ds_read_b64_tr_b16 v[196:197], %[fa] offset:3560\n\tds_read_b64_tr_b16 v[198:199], %[fa] offset:3552\n\ts_waitcnt lgkmcnt(6)\n\t"
      "v_mfma_f32_32x32x16_bf16 v[128:143], v[176:179], v[184:187], v[128:143]\n\tds_read_b64_tr_b16 v[208:209], %[fa] offset:1480\n\t"
      "ds_read_b64_tr_b16 v[210:211], %[fa] offset:1472\n\tv_mfma_f32_32x32x16_bf16 v[144:159], v[176:179], v[188:191], v[144:159]\n\t"
      "ds_read_b128 v[216:219], %[ua] offset:672\n\tds_read_b128 v[220:223], %[ua] offset:3232\n\tv_mfma_f32_32x32x16_bf16 v[144:159], v[180:183], v[184:187], v[144:159]\n\t"
      "ds_read_b64_tr_b16 v[212:213], %[fa] offset:3528\n\tds_read_b64_tr_b16 v[214:215], %[fa] offset:3520\n\ts_waitcnt lgkmcnt(6)\n\t"
      "v_mfma_f32_32x32x16_bf16 v[128:143], v[192:195], v[200:203], v[128:143]\n\tds_read_b64_tr_b16 v[160:161], %[fa] offset:1576\n\t"
      "ds_read_b64_tr_b16 v[162:163], %[fa] offset:1568\n\tv_mfma_f32_32x32x16_bf16 v[144:159], v[192:195], v[204:207], v[144:159]\n\t"
      "ds_read_b128 v[168:171], %[ua] offset:560\n\tds_read_b128 v[172:175], %[ua] offset:3120\n\tv_mfma_f32_32x32x16_bf16 v[144:159], v[196:199], v[200:203], v[144:159]\n\t"
      "ds_read_b64_tr_b16 v[164:165], %[fa] offset:3624\n\tds_read_b64_tr_b16 v[166:167], %[fa] offset:3616\n\ts_waitcnt lgkmcnt(6)\n\t"
      "v_mfma_f32_32x32x16_bf16 v[128:143], v[208:211], v[216:219], v[128:143]\n\tds_read_b64_tr_b16 v[176:177], %[fa] offset:1544\n\t"
      "ds_read_b64_tr_b16 v[178:179], %[fa] offset:1536\n\tv_mfma_f32_32x32x16_bf16 v[144:159], v[208:211], v[220:223], v[144:159]\n\t"
      "ds_read_b128 v[184:187], %[ua] offset:592\n\tds_read_b128 v[188:191], %[ua] offset:3152\n\tv_mfma_f32_32x32x16_bf16 v[144:159], v[212:215], v[216:219], v[144:159]\n\t"
      "ds_read_b64_tr_b16 v[180:181], %[fa] offset:3592\n\tds_read_b64_tr_b16 v[182:183], %[fa] offset:3584\n\ts_waitcnt lgkmcnt(6)\n\t"
      "v_mfma_f32_32x32x16_bf16 v[128:143], v[160:163], v[168:171], v[128:143]\n\tds_read_b64_tr_b16 v[192:193], %[fa] offset:1640\n\t"
      "ds_read_b64_tr_b16 v[194:195], %[fa] offset:1632\n\tv_mfma_f32_32x32x16_bf16 v[144:159], v[160:163], v[172:175], v[144:159]\n\t"
      "ds_read_b128 v[200:203], %[ua] offset:480\n\tds_read_b128 v[204:207], %[ua] offset:3040\n\tv_mfma_f32_32x32x16_bf16 v[144:159], v[164:167], v[168:171], v[144:159]\n\t"
      "ds_read_b64_tr_b16 v[196:197], %[fa] offset:3688\n\tds_read_b64_tr_b16 v[198:199], %[fa] offset:3680\n\ts_waitcnt lgkmcnt(6)\n\t"
      "v_mfma_f32_32x32x16_bf16 v[128:143], v[176:179], v[184:187], v[128:143]\n\tds_read_b64_tr_b16 v[208:209], %[fa] offset:1608\n\t"
      "ds_read_b64_tr_b16 v[210:211], %[fa] offset:1600\n\tv_mfma_f32_32x32x16_bf16 v[144:159], v[176:179], v[188:191], v[144:159]\n\t"
      "ds_read_b128 v[216:219], %[ua] offset:512\n\tds_read_b128 v[220:223], %[ua] offset:3072\n\tv_mfma_f32_32x32x16_bf16 v[144:159], v[180:183], v[184:187], v[144:159]\n\t"
      "ds_read_b64_tr_b16 v[212:213], %[fa] offset:3656\n\tds_read_b64_tr_b16 v[214:215], %[fa] offset:3648\n\ts_waitcnt lgkmcnt(6)\n\t"
      "v_mfma_f32_32x32x16_bf16 v[128:143], v[192:195], v[200:203], v[128:143]\n\tds_read_b64_tr_b16 v[160:161], %[fa] offset:1704\n\t"
      "ds_read_b64_tr_b16 v[162:163], %[fa] offset:1696\n\tv_mfma_f32_32x32x16_bf16 v[144:159], v[192:195], v[204:207], v[144:159]\n\t"
      "ds_read_b128 v[168:171], %[ua] offset:400\n\tds_read_b128 v[172:175], %[ua] offset:2960\n\tv_mfma_f32_32x32x16_bf16 v[144:159], v[196:199], v[200:203], v[144:159]\n\t"
      "ds_read_b64_tr_b16 v[164:165], %[fa] offset:3752\n\tds_read_b64_tr_b16 v[166:167], %[fa] offset:3744\n\ts_waitcnt lgkmcnt(6)\n\t"
      "v_mfma_f32_32x32x16_bf16 v[128:143], v[208:211], v[216:219], v[128:143]\n\tds_read_b64_tr_b16 v[176:177], %[fa] offset:1672\n\t"
      "ds_read_b64_tr_b16 v[178:179], %[fa] offset:1664\n\tv_mfma_f32_32x32x16_bf16 v[144:159], v[208:211], v[220:223], v[144:159]\n\t"
      "ds_read_b128 v[184:187], %[ua] offset:432\n\tds_read_b128 v[188:191], %[ua] offset:2992\n\tv_mfma_f32_32x32x16_bf16 v[144:159], v[212:215], v[216:219], v[144:159]\n\t"
      "ds_read_b64_tr_b16 v[180:181], %[fa] offset:3720\n\tds_read_b64_tr_b16 v[182:183], %[fa] offset:3712\n\ts_waitcnt lgkmcnt(6)\n\t"
      "v_mfma_f32_32x32x16_bf16 v[128:143], v[160:163], v[168:171], v[128:143]\n\tds_read_b64_tr_b16 v[192:193], %[fa] offset:1768\n\t"
      "ds_read_b64_tr_b16 v[194:195], %[fa] offset:1760\n\tv_mfma_f32_32x32x16_bf16 v[144:159], v[160:163], v[172:175], v[144:159]\n\t"
      "ds_read_b128 v[200:203], %[ua] offset:320\n\tds_read_b128 v[204:207], %[ua] offset:2880\n\tv_mfma_f32_32x32x16_bf16 v[144:159], v[164:167], v[168:171], v[144:159]\n\t"
      "ds_read_b64_tr_b16 v[196:197], %[fa] offset:3816\n\tds_read_b64_tr_b16 v[198:199], %[fa] offset:3808\n\ts_waitcnt lgkmcnt(6)\n\t"
      "v_mfma_f32_32x32x16_bf16 v[128:143], v[176:179], v[184:187], v[128:143]\n\tds_read_b64_tr_b16 v[208:209], %[fa] offset:1736\n\t"
      "ds_read_b64_tr_b16 v[210:211], %[fa] offset:1728\n\tv_mfma_f32_32x32x16_bf16 v[144:159], v[176:179], v[188:191], v[144:159]\n\t"
      "ds_read_b128 v[216:219], %[ua] offset:352\n\tds_read_b128 v[220:223], %[ua] offset:2912\n\tv_mfma_f32_32x32x16_bf16 v[144:159], v[180:183], v[184:187], v[144:159]\n\t"
      "ds_read_b64_tr_b16 v[212:213], %[fa] offset:3784\n\tds_read_b64_tr_b16 v[214:215], %[fa] offset:3776\n\ts_waitcnt lgkmcnt(6)\n\t"
      "v_mfma_f32_32x32x16_bf16 v[128:143], v[192:195], v[200:203], v[128:143]\n\tds_read_b64_tr_b16 v[160:161], %[fa] offset:1832\n\t"
      "ds_read_b64_tr_b16 v[162:163], %[fa] offset:1824\n\tv_mfma_f32_32x32x16_bf16 v[144:159], v[192:195], v[204:207], v[144:159]\n\t"
      "ds_read_b128 v[168:171], %[ua] offset:240\n\tds_read_b128 v[172:175], %[ua] offset:2800\n\tv_mfma_f32_32x32x16_bf16 v[144:159], v[196:199], v[200:203], v[144:159]\n\t"
      "ds_read_b64_tr_b16 v[164:165], %[fa] offset:3880\n\tds_read_b64_tr_b16 v[166:167], %[fa] offset:3872\n\ts_waitcnt lgkmcnt(6)\n\t"
      "v_mfma_f32_32x32x16_bf16 v[128:143], v[208:211], v[216:219], v[128:143]\n\tds_read_b64_tr_b16 v[176:177], %[fa] offset:1800\n\t"
      "ds_read_b64_tr_b16 v[178:179], %[fa] offset:1792\n\tv_mfma_f32_32x32x16_bf16 v[144:159], v[208:211], v[220:223], v[144:159]\n\t"
      "ds_read_b128 v[184:187], %[ua] offset:272\n\tds_read_b128 v[188:191], %[ua] offset:2832\n\tv_mfma_f32_32x32x16_bf16 v[144:159], v[212:215], v[216:219], v[144:159]\n\t"
      "ds_read_b64_tr_b16 v[180:181], %[fa] offset:3848\n\tds_read_b64_tr_b16 v[182:183], %[fa] offset:3840\n\ts_waitcnt lgkmcnt(6)\n\t"
      "v_mfma_f32_32x32x16_bf16 v[128:143], v[160:163], v[168:171], v[128:143]\n\tds_read_b64_tr_b16 v[192:193], %[fa] offset:1896\n\t"
      "ds_read_b64_tr_b16 v[194:195], %[fa] offset:1888\n\tv_mfma_f32_32x32x16_bf16 v[144:159], v[160:163], v[172:175], v[144:159]\n\t"
      "ds_read_b128 v[200:203], %[ua] offset:160\n\tds_read_b128 v[204:207], %[ua] offset:2720\n\tv_mfma_f32_32x32x16_bf16 v[144:159], v[164:167], v[168:171], v[144:159]\n\t"
      "ds_read_b64_tr_b16 v[196:197], %[fa] offset:3944\n\tds_read_b64_tr_b16 v[198:199], %[fa] offset:3936\n\ts_waitcnt lgkmcnt(6)\n\t"
      "v_mfma_f32_32x32x16_bf16 v[128:143], v[176:179], v[184:187], v[128:143]\n\tds_read_b64_tr_b16 v[208:209], %[fa] offset:1864\n\t"
      "ds_read_b64_tr_b16 v[210:211], %[fa] offset:1856\n\tv_mfma_f32_32x32x16_bf16 v[144:159], v[176:179], v[188:191], v[144:159]\n\t"
      "ds_read_b128 v[216:219], %[ua] offset:192\n\tds_read_b128 v[220:223], %[ua] offset:2752\n\tv_mfma_f32_32x32x16_bf16 v[144:159], v[180:183], v[184:187], v[144:159]\n\t"
      "ds_read_b64_tr_b16 v[212:213], %[fa] offset:3912\n\tds_read_b64_tr_b16 v[214:215], %[fa] offset:3904\n\ts_waitcnt lgkmcnt(6)\n\t"
      "v_mfma_f32_32x32x16_bf16 v[128:143], v[192:195], v[200:203], v[128:143]\n\tds_read_b64_tr_b16 v[160:161], %[fa] offset:1960\n\t"
      "ds_read_b64_tr_b16 v[162:163], %[fa] offset:1952\n\tv_mfma_f32_32x32x16_bf16 v[144:159], v[192:195], v[204:207], v[144:159]\n\t"
      "ds_read_b128 v[168:171], %[ua] offset:80\n\tds_read_b128 v[172:175], %[ua] offset:2640\n\tv_mfma_f32_32x32x16_bf16 v[144:159], v[196:199], v[200:203], v[144:159]\n\t"
      "ds_read_b64_tr_b16 v[164:165], %[fa] offset:4008\n\tds_read_b64_tr_b16 v[166:167], %[fa] offset:4000\n\ts_waitcnt lgkmcnt(6)\n\t"
      "v_mfma_f32_32x32x16_bf16 v[128:143], v[208:211], v[216:219], v[128:143]\n\tds_read_b64_tr_b16 v[176:177], %[fa] offset:1928\n\t"
      "ds_read_b64_tr_b16 v[178:179], %[fa] offset:1920\n\tv_mfma_f32_32x32x16_bf16 v[144:159], v[208:211], v[220:223], v[144:159]\n\t"
      "ds_read_b128 v[184:187], %[ua] offset:112\n\tds_read_b128 v[188:191], %[ua] offset:2672\n\tv_mfma_f32_32x32x16_bf16 v[144:159], v[212:215], v[216:219], v[144:159]\n\t"
      "ds_read_b64_tr_b16 v[180:181], %[fa] offset:3976\n\tds_read_b64_tr_b16 v[182:183], %[fa] offset:3968\n\ts_waitcnt lgkmcnt(6)\n\t"
      "v_mfma_f32_32x32x16_bf16 v[128:143], v[160:163], v[168:171], v[128:143]\n\tds_read_b64_tr_b16 v[192:193], %[fa] offset:2024\n\t"
      "ds_read_b64_tr_b16 v[194:195], %[fa] offset:2016\n\tv_mfma_f32_32x32x16_bf16 v[144:159], v[160:163], v[172:175], v[144:159]\n\tds_read_b128 v[200:203], %[ua] offset:0\n\t"
      "ds_read_b128 v[204:207], %[ua] offset:2560\n\tv_mfma_f32_32x32x16_bf16 v[144:159], v[164:167], v[168:171], v[144:159]\n\t"
      "ds_read_b64_tr_b16 v[196:197], %[fa] offset:4072\n\tds_read_b64_tr_b16 v[198:199], %[fa] offset:4064\n\ts_waitcnt lgkmcnt(6)\n\t"
      "v_mfma_f32_32x32x16_bf16 v[128:143], v[176:179], v[184:187], v[128:143]\n\tds_read_b64_tr_b16 v[208:209], %[fa] offset:1992\n\t"
      "ds_read_b64_tr_b16 v[210:211], %[fa] offset:1984\n\tv_mfma_f32_32x32x16_bf16 v[144:159], v[176:179], v[188:191], v[144:159]\n\t"
      "ds_read_b128 v[216:219], %[ua] offset:32\n\tds_read_b128 v[220:223], %[ua] offset:2592\n\tv_mfma_f32_32x32x16_bf16 v[144:159], v[180:183], v[184:187], v[144:159]\n\t"
      "ds_read_b64_tr_b16 v[212:213], %[fa] offset:4040\n\tds_read_b64_tr_b16 v[214:215], %[fa] offset:4032\n\ts_waitcnt lgkmcnt(6)\n\t"
      "v_mfma_f32_32x32x16_bf16 v[128:143], v[192:195], v[200:203], v[128:143]\n\tv_mfma_f32_32x32x16_bf16 v[144:159], v[192:195], v[204:207], v[144:159]\n\t"
      "v_mfma_f32_32x32x16_bf16 v[144:159], v[196:199], v[200:203], v[144:159]\n\ts_waitcnt lgkmcnt(0)\n\t"
      "v_mfma_f32_32x32x16_bf16 v[128:143], v[208:211], v[216:219], v[128:143]\n\tv_mfma_f32_32x32x16_bf16 v[144:159], v[208:211], v[220:223], v[144:159]\n\t"
      "v_mfma_f32_32x32x16_bf16 v[144:159], v[212:215], v[216:219], v[144:159]\n\ts_nop 11\n\t"
      : "=&{v[128:143]}"(acc[0]), "=&{v[144:159]}"(acc[1]), "=&{v[160:191]}"(x0), "=&{v[192:223]}"(x1)
      : [fa] "v"(FC + (4 * w + ((lane >> 2) & 3)) * CP + 24 + (lane & 16) * 2 + (lane & 3) * 8 - h2 * 16), [ua] "v"(ub + (r + 1) * 80 + h2 * 16) : "memory");
    const float dv = k ? bhi(dd) : blo(dd);
#pragma unroll
    for (int i = 0; i < 8; ++i) {
      LAS v2u* at = (LAS v2u*)(c.L + ub + (32 + (i >> 2) * 32 + r) * 80 + (i & 3) * 16 + h2 * 8);
      const v2u uu = *at;
      const f32x16& a = acc[i >> 2];
      const int m = 4 * (i & 3);
      const f32x2 y0 = rb2(f32x2{a[m], a[m + 1]}) + rb2(f32x2{blo(uu.x), bhi(uu.x)} * dv), y1 = rb2(f32x2{a[m + 2], a[m + 3]}) + rb2(f32x2{blo(uu.y), bhi(uu.y)} * dv);
      *at = v2u{pk(y0.x, y0.y), pk(y1.x, y1.y)};
    }
    LGKM(0);
#pragma unroll
    for (int i = 0; i < 4; ++i) st16s(c.rs, i * 1024 + lane * 16, R_YT + (d0 + k) * 4096, lds16(c.L, ub + (32 + i * 16 + (lane >> 2)) * 80 + (lane & 3) * 16));
  }
  STAMP(6);
  {
    const rsrc_t ro = mkrs(p->out, 2048 * 2048 * 2);
    v4u yv[8];
    group_sync(1);
#pragma unroll
    for (int ch = 0; ch < 8; ++ch) yv[ch] = ld16s(c.rs, de * 4096 + t0 * 2, R_YT + ch * 4096);
#pragma unroll
    for (int i = 0; i < 8; ++i) {
      float o[8];
#pragma unroll
      for (int ch = 0; ch < 8; ++ch) o[ch] = ((ch & 1) ? bhi(qr[i][ch >> 1]) : blo(qr[i][ch >> 1])) * ((i & 1) ? bhi(yv[ch][i >> 1]) : blo(yv[ch][i >> 1]));
      __builtin_amdgcn_raw_buffer_store_b128(v4u{pk(o[0], o[1]), pk(o[2], o[3]), pk(o[4], o[5]), pk(o[6], o[7])}, ro, (t0 + i) * 4096 + (1024 + de) * 2, 0, 0);
    }
  }
  if (c.budget == 0u) __hip_atomic_store(c.ctrl + C_TMO / 4, 1u, RLX_AGENT);
}

extern "C" __global__ void __launch_bounds__(256, 1) mk_mega(Params p) {
  extern __shared__ __attribute__((aligned(16))) unsigned char lds_raw[];
  Ctx c;
  c.ctrl = (gu32*)p.ctrl;
  c.L = (lds_t)lds_raw;
  c.w = __builtin_amdgcn_readfirstlane(threadIdx.x >> 6);
  c.lane = threadIdx.x & 63;
  c.b = (int)(blockIdx.x & 255u);
  c.att = (int)(blockIdx.x >> 8);
  c.ask = 0;
  if (c.w == 0 && c.att == 0) {
    c.ask = __builtin_amdgcn_readfirstlane(__hip_atomic_load(c.ctrl + C_ECHO / 4 + c.b, RLX_AGENT)) + 1u;
    if (c.lane == 0) __hip_atomic_store(c.ctrl + (C_ASK + c.b * 64) / 4, c.ask, RLX_AGENT);
  }
  c.slot = (c.att ? (c.b & 7) : (int)((unsigned)__builtin_amdgcn_s_getreg(20 | (3 << 11)) & 7u)) * 32 + (c.b >> 3);
  if (c.b == 130 && c.w == 3) serve(c);
  const int x = c.slot >> 5, t = c.slot & 31;
  if (x < 4) {
    if (t < 8) qk_role(c, x * 8 + t);
    else if (t < 16) vo_role(c, x * 8 + t - 8);
    else y_role(c, x * 16 + t - 16);
  } else {
    if (t < 24) { const int i = (x - 4) * 24 + t; g_role(c, i >> 5, i & 31, t); }
    else zc_role(c, (x - 4) * 8 + t - 24);
    STAMP(2);
    conv_role(c, (x - 4) * 32 + t);
  }
  STAMP(3);
}

}

extern "C" void kernel_launch(void* const* d_in, const int* in_sizes, int n_in, void* d_out, int out_size, void* d_ws, size_t ws_size,
                              hipStream_t stream) {
  using namespace mk;
  (void)in_sizes; (void)n_in; (void)out_size; (void)ws_size;
  Params p;
  p.x = (const u16*)d_in[0]; p.mem0 = (const u16*)d_in[1];
  p.Wq = (const u16*)d_in[2]; p.Wk = (const u16*)d_in[3]; p.Wv = (const u16*)d_in[4]; p.Wo = (const u16*)d_in[5];
  p.bq = (const u16*)d_in[7]; p.bk = (const u16*)d_in[8]; p.bv = (const u16*)d_in[9];
  p.shift = (const u16*)d_in[10]; p.ssmk = (const u16*)d_in[11]; p.ssmD = (const u16*)d_in[12];
  p.out = (u16*)d_out;
  p.ctrl = (unsigned char*)d_ws;
  p.data = (unsigned char*)(((size_t)d_ws + ws_size - R_END - MB) & ~(size_t)((2u << 20) - 1));
  (void)hipFuncSetAttribute((const void*)mk_mega, hipFuncAttributeMaxDynamicSharedMemorySize, LDS_BYTES);
  hipLaunchKernelGGL(mk_mega, dim3(256), dim3(256), LDS_BYTES, stream, p);
}
```

```cpp
#include <hip/hip_runtime.h>
#include <hip/hip_bf16.h>

namespace mk {

#define GAS __attribute__((address_space(1)))
#define LAS __attribute__((address_space(3)))
#define DEV __device__ __forceinline__
#define RLX_AGENT __ATOMIC_RELAXED, __HIP_MEMORY_SCOPE_AGENT
typedef unsigned short u16;
typedef unsigned v2u __attribute__((ext_vector_type(2)));
typedef unsigned v4u __attribute__((ext_vector_type(4)));
typedef unsigned v8u __attribute__((ext_vector_type(8)));
typedef unsigned v16u __attribute__((ext_vector_type(16)));
typedef unsigned v32u __attribute__((ext_vector_type(32)));
typedef float f32x2 __attribute__((ext_vector_type(2)));
typedef float f32x4 __attribute__((ext_vector_type(4)));
typedef float f32x16 __attribute__((ext_vector_type(16)));
typedef __bf16 bf16x2 __attribute__((ext_vector_type(2)));
typedef __bf16 bf16x8 __attribute__((ext_vector_type(8)));
typedef __bf16 bf16x16 __attribute__((ext_vector_type(16)));
typedef GAS unsigned gu32;
typedef __amdgpu_buffer_rsrc_t rsrc_t;
typedef LAS unsigned char* lds_t;

constexpr int NSEG = 64, SERVER = 130;
constexpr unsigned SENT = 0xFFFFFFFFu;
constexpr int AUX_SC1 = 16, AUX_NT = 2;
constexpr int T_LAG = 61;
constexpr int G_AHEAD = 4;
#define SPINS 21
constexpr unsigned SPIN_CAP = 1u << SPINS;
constexpr int LDS_BYTES = 163840;

constexpr int C_ECHO = 0, C_CLAIM = 1024, C_VERDICT = 3072, C_TMO = 3328, C_PROG = 3840, C_FLAG = 4096, C_ASK = C_FLAG + 256 * 128;
static_assert(C_TMO == 3328 && C_ECHO == 0 && C_ASK == 36864 && C_PROG == 3840, "known to the generated code");
constexpr int F_Q = 0, F_K = 32, F_V = 64, F_Z = 96, F_C = 224;
constexpr int CTRL_BYTES = C_ASK + 256 * 64;
constexpr int MB = 1 << 20;
constexpr int R_U = 0;
constexpr int R_MEM = 2 * MB;
constexpr int R_PT = 3 * MB;
#define ZRN 14
#define GRN 16
constexpr int R_QX = 4 * MB, R_KX = R_QX + GRN * MB / 2, R_VX = R_QX + GRN * MB, R_Z = R_QX + GRN * MB * 3 / 2, R_END = R_Z + ZRN * 4 * MB;
constexpr int R_KVT = R_Z, R_YT = R_Z + 8 * MB;
#define QKFAR 8192
DEV int hblock(int h, int flip) { return h * GRN * 16384 + ((((h * GRN) << 6) ^ flip) & 8192); }

struct Params {
  const u16 *x, *mem0, *Wq, *Wk, *Wv, *Wo, *bq, *bk, *bv, *shift, *ssmk, *ssmD;
  u16* out;
  unsigned char* ctrl;
  unsigned char* data;
};

typedef const __attribute__((address_space(4))) Params* KArg;
DEV KArg args() {
  KArg a = (KArg)__builtin_amdgcn_kernarg_segment_ptr(); asm volatile("" : "+s"(a)); return a;
}
DEV rsrc_t mkrs(const void* p, unsigned bytes) { return __builtin_amdgcn_make_buffer_rsrc((void*)p, 0, bytes, 0x00020000); }
DEV v4u ld16s(rsrc_t rs, int v, int s) { return __builtin_amdgcn_raw_buffer_load_b128(rs, v, s, AUX_SC1); }
DEV v2u ld8s(rsrc_t rs, int v, int s) { return __builtin_amdgcn_raw_buffer_load_b64(rs, v, s, AUX_SC1); }
DEV v4u ld16(rsrc_t rs, int v, int s) { return __builtin_amdgcn_raw_buffer_load_b128(rs, v, s, 0); }
DEV v4u ld16n(rsrc_t rs, int v, int s) { return __builtin_amdgcn_raw_buffer_load_b128(rs, v, s, AUX_NT); }
DEV v2u ld8n(rsrc_t rs, int v, int s) { return __builtin_amdgcn_raw_buffer_load_b64(rs, v, s, AUX_NT); }
DEV v2u ld8(rsrc_t rs, int v, int s) { return __builtin_amdgcn_raw_buffer_load_b64(rs, v, s, 0); }
DEV void st16s(rsrc_t rs, int v, int s, v4u d) { __builtin_amdgcn_raw_buffer_store_b128(d, rs, v, s, AUX_SC1); }
DEV void st8s(rsrc_t rs, int v, int s, v2u d) { __builtin_amdgcn_raw_buffer_store_b64(d, rs, v, s, AUX_SC1); }
DEV void st4s(rsrc_t rs, int v, int s, unsigned d) { __builtin_amdgcn_raw_buffer_store_b32(d, rs, v, s, AUX_SC1); }
DEV int amap(int o, int par) { return ((o >> 13) << 14) | (par << 13) | (o & 8191); }
DEV unsigned umax(unsigned a, unsigned b) { return a > b ? a : b; }
DEV unsigned top(v4u v) { return umax(umax(v.x, v.y), umax(v.z, v.w)); }
DEV unsigned pk(float a, float b) { bf16x2 v = {(__bf16)a, (__bf16)b}; return __builtin_bit_cast(unsigned, v); }
DEV unsigned pks(float a, float b) { unsigned u = pk(a, b); return u < SENT - 1u ? u : SENT - 1u; }
DEV float blo(unsigned u) { return __builtin_bit_cast(float, u << 16); }
DEV float bhi(unsigned u) { return __builtin_bit_cast(float, u & 0xFFFF0000u); }
DEV float rb(float a) { return blo(pk(a, a)); }
DEV f32x2 rb2(f32x2 a) { const unsigned u = pk(a.x, a.y); return f32x2{blo(u), bhi(u)}; }
DEV unsigned fu(float f) { return __builtin_bit_cast(unsigned, f); }
DEV float uf(unsigned u) { return __builtin_bit_cast(float, u); }
DEV f32x4 mfma16(v4u a, v4u b, f32x4 c) {
  return __builtin_amdgcn_mfma_f32_16x16x32_bf16(__builtin_bit_cast(bf16x8, a), __builtin_bit_cast(bf16x8, b), c, 0, 0, 0);
}
DEV f32x16 mfma32(v4u a, v4u b, f32x16 c) {
  return __builtin_amdgcn_mfma_f32_32x32x16_bf16(__builtin_bit_cast(bf16x8, a), __builtin_bit_cast(bf16x8, b), c, 0, 0, 0);
}
DEV f32x4 smf(v4u a, v8u b, f32x4 c, int idx) {
  return __builtin_amdgcn_smfmac_f32_16x16x64_bf16(__builtin_bit_cast(bf16x8, a), __builtin_bit_cast(bf16x16, b), c, idx, 0, 0);
}
DEV v8u cat(v4u a, v4u b) { return __builtin_shufflevector(a, b, 0, 1, 2, 3, 4, 5, 6, 7); }
DEV v4u lo4(v8u a) { return __builtin_shufflevector(a, a, 0, 1, 2, 3); }
DEV v4u hi4(v8u a) { return __builtin_shufflevector(a, a, 4, 5, 6, 7); }
DEV v4u lds16(lds_t L, int o) { return *(LAS v4u*)(L + o); }
DEV void sts16(lds_t L, int o, v4u v) { *(LAS v4u*)(L + o) = v; }
DEV void sts8(lds_t L, int o, v2u v) { *(LAS v2u*)(L + o) = v; }
#define SCHED() __builtin_amdgcn_sched_barrier(0)
#define PIN_A(x) asm volatile("" : "+a"(x))
#define PIN_V(x) asm volatile("" : "+v"(x))
DEV int opq(int v) { asm volatile("" : "+v"(v)); return v; }
DEV int fresh_lane() {
  int l; asm volatile("v_mbcnt_lo_u32_b32 %0, -1, 0\n\tv_mbcnt_hi_u32_b32 %0, -1, %0" : "=v"(l)); return l;
}
DEV f32x4 fresh_zero() {
  f32x2 a, b;
  asm volatile("v_mov_b64 %0, 0" : "=v"(a));
  asm volatile("v_mov_b64 %0, 0" : "=v"(b));
  return __builtin_shufflevector(a, b, 0, 1, 2, 3);
}
DEV unsigned fresh_sent() { unsigned z; asm volatile("v_mov_b32 %0, -1" : "=v"(z)); return z; }
#define SMF "v_smfmac_f32_16x16x64_bf16 "
#define MF16 "v_mfma_f32_16x16x32_bf16 "
#define SMF1(cls, d, a, b) asm volatile(SMF "%0, %1, %2, %3" : "+v"(d) : "v"(a), cls(b), "v"(idx))
#define SMF1F(cls, lo, hi, d, a, b) asm volatile(SMF "v[" #lo ":" #hi "], %1, %2, %3" : "+{v[" #lo ":" #hi "]}"(d) : "v"(a), cls(b), "v"(idx))
#define AT_HOME(M, j) do { const int j_ = (j); if (j_ == 0) M("{v[32:39]}"); if (j_ == 1) M("{v[40:47]}"); if (j_ == 2) M("{v[48:55]}"); if (j_ == 3) M("{v[56:63]}"); if (j_ == 4) M("{v[64:71]}"); if (j_ == 5) M("{v[72:79]}"); if (j_ == 6) M("{v[80:87]}"); if (j_ == 7) M("{v[88:95]}"); if (j_ == 8) M("{v[96:103]}"); if (j_ == 9) M("{v[104:111]}"); if (j_ == 10) M("{v[112:119]}"); if (j_ == 11) M("{v[120:127]}"); if (j_ == 12) M("{v[128:135]}"); if (j_ == 13) M("{v[136:143]}"); if (j_ == 14) M("{v[144:151]}"); if (j_ == 15) M("{v[152:159]}"); } while (0)
#define MF1(d, a, b) asm volatile(MF16 "%0, %1, %2, %0" : "+v"(d) : "v"(a), "v"(b))
#define MF1Z(d, a, b) asm volatile(MF16 "%0, %1, %2, 0" : "=&v"(d) : "v"(a), "v"(b))
#define LGKM(n) asm volatile("s_waitcnt lgkmcnt(" #n ")" ::: "memory")
#define RAWBAR() do { __builtin_amdgcn_s_barrier(); asm volatile("" ::: "memory"); } while (0)
DEV void settle3(f32x4& s0, f32x4& s1, f32x4& s2) { asm volatile("s_nop 7" : "+v"(s0), "+v"(s1), "+v"(s2)); }
DEV void settle4(f32x4 (&d)[4]) { asm volatile("s_nop 7" : "+v"(d[0]), "+v"(d[1]), "+v"(d[2]), "+v"(d[3])); }
#define BAR() do { asm volatile("s_waitcnt lgkmcnt(0)" ::: "memory"); __builtin_amdgcn_s_barrier(); asm volatile("" ::: "memory"); } while (0)
#define VMWAIT(n) asm volatile("s_waitcnt vmcnt(" #n ")" ::: "memory")
DEV float add1(float a, float b) {
  float o; asm("v_add_f32 %0, %1, %2" : "=v"(o) : "v"(a), "v"(b)); return o;
}
DEV float xmax(float v) {
  auto r = __builtin_amdgcn_permlane32_swap(fu(v), fu(v), false, false); v = fmaxf(uf(r[0]), uf(r[1]));
  auto q = __builtin_amdgcn_permlane16_swap(fu(v), fu(v), false, false); return fmaxf(uf(q[0]), uf(q[1]));
}
DEV float xsum(float v) {
  auto r = __builtin_amdgcn_permlane32_swap(fu(v), fu(v), false, false); v = uf(r[0]) + uf(r[1]);
  auto q = __builtin_amdgcn_permlane16_swap(fu(v), fu(v), false, false); return uf(q[0]) + uf(q[1]);
}

DEV float ror8(float v) { return uf(__builtin_amdgcn_update_dpp(0u, fu(v), 0x128, 0xf, 0xf, true)); }
DEV float fmax2(float a, float b) { return __builtin_amdgcn_fmed3f(a, b, __builtin_inff()); }
DEV float fmax3(float a, float b, float c) { float r; asm("v_max3_f32 %0, %1, %2, %3" : "=v"(r) : "v"(a), "v"(b), "v"(c)); return r; }
DEV bool allok(unsigned t) { return __all(t != SENT); }

template <int N> struct IC { static constexpr int value = N; };
struct Ctx {
  gu32* ctrl;
  rsrc_t rs;
  lds_t L;
  int w, lane;
  int b, slot, att;
  unsigned ask;
  unsigned budget;
};
DEV KArg enter(Ctx& c) {
  const KArg a = args();
  c.ctrl = (gu32*)a->ctrl;
  c.rs = mkrs(a->data, R_END);
  asm volatile("" : "+s"(c.w), "+s"(c.b), "+s"(c.att), "+s"(c.slot));
  return a;
}
DEV bool spend(Ctx& c) {
  if (c.budget == 0u) return true;
  c.budget -= 1u;
  return false;
}
DEV unsigned wait_flag(Ctx& c, int flag, unsigned need) {
  gu32* f = c.ctrl + (C_FLAG + flag * 128) / 4;
  unsigned v;
#pragma unroll 1
  for (;;) {
    v = __builtin_amdgcn_readfirstlane(__hip_atomic_load(f, RLX_AGENT));
    if (v >= need || spend(c)) break;
    __builtin_amdgcn_s_sleep(2);
  }
  __builtin_amdgcn_fence(__ATOMIC_ACQUIRE, "wavefront");
  return v;
}
DEV void post_flag(Ctx& c, int flag, unsigned v, int off = 0) {
  st4s(mkrs((const void*)c.ctrl, CTRL_BYTES), (c.w | off | fresh_lane()) ? 1 << 30 : 0, C_FLAG + flag * 128, v);
}
DEV void claim(Ctx& c, unsigned votes) {
  if (c.w != 0) return;
  if (c.att == 0) {
#pragma unroll 1
    while (__builtin_amdgcn_readfirstlane(__hip_atomic_load(c.ctrl + C_ECHO / 4 + c.b, RLX_AGENT)) != c.ask) {
      if (spend(c)) break;
      __builtin_amdgcn_s_sleep(2);
    }
  }
  if (fresh_lane() == 0) __hip_atomic_fetch_add(c.ctrl + (C_CLAIM + c.att * 1024) / 4 + c.slot, 1u + votes, RLX_AGENT);
}
DEV unsigned wait_verdict(Ctx& c) {
  unsigned v;
#pragma unroll 1
  for (;;) {
    v = __builtin_amdgcn_readfirstlane(__hip_atomic_load(c.ctrl + (C_VERDICT + c.att * 128) / 4, RLX_AGENT));
    if ((v & 1u) || spend(c)) break;
    __builtin_amdgcn_s_sleep(2);
  }
  __builtin_amdgcn_fence(__ATOMIC_ACQUIRE, "wavefront");
  if ((v & 3u) == 3u && c.att == 0) {
    asm volatile("s_waitcnt vmcnt(0) lgkmcnt(0)\n\ts_barrier\n\ts_mov_b64 s[0:1], %0\n\ts_mov_b32 s2, %1\n\tv_mov_b32 v0, %2\n\t"
                 "s_getpc_b64 s[4:5]\n1:\n\ts_sub_u32 s4, s4, 1b-mk_mega\n\ts_subb_u32 s5, s5, 0\n\ts_setpc_b64 s[4:5]"
                 :: "s"(__builtin_amdgcn_kernarg_segment_ptr()), "s"(c.b + 256), "v"(c.w * 64 + fresh_lane()) : "s0", "s1", "s2", "s4", "s5", "v0", "scc", "memory");
    __builtin_unreachable();
  }
  return v;
}
DEV void serve(Ctx& c) {
  c.budget = SPIN_CAP;
  const int lane = fresh_lane();
  const rsrc_t rc = mkrs((const void*)c.ctrl, CTRL_BYTES);
  if (c.att == 0) {
#pragma unroll
    for (int i = C_CLAIM / 1024; i < C_ASK / 1024; ++i) st16s(rc, lane * 16, i * 1024, v4u{0, 0, 0, 0});
    VMWAIT(0);
  }
#pragma unroll 1
  for (;;) {
    asm volatile("" ::: "memory");
    const v4u cl = ld16s(rc, lane * 16, C_CLAIM + c.att * 1024);
    if (c.att == 0) {
      v4u e;
#pragma unroll
      for (int i = 0; i < 4; ++i) e[i] = __builtin_amdgcn_raw_buffer_load_b32(rc, (lane * 4 + i) * 64, C_ASK, AUX_SC1);
      st16s(rc, lane * 16, C_ECHO, e);
    }
    unsigned n = 0, one = 1, va = 0, vb = 0;
#pragma unroll
    for (int i = 0; i < 4; ++i) { n += cl[i] & 0xFFFFu; one &= (cl[i] & 0xFFFFu) == 1u; va += (cl[i] >> 16) & 1u; vb += (cl[i] >> 24) & 1u; }
    unsigned sum = n | (va << 12) | (vb << 22);
#pragma unroll
    for (int o = 1; o < 64; o <<= 1) sum += (unsigned)__shfl_xor((int)sum, o);
    sum = __builtin_amdgcn_readfirstlane(sum);
    if ((sum & 0xFFFu) >= 256u) {
      const unsigned v = 1u | (__all(one) ? 0u : 2u) | (((sum >> 12) & 0x3FFu) > 32u ? 4u : 0u) | ((sum >> 22) > 32u ? 8u : 0u);
      if (lane == 0) __hip_atomic_store(c.ctrl + (C_VERDICT + c.att * 128) / 4, v, RLX_AGENT);
      break;
    }
    if (spend(c)) break;
  }
}
DEV int vote_a(unsigned v) { return (int)((v >> 2) & 1u); }
DEV int vote_b(unsigned v) { return (int)((v >> 3) & 1u); }

#define STAMP(slot) do {} while (0)
#define TICK_DECL
#define TICK(i) do {} while (0)
#define TICK_SUM(n) do {} while (0)
#define TICK_OUT() do {} while (0)
#define TKS(x) ""
#define TK_CLOB
#define TK0 TKS("s_memtime s[74:75]\n\t")
#define TK1 TKS("s_memtime s[76:77]\n\t")
#define TK2 TKS("s_memtime s[78:79]\n\t")
#define TK3 TKS("s_memtime s[80:81]\n\t")
#define TK4 TKS("s_memtime s[82:83]\n\t")
#define TK5 TKS("s_memtime s[84:85]\n\t")
#define TK6 TKS("s_memtime s[86:87]\n\t")
#define TK7 TKS("s_memtime s[88:89]\n\t")
#define DIAG(slot, v) do {} while (0)
#define STS(x) ""
#define STC(x) ""
#define STF(x) ""

DEV constexpr int krow(int rho8, int i) { return 32 * (rho8 >> 2) + 8 * (rho8 & 1) + 2 * ((rho8 >> 1) & 1) + i; }
DEV int krow_lane(int g) { return 16 * (g >> 1) + 4 * (g & 1); }
DEV unsigned prm(unsigned hi, unsigned lo, int odd) { return __builtin_amdgcn_perm(hi, lo, odd ? 0x07060302u : 0x05040100u); }

#define ISBAD "v_cmp_eq_u32 vcc, -1, %[t]\n\t"
#define SPEND(lbl) "s_cmp_eq_u32 %[bd], 0\n\ts_cbranch_scc1 " lbl "\n\ts_sub_u32 %[bd], %[bd], 1\n\t"
#define KNOB(i, d) ((unsigned)(d))
#define CQ0 300
#define CV0 200
#define D1 32
#define D2 108
#define C1 KNOB(0, D1)
#define C2 KNOB(1, D2)
#define CU KNOB(2, 40)
#define KLT KNOB(4, 32)
#define KLP KNOB(3, 90)
#define KH KNOB(5, 104)
#define ZDLY KNOB(7, 2)
#define ZRING 63
#define HACK KNOB(6, 0)
#define NLK 1
#define BAUX 0
#define NPROBE 4
#define HELP 0
#define HAUX 0
#define CWARM 0
#define KV_EARLY 0
#define XWARM 1
#define CAUX AUX_NT
#define L0 6u
#define LA 10u
#define ZWAIT 60
static_assert(L0 + LA <= GRN, "ring");
#define PACE 0, 0, C2, KLT, KLP, 0
DEV unsigned uni(unsigned v) { return __builtin_amdgcn_readfirstlane(v); }
#define ALL_REGS "=&{a[0:31]}"(wa[0]), "=&{a[32:63]}"(wa[1]), "=&{a[64:95]}"(wa[2]), "=&{a[96:127]}"(wa[3]), "=&{a[128:159]}"(wa[4]), "=&{a[160:191]}"(wa[5]), "=&{a[192:223]}"(wa[6]), \
    "=&{a[224:255]}"(wa[7]), "=&{v[32:63]}"(wv[0]), "=&{v[64:95]}"(wv[1]), "=&{v[96:127]}"(wv[2]), "=&{v[128:159]}"(wv[3]), "=&{v[224:255]}"(wv[4])
#define S_CLOB "s8", "s9", "s10", "s11", "s12", "s13", "s14", "s15", "s16", "s17", "s18", "s19", "s20", "s21", "s22", "s23", "s24", "s25", "s26", "s27", "s28", "s29", "s30", "s31", "s33", "s34", "s35", "s36", "s37", "s38", "s39", "s40", "s41", "s42", "s43", "s44", "s45", "s46", "s47", "s48", "s49", "s50", "s51", "s52", "s53", "s54", "s55", "s56", "s57", "s58", "s59", "s60", "s61", "s62", "s63", "s64", "s65", "s66", "s67", "s68", "s69", "s70", "s71", "s72", "s73"
#define ROFF 1
#define ROTS(x) (unsigned)((((x) + ROFF) & 3) << 19), (unsigned)((((x) + ROFF + 1) & 3) << 19), (unsigned)((((x) + ROFF + 2) & 3) << 19), (unsigned)((((x) + ROFF + 3) & 3) << 19)
#define HELLO c.w ? 0u : c.att ? 2u : c.b == SERVER ? 3u : 1u, (unsigned)c.b * 4u, (unsigned)opq(C_CLAIM + c.att * 1024 + c.slot * 4)
#define ENTRY plo((const void*)p), phi((const void*)p), c.att ? 0u : (unsigned)c.b + 256u, (unsigned)(C_VERDICT + c.att * 128)
#define RSRCS plo(p->data), phi(p->data), (unsigned)opq(R_END), 0x00020000, plo(p->ctrl), phi(p->ctrl), CTRL_BYTES, 0x00020000
DEV unsigned plo(const void* q) { return (unsigned)(size_t)q; }
DEV unsigned phi(const void* q) { return (unsigned)((size_t)q >> 32); }

DEV constexpr int qk_r(int j) { return j < 4 ? 0 : (j - 4) % 3 + 1; }
DEV constexpr int qk_i(int j) { return j < 4 ? j : (j - 4) / 3; }
DEV void qk_role(Ctx& c, int h) {
  const KArg p = enter(c);
  constexpr int WL = 0, MEMS = 131072, KXL = 147456, QKX = 155648, QROW = 272, CTL = 160000;
  c.budget = SPIN_CAP;
  const int w = c.w, lane = fresh_lane(), g = lane >> 4, cc = lane & 15, q = (lane >> 2) & 3, pp = lane & 3;
  const int prow = (2 * g + (cc & 1)) * 2 + ((cc >> 1) & 1);
  v32u k;
#pragma unroll
  for (int i = 0; i < 32; ++i) k[i] = 0;
  k[0] = amap(w * 4096 + lane * 16, 0);
  k[1] = MEMS + w * 4096 + lane * 16;
#pragma unroll
  for (int r = 0; r < 3; ++r) k[2 + r] = MEMS + ((w + r + 1) & 3) * 4096 + lane * 16;
  k[5] = WL + w * 32768 + lane * 16;
  k[6] = (lane & 1) ? 0xEEEE : 0x4444;
  k[7] = w < 2 ? QKX + (4 * w + (cc >> 2)) * 256 + g * 64 + (cc & 3) * 4 : QKX + (8 + 2 * g) * QROW + (64 * (w & 1) + 4 * cc) * 2;
  k[8] = QKX + g * 16 + (8 + (cc & 7)) * QROW;
  k[9] = KXL + g * 512 + cc * 16;
  k[10] = w == 0 ? amap((h >> 1) * 1024 + ((2 * (h & 1) + (cc >> 3)) * 16 + (prow >> 1) + 8 * (prow & 1)) * 16 + ((cc >> 2) & 1) * 8, 0)
        : w == 1 ? amap(h * 512 + (g & 1) * 256 + cc * 16 + (g >> 1) * 8, 0) : w == 2 ? amap(16384 + h * 1024 + lane * 16, 0) : lane * 16;
  k[11] = w == 0 ? (cc < 8 ? amap(16384 + h * 128 + prow * 8 + (cc >> 2) * 4, 0) : 1 << 30)
        : w < 3 ? (g < 2 ? amap(49152 + ((w - 1) * 32 + h) * 256 + cc * 16 + g * 8, 0) : 1 << 30) : lane * 128;
  k[12] = w == 3 ? ((lane | h) ? 1 << 30 : C_PROG) : g * 512 + (16 * ((w + 1) & 1) + cc) * 16;
  k[13] = fu((w == 0 ? cc < 8 : g < 2) ? 1.0f : 0.0f);
  k[14] = (lane >= 4 * NLK || w) ? 1 << 30 : amap(24576 + lane * 4096 + h * 128, 0);
  k[15] = amap(24576 + (c.slot >> 5) * 4096 + (lane & 31) * 128, 0);
  k[16] = CTL;
#pragma unroll
  for (int i = 0; i < 2; ++i) k[20 + i] = (lane >> 3) * 8192 + (h * 128 + 64 * (w & 1)) * 2 + 16 * ((lane & 7) ^ (4 * i + 2 * (lane >> 5)));
#pragma unroll
  for (int t = 0; t < 4; ++t) k[22 + t] = WL + w * 32768 + (8 * (q >> 1) + (q & 1) + krow_lane(g)) * 128 + 16 * ((2 * t + (pp >> 1)) ^ (4 * (q >> 1) + 2 * (g & 1))) + 8 * (pp & 1);
  const u16* W = (w >> 1) ? p->Wk : p->Wq;
  constexpr float SC = 0.08838834764831845f, CE = 1.4426950408889634f * SC;
  v32u s0 = {RSRCS, (unsigned)w, ~0u, c.budget, 0, CQ0 * 16, 0, (unsigned)(R_QX + hblock(h, QKFAR) + (GRN - 1) * 16384), C2, KLT, KLP, 0, 0, fu(CE), fu(CE), fu(-1e9f / SC), (unsigned)(R_KX + hblock(h, QKFAR))};
#pragma unroll
  for (int i = 0; i < 4; ++i) { const unsigned long long m = __ballot(4 * g + i <= cc); s0[24 + 2 * i] = (unsigned)m; s0[25 + 2 * i] = (unsigned)(m >> 32); }
  v32u s1 = {plo(W), phi(W), 1024 * 4096 * 2, 0x00020000, 0, (unsigned)(C_FLAG + (F_Q + h) * 128), 0, 0, 0, (unsigned)(WL + w * 32768), 0, 0, 0, 0, 0, 0,
             (unsigned)(w << 21), (unsigned)(((w + 1) & 3) << 21), (unsigned)(((w + 2) & 3) << 21), (unsigned)(((w + 3) & 3) << 21), plo((const void*)p), phi((const void*)p),
             c.att ? 0u : (unsigned)c.b + 256u, (unsigned)(C_VERDICT + c.att * 128), ROTS(c.slot >> 5), HELLO};
  v32u wa[8], wv[5];
  asm volatile(
      "s_nop 0\n\tv_readfirstlane_b32 s8, v160\n\tv_readfirstlane_b32 s9, v161\n\tv_readfirstlane_b32 s10, v162\n\tv_readfirstlane_b32 s11, v163\n\t"
      "v_readfirstlane_b32 s12, v164\n\tv_readfirstlane_b32 s13, v165\n\tv_readfirstlane_b32 s14, v166\n\tv_readfirstlane_b32 s15, v167\n\tv_readfirstlane_b32 s16, v168\n\t"
      "v_readfirstlane_b32 s17, v169\n\tv_readfirstlane_b32 s18, v170\n\tv_readfirstlane_b32 s19, v171\n\tv_readfirstlane_b32 s20, v172\n\tv_readfirstlane_b32 s21, v173\n\t"
      "v_readfirstlane_b32 s22, v174\n\tv_readfirstlane_b32 s23, v175\n\tv_readfirstlane_b32 s24, v176\n\tv_readfirstlane_b32 s25, v177\n\tv_readfirstlane_b32 s26, v178\n\t"
      "v_readfirstlane_b32 s27, v179\n\tv_readfirstlane_b32 s28, v180\n\tv_readfirstlane_b32 s29, v181\n\tv_readfirstlane_b32 s30, v182\n\tv_readfirstlane_b32 s31, v183\n\t"
      "v_readfirstlane_b32 s32, v184\n\tv_readfirstlane_b32 s33, v185\n\tv_readfirstlane_b32 s34, v186\n\tv_readfirstlane_b32 s35, v187\n\tv_readfirstlane_b32 s36, v188\n\t"
      "v_readfirstlane_b32 s37, v189\n\tv_readfirstlane_b32 s38, v190\n\tv_readfirstlane_b32 s39, v191\n\tv_readfirstlane_b32 s40, v192\n\tv_readfirstlane_b32 s41, v193\n\t"
      "v_readfirstlane_b32 s42, v194\n\tv_readfirstlane_b32 s43, v195\n\tv_readfirstlane_b32 s45, v197\n\tv_readfirstlane_b32 s49, v201\n\tv_readfirstlane_b32 s54, v206\n\t"
      "v_readfirstlane_b32 s55, v207\n\tv_readfirstlane_b32 s56, v208\n\tv_readfirstlane_b32 s57, v209\n\tv_readfirstlane_b32 s58, v210\n\tv_readfirstlane_b32 s59, v211\n\t"
      "v_readfirstlane_b32 s60, v212\n\tv_readfirstlane_b32 s61, v213\n\tv_readfirstlane_b32 s62, v214\n\tv_readfirstlane_b32 s63, v215\n\tv_readfirstlane_b32 s64, v216\n\t"
      "v_readfirstlane_b32 s65, v217\n\tv_readfirstlane_b32 s66, v218\n\tv_readfirstlane_b32 s67, v219\n\tv_readfirstlane_b32 s68, v220\n\tv_readfirstlane_b32 s69, v221\n\t"
      "v_readfirstlane_b32 s70, v222\n\ts_nop 4\n\t"
      TKS("s_mul_i32 s48, s16, 72\n\ts_add_u32 s48, s48, 162080\n\tv_mov_b32 v224, s48\n\tv_mov_b32 v225, s74\n\tds_write_b32 v224, v225 offset:0\n\tv_mov_b32 v225, s75\n\tds_write_b32 v224, v225 offset:4\n\tv_mov_b32 v225, s76\n\tds_write_b32 v224, v225 offset:8\n\tv_mov_b32 v225, s77\n\tds_write_b32 v224, v225 offset:12\n\tv_mov_b32 v225, s78\n\tds_write_b32 v224, v225 offset:16\n\tv_mov_b32 v225, s79\n\tds_write_b32 v224, v225 offset:20\n\tv_mov_b32 v225, s80\n\tds_write_b32 v224, v225 offset:24\n\tv_mov_b32 v225, s81\n\tds_write_b32 v224, v225 offset:28\n\tv_mov_b32 v225, s82\n\tds_write_b32 v224, v225 offset:32\n\tv_mov_b32 v225, s83\n\tds_write_b32 v224, v225 offset:36\n\tv_mov_b32 v225, s84\n\tds_write_b32 v224, v225 offset:40\n\tv_mov_b32 v225, s85\n\tds_write_b32 v224, v225 offset:44\n\tv_mov_b32 v225, s86\n\tds_write_b32 v224, v225 offset:48\n\tv_mov_b32 v225, s87\n\tds_write_b32 v224, v225 offset:52\n\tv_mov_b32 v225, s88\n\tds_write_b32 v224, v225 offset:56\n\tv_mov_b32 v225, s89\n\tds_write_b32 v224, v225 offset:60\n\tv_mov_b32 v225, s90\n\tds_write_b32 v224, v225 offset:64\n\tv_mov_b32 v225, s91\n\tds_write_b32 v224, v225 offset:68\n\t")
      "s_buffer_load_dword s71, s[12:15], s69 glc\n\ts_add_u32 s50, s56, s64\n\ts_add_u32 m0, s49, 0\n\ts_add_u32 s48, s50, 0x0\n\t"
      "buffer_load_dwordx4 v20, s[40:43], s48 offen  lds\n\ts_add_u32 m0, s49, 1024\n\ts_add_u32 s48, s50, 0x10000\n\tbuffer_load_dwordx4 v21, s[40:43], s48 offen  lds\n\t"
      "s_add_u32 m0, s49, 2048\n\ts_add_u32 s48, s50, 0x20000\n\tbuffer_load_dwordx4 v20, s[40:43], s48 offen  lds\n\ts_add_u32 m0, s49, 3072\n\ts_add_u32 s48, s50, 0x30000\n\t"
      "buffer_load_dwordx4 v21, s[40:43], s48 offen  lds\n\ts_add_u32 m0, s49, 4096\n\ts_add_u32 s48, s50, 0x40000\n\tbuffer_load_dwordx4 v20, s[40:43], s48 offen  lds\n\t"
      "s_add_u32 m0, s49, 5120\n\ts_add_u32 s48, s50, 0x50000\n\tbuffer_load_dwordx4 v21, s[40:43], s48 offen  lds\n\ts_add_u32 m0, s49, 6144\n\ts_add_u32 s48, s50, 0x60000\n\t"
      "buffer_load_dwordx4 v20, s[40:43], s48 offen  lds\n\ts_add_u32 m0, s49, 7168\n\ts_add_u32 s48, s50, 0x70000\n\tbuffer_load_dwordx4 v21, s[40:43], s48 offen  lds\n\t"
      "s_waitcnt lgkmcnt(0)\n\ts_bitcmp0_b32 s68, 0\n\ts_cbranch_scc1 5f\n\ts_add_u32 s71, s71, 1\n\ts_lshl_b32 s48, s69, 4\n\ts_add_u32 s48, s48, 36864\n\t"
      "v_mov_b32 v17, s71\n\ts_mov_b64 exec, 1\n\tbuffer_store_dword v17, off, s[12:15], s48 sc1\n\ts_mov_b64 exec, -1\n\t"
      "5:\n\ts_add_u32 s50, s56, s65\n\ts_add_u32 m0, s49, 8192\n\ts_add_u32 s48, s50, 0x0\n\tbuffer_load_dwordx4 v20, s[40:43], s48 offen  lds\n\ts_add_u32 m0, s49, 9216\n\t"
      "s_add_u32 s48, s50, 0x10000\n\tbuffer_load_dwordx4 v21, s[40:43], s48 offen  lds\n\ts_add_u32 m0, s49, 10240\n\ts_add_u32 s48, s50, 0x20000\n\t"
      "buffer_load_dwordx4 v20, s[40:43], s48 offen  lds\n\ts_add_u32 m0, s49, 11264\n\ts_add_u32 s48, s50, 0x30000\n\tbuffer_load_dwordx4 v21, s[40:43], s48 offen  lds\n\t"
      "s_add_u32 m0, s49, 12288\n\ts_add_u32 s48, s50, 0x40000\n\tbuffer_load_dwordx4 v20, s[40:43], s48 offen  lds\n\ts_add_u32 m0, s49, 13312\n\t"
      "s_add_u32 s48, s50, 0x50000\n\tbuffer_load_dwordx4 v21, s[40:43], s48 offen  lds\n\ts_add_u32 m0, s49, 14336\n\ts_add_u32 s48, s50, 0x60000\n\t"
      "buffer_load_dwordx4 v20, s[40:43], s48 offen  lds\n\ts_add_u32 m0, s49, 15360\n\ts_add_u32 s48, s50, 0x70000\n\tbuffer_load_dwordx4 v21, s[40:43], s48 offen  lds\n\t"
      "s_add_u32 s50, s56, s66\n\ts_add_u32 m0, s49, 16384\n\ts_add_u32 s48, s50, 0x0\n\tbuffer_load_dwordx4 v20, s[40:43], s48 offen  lds\n\ts_add_u32 m0, s49, 17408\n\t"
      "s_add_u32 s48, s50, 0x10000\n\tbuffer_load_dwordx4 v21, s[40:43], s48 offen  lds\n\ts_add_u32 m0, s49, 18432\n\ts_add_u32 s48, s50, 0x20000\n\t"
      "buffer_load_dwordx4 v20, s[40:43], s48 offen  lds\n\ts_add_u32 m0, s49, 19456\n\ts_add_u32 s48, s50, 0x30000\n\tbuffer_load_dwordx4 v21, s[40:43], s48 offen  lds\n\t"
      "s_add_u32 m0, s49, 20480\n\ts_add_u32 s48, s50, 0x40000\n\tbuffer_load_dwordx4 v20, s[40:43], s48 offen  lds\n\ts_add_u32 m0, s49, 21504\n\t"
      "s_add_u32 s48, s50, 0x50000\n\tbuffer_load_dwordx4 v21, s[40:43], s48 offen  lds\n\ts_add_u32 m0, s49, 22528\n\ts_add_u32 s48, s50, 0x60000\n\t"
      "buffer_load_dwordx4 v20, s[40:43], s48 offen  lds\n\ts_add_u32 m0, s49, 23552\n\ts_add_u32 s48, s50, 0x70000\n\tbuffer_load_dwordx4 v21, s[40:43], s48 offen  lds\n\t"
      "s_cmp_lt_u32 s68, 1\n\ts_cbranch_scc1 7f\n\ts_cmp_eq_u32 s68, 2\n\ts_cbranch_scc1 6f\n\t"
      "5:\n\ts_buffer_load_dword s48, s[12:15], s69 glc\n\ts_waitcnt lgkmcnt(0)\n\ts_cmp_eq_u32 s48, s71\n\ts_cbranch_scc1 6f\n\ts_cmp_lg_u32 s68, 3\n\ts_cbranch_scc1 7f\n\t"
      "s_cmp_eq_u32 s18, 0\n\ts_cbranch_scc1 6f\n\ts_sub_u32 s18, s18, 1\n\ts_branch 5b\n\t"
      "6:\n\tv_mov_b32 v17, 1\n\ts_mov_b64 exec, 1\n\tbuffer_atomic_add v17, off, s[12:15], s70\n\ts_mov_b64 exec, -1\n\ts_mov_b32 s68, 0\n\t"
      "7:\n\ts_waitcnt vmcnt(16)\n\tds_read_b64_tr_b16 a[0:1], v22 offset:0\n\tds_read_b64_tr_b16 a[2:3], v22 offset:256\n\tds_read_b64_tr_b16 a[4:5], v22 offset:4096\n\t"
      "ds_read_b64_tr_b16 a[6:7], v22 offset:4352\n\tds_read_b64_tr_b16 a[8:9], v23 offset:0\n\tds_read_b64_tr_b16 a[10:11], v23 offset:256\n\t"
      "ds_read_b64_tr_b16 a[12:13], v23 offset:4096\n\tds_read_b64_tr_b16 a[14:15], v23 offset:4352\n\tds_read_b64_tr_b16 v[32:33], v24 offset:0\n\t"
      "ds_read_b64_tr_b16 v[34:35], v24 offset:256\n\tds_read_b64_tr_b16 v[36:37], v24 offset:4096\n\tds_read_b64_tr_b16 v[38:39], v24 offset:4352\n\t"
      "ds_read_b64_tr_b16 v[152:153], v25 offset:0\n\tds_read_b64_tr_b16 v[154:155], v25 offset:256\n\tds_read_b64_tr_b16 v[156:157], v25 offset:4096\n\t"
      "ds_read_b64_tr_b16 v[158:159], v25 offset:4352\n\ts_waitcnt lgkmcnt(0)\n\ts_add_u32 s50, s56, s67\n\ts_add_u32 m0, s49, 24576\n\ts_add_u32 s48, s50, 0x0\n\t"
      "buffer_load_dwordx4 v20, s[40:43], s48 offen  lds\n\ts_add_u32 m0, s49, 25600\n\ts_add_u32 s48, s50, 0x10000\n\tbuffer_load_dwordx4 v21, s[40:43], s48 offen  lds\n\t"
      "s_add_u32 m0, s49, 26624\n\ts_add_u32 s48, s50, 0x20000\n\tbuffer_load_dwordx4 v20, s[40:43], s48 offen  lds\n\ts_add_u32 m0, s49, 27648\n\t"
      "s_add_u32 s48, s50, 0x30000\n\tbuffer_load_dwordx4 v21, s[40:43], s48 offen  lds\n\ts_add_u32 m0, s49, 28672\n\ts_add_u32 s48, s50, 0x40000\n\t"
      "buffer_load_dwordx4 v20, s[40:43], s48 offen  lds\n\ts_add_u32 m0, s49, 29696\n\ts_add_u32 s48, s50, 0x50000\n\tbuffer_load_dwordx4 v21, s[40:43], s48 offen  lds\n\t"
      "s_add_u32 m0, s49, 30720\n\ts_add_u32 s48, s50, 0x60000\n\tbuffer_load_dwordx4 v20, s[40:43], s48 offen  lds\n\ts_add_u32 m0, s49, 31744\n\t"
      "s_add_u32 s48, s50, 0x70000\n\tbuffer_load_dwordx4 v21, s[40:43], s48 offen  lds\n\ts_cmp_lt_u32 s68, 1\n\ts_cbranch_scc1 7f\n\ts_cmp_eq_u32 s68, 2\n\t"
      "s_cbranch_scc1 6f\n\t"
      "5:\n\ts_buffer_load_dword s48, s[12:15], s69 glc\n\ts_waitcnt lgkmcnt(0)\n\ts_cmp_eq_u32 s48, s71\n\ts_cbranch_scc0 7f\n\t"
      "6:\n\tv_mov_b32 v17, 1\n\ts_mov_b64 exec, 1\n\tbuffer_atomic_add v17, off, s[12:15], s70\n\ts_mov_b64 exec, -1\n\ts_mov_b32 s68, 0\n\t"
      "7:\n\ts_waitcnt vmcnt(16)\n\tds_read_b64_tr_b16 a[16:17], v22 offset:8192\n\tds_read_b64_tr_b16 a[18:19], v22 offset:8448\n\t"
      "ds_read_b64_tr_b16 a[20:21], v22 offset:12288\n\tds_read_b64_tr_b16 a[22:23], v22 offset:12544\n\tds_read_b64_tr_b16 a[24:25], v23 offset:8192\n\t"
      "ds_read_b64_tr_b16 a[26:27], v23 offset:8448\n\tds_read_b64_tr_b16 a[28:29], v23 offset:12288\n\tds_read_b64_tr_b16 a[30:31], v23 offset:12544\n\t"
      "ds_read_b64_tr_b16 v[40:41], v24 offset:8192\n\tds_read_b64_tr_b16 v[42:43], v24 offset:8448\n\tds_read_b64_tr_b16 v[44:45], v24 offset:12288\n\t"
      "ds_read_b64_tr_b16 v[46:47], v24 offset:12544\n\tds_read_b64_tr_b16 v[160:161], v25 offset:8192\n\tds_read_b64_tr_b16 v[162:163], v25 offset:8448\n\t"
      "ds_read_b64_tr_b16 v[164:165], v25 offset:12288\n\tds_read_b64_tr_b16 v[166:167], v25 offset:12544\n\ts_waitcnt lgkmcnt(0)\n\ts_add_u32 s50, s57, s64\n\t"
      "s_add_u32 m0, s49, 0\n\ts_add_u32 s48, s50, 0x0\n\tbuffer_load_dwordx4 v20, s[40:43], s48 offen  lds\n\ts_add_u32 m0, s49, 1024\n\ts_add_u32 s48, s50, 0x10000\n\t"
      "buffer_load_dwordx4 v21, s[40:43], s48 offen  lds\n\ts_add_u32 m0, s49, 2048\n\ts_add_u32 s48, s50, 0x20000\n\tbuffer_load_dwordx4 v20, s[40:43], s48 offen  lds\n\t"
      "s_add_u32 m0, s49, 3072\n\ts_add_u32 s48, s50, 0x30000\n\tbuffer_load_dwordx4 v21, s[40:43], s48 offen  lds\n\ts_add_u32 m0, s49, 4096\n\ts_add_u32 s48, s50, 0x40000\n\t"
      "buffer_load_dwordx4 v20, s[40:43], s48 offen  lds\n\ts_add_u32 m0, s49, 5120\n\ts_add_u32 s48, s50, 0x50000\n\tbuffer_load_dwordx4 v21, s[40:43], s48 offen  lds\n\t"
      "s_add_u32 m0, s49, 6144\n\ts_add_u32 s48, s50, 0x60000\n\tbuffer_load_dwordx4 v20, s[40:43], s48 offen  lds\n\ts_add_u32 m0, s49, 7168\n\ts_add_u32 s48, s50, 0x70000\n\t"
      "buffer_load_dwordx4 v21, s[40:43], s48 offen  lds\n\ts_cmp_lt_u32 s68, 1\n\ts_cbranch_scc1 7f\n\ts_cmp_eq_u32 s68, 2\n\ts_cbranch_scc1 6f\n\t"
      "5:\n\ts_buffer_load_dword s48, s[12:15], s69 glc\n\ts_waitcnt lgkmcnt(0)\n\ts_cmp_eq_u32 s48, s71\n\ts_cbranch_scc0 7f\n\t"
      "6:\n\tv_mov_b32 v17, 1\n\ts_mov_b64 exec, 1\n\tbuffer_atomic_add v17, off, s[12:15], s70\n\ts_mov_b64 exec, -1\n\ts_mov_b32 s68, 0\n\t"
      "7:\n\ts_waitcnt vmcnt(16)\n\tds_read_b64_tr_b16 a[32:33], v22 offset:16384\n\tds_read_b64_tr_b16 a[34:35], v22 offset:16640\n\t"
      "ds_read_b64_tr_b16 a[36:37], v22 offset:20480\n\tds_read_b64_tr_b16 a[38:39], v22 offset:20736\n\tds_read_b64_tr_b16 a[40:41], v23 offset:16384\n\t"
      "ds_read_b64_tr_b16 a[42:43], v23 offset:16640\n\tds_read_b64_tr_b16 a[44:45], v23 offset:20480\n\tds_read_b64_tr_b16 a[46:47], v23 offset:20736\n\t"
      "ds_read_b64_tr_b16 v[48:49], v24 offset:16384\n\tds_read_b64_tr_b16 v[50:51], v24 offset:16640\n\tds_read_b64_tr_b16 v[52:53], v24 offset:20480\n\t"
      "ds_read_b64_tr_b16 v[54:55], v24 offset:20736\n\tds_read_b64_tr_b16 v[168:169], v25 offset:16384\n\tds_read_b64_tr_b16 v[170:171], v25 offset:16640\n\t"
      "ds_read_b64_tr_b16 v[172:173], v25 offset:20480\n\tds_read_b64_tr_b16 v[174:175], v25 offset:20736\n\ts_waitcnt lgkmcnt(0)\n\ts_add_u32 s50, s58, s64\n\t"
      "s_add_u32 m0, s49, 8192\n\ts_add_u32 s48, s50, 0x0\n\tbuffer_load_dwordx4 v20, s[40:43], s48 offen  lds\n\ts_add_u32 m0, s49, 9216\n\ts_add_u32 s48, s50, 0x10000\n\t"
      "buffer_load_dwordx4 v21, s[40:43], s48 offen  lds\n\ts_add_u32 m0, s49, 10240\n\ts_add_u32 s48, s50, 0x20000\n\tbuffer_load_dwordx4 v20, s[40:43], s48 offen  lds\n\t"
      "s_add_u32 m0, s49, 11264\n\ts_add_u32 s48, s50, 0x30000\n\tbuffer_load_dwordx4 v21, s[40:43], s48 offen  lds\n\ts_add_u32 m0, s49, 12288\n\t"
      "s_add_u32 s48, s50, 0x40000\n\tbuffer_load_dwordx4 v20, s[40:43], s48 offen  lds\n\ts_add_u32 m0, s49, 13312\n\ts_add_u32 s48, s50, 0x50000\n\t"
      "buffer_load_dwordx4 v21, s[40:43], s48 offen  lds\n\ts_add_u32 m0, s49, 14336\n\ts_add_u32 s48, s50, 0x60000\n\tbuffer_load_dwordx4 v20, s[40:43], s48 offen  lds\n\t"
      "s_add_u32 m0, s49, 15360\n\ts_add_u32 s48, s50, 0x70000\n\tbuffer_load_dwordx4 v21, s[40:43], s48 offen  lds\n\ts_cmp_lt_u32 s68, 1\n\ts_cbranch_scc1 7f\n\t"
      "s_cmp_eq_u32 s68, 2\n\ts_cbranch_scc1 6f\n\t"
      "5:\n\ts_buffer_load_dword s48, s[12:15], s69 glc\n\ts_waitcnt lgkmcnt(0)\n\ts_cmp_eq_u32 s48, s71\n\ts_cbranch_scc0 7f\n\t"
      "6:\n\tv_mov_b32 v17, 1\n\ts_mov_b64 exec, 1\n\tbuffer_atomic_add v17, off, s[12:15], s70\n\ts_mov_b64 exec, -1\n\ts_mov_b32 s68, 0\n\t"
      "7:\n\ts_waitcnt vmcnt(16)\n\tds_read_b64_tr_b16 a[48:49], v22 offset:24576\n\tds_read_b64_tr_b16 a[50:51], v22 offset:24832\n\t"
      "ds_read_b64_tr_b16 a[52:53], v22 offset:28672\n\tds_read_b64_tr_b16 a[54:55], v22 offset:28928\n\tds_read_b64_tr_b16 a[56:57], v23 offset:24576\n\t"
      "ds_read_b64_tr_b16 a[58:59], v23 offset:24832\n\tds_read_b64_tr_b16 a[60:61], v23 offset:28672\n\tds_read_b64_tr_b16 a[62:63], v23 offset:28928\n\t"
      "ds_read_b64_tr_b16 v[56:57], v24 offset:24576\n\tds_read_b64_tr_b16 v[58:59], v24 offset:24832\n\tds_read_b64_tr_b16 v[60:61], v24 offset:28672\n\t"
      "ds_read_b64_tr_b16 v[62:63], v24 offset:28928\n\tds_read_b64_tr_b16 v[176:177], v25 offset:24576\n\tds_read_b64_tr_b16 v[178:179], v25 offset:24832\n\t"
      "ds_read_b64_tr_b16 v[180:181], v25 offset:28672\n\tds_read_b64_tr_b16 v[182:183], v25 offset:28928\n\ts_waitcnt lgkmcnt(0)\n\ts_add_u32 s50, s59, s64\n\t"
      "s_add_u32 m0, s49, 16384\n\ts_add_u32 s48, s50, 0x0\n\tbuffer_load_dwordx4 v20, s[40:43], s48 offen  lds\n\ts_add_u32 m0, s49, 17408\n\ts_add_u32 s48, s50, 0x10000\n\t"
      "buffer_load_dwordx4 v21, s[40:43], s48 offen  lds\n\ts_add_u32 m0, s49, 18432\n\ts_add_u32 s48, s50, 0x20000\n\tbuffer_load_dwordx4 v20, s[40:43], s48 offen  lds\n\t"
      "s_add_u32 m0, s49, 19456\n\ts_add_u32 s48, s50, 0x30000\n\tbuffer_load_dwordx4 v21, s[40:43], s48 offen  lds\n\ts_add_u32 m0, s49, 20480\n\t"
      "s_add_u32 s48, s50, 0x40000\n\tbuffer_load_dwordx4 v20, s[40:43], s48 offen  lds\n\ts_add_u32 m0, s49, 21504\n\ts_add_u32 s48, s50, 0x50000\n\t"
      "buffer_load_dwordx4 v21, s[40:43], s48 offen  lds\n\ts_add_u32 m0, s49, 22528\n\ts_add_u32 s48, s50, 0x60000\n\tbuffer_load_dwordx4 v20, s[40:43], s48 offen  lds\n\t"
      "s_add_u32 m0, s49, 23552\n\ts_add_u32 s48, s50, 0x70000\n\tbuffer_load_dwordx4 v21, s[40:43], s48 offen  lds\n\ts_cmp_lt_u32 s68, 1\n\ts_cbranch_scc1 7f\n\t"
      "s_cmp_eq_u32 s68, 2\n\ts_cbranch_scc1 6f\n\t"
      "5:\n\ts_buffer_load_dword s48, s[12:15], s69 glc\n\ts_waitcnt lgkmcnt(0)\n\ts_cmp_eq_u32 s48, s71\n\ts_cbranch_scc0 7f\n\t"
      "6:\n\tv_mov_b32 v17, 1\n\ts_mov_b64 exec, 1\n\tbuffer_atomic_add v17, off, s[12:15], s70\n\ts_mov_b64 exec, -1\n\ts_mov_b32 s68, 0\n\t"
      "7:\n\ts_waitcnt vmcnt(16)\n\tds_read_b64_tr_b16 a[64:65], v22 offset:0\n\tds_read_b64_tr_b16 a[66:67], v22 offset:256\n\tds_read_b64_tr_b16 a[68:69], v22 offset:4096\n\t"
      "ds_read_b64_tr_b16 a[70:71], v22 offset:4352\n\tds_read_b64_tr_b16 a[72:73], v23 offset:0\n\tds_read_b64_tr_b16 a[74:75], v23 offset:256\n\t"
      "ds_read_b64_tr_b16 a[76:77], v23 offset:4096\n\tds_read_b64_tr_b16 a[78:79], v23 offset:4352\n\tds_read_b64_tr_b16 v[64:65], v24 offset:0\n\t"
      "ds_read_b64_tr_b16 v[66:67], v24 offset:256\n\tds_read_b64_tr_b16 v[68:69], v24 offset:4096\n\tds_read_b64_tr_b16 v[70:71], v24 offset:4352\n\t"
      "ds_read_b64_tr_b16 v[184:185], v25 offset:0\n\tds_read_b64_tr_b16 v[186:187], v25 offset:256\n\tds_read_b64_tr_b16 v[188:189], v25 offset:4096\n\t"
      "ds_read_b64_tr_b16 v[190:191], v25 offset:4352\n\ts_waitcnt lgkmcnt(0)\n\ts_add_u32 s50, s57, s65\n\ts_add_u32 m0, s49, 24576\n\ts_add_u32 s48, s50, 0x0\n\t"
      "buffer_load_dwordx4 v20, s[40:43], s48 offen  lds\n\ts_add_u32 m0, s49, 25600\n\ts_add_u32 s48, s50, 0x10000\n\tbuffer_load_dwordx4 v21, s[40:43], s48 offen  lds\n\t"
      "s_add_u32 m0, s49, 26624\n\ts_add_u32 s48, s50, 0x20000\n\tbuffer_load_dwordx4 v20, s[40:43], s48 offen  lds\n\ts_add_u32 m0, s49, 27648\n\t"
      "s_add_u32 s48, s50, 0x30000\n\tbuffer_load_dwordx4 v21, s[40:43], s48 offen  lds\n\ts_add_u32 m0, s49, 28672\n\ts_add_u32 s48, s50, 0x40000\n\t"
      "buffer_load_dwordx4 v20, s[40:43], s48 offen  lds\n\ts_add_u32 m0, s49, 29696\n\ts_add_u32 s48, s50, 0x50000\n\tbuffer_load_dwordx4 v21, s[40:43], s48 offen  lds\n\t"
      "s_add_u32 m0, s49, 30720\n\ts_add_u32 s48, s50, 0x60000\n\tbuffer_load_dwordx4 v20, s[40:43], s48 offen  lds\n\ts_add_u32 m0, s49, 31744\n\t"
      "s_add_u32 s48, s50, 0x70000\n\tbuffer_load_dwordx4 v21, s[40:43], s48 offen  lds\n\ts_cmp_lt_u32 s68, 1\n\ts_cbranch_scc1 7f\n\ts_cmp_eq_u32 s68, 2\n\t"
      "s_cbranch_scc1 6f\n\t"
      "5:\n\ts_buffer_load_dword s48, s[12:15], s69 glc\n\ts_waitcnt lgkmcnt(0)\n\ts_cmp_eq_u32 s48, s71\n\ts_cbranch_scc0 7f\n\t"
      "6:\n\tv_mov_b32 v17, 1\n\ts_mov_b64 exec, 1\n\tbuffer_atomic_add v17, off, s[12:15], s70\n\ts_mov_b64 exec, -1\n\ts_mov_b32 s68, 0\n\t"
      "7:\n\ts_waitcnt vmcnt(16)\n\tds_read_b64_tr_b16 a[80:81], v22 offset:8192\n\tds_read_b64_tr_b16 a[82:83], v22 offset:8448\n\t"
      "ds_read_b64_tr_b16 a[84:85], v22 offset:12288\n\tds_read_b64_tr_b16 a[86:87], v22 offset:12544\n\tds_read_b64_tr_b16 a[88:89], v23 offset:8192\n\t"
      "ds_read_b64_tr_b16 a[90:91], v23 offset:8448\n\tds_read_b64_tr_b16 a[92:93], v23 offset:12288\n\tds_read_b64_tr_b16 a[94:95], v23 offset:12544\n\t"
      "ds_read_b64_tr_b16 v[72:73], v24 offset:8192\n\tds_read_b64_tr_b16 v[74:75], v24 offset:8448\n\tds_read_b64_tr_b16 v[76:77], v24 offset:12288\n\t"
      "ds_read_b64_tr_b16 v[78:79], v24 offset:12544\n\tds_read_b64_tr_b16 v[192:193], v25 offset:8192\n\tds_read_b64_tr_b16 v[194:195], v25 offset:8448\n\t"
      "ds_read_b64_tr_b16 v[196:197], v25 offset:12288\n\tds_read_b64_tr_b16 v[198:199], v25 offset:12544\n\ts_waitcnt lgkmcnt(0)\n\ts_add_u32 s50, s58, s65\n\t"
      "s_add_u32 m0, s49, 0\n\ts_add_u32 s48, s50, 0x0\n\tbuffer_load_dwordx4 v20, s[40:43], s48 offen  lds\n\ts_add_u32 m0, s49, 1024\n\ts_add_u32 s48, s50, 0x10000\n\t"
      "buffer_load_dwordx4 v21, s[40:43], s48 offen  lds\n\ts_add_u32 m0, s49, 2048\n\ts_add_u32 s48, s50, 0x20000\n\tbuffer_load_dwordx4 v20, s[40:43], s48 offen  lds\n\t"
      "s_add_u32 m0, s49, 3072\n\ts_add_u32 s48, s50, 0x30000\n\tbuffer_load_dwordx4 v21, s[40:43], s48 offen  lds\n\ts_add_u32 m0, s49, 4096\n\ts_add_u32 s48, s50, 0x40000\n\t"
      "buffer_load_dwordx4 v20, s[40:43], s48 offen  lds\n\ts_add_u32 m0, s49, 5120\n\ts_add_u32 s48, s50, 0x50000\n\tbuffer_load_dwordx4 v21, s[40:43], s48 offen  lds\n\t"
      "s_add_u32 m0, s49, 6144\n\ts_add_u32 s48, s50, 0x60000\n\tbuffer_load_dwordx4 v20, s[40:43], s48 offen  lds\n\ts_add_u32 m0, s49, 7168\n\ts_add_u32 s48, s50, 0x70000\n\t"
      "buffer_load_dwordx4 v21, s[40:43], s48 offen  lds\n\ts_cmp_lt_u32 s68, 1\n\ts_cbranch_scc1 7f\n\ts_cmp_eq_u32 s68, 2\n\ts_cbranch_scc1 6f\n\t"
      "5:\n\ts_buffer_load_dword s48, s[12:15], s69 glc\n\ts_waitcnt lgkmcnt(0)\n\ts_cmp_eq_u32 s48, s71\n\ts_cbranch_scc0 7f\n\t"
      "6:\n\tv_mov_b32 v17, 1\n\ts_mov_b64 exec, 1\n\tbuffer_atomic_add v17, off, s[12:15], s70\n\ts_mov_b64 exec, -1\n\ts_mov_b32 s68, 0\n\t"
      "7:\n\ts_waitcnt vmcnt(16)\n\tds_read_b64_tr_b16 a[96:97], v22 offset:16384\n\tds_read_b64_tr_b16 a[98:99], v22 offset:16640\n\t"
      "ds_read_b64_tr_b16 a[100:101], v22 offset:20480\n\tds_read_b64_tr_b16 a[102:103], v22 offset:20736\n\tds_read_b64_tr_b16 a[104:105], v23 offset:16384\n\t"
      "ds_read_b64_tr_b16 a[106:107], v23 offset:16640\n\tds_read_b64_tr_b16 a[108:109], v23 offset:20480\n\tds_read_b64_tr_b16 a[110:111], v23 offset:20736\n\t"
      "ds_read_b64_tr_b16 v[80:81], v24 offset:16384\n\tds_read_b64_tr_b16 v[82:83], v24 offset:16640\n\tds_read_b64_tr_b16 v[84:85], v24 offset:20480\n\t"
      "ds_read_b64_tr_b16 v[86:87], v24 offset:20736\n\tds_read_b64_tr_b16 v[200:201], v25 offset:16384\n\tds_read_b64_tr_b16 v[202:203], v25 offset:16640\n\t"
      "ds_read_b64_tr_b16 v[204:205], v25 offset:20480\n\tds_read_b64_tr_b16 v[206:207], v25 offset:20736\n\ts_waitcnt lgkmcnt(0)\n\ts_add_u32 s50, s59, s65\n\t"
      "s_add_u32 m0, s49, 8192\n\ts_add_u32 s48, s50, 0x0\n\tbuffer_load_dwordx4 v20, s[40:43], s48 offen  lds\n\ts_add_u32 m0, s49, 9216\n\ts_add_u32 s48, s50, 0x10000\n\t"
      "buffer_load_dwordx4 v21, s[40:43], s48 offen  lds\n\ts_add_u32 m0, s49, 10240\n\ts_add_u32 s48, s50, 0x20000\n\tbuffer_load_dwordx4 v20, s[40:43], s48 offen  lds\n\t"
      "s_add_u32 m0, s49, 11264\n\ts_add_u32 s48, s50, 0x30000\n\tbuffer_load_dwordx4 v21, s[40:43], s48 offen  lds\n\ts_add_u32 m0, s49, 12288\n\t"
      "s_add_u32 s48, s50, 0x40000\n\tbuffer_load_dwordx4 v20, s[40:43], s48 offen  lds\n\ts_add_u32 m0, s49, 13312\n\ts_add_u32 s48, s50, 0x50000\n\t"
      "buffer_load_dwordx4 v21, s[40:43], s48 offen  lds\n\ts_add_u32 m0, s49, 14336\n\ts_add_u32 s48, s50, 0x60000\n\tbuffer_load_dwordx4 v20, s[40:43], s48 offen  lds\n\t"
      "s_add_u32 m0, s49, 15360\n\ts_add_u32 s48, s50, 0x70000\n\tbuffer_load_dwordx4 v21, s[40:43], s48 offen  lds\n\ts_cmp_lt_u32 s68, 1\n\ts_cbranch_scc1 7f\n\t"
      "s_cmp_eq_u32 s68, 2\n\ts_cbranch_scc1 6f\n\t"
      "5:\n\ts_buffer_load_dword s48, s[12:15], s69 glc\n\ts_waitcnt lgkmcnt(0)\n\ts_cmp_eq_u32 s48, s71\n\ts_cbranch_scc0 7f\n\t"
      "6:\n\tv_mov_b32 v17, 1\n\ts_mov_b64 exec, 1\n\tbuffer_atomic_add v17, off, s[12:15], s70\n\ts_mov_b64 exec, -1\n\ts_mov_b32 s68, 0\n\t"
      "7:\n\ts_waitcnt vmcnt(16)\n\tds_read_b64_tr_b16 a[112:113], v22 offset:24576\n\tds_read_b64_tr_b16 a[114:115], v22 offset:24832\n\t"
      "ds_read_b64_tr_b16 a[116:117], v22 offset:28672\n\tds_read_b64_tr_b16 a[118:119], v22 offset:28928\n\tds_read_b64_tr_b16 a[120:121], v23 offset:24576\n\t"
      "ds_read_b64_tr_b16 a[122:123], v23 offset:24832\n\tds_read_b64_tr_b16 a[124:125], v23 offset:28672\n\tds_read_b64_tr_b16 a[126:127], v23 offset:28928\n\t"
      "ds_read_b64_tr_b16 v[88:89], v24 offset:24576\n\tds_read_b64_tr_b16 v[90:91], v24 offset:24832\n\tds_read_b64_tr_b16 v[92:93], v24 offset:28672\n\t"
      "ds_read_b64_tr_b16 v[94:95], v24 offset:28928\n\tds_read_b64_tr_b16 v[208:209], v25 offset:24576\n\tds_read_b64_tr_b16 v[210:211], v25 offset:24832\n\t"
      "ds_read_b64_tr_b16 v[212:213], v25 offset:28672\n\tds_read_b64_tr_b16 v[214:215], v25 offset:28928\n\ts_waitcnt lgkmcnt(0)\n\ts_add_u32 s50, s57, s66\n\t"
      "s_add_u32 m0, s49, 16384\n\ts_add_u32 s48, s50, 0x0\n\tbuffer_load_dwordx4 v20, s[40:43], s48 offen  lds\n\ts_add_u32 m0, s49, 17408\n\ts_add_u32 s48, s50, 0x10000\n\t"
      "buffer_load_dwordx4 v21, s[40:43], s48 offen  lds\n\ts_add_u32 m0, s49, 18432\n\ts_add_u32 s48, s50, 0x20000\n\tbuffer_load_dwordx4 v20, s[40:43], s48 offen  lds\n\t"
      "s_add_u32 m0, s49, 19456\n\ts_add_u32 s48, s50, 0x30000\n\tbuffer_load_dwordx4 v21, s[40:43], s48 offen  lds\n\ts_add_u32 m0, s49, 20480\n\t"
      "s_add_u32 s48, s50, 0x40000\n\tbuffer_load_dwordx4 v20, s[40:43], s48 offen  lds\n\ts_add_u32 m0, s49, 21504\n\ts_add_u32 s48, s50, 0x50000\n\t"
      "buffer_load_dwordx4 v21, s[40:43], s48 offen  lds\n\ts_add_u32 m0, s49, 22528\n\ts_add_u32 s48, s50, 0x60000\n\tbuffer_load_dwordx4 v20, s[40:43], s48 offen  lds\n\t"
      "s_add_u32 m0, s49, 23552\n\ts_add_u32 s48, s50, 0x70000\n\tbuffer_load_dwordx4 v21, s[40:43], s48 offen  lds\n\ts_cmp_lt_u32 s68, 1\n\ts_cbranch_scc1 7f\n\t"
      "s_cmp_eq_u32 s68, 2\n\ts_cbranch_scc1 6f\n\t"
      "5:\n\ts_buffer_load_dword s48, s[12:15], s69 glc\n\ts_waitcnt lgkmcnt(0)\n\ts_cmp_eq_u32 s48, s71\n\ts_cbranch_scc0 7f\n\t"
      "6:\n\tv_mov_b32 v17, 1\n\ts_mov_b64 exec, 1\n\tbuffer_atomic_add v17, off, s[12:15], s70\n\ts_mov_b64 exec, -1\n\ts_mov_b32 s68, 0\n\t"
      "7:\n\ts_waitcnt vmcnt(16)\n\tds_read_b64_tr_b16 a[128:129], v22 offset:0\n\tds_read_b64_tr_b16 a[130:131], v22 offset:256\n\t"
      "ds_read_b64_tr_b16 a[132:133], v22 offset:4096\n\tds_read_b64_tr_b16 a[134:135], v22 offset:4352\n\tds_read_b64_tr_b16 a[136:137], v23 offset:0\n\t"
      "ds_read_b64_tr_b16 a[138:139], v23 offset:256\n\tds_read_b64_tr_b16 a[140:141], v23 offset:4096\n\tds_read_b64_tr_b16 a[142:143], v23 offset:4352\n\t"
      "ds_read_b64_tr_b16 v[96:97], v24 offset:0\n\tds_read_b64_tr_b16 v[98:99], v24 offset:256\n\tds_read_b64_tr_b16 v[100:101], v24 offset:4096\n\t"
      "ds_read_b64_tr_b16 v[102:103], v24 offset:4352\n\tds_read_b64_tr_b16 v[216:217], v25 offset:0\n\tds_read_b64_tr_b16 v[218:219], v25 offset:256\n\t"
      "ds_read_b64_tr_b16 v[220:221], v25 offset:4096\n\tds_read_b64_tr_b16 v[222:223], v25 offset:4352\n\ts_waitcnt lgkmcnt(0)\n\ts_add_u32 s50, s58, s66\n\t"
      "s_add_u32 m0, s49, 24576\n\ts_add_u32 s48, s50, 0x0\n\tbuffer_load_dwordx4 v20, s[40:43], s48 offen  lds\n\ts_add_u32 m0, s49, 25600\n\ts_add_u32 s48, s50, 0x10000\n\t"
      "buffer_load_dwordx4 v21, s[40:43], s48 offen  lds\n\ts_add_u32 m0, s49, 26624\n\ts_add_u32 s48, s50, 0x20000\n\tbuffer_load_dwordx4 v20, s[40:43], s48 offen  lds\n\t"
      "s_add_u32 m0, s49, 27648\n\ts_add_u32 s48, s50, 0x30000\n\tbuffer_load_dwordx4 v21, s[40:43], s48 offen  lds\n\ts_add_u32 m0, s49, 28672\n\t"
      "s_add_u32 s48, s50, 0x40000\n\tbuffer_load_dwordx4 v20, s[40:43], s48 offen  lds\n\ts_add_u32 m0, s49, 29696\n\ts_add_u32 s48, s50, 0x50000\n\t"
      "buffer_load_dwordx4 v21, s[40:43], s48 offen  lds\n\ts_add_u32 m0, s49, 30720\n\ts_add_u32 s48, s50, 0x60000\n\tbuffer_load_dwordx4 v20, s[40:43], s48 offen  lds\n\t"
      "s_add_u32 m0, s49, 31744\n\ts_add_u32 s48, s50, 0x70000\n\tbuffer_load_dwordx4 v21, s[40:43], s48 offen  lds\n\ts_cmp_lt_u32 s68, 1\n\ts_cbranch_scc1 7f\n\t"
      "s_cmp_eq_u32 s68, 2\n\ts_cbranch_scc1 6f\n\t"
      "5:\n\ts_buffer_load_dword s48, s[12:15], s69 glc\n\ts_waitcnt lgkmcnt(0)\n\ts_cmp_eq_u32 s48, s71\n\ts_cbranch_scc0 7f\n\t"
      "6:\n\tv_mov_b32 v17, 1\n\ts_mov_b64 exec, 1\n\tbuffer_atomic_add v17, off, s[12:15], s70\n\ts_mov_b64 exec, -1\n\ts_mov_b32 s68, 0\n\t"
      "7:\n\ts_waitcnt vmcnt(16)\n\tds_read_b64_tr_b16 a[144:145], v22 offset:8192\n\tds_read_b64_tr_b16 a[146:147], v22 offset:8448\n\t"
      "ds_read_b64_tr_b16 a[148:149], v22 offset:12288\n\tds_read_b64_tr_b16 a[150:151], v22 offset:12544\n\tds_read_b64_tr_b16 a[152:153], v23 offset:8192\n\t"
      "ds_read_b64_tr_b16 a[154:155], v23 offset:8448\n\tds_read_b64_tr_b16 a[156:157], v23 offset:12288\n\tds_read_b64_tr_b16 a[158:159], v23 offset:12544\n\t"
      "ds_read_b64_tr_b16 v[104:105], v24 offset:8192\n\tds_read_b64_tr_b16 v[106:107], v24 offset:8448\n\tds_read_b64_tr_b16 v[108:109], v24 offset:12288\n\t"
      "ds_read_b64_tr_b16 v[110:111], v24 offset:12544\n\tds_read_b64_tr_b16 v[224:225], v25 offset:8192\n\tds_read_b64_tr_b16 v[226:227], v25 offset:8448\n\t"
      "ds_read_b64_tr_b16 v[228:229], v25 offset:12288\n\tds_read_b64_tr_b16 v[230:231], v25 offset:12544\n\ts_waitcnt lgkmcnt(0)\n\ts_add_u32 s50, s59, s66\n\t"
      "s_add_u32 m0, s49, 0\n\ts_add_u32 s48, s50, 0x0\n\tbuffer_load_dwordx4 v20, s[40:43], s48 offen  lds\n\ts_add_u32 m0, s49, 1024\n\ts_add_u32 s48, s50, 0x10000\n\t"
      "buffer_load_dwordx4 v21, s[40:43], s48 offen  lds\n\ts_add_u32 m0, s49, 2048\n\ts_add_u32 s48, s50, 0x20000\n\tbuffer_load_dwordx4 v20, s[40:43], s48 offen  lds\n\t"
      "s_add_u32 m0, s49, 3072\n\ts_add_u32 s48, s50, 0x30000\n\tbuffer_load_dwordx4 v21, s[40:43], s48 offen  lds\n\ts_add_u32 m0, s49, 4096\n\ts_add_u32 s48, s50, 0x40000\n\t"
      "buffer_load_dwordx4 v20, s[40:43], s48 offen  lds\n\ts_add_u32 m0, s49, 5120\n\ts_add_u32 s48, s50, 0x50000\n\tbuffer_load_dwordx4 v21, s[40:43], s48 offen  lds\n\t"
      "s_add_u32 m0, s49, 6144\n\ts_add_u32 s48, s50, 0x60000\n\tbuffer_load_dwordx4 v20, s[40:43], s48 offen  lds\n\ts_add_u32 m0, s49, 7168\n\ts_add_u32 s48, s50, 0x70000\n\t"
      "buffer_load_dwordx4 v21, s[40:43], s48 offen  lds\n\ts_waitcnt vmcnt(16)\n\tds_read_b64_tr_b16 a[160:161], v22 offset:16384\n\t"
      "ds_read_b64_tr_b16 a[162:163], v22 offset:16640\n\tds_read_b64_tr_b16 a[164:165], v22 offset:20480\n\tds_read_b64_tr_b16 a[166:167], v22 offset:20736\n\t"
      "ds_read_b64_tr_b16 a[168:169], v23 offset:16384\n\tds_read_b64_tr_b16 a[170:171], v23 offset:16640\n\tds_read_b64_tr_b16 a[172:173], v23 offset:20480\n\t"
      "ds_read_b64_tr_b16 a[174:175], v23 offset:20736\n\tds_read_b64_tr_b16 v[112:113], v24 offset:16384\n\tds_read_b64_tr_b16 v[114:115], v24 offset:16640\n\t"
      "ds_read_b64_tr_b16 v[116:117], v24 offset:20480\n\tds_read_b64_tr_b16 v[118:119], v24 offset:20736\n\tds_read_b64_tr_b16 v[232:233], v25 offset:16384\n\t"
      "ds_read_b64_tr_b16 v[234:235], v25 offset:16640\n\tds_read_b64_tr_b16 v[236:237], v25 offset:20480\n\tds_read_b64_tr_b16 v[238:239], v25 offset:20736\n\t"
      "s_waitcnt lgkmcnt(0)\n\ts_add_u32 s50, s57, s67\n\ts_add_u32 m0, s49, 8192\n\ts_add_u32 s48, s50, 0x0\n\tbuffer_load_dwordx4 v20, s[40:43], s48 offen  lds\n\t"
      "s_add_u32 m0, s49, 9216\n\ts_add_u32 s48, s50, 0x10000\n\tbuffer_load_dwordx4 v21, s[40:43], s48 offen  lds\n\ts_add_u32 m0, s49, 10240\n\ts_add_u32 s48, s50, 0x20000\n\t"
      "buffer_load_dwordx4 v20, s[40:43], s48 offen  lds\n\ts_add_u32 m0, s49, 11264\n\ts_add_u32 s48, s50, 0x30000\n\tbuffer_load_dwordx4 v21, s[40:43], s48 offen  lds\n\t"
      "s_add_u32 m0, s49, 12288\n\ts_add_u32 s48, s50, 0x40000\n\tbuffer_load_dwordx4 v20, s[40:43], s48 offen  lds\n\ts_add_u32 m0, s49, 13312\n\t"
      "s_add_u32 s48, s50, 0x50000\n\tbuffer_load_dwordx4 v21, s[40:43], s48 offen  lds\n\ts_add_u32 m0, s49, 14336\n\ts_add_u32 s48, s50, 0x60000\n\t"
      "buffer_load_dwordx4 v20, s[40:43], s48 offen  lds\n\ts_add_u32 m0, s49, 15360\n\ts_add_u32 s48, s50, 0x70000\n\tbuffer_load_dwordx4 v21, s[40:43], s48 offen  lds\n\t"
      "s_waitcnt vmcnt(16)\n\tds_read_b64_tr_b16 a[176:177], v22 offset:24576\n\tds_read_b64_tr_b16 a[178:179], v22 offset:24832\n\t"
      "ds_read_b64_tr_b16 a[180:181], v22 offset:28672\n\tds_read_b64_tr_b16 a[182:183], v22 offset:28928\n\tds_read_b64_tr_b16 a[184:185], v23 offset:24576\n\t"
      "ds_read_b64_tr_b16 a[186:187], v23 offset:24832\n\tds_read_b64_tr_b16 a[188:189], v23 offset:28672\n\tds_read_b64_tr_b16 a[190:191], v23 offset:28928\n\t"
      "ds_read_b64_tr_b16 v[120:121], v24 offset:24576\n\tds_read_b64_tr_b16 v[122:123], v24 offset:24832\n\tds_read_b64_tr_b16 v[124:125], v24 offset:28672\n\t"
      "ds_read_b64_tr_b16 v[126:127], v24 offset:28928\n\tds_read_b64_tr_b16 v[240:241], v25 offset:24576\n\tds_read_b64_tr_b16 v[242:243], v25 offset:24832\n\t"
      "ds_read_b64_tr_b16 v[244:245], v25 offset:28672\n\tds_read_b64_tr_b16 v[246:247], v25 offset:28928\n\ts_waitcnt lgkmcnt(0)\n\ts_add_u32 s50, s58, s67\n\t"
      "s_add_u32 m0, s49, 16384\n\ts_add_u32 s48, s50, 0x0\n\tbuffer_load_dwordx4 v20, s[40:43], s48 offen  lds\n\ts_add_u32 m0, s49, 17408\n\ts_add_u32 s48, s50, 0x10000\n\t"
      "buffer_load_dwordx4 v21, s[40:43], s48 offen  lds\n\ts_add_u32 m0, s49, 18432\n\ts_add_u32 s48, s50, 0x20000\n\tbuffer_load_dwordx4 v20, s[40:43], s48 offen  lds\n\t"
      "s_add_u32 m0, s49, 19456\n\ts_add_u32 s48, s50, 0x30000\n\tbuffer_load_dwordx4 v21, s[40:43], s48 offen  lds\n\ts_add_u32 m0, s49, 20480\n\t"
      "s_add_u32 s48, s50, 0x40000\n\tbuffer_load_dwordx4 v20, s[40:43], s48 offen  lds\n\ts_add_u32 m0, s49, 21504\n\ts_add_u32 s48, s50, 0x50000\n\t"
      "buffer_load_dwordx4 v21, s[40:43], s48 offen  lds\n\ts_add_u32 m0, s49, 22528\n\ts_add_u32 s48, s50, 0x60000\n\tbuffer_load_dwordx4 v20, s[40:43], s48 offen  lds\n\t"
      "s_add_u32 m0, s49, 23552\n\ts_add_u32 s48, s50, 0x70000\n\tbuffer_load_dwordx4 v21, s[40:43], s48 offen  lds\n\ts_waitcnt vmcnt(16)\n\t"
      "ds_read_b64_tr_b16 a[192:193], v22 offset:0\n\tds_read_b64_tr_b16 a[194:195], v22 offset:256\n\tds_read_b64_tr_b16 a[196:197], v22 offset:4096\n\t"
      "ds_read_b64_tr_b16 a[198:199], v22 offset:4352\n\tds_read_b64_tr_b16 a[200:201], v23 offset:0\n\tds_read_b64_tr_b16 a[202:203], v23 offset:256\n\t"
      "ds_read_b64_tr_b16 a[204:205], v23 offset:4096\n\tds_read_b64_tr_b16 a[206:207], v23 offset:4352\n\tds_read_b64_tr_b16 v[128:129], v24 offset:0\n\t"
      "ds_read_b64_tr_b16 v[130:131], v24 offset:256\n\tds_read_b64_tr_b16 v[132:133], v24 offset:4096\n\tds_read_b64_tr_b16 v[134:135], v24 offset:4352\n\t"
      "ds_read_b64_tr_b16 v[248:249], v25 offset:0\n\tds_read_b64_tr_b16 v[250:251], v25 offset:256\n\tds_read_b64_tr_b16 v[252:253], v25 offset:4096\n\t"
      "ds_read_b64_tr_b16 v[254:255], v25 offset:4352\n\ts_waitcnt lgkmcnt(0)\n\tds_write_b128 v5, v[152:155] offset:0\n\tds_write_b128 v5, v[156:159] offset:1024\n\t"
      "ds_write_b128 v5, v[160:163] offset:2048\n\tds_write_b128 v5, v[164:167] offset:3072\n\tds_write_b128 v5, v[168:171] offset:4096\n\t"
      "ds_write_b128 v5, v[172:175] offset:5120\n\tds_write_b128 v5, v[176:179] offset:6144\n\tds_write_b128 v5, v[180:183] offset:7168\n\ts_add_u32 s50, s59, s67\n\t"
      "s_add_u32 m0, s49, 24576\n\ts_add_u32 s48, s50, 0x0\n\tbuffer_load_dwordx4 v20, s[40:43], s48 offen  lds\n\ts_add_u32 m0, s49, 25600\n\ts_add_u32 s48, s50, 0x10000\n\t"
      "buffer_load_dwordx4 v21, s[40:43], s48 offen  lds\n\ts_add_u32 m0, s49, 26624\n\ts_add_u32 s48, s50, 0x20000\n\tbuffer_load_dwordx4 v20, s[40:43], s48 offen  lds\n\t"
      "s_add_u32 m0, s49, 27648\n\ts_add_u32 s48, s50, 0x30000\n\tbuffer_load_dwordx4 v21, s[40:43], s48 offen  lds\n\ts_add_u32 m0, s49, 28672\n\t"
      "s_add_u32 s48, s50, 0x40000\n\tbuffer_load_dwordx4 v20, s[40:43], s48 offen  lds\n\ts_add_u32 m0, s49, 29696\n\ts_add_u32 s48, s50, 0x50000\n\t"
      "buffer_load_dwordx4 v21, s[40:43], s48 offen  lds\n\ts_add_u32 m0, s49, 30720\n\ts_add_u32 s48, s50, 0x60000\n\tbuffer_load_dwordx4 v20, s[40:43], s48 offen  lds\n\t"
      "s_add_u32 m0, s49, 31744\n\ts_add_u32 s48, s50, 0x70000\n\tbuffer_load_dwordx4 v21, s[40:43], s48 offen  lds\n\ts_waitcnt vmcnt(16)\n\t"
      "ds_read_b64_tr_b16 a[208:209], v22 offset:8192\n\tds_read_b64_tr_b16 a[210:211], v22 offset:8448\n\tds_read_b64_tr_b16 a[212:213], v22 offset:12288\n\t"
      "ds_read_b64_tr_b16 a[214:215], v22 offset:12544\n\tds_read_b64_tr_b16 a[216:217], v23 offset:8192\n\tds_read_b64_tr_b16 a[218:219], v23 offset:8448\n\t"
      "ds_read_b64_tr_b16 a[220:221], v23 offset:12288\n\tds_read_b64_tr_b16 a[222:223], v23 offset:12544\n\tds_read_b64_tr_b16 v[136:137], v24 offset:8192\n\t"
      "ds_read_b64_tr_b16 v[138:139], v24 offset:8448\n\tds_read_b64_tr_b16 v[140:141], v24 offset:12288\n\tds_read_b64_tr_b16 v[142:143], v24 offset:12544\n\t"
      "ds_read_b64_tr_b16 v[160:161], v25 offset:8192\n\tds_read_b64_tr_b16 v[162:163], v25 offset:8448\n\tds_read_b64_tr_b16 v[164:165], v25 offset:12288\n\t"
      "ds_read_b64_tr_b16 v[166:167], v25 offset:12544\n\ts_waitcnt lgkmcnt(0)\n\tds_write_b128 v5, v[184:187] offset:8192\n\tds_write_b128 v5, v[188:191] offset:9216\n\t"
      "ds_write_b128 v5, v[192:195] offset:10240\n\tds_write_b128 v5, v[196:199] offset:11264\n\tds_write_b128 v5, v[200:203] offset:12288\n\t"
      "ds_write_b128 v5, v[204:207] offset:13312\n\tds_write_b128 v5, v[208:211] offset:14336\n\tds_write_b128 v5, v[212:215] offset:15360\n\ts_waitcnt vmcnt(8)\n\t"
      "ds_read_b64_tr_b16 a[224:225], v22 offset:16384\n\tds_read_b64_tr_b16 a[226:227], v22 offset:16640\n\tds_read_b64_tr_b16 a[228:229], v22 offset:20480\n\t"
      "ds_read_b64_tr_b16 a[230:231], v22 offset:20736\n\tds_read_b64_tr_b16 a[232:233], v23 offset:16384\n\tds_read_b64_tr_b16 a[234:235], v23 offset:16640\n\t"
      "ds_read_b64_tr_b16 a[236:237], v23 offset:20480\n\tds_read_b64_tr_b16 a[238:239], v23 offset:20736\n\tds_read_b64_tr_b16 v[144:145], v24 offset:16384\n\t"
      "ds_read_b64_tr_b16 v[146:147], v24 offset:16640\n\tds_read_b64_tr_b16 v[148:149], v24 offset:20480\n\tds_read_b64_tr_b16 v[150:151], v24 offset:20736\n\t"
      "ds_read_b64_tr_b16 v[168:169], v25 offset:16384\n\tds_read_b64_tr_b16 v[170:171], v25 offset:16640\n\tds_read_b64_tr_b16 v[172:173], v25 offset:20480\n\t"
      "ds_read_b64_tr_b16 v[174:175], v25 offset:20736\n\ts_waitcnt lgkmcnt(0)\n\tds_write_b128 v5, v[216:219] offset:16384\n\tds_write_b128 v5, v[220:223] offset:17408\n\t"
      "ds_write_b128 v5, v[224:227] offset:18432\n\tds_write_b128 v5, v[228:231] offset:19456\n\tds_write_b128 v5, v[232:235] offset:20480\n\t"
      "ds_write_b128 v5, v[236:239] offset:21504\n\tds_write_b128 v5, v[240:243] offset:22528\n\tds_write_b128 v5, v[244:247] offset:23552\n\ts_cmp_lt_u32 s68, 1\n\t"
      "s_cbranch_scc1 7f\n\ts_cmp_eq_u32 s68, 2\n\ts_cbranch_scc1 6f\n\t"
      "5:\n\ts_buffer_load_dword s48, s[12:15], s69 glc\n\ts_waitcnt lgkmcnt(0)\n\ts_cmp_eq_u32 s48, s71\n\ts_cbranch_scc1 6f\n\ts_cmp_eq_u32 s18, 0\n\ts_cbranch_scc1 6f\n\t"
      "s_sub_u32 s18, s18, 1\n\ts_branch 5b\n\t"
      "6:\n\tv_mov_b32 v17, 1\n\ts_mov_b64 exec, 1\n\tbuffer_atomic_add v17, off, s[12:15], s70\n\ts_mov_b64 exec, -1\n\ts_mov_b32 s68, 0\n\t"
      "7:\n\ts_waitcnt vmcnt(0)\n\tds_read_b64_tr_b16 a[240:241], v22 offset:24576\n\tds_read_b64_tr_b16 a[242:243], v22 offset:24832\n\t"
      "ds_read_b64_tr_b16 a[244:245], v22 offset:28672\n\tds_read_b64_tr_b16 a[246:247], v22 offset:28928\n\tds_read_b64_tr_b16 a[248:249], v23 offset:24576\n\t"
      "ds_read_b64_tr_b16 a[250:251], v23 offset:24832\n\tds_read_b64_tr_b16 a[252:253], v23 offset:28672\n\tds_read_b64_tr_b16 a[254:255], v23 offset:28928\n\t"
      "ds_read_b64_tr_b16 v[152:153], v24 offset:24576\n\tds_read_b64_tr_b16 v[154:155], v24 offset:24832\n\tds_read_b64_tr_b16 v[156:157], v24 offset:28672\n\t"
      "ds_read_b64_tr_b16 v[158:159], v24 offset:28928\n\tds_read_b64_tr_b16 v[176:177], v25 offset:24576\n\tds_read_b64_tr_b16 v[178:179], v25 offset:24832\n\t"
      "ds_read_b64_tr_b16 v[180:181], v25 offset:28672\n\tds_read_b64_tr_b16 v[182:183], v25 offset:28928\n\ts_waitcnt lgkmcnt(0)\n\t"
      "ds_write_b128 v5, v[248:251] offset:24576\n\tds_write_b128 v5, v[252:255] offset:25600\n\tds_write_b128 v5, v[160:163] offset:26624\n\t"
      "ds_write_b128 v5, v[164:167] offset:27648\n\tds_write_b128 v5, v[168:171] offset:28672\n\tds_write_b128 v5, v[172:175] offset:29696\n\t"
      "ds_write_b128 v5, v[176:179] offset:30720\n\tds_write_b128 v5, v[180:183] offset:31744\n\t"
      "v_mov_b64 v[30:31], 0\n\tv_mov_b32 v29, 160000\n\tds_write_b64 v29, v[30:31]\n\t"
      TKS("s_lshr_b32 s91, s62, 3\n\ts_and_b32 s91, s91, 3\n\t")
      "s_waitcnt lgkmcnt(0)\n\ts_barrier\n\t"
      STS("s_memrealtime vcc\n\ts_waitcnt lgkmcnt(0)\n\tv_mov_b32 v31, vcc_lo\n\tv_mov_b32 v30, 161024\n\tds_write_b32 v30, v31\n\t")
      "1:\n\tbuffer_load_dword v29, off, s[12:15], s63 sc1\n\ts_waitcnt vmcnt(0)\n\tv_readfirstlane_b32 s49, v29\n\ts_bitcmp1_b32 s49, 0\n\ts_cbranch_scc1 2f\n\t"
      "s_cmp_eq_u32 s18, 0\n\ts_cbranch_scc1 2f\n\ts_sub_u32 s18, s18, 1\n\ts_sleep 2\n\ts_branch 1b\n\t"
      "2:\n\ts_and_b32 s48, s49, 3\n\ts_cmp_lg_u32 s48, 3\n\ts_cbranch_scc1 4f\n\ts_cmp_eq_u32 s62, 0\n\ts_cbranch_scc1 4f\n\t"
      "s_waitcnt lgkmcnt(0)\n\ts_barrier\n\ts_mov_b64 s[0:1], s[60:61]\n\ts_mov_b32 s2, s62\n\ts_lshl_b32 s48, s16, 6\n\tv_mbcnt_lo_u32_b32 v0, -1, 0\n\t"
      "v_mbcnt_hi_u32_b32 v0, -1, v0\n\tv_add_u32 v0, s48, v0\n\ts_getpc_b64 s[4:5]\n\t"
      "3:\n\ts_sub_u32 s4, s4, 3b-mk_mega\n\ts_subb_u32 s5, s5, 0\n\ts_setpc_b64 s[4:5]\n\t"
      "4:\n\ts_memrealtime s[46:47]\n\ts_waitcnt lgkmcnt(0)\n\ts_lshl_b32 s19, s46, 2\n\ts_sub_u32 s19, s19, 0x100000\n\ts_mov_b32 s62, 470\n\ts_mov_b32 s63, 0\n\t"
      "s_lshr_b32 s64, s64, 9\n\ts_lshr_b32 s65, s65, 9\n\ts_lshr_b32 s66, s66, 9\n\ts_lshr_b32 s67, s67, 9\n\ts_lshl_b32 s48, s49, 10\n\ts_and_b32 s48, s48, 0x2000\n\t"
      "v_or_b32 v0, s48, v0\n\tv_or_b32 v14, s48, v14\n\tv_or_b32 v15, s48, v15\n\ts_mov_b64 s[72:73], s[24:25]\n\ts_mov_b64 s[56:57], s[8:9]\n\ts_mov_b32 s59, s11\n\t"
      "s_mov_b64 s[24:25], s[8:9]\n\ts_mov_b32 s27, s11\n\ts_add_u32 s60, s16, 1\n\ts_bitcmp1_b32 s60, 1\n\ts_cselect_b32 s60, s10, 0\n\ts_lshl_b32 s49, s49, 11\n\t"
      "s_and_b32 s49, s49, 0x2000\n\ts_xor_b32 s22, s22, s49\n\ts_xor_b32 s31, s31, s49\n\ts_cmp_eq_u32 s16, 3\n\ts_cbranch_scc1 64f\n\tv_or_b32 v10, s48, v10\n\t"
      "v_or_b32 v11, s48, v11\n\ts_branch 69f\n\t"
      "10:\n\tds_read_b128 v[160:163], v5 offset:0\n\tds_read_b128 v[164:167], v5 offset:1024\n\tds_read_b128 v[168:171], v5 offset:2048\n\t"
      "ds_read_b128 v[172:175], v5 offset:3072\n\tds_read_b128 v[176:179], v5 offset:4096\n\tds_read_b128 v[180:183], v5 offset:5120\n\t"
      "ds_read_b128 v[184:187], v5 offset:6144\n\tds_read_b128 v[188:191], v5 offset:7168\n\tds_read_b128 v[192:195], v5 offset:8192\n\t"
      "ds_read_b128 v[196:199], v5 offset:9216\n\tds_read_b128 v[200:203], v5 offset:10240\n\tds_read_b128 v[204:207], v5 offset:11264\n\t"
      "ds_read_b128 v[208:211], v5 offset:12288\n\tds_read_b128 v[212:215], v5 offset:13312\n\tds_read_b128 v[216:219], v5 offset:14336\n\t"
      "ds_read_b128 v[220:223], v5 offset:15360\n\tv_mov_b64 v[224:225], 0\n\tv_mov_b64 v[226:227], 0\n\tv_mov_b64 v[228:229], 0\n\tv_mov_b64 v[230:231], 0\n\t"
      "v_mov_b64 v[232:233], 0\n\tv_mov_b64 v[234:235], 0\n\tv_mov_b64 v[236:237], 0\n\tv_mov_b64 v[238:239], 0\n\ts_and_b32 s48, s17, 3\n\ts_lshl_b32 s40, s48, 18\n\t"
      "s_add_u32 s40, s40, 0x200000\n\ts_lshl_b32 s41, s48, 18\n\ts_add_u32 s41, s41, 0x208000\n\ts_xor_b32 s48, s48, 2\n\ts_lshl_b32 s42, s48, 18\n\t"
      "s_add_u32 s42, s42, 0x208000\n\ts_add_u32 s68, s40, s64\n\ts_add_u32 s69, s40, s65\n\ts_add_u32 s70, s40, s66\n\ts_add_u32 s71, s40, s67\n\ts_lshr_b32 s21, s20, 4\n\t"
      "s_and_b32 s21, s21, -2\n\ts_add_u32 s49, s22, 0x1000\n\ts_waitcnt lgkmcnt(0)\n\ts_cmp_lg_u32 s16, 3\n\ts_cbranch_scc1 73f\n\t"
      "71:\n\ts_memrealtime vcc\n\ts_waitcnt lgkmcnt(0)\n\ts_lshl_b32 vcc_lo, vcc_lo, 2\n\t"
      "70:\n\ts_sub_u32 s48, s19, vcc_lo\n\ts_cmp_gt_u32 s48, 4000\n\ts_cbranch_scc1 73f\n\ts_cmp_gt_u32 s48, 28\n\ts_cbranch_scc1 71b\n\ts_mul_i32 s48, s48, 45\n\t"
      "s_lshr_b32 s48, s48, 5\n\ts_lshl_b32 s48, s48, 2\n\ts_getpc_b64 vcc\n\t"
      "72:\n\ts_sub_u32 s48, 73f-72b, s48\n\ts_add_u32 vcc_lo, vcc_lo, s48\n\ts_addc_u32 vcc_hi, vcc_hi, 0\n\ts_setpc_b64 vcc\n\t.rept 40\n\ts_nop 0\n\t.endr\n\t"
      "73:\n\ts_barrier\n\t"
      TK0
      "buffer_load_dwordx4 v[240:243], v0, s[8:11], s68 offen sc1\n\ts_nop 11\n\tbuffer_load_dwordx4 v[244:247], v0, s[8:11], s69 offen sc1\n\ts_nop 11\n\t"
      "buffer_load_dwordx4 v[248:251], v0, s[8:11], s70 offen sc1\n\ts_nop 11\n\tbuffer_load_dwordx4 v[252:255], v0, s[8:11], s71 offen sc1\n\t"
      "ds_read_b32 v17, v16 offset:4\n\ts_waitcnt lgkmcnt(0)\n\tv_readfirstlane_b32 s55, v17\n\ts_cmp_gt_u32 s55, s17\n\ts_cselect_b32 s58, s60, 0\n\ts_min_u32 s48, s55, s54\n\t"
      "s_sub_u32 s48, s48, 1\n\ts_cmp_gt_i32 s48, s17\n\ts_cselect_b32 s26, s10, 0\n\ts_sub_u32 s53, s31, 0x800000\n\t"
      "11:\n\ts_cmp_lt_u32 s16, 2\n\ts_cbranch_scc1 15f\n\ts_waitcnt vmcnt(3)\n\t"
      TK1
      "v_smfmac_f32_16x16x64_bf16 v[224:227], v[240:243], a[0:7], v6\n\tv_max3_u32 v17, v240, v241, v242\n\tv_smfmac_f32_16x16x64_bf16 v[228:231], v[240:243], a[8:15], v6\n\t"
      "v_max_u32 v17, v17, v243\n\tv_smfmac_f32_16x16x64_bf16 v[232:235], v[240:243], v[32:39], v6\n\tds_write_b128 v1, v[240:243] offset:0\n\t"
      "v_smfmac_f32_16x16x64_bf16 v[236:239], v[240:243], v[160:167], v6\n\ts_waitcnt vmcnt(2)\n\tv_cmp_eq_u32 vcc, -1, v17\n\t"
      "v_smfmac_f32_16x16x64_bf16 v[224:227], v[244:247], a[16:23], v6\n\tv_max3_u32 v17, v244, v245, v246\n\tv_smfmac_f32_16x16x64_bf16 v[228:231], v[244:247], a[24:31], v6\n\t"
      "s_cbranch_vccnz 86f\n\t"
      "24:\n\tv_max_u32 v17, v17, v247\n\tv_smfmac_f32_16x16x64_bf16 v[232:235], v[244:247], v[40:47], v6\n\tds_write_b128 v1, v[244:247] offset:1024\n\t"
      "v_smfmac_f32_16x16x64_bf16 v[236:239], v[244:247], v[168:175], v6\n\ts_waitcnt lgkmcnt(1)\n\ts_barrier\n\ts_waitcnt vmcnt(1)\n\tv_cmp_eq_u32 vcc, -1, v17\n\t"
      "v_smfmac_f32_16x16x64_bf16 v[224:227], v[248:251], a[32:39], v6\n\tv_max3_u32 v17, v248, v249, v250\n\tv_smfmac_f32_16x16x64_bf16 v[228:231], v[248:251], a[40:47], v6\n\t"
      "s_cbranch_vccnz 87f\n\t"
      "25:\n\tv_max_u32 v17, v17, v251\n\tv_smfmac_f32_16x16x64_bf16 v[232:235], v[248:251], v[48:55], v6\n\tds_write_b128 v1, v[248:251] offset:2048\n\t"
      "v_smfmac_f32_16x16x64_bf16 v[236:239], v[248:251], v[176:183], v6\n\ts_waitcnt vmcnt(0)\n\tv_cmp_eq_u32 vcc, -1, v17\n\t"
      "v_smfmac_f32_16x16x64_bf16 v[224:227], v[252:255], a[48:55], v6\n\tds_read_b128 v[20:23], v2 offset:0\n\tds_read_b128 v[24:27], v3 offset:0\n\t"
      "v_max3_u32 v17, v252, v253, v254\n\tv_smfmac_f32_16x16x64_bf16 v[228:231], v[252:255], a[56:63], v6\n\ts_cbranch_vccnz 88f\n\t"
      "26:\n\tv_max_u32 v17, v17, v255\n\tv_smfmac_f32_16x16x64_bf16 v[232:235], v[252:255], v[56:63], v6\n\tds_write_b128 v1, v[252:255] offset:3072\n\t"
      "v_smfmac_f32_16x16x64_bf16 v[236:239], v[252:255], v[184:191], v6\n\ts_waitcnt lgkmcnt(2)\n\t"
      TK2
      "v_cmp_eq_u32 vcc, -1, v17\n\tv_smfmac_f32_16x16x64_bf16 v[224:227], v[20:23], a[64:71], v6\n\tds_read_b128 v[28:31], v4 offset:0\n\t"
      "v_smfmac_f32_16x16x64_bf16 v[228:231], v[20:23], a[72:79], v6\n\ts_cbranch_vccnz 89f\n\t"
      "27:\n\tv_smfmac_f32_16x16x64_bf16 v[232:235], v[20:23], v[64:71], v6\n\ts_barrier\n\tv_smfmac_f32_16x16x64_bf16 v[236:239], v[20:23], v[192:199], v6\n\t"
      "s_waitcnt lgkmcnt(1)\n\ts_barrier\n\t"
      TK3
      "v_smfmac_f32_16x16x64_bf16 v[224:227], v[24:27], a[80:87], v6\n\tds_read_b128 v[20:23], v2 offset:1024\n\t"
      "v_smfmac_f32_16x16x64_bf16 v[228:231], v[24:27], a[88:95], v6\n\tds_read_b128 v[160:163], v5 offset:16384\n\tds_read_b128 v[164:167], v5 offset:17408\n\t"
      "v_smfmac_f32_16x16x64_bf16 v[232:235], v[24:27], v[72:79], v6\n\tv_smfmac_f32_16x16x64_bf16 v[236:239], v[24:27], v[200:207], v6\n\ts_waitcnt lgkmcnt(3)\n\t"
      "v_smfmac_f32_16x16x64_bf16 v[224:227], v[28:31], a[96:103], v6\n\tds_read_b128 v[24:27], v3 offset:1024\n\t"
      "v_smfmac_f32_16x16x64_bf16 v[228:231], v[28:31], a[104:111], v6\n\tds_read_b128 v[168:171], v5 offset:18432\n\tds_read_b128 v[172:175], v5 offset:19456\n\t"
      "v_smfmac_f32_16x16x64_bf16 v[232:235], v[28:31], v[80:87], v6\n\tv_smfmac_f32_16x16x64_bf16 v[236:239], v[28:31], v[208:215], v6\n\ts_waitcnt lgkmcnt(5)\n\t"
      "v_smfmac_f32_16x16x64_bf16 v[224:227], v[20:23], a[112:119], v6\n\tds_read_b128 v[28:31], v4 offset:1024\n\t"
      "v_smfmac_f32_16x16x64_bf16 v[228:231], v[20:23], a[120:127], v6\n\tds_read_b128 v[176:179], v5 offset:20480\n\tds_read_b128 v[180:183], v5 offset:21504\n\t"
      "v_smfmac_f32_16x16x64_bf16 v[232:235], v[20:23], v[88:95], v6\n\tbuffer_load_dwordx4 v[240:243], v12, s[56:59], s22 offen offset:0 sc1\n\t"
      "v_smfmac_f32_16x16x64_bf16 v[236:239], v[20:23], v[216:223], v6\n\ts_waitcnt lgkmcnt(5)\n\tv_smfmac_f32_16x16x64_bf16 v[224:227], v[24:27], a[128:135], v6\n\t"
      "ds_read_b128 v[20:23], v2 offset:2048\n\tv_smfmac_f32_16x16x64_bf16 v[228:231], v[24:27], a[136:143], v6\n\tds_read_b128 v[184:187], v5 offset:22528\n\t"
      "ds_read_b128 v[188:191], v5 offset:23552\n\tv_smfmac_f32_16x16x64_bf16 v[232:235], v[24:27], v[96:103], v6\n\t"
      "buffer_load_dwordx4 v[244:247], v12, s[56:59], s22 offen offset:2048 sc1\n\tv_smfmac_f32_16x16x64_bf16 v[236:239], v[24:27], v[160:167], v6\n\ts_waitcnt lgkmcnt(5)\n\t"
      "v_smfmac_f32_16x16x64_bf16 v[224:227], v[28:31], a[144:151], v6\n\tds_read_b128 v[24:27], v3 offset:2048\n\t"
      "v_smfmac_f32_16x16x64_bf16 v[228:231], v[28:31], a[152:159], v6\n\tds_read_b128 v[192:195], v5 offset:24576\n\tds_read_b128 v[196:199], v5 offset:25600\n\t"
      "v_smfmac_f32_16x16x64_bf16 v[232:235], v[28:31], v[104:111], v6\n\tbuffer_load_dwordx4 v[248:251], v12, s[56:59], s49 offen offset:0 sc1\n\t"
      "v_smfmac_f32_16x16x64_bf16 v[236:239], v[28:31], v[168:175], v6\n\ts_waitcnt lgkmcnt(5)\n\tds_read_b128 v[160:163], v9 offset:0\n\t"
      "v_smfmac_f32_16x16x64_bf16 v[224:227], v[20:23], a[160:167], v6\n\tds_read_b128 v[28:31], v4 offset:2048\n\tds_read_b128 v[164:167], v9 offset:2048\n\t"
      "v_smfmac_f32_16x16x64_bf16 v[228:231], v[20:23], a[168:175], v6\n\tds_read_b128 v[200:203], v5 offset:26624\n\tds_read_b128 v[204:207], v5 offset:27648\n\t"
      "v_smfmac_f32_16x16x64_bf16 v[232:235], v[20:23], v[112:119], v6\n\tbuffer_load_dwordx4 v[252:255], v12, s[56:59], s49 offen offset:2048 sc1\n\t"
      "v_smfmac_f32_16x16x64_bf16 v[236:239], v[20:23], v[176:183], v6\n\ts_waitcnt lgkmcnt(7)\n\tds_read_b128 v[168:171], v9 offset:4096\n\t"
      "v_smfmac_f32_16x16x64_bf16 v[224:227], v[24:27], a[176:183], v6\n\tds_read_b128 v[20:23], v2 offset:3072\n\tds_read_b128 v[172:175], v9 offset:6144\n\t"
      "v_smfmac_f32_16x16x64_bf16 v[228:231], v[24:27], a[184:191], v6\n\tds_read_b128 v[208:211], v5 offset:28672\n\tds_read_b128 v[212:215], v5 offset:29696\n\t"
      "v_smfmac_f32_16x16x64_bf16 v[232:235], v[24:27], v[120:127], v6\n\tbuffer_load_dword v19, v11, s[24:27], s31 offen sc1\n\t"
      "v_smfmac_f32_16x16x64_bf16 v[236:239], v[24:27], v[184:191], v6\n\ts_waitcnt lgkmcnt(8)\n\tds_read_b128 v[176:179], v9 offset:256\n\t"
      "v_smfmac_f32_16x16x64_bf16 v[224:227], v[28:31], a[192:199], v6\n\tds_read_b128 v[24:27], v3 offset:3072\n\tds_read_b128 v[180:183], v9 offset:2304\n\t"
      "v_smfmac_f32_16x16x64_bf16 v[228:231], v[28:31], a[200:207], v6\n\tds_read_b128 v[216:219], v5 offset:30720\n\tds_read_b128 v[220:223], v5 offset:31744\n\t"
      "v_smfmac_f32_16x16x64_bf16 v[232:235], v[28:31], v[128:135], v6\n\ts_cmp_eq_u32 s16, 2\n\tv_smfmac_f32_16x16x64_bf16 v[236:239], v[28:31], v[192:199], v6\n\t"
      "s_waitcnt lgkmcnt(8)\n\tds_read_b128 v[184:187], v9 offset:4352\n\tv_smfmac_f32_16x16x64_bf16 v[224:227], v[20:23], a[208:215], v6\n\t"
      "ds_read_b128 v[28:31], v4 offset:3072\n\tds_read_b128 v[188:191], v9 offset:6400\n\tv_smfmac_f32_16x16x64_bf16 v[228:231], v[20:23], a[216:223], v6\n\t"
      "v_smfmac_f32_16x16x64_bf16 v[232:235], v[20:23], v[136:143], v6\n\tbuffer_load_dword v19, v11, s[24:27], s53 offen sc1\n\t"
      "v_smfmac_f32_16x16x64_bf16 v[236:239], v[20:23], v[200:207], v6\n\ts_waitcnt lgkmcnt(6)\n\tv_smfmac_f32_16x16x64_bf16 v[224:227], v[24:27], a[224:231], v6\n\t"
      "v_smfmac_f32_16x16x64_bf16 v[228:231], v[24:27], a[232:239], v6\n\ts_waitcnt lgkmcnt(1)\n\tv_smfmac_f32_16x16x64_bf16 v[224:227], v[28:31], a[240:247], v6\n\ts_nop 2\n\t"
      "v_smfmac_f32_16x16x64_bf16 v[228:231], v[28:31], a[248:255], v6\n\ts_nop 2\n\tv_smfmac_f32_16x16x64_bf16 v[232:235], v[24:27], v[144:151], v6\n\t"
      "v_add_f32 v224, v224, v225\n\tv_smfmac_f32_16x16x64_bf16 v[232:235], v[28:31], v[152:159], v6\n\ts_nop 0\n\tv_add_f32 v226, v226, v227\n\t"
      "v_smfmac_f32_16x16x64_bf16 v[236:239], v[24:27], v[208:215], v6\n\tv_add_f32 v228, v228, v229\n\tv_smfmac_f32_16x16x64_bf16 v[236:239], v[28:31], v[216:223], v6\n\t"
      TK4
      "v_add_f32 v230, v230, v231\n\tv_cvt_pk_bf16_f32 v224, v224, v228\n\tv_cvt_pk_bf16_f32 v226, v226, v230\n\tv_add_f32 v232, v232, v233\n\tv_add_f32 v234, v234, v235\n\t"
      "s_nop 2\n\tv_add_f32 v236, v236, v237\n\tv_add_f32 v238, v238, v239\n\tv_cvt_pk_bf16_f32 v225, v232, v236\n\tv_cvt_pk_bf16_f32 v227, v234, v238\n\t"
      "ds_write2_b64 v7, v[224:225], v[226:227] offset1:34\n\ts_waitcnt lgkmcnt(0)\n\ts_barrier\n\ts_cbranch_scc1 40f\n\ts_branch 60f\n\t"
      "15:\n\ts_waitcnt vmcnt(3)\n\t"
      TK1
      "v_smfmac_f32_16x16x64_bf16 v[224:227], v[240:243], a[0:7], v6\n\tds_write_b128 v1, v[240:243] offset:0\n\t"
      "v_smfmac_f32_16x16x64_bf16 v[228:231], v[240:243], a[8:15], v6\n\tv_max3_u32 v17, v240, v241, v242\n\tv_smfmac_f32_16x16x64_bf16 v[232:235], v[240:243], v[32:39], v6\n\t"
      "v_max_u32 v17, v17, v243\n\tv_smfmac_f32_16x16x64_bf16 v[236:239], v[240:243], v[160:167], v6\n\ts_waitcnt vmcnt(2)\n\tv_cmp_eq_u32 vcc, -1, v17\n\t"
      "v_smfmac_f32_16x16x64_bf16 v[224:227], v[244:247], a[16:23], v6\n\tds_write_b128 v1, v[244:247] offset:1024\n\t"
      "v_smfmac_f32_16x16x64_bf16 v[228:231], v[244:247], a[24:31], v6\n\ts_cbranch_vccnz 86f\n\t"
      "20:\n\tv_max3_u32 v17, v244, v245, v246\n\tv_smfmac_f32_16x16x64_bf16 v[232:235], v[244:247], v[40:47], v6\n\tv_max_u32 v17, v17, v247\n\t"
      "v_smfmac_f32_16x16x64_bf16 v[236:239], v[244:247], v[168:175], v6\n\ts_waitcnt lgkmcnt(1)\n\ts_barrier\n\ts_waitcnt vmcnt(1)\n\tv_cmp_eq_u32 vcc, -1, v17\n\t"
      "v_smfmac_f32_16x16x64_bf16 v[224:227], v[248:251], a[32:39], v6\n\tds_write_b128 v1, v[248:251] offset:2048\n\t"
      "v_smfmac_f32_16x16x64_bf16 v[228:231], v[248:251], a[40:47], v6\n\ts_cbranch_vccnz 87f\n\t"
      "21:\n\tv_max3_u32 v17, v248, v249, v250\n\tv_smfmac_f32_16x16x64_bf16 v[232:235], v[248:251], v[48:55], v6\n\tv_max_u32 v17, v17, v251\n\t"
      "v_smfmac_f32_16x16x64_bf16 v[236:239], v[248:251], v[176:183], v6\n\tds_read_b128 v[20:23], v2 offset:0\n\tds_read_b128 v[24:27], v3 offset:0\n\ts_waitcnt vmcnt(0)\n\t"
      "v_cmp_eq_u32 vcc, -1, v17\n\tv_smfmac_f32_16x16x64_bf16 v[224:227], v[252:255], a[48:55], v6\n\tds_write_b128 v1, v[252:255] offset:3072\n\t"
      "v_smfmac_f32_16x16x64_bf16 v[228:231], v[252:255], a[56:63], v6\n\ts_cbranch_vccnz 88f\n\t"
      "22:\n\tv_max3_u32 v17, v252, v253, v254\n\tv_smfmac_f32_16x16x64_bf16 v[232:235], v[252:255], v[56:63], v6\n\tv_max_u32 v17, v17, v255\n\t"
      "v_smfmac_f32_16x16x64_bf16 v[236:239], v[252:255], v[184:191], v6\n\tds_read_b128 v[28:31], v4 offset:0\n\ts_waitcnt lgkmcnt(3)\n\t"
      TK2
      "v_cmp_eq_u32 vcc, -1, v17\n\tv_smfmac_f32_16x16x64_bf16 v[224:227], v[20:23], a[64:71], v6\n\tv_smfmac_f32_16x16x64_bf16 v[228:231], v[20:23], a[72:79], v6\n\t"
      "s_cbranch_vccnz 89f\n\t"
      "23:\n\tv_smfmac_f32_16x16x64_bf16 v[232:235], v[20:23], v[64:71], v6\n\ts_barrier\n\ts_memrealtime s[46:47]\n\t"
      "v_smfmac_f32_16x16x64_bf16 v[236:239], v[20:23], v[192:199], v6\n\ts_waitcnt lgkmcnt(1)\n\ts_barrier\n\t"
      TK3
      "v_smfmac_f32_16x16x64_bf16 v[224:227], v[24:27], a[80:87], v6\n\tv_smfmac_f32_16x16x64_bf16 v[228:231], v[24:27], a[88:95], v6\n\t"
      "v_smfmac_f32_16x16x64_bf16 v[232:235], v[24:27], v[72:79], v6\n\ts_waitcnt lgkmcnt(0)\n\tds_read_b128 v[20:23], v2 offset:1024\n\ts_lshl2_add_u32 s47, s46, s21\n\t"
      "v_smfmac_f32_16x16x64_bf16 v[236:239], v[24:27], v[200:207], v6\n\tds_read_b128 v[160:163], v5 offset:16384\n\tds_read_b128 v[164:167], v5 offset:17408\n\t"
      "v_mov_b32 v18, s47\n\tv_smfmac_f32_16x16x64_bf16 v[224:227], v[28:31], a[96:103], v6\n\tbuffer_store_dword v18, v14, s[8:11], s41 offen sc1\n\t"
      "v_smfmac_f32_16x16x64_bf16 v[228:231], v[28:31], a[104:111], v6\n\tds_read_b128 v[168:171], v5 offset:18432\n\tds_read_b128 v[172:175], v5 offset:19456\n\t"
      "v_smfmac_f32_16x16x64_bf16 v[232:235], v[28:31], v[80:87], v6\n\tds_read_b128 v[24:27], v3 offset:1024\n\t"
      "v_smfmac_f32_16x16x64_bf16 v[236:239], v[28:31], v[208:215], v6\n\ts_waitcnt lgkmcnt(5)\n\tv_smfmac_f32_16x16x64_bf16 v[224:227], v[20:23], a[112:119], v6\n\t"
      "ds_read_b128 v[28:31], v4 offset:1024\n\tv_smfmac_f32_16x16x64_bf16 v[228:231], v[20:23], a[120:127], v6\n\tds_read_b128 v[176:179], v5 offset:20480\n\t"
      "ds_read_b128 v[180:183], v5 offset:21504\n\tv_smfmac_f32_16x16x64_bf16 v[232:235], v[20:23], v[88:95], v6\n\t"
      "buffer_load_dwordx4 v[240:243], v12, s[56:59], s22 offen offset:0 sc1\n\tv_smfmac_f32_16x16x64_bf16 v[236:239], v[20:23], v[216:223], v6\n\ts_waitcnt lgkmcnt(3)\n\t"
      "v_smfmac_f32_16x16x64_bf16 v[224:227], v[24:27], a[128:135], v6\n\tds_read_b128 v[20:23], v2 offset:2048\n\t"
      "v_smfmac_f32_16x16x64_bf16 v[228:231], v[24:27], a[136:143], v6\n\tds_read_b128 v[184:187], v5 offset:22528\n\tds_read_b128 v[188:191], v5 offset:23552\n\t"
      "v_smfmac_f32_16x16x64_bf16 v[232:235], v[24:27], v[96:103], v6\n\tbuffer_load_dwordx4 v[244:247], v12, s[56:59], s22 offen offset:2048 sc1\n\t"
      "v_smfmac_f32_16x16x64_bf16 v[236:239], v[24:27], v[160:167], v6\n\ts_waitcnt lgkmcnt(5)\n\tv_smfmac_f32_16x16x64_bf16 v[224:227], v[28:31], a[144:151], v6\n\t"
      "ds_read_b128 v[24:27], v3 offset:2048\n\tv_smfmac_f32_16x16x64_bf16 v[228:231], v[28:31], a[152:159], v6\n\tds_read_b128 v[192:195], v5 offset:24576\n\t"
      "ds_read_b128 v[196:199], v5 offset:25600\n\tv_smfmac_f32_16x16x64_bf16 v[232:235], v[28:31], v[104:111], v6\n\t"
      "buffer_load_dwordx4 v[248:251], v12, s[56:59], s49 offen offset:0 sc1\n\tv_smfmac_f32_16x16x64_bf16 v[236:239], v[28:31], v[168:175], v6\n\ts_waitcnt lgkmcnt(5)\n\t"
      "ds_read_b128 v[160:163], v9 offset:0\n\tv_smfmac_f32_16x16x64_bf16 v[224:227], v[20:23], a[160:167], v6\n\tds_read_b128 v[28:31], v4 offset:2048\n\t"
      "ds_read_b128 v[164:167], v9 offset:2048\n\tv_smfmac_f32_16x16x64_bf16 v[228:231], v[20:23], a[168:175], v6\n\tds_read_b128 v[200:203], v5 offset:26624\n\t"
      "ds_read_b128 v[204:207], v5 offset:27648\n\tv_smfmac_f32_16x16x64_bf16 v[232:235], v[20:23], v[112:119], v6\n\t"
      "buffer_load_dwordx4 v[252:255], v12, s[56:59], s49 offen offset:2048 sc1\n\tv_smfmac_f32_16x16x64_bf16 v[236:239], v[20:23], v[176:183], v6\n\ts_waitcnt lgkmcnt(7)\n\t"
      "ds_read_b128 v[168:171], v9 offset:4096\n\tv_smfmac_f32_16x16x64_bf16 v[224:227], v[24:27], a[176:183], v6\n\tds_read_b128 v[20:23], v2 offset:3072\n\t"
      "ds_read_b128 v[172:175], v9 offset:6144\n\tv_smfmac_f32_16x16x64_bf16 v[228:231], v[24:27], a[184:191], v6\n\tds_read_b128 v[208:211], v5 offset:28672\n\t"
      "ds_read_b128 v[212:215], v5 offset:29696\n\tv_smfmac_f32_16x16x64_bf16 v[232:235], v[24:27], v[120:127], v6\n\t"
      "v_smfmac_f32_16x16x64_bf16 v[236:239], v[24:27], v[184:191], v6\n\ts_waitcnt lgkmcnt(8)\n\tds_read_b128 v[176:179], v9 offset:256\n\t"
      "v_smfmac_f32_16x16x64_bf16 v[224:227], v[28:31], a[192:199], v6\n\tds_read_b128 v[24:27], v3 offset:3072\n\tds_read_b128 v[180:183], v9 offset:2304\n\t"
      "v_smfmac_f32_16x16x64_bf16 v[228:231], v[28:31], a[200:207], v6\n\tds_read_b128 v[216:219], v5 offset:30720\n\tds_read_b128 v[220:223], v5 offset:31744\n\t"
      "v_smfmac_f32_16x16x64_bf16 v[232:235], v[28:31], v[128:135], v6\n\ts_cmp_eq_u32 s16, 0\n\tv_smfmac_f32_16x16x64_bf16 v[236:239], v[28:31], v[192:199], v6\n\t"
      "s_waitcnt lgkmcnt(8)\n\tds_read_b128 v[184:187], v9 offset:4352\n\tv_smfmac_f32_16x16x64_bf16 v[224:227], v[20:23], a[208:215], v6\n\t"
      "ds_read_b128 v[28:31], v4 offset:3072\n\tds_read_b128 v[188:191], v9 offset:6400\n\tv_smfmac_f32_16x16x64_bf16 v[228:231], v[20:23], a[216:223], v6\n\t"
      "v_smfmac_f32_16x16x64_bf16 v[232:235], v[20:23], v[136:143], v6\n\tv_smfmac_f32_16x16x64_bf16 v[236:239], v[20:23], v[200:207], v6\n\ts_waitcnt lgkmcnt(6)\n\t"
      "v_smfmac_f32_16x16x64_bf16 v[224:227], v[24:27], a[224:231], v6\n\tv_smfmac_f32_16x16x64_bf16 v[228:231], v[24:27], a[232:239], v6\n\ts_waitcnt lgkmcnt(1)\n\t"
      "v_smfmac_f32_16x16x64_bf16 v[224:227], v[28:31], a[240:247], v6\n\ts_nop 2\n\tv_smfmac_f32_16x16x64_bf16 v[228:231], v[28:31], a[248:255], v6\n\ts_nop 2\n\t"
      "v_smfmac_f32_16x16x64_bf16 v[232:235], v[24:27], v[144:151], v6\n\tv_add_f32 v224, v224, v225\n\tv_smfmac_f32_16x16x64_bf16 v[232:235], v[28:31], v[152:159], v6\n\t"
      "s_nop 0\n\tv_add_f32 v226, v226, v227\n\tv_smfmac_f32_16x16x64_bf16 v[236:239], v[24:27], v[208:215], v6\n\tv_add_f32 v228, v228, v229\n\t"
      "v_smfmac_f32_16x16x64_bf16 v[236:239], v[28:31], v[216:223], v6\n\t"
      TK4
      "v_add_f32 v230, v230, v231\n\tv_cvt_pk_bf16_f32 v224, v224, v228\n\tv_cvt_pk_bf16_f32 v226, v226, v230\n\tv_add_f32 v232, v232, v233\n\tv_add_f32 v234, v234, v235\n\t"
      "s_nop 2\n\tv_add_f32 v236, v236, v237\n\tv_add_f32 v238, v238, v239\n\tv_cvt_pk_bf16_f32 v225, v232, v236\n\tv_cvt_pk_bf16_f32 v227, v234, v238\n\t"
      "ds_write2_b32 v7, v224, v225 offset1:4\n\tds_write2_b32 v7, v226, v227 offset0:8 offset1:12\n\ts_cbranch_scc0 39f\n\t"
      "14:\n\tv_mov_b64 v[228:229], 0\n\tv_mov_b64 v[230:231], 0\n\tv_mov_b64 v[232:233], 0\n\tv_mov_b64 v[234:235], 0\n\tv_mov_b64 v[236:237], 0\n\tv_mov_b64 v[238:239], 0\n\t"
      "s_mov_b32 vcc_lo, 0x55555555\n\ts_mov_b32 vcc_hi, 0x55555555\n\ts_waitcnt lgkmcnt(0)\n\ts_barrier\n\t"
      TK5
      "ds_read_b128 v[20:23], v1 offset:24576\n\tds_read_b128 v[24:27], v1 offset:25600\n\tds_read_b128 v[208:211], v8 offset:0\n\tds_read_b128 v[212:215], v8 offset:64\n\t"
      "ds_read_b128 v[216:219], v8 offset:128\n\tds_read_b128 v[220:223], v8 offset:192\n\ts_waitcnt lgkmcnt(5)\n\t"
      "v_smfmac_f32_16x16x64_bf16 v[232:235], v[20:23], v[160:167], v6\n\tv_smfmac_f32_16x16x64_bf16 v[236:239], v[20:23], v[176:183], v6\n\ts_waitcnt lgkmcnt(4)\n\t"
      "v_smfmac_f32_16x16x64_bf16 v[232:235], v[24:27], v[168:175], v6\n\tv_smfmac_f32_16x16x64_bf16 v[236:239], v[24:27], v[184:191], v6\n\ts_nop 1\n\ts_waitcnt lgkmcnt(2)\n\t"
      "v_smfmac_f32_16x16x64_bf16 v[228:231], v[20:23], v[208:215], v6\n\ts_nop 1\n\ts_waitcnt lgkmcnt(0)\n\tv_smfmac_f32_16x16x64_bf16 v[228:231], v[24:27], v[216:223], v6\n\t"
      "v_add_f32 v232, v232, v233\n\tv_add_f32 v234, v234, v235\n\tv_add_f32 v236, v236, v237\n\tv_add_f32 v238, v238, v239\n\t"
      "v_cndmask_b32_dpp v194, v234, v232, vcc quad_perm:[1,0,3,2] row_mask:0xf bank_mask:0xf\n\ts_not_b64 vcc, vcc\n\t"
      "v_cndmask_b32_dpp v195, v232, v234, vcc quad_perm:[1,0,3,2] row_mask:0xf bank_mask:0xf\n\t"
      "v_cndmask_b32_dpp v197, v236, v238, vcc quad_perm:[1,0,3,2] row_mask:0xf bank_mask:0xf\n\tv_add_f32 v228, v228, v229\n\tv_add_f32 v230, v230, v231\n\t"
      "v_max3_f32 v198, v194, v195, v197\n\tv_pk_mul_f32 v[194:195], v[194:195], s[28:29]\n\t"
      "v_cndmask_b32_dpp v193, v228, v230, vcc quad_perm:[1,0,3,2] row_mask:0xf bank_mask:0xf\n\ts_not_b64 vcc, vcc\n\t"
      "v_cndmask_b32_dpp v196, v238, v236, vcc quad_perm:[1,0,3,2] row_mask:0xf bank_mask:0xf\n\t"
      "v_cndmask_b32_dpp v192, v230, v228, vcc quad_perm:[1,0,3,2] row_mask:0xf bank_mask:0xf\n\tv_max3_f32 v198, v198, v196, v193\n\tv_max_f32 v198, v198, v192\n\t"
      "v_pk_mul_f32 v[196:197], v[196:197], s[28:29]\n\tv_pk_mul_f32 v[192:193], v[192:193], s[28:29]\n\tv_max_f32_dpp v198, v198, v198 row_ror:8 row_mask:0xf bank_mask:0xf\n\t"
      "s_nop 1\n\tv_max_f32_dpp v198, v198, v198 row_ror:4 row_mask:0xf bank_mask:0xf\n\ts_nop 1\n\tv_max_f32_dpp v198, v198, v198 row_ror:2 row_mask:0xf bank_mask:0xf\n\t"
      "v_mul_f32 v198, s28, v198\n\tv_pk_add_f32 v[192:193], v[192:193], v[198:199] op_sel_hi:[1,0] neg_lo:[0,1] neg_hi:[0,1]\n\t"
      "v_pk_add_f32 v[194:195], v[194:195], v[198:199] op_sel_hi:[1,0] neg_lo:[0,1] neg_hi:[0,1]\n\t"
      "v_pk_add_f32 v[196:197], v[196:197], v[198:199] op_sel_hi:[1,0] neg_lo:[0,1] neg_hi:[0,1]\n\tv_exp_f32 v192, v192\n\tv_exp_f32 v193, v193\n\tv_exp_f32 v194, v194\n\t"
      "v_exp_f32 v195, v195\n\tv_exp_f32 v196, v196\n\tv_pk_fma_f32 v[198:199], v[192:193], v[12:13], v[194:195] op_sel:[0,1,0]\n\tv_exp_f32 v197, v197\n\t"
      "v_add_f32 v198, v198, v196\n\tv_add_f32 v199, v199, v197\n\tv_add_f32 v198, v198, v199\n\ts_nop 1\n\t"
      "v_add_f32_dpp v198, v198, v198 row_ror:8 row_mask:0xf bank_mask:0xf\n\ts_nop 1\n\tv_add_f32_dpp v198, v198, v198 row_ror:4 row_mask:0xf bank_mask:0xf\n\ts_nop 1\n\t"
      "v_add_f32_dpp v198, v198, v198 row_ror:2 row_mask:0xf bank_mask:0xf\n\tv_rcp_f32 v198, v198\n\ts_nop 0\n\t"
      "v_pk_mul_f32 v[194:195], v[194:195], v[198:199] op_sel_hi:[1,0]\n\tv_pk_mul_f32 v[196:197], v[196:197], v[198:199] op_sel_hi:[1,0]\n\t"
      "v_cvt_pk_bf16_f32 v200, v194, v195\n\tv_cvt_pk_bf16_f32 v201, v196, v197\n\tv_pk_mul_f32 v[192:193], v[192:193], v[198:199] op_sel_hi:[1,0]\n\t"
      "buffer_store_dwordx2 v[200:201], v10, s[8:11], s41 offen sc1\n\tv_cvt_pk_bf16_f32 v202, v192, v193\n\tbuffer_store_dword v202, v11, s[8:11], s41 offen sc1\n\t"
      TK6
      "s_memrealtime vcc\n\ts_waitcnt lgkmcnt(0)\n\ts_sub_u32 s48, vcc_lo, s46\n\ts_lshl_b32 s48, s48, 2\n\ts_min_u32 s48, s48, 800\n\t"
      STS("s_add_u32 s51, s51, s48\n\t")
      "s_lshl_b32 s48, s48, 4\n\ts_sub_i32 s48, s48, s20\n\ts_ashr_i32 s48, s48, 2\n\ts_add_i32 s20, s20, s48\n\tv_mov_b64 v[204:205], -1\n\t"
      "buffer_store_dwordx2 v[204:205], v10, s[8:11], s42 offen sc1\n\tbuffer_store_dword v204, v11, s[8:11], s42 offen sc1\n\t"
      "buffer_store_dword v204, v14, s[8:11], s42 offen sc1\n\ts_branch 69f\n\t"
      "39:\n\ts_waitcnt lgkmcnt(0)\n\ts_barrier\n\t"
      "40:\n\t"
      TK5
      "s_sleep 2\n\tds_read_b128 v[208:211], v8 offset:0\n\tds_read_b128 v[212:215], v8 offset:64\n\tds_read_b128 v[216:219], v8 offset:128\n\t"
      "ds_read_b128 v[220:223], v8 offset:192\n\ts_and_b32 s48, s17, 7\n\ts_lshl_b32 s43, s48, 17\n\ts_add_u32 s43, s43, 0x300000\n\ts_add_u32 s48, s17, 3\n\t"
      "s_and_b32 s48, s48, 7\n\ts_lshl_b32 s44, s48, 17\n\ts_add_u32 s44, s44, 0x300000\n\ts_cmp_lg_u32 s58, 0\n\ts_cbranch_scc1 43f\n\t"
      "41:\n\ts_cmp_gt_u32 s55, s17\n\ts_cbranch_scc1 42f\n\tbuffer_load_dword v17, off, s[12:15], s45 sc1\n\ts_waitcnt vmcnt(0)\n\tv_readfirstlane_b32 s55, v17\n\t"
      "s_cmp_eq_u32 s18, 0\n\ts_cbranch_scc1 42f\n\ts_sub_u32 s18, s18, 1\n\ts_sleep 1\n\ts_branch 41b\n\t"
      "42:\n\tbuffer_load_dwordx4 v[240:243], v12, s[8:11], s22 offen offset:0 sc1\n\tbuffer_load_dwordx4 v[244:247], v12, s[8:11], s22 offen offset:2048 sc1\n\t"
      "buffer_load_dwordx4 v[248:251], v12, s[8:11], s49 offen offset:0 sc1\n\tbuffer_load_dwordx4 v[252:255], v12, s[8:11], s49 offen offset:2048 sc1\n\t"
      "43:\n\ts_waitcnt vmcnt(0)\n\tv_mov_b64 v[204:205], -1\n\tv_mov_b64 v[206:207], -1\n\tv_mov_b32 v203, s30\n\ts_cmp_eq_u32 s16, 2\n\ts_cbranch_scc1 50f\n\t"
      "v_mfma_f32_16x16x32_bf16 v[228:231], v[160:163], v[240:243], 0\n\tv_mfma_f32_16x16x32_bf16 v[228:231], v[164:167], v[244:247], v[228:231]\n\t"
      "v_mfma_f32_16x16x32_bf16 v[228:231], v[168:171], v[248:251], v[228:231]\n\tv_mfma_f32_16x16x32_bf16 v[228:231], v[172:175], v[252:255], v[228:231]\n\t"
      "s_waitcnt lgkmcnt(0)\n\tv_mfma_f32_16x16x32_bf16 v[224:227], v[208:211], v[240:243], 0\n\tv_mfma_f32_16x16x32_bf16 v[224:227], v[212:215], v[244:247], v[224:227]\n\t"
      "v_mfma_f32_16x16x32_bf16 v[224:227], v[216:219], v[248:251], v[224:227]\n\tv_mfma_f32_16x16x32_bf16 v[224:227], v[220:223], v[252:255], v[224:227]\n\ts_nop 2\n\t"
      "v_cndmask_b32 v228, v203, v228, s[32:33]\n\tv_cndmask_b32 v229, v203, v229, s[34:35]\n\tv_cndmask_b32 v230, v203, v230, s[36:37]\n\t"
      "v_cndmask_b32 v231, v203, v231, s[38:39]\n\tv_max3_f32 v192, v228, v229, v230\n\tv_max3_f32 v193, v231, v224, v225\n\tv_max3_f32 v192, v192, v226, v227\n\t"
      "v_max_f32 v192, v192, v193\n\tv_mov_b32 v193, v192\n\ts_nop 1\n\tv_permlane32_swap_b32 v192, v193\n\tv_max_f32 v192, v192, v193\n\tv_mov_b32 v193, v192\n\ts_nop 1\n\t"
      "v_permlane16_swap_b32 v192, v193\n\tv_max_f32 v192, v192, v193\n\tv_mov_b32 v193, v192\n\tv_pk_add_f32 v[224:225], v[224:225], v[192:193] neg_lo:[0,1] neg_hi:[0,1]\n\t"
      "v_pk_add_f32 v[226:227], v[226:227], v[192:193] neg_lo:[0,1] neg_hi:[0,1]\n\tv_pk_add_f32 v[228:229], v[228:229], v[192:193] neg_lo:[0,1] neg_hi:[0,1]\n\t"
      "v_pk_add_f32 v[230:231], v[230:231], v[192:193] neg_lo:[0,1] neg_hi:[0,1]\n\tv_pk_mul_f32 v[224:225], v[224:225], s[28:29]\n\t"
      "v_pk_mul_f32 v[226:227], v[226:227], s[28:29]\n\tv_pk_mul_f32 v[228:229], v[228:229], s[28:29]\n\tv_pk_mul_f32 v[230:231], v[230:231], s[28:29]\n\t"
      "v_exp_f32 v224, v224\n\tv_exp_f32 v225, v225\n\tv_exp_f32 v226, v226\n\tv_exp_f32 v227, v227\n\tv_exp_f32 v228, v228\n\tv_exp_f32 v229, v229\n\tv_exp_f32 v230, v230\n\t"
      "v_exp_f32 v231, v231\n\tv_pk_add_f32 v[192:193], v[224:225], v[226:227]\n\tv_pk_add_f32 v[194:195], v[228:229], v[230:231]\n\tv_add_f32 v192, v192, v193\n\t"
      "v_add_f32 v194, v194, v195\n\tv_fma_f32 v192, v192, v13, v194\n\tv_mov_b32 v193, v192\n\ts_nop 1\n\tv_permlane32_swap_b32 v192, v193\n\tv_add_f32 v192, v192, v193\n\t"
      "v_mov_b32 v193, v192\n\ts_nop 1\n\tv_permlane16_swap_b32 v192, v193\n\tv_add_f32 v192, v192, v193\n\tv_rcp_f32 v192, v192\n\ts_nop 0\n\tv_mov_b32 v193, v192\n\t"
      "v_pk_mul_f32 v[224:225], v[224:225], v[192:193]\n\tv_pk_mul_f32 v[226:227], v[226:227], v[192:193]\n\tv_pk_mul_f32 v[228:229], v[228:229], v[192:193]\n\t"
      "v_pk_mul_f32 v[230:231], v[230:231], v[192:193]\n\tv_cvt_pk_bf16_f32 v194, v224, v225\n\tv_cvt_pk_bf16_f32 v195, v226, v227\n\tv_cvt_pk_bf16_f32 v196, v228, v229\n\t"
      "v_cvt_pk_bf16_f32 v197, v230, v231\n\tbuffer_store_dwordx2 v[196:197], v10, s[8:11], s43 offen sc1\n\tbuffer_store_dwordx2 v[194:195], v11, s[8:11], s43 offen sc1\n\t"
      "buffer_store_dwordx2 v[204:205], v10, s[8:11], s44 offen sc1\n\tbuffer_store_dwordx2 v[204:205], v11, s[8:11], s44 offen sc1\n\t"
      TK6
      "s_branch 69f\n\t"
      "50:\n\tv_mfma_f32_16x16x32_bf16 v[228:231], v[160:163], v[240:243], 0\n\tv_mfma_f32_16x16x32_bf16 v[232:235], v[176:179], v[240:243], 0\n\t"
      "v_mfma_f32_16x16x32_bf16 v[228:231], v[164:167], v[244:247], v[228:231]\n\tv_mfma_f32_16x16x32_bf16 v[232:235], v[180:183], v[244:247], v[232:235]\n\t"
      "v_mfma_f32_16x16x32_bf16 v[228:231], v[168:171], v[248:251], v[228:231]\n\tv_mfma_f32_16x16x32_bf16 v[232:235], v[184:187], v[248:251], v[232:235]\n\t"
      "v_mfma_f32_16x16x32_bf16 v[228:231], v[172:175], v[252:255], v[228:231]\n\tv_mfma_f32_16x16x32_bf16 v[232:235], v[188:191], v[252:255], v[232:235]\n\t"
      "s_waitcnt lgkmcnt(0)\n\tv_mfma_f32_16x16x32_bf16 v[224:227], v[208:211], v[240:243], 0\n\tv_mfma_f32_16x16x32_bf16 v[224:227], v[212:215], v[244:247], v[224:227]\n\t"
      "v_mfma_f32_16x16x32_bf16 v[224:227], v[216:219], v[248:251], v[224:227]\n\tv_mfma_f32_16x16x32_bf16 v[224:227], v[220:223], v[252:255], v[224:227]\n\ts_nop 2\n\t"
      "v_cndmask_b32 v232, v203, v232, s[32:33]\n\tv_cndmask_b32 v233, v203, v233, s[34:35]\n\tv_cndmask_b32 v234, v203, v234, s[36:37]\n\t"
      "v_cndmask_b32 v235, v203, v235, s[38:39]\n\tv_max3_f32 v192, v228, v229, v230\n\tv_max3_f32 v193, v231, v224, v225\n\tv_max3_f32 v192, v192, v226, v227\n\t"
      "v_max3_f32 v193, v193, v232, v233\n\tv_max3_f32 v192, v192, v234, v235\n\tv_max_f32 v192, v192, v193\n\tv_mov_b32 v193, v192\n\ts_nop 1\n\t"
      "v_permlane32_swap_b32 v192, v193\n\tv_max_f32 v192, v192, v193\n\tv_mov_b32 v193, v192\n\ts_nop 1\n\tv_permlane16_swap_b32 v192, v193\n\tv_max_f32 v192, v192, v193\n\t"
      "v_mov_b32 v193, v192\n\tv_pk_add_f32 v[224:225], v[224:225], v[192:193] neg_lo:[0,1] neg_hi:[0,1]\n\t"
      "v_pk_add_f32 v[226:227], v[226:227], v[192:193] neg_lo:[0,1] neg_hi:[0,1]\n\tv_pk_add_f32 v[228:229], v[228:229], v[192:193] neg_lo:[0,1] neg_hi:[0,1]\n\t"
      "v_pk_add_f32 v[230:231], v[230:231], v[192:193] neg_lo:[0,1] neg_hi:[0,1]\n\tv_pk_add_f32 v[232:233], v[232:233], v[192:193] neg_lo:[0,1] neg_hi:[0,1]\n\t"
      "v_pk_add_f32 v[234:235], v[234:235], v[192:193] neg_lo:[0,1] neg_hi:[0,1]\n\tv_pk_mul_f32 v[224:225], v[224:225], s[28:29]\n\t"
      "v_pk_mul_f32 v[226:227], v[226:227], s[28:29]\n\tv_pk_mul_f32 v[228:229], v[228:229], s[28:29]\n\tv_pk_mul_f32 v[230:231], v[230:231], s[28:29]\n\t"
      "v_pk_mul_f32 v[232:233], v[232:233], s[28:29]\n\tv_pk_mul_f32 v[234:235], v[234:235], s[28:29]\n\tv_exp_f32 v224, v224\n\tv_exp_f32 v225, v225\n\tv_exp_f32 v226, v226\n\t"
      "v_exp_f32 v227, v227\n\tv_exp_f32 v228, v228\n\tv_exp_f32 v229, v229\n\tv_exp_f32 v230, v230\n\tv_exp_f32 v231, v231\n\tv_exp_f32 v232, v232\n\tv_exp_f32 v233, v233\n\t"
      "v_exp_f32 v234, v234\n\tv_exp_f32 v235, v235\n\tv_pk_add_f32 v[192:193], v[224:225], v[226:227]\n\tv_pk_add_f32 v[194:195], v[228:229], v[230:231]\n\t"
      "v_pk_add_f32 v[196:197], v[232:233], v[234:235]\n\tv_pk_add_f32 v[194:195], v[194:195], v[196:197]\n\tv_add_f32 v192, v192, v193\n\tv_add_f32 v194, v194, v195\n\t"
      "v_fma_f32 v192, v192, v13, v194\n\tv_mov_b32 v193, v192\n\ts_nop 1\n\tv_permlane32_swap_b32 v192, v193\n\tv_add_f32 v192, v192, v193\n\tv_mov_b32 v193, v192\n\t"
      "s_nop 1\n\tv_permlane16_swap_b32 v192, v193\n\tv_add_f32 v192, v192, v193\n\tv_rcp_f32 v192, v192\n\ts_nop 0\n\tv_mov_b32 v193, v192\n\t"
      "v_pk_mul_f32 v[224:225], v[224:225], v[192:193]\n\tv_pk_mul_f32 v[226:227], v[226:227], v[192:193]\n\tv_pk_mul_f32 v[228:229], v[228:229], v[192:193]\n\t"
      "v_pk_mul_f32 v[230:231], v[230:231], v[192:193]\n\tv_pk_mul_f32 v[232:233], v[232:233], v[192:193]\n\tv_pk_mul_f32 v[234:235], v[234:235], v[192:193]\n\t"
      "v_cvt_pk_bf16_f32 v194, v224, v225\n\tv_cvt_pk_bf16_f32 v195, v226, v227\n\tv_cvt_pk_bf16_f32 v196, v228, v229\n\tv_cvt_pk_bf16_f32 v197, v230, v231\n\t"
      "v_cvt_pk_bf16_f32 v198, v232, v233\n\tv_cvt_pk_bf16_f32 v199, v234, v235\n\tbuffer_store_dwordx4 v[196:199], v10, s[8:11], s43 offen sc1\n\t"
      "buffer_store_dwordx2 v[194:195], v11, s[8:11], s43 offen sc1\n\tbuffer_store_dwordx4 v[204:207], v10, s[8:11], s44 offen sc1\n\t"
      "buffer_store_dwordx2 v[204:205], v11, s[8:11], s44 offen sc1\n\t"
      TK6
      "s_branch 69f\n\t"
      "60:\n\t"
      TK5
      "s_sleep 7\n\tv_mov_b32 v17, s17\n\tbuffer_store_dword v17, v12, s[12:15], 0 offen sc1\n\tbuffer_load_dword v18, v15, s[8:11], s41 offen sc1\n\t"
      "64:\n\ts_add_u32 s49, s17, 1\n\ts_cmp_eq_u32 s49, 64\n\ts_cbranch_scc1 69f\n\ts_add_u32 s48, s45, 0x1000\n\t"
      "61:\n\ts_cmp_gt_u32 s54, s49\n\ts_cbranch_scc1 62f\n\tbuffer_load_dword v17, off, s[12:15], s48 sc1\n\ts_waitcnt vmcnt(0)\n\tv_readfirstlane_b32 s54, v17\n\t"
      "s_cmp_eq_u32 s18, 0\n\ts_cbranch_scc1 62f\n\ts_sub_u32 s18, s18, 1\n\ts_sleep 1\n\ts_branch 61b\n\t"
      "62:\n\ts_add_u32 s46, s31, 0x1000\n\ts_mov_b32 m0, 147456\n\ts_add_u32 s49, s49, 1\n\tbuffer_load_dwordx4 v10, s[8:11], s31 offen offset:0 sc1 lds\n\t"
      "buffer_load_dwordx4 v10, s[8:11], s31 offen offset:1024 sc1 lds\n\tbuffer_load_dwordx4 v10, s[8:11], s31 offen offset:2048 sc1 lds\n\t"
      "buffer_load_dwordx4 v10, s[8:11], s31 offen offset:3072 sc1 lds\n\ts_mov_b32 m0, 151552\n\ts_nop 0\n\tbuffer_load_dwordx4 v10, s[8:11], s46 offen offset:0 sc1 lds\n\t"
      "buffer_load_dwordx4 v10, s[8:11], s46 offen offset:1024 sc1 lds\n\tbuffer_load_dwordx4 v10, s[8:11], s46 offen offset:2048 sc1 lds\n\t"
      "buffer_load_dwordx4 v10, s[8:11], s46 offen offset:3072 sc1 lds\n\t"
      "s_min_u32 s46, s55, s54\n\ts_cmp_gt_u32 s46, s49\n\ts_cbranch_scc1 63f\n\tbuffer_load_dword v17, off, s[12:15], s45 sc1\n\t"
      "buffer_load_dword v19, off, s[12:15], s48 sc1\n\ts_waitcnt vmcnt(0)\n\tds_write_b32 v16, v17 offset:4\n\tv_readfirstlane_b32 s54, v19\n\t"
      "63:\n\ts_cmp_lt_i32 s17, 0\n\ts_cbranch_scc1 69f\n\t"
      TK6
      "s_waitcnt vmcnt(8)\n\ts_branch 76f\n\t"
      "74:\n\tbuffer_load_dword v18, v15, s[8:11], s41 offen sc1\n\ts_waitcnt vmcnt(0)\n\t"
      "76:\n\tv_max_u32_dpp v18, v18, v18 row_ror:8 row_mask:0xf bank_mask:0xf\n\ts_nop 1\n\tv_max_u32_dpp v18, v18, v18 row_ror:4 row_mask:0xf bank_mask:0xf\n\ts_nop 1\n\t"
      "v_max_u32_dpp v18, v18, v18 row_ror:2 row_mask:0xf bank_mask:0xf\n\ts_nop 1\n\tv_max_u32_dpp v18, v18, v18 row_ror:1 row_mask:0xf bank_mask:0xf\n\ts_nop 0\n\t"
      "v_readlane_b32 s52, v18, 0\n\tv_readlane_b32 s48, v18, 16\n\ts_max_u32 s52, s52, s48\n\ts_cmp_lg_u32 s52, -1\n\ts_cbranch_scc1 75f\n\ts_cmp_eq_u32 s18, 0\n\t"
      "s_cbranch_scc1 75f\n\ts_sub_u32 s18, s18, 1\n\ts_branch 74b\n\t"
      "75:\n\ts_sub_u32 s48, s52, s19\n\ts_sub_u32 s48, s48, s62\n\ts_cmp_gt_i32 s48, s72\n\ts_cselect_b32 s19, s73, 0\n\ts_cmp_gt_i32 s48, 40\n\t"
      "s_cselect_b32 s48, 40, s48\n\ts_ashr_i32 s48, s48, 2\n\ts_add_i32 s62, s62, s48\n\ts_min_u32 s62, s62, 1000\n\ts_mul_i32 s63, s62, 3\n\ts_lshr_b32 s63, s63, 3\n\t"
      "s_add_u32 s19, s19, s52\n\ts_add_u32 s19, s19, s23\n\ts_add_u32 s19, s19, s63\n\ts_branch 69f\n\t"
      "86:\n\t"
      STS("s_add_u32 s50, s50, 1\n\t")
      "s_waitcnt lgkmcnt(0)\n\tbuffer_load_dwordx4 v[240:243], v0, s[8:11], s68 offen sc1\n\tbuffer_load_dwordx4 v[244:247], v0, s[8:11], s69 offen sc1\n\t"
      "buffer_load_dwordx4 v[248:251], v0, s[8:11], s70 offen sc1\n\tbuffer_load_dwordx4 v[252:255], v0, s[8:11], s71 offen sc1\n\ts_nop 3\n\tv_mov_b64 v[224:225], 0\n\t"
      "v_mov_b64 v[226:227], 0\n\tv_mov_b64 v[228:229], 0\n\tv_mov_b64 v[230:231], 0\n\tv_mov_b64 v[232:233], 0\n\tv_mov_b64 v[234:235], 0\n\tv_mov_b64 v[236:237], 0\n\t"
      "v_mov_b64 v[238:239], 0\n\ts_nop 1\n\ts_branch 84f\n\t"
      "83:\n\tbuffer_load_dwordx4 v[240:243], v0, s[8:11], s68 offen sc1\n\tbuffer_load_dwordx4 v[244:247], v0, s[8:11], s69 offen sc1\n\t"
      "buffer_load_dwordx4 v[248:251], v0, s[8:11], s70 offen sc1\n\tbuffer_load_dwordx4 v[252:255], v0, s[8:11], s71 offen sc1\n\t"
      "84:\n\ts_waitcnt vmcnt(0)\n\tv_max3_u32 v17, v240, v241, v242\n\tv_max_u32 v17, v17, v243\n\tv_max3_u32 v17, v17, v244, v245\n\tv_max3_u32 v17, v17, v246, v247\n\t"
      "v_max3_u32 v17, v17, v248, v249\n\tv_max3_u32 v17, v17, v250, v251\n\tv_max3_u32 v17, v17, v252, v253\n\tv_max3_u32 v17, v17, v254, v255\n\tv_cmp_eq_u32 vcc, -1, v17\n\t"
      "s_cbranch_vccz 85f\n\ts_cmp_eq_u32 s18, 0\n\ts_cbranch_scc1 85f\n\ts_sub_u32 s18, s18, 1\n\ts_branch 83b\n\t"
      "85:\n\tv_smfmac_f32_16x16x64_bf16 v[224:227], v[240:243], a[0:7], v6\n\tv_smfmac_f32_16x16x64_bf16 v[228:231], v[240:243], a[8:15], v6\n\t"
      "v_smfmac_f32_16x16x64_bf16 v[232:235], v[240:243], v[32:39], v6\n\tv_smfmac_f32_16x16x64_bf16 v[236:239], v[240:243], v[160:167], v6\n\t"
      "v_smfmac_f32_16x16x64_bf16 v[224:227], v[244:247], a[16:23], v6\n\tv_smfmac_f32_16x16x64_bf16 v[228:231], v[244:247], a[24:31], v6\n\t"
      "ds_write_b128 v1, v[240:243] offset:0\n\tds_write_b128 v1, v[244:247] offset:1024\n\tds_write_b128 v1, v[248:251] offset:2048\n\t"
      "ds_write_b128 v1, v[252:255] offset:3072\n\tv_mov_b32 v17, 0\n\ts_mov_b64 vcc, 0\n\ts_waitcnt lgkmcnt(0)\n\ts_cmp_lt_u32 s16, 2\n\ts_cbranch_scc1 20b\n\ts_branch 24b\n\t"
      "87:\n\t"
      STS("s_add_u32 s50, s50, 1\n\t")
      "s_waitcnt lgkmcnt(0)\n\tbuffer_load_dwordx4 v[244:247], v0, s[8:11], s69 offen sc1\n\tbuffer_load_dwordx4 v[248:251], v0, s[8:11], s70 offen sc1\n\t"
      "buffer_load_dwordx4 v[252:255], v0, s[8:11], s71 offen sc1\n\ts_nop 3\n\tv_mov_b64 v[224:225], 0\n\tv_mov_b64 v[226:227], 0\n\tv_mov_b64 v[228:229], 0\n\t"
      "v_mov_b64 v[230:231], 0\n\tv_mov_b64 v[232:233], 0\n\tv_mov_b64 v[234:235], 0\n\tv_mov_b64 v[236:237], 0\n\tv_mov_b64 v[238:239], 0\n\ts_nop 1\n\t"
      "v_smfmac_f32_16x16x64_bf16 v[224:227], v[240:243], a[0:7], v6\n\tv_smfmac_f32_16x16x64_bf16 v[228:231], v[240:243], a[8:15], v6\n\t"
      "v_smfmac_f32_16x16x64_bf16 v[232:235], v[240:243], v[32:39], v6\n\tv_smfmac_f32_16x16x64_bf16 v[236:239], v[240:243], v[160:167], v6\n\ts_branch 84f\n\t"
      "83:\n\tbuffer_load_dwordx4 v[244:247], v0, s[8:11], s69 offen sc1\n\tbuffer_load_dwordx4 v[248:251], v0, s[8:11], s70 offen sc1\n\t"
      "buffer_load_dwordx4 v[252:255], v0, s[8:11], s71 offen sc1\n\t"
      "84:\n\ts_waitcnt vmcnt(0)\n\tv_max3_u32 v17, v244, v245, v246\n\tv_max_u32 v17, v17, v247\n\tv_max3_u32 v17, v17, v248, v249\n\tv_max3_u32 v17, v17, v250, v251\n\t"
      "v_max3_u32 v17, v17, v252, v253\n\tv_max3_u32 v17, v17, v254, v255\n\tv_cmp_eq_u32 vcc, -1, v17\n\ts_cbranch_vccz 85f\n\ts_cmp_eq_u32 s18, 0\n\ts_cbranch_scc1 85f\n\t"
      "s_sub_u32 s18, s18, 1\n\ts_branch 83b\n\t"
      "85:\n\tv_smfmac_f32_16x16x64_bf16 v[224:227], v[244:247], a[16:23], v6\n\tv_smfmac_f32_16x16x64_bf16 v[228:231], v[244:247], a[24:31], v6\n\t"
      "v_smfmac_f32_16x16x64_bf16 v[232:235], v[244:247], v[40:47], v6\n\tv_smfmac_f32_16x16x64_bf16 v[236:239], v[244:247], v[168:175], v6\n\t"
      "v_smfmac_f32_16x16x64_bf16 v[224:227], v[248:251], a[32:39], v6\n\tv_smfmac_f32_16x16x64_bf16 v[228:231], v[248:251], a[40:47], v6\n\t"
      "ds_write_b128 v1, v[244:247] offset:1024\n\tds_write_b128 v1, v[248:251] offset:2048\n\tds_write_b128 v1, v[252:255] offset:3072\n\tv_mov_b32 v17, 0\n\t"
      "s_mov_b64 vcc, 0\n\ts_waitcnt lgkmcnt(0)\n\ts_cmp_lt_u32 s16, 2\n\ts_cbranch_scc1 21b\n\ts_branch 25b\n\t"
      "88:\n\t"
      STS("s_add_u32 s50, s50, 1\n\t")
      "s_waitcnt lgkmcnt(0)\n\tbuffer_load_dwordx4 v[248:251], v0, s[8:11], s70 offen sc1\n\tbuffer_load_dwordx4 v[252:255], v0, s[8:11], s71 offen sc1\n\ts_nop 3\n\t"
      "v_mov_b64 v[224:225], 0\n\tv_mov_b64 v[226:227], 0\n\tv_mov_b64 v[228:229], 0\n\tv_mov_b64 v[230:231], 0\n\tv_mov_b64 v[232:233], 0\n\tv_mov_b64 v[234:235], 0\n\t"
      "v_mov_b64 v[236:237], 0\n\tv_mov_b64 v[238:239], 0\n\ts_nop 1\n\tv_smfmac_f32_16x16x64_bf16 v[224:227], v[240:243], a[0:7], v6\n\t"
      "v_smfmac_f32_16x16x64_bf16 v[228:231], v[240:243], a[8:15], v6\n\tv_smfmac_f32_16x16x64_bf16 v[232:235], v[240:243], v[32:39], v6\n\t"
      "v_smfmac_f32_16x16x64_bf16 v[236:239], v[240:243], v[160:167], v6\n\tv_smfmac_f32_16x16x64_bf16 v[224:227], v[244:247], a[16:23], v6\n\t"
      "v_smfmac_f32_16x16x64_bf16 v[228:231], v[244:247], a[24:31], v6\n\tv_smfmac_f32_16x16x64_bf16 v[232:235], v[244:247], v[40:47], v6\n\t"
      "v_smfmac_f32_16x16x64_bf16 v[236:239], v[244:247], v[168:175], v6\n\ts_branch 84f\n\t"
      "83:\n\tbuffer_load_dwordx4 v[248:251], v0, s[8:11], s70 offen sc1\n\tbuffer_load_dwordx4 v[252:255], v0, s[8:11], s71 offen sc1\n\t"
      "84:\n\ts_waitcnt vmcnt(0)\n\tv_max3_u32 v17, v248, v249, v250\n\tv_max_u32 v17, v17, v251\n\tv_max3_u32 v17, v17, v252, v253\n\tv_max3_u32 v17, v17, v254, v255\n\t"
      "v_cmp_eq_u32 vcc, -1, v17\n\ts_cbranch_vccz 85f\n\ts_cmp_eq_u32 s18, 0\n\ts_cbranch_scc1 85f\n\ts_sub_u32 s18, s18, 1\n\ts_branch 83b\n\t"
      "85:\n\tv_smfmac_f32_16x16x64_bf16 v[224:227], v[248:251], a[32:39], v6\n\tv_smfmac_f32_16x16x64_bf16 v[228:231], v[248:251], a[40:47], v6\n\t"
      "v_smfmac_f32_16x16x64_bf16 v[232:235], v[248:251], v[48:55], v6\n\tv_smfmac_f32_16x16x64_bf16 v[236:239], v[248:251], v[176:183], v6\n\t"
      "v_smfmac_f32_16x16x64_bf16 v[224:227], v[252:255], a[48:55], v6\n\tv_smfmac_f32_16x16x64_bf16 v[228:231], v[252:255], a[56:63], v6\n\t"
      "ds_write_b128 v1, v[248:251] offset:2048\n\tds_write_b128 v1, v[252:255] offset:3072\n\tv_mov_b32 v17, 0\n\ts_mov_b64 vcc, 0\n\ts_waitcnt lgkmcnt(0)\n\t"
      "s_cmp_lt_u32 s16, 2\n\ts_cbranch_scc1 22b\n\ts_branch 26b\n\t"
      "89:\n\t"
      STS("s_add_u32 s50, s50, 1\n\t")
      "s_waitcnt lgkmcnt(0)\n\tbuffer_load_dwordx4 v[252:255], v0, s[8:11], s71 offen sc1\n\ts_nop 3\n\tv_mov_b64 v[224:225], 0\n\tv_mov_b64 v[226:227], 0\n\t"
      "v_mov_b64 v[228:229], 0\n\tv_mov_b64 v[230:231], 0\n\tv_mov_b64 v[232:233], 0\n\tv_mov_b64 v[234:235], 0\n\tv_mov_b64 v[236:237], 0\n\tv_mov_b64 v[238:239], 0\n\t"
      "s_nop 1\n\tv_smfmac_f32_16x16x64_bf16 v[224:227], v[240:243], a[0:7], v6\n\tv_smfmac_f32_16x16x64_bf16 v[228:231], v[240:243], a[8:15], v6\n\t"
      "v_smfmac_f32_16x16x64_bf16 v[232:235], v[240:243], v[32:39], v6\n\tv_smfmac_f32_16x16x64_bf16 v[236:239], v[240:243], v[160:167], v6\n\t"
      "v_smfmac_f32_16x16x64_bf16 v[224:227], v[244:247], a[16:23], v6\n\tv_smfmac_f32_16x16x64_bf16 v[228:231], v[244:247], a[24:31], v6\n\t"
      "v_smfmac_f32_16x16x64_bf16 v[232:235], v[244:247], v[40:47], v6\n\tv_smfmac_f32_16x16x64_bf16 v[236:239], v[244:247], v[168:175], v6\n\t"
      "v_smfmac_f32_16x16x64_bf16 v[224:227], v[248:251], a[32:39], v6\n\tv_smfmac_f32_16x16x64_bf16 v[228:231], v[248:251], a[40:47], v6\n\t"
      "v_smfmac_f32_16x16x64_bf16 v[232:235], v[248:251], v[48:55], v6\n\tv_smfmac_f32_16x16x64_bf16 v[236:239], v[248:251], v[176:183], v6\n\ts_branch 84f\n\t"
      "83:\n\tbuffer_load_dwordx4 v[252:255], v0, s[8:11], s71 offen sc1\n\t"
      "84:\n\ts_waitcnt vmcnt(0)\n\tv_max3_u32 v17, v252, v253, v254\n\tv_max_u32 v17, v17, v255\n\tv_cmp_eq_u32 vcc, -1, v17\n\ts_cbranch_vccz 85f\n\ts_cmp_eq_u32 s18, 0\n\t"
      "s_cbranch_scc1 85f\n\ts_sub_u32 s18, s18, 1\n\ts_branch 83b\n\t"
      "85:\n\tv_smfmac_f32_16x16x64_bf16 v[224:227], v[252:255], a[48:55], v6\n\tv_smfmac_f32_16x16x64_bf16 v[228:231], v[252:255], a[56:63], v6\n\t"
      "v_smfmac_f32_16x16x64_bf16 v[232:235], v[252:255], v[56:63], v6\n\tv_smfmac_f32_16x16x64_bf16 v[236:239], v[252:255], v[184:191], v6\n\t"
      "v_smfmac_f32_16x16x64_bf16 v[224:227], v[20:23], a[64:71], v6\n\tv_smfmac_f32_16x16x64_bf16 v[228:231], v[20:23], a[72:79], v6\n\t"
      "ds_write_b128 v1, v[252:255] offset:3072\n\tv_mov_b32 v17, 0\n\ts_mov_b64 vcc, 0\n\ts_waitcnt lgkmcnt(0)\n\ts_cmp_lt_u32 s16, 2\n\ts_cbranch_scc1 23b\n\ts_branch 27b\n\t"
      "69:\n\t"
      TK7
      STS("s_cmp_lt_i32 s17, 8\n\ts_cselect_b32 s50, 0, s50\n\ts_cselect_b32 s51, 0, s51\n\ts_cselect_b32 s61, 0, s61\n\t")
      TKS("s_waitcnt lgkmcnt(0)\n\ts_cmp_lt_i32 s17, 0\n\ts_cbranch_scc1 98f\n\ts_cmp_lg_u32 s16, s91\n\ts_cbranch_scc1 98f\n\ts_mov_b64 exec, 1\n\ts_lshl_b32 s48, s17, 4\n\ts_add_u32 s48, s48, 161056\n\tv_mov_b32 v17, s48\n\ts_sub_u32 s48, s74, s90\n\ts_min_u32 s48, s48, 0xffff\n\tv_mov_b32 v18, s48\n\tds_write_b16 v17, v18 offset:0\n\ts_sub_u32 s48, s76, s74\n\ts_min_u32 s48, s48, 0xffff\n\tv_mov_b32 v18, s48\n\tds_write_b16 v17, v18 offset:2\n\ts_sub_u32 s48, s78, s76\n\ts_min_u32 s48, s48, 0xffff\n\tv_mov_b32 v18, s48\n\tds_write_b16 v17, v18 offset:4\n\ts_sub_u32 s48, s80, s78\n\ts_min_u32 s48, s48, 0xffff\n\tv_mov_b32 v18, s48\n\tds_write_b16 v17, v18 offset:6\n\ts_sub_u32 s48, s82, s80\n\ts_min_u32 s48, s48, 0xffff\n\tv_mov_b32 v18, s48\n\tds_write_b16 v17, v18 offset:8\n\ts_sub_u32 s48, s84, s82\n\ts_min_u32 s48, s48, 0xffff\n\tv_mov_b32 v18, s48\n\tds_write_b16 v17, v18 offset:10\n\ts_sub_u32 s48, s86, s84\n\ts_min_u32 s48, s48, 0xffff\n\tv_mov_b32 v18, s48\n\tds_write_b16 v17, v18 offset:12\n\ts_sub_u32 s48, s88, s86\n\ts_min_u32 s48, s48, 0xffff\n\tv_mov_b32 v18, s48\n\tds_write_b16 v17, v18 offset:14\n\ts_mov_b64 exec, -1\n\t98:\n\ts_mov_b32 s90, s88\n\t")
      "s_add_u32 s48, s22, 0x4000\n\ts_and_b32 s48, s48, 0x3c000\n\ts_andn2_b32 s22, s22, 0x3c000\n\ts_or_b32 s22, s22, s48\n\ts_add_u32 s48, s31, 0x4000\n\t"
      "s_and_b32 s48, s48, 0x3c000\n\ts_andn2_b32 s31, s31, 0x3c000\n\ts_or_b32 s31, s31, s48\n\ts_add_u32 s17, s17, 1\n\ts_cmp_lt_u32 s17, 64\n\ts_cbranch_scc1 10b\n\t"
      ".Lmk_end4:\n\ts_waitcnt vmcnt(0) lgkmcnt(0)\n\t"
      TKS("s_mul_i32 s48, s16, 72\n\ts_add_u32 s48, s48, 162080\n\tv_mov_b32 v224, s48\n\tds_read_b32 v225, v224 offset:0\n\ts_waitcnt lgkmcnt(0)\n\tv_readfirstlane_b32 s74, v225\n\tds_read_b32 v225, v224 offset:4\n\ts_waitcnt lgkmcnt(0)\n\tv_readfirstlane_b32 s75, v225\n\tds_read_b32 v225, v224 offset:8\n\ts_waitcnt lgkmcnt(0)\n\tv_readfirstlane_b32 s76, v225\n\tds_read_b32 v225, v224 offset:12\n\ts_waitcnt lgkmcnt(0)\n\tv_readfirstlane_b32 s77, v225\n\tds_read_b32 v225, v224 offset:16\n\ts_waitcnt lgkmcnt(0)\n\tv_readfirstlane_b32 s78, v225\n\tds_read_b32 v225, v224 offset:20\n\ts_waitcnt lgkmcnt(0)\n\tv_readfirstlane_b32 s79, v225\n\tds_read_b32 v225, v224 offset:24\n\ts_waitcnt lgkmcnt(0)\n\tv_readfirstlane_b32 s80, v225\n\tds_read_b32 v225, v224 offset:28\n\ts_waitcnt lgkmcnt(0)\n\tv_readfirstlane_b32 s81, v225\n\tds_read_b32 v225, v224 offset:32\n\ts_waitcnt lgkmcnt(0)\n\tv_readfirstlane_b32 s82, v225\n\tds_read_b32 v225, v224 offset:36\n\ts_waitcnt lgkmcnt(0)\n\tv_readfirstlane_b32 s83, v225\n\tds_read_b32 v225, v224 offset:40\n\ts_waitcnt lgkmcnt(0)\n\tv_readfirstlane_b32 s84, v225\n\tds_read_b32 v225, v224 offset:44\n\ts_waitcnt lgkmcnt(0)\n\tv_readfirstlane_b32 s85, v225\n\tds_read_b32 v225, v224 offset:48\n\ts_waitcnt lgkmcnt(0)\n\tv_readfirstlane_b32 s86, v225\n\tds_read_b32 v225, v224 offset:52\n\ts_waitcnt lgkmcnt(0)\n\tv_readfirstlane_b32 s87, v225\n\tds_read_b32 v225, v224 offset:56\n\ts_waitcnt lgkmcnt(0)\n\tv_readfirstlane_b32 s88, v225\n\tds_read_b32 v225, v224 offset:60\n\ts_waitcnt lgkmcnt(0)\n\tv_readfirstlane_b32 s89, v225\n\tds_read_b32 v225, v224 offset:64\n\ts_waitcnt lgkmcnt(0)\n\tv_readfirstlane_b32 s90, v225\n\tds_read_b32 v225, v224 offset:68\n\ts_waitcnt lgkmcnt(0)\n\tv_readfirstlane_b32 s91, v225\n\t")
      STS("s_lshl_b32 s48, s16, 2\n\ts_add_u32 s48, s48, 162944\n\tv_mov_b32 v0, s48\n\tv_mov_b32 v1, s50\n\tds_write_b32 v0, v1\n\ts_cmp_lg_u32 s16, 0\n\ts_cbranch_scc1 97f\n\tv_mov_b32 v1, s51\n\tds_write_b32 v0, v1 offset:16\n\t97:\n\ts_cmp_lg_u32 s16, 3\n\ts_cbranch_scc1 95f\n\tv_mov_b32 v1, s61\n\tds_write_b32 v0, v1 offset:8\n\tv_mov_b32 v1, s21\n\tds_write_b32 v0, v1 offset:12\n\tv_mov_b32 v1, s22\n\tds_write_b32 v0, v1 offset:16\n\t95:\n\t")
      "s_cmp_lg_u32 s18, 0\n\ts_cbranch_scc1 96f\n\tv_mov_b32 v1, 1\n\tbuffer_store_dword v1, off, s[12:15], 0 offset:3328 sc1\n\t"
      "96:\n\t"
      : "+{v[0:31]}"(k), "+{v[160:191]}"(s0), "+{v[192:223]}"(s1), ALL_REGS :: "memory", "vcc", "scc", S_CLOB TK_CLOB);
}

DEV void vo_role(Ctx& c, int h) {
  const KArg p = enter(c);
  constexpr int WL = 0, MEMS = 131072, VM = 147456, VROW = 272;
  c.budget = SPIN_CAP;
  const int w = c.w, lane = fresh_lane(), g = lane >> 4, cc = lane & 15, q = (lane >> 2) & 3, pp = lane & 3;
  auto grp = [&](int i) { return ((h >> 3) - ROFF - i) & 3; };
  v32u k;
  k[0] = amap(w * 4096 + lane * 16, 0);
  k[1] = MEMS + w * 4096 + lane * 16;
#pragma unroll
  for (int r = 0; r < 3; ++r) k[2 + r] = MEMS + ((w + r + 1) & 3) * 4096 + lane * 16;
  k[5] = WL + w * 32768 + lane * 16;
  k[6] = (lane & 1) ? 0xEEEE : 0x4444;
  k[7] = VM + 2 * g * VROW + (32 * w + 2 * cc) * 2;
  k[8] = VM + (cc & 7) * VROW + 16 * g;
  k[9] = R_U + (16 * w + (g >> 1)) * 16384 + h * 256 + cc * 16 + (g & 1) * 8;
  k[10] = 0;
  k[11] = amap(24576 + (c.slot >> 5) * 4096 + (lane & 31) * 128, 0);
  k[12] = (lane & 8) ? 0xEEEE : 0x4444;
#pragma unroll
  for (int i = 0; i < 4; ++i) k[14 + i] = g * 8192 + h * 256 + 16 * (cc ^ (2 * (g & 1) + 4 * i));
#pragma unroll
  for (int i = 0; i < 2; ++i) { const int ch = cc ^ (4 * (g >> 1) + 8 * i); k[18 + i] = g * 2048 + w * 512 + grp(2 + (ch >> 3)) * 128 + 16 * (ch & 7); }
#pragma unroll
  for (int i = 0; i < 8; ++i)
    k[20 + i] = WL + ((w + (i >> 1)) & 3) * 32768 + (8 * (q >> 1) + (q & 1) + krow_lane(g)) * 256 + 16 * ((4 * w + 2 * (i & 1) + (pp >> 1)) ^ (2 * (q & 1) + 4 * (g & 1) + 8 * (q >> 1))) + 8 * (pp & 1);
#pragma unroll
  for (int i = 0; i < 4; ++i) k[28 + i] = WL + w * 32768 + (16 * (q & 1) + 2 * g) * 256 + 16 * ((4 * i + 2 * (q >> 1) + (pp >> 1)) ^ (4 * (g & 1) + 8 * (q & 1))) + 8 * (pp & 1);
  k[13] = 0;
  v32u s0 = {RSRCS, (unsigned)w, 0, c.budget, 0, 0, PACE, (unsigned)grp(0) << 16, (unsigned)grp(1) << 16, (unsigned)grp(2) << 16, (unsigned)grp(3) << 16};
  v32u s1 = {plo(p->Wv), phi(p->Wv), 1024 * 4096 * 2, 0x00020000, plo(p->Wo), phi(p->Wo), 1024 * 4096 * 2, 0x00020000, 0, (unsigned)(WL + w * 32768), 0, 0, 0, 0, 0, 0,
             (unsigned)(w << 21), (unsigned)(h << 18), 0, 0, plo((const void*)p), phi((const void*)p), c.att ? 0u : (unsigned)c.b + 256u, (unsigned)(C_VERDICT + c.att * 128), ROTS(c.slot >> 5), HELLO};
  v32u wa[8], wv[5];
  asm volatile(
      "s_nop 0\n\tv_readfirstlane_b32 s8, v160\n\tv_readfirstlane_b32 s9, v161\n\tv_readfirstlane_b32 s10, v162\n\tv_readfirstlane_b32 s11, v163\n\t"
      "v_readfirstlane_b32 s12, v164\n\tv_readfirstlane_b32 s13, v165\n\tv_readfirstlane_b32 s14, v166\n\tv_readfirstlane_b32 s15, v167\n\tv_readfirstlane_b32 s16, v168\n\t"
      "v_readfirstlane_b32 s17, v169\n\tv_readfirstlane_b32 s18, v170\n\tv_readfirstlane_b32 s19, v171\n\tv_readfirstlane_b32 s20, v172\n\tv_readfirstlane_b32 s21, v173\n\t"
      "v_readfirstlane_b32 s22, v174\n\tv_readfirstlane_b32 s23, v175\n\tv_readfirstlane_b32 s24, v176\n\tv_readfirstlane_b32 s25, v177\n\tv_readfirstlane_b32 s26, v178\n\t"
      "v_readfirstlane_b32 s27, v179\n\tv_readfirstlane_b32 s28, v180\n\tv_readfirstlane_b32 s29, v181\n\tv_readfirstlane_b32 s30, v182\n\tv_readfirstlane_b32 s40, v192\n\t"
      "v_readfirstlane_b32 s41, v193\n\tv_readfirstlane_b32 s42, v194\n\tv_readfirstlane_b32 s43, v195\n\tv_readfirstlane_b32 s44, v196\n\tv_readfirstlane_b32 s45, v197\n\t"
      "v_readfirstlane_b32 s46, v198\n\tv_readfirstlane_b32 s47, v199\n\tv_readfirstlane_b32 s49, v201\n\tv_readfirstlane_b32 s56, v208\n\tv_readfirstlane_b32 s57, v209\n\t"
      "v_readfirstlane_b32 s60, v212\n\tv_readfirstlane_b32 s61, v213\n\tv_readfirstlane_b32 s62, v214\n\tv_readfirstlane_b32 s63, v215\n\tv_readfirstlane_b32 s64, v216\n\t"
      "v_readfirstlane_b32 s65, v217\n\tv_readfirstlane_b32 s66, v218\n\tv_readfirstlane_b32 s67, v219\n\tv_readfirstlane_b32 s68, v220\n\tv_readfirstlane_b32 s69, v221\n\t"
      "v_readfirstlane_b32 s70, v222\n\ts_nop 4\n\t"
      TKS("s_mul_i32 s48, s16, 72\n\ts_add_u32 s48, s48, 162080\n\tv_mov_b32 v224, s48\n\tv_mov_b32 v225, s74\n\tds_write_b32 v224, v225 offset:0\n\tv_mov_b32 v225, s75\n\tds_write_b32 v224, v225 offset:4\n\tv_mov_b32 v225, s76\n\tds_write_b32 v224, v225 offset:8\n\tv_mov_b32 v225, s77\n\tds_write_b32 v224, v225 offset:12\n\tv_mov_b32 v225, s78\n\tds_write_b32 v224, v225 offset:16\n\tv_mov_b32 v225, s79\n\tds_write_b32 v224, v225 offset:20\n\tv_mov_b32 v225, s80\n\tds_write_b32 v224, v225 offset:24\n\tv_mov_b32 v225, s81\n\tds_write_b32 v224, v225 offset:28\n\tv_mov_b32 v225, s82\n\tds_write_b32 v224, v225 offset:32\n\tv_mov_b32 v225, s83\n\tds_write_b32 v224, v225 offset:36\n\tv_mov_b32 v225, s84\n\tds_write_b32 v224, v225 offset:40\n\tv_mov_b32 v225, s85\n\tds_write_b32 v224, v225 offset:44\n\tv_mov_b32 v225, s86\n\tds_write_b32 v224, v225 offset:48\n\tv_mov_b32 v225, s87\n\tds_write_b32 v224, v225 offset:52\n\tv_mov_b32 v225, s88\n\tds_write_b32 v224, v225 offset:56\n\tv_mov_b32 v225, s89\n\tds_write_b32 v224, v225 offset:60\n\tv_mov_b32 v225, s90\n\tds_write_b32 v224, v225 offset:64\n\tv_mov_b32 v225, s91\n\tds_write_b32 v224, v225 offset:68\n\t")
      "s_buffer_load_dword s71, s[12:15], s69 glc\n\ts_add_u32 s50, s56, s64\n\ts_add_u32 m0, s49, 0\n\ts_add_u32 s48, s50, 0x0\n\t"
      "buffer_load_dwordx4 v14, s[40:43], s48 offen  lds\n\ts_add_u32 m0, s49, 1024\n\ts_add_u32 s48, s50, 0x8000\n\tbuffer_load_dwordx4 v15, s[40:43], s48 offen  lds\n\t"
      "s_add_u32 m0, s49, 2048\n\ts_add_u32 s48, s50, 0x10000\n\tbuffer_load_dwordx4 v16, s[40:43], s48 offen  lds\n\ts_add_u32 m0, s49, 3072\n\ts_add_u32 s48, s50, 0x18000\n\t"
      "buffer_load_dwordx4 v17, s[40:43], s48 offen  lds\n\ts_add_u32 m0, s49, 4096\n\ts_add_u32 s48, s50, 0x20000\n\tbuffer_load_dwordx4 v14, s[40:43], s48 offen  lds\n\t"
      "s_add_u32 m0, s49, 5120\n\ts_add_u32 s48, s50, 0x28000\n\tbuffer_load_dwordx4 v15, s[40:43], s48 offen  lds\n\ts_add_u32 m0, s49, 6144\n\ts_add_u32 s48, s50, 0x30000\n\t"
      "buffer_load_dwordx4 v16, s[40:43], s48 offen  lds\n\ts_add_u32 m0, s49, 7168\n\ts_add_u32 s48, s50, 0x38000\n\tbuffer_load_dwordx4 v17, s[40:43], s48 offen  lds\n\t"
      "s_waitcnt lgkmcnt(0)\n\ts_bitcmp0_b32 s68, 0\n\ts_cbranch_scc1 5f\n\ts_add_u32 s71, s71, 1\n\ts_lshl_b32 s48, s69, 4\n\ts_add_u32 s48, s48, 36864\n\t"
      "v_mov_b32 v13, s71\n\ts_mov_b64 exec, 1\n\tbuffer_store_dword v13, off, s[12:15], s48 sc1\n\ts_mov_b64 exec, -1\n\t"
      "5:\n\ts_add_u32 s50, s56, s64\n\ts_add_u32 m0, s49, 8192\n\ts_add_u32 s48, s50, 0x40000\n\tbuffer_load_dwordx4 v14, s[40:43], s48 offen  lds\n\t"
      "s_add_u32 m0, s49, 9216\n\ts_add_u32 s48, s50, 0x48000\n\tbuffer_load_dwordx4 v15, s[40:43], s48 offen  lds\n\ts_add_u32 m0, s49, 10240\n\ts_add_u32 s48, s50, 0x50000\n\t"
      "buffer_load_dwordx4 v16, s[40:43], s48 offen  lds\n\ts_add_u32 m0, s49, 11264\n\ts_add_u32 s48, s50, 0x58000\n\tbuffer_load_dwordx4 v17, s[40:43], s48 offen  lds\n\t"
      "s_add_u32 m0, s49, 12288\n\ts_add_u32 s48, s50, 0x60000\n\tbuffer_load_dwordx4 v14, s[40:43], s48 offen  lds\n\ts_add_u32 m0, s49, 13312\n\t"
      "s_add_u32 s48, s50, 0x68000\n\tbuffer_load_dwordx4 v15, s[40:43], s48 offen  lds\n\ts_add_u32 m0, s49, 14336\n\ts_add_u32 s48, s50, 0x70000\n\t"
      "buffer_load_dwordx4 v16, s[40:43], s48 offen  lds\n\ts_add_u32 m0, s49, 15360\n\ts_add_u32 s48, s50, 0x78000\n\tbuffer_load_dwordx4 v17, s[40:43], s48 offen  lds\n\t"
      "s_add_u32 s50, s56, s65\n\ts_add_u32 m0, s49, 16384\n\ts_add_u32 s48, s50, 0x0\n\tbuffer_load_dwordx4 v14, s[40:43], s48 offen  lds\n\ts_add_u32 m0, s49, 17408\n\t"
      "s_add_u32 s48, s50, 0x8000\n\tbuffer_load_dwordx4 v15, s[40:43], s48 offen  lds\n\ts_add_u32 m0, s49, 18432\n\ts_add_u32 s48, s50, 0x10000\n\t"
      "buffer_load_dwordx4 v16, s[40:43], s48 offen  lds\n\ts_add_u32 m0, s49, 19456\n\ts_add_u32 s48, s50, 0x18000\n\tbuffer_load_dwordx4 v17, s[40:43], s48 offen  lds\n\t"
      "s_add_u32 m0, s49, 20480\n\ts_add_u32 s48, s50, 0x20000\n\tbuffer_load_dwordx4 v14, s[40:43], s48 offen  lds\n\ts_add_u32 m0, s49, 21504\n\t"
      "s_add_u32 s48, s50, 0x28000\n\tbuffer_load_dwordx4 v15, s[40:43], s48 offen  lds\n\ts_add_u32 m0, s49, 22528\n\ts_add_u32 s48, s50, 0x30000\n\t"
      "buffer_load_dwordx4 v16, s[40:43], s48 offen  lds\n\ts_add_u32 m0, s49, 23552\n\ts_add_u32 s48, s50, 0x38000\n\tbuffer_load_dwordx4 v17, s[40:43], s48 offen  lds\n\t"
      "s_cmp_lt_u32 s68, 1\n\ts_cbranch_scc1 7f\n\ts_cmp_eq_u32 s68, 2\n\ts_cbranch_scc1 6f\n\t"
      "5:\n\ts_buffer_load_dword s48, s[12:15], s69 glc\n\ts_waitcnt lgkmcnt(0)\n\ts_cmp_eq_u32 s48, s71\n\ts_cbranch_scc1 6f\n\ts_cmp_lg_u32 s68, 3\n\ts_cbranch_scc1 7f\n\t"
      "s_cmp_eq_u32 s18, 0\n\ts_cbranch_scc1 6f\n\ts_sub_u32 s18, s18, 1\n\ts_branch 5b\n\t"
      "6:\n\tv_mov_b32 v13, 1\n\ts_mov_b64 exec, 1\n\tbuffer_atomic_add v13, off, s[12:15], s70\n\ts_mov_b64 exec, -1\n\ts_mov_b32 s68, 0\n\t"
      "7:\n\ts_waitcnt vmcnt(16)\n\ts_barrier\n\tds_read_b64_tr_b16 a[0:1], v20 offset:0\n\tds_read_b64_tr_b16 a[2:3], v20 offset:512\n\t"
      "ds_read_b64_tr_b16 a[8:9], v21 offset:0\n\tds_read_b64_tr_b16 a[10:11], v21 offset:512\n\tds_read_b64_tr_b16 a[48:49], v22 offset:0\n\t"
      "ds_read_b64_tr_b16 a[50:51], v22 offset:512\n\tds_read_b64_tr_b16 a[56:57], v23 offset:0\n\tds_read_b64_tr_b16 a[58:59], v23 offset:512\n\t"
      "ds_read_b64_tr_b16 a[64:65], v24 offset:0\n\tds_read_b64_tr_b16 a[66:67], v24 offset:512\n\tds_read_b64_tr_b16 v[208:209], v25 offset:0\n\t"
      "ds_read_b64_tr_b16 v[210:211], v25 offset:512\n\tds_read_b64_tr_b16 a[72:73], v26 offset:0\n\tds_read_b64_tr_b16 a[74:75], v26 offset:512\n\t"
      "ds_read_b64_tr_b16 a[80:81], v27 offset:0\n\tds_read_b64_tr_b16 a[82:83], v27 offset:512\n\ts_waitcnt lgkmcnt(0)\n\ts_add_u32 s50, s56, s65\n\t"
      "s_add_u32 m0, s49, 24576\n\ts_add_u32 s48, s50, 0x40000\n\tbuffer_load_dwordx4 v14, s[40:43], s48 offen  lds\n\ts_add_u32 m0, s49, 25600\n\t"
      "s_add_u32 s48, s50, 0x48000\n\tbuffer_load_dwordx4 v15, s[40:43], s48 offen  lds\n\ts_add_u32 m0, s49, 26624\n\ts_add_u32 s48, s50, 0x50000\n\t"
      "buffer_load_dwordx4 v16, s[40:43], s48 offen  lds\n\ts_add_u32 m0, s49, 27648\n\ts_add_u32 s48, s50, 0x58000\n\tbuffer_load_dwordx4 v17, s[40:43], s48 offen  lds\n\t"
      "s_add_u32 m0, s49, 28672\n\ts_add_u32 s48, s50, 0x60000\n\tbuffer_load_dwordx4 v14, s[40:43], s48 offen  lds\n\ts_add_u32 m0, s49, 29696\n\t"
      "s_add_u32 s48, s50, 0x68000\n\tbuffer_load_dwordx4 v15, s[40:43], s48 offen  lds\n\ts_add_u32 m0, s49, 30720\n\ts_add_u32 s48, s50, 0x70000\n\t"
      "buffer_load_dwordx4 v16, s[40:43], s48 offen  lds\n\ts_add_u32 m0, s49, 31744\n\ts_add_u32 s48, s50, 0x78000\n\tbuffer_load_dwordx4 v17, s[40:43], s48 offen  lds\n\t"
      "s_cmp_lt_u32 s68, 1\n\ts_cbranch_scc1 7f\n\ts_cmp_eq_u32 s68, 2\n\ts_cbranch_scc1 6f\n\t"
      "5:\n\ts_buffer_load_dword s48, s[12:15], s69 glc\n\ts_waitcnt lgkmcnt(0)\n\ts_cmp_eq_u32 s48, s71\n\ts_cbranch_scc0 7f\n\t"
      "6:\n\tv_mov_b32 v13, 1\n\ts_mov_b64 exec, 1\n\tbuffer_atomic_add v13, off, s[12:15], s70\n\ts_mov_b64 exec, -1\n\ts_mov_b32 s68, 0\n\t"
      "7:\n\ts_waitcnt vmcnt(16)\n\ts_barrier\n\tds_read_b64_tr_b16 a[4:5], v20 offset:8192\n\tds_read_b64_tr_b16 a[6:7], v20 offset:8704\n\t"
      "ds_read_b64_tr_b16 a[12:13], v21 offset:8192\n\tds_read_b64_tr_b16 a[14:15], v21 offset:8704\n\tds_read_b64_tr_b16 a[52:53], v22 offset:8192\n\t"
      "ds_read_b64_tr_b16 a[54:55], v22 offset:8704\n\tds_read_b64_tr_b16 a[60:61], v23 offset:8192\n\tds_read_b64_tr_b16 a[62:63], v23 offset:8704\n\t"
      "ds_read_b64_tr_b16 a[68:69], v24 offset:8192\n\tds_read_b64_tr_b16 a[70:71], v24 offset:8704\n\tds_read_b64_tr_b16 v[212:213], v25 offset:8192\n\t"
      "ds_read_b64_tr_b16 v[214:215], v25 offset:8704\n\tds_read_b64_tr_b16 a[76:77], v26 offset:8192\n\tds_read_b64_tr_b16 a[78:79], v26 offset:8704\n\t"
      "ds_read_b64_tr_b16 a[84:85], v27 offset:8192\n\tds_read_b64_tr_b16 a[86:87], v27 offset:8704\n\ts_waitcnt lgkmcnt(0)\n\ts_add_u32 s50, s56, s66\n\t"
      "s_add_u32 m0, s49, 0\n\ts_add_u32 s48, s50, 0x0\n\tbuffer_load_dwordx4 v14, s[40:43], s48 offen  lds\n\ts_add_u32 m0, s49, 1024\n\ts_add_u32 s48, s50, 0x8000\n\t"
      "buffer_load_dwordx4 v15, s[40:43], s48 offen  lds\n\ts_add_u32 m0, s49, 2048\n\ts_add_u32 s48, s50, 0x10000\n\tbuffer_load_dwordx4 v16, s[40:43], s48 offen  lds\n\t"
      "s_add_u32 m0, s49, 3072\n\ts_add_u32 s48, s50, 0x18000\n\tbuffer_load_dwordx4 v17, s[40:43], s48 offen  lds\n\ts_add_u32 m0, s49, 4096\n\ts_add_u32 s48, s50, 0x20000\n\t"
      "buffer_load_dwordx4 v14, s[40:43], s48 offen  lds\n\ts_add_u32 m0, s49, 5120\n\ts_add_u32 s48, s50, 0x28000\n\tbuffer_load_dwordx4 v15, s[40:43], s48 offen  lds\n\t"
      "s_add_u32 m0, s49, 6144\n\ts_add_u32 s48, s50, 0x30000\n\tbuffer_load_dwordx4 v16, s[40:43], s48 offen  lds\n\ts_add_u32 m0, s49, 7168\n\ts_add_u32 s48, s50, 0x38000\n\t"
      "buffer_load_dwordx4 v17, s[40:43], s48 offen  lds\n\ts_cmp_lt_u32 s68, 1\n\ts_cbranch_scc1 7f\n\ts_cmp_eq_u32 s68, 2\n\ts_cbranch_scc1 6f\n\t"
      "5:\n\ts_buffer_load_dword s48, s[12:15], s69 glc\n\ts_waitcnt lgkmcnt(0)\n\ts_cmp_eq_u32 s48, s71\n\ts_cbranch_scc0 7f\n\t"
      "6:\n\tv_mov_b32 v13, 1\n\ts_mov_b64 exec, 1\n\tbuffer_atomic_add v13, off, s[12:15], s70\n\ts_mov_b64 exec, -1\n\ts_mov_b32 s68, 0\n\t"
      "7:\n\ts_waitcnt vmcnt(16)\n\ts_barrier\n\tds_read_b64_tr_b16 a[16:17], v20 offset:16384\n\tds_read_b64_tr_b16 a[18:19], v20 offset:16896\n\t"
      "ds_read_b64_tr_b16 v[192:193], v21 offset:16384\n\tds_read_b64_tr_b16 v[194:195], v21 offset:16896\n\tds_read_b64_tr_b16 a[88:89], v22 offset:16384\n\t"
      "ds_read_b64_tr_b16 a[90:91], v22 offset:16896\n\tds_read_b64_tr_b16 v[216:217], v23 offset:16384\n\tds_read_b64_tr_b16 v[218:219], v23 offset:16896\n\t"
      "ds_read_b64_tr_b16 a[96:97], v24 offset:16384\n\tds_read_b64_tr_b16 a[98:99], v24 offset:16896\n\tds_read_b64_tr_b16 a[104:105], v25 offset:16384\n\t"
      "ds_read_b64_tr_b16 a[106:107], v25 offset:16896\n\tds_read_b64_tr_b16 a[112:113], v26 offset:16384\n\tds_read_b64_tr_b16 a[114:115], v26 offset:16896\n\t"
      "ds_read_b64_tr_b16 v[224:225], v27 offset:16384\n\tds_read_b64_tr_b16 v[226:227], v27 offset:16896\n\ts_waitcnt lgkmcnt(0)\n\ts_add_u32 s50, s56, s66\n\t"
      "s_add_u32 m0, s49, 8192\n\ts_add_u32 s48, s50, 0x40000\n\tbuffer_load_dwordx4 v14, s[40:43], s48 offen  lds\n\ts_add_u32 m0, s49, 9216\n\ts_add_u32 s48, s50, 0x48000\n\t"
      "buffer_load_dwordx4 v15, s[40:43], s48 offen  lds\n\ts_add_u32 m0, s49, 10240\n\ts_add_u32 s48, s50, 0x50000\n\tbuffer_load_dwordx4 v16, s[40:43], s48 offen  lds\n\t"
      "s_add_u32 m0, s49, 11264\n\ts_add_u32 s48, s50, 0x58000\n\tbuffer_load_dwordx4 v17, s[40:43], s48 offen  lds\n\ts_add_u32 m0, s49, 12288\n\t"
      "s_add_u32 s48, s50, 0x60000\n\tbuffer_load_dwordx4 v14, s[40:43], s48 offen  lds\n\ts_add_u32 m0, s49, 13312\n\ts_add_u32 s48, s50, 0x68000\n\t"
      "buffer_load_dwordx4 v15, s[40:43], s48 offen  lds\n\ts_add_u32 m0, s49, 14336\n\ts_add_u32 s48, s50, 0x70000\n\tbuffer_load_dwordx4 v16, s[40:43], s48 offen  lds\n\t"
      "s_add_u32 m0, s49, 15360\n\ts_add_u32 s48, s50, 0x78000\n\tbuffer_load_dwordx4 v17, s[40:43], s48 offen  lds\n\ts_cmp_lt_u32 s68, 1\n\ts_cbranch_scc1 7f\n\t"
      "s_cmp_eq_u32 s68, 2\n\ts_cbranch_scc1 6f\n\t"
      "5:\n\ts_buffer_load_dword s48, s[12:15], s69 glc\n\ts_waitcnt lgkmcnt(0)\n\ts_cmp_eq_u32 s48, s71\n\ts_cbranch_scc0 7f\n\t"
      "6:\n\tv_mov_b32 v13, 1\n\ts_mov_b64 exec, 1\n\tbuffer_atomic_add v13, off, s[12:15], s70\n\ts_mov_b64 exec, -1\n\ts_mov_b32 s68, 0\n\t"
      "7:\n\ts_waitcnt vmcnt(16)\n\ts_barrier\n\tds_read_b64_tr_b16 a[20:21], v20 offset:24576\n\tds_read_b64_tr_b16 a[22:23], v20 offset:25088\n\t"
      "ds_read_b64_tr_b16 v[196:197], v21 offset:24576\n\tds_read_b64_tr_b16 v[198:199], v21 offset:25088\n\tds_read_b64_tr_b16 a[92:93], v22 offset:24576\n\t"
      "ds_read_b64_tr_b16 a[94:95], v22 offset:25088\n\tds_read_b64_tr_b16 v[220:221], v23 offset:24576\n\tds_read_b64_tr_b16 v[222:223], v23 offset:25088\n\t"
      "ds_read_b64_tr_b16 a[100:101], v24 offset:24576\n\tds_read_b64_tr_b16 a[102:103], v24 offset:25088\n\tds_read_b64_tr_b16 a[108:109], v25 offset:24576\n\t"
      "ds_read_b64_tr_b16 a[110:111], v25 offset:25088\n\tds_read_b64_tr_b16 a[116:117], v26 offset:24576\n\tds_read_b64_tr_b16 a[118:119], v26 offset:25088\n\t"
      "ds_read_b64_tr_b16 v[228:229], v27 offset:24576\n\tds_read_b64_tr_b16 v[230:231], v27 offset:25088\n\ts_waitcnt lgkmcnt(0)\n\ts_add_u32 s50, s56, s67\n\t"
      "s_add_u32 m0, s49, 16384\n\ts_add_u32 s48, s50, 0x0\n\tbuffer_load_dwordx4 v14, s[40:43], s48 offen  lds\n\ts_add_u32 m0, s49, 17408\n\ts_add_u32 s48, s50, 0x8000\n\t"
      "buffer_load_dwordx4 v15, s[40:43], s48 offen  lds\n\ts_add_u32 m0, s49, 18432\n\ts_add_u32 s48, s50, 0x10000\n\tbuffer_load_dwordx4 v16, s[40:43], s48 offen  lds\n\t"
      "s_add_u32 m0, s49, 19456\n\ts_add_u32 s48, s50, 0x18000\n\tbuffer_load_dwordx4 v17, s[40:43], s48 offen  lds\n\ts_add_u32 m0, s49, 20480\n\t"
      "s_add_u32 s48, s50, 0x20000\n\tbuffer_load_dwordx4 v14, s[40:43], s48 offen  lds\n\ts_add_u32 m0, s49, 21504\n\ts_add_u32 s48, s50, 0x28000\n\t"
      "buffer_load_dwordx4 v15, s[40:43], s48 offen  lds\n\ts_add_u32 m0, s49, 22528\n\ts_add_u32 s48, s50, 0x30000\n\tbuffer_load_dwordx4 v16, s[40:43], s48 offen  lds\n\t"
      "s_add_u32 m0, s49, 23552\n\ts_add_u32 s48, s50, 0x38000\n\tbuffer_load_dwordx4 v17, s[40:43], s48 offen  lds\n\ts_cmp_lt_u32 s68, 1\n\ts_cbranch_scc1 7f\n\t"
      "s_cmp_eq_u32 s68, 2\n\ts_cbranch_scc1 6f\n\t"
      "5:\n\ts_buffer_load_dword s48, s[12:15], s69 glc\n\ts_waitcnt lgkmcnt(0)\n\ts_cmp_eq_u32 s48, s71\n\ts_cbranch_scc0 7f\n\t"
      "6:\n\tv_mov_b32 v13, 1\n\ts_mov_b64 exec, 1\n\tbuffer_atomic_add v13, off, s[12:15], s70\n\ts_mov_b64 exec, -1\n\ts_mov_b32 s68, 0\n\t"
      "7:\n\ts_waitcnt vmcnt(16)\n\ts_barrier\n\tds_read_b64_tr_b16 a[24:25], v20 offset:0\n\tds_read_b64_tr_b16 a[26:27], v20 offset:512\n\t"
      "ds_read_b64_tr_b16 a[32:33], v21 offset:0\n\tds_read_b64_tr_b16 a[34:35], v21 offset:512\n\tds_read_b64_tr_b16 a[120:121], v22 offset:0\n\t"
      "ds_read_b64_tr_b16 a[122:123], v22 offset:512\n\tds_read_b64_tr_b16 a[128:129], v23 offset:0\n\tds_read_b64_tr_b16 a[130:131], v23 offset:512\n\t"
      "ds_read_b64_tr_b16 a[136:137], v24 offset:0\n\tds_read_b64_tr_b16 a[138:139], v24 offset:512\n\tds_read_b64_tr_b16 v[232:233], v25 offset:0\n\t"
      "ds_read_b64_tr_b16 v[234:235], v25 offset:512\n\tds_read_b64_tr_b16 a[144:145], v26 offset:0\n\tds_read_b64_tr_b16 a[146:147], v26 offset:512\n\t"
      "ds_read_b64_tr_b16 a[152:153], v27 offset:0\n\tds_read_b64_tr_b16 a[154:155], v27 offset:512\n\ts_waitcnt lgkmcnt(0)\n\ts_add_u32 s50, s56, s67\n\t"
      "s_add_u32 m0, s49, 24576\n\ts_add_u32 s48, s50, 0x40000\n\tbuffer_load_dwordx4 v14, s[40:43], s48 offen  lds\n\ts_add_u32 m0, s49, 25600\n\t"
      "s_add_u32 s48, s50, 0x48000\n\tbuffer_load_dwordx4 v15, s[40:43], s48 offen  lds\n\ts_add_u32 m0, s49, 26624\n\ts_add_u32 s48, s50, 0x50000\n\t"
      "buffer_load_dwordx4 v16, s[40:43], s48 offen  lds\n\ts_add_u32 m0, s49, 27648\n\ts_add_u32 s48, s50, 0x58000\n\tbuffer_load_dwordx4 v17, s[40:43], s48 offen  lds\n\t"
      "s_add_u32 m0, s49, 28672\n\ts_add_u32 s48, s50, 0x60000\n\tbuffer_load_dwordx4 v14, s[40:43], s48 offen  lds\n\ts_add_u32 m0, s49, 29696\n\t"
      "s_add_u32 s48, s50, 0x68000\n\tbuffer_load_dwordx4 v15, s[40:43], s48 offen  lds\n\ts_add_u32 m0, s49, 30720\n\ts_add_u32 s48, s50, 0x70000\n\t"
      "buffer_load_dwordx4 v16, s[40:43], s48 offen  lds\n\ts_add_u32 m0, s49, 31744\n\ts_add_u32 s48, s50, 0x78000\n\tbuffer_load_dwordx4 v17, s[40:43], s48 offen  lds\n\t"
      "s_cmp_lt_u32 s68, 1\n\ts_cbranch_scc1 7f\n\ts_cmp_eq_u32 s68, 2\n\ts_cbranch_scc1 6f\n\t"
      "5:\n\ts_buffer_load_dword s48, s[12:15], s69 glc\n\ts_waitcnt lgkmcnt(0)\n\ts_cmp_eq_u32 s48, s71\n\ts_cbranch_scc0 7f\n\t"
      "6:\n\tv_mov_b32 v13, 1\n\ts_mov_b64 exec, 1\n\tbuffer_atomic_add v13, off, s[12:15], s70\n\ts_mov_b64 exec, -1\n\ts_mov_b32 s68, 0\n\t"
      "7:\n\ts_waitcnt vmcnt(16)\n\ts_barrier\n\tds_read_b64_tr_b16 a[28:29], v20 offset:8192\n\tds_read_b64_tr_b16 a[30:31], v20 offset:8704\n\t"
      "ds_read_b64_tr_b16 a[36:37], v21 offset:8192\n\tds_read_b64_tr_b16 a[38:39], v21 offset:8704\n\tds_read_b64_tr_b16 a[124:125], v22 offset:8192\n\t"
      "ds_read_b64_tr_b16 a[126:127], v22 offset:8704\n\tds_read_b64_tr_b16 a[132:133], v23 offset:8192\n\tds_read_b64_tr_b16 a[134:135], v23 offset:8704\n\t"
      "ds_read_b64_tr_b16 a[140:141], v24 offset:8192\n\tds_read_b64_tr_b16 a[142:143], v24 offset:8704\n\tds_read_b64_tr_b16 v[236:237], v25 offset:8192\n\t"
      "ds_read_b64_tr_b16 v[238:239], v25 offset:8704\n\tds_read_b64_tr_b16 a[148:149], v26 offset:8192\n\tds_read_b64_tr_b16 a[150:151], v26 offset:8704\n\t"
      "ds_read_b64_tr_b16 a[156:157], v27 offset:8192\n\tds_read_b64_tr_b16 a[158:159], v27 offset:8704\n\ts_waitcnt lgkmcnt(0)\n\ts_add_u32 m0, s49, 0\n\t"
      "s_add_u32 s48, s57, 0x0\n\tbuffer_load_dwordx4 v18, s[44:47], s48 offen  lds\n\ts_add_u32 m0, s49, 1024\n\ts_add_u32 s48, s57, 0x2000\n\t"
      "buffer_load_dwordx4 v18, s[44:47], s48 offen  lds\n\ts_add_u32 m0, s49, 2048\n\ts_add_u32 s48, s57, 0x4000\n\tbuffer_load_dwordx4 v18, s[44:47], s48 offen  lds\n\t"
      "s_add_u32 m0, s49, 3072\n\ts_add_u32 s48, s57, 0x6000\n\tbuffer_load_dwordx4 v18, s[44:47], s48 offen  lds\n\ts_add_u32 m0, s49, 4096\n\ts_add_u32 s48, s57, 0x8000\n\t"
      "buffer_load_dwordx4 v19, s[44:47], s48 offen  lds\n\ts_add_u32 m0, s49, 5120\n\ts_add_u32 s48, s57, 0xa000\n\tbuffer_load_dwordx4 v19, s[44:47], s48 offen  lds\n\t"
      "s_add_u32 m0, s49, 6144\n\ts_add_u32 s48, s57, 0xc000\n\tbuffer_load_dwordx4 v19, s[44:47], s48 offen  lds\n\ts_add_u32 m0, s49, 7168\n\ts_add_u32 s48, s57, 0xe000\n\t"
      "buffer_load_dwordx4 v19, s[44:47], s48 offen  lds\n\ts_cmp_lt_u32 s68, 1\n\ts_cbranch_scc1 7f\n\ts_cmp_eq_u32 s68, 2\n\ts_cbranch_scc1 6f\n\t"
      "5:\n\ts_buffer_load_dword s48, s[12:15], s69 glc\n\ts_waitcnt lgkmcnt(0)\n\ts_cmp_eq_u32 s48, s71\n\ts_cbranch_scc0 7f\n\t"
      "6:\n\tv_mov_b32 v13, 1\n\ts_mov_b64 exec, 1\n\tbuffer_atomic_add v13, off, s[12:15], s70\n\ts_mov_b64 exec, -1\n\ts_mov_b32 s68, 0\n\t"
      "7:\n\ts_waitcnt vmcnt(16)\n\ts_barrier\n\tds_read_b64_tr_b16 a[40:41], v20 offset:16384\n\tds_read_b64_tr_b16 a[42:43], v20 offset:16896\n\t"
      "ds_read_b64_tr_b16 v[200:201], v21 offset:16384\n\tds_read_b64_tr_b16 v[202:203], v21 offset:16896\n\tds_read_b64_tr_b16 a[160:161], v22 offset:16384\n\t"
      "ds_read_b64_tr_b16 a[162:163], v22 offset:16896\n\tds_read_b64_tr_b16 v[240:241], v23 offset:16384\n\tds_read_b64_tr_b16 v[242:243], v23 offset:16896\n\t"
      "ds_read_b64_tr_b16 a[168:169], v24 offset:16384\n\tds_read_b64_tr_b16 a[170:171], v24 offset:16896\n\tds_read_b64_tr_b16 a[176:177], v25 offset:16384\n\t"
      "ds_read_b64_tr_b16 a[178:179], v25 offset:16896\n\tds_read_b64_tr_b16 a[184:185], v26 offset:16384\n\tds_read_b64_tr_b16 a[186:187], v26 offset:16896\n\t"
      "ds_read_b64_tr_b16 v[248:249], v27 offset:16384\n\tds_read_b64_tr_b16 v[250:251], v27 offset:16896\n\ts_waitcnt lgkmcnt(0)\n\ts_add_u32 m0, s49, 8192\n\t"
      "s_add_u32 s48, s57, 0x10000\n\tbuffer_load_dwordx4 v18, s[44:47], s48 offen  lds\n\ts_add_u32 m0, s49, 9216\n\ts_add_u32 s48, s57, 0x12000\n\t"
      "buffer_load_dwordx4 v18, s[44:47], s48 offen  lds\n\ts_add_u32 m0, s49, 10240\n\ts_add_u32 s48, s57, 0x14000\n\tbuffer_load_dwordx4 v18, s[44:47], s48 offen  lds\n\t"
      "s_add_u32 m0, s49, 11264\n\ts_add_u32 s48, s57, 0x16000\n\tbuffer_load_dwordx4 v18, s[44:47], s48 offen  lds\n\ts_add_u32 m0, s49, 12288\n\t"
      "s_add_u32 s48, s57, 0x18000\n\tbuffer_load_dwordx4 v19, s[44:47], s48 offen  lds\n\ts_add_u32 m0, s49, 13312\n\ts_add_u32 s48, s57, 0x1a000\n\t"
      "buffer_load_dwordx4 v19, s[44:47], s48 offen  lds\n\ts_add_u32 m0, s49, 14336\n\ts_add_u32 s48, s57, 0x1c000\n\tbuffer_load_dwordx4 v19, s[44:47], s48 offen  lds\n\t"
      "s_add_u32 m0, s49, 15360\n\ts_add_u32 s48, s57, 0x1e000\n\tbuffer_load_dwordx4 v19, s[44:47], s48 offen  lds\n\ts_cmp_lt_u32 s68, 1\n\ts_cbranch_scc1 7f\n\t"
      "s_cmp_eq_u32 s68, 2\n\ts_cbranch_scc1 6f\n\t"
      "5:\n\ts_buffer_load_dword s48, s[12:15], s69 glc\n\ts_waitcnt lgkmcnt(0)\n\ts_cmp_eq_u32 s48, s71\n\ts_cbranch_scc0 7f\n\t"
      "6:\n\tv_mov_b32 v13, 1\n\ts_mov_b64 exec, 1\n\tbuffer_atomic_add v13, off, s[12:15], s70\n\ts_mov_b64 exec, -1\n\ts_mov_b32 s68, 0\n\t"
      "7:\n\ts_waitcnt vmcnt(16)\n\ts_barrier\n\tds_read_b64_tr_b16 a[44:45], v20 offset:24576\n\tds_read_b64_tr_b16 a[46:47], v20 offset:25088\n\t"
      "ds_read_b64_tr_b16 v[204:205], v21 offset:24576\n\tds_read_b64_tr_b16 v[206:207], v21 offset:25088\n\tds_read_b64_tr_b16 a[164:165], v22 offset:24576\n\t"
      "ds_read_b64_tr_b16 a[166:167], v22 offset:25088\n\tds_read_b64_tr_b16 v[244:245], v23 offset:24576\n\tds_read_b64_tr_b16 v[246:247], v23 offset:25088\n\t"
      "ds_read_b64_tr_b16 a[172:173], v24 offset:24576\n\tds_read_b64_tr_b16 a[174:175], v24 offset:25088\n\tds_read_b64_tr_b16 a[180:181], v25 offset:24576\n\t"
      "ds_read_b64_tr_b16 a[182:183], v25 offset:25088\n\tds_read_b64_tr_b16 a[188:189], v26 offset:24576\n\tds_read_b64_tr_b16 a[190:191], v26 offset:25088\n\t"
      "ds_read_b64_tr_b16 v[252:253], v27 offset:24576\n\tds_read_b64_tr_b16 v[254:255], v27 offset:25088\n\ts_waitcnt lgkmcnt(0)\n\ts_add_u32 m0, s49, 16384\n\t"
      "s_add_u32 s48, s57, 0x20000\n\tbuffer_load_dwordx4 v18, s[44:47], s48 offen  lds\n\ts_add_u32 m0, s49, 17408\n\ts_add_u32 s48, s57, 0x22000\n\t"
      "buffer_load_dwordx4 v18, s[44:47], s48 offen  lds\n\ts_add_u32 m0, s49, 18432\n\ts_add_u32 s48, s57, 0x24000\n\tbuffer_load_dwordx4 v18, s[44:47], s48 offen  lds\n\t"
      "s_add_u32 m0, s49, 19456\n\ts_add_u32 s48, s57, 0x26000\n\tbuffer_load_dwordx4 v18, s[44:47], s48 offen  lds\n\ts_add_u32 m0, s49, 20480\n\t"
      "s_add_u32 s48, s57, 0x28000\n\tbuffer_load_dwordx4 v19, s[44:47], s48 offen  lds\n\ts_add_u32 m0, s49, 21504\n\ts_add_u32 s48, s57, 0x2a000\n\t"
      "buffer_load_dwordx4 v19, s[44:47], s48 offen  lds\n\ts_add_u32 m0, s49, 22528\n\ts_add_u32 s48, s57, 0x2c000\n\tbuffer_load_dwordx4 v19, s[44:47], s48 offen  lds\n\t"
      "s_add_u32 m0, s49, 23552\n\ts_add_u32 s48, s57, 0x2e000\n\tbuffer_load_dwordx4 v19, s[44:47], s48 offen  lds\n\ts_cmp_lt_u32 s68, 1\n\ts_cbranch_scc1 7f\n\t"
      "s_cmp_eq_u32 s68, 2\n\ts_cbranch_scc1 6f\n\t"
      "5:\n\ts_buffer_load_dword s48, s[12:15], s69 glc\n\ts_waitcnt lgkmcnt(0)\n\ts_cmp_eq_u32 s48, s71\n\ts_cbranch_scc0 7f\n\t"
      "6:\n\tv_mov_b32 v13, 1\n\ts_mov_b64 exec, 1\n\tbuffer_atomic_add v13, off, s[12:15], s70\n\ts_mov_b64 exec, -1\n\ts_mov_b32 s68, 0\n\t"
      "7:\n\ts_waitcnt vmcnt(16)\n\ts_barrier\n\tds_read_b64_tr_b16 a[192:193], v28 offset:0\n\tds_read_b64_tr_b16 a[194:195], v28 offset:256\n\t"
      "ds_read_b64_tr_b16 a[196:197], v28 offset:2048\n\tds_read_b64_tr_b16 a[198:199], v28 offset:2304\n\tds_read_b64_tr_b16 a[224:225], v29 offset:0\n\t"
      "ds_read_b64_tr_b16 a[226:227], v29 offset:256\n\tds_read_b64_tr_b16 a[228:229], v29 offset:2048\n\tds_read_b64_tr_b16 a[230:231], v29 offset:2304\n\t"
      "ds_read_b64_tr_b16 v[56:57], v30 offset:0\n\tds_read_b64_tr_b16 v[58:59], v30 offset:256\n\tds_read_b64_tr_b16 v[60:61], v30 offset:2048\n\t"
      "ds_read_b64_tr_b16 v[62:63], v30 offset:2304\n\tds_read_b64_tr_b16 v[160:161], v31 offset:0\n\tds_read_b64_tr_b16 v[162:163], v31 offset:256\n\t"
      "ds_read_b64_tr_b16 v[164:165], v31 offset:2048\n\tds_read_b64_tr_b16 v[166:167], v31 offset:2304\n\ts_waitcnt lgkmcnt(0)\n\ts_add_u32 m0, s49, 24576\n\t"
      "s_add_u32 s48, s57, 0x30000\n\tbuffer_load_dwordx4 v18, s[44:47], s48 offen  lds\n\ts_add_u32 m0, s49, 25600\n\ts_add_u32 s48, s57, 0x32000\n\t"
      "buffer_load_dwordx4 v18, s[44:47], s48 offen  lds\n\ts_add_u32 m0, s49, 26624\n\ts_add_u32 s48, s57, 0x34000\n\tbuffer_load_dwordx4 v18, s[44:47], s48 offen  lds\n\t"
      "s_add_u32 m0, s49, 27648\n\ts_add_u32 s48, s57, 0x36000\n\tbuffer_load_dwordx4 v18, s[44:47], s48 offen  lds\n\ts_add_u32 m0, s49, 28672\n\t"
      "s_add_u32 s48, s57, 0x38000\n\tbuffer_load_dwordx4 v19, s[44:47], s48 offen  lds\n\ts_add_u32 m0, s49, 29696\n\ts_add_u32 s48, s57, 0x3a000\n\t"
      "buffer_load_dwordx4 v19, s[44:47], s48 offen  lds\n\ts_add_u32 m0, s49, 30720\n\ts_add_u32 s48, s57, 0x3c000\n\tbuffer_load_dwordx4 v19, s[44:47], s48 offen  lds\n\t"
      "s_add_u32 m0, s49, 31744\n\ts_add_u32 s48, s57, 0x3e000\n\tbuffer_load_dwordx4 v19, s[44:47], s48 offen  lds\n\tv_xor_b32 v18, 0x100, v18\n\tv_xor_b32 v19, 0x100, v19\n\t"
      "s_cmp_lt_u32 s68, 1\n\ts_cbranch_scc1 7f\n\ts_cmp_eq_u32 s68, 2\n\ts_cbranch_scc1 6f\n\t"
      "5:\n\ts_buffer_load_dword s48, s[12:15], s69 glc\n\ts_waitcnt lgkmcnt(0)\n\ts_cmp_eq_u32 s48, s71\n\ts_cbranch_scc0 7f\n\t"
      "6:\n\tv_mov_b32 v13, 1\n\ts_mov_b64 exec, 1\n\tbuffer_atomic_add v13, off, s[12:15], s70\n\ts_mov_b64 exec, -1\n\ts_mov_b32 s68, 0\n\t"
      "7:\n\ts_waitcnt vmcnt(16)\n\tds_read_b64_tr_b16 a[200:201], v28 offset:8192\n\tds_read_b64_tr_b16 a[202:203], v28 offset:8448\n\t"
      "ds_read_b64_tr_b16 a[204:205], v28 offset:10240\n\tds_read_b64_tr_b16 a[206:207], v28 offset:10496\n\tds_read_b64_tr_b16 a[232:233], v29 offset:8192\n\t"
      "ds_read_b64_tr_b16 a[234:235], v29 offset:8448\n\tds_read_b64_tr_b16 a[236:237], v29 offset:10240\n\tds_read_b64_tr_b16 a[238:239], v29 offset:10496\n\t"
      "ds_read_b64_tr_b16 v[88:89], v30 offset:8192\n\tds_read_b64_tr_b16 v[90:91], v30 offset:8448\n\tds_read_b64_tr_b16 v[92:93], v30 offset:10240\n\t"
      "ds_read_b64_tr_b16 v[94:95], v30 offset:10496\n\tds_read_b64_tr_b16 v[168:169], v31 offset:8192\n\tds_read_b64_tr_b16 v[170:171], v31 offset:8448\n\t"
      "ds_read_b64_tr_b16 v[172:173], v31 offset:10240\n\tds_read_b64_tr_b16 v[174:175], v31 offset:10496\n\ts_waitcnt lgkmcnt(0)\n\ts_add_u32 m0, s49, 0\n\t"
      "s_add_u32 s48, s57, 0x0\n\tbuffer_load_dwordx4 v18, s[44:47], s48 offen  lds\n\ts_add_u32 m0, s49, 1024\n\ts_add_u32 s48, s57, 0x2000\n\t"
      "buffer_load_dwordx4 v18, s[44:47], s48 offen  lds\n\ts_add_u32 m0, s49, 2048\n\ts_add_u32 s48, s57, 0x4000\n\tbuffer_load_dwordx4 v18, s[44:47], s48 offen  lds\n\t"
      "s_add_u32 m0, s49, 3072\n\ts_add_u32 s48, s57, 0x6000\n\tbuffer_load_dwordx4 v18, s[44:47], s48 offen  lds\n\ts_add_u32 m0, s49, 4096\n\ts_add_u32 s48, s57, 0x8000\n\t"
      "buffer_load_dwordx4 v19, s[44:47], s48 offen  lds\n\ts_add_u32 m0, s49, 5120\n\ts_add_u32 s48, s57, 0xa000\n\tbuffer_load_dwordx4 v19, s[44:47], s48 offen  lds\n\t"
      "s_add_u32 m0, s49, 6144\n\ts_add_u32 s48, s57, 0xc000\n\tbuffer_load_dwordx4 v19, s[44:47], s48 offen  lds\n\ts_add_u32 m0, s49, 7168\n\ts_add_u32 s48, s57, 0xe000\n\t"
      "buffer_load_dwordx4 v19, s[44:47], s48 offen  lds\n\ts_waitcnt vmcnt(16)\n\tds_read_b64_tr_b16 a[208:209], v28 offset:16384\n\t"
      "ds_read_b64_tr_b16 a[210:211], v28 offset:16640\n\tds_read_b64_tr_b16 a[212:213], v28 offset:18432\n\tds_read_b64_tr_b16 a[214:215], v28 offset:18688\n\t"
      "ds_read_b64_tr_b16 a[240:241], v29 offset:16384\n\tds_read_b64_tr_b16 a[242:243], v29 offset:16640\n\tds_read_b64_tr_b16 a[244:245], v29 offset:18432\n\t"
      "ds_read_b64_tr_b16 a[246:247], v29 offset:18688\n\tds_read_b64_tr_b16 v[120:121], v30 offset:16384\n\tds_read_b64_tr_b16 v[122:123], v30 offset:16640\n\t"
      "ds_read_b64_tr_b16 v[124:125], v30 offset:18432\n\tds_read_b64_tr_b16 v[126:127], v30 offset:18688\n\tds_read_b64_tr_b16 v[176:177], v31 offset:16384\n\t"
      "ds_read_b64_tr_b16 v[178:179], v31 offset:16640\n\tds_read_b64_tr_b16 v[180:181], v31 offset:18432\n\tds_read_b64_tr_b16 v[182:183], v31 offset:18688\n\t"
      "s_waitcnt lgkmcnt(0)\n\ts_add_u32 m0, s49, 8192\n\ts_add_u32 s48, s57, 0x10000\n\tbuffer_load_dwordx4 v18, s[44:47], s48 offen  lds\n\ts_add_u32 m0, s49, 9216\n\t"
      "s_add_u32 s48, s57, 0x12000\n\tbuffer_load_dwordx4 v18, s[44:47], s48 offen  lds\n\ts_add_u32 m0, s49, 10240\n\ts_add_u32 s48, s57, 0x14000\n\t"
      "buffer_load_dwordx4 v18, s[44:47], s48 offen  lds\n\ts_add_u32 m0, s49, 11264\n\ts_add_u32 s48, s57, 0x16000\n\tbuffer_load_dwordx4 v18, s[44:47], s48 offen  lds\n\t"
      "s_add_u32 m0, s49, 12288\n\ts_add_u32 s48, s57, 0x18000\n\tbuffer_load_dwordx4 v19, s[44:47], s48 offen  lds\n\ts_add_u32 m0, s49, 13312\n\t"
      "s_add_u32 s48, s57, 0x1a000\n\tbuffer_load_dwordx4 v19, s[44:47], s48 offen  lds\n\ts_add_u32 m0, s49, 14336\n\ts_add_u32 s48, s57, 0x1c000\n\t"
      "buffer_load_dwordx4 v19, s[44:47], s48 offen  lds\n\ts_add_u32 m0, s49, 15360\n\ts_add_u32 s48, s57, 0x1e000\n\tbuffer_load_dwordx4 v19, s[44:47], s48 offen  lds\n\t"
      "s_waitcnt vmcnt(16)\n\tds_read_b64_tr_b16 a[216:217], v28 offset:24576\n\tds_read_b64_tr_b16 a[218:219], v28 offset:24832\n\t"
      "ds_read_b64_tr_b16 a[220:221], v28 offset:26624\n\tds_read_b64_tr_b16 a[222:223], v28 offset:26880\n\tds_read_b64_tr_b16 a[248:249], v29 offset:24576\n\t"
      "ds_read_b64_tr_b16 a[250:251], v29 offset:24832\n\tds_read_b64_tr_b16 a[252:253], v29 offset:26624\n\tds_read_b64_tr_b16 a[254:255], v29 offset:26880\n\t"
      "ds_read_b64_tr_b16 v[152:153], v30 offset:24576\n\tds_read_b64_tr_b16 v[154:155], v30 offset:24832\n\tds_read_b64_tr_b16 v[156:157], v30 offset:26624\n\t"
      "ds_read_b64_tr_b16 v[158:159], v30 offset:26880\n\tds_read_b64_tr_b16 v[184:185], v31 offset:24576\n\tds_read_b64_tr_b16 v[186:187], v31 offset:24832\n\t"
      "ds_read_b64_tr_b16 v[188:189], v31 offset:26624\n\tds_read_b64_tr_b16 v[190:191], v31 offset:26880\n\ts_waitcnt lgkmcnt(0)\n\ts_add_u32 m0, s49, 16384\n\t"
      "s_add_u32 s48, s57, 0x20000\n\tbuffer_load_dwordx4 v18, s[44:47], s48 offen  lds\n\ts_add_u32 m0, s49, 17408\n\ts_add_u32 s48, s57, 0x22000\n\t"
      "buffer_load_dwordx4 v18, s[44:47], s48 offen  lds\n\ts_add_u32 m0, s49, 18432\n\ts_add_u32 s48, s57, 0x24000\n\tbuffer_load_dwordx4 v18, s[44:47], s48 offen  lds\n\t"
      "s_add_u32 m0, s49, 19456\n\ts_add_u32 s48, s57, 0x26000\n\tbuffer_load_dwordx4 v18, s[44:47], s48 offen  lds\n\ts_add_u32 m0, s49, 20480\n\t"
      "s_add_u32 s48, s57, 0x28000\n\tbuffer_load_dwordx4 v19, s[44:47], s48 offen  lds\n\ts_add_u32 m0, s49, 21504\n\ts_add_u32 s48, s57, 0x2a000\n\t"
      "buffer_load_dwordx4 v19, s[44:47], s48 offen  lds\n\ts_add_u32 m0, s49, 22528\n\ts_add_u32 s48, s57, 0x2c000\n\tbuffer_load_dwordx4 v19, s[44:47], s48 offen  lds\n\t"
      "s_add_u32 m0, s49, 23552\n\ts_add_u32 s48, s57, 0x2e000\n\tbuffer_load_dwordx4 v19, s[44:47], s48 offen  lds\n\ts_waitcnt vmcnt(16)\n\t"
      "ds_read_b64_tr_b16 v[32:33], v28 offset:0\n\tds_read_b64_tr_b16 v[34:35], v28 offset:256\n\tds_read_b64_tr_b16 v[36:37], v28 offset:2048\n\t"
      "ds_read_b64_tr_b16 v[38:39], v28 offset:2304\n\tds_read_b64_tr_b16 v[64:65], v29 offset:0\n\tds_read_b64_tr_b16 v[66:67], v29 offset:256\n\t"
      "ds_read_b64_tr_b16 v[68:69], v29 offset:2048\n\tds_read_b64_tr_b16 v[70:71], v29 offset:2304\n\tds_read_b64_tr_b16 v[96:97], v30 offset:0\n\t"
      "ds_read_b64_tr_b16 v[98:99], v30 offset:256\n\tds_read_b64_tr_b16 v[100:101], v30 offset:2048\n\tds_read_b64_tr_b16 v[102:103], v30 offset:2304\n\t"
      "ds_read_b64_tr_b16 v[128:129], v31 offset:0\n\tds_read_b64_tr_b16 v[130:131], v31 offset:256\n\tds_read_b64_tr_b16 v[132:133], v31 offset:2048\n\t"
      "ds_read_b64_tr_b16 v[134:135], v31 offset:2304\n\ts_waitcnt lgkmcnt(0)\n\tds_write_b128 v5, v[56:59] offset:0\n\tds_write_b128 v5, v[60:63] offset:1024\n\t"
      "ds_write_b128 v5, v[88:91] offset:2048\n\tds_write_b128 v5, v[92:95] offset:3072\n\tds_write_b128 v5, v[120:123] offset:4096\n\t"
      "ds_write_b128 v5, v[124:127] offset:5120\n\tds_write_b128 v5, v[152:155] offset:6144\n\tds_write_b128 v5, v[156:159] offset:7168\n\ts_add_u32 m0, s49, 24576\n\t"
      "s_add_u32 s48, s57, 0x30000\n\tbuffer_load_dwordx4 v18, s[44:47], s48 offen  lds\n\ts_add_u32 m0, s49, 25600\n\ts_add_u32 s48, s57, 0x32000\n\t"
      "buffer_load_dwordx4 v18, s[44:47], s48 offen  lds\n\ts_add_u32 m0, s49, 26624\n\ts_add_u32 s48, s57, 0x34000\n\tbuffer_load_dwordx4 v18, s[44:47], s48 offen  lds\n\t"
      "s_add_u32 m0, s49, 27648\n\ts_add_u32 s48, s57, 0x36000\n\tbuffer_load_dwordx4 v18, s[44:47], s48 offen  lds\n\ts_add_u32 m0, s49, 28672\n\t"
      "s_add_u32 s48, s57, 0x38000\n\tbuffer_load_dwordx4 v19, s[44:47], s48 offen  lds\n\ts_add_u32 m0, s49, 29696\n\ts_add_u32 s48, s57, 0x3a000\n\t"
      "buffer_load_dwordx4 v19, s[44:47], s48 offen  lds\n\ts_add_u32 m0, s49, 30720\n\ts_add_u32 s48, s57, 0x3c000\n\tbuffer_load_dwordx4 v19, s[44:47], s48 offen  lds\n\t"
      "s_add_u32 m0, s49, 31744\n\ts_add_u32 s48, s57, 0x3e000\n\tbuffer_load_dwordx4 v19, s[44:47], s48 offen  lds\n\ts_waitcnt vmcnt(16)\n\t"
      "ds_read_b64_tr_b16 v[40:41], v28 offset:8192\n\tds_read_b64_tr_b16 v[42:43], v28 offset:8448\n\tds_read_b64_tr_b16 v[44:45], v28 offset:10240\n\t"
      "ds_read_b64_tr_b16 v[46:47], v28 offset:10496\n\tds_read_b64_tr_b16 v[72:73], v29 offset:8192\n\tds_read_b64_tr_b16 v[74:75], v29 offset:8448\n\t"
      "ds_read_b64_tr_b16 v[76:77], v29 offset:10240\n\tds_read_b64_tr_b16 v[78:79], v29 offset:10496\n\tds_read_b64_tr_b16 v[104:105], v30 offset:8192\n\t"
      "ds_read_b64_tr_b16 v[106:107], v30 offset:8448\n\tds_read_b64_tr_b16 v[108:109], v30 offset:10240\n\tds_read_b64_tr_b16 v[110:111], v30 offset:10496\n\t"
      "ds_read_b64_tr_b16 v[136:137], v31 offset:8192\n\tds_read_b64_tr_b16 v[138:139], v31 offset:8448\n\tds_read_b64_tr_b16 v[140:141], v31 offset:10240\n\t"
      "ds_read_b64_tr_b16 v[142:143], v31 offset:10496\n\ts_waitcnt lgkmcnt(0)\n\tds_write_b128 v5, v[160:163] offset:8192\n\tds_write_b128 v5, v[164:167] offset:9216\n\t"
      "ds_write_b128 v5, v[168:171] offset:10240\n\tds_write_b128 v5, v[172:175] offset:11264\n\tds_write_b128 v5, v[176:179] offset:12288\n\t"
      "ds_write_b128 v5, v[180:183] offset:13312\n\tds_write_b128 v5, v[184:187] offset:14336\n\tds_write_b128 v5, v[188:191] offset:15360\n\ts_waitcnt vmcnt(8)\n\t"
      "ds_read_b64_tr_b16 v[48:49], v28 offset:16384\n\tds_read_b64_tr_b16 v[50:51], v28 offset:16640\n\tds_read_b64_tr_b16 v[52:53], v28 offset:18432\n\t"
      "ds_read_b64_tr_b16 v[54:55], v28 offset:18688\n\tds_read_b64_tr_b16 v[80:81], v29 offset:16384\n\tds_read_b64_tr_b16 v[82:83], v29 offset:16640\n\t"
      "ds_read_b64_tr_b16 v[84:85], v29 offset:18432\n\tds_read_b64_tr_b16 v[86:87], v29 offset:18688\n\tds_read_b64_tr_b16 v[112:113], v30 offset:16384\n\t"
      "ds_read_b64_tr_b16 v[114:115], v30 offset:16640\n\tds_read_b64_tr_b16 v[116:117], v30 offset:18432\n\tds_read_b64_tr_b16 v[118:119], v30 offset:18688\n\t"
      "ds_read_b64_tr_b16 v[144:145], v31 offset:16384\n\tds_read_b64_tr_b16 v[146:147], v31 offset:16640\n\tds_read_b64_tr_b16 v[148:149], v31 offset:18432\n\t"
      "ds_read_b64_tr_b16 v[150:151], v31 offset:18688\n\ts_waitcnt lgkmcnt(0)\n\tds_write_b128 v5, v[192:195] offset:16384\n\tds_write_b128 v5, v[196:199] offset:17408\n\t"
      "ds_write_b128 v5, v[200:203] offset:18432\n\tds_write_b128 v5, v[204:207] offset:19456\n\tds_write_b128 v5, v[208:211] offset:20480\n\t"
      "ds_write_b128 v5, v[212:215] offset:21504\n\tds_write_b128 v5, v[216:219] offset:22528\n\tds_write_b128 v5, v[220:223] offset:23552\n\ts_cmp_lt_u32 s68, 1\n\t"
      "s_cbranch_scc1 7f\n\ts_cmp_eq_u32 s68, 2\n\ts_cbranch_scc1 6f\n\t"
      "5:\n\ts_buffer_load_dword s48, s[12:15], s69 glc\n\ts_waitcnt lgkmcnt(0)\n\ts_cmp_eq_u32 s48, s71\n\ts_cbranch_scc1 6f\n\ts_cmp_eq_u32 s18, 0\n\ts_cbranch_scc1 6f\n\t"
      "s_sub_u32 s18, s18, 1\n\ts_branch 5b\n\t"
      "6:\n\tv_mov_b32 v13, 1\n\ts_mov_b64 exec, 1\n\tbuffer_atomic_add v13, off, s[12:15], s70\n\ts_mov_b64 exec, -1\n\ts_mov_b32 s68, 0\n\t"
      "7:\n\ts_waitcnt vmcnt(0)\n\tds_read_b64_tr_b16 v[56:57], v28 offset:24576\n\tds_read_b64_tr_b16 v[58:59], v28 offset:24832\n\t"
      "ds_read_b64_tr_b16 v[60:61], v28 offset:26624\n\tds_read_b64_tr_b16 v[62:63], v28 offset:26880\n\tds_read_b64_tr_b16 v[88:89], v29 offset:24576\n\t"
      "ds_read_b64_tr_b16 v[90:91], v29 offset:24832\n\tds_read_b64_tr_b16 v[92:93], v29 offset:26624\n\tds_read_b64_tr_b16 v[94:95], v29 offset:26880\n\t"
      "ds_read_b64_tr_b16 v[120:121], v30 offset:24576\n\tds_read_b64_tr_b16 v[122:123], v30 offset:24832\n\tds_read_b64_tr_b16 v[124:125], v30 offset:26624\n\t"
      "ds_read_b64_tr_b16 v[126:127], v30 offset:26880\n\tds_read_b64_tr_b16 v[152:153], v31 offset:24576\n\tds_read_b64_tr_b16 v[154:155], v31 offset:24832\n\t"
      "ds_read_b64_tr_b16 v[156:157], v31 offset:26624\n\tds_read_b64_tr_b16 v[158:159], v31 offset:26880\n\ts_waitcnt lgkmcnt(0)\n\t"
      "ds_write_b128 v5, v[224:227] offset:24576\n\tds_write_b128 v5, v[228:231] offset:25600\n\tds_write_b128 v5, v[232:235] offset:26624\n\t"
      "ds_write_b128 v5, v[236:239] offset:27648\n\tds_write_b128 v5, v[240:243] offset:28672\n\tds_write_b128 v5, v[244:247] offset:29696\n\t"
      "ds_write_b128 v5, v[248:251] offset:30720\n\tds_write_b128 v5, v[252:255] offset:31744\n\t"
      "v_mov_b64 v[30:31], 0\n\tv_mov_b32 v29, 160000\n\tds_write_b64 v29, v[30:31]\n\t"
      TKS("s_lshr_b32 s91, s62, 3\n\ts_and_b32 s91, s91, 3\n\t")
      "s_waitcnt lgkmcnt(0)\n\ts_barrier\n\t"
      STS("s_memrealtime vcc\n\ts_waitcnt lgkmcnt(0)\n\tv_mov_b32 v31, vcc_lo\n\tv_mov_b32 v30, 161024\n\tds_write_b32 v30, v31\n\t")
      "1:\n\tbuffer_load_dword v29, off, s[12:15], s63 sc1\n\ts_waitcnt vmcnt(0)\n\tv_readfirstlane_b32 s49, v29\n\ts_bitcmp1_b32 s49, 0\n\ts_cbranch_scc1 2f\n\t"
      "s_cmp_eq_u32 s18, 0\n\ts_cbranch_scc1 2f\n\ts_sub_u32 s18, s18, 1\n\ts_sleep 2\n\ts_branch 1b\n\t"
      "2:\n\ts_and_b32 s48, s49, 3\n\ts_cmp_lg_u32 s48, 3\n\ts_cbranch_scc1 4f\n\ts_cmp_eq_u32 s62, 0\n\ts_cbranch_scc1 4f\n\t"
      "s_waitcnt lgkmcnt(0)\n\ts_barrier\n\ts_mov_b64 s[0:1], s[60:61]\n\ts_mov_b32 s2, s62\n\ts_lshl_b32 s48, s16, 6\n\tv_mbcnt_lo_u32_b32 v0, -1, 0\n\t"
      "v_mbcnt_hi_u32_b32 v0, -1, v0\n\tv_add_u32 v0, s48, v0\n\ts_getpc_b64 s[4:5]\n\t"
      "3:\n\ts_sub_u32 s4, s4, 3b-mk_mega\n\ts_subb_u32 s5, s5, 0\n\ts_setpc_b64 s[4:5]\n\t"
      "4:\n\ts_memrealtime s[46:47]\n\ts_waitcnt lgkmcnt(0)\n\ts_lshl_b32 s19, s46, 2\n\ts_sub_u32 s19, s19, 0x100000\n\ts_mov_b32 s62, 470\n\ts_mov_b32 s63, 0\n\t"
      "s_lshr_b32 s64, s64, 9\n\ts_lshr_b32 s65, s65, 9\n\ts_lshr_b32 s66, s66, 9\n\ts_lshr_b32 s67, s67, 9\n\ts_lshl_b32 s48, s49, 10\n\ts_and_b32 s48, s48, 0x2000\n\t"
      "s_mov_b64 s[72:73], s[24:25]\n\tv_or_b32 v0, s48, v0\n\tv_or_b32 v11, s48, v11\n\ts_lshl_b32 s48, s49, 11\n\ts_and_b32 s48, s48, 0x2000\n\tv_or_b32 v9, s48, v9\n\t"
      "10:\n\tds_read_b128 v[160:163], v5 offset:16384\n\tds_read_b128 v[164:167], v5 offset:17408\n\tds_read_b128 v[168:171], v5 offset:18432\n\t"
      "ds_read_b128 v[172:175], v5 offset:19456\n\tds_read_b128 v[176:179], v5 offset:20480\n\tds_read_b128 v[180:183], v5 offset:21504\n\t"
      "ds_read_b128 v[184:187], v5 offset:22528\n\tds_read_b128 v[188:191], v5 offset:23552\n\tds_read_b128 v[192:195], v5 offset:24576\n\t"
      "ds_read_b128 v[196:199], v5 offset:25600\n\tds_read_b128 v[200:203], v5 offset:26624\n\tds_read_b128 v[204:207], v5 offset:27648\n\t"
      "ds_read_b128 v[208:211], v5 offset:28672\n\tds_read_b128 v[212:215], v5 offset:29696\n\tds_read_b128 v[216:219], v5 offset:30720\n\t"
      "ds_read_b128 v[220:223], v5 offset:31744\n\tv_mov_b64 v[16:17], 0\n\tv_mov_b64 v[18:19], 0\n\tv_mov_b64 v[20:21], 0\n\tv_mov_b64 v[22:23], 0\n\ts_and_b32 s48, s17, 3\n\t"
      "s_lshl_b32 s40, s48, 18\n\ts_add_u32 s40, s40, 0x200000\n\ts_lshl_b32 s41, s48, 18\n\ts_add_u32 s41, s41, 0x208000\n\ts_xor_b32 s48, s48, 2\n\ts_lshl_b32 s42, s48, 18\n\t"
      "s_add_u32 s42, s42, 0x208000\n\ts_add_u32 s68, s40, s64\n\ts_add_u32 s69, s40, s65\n\ts_add_u32 s70, s40, s66\n\ts_add_u32 s71, s40, s67\n\ts_and_b32 s43, s17, 1\n\t"
      "s_lshl_b32 s43, s43, 20\n\ts_add_u32 s44, s43, s27\n\ts_waitcnt lgkmcnt(0)\n\ts_cmp_lg_u32 s16, 1\n\ts_cbranch_scc1 73f\n\t"
      "71:\n\ts_memrealtime vcc\n\ts_waitcnt lgkmcnt(0)\n\ts_lshl_b32 vcc_lo, vcc_lo, 2\n\t"
      "70:\n\ts_sub_u32 s48, s19, vcc_lo\n\ts_cmp_gt_u32 s48, 4000\n\ts_cbranch_scc1 73f\n\ts_cmp_gt_u32 s48, 28\n\ts_cbranch_scc1 71b\n\ts_mul_i32 s48, s48, 45\n\t"
      "s_lshr_b32 s48, s48, 5\n\ts_lshl_b32 s48, s48, 2\n\ts_getpc_b64 vcc\n\t"
      "72:\n\ts_sub_u32 s48, 73f-72b, s48\n\ts_add_u32 vcc_lo, vcc_lo, s48\n\ts_addc_u32 vcc_hi, vcc_hi, 0\n\ts_setpc_b64 vcc\n\t.rept 40\n\ts_nop 0\n\t.endr\n\t"
      "73:\n\ts_barrier\n\t"
      TK0
      "buffer_load_dwordx4 v[240:243], v0, s[8:11], s68 offen sc1\n\ts_nop 11\n\tbuffer_load_dwordx4 v[244:247], v0, s[8:11], s69 offen sc1\n\ts_nop 11\n\t"
      "buffer_load_dwordx4 v[248:251], v0, s[8:11], s70 offen sc1\n\ts_nop 11\n\tbuffer_load_dwordx4 v[252:255], v0, s[8:11], s71 offen sc1\n\t"
      "11:\n\ts_waitcnt vmcnt(3)\n\t"
      TK1
      "ds_write_b128 v1, v[240:243] offset:0\n\tv_max3_u32 v13, v240, v241, v242\n\tv_max_u32 v13, v13, v243\n\tv_cmp_eq_u32 vcc, -1, v13\n\t"
      "v_smfmac_f32_16x16x64_bf16 v[16:19], v[240:243], a[0:7], v6\n\tv_smfmac_f32_16x16x64_bf16 v[20:23], v[240:243], a[8:15], v6\n\ts_cbranch_vccnz 86f\n\t"
      "20:\n\ts_waitcnt lgkmcnt(0)\n\ts_barrier\n\tds_read_b128 v[24:27], v2 offset:0\n\tds_read_b128 v[28:31], v3 offset:0\n\tds_read_b128 v[224:227], v4 offset:0\n\t"
      "s_waitcnt vmcnt(2)\n\tv_smfmac_f32_16x16x64_bf16 v[16:19], v[244:247], a[16:23], v6\n\tds_write_b128 v1, v[244:247] offset:1024\n\tv_max3_u32 v13, v244, v245, v246\n\t"
      "v_smfmac_f32_16x16x64_bf16 v[20:23], v[244:247], v[160:167], v6\n\ts_waitcnt vmcnt(1)\n\tv_max_u32 v13, v13, v247\n\t"
      "v_smfmac_f32_16x16x64_bf16 v[16:19], v[248:251], a[24:31], v6\n\tv_cmp_eq_u32 vcc, -1, v13\n\tv_smfmac_f32_16x16x64_bf16 v[20:23], v[248:251], a[32:39], v6\n\t"
      "s_waitcnt lgkmcnt(3)\n\tv_max3_u32 v13, v248, v249, v250\n\tv_smfmac_f32_16x16x64_bf16 v[16:19], v[24:27], a[48:55], v6\n\ts_cbranch_vccnz 87f\n\t"
      "21:\n\tv_max_u32 v13, v13, v251\n\tv_smfmac_f32_16x16x64_bf16 v[20:23], v[24:27], a[56:63], v6\n\ts_waitcnt lgkmcnt(2)\n\ts_waitcnt lgkmcnt(0)\n\ts_barrier\n\t"
      "s_waitcnt vmcnt(0)\n\tv_smfmac_f32_16x16x64_bf16 v[16:19], v[252:255], a[40:47], v6\n\tds_read_b128 v[228:231], v2 offset:1024\n\t"
      "ds_read_b128 v[232:235], v3 offset:1024\n\tv_max3_u32 v13, v13, v252, v253\n\tv_smfmac_f32_16x16x64_bf16 v[20:23], v[252:255], v[168:175], v6\n\t"
      "ds_read_b128 v[236:239], v4 offset:1024\n\tv_max3_u32 v13, v13, v254, v255\n\tv_smfmac_f32_16x16x64_bf16 v[16:19], v[28:31], a[64:71], v6\n\t"
      "ds_write_b128 v1, v[248:251] offset:2048\n\tv_smfmac_f32_16x16x64_bf16 v[20:23], v[28:31], v[176:183], v6\n\tv_cmp_eq_u32 vcc, -1, v13\n\t"
      TK2
      "v_smfmac_f32_16x16x64_bf16 v[16:19], v[224:227], a[72:79], v6\n\tv_smfmac_f32_16x16x64_bf16 v[20:23], v[224:227], a[80:87], v6\n\ts_waitcnt lgkmcnt(3)\n\t"
      "ds_write_b128 v1, v[252:255] offset:3072\n\tv_smfmac_f32_16x16x64_bf16 v[16:19], v[228:231], a[88:95], v6\n\ts_cbranch_vccnz 88f\n\t"
      "22:\n\tv_smfmac_f32_16x16x64_bf16 v[20:23], v[228:231], v[184:191], v6\n\ts_waitcnt lgkmcnt(3)\n\tv_smfmac_f32_16x16x64_bf16 v[16:19], v[232:235], a[96:103], v6\n\t"
      "v_smfmac_f32_16x16x64_bf16 v[20:23], v[232:235], a[104:111], v6\n\ts_waitcnt lgkmcnt(2)\n\ts_waitcnt lgkmcnt(0)\n\ts_barrier\n\t"
      "ds_read_b128 v[240:243], v2 offset:2048\n\t"
      TK3
      "v_smfmac_f32_16x16x64_bf16 v[16:19], v[236:239], a[112:119], v6\n\tds_read_b128 v[244:247], v3 offset:2048\n\tds_read_b128 v[248:251], v4 offset:2048\n\t"
      "v_smfmac_f32_16x16x64_bf16 v[20:23], v[236:239], v[192:199], v6\n\tds_read_b128 v[24:27], v2 offset:3072\n\ts_waitcnt lgkmcnt(3)\n\t"
      "v_smfmac_f32_16x16x64_bf16 v[16:19], v[240:243], a[120:127], v6\n\tds_read_b128 v[28:31], v3 offset:3072\n\tds_read_b128 v[224:227], v4 offset:3072\n\t"
      "v_smfmac_f32_16x16x64_bf16 v[20:23], v[240:243], a[128:135], v6\n\ts_waitcnt lgkmcnt(4)\n\tds_read_b128 v[160:163], v5 offset:0\n\t"
      "v_smfmac_f32_16x16x64_bf16 v[16:19], v[244:247], a[136:143], v6\n\tds_read_b128 v[164:167], v5 offset:1024\n\tds_read_b128 v[168:171], v5 offset:2048\n\t"
      "v_smfmac_f32_16x16x64_bf16 v[20:23], v[244:247], v[200:207], v6\n\ts_waitcnt lgkmcnt(6)\n\tds_read_b128 v[172:175], v5 offset:3072\n\t"
      "v_smfmac_f32_16x16x64_bf16 v[16:19], v[248:251], a[144:151], v6\n\tds_read_b128 v[176:179], v5 offset:4096\n\tds_read_b128 v[180:183], v5 offset:5120\n\t"
      "v_smfmac_f32_16x16x64_bf16 v[20:23], v[248:251], a[152:159], v6\n\ts_waitcnt lgkmcnt(8)\n\tds_read_b128 v[184:187], v5 offset:6144\n\t"
      "v_smfmac_f32_16x16x64_bf16 v[16:19], v[24:27], a[160:167], v6\n\tds_read_b128 v[188:191], v5 offset:7168\n\tds_read_b128 v[192:195], v5 offset:8192\n\t"
      "v_smfmac_f32_16x16x64_bf16 v[20:23], v[24:27], v[208:215], v6\n\ts_waitcnt lgkmcnt(10)\n\tds_read_b128 v[196:199], v5 offset:9216\n\t"
      "v_smfmac_f32_16x16x64_bf16 v[16:19], v[28:31], a[168:175], v6\n\tds_read_b128 v[200:203], v5 offset:10240\n\tds_read_b128 v[204:207], v5 offset:11264\n\t"
      "v_smfmac_f32_16x16x64_bf16 v[20:23], v[28:31], a[176:183], v6\n\ts_waitcnt lgkmcnt(12)\n\tds_read_b128 v[208:211], v5 offset:12288\n\t"
      "v_smfmac_f32_16x16x64_bf16 v[16:19], v[224:227], a[184:191], v6\n\tds_read_b128 v[212:215], v5 offset:13312\n\t"
      "v_smfmac_f32_16x16x64_bf16 v[20:23], v[224:227], v[216:223], v6\n\tds_read_b128 v[216:219], v5 offset:14336\n\t"
      TK4
      "v_mov_b64 v[228:229], 0\n\tv_mov_b64 v[230:231], 0\n\tv_mov_b64 v[232:233], 0\n\tv_mov_b64 v[234:235], 0\n\tv_mov_b64 v[236:237], 0\n\tv_add_f32 v16, v16, v17\n\t"
      "v_add_f32 v18, v18, v19\n\tv_add_f32 v20, v20, v21\n\tv_add_f32 v22, v22, v23\n\tv_cvt_pk_bf16_f32 v16, v16, v20\n\tv_cvt_pk_bf16_f32 v18, v18, v22\n\t"
      "ds_write2_b32 v7, v16, v18 offset1:68\n\tv_mov_b64 v[238:239], 0\n\tv_mov_b64 v[224:225], 0\n\tv_mov_b64 v[226:227], 0\n\tv_mov_b64 v[24:25], 0\n\t"
      "v_mov_b64 v[26:27], 0\n\tv_mov_b64 v[28:29], 0\n\tv_mov_b64 v[30:31], 0\n\ts_waitcnt lgkmcnt(0)\n\ts_barrier\n\t"
      TK5
      "ds_read_b128 v[240:243], v8 offset:0\n\tds_read_b128 v[244:247], v8 offset:64\n\tds_read_b128 v[248:251], v8 offset:128\n\tds_read_b128 v[252:255], v8 offset:192\n\t"
      "v_mov_b64 v[16:17], 0\n\tv_mov_b64 v[18:19], 0\n\tv_mov_b64 v[20:21], 0\n\tv_mov_b64 v[22:23], 0\n\ts_waitcnt lgkmcnt(3)\n\t"
      "v_smfmac_f32_16x16x64_bf16 v[224:227], v[240:243], v[32:39], v12\n\tds_read_b128 v[220:223], v5 offset:15360\n\t"
      "v_smfmac_f32_16x16x64_bf16 v[228:231], v[240:243], v[64:71], v12\n\ts_waitcnt lgkmcnt(3)\n\tv_smfmac_f32_16x16x64_bf16 v[224:227], v[244:247], v[40:47], v12\n\t"
      "v_smfmac_f32_16x16x64_bf16 v[228:231], v[244:247], v[72:79], v12\n\ts_waitcnt lgkmcnt(2)\n\tv_smfmac_f32_16x16x64_bf16 v[224:227], v[248:251], v[48:55], v12\n\t"
      "v_smfmac_f32_16x16x64_bf16 v[228:231], v[248:251], v[80:87], v12\n\ts_waitcnt lgkmcnt(1)\n\tv_smfmac_f32_16x16x64_bf16 v[224:227], v[252:255], v[56:63], v12\n\t"
      "v_smfmac_f32_16x16x64_bf16 v[228:231], v[252:255], v[88:95], v12\n\ts_nop 1\n\tv_smfmac_f32_16x16x64_bf16 v[232:235], v[240:243], v[96:103], v12\n\ts_nop 2\n\t"
      "v_smfmac_f32_16x16x64_bf16 v[236:239], v[240:243], v[128:135], v12\n\tv_cvt_pk_bf16_f32 v224, v224, v225\n\t"
      "v_smfmac_f32_16x16x64_bf16 v[232:235], v[244:247], v[104:111], v12\n\tv_cvt_pk_bf16_f32 v225, v226, v227\n\t"
      "v_smfmac_f32_16x16x64_bf16 v[236:239], v[244:247], v[136:143], v12\n\tbuffer_store_dwordx2 v[224:225], v9, s[8:11], s44 offen sc1\n\t"
      "v_smfmac_f32_16x16x64_bf16 v[232:235], v[248:251], v[112:119], v12\n\ts_add_u32 s44, s44, 0x8000\n\tv_cvt_pk_bf16_f32 v228, v228, v229\n\t"
      "v_smfmac_f32_16x16x64_bf16 v[236:239], v[248:251], v[144:151], v12\n\tv_cvt_pk_bf16_f32 v229, v230, v231\n\t"
      "v_smfmac_f32_16x16x64_bf16 v[232:235], v[252:255], v[120:127], v12\n\tbuffer_store_dwordx2 v[228:229], v9, s[8:11], s44 offen sc1\n\t"
      "v_smfmac_f32_16x16x64_bf16 v[236:239], v[252:255], v[152:159], v12\n\ts_nop 1\n\ts_add_u32 s44, s43, s28\n\t"
      "v_smfmac_f32_16x16x64_bf16 v[24:27], v[240:243], a[192:199], v12\n\ts_nop 2\n\tv_smfmac_f32_16x16x64_bf16 v[28:31], v[240:243], a[224:231], v12\n\t"
      "v_cvt_pk_bf16_f32 v232, v232, v233\n\tv_smfmac_f32_16x16x64_bf16 v[24:27], v[244:247], a[200:207], v12\n\tv_cvt_pk_bf16_f32 v233, v234, v235\n\t"
      "v_smfmac_f32_16x16x64_bf16 v[28:31], v[244:247], a[232:239], v12\n\tbuffer_store_dwordx2 v[232:233], v9, s[8:11], s44 offen sc1\n\t"
      "v_smfmac_f32_16x16x64_bf16 v[24:27], v[248:251], a[208:215], v12\n\ts_add_u32 s44, s44, 0x8000\n\tv_cvt_pk_bf16_f32 v236, v236, v237\n\t"
      "v_smfmac_f32_16x16x64_bf16 v[28:31], v[248:251], a[240:247], v12\n\tv_cvt_pk_bf16_f32 v237, v238, v239\n\t"
      "v_smfmac_f32_16x16x64_bf16 v[24:27], v[252:255], a[216:223], v12\n\ts_waitcnt lgkmcnt(0)\n\tbuffer_store_dwordx2 v[236:237], v9, s[8:11], s44 offen sc1\n\t"
      "v_smfmac_f32_16x16x64_bf16 v[28:31], v[252:255], a[248:255], v12\n\ts_nop 1\n\ts_add_u32 s44, s43, s29\n\t"
      "v_smfmac_f32_16x16x64_bf16 v[16:19], v[240:243], v[160:167], v12\n\ts_nop 2\n\tv_smfmac_f32_16x16x64_bf16 v[20:23], v[240:243], v[192:199], v12\n\t"
      "v_cvt_pk_bf16_f32 v24, v24, v25\n\tv_smfmac_f32_16x16x64_bf16 v[16:19], v[244:247], v[168:175], v12\n\tv_cvt_pk_bf16_f32 v25, v26, v27\n\t"
      "v_smfmac_f32_16x16x64_bf16 v[20:23], v[244:247], v[200:207], v12\n\tbuffer_store_dwordx2 v[24:25], v9, s[8:11], s44 offen sc1\n\t"
      "v_smfmac_f32_16x16x64_bf16 v[16:19], v[248:251], v[176:183], v12\n\ts_add_u32 s44, s44, 0x8000\n\tv_cvt_pk_bf16_f32 v28, v28, v29\n\t"
      "v_smfmac_f32_16x16x64_bf16 v[20:23], v[248:251], v[208:215], v12\n\tv_cvt_pk_bf16_f32 v29, v30, v31\n\t"
      "v_smfmac_f32_16x16x64_bf16 v[16:19], v[252:255], v[184:191], v12\n\tbuffer_store_dwordx2 v[28:29], v9, s[8:11], s44 offen sc1\n\t"
      "v_smfmac_f32_16x16x64_bf16 v[20:23], v[252:255], v[216:223], v12\n\ts_add_u32 s44, s43, s30\n\ts_nop 4\n\tv_cvt_pk_bf16_f32 v16, v16, v17\n\t"
      "v_cvt_pk_bf16_f32 v17, v18, v19\n\tv_cvt_pk_bf16_f32 v20, v20, v21\n\tv_cvt_pk_bf16_f32 v21, v22, v23\n\tbuffer_store_dwordx2 v[16:17], v9, s[8:11], s44 offen sc1\n\t"
      "s_add_u32 s44, s44, 0x8000\n\tbuffer_store_dwordx2 v[20:21], v9, s[8:11], s44 offen sc1\n\t"
      TK6
      "s_cmp_lg_u32 s16, 1\n\ts_cbranch_scc1 69f\n\t"
      STS("s_memrealtime s[46:47]\n\t")
      "74:\n\tbuffer_load_dword v14, v11, s[8:11], s41 offen sc1\n\ts_waitcnt vmcnt(0)\n\t"
      "76:\n\tv_max_u32_dpp v14, v14, v14 row_ror:8 row_mask:0xf bank_mask:0xf\n\ts_nop 1\n\tv_max_u32_dpp v14, v14, v14 row_ror:4 row_mask:0xf bank_mask:0xf\n\ts_nop 1\n\t"
      "v_max_u32_dpp v14, v14, v14 row_ror:2 row_mask:0xf bank_mask:0xf\n\ts_nop 1\n\tv_max_u32_dpp v14, v14, v14 row_ror:1 row_mask:0xf bank_mask:0xf\n\ts_nop 0\n\t"
      "v_readlane_b32 s52, v14, 0\n\tv_readlane_b32 s48, v14, 16\n\ts_max_u32 s52, s52, s48\n\ts_cmp_lg_u32 s52, -1\n\ts_cbranch_scc1 75f\n\ts_cmp_eq_u32 s18, 0\n\t"
      "s_cbranch_scc1 75f\n\ts_sub_u32 s18, s18, 1\n\ts_branch 74b\n\t"
      "75:\n\ts_sub_u32 s48, s52, s19\n\ts_sub_u32 s48, s48, s62\n\ts_cmp_gt_i32 s48, s72\n\ts_cselect_b32 s19, s73, 0\n\ts_cmp_gt_i32 s48, 40\n\t"
      "s_cselect_b32 s48, 40, s48\n\ts_ashr_i32 s48, s48, 2\n\ts_add_i32 s62, s62, s48\n\ts_min_u32 s62, s62, 1000\n\ts_mul_i32 s63, s62, 3\n\ts_lshr_b32 s63, s63, 3\n\t"
      "s_add_u32 s19, s19, s52\n\ts_add_u32 s19, s19, s23\n\ts_add_u32 s19, s19, s63\n\t"
      STS("s_waitcnt lgkmcnt(0)\n\ts_lshl_b32 s46, s46, 2\n\ts_sub_u32 s46, s52, s46\n\ts_add_u32 s61, s61, s46\n\ts_min_i32 s21, s21, s46\n\ts_cmp_lt_u32 s17, 8\n\ts_cselect_b32 s21, 1000, s21\n\t")
      "s_branch 69f\n\t"
      "86:\n\t"
      STS("s_add_u32 s50, s50, 1\n\t")
      "s_waitcnt lgkmcnt(0)\n\tbuffer_load_dwordx4 v[240:243], v0, s[8:11], s68 offen sc1\n\tbuffer_load_dwordx4 v[244:247], v0, s[8:11], s69 offen sc1\n\t"
      "buffer_load_dwordx4 v[248:251], v0, s[8:11], s70 offen sc1\n\tbuffer_load_dwordx4 v[252:255], v0, s[8:11], s71 offen sc1\n\ts_nop 3\n\tv_mov_b64 v[16:17], 0\n\t"
      "v_mov_b64 v[18:19], 0\n\tv_mov_b64 v[20:21], 0\n\tv_mov_b64 v[22:23], 0\n\ts_nop 1\n\ts_branch 84f\n\t"
      "83:\n\tbuffer_load_dwordx4 v[240:243], v0, s[8:11], s68 offen sc1\n\tbuffer_load_dwordx4 v[244:247], v0, s[8:11], s69 offen sc1\n\t"
      "buffer_load_dwordx4 v[248:251], v0, s[8:11], s70 offen sc1\n\tbuffer_load_dwordx4 v[252:255], v0, s[8:11], s71 offen sc1\n\t"
      "84:\n\ts_waitcnt vmcnt(0)\n\tv_max3_u32 v13, v240, v241, v242\n\tv_max_u32 v13, v13, v243\n\tv_max3_u32 v13, v13, v244, v245\n\tv_max3_u32 v13, v13, v246, v247\n\t"
      "v_max3_u32 v13, v13, v248, v249\n\tv_max3_u32 v13, v13, v250, v251\n\tv_max3_u32 v13, v13, v252, v253\n\tv_max3_u32 v13, v13, v254, v255\n\tv_cmp_eq_u32 vcc, -1, v13\n\t"
      "s_cbranch_vccz 85f\n\ts_cmp_eq_u32 s18, 0\n\ts_cbranch_scc1 85f\n\ts_sub_u32 s18, s18, 1\n\ts_branch 83b\n\t"
      "85:\n\tv_smfmac_f32_16x16x64_bf16 v[16:19], v[240:243], a[0:7], v6\n\tv_smfmac_f32_16x16x64_bf16 v[20:23], v[240:243], a[8:15], v6\n\t"
      "ds_write_b128 v1, v[240:243] offset:0\n\tds_write_b128 v1, v[244:247] offset:1024\n\tds_write_b128 v1, v[248:251] offset:2048\n\t"
      "ds_write_b128 v1, v[252:255] offset:3072\n\tv_mov_b32 v13, 0\n\ts_mov_b64 vcc, 0\n\ts_waitcnt lgkmcnt(0)\n\ts_branch 20b\n\t"
      "87:\n\t"
      STS("s_add_u32 s50, s50, 1\n\t")
      "s_waitcnt lgkmcnt(0)\n\tbuffer_load_dwordx4 v[244:247], v0, s[8:11], s69 offen sc1\n\tbuffer_load_dwordx4 v[248:251], v0, s[8:11], s70 offen sc1\n\t"
      "buffer_load_dwordx4 v[252:255], v0, s[8:11], s71 offen sc1\n\ts_nop 3\n\tv_mov_b64 v[16:17], 0\n\tv_mov_b64 v[18:19], 0\n\tv_mov_b64 v[20:21], 0\n\t"
      "v_mov_b64 v[22:23], 0\n\ts_nop 1\n\tv_smfmac_f32_16x16x64_bf16 v[16:19], v[240:243], a[0:7], v6\n\tv_smfmac_f32_16x16x64_bf16 v[20:23], v[240:243], a[8:15], v6\n\t"
      "s_branch 84f\n\t"
      "83:\n\tbuffer_load_dwordx4 v[244:247], v0, s[8:11], s69 offen sc1\n\tbuffer_load_dwordx4 v[248:251], v0, s[8:11], s70 offen sc1\n\t"
      "buffer_load_dwordx4 v[252:255], v0, s[8:11], s71 offen sc1\n\t"
      "84:\n\ts_waitcnt vmcnt(0)\n\tv_max3_u32 v13, v244, v245, v246\n\tv_max_u32 v13, v13, v247\n\tv_max3_u32 v13, v13, v248, v249\n\tv_max3_u32 v13, v13, v250, v251\n\t"
      "v_max3_u32 v13, v13, v252, v253\n\tv_max3_u32 v13, v13, v254, v255\n\tv_cmp_eq_u32 vcc, -1, v13\n\ts_cbranch_vccz 85f\n\ts_cmp_eq_u32 s18, 0\n\ts_cbranch_scc1 85f\n\t"
      "s_sub_u32 s18, s18, 1\n\ts_branch 83b\n\t"
      "85:\n\tv_smfmac_f32_16x16x64_bf16 v[16:19], v[244:247], a[16:23], v6\n\tv_smfmac_f32_16x16x64_bf16 v[20:23], v[244:247], v[160:167], v6\n\t"
      "v_smfmac_f32_16x16x64_bf16 v[16:19], v[248:251], a[24:31], v6\n\tv_smfmac_f32_16x16x64_bf16 v[20:23], v[248:251], a[32:39], v6\n\t"
      "v_smfmac_f32_16x16x64_bf16 v[16:19], v[24:27], a[48:55], v6\n\tds_write_b128 v1, v[244:247] offset:1024\n\tds_write_b128 v1, v[248:251] offset:2048\n\t"
      "ds_write_b128 v1, v[252:255] offset:3072\n\tv_mov_b32 v13, 0\n\ts_mov_b64 vcc, 0\n\ts_waitcnt lgkmcnt(0)\n\ts_branch 21b\n\t"
      "88:\n\t"
      STS("s_add_u32 s50, s50, 1\n\t")
      "s_waitcnt lgkmcnt(0)\n\tbuffer_load_dwordx4 v[248:251], v0, s[8:11], s70 offen sc1\n\tbuffer_load_dwordx4 v[252:255], v0, s[8:11], s71 offen sc1\n\ts_nop 3\n\t"
      "v_mov_b64 v[16:17], 0\n\tv_mov_b64 v[18:19], 0\n\tv_mov_b64 v[20:21], 0\n\tv_mov_b64 v[22:23], 0\n\ts_nop 1\n\t"
      "v_smfmac_f32_16x16x64_bf16 v[16:19], v[240:243], a[0:7], v6\n\tv_smfmac_f32_16x16x64_bf16 v[20:23], v[240:243], a[8:15], v6\n\t"
      "v_smfmac_f32_16x16x64_bf16 v[16:19], v[244:247], a[16:23], v6\n\tv_smfmac_f32_16x16x64_bf16 v[20:23], v[244:247], v[160:167], v6\n\ts_branch 84f\n\t"
      "83:\n\tbuffer_load_dwordx4 v[248:251], v0, s[8:11], s70 offen sc1\n\tbuffer_load_dwordx4 v[252:255], v0, s[8:11], s71 offen sc1\n\t"
      "84:\n\ts_waitcnt vmcnt(0)\n\tv_max3_u32 v13, v248, v249, v250\n\tv_max_u32 v13, v13, v251\n\tv_max3_u32 v13, v13, v252, v253\n\tv_max3_u32 v13, v13, v254, v255\n\t"
      "v_cmp_eq_u32 vcc, -1, v13\n\ts_cbranch_vccz 85f\n\ts_cmp_eq_u32 s18, 0\n\ts_cbranch_scc1 85f\n\ts_sub_u32 s18, s18, 1\n\ts_branch 83b\n\t"
      "85:\n\tv_smfmac_f32_16x16x64_bf16 v[16:19], v[248:251], a[24:31], v6\n\tv_smfmac_f32_16x16x64_bf16 v[20:23], v[248:251], a[32:39], v6\n\t"
      "v_smfmac_f32_16x16x64_bf16 v[16:19], v[24:27], a[48:55], v6\n\tv_smfmac_f32_16x16x64_bf16 v[20:23], v[24:27], a[56:63], v6\n\t"
      "v_smfmac_f32_16x16x64_bf16 v[16:19], v[252:255], a[40:47], v6\n\tv_smfmac_f32_16x16x64_bf16 v[20:23], v[252:255], v[168:175], v6\n\t"
      "v_smfmac_f32_16x16x64_bf16 v[16:19], v[28:31], a[64:71], v6\n\tv_smfmac_f32_16x16x64_bf16 v[20:23], v[28:31], v[176:183], v6\n\t"
      "v_smfmac_f32_16x16x64_bf16 v[16:19], v[224:227], a[72:79], v6\n\tv_smfmac_f32_16x16x64_bf16 v[20:23], v[224:227], a[80:87], v6\n\t"
      "v_smfmac_f32_16x16x64_bf16 v[16:19], v[228:231], a[88:95], v6\n\tds_write_b128 v1, v[248:251] offset:2048\n\tds_write_b128 v1, v[252:255] offset:3072\n\t"
      "v_mov_b32 v13, 0\n\ts_mov_b64 vcc, 0\n\ts_waitcnt lgkmcnt(0)\n\ts_branch 22b\n\t"
      "69:\n\t"
      TK7
      STS("s_cmp_lt_i32 s17, 8\n\ts_cselect_b32 s50, 0, s50\n\ts_cselect_b32 s51, 0, s51\n\ts_cselect_b32 s61, 0, s61\n\t")
      TKS("s_waitcnt lgkmcnt(0)\n\ts_cmp_lt_i32 s17, 0\n\ts_cbranch_scc1 98f\n\ts_cmp_lg_u32 s16, s91\n\ts_cbranch_scc1 98f\n\ts_mov_b64 exec, 1\n\ts_lshl_b32 s48, s17, 4\n\ts_add_u32 s48, s48, 161056\n\tv_mov_b32 v13, s48\n\ts_sub_u32 s48, s74, s90\n\ts_min_u32 s48, s48, 0xffff\n\tv_mov_b32 v14, s48\n\tds_write_b16 v13, v14 offset:0\n\ts_sub_u32 s48, s76, s74\n\ts_min_u32 s48, s48, 0xffff\n\tv_mov_b32 v14, s48\n\tds_write_b16 v13, v14 offset:2\n\ts_sub_u32 s48, s78, s76\n\ts_min_u32 s48, s48, 0xffff\n\tv_mov_b32 v14, s48\n\tds_write_b16 v13, v14 offset:4\n\ts_sub_u32 s48, s80, s78\n\ts_min_u32 s48, s48, 0xffff\n\tv_mov_b32 v14, s48\n\tds_write_b16 v13, v14 offset:6\n\ts_sub_u32 s48, s82, s80\n\ts_min_u32 s48, s48, 0xffff\n\tv_mov_b32 v14, s48\n\tds_write_b16 v13, v14 offset:8\n\ts_sub_u32 s48, s84, s82\n\ts_min_u32 s48, s48, 0xffff\n\tv_mov_b32 v14, s48\n\tds_write_b16 v13, v14 offset:10\n\ts_sub_u32 s48, s86, s84\n\ts_min_u32 s48, s48, 0xffff\n\tv_mov_b32 v14, s48\n\tds_write_b16 v13, v14 offset:12\n\ts_sub_u32 s48, s88, s86\n\ts_min_u32 s48, s48, 0xffff\n\tv_mov_b32 v14, s48\n\tds_write_b16 v13, v14 offset:14\n\ts_mov_b64 exec, -1\n\t98:\n\ts_mov_b32 s90, s88\n\t")
      "s_add_u32 s17, s17, 1\n\ts_cmp_lt_u32 s17, 64\n\ts_cbranch_scc1 10b\n\t"
      ".Lmk_end5:\n\ts_waitcnt vmcnt(0) lgkmcnt(0)\n\t"
      TKS("s_mul_i32 s48, s16, 72\n\ts_add_u32 s48, s48, 162080\n\tv_mov_b32 v224, s48\n\tds_read_b32 v225, v224 offset:0\n\ts_waitcnt lgkmcnt(0)\n\tv_readfirstlane_b32 s74, v225\n\tds_read_b32 v225, v224 offset:4\n\ts_waitcnt lgkmcnt(0)\n\tv_readfirstlane_b32 s75, v225\n\tds_read_b32 v225, v224 offset:8\n\ts_waitcnt lgkmcnt(0)\n\tv_readfirstlane_b32 s76, v225\n\tds_read_b32 v225, v224 offset:12\n\ts_waitcnt lgkmcnt(0)\n\tv_readfirstlane_b32 s77, v225\n\tds_read_b32 v225, v224 offset:16\n\ts_waitcnt lgkmcnt(0)\n\tv_readfirstlane_b32 s78, v225\n\tds_read_b32 v225, v224 offset:20\n\ts_waitcnt lgkmcnt(0)\n\tv_readfirstlane_b32 s79, v225\n\tds_read_b32 v225, v224 offset:24\n\ts_waitcnt lgkmcnt(0)\n\tv_readfirstlane_b32 s80, v225\n\tds_read_b32 v225, v224 offset:28\n\ts_waitcnt lgkmcnt(0)\n\tv_readfirstlane_b32 s81, v225\n\tds_read_b32 v225, v224 offset:32\n\ts_waitcnt lgkmcnt(0)\n\tv_readfirstlane_b32 s82, v225\n\tds_read_b32 v225, v224 offset:36\n\ts_waitcnt lgkmcnt(0)\n\tv_readfirstlane_b32 s83, v225\n\tds_read_b32 v225, v224 offset:40\n\ts_waitcnt lgkmcnt(0)\n\tv_readfirstlane_b32 s84, v225\n\tds_read_b32 v225, v224 offset:44\n\ts_waitcnt lgkmcnt(0)\n\tv_readfirstlane_b32 s85, v225\n\tds_read_b32 v225, v224 offset:48\n\ts_waitcnt lgkmcnt(0)\n\tv_readfirstlane_b32 s86, v225\n\tds_read_b32 v225, v224 offset:52\n\ts_waitcnt lgkmcnt(0)\n\tv_readfirstlane_b32 s87, v225\n\tds_read_b32 v225, v224 offset:56\n\ts_waitcnt lgkmcnt(0)\n\tv_readfirstlane_b32 s88, v225\n\tds_read_b32 v225, v224 offset:60\n\ts_waitcnt lgkmcnt(0)\n\tv_readfirstlane_b32 s89, v225\n\tds_read_b32 v225, v224 offset:64\n\ts_waitcnt lgkmcnt(0)\n\tv_readfirstlane_b32 s90, v225\n\tds_read_b32 v225, v224 offset:68\n\ts_waitcnt lgkmcnt(0)\n\tv_readfirstlane_b32 s91, v225\n\t")
      STS("s_lshl_b32 s48, s16, 2\n\ts_add_u32 s48, s48, 162944\n\tv_mov_b32 v0, s48\n\tv_mov_b32 v1, s50\n\tds_write_b32 v0, v1\n\ts_cmp_lg_u32 s16, 0\n\ts_cbranch_scc1 97f\n\tv_mov_b32 v1, s51\n\tds_write_b32 v0, v1 offset:16\n\t97:\n\ts_cmp_lg_u32 s16, 1\n\ts_cbranch_scc1 95f\n\tv_mov_b32 v1, s61\n\tds_write_b32 v0, v1 offset:16\n\tv_mov_b32 v1, s21\n\tds_write_b32 v0, v1 offset:20\n\tv_mov_b32 v1, s22\n\tds_write_b32 v0, v1 offset:24\n\t95:\n\t")
      "s_cmp_lg_u32 s18, 0\n\ts_cbranch_scc1 96f\n\tv_mov_b32 v1, 1\n\tbuffer_store_dword v1, off, s[12:15], 0 offset:3328 sc1\n\t"
      "96:\n\t"
      : "+{v[0:31]}"(k), "+{v[160:191]}"(s0), "+{v[192:223]}"(s1), ALL_REGS :: "memory", "vcc", "scc", S_CLOB TK_CLOB);
}

struct KvIn { v4u kr[11], vr[8], sh[4]; };
DEV void kv_load(KArg p, KvIn& in, int u, int lane) {
  const rsrc_t rk = mkrs(p->bk, 2048 * 2048), rv = mkrs(p->bv, 2048 * 2048);
  const int de = (u >> 5) * 64 + (lane & 7) * 8, t0 = (u & 31) * 64 + (lane >> 3) * 8;
#pragma unroll
  for (int i = 0; i < 11; ++i) in.kr[i] = ld16n(rk, (t0 + i - 3) * 2048 + de * 2, 0);
#pragma unroll
  for (int i = 0; i < 8; ++i) in.vr[i] = ld16n(rv, (t0 + i) * 2048 + de * 2, 0);
#pragma unroll
  for (int i = 0; i < 4; ++i) in.sh[i] = *(const GAS v4u*)(p->shift + de * 4 + i * 8);
}
DEV f32x2 both(unsigned u) { return f32x2{blo(u), bhi(u)}; }
DEV void kv_make(Ctx& c, const KvIn& in, int at, int lane) {
#pragma unroll
  for (int cp = 0; cp < 4; ++cp) {
    const v4u sh = in.sh[cp];
    const f32x2 s0 = {blo(sh.x), blo(sh.z)}, s1 = {bhi(sh.x), bhi(sh.z)}, s2 = {blo(sh.y), blo(sh.w)}, s3 = {bhi(sh.y), bhi(sh.w)};
    f32x2 kk[11], o[8];
#pragma unroll
    for (int i = 0; i < 11; ++i) kk[i] = both(in.kr[i][cp]);
#pragma unroll
    for (int i = 0; i < 8; ++i)
      o[i] = rb2(__builtin_elementwise_fma(kk[i + 3], s0, __builtin_elementwise_fma(kk[i + 2], s1, __builtin_elementwise_fma(kk[i + 1], s2, kk[i] * s3)))) * both(in.vr[i][cp]);
#pragma unroll
    for (int e = 0; e < 2; ++e)
      sts16(c.L, at + (2 * cp + e) * 1152 + (lane >> 3) * 16 + (lane & 7) * 144, v4u{pk(o[0][e], o[1][e]), pk(o[2][e], o[3][e]), pk(o[4][e], o[5][e]), pk(o[6][e], o[7][e])});
  }
}
DEV int seq_off(int u, int lane, int va) { return amap(((u >> 5) * 64 + (lane >> 3) * 8) * 4096, va ^ ((u >> 7) & 1) ^ 1) + ((u & 31) * 64 + (lane & 7) * 8) * 2; }
DEV void kv_store(Ctx& c, int at, int u, int lane, int va) {
  LGKM(0);
#pragma unroll
  for (int ch = 0; ch < 8; ++ch) st16s(c.rs, seq_off(u, lane, va), R_KVT + amap(ch * 4096, 0), lds16(c.L, at + ch * 1152 + (lane & 7) * 16 + (lane >> 3) * 144));
}

DEV void y_role(Ctx& c, int y) {
  const KArg p = enter(c);
  constexpr int ZR = 0, UL = 131072, CW = 155648;
  c.budget = SPIN_CAP;
  const int w = c.w, lane = c.lane = fresh_lane(), g = lane >> 4, cc = lane & 15;
  const v4u sent4 = {SENT, SENT, SENT, SENT};
  unsigned m0 = 0, votes = 0;
  if (w == 0) {
    if (c.att == 0) {
      c.ask = __builtin_amdgcn_readfirstlane(__hip_atomic_load(c.ctrl + C_ECHO / 4 + c.b, RLX_AGENT)) + 1u;
      if (lane == 0) __hip_atomic_store(c.ctrl + (C_ASK + c.b * 64) / 4, c.ask, RLX_AGENT);
    }
    m0 = *(const GAS unsigned*)(p->mem0 + (lane >> 3) * 1024 + 16 * y + 2 * (lane & 7));
    m0 = m0 < SENT - 1u ? m0 : SENT - 1u;
    const int po = MB + y * 16384 + (y & 31) * 256 + (lane & 31) * 4;
    st4s(c.rs, po + (lane >> 5) * 8192, 0, SENT); st4s(c.rs, po + (lane >> 5) * 8192, 2 * MB, SENT);
    unsigned best[4] = {~0u, ~0u, ~0u, ~0u};
#pragma unroll
    for (int r = 0; r < NPROBE; ++r) {
      VMWAIT(0);
      const unsigned t0 = (unsigned)__builtin_amdgcn_s_memtime();
      LGKM(0);
      st4s(c.rs, po + (r & 1) * 8192, (r & 2) * MB, SENT);
      VMWAIT(0);
      const unsigned t1 = (unsigned)__builtin_amdgcn_s_memtime();
      LGKM(0);
      best[r & 3] = min(best[r & 3], t1 - t0);
    }
    votes = (best[1] < best[0] ? 1u << 16 : 0u) | (best[3] < best[2] ? 1u << 24 : 0u);
    if (lane < 8) *(LAS unsigned*)(c.L + CW + lane * 4) = 0u;
  }
  const bool srv = c.b == SERVER;
#pragma unroll 1
  for (int f = 0; f < 2; ++f) {
    if (srv ? w != 0 : w != f + 1) continue;
#pragma unroll
    for (int i = 0; i < 8; ++i) st16s(c.rs, y * 16384 + f * 8192 + lane * 16, R_U + i * 1024, sent4);
#pragma unroll
    for (int i = 0; i < 2; ++i) st16s(c.rs, f * 262144 + y * 2048 + lane * 16, R_MEM + i * 1024, sent4);
#pragma unroll
    for (int i = 0; i < 3; ++i) st16s(c.rs, (2 * y + f) * 3072 + lane * 16, R_PT + i * 1024, sent4);
  }
  VMWAIT(0);
  if (!srv) BAR();
  claim(c, votes);
  STAMP(2);
  const int kvu = 8 * y + 2 * w, kva = 65536 + w * 18432;
  BAR();
  {
    const rsrc_t rx = mkrs(p->x, 2048 * 2048);
    unsigned t = 0;
#pragma unroll
    for (int i = 0; i < 2 * XWARM; ++i) t += __builtin_amdgcn_raw_buffer_load_b32(rx, (4 * y + w) * 8192 + lane * 128, i * 2 * MB, 0);
    asm volatile("" :: "v"(t));
  }
  const unsigned vote = wait_verdict(c);
  const int par = vote_b(vote), para = vote_a(vote);
  const int mo = amap(y * 256 + lane * 4, par);
  if (w == 0) st4s(c.rs, mo, R_MEM, m0);
  STAMP(0);
  if (w == 1) DIAG(6, (unsigned)__builtin_amdgcn_s_memrealtime());
  volatile LAS unsigned* const ZFULL = (volatile LAS unsigned*)(c.L + CW);
  volatile LAS unsigned* const GO = ZFULL + 1;
  volatile LAS unsigned* const TDONE = ZFULL + 2;
  volatile LAS unsigned* const PUB = ZFULL + 4;
  auto lds_wait = [&](volatile LAS unsigned* wd, unsigned need) {
#pragma unroll 1
    while (__builtin_amdgcn_readfirstlane(*wd) < need) { if (spend(c)) break; __builtin_amdgcn_s_sleep(1); }
    asm volatile("" ::: "memory");
  };
  auto lds_post = [&](volatile LAS unsigned* wd, unsigned v) { LGKM(0); *wd = v; };
  TICK_DECL;
  if (w == 0) {
    v32u k;
#pragma unroll
    for (int i = 0; i < 4; ++i) {
      const int o = (i + (y >> 2) + ROFF) & 3;
      k[i] = amap(o * 4096 + lane * 16, par);
      k[4 + i] = amap(16384 + o * 1024 + lane * 16, par);
      k[8 + i] = R_U + y * 16384 + para * 8192 + o * 2048 + lane * 16;
      k[12 + i] = ZR + o * 8192 + lane * 16;
      k[16 + i] = UL + o * 2048 + lane * 16;
    }
    k[20] = amap(y * 256 + (4 * (g & 1) + (g & 2) + (cc & 1)) * 32 + (cc >> 1) * 4, par);
    k[21] = (cc & 1) ? 0x03020706 : 0x05040100;
    k[22] = (lane & 8) ? 0xEEEE : 0x4444;
    k[23] = amap(24576 + ((y >> 2) & 3) * 4096 + (lane & 31) * 128, par);
    k[24] = CW;
#pragma unroll
    for (int i = 25; i < 32; ++i) k[i] = 0;
    v32u s0 = {RSRCS, 0, 0, c.budget, 0, 0, PACE, C1, CU, KH, ~0u, 0};
    v16u x0; v8u x3; v32u z[4], x[3];
    asm volatile(
      "v_mov_b32 v32, v23\n\tv_add_u32 v33, 0x8000, v23\n\tv_add_u32 v34, 0x10000, v23\n\ts_nop 0\n\tv_readfirstlane_b32 s8, v160\n\tv_readfirstlane_b32 s9, v161\n\t"
      "v_readfirstlane_b32 s10, v162\n\tv_readfirstlane_b32 s11, v163\n\tv_readfirstlane_b32 s12, v164\n\tv_readfirstlane_b32 s13, v165\n\tv_readfirstlane_b32 s14, v166\n\t"
      "v_readfirstlane_b32 s15, v167\n\tv_readfirstlane_b32 s16, v168\n\tv_readfirstlane_b32 s17, v169\n\tv_readfirstlane_b32 s18, v170\n\tv_readfirstlane_b32 s19, v171\n\t"
      "v_readfirstlane_b32 s20, v172\n\tv_readfirstlane_b32 s21, v173\n\tv_readfirstlane_b32 s22, v174\n\tv_readfirstlane_b32 s23, v175\n\tv_readfirstlane_b32 s24, v176\n\t"
      "v_readfirstlane_b32 s25, v177\n\tv_readfirstlane_b32 s26, v178\n\tv_readfirstlane_b32 s27, v179\n\tv_readfirstlane_b32 s28, v180\n\tv_readfirstlane_b32 s29, v181\n\t"
      "v_readfirstlane_b32 s30, v182\n\tv_readfirstlane_b32 s31, v183\n\ts_nop 4\n\t"
      TKS("s_mul_i32 s48, s16, 72\n\ts_add_u32 s48, s48, 162080\n\tv_mov_b32 v224, s48\n\tv_mov_b32 v225, s74\n\tds_write_b32 v224, v225 offset:0\n\tv_mov_b32 v225, s75\n\tds_write_b32 v224, v225 offset:4\n\tv_mov_b32 v225, s76\n\tds_write_b32 v224, v225 offset:8\n\tv_mov_b32 v225, s77\n\tds_write_b32 v224, v225 offset:12\n\tv_mov_b32 v225, s78\n\tds_write_b32 v224, v225 offset:16\n\tv_mov_b32 v225, s79\n\tds_write_b32 v224, v225 offset:20\n\tv_mov_b32 v225, s80\n\tds_write_b32 v224, v225 offset:24\n\tv_mov_b32 v225, s81\n\tds_write_b32 v224, v225 offset:28\n\tv_mov_b32 v225, s82\n\tds_write_b32 v224, v225 offset:32\n\tv_mov_b32 v225, s83\n\tds_write_b32 v224, v225 offset:36\n\tv_mov_b32 v225, s84\n\tds_write_b32 v224, v225 offset:40\n\tv_mov_b32 v225, s85\n\tds_write_b32 v224, v225 offset:44\n\tv_mov_b32 v225, s86\n\tds_write_b32 v224, v225 offset:48\n\tv_mov_b32 v225, s87\n\tds_write_b32 v224, v225 offset:52\n\tv_mov_b32 v225, s88\n\tds_write_b32 v224, v225 offset:56\n\tv_mov_b32 v225, s89\n\tds_write_b32 v224, v225 offset:60\n\tv_mov_b32 v225, s90\n\tds_write_b32 v224, v225 offset:64\n\tv_mov_b32 v225, s91\n\tds_write_b32 v224, v225 offset:68\n\t")
      "s_mov_b64 s[72:73], s[24:25]\n\t"
      TKS("s_mov_b32 s91, 0\n\t")
      "v_mov_b64 v[36:37], -1\n\tv_mov_b64 v[38:39], -1\n\ts_mov_b32 s42, 0x280000\n\ts_mov_b32 s43, 0x100000\n\ts_memrealtime s[46:47]\n\ts_waitcnt lgkmcnt(0)\n\t"
      "s_lshl_b32 s19, s46, 2\n\ts_sub_u32 s19, s19, 0x100000\n\ts_mov_b32 s62, 470\n\ts_mov_b32 s63, 0\n\t"
      "10:\n\t"
      "20:\n\tds_read_b32 v25, v24 offset:0\n\ts_waitcnt lgkmcnt(0)\n\tv_readfirstlane_b32 s48, v25\n\ts_cmp_gt_i32 s48, s17\n\ts_cbranch_scc1 21f\n\ts_cmp_eq_u32 s18, 0\n\t"
      "s_cbranch_scc1 21f\n\ts_sub_u32 s18, s18, 1\n\ts_sleep 1\n\ts_branch 20b\n\t"
      "21:\n\ts_and_b32 s48, s17, 3\n\ts_lshl_b32 s49, s48, 15\n\tv_add_u32 v28, s49, v12\n\tv_add_u32 v29, s49, v13\n\tv_add_u32 v30, s49, v14\n\tv_add_u32 v31, s49, v15\n\t"
      "ds_read_b128 a[0:3], v28 offset:0\n\tds_read_b128 a[4:7], v28 offset:1024\n\tds_read_b128 a[8:11], v28 offset:2048\n\tds_read_b128 a[12:15], v28 offset:3072\n\t"
      "ds_read_b128 a[16:19], v28 offset:4096\n\tds_read_b128 a[20:23], v28 offset:5120\n\tds_read_b128 a[24:27], v28 offset:6144\n\tds_read_b128 a[28:31], v28 offset:7168\n\t"
      "ds_read_b128 a[32:35], v29 offset:0\n\tds_read_b128 a[36:39], v29 offset:1024\n\tds_read_b128 a[40:43], v29 offset:2048\n\tds_read_b128 a[44:47], v29 offset:3072\n\t"
      "ds_read_b128 a[48:51], v29 offset:4096\n\tds_read_b128 a[52:55], v29 offset:5120\n\tds_read_b128 a[56:59], v29 offset:6144\n\t"
      "buffer_store_dword v36, v20, s[8:11], s42 offen sc1\n\tds_read_b128 a[60:63], v29 offset:7168\n\tds_read_b128 a[64:67], v30 offset:0\n\t"
      "buffer_store_dwordx4 v[36:39], v8, s[8:11], s43 offen offset:0 sc1\n\tds_read_b128 a[68:71], v30 offset:1024\n\tds_read_b128 a[72:75], v30 offset:2048\n\t"
      "buffer_store_dwordx4 v[36:39], v8, s[8:11], s43 offen offset:1024 sc1\n\tds_read_b128 a[76:79], v30 offset:3072\n\tds_read_b128 a[80:83], v30 offset:4096\n\t"
      "buffer_store_dwordx4 v[36:39], v9, s[8:11], s43 offen offset:0 sc1\n\tds_read_b128 a[84:87], v30 offset:5120\n\tds_read_b128 a[88:91], v30 offset:6144\n\t"
      "buffer_store_dwordx4 v[36:39], v9, s[8:11], s43 offen offset:1024 sc1\n\tds_read_b128 a[92:95], v30 offset:7168\n\tds_read_b128 a[96:99], v31 offset:0\n\t"
      "buffer_store_dwordx4 v[36:39], v10, s[8:11], s43 offen offset:0 sc1\n\tds_read_b128 a[100:103], v31 offset:1024\n\tds_read_b128 a[104:107], v31 offset:2048\n\t"
      "buffer_store_dwordx4 v[36:39], v10, s[8:11], s43 offen offset:1024 sc1\n\tds_read_b128 a[108:111], v31 offset:3072\n\tds_read_b128 a[112:115], v31 offset:4096\n\t"
      "buffer_store_dwordx4 v[36:39], v11, s[8:11], s43 offen offset:0 sc1\n\tds_read_b128 a[116:119], v31 offset:5120\n\tds_read_b128 a[120:123], v31 offset:6144\n\t"
      "buffer_store_dwordx4 v[36:39], v11, s[8:11], s43 offen offset:1024 sc1\n\tds_read_b128 a[124:127], v31 offset:7168\n\ts_mov_b32 s64, s42\n\ts_mov_b32 s65, s43\n\t"
      "s_lshl_b32 s41, s48, 18\n\ts_add_u32 s41, s41, 0x208000\n\ts_add_u32 s48, s17, 1\n\ts_and_b32 s48, s48, 3\n\ts_lshl_b32 s40, s48, 18\n\ts_add_u32 s40, s40, 0x200000\n\t"
      "s_xor_b32 s42, s40, 0x80000\n\ts_and_b32 s43, s17, 1\n\ts_lshl_b32 s43, s43, 20\n\tv_mov_b64 v[116:117], 0\n\tv_mov_b64 v[118:119], 0\n\tv_mov_b64 v[120:121], 0\n\t"
      "v_mov_b64 v[122:123], 0\n\ts_add_u32 s46, s19, s29\n\ts_add_u32 s46, s46, s63\n\t"
      TK0
      STC("s_mov_b32 s57, s18\n\t")
      "71:\n\ts_memrealtime vcc\n\ts_waitcnt lgkmcnt(0)\n\ts_lshl_b32 vcc_lo, vcc_lo, 2\n\t"
      "70:\n\ts_sub_u32 s48, s46, vcc_lo\n\ts_cmp_gt_u32 s48, 4000\n\ts_cbranch_scc1 73f\n\ts_cmp_gt_u32 s48, 28\n\ts_cbranch_scc1 71b\n\ts_mul_i32 s48, s48, 45\n\t"
      "s_lshr_b32 s48, s48, 5\n\ts_lshl_b32 s48, s48, 2\n\ts_getpc_b64 vcc\n\t"
      "72:\n\ts_sub_u32 s48, 73f-72b, s48\n\ts_add_u32 vcc_lo, vcc_lo, s48\n\ts_addc_u32 vcc_hi, vcc_hi, 0\n\ts_setpc_b64 vcc\n\t.rept 40\n\ts_nop 0\n\t.endr\n\t"
      "73:\n\tbuffer_load_dword v112, v32, s[8:11], s41 offen sc1\n\ts_waitcnt vmcnt(0)\n\ts_branch 76f\n\t"
      "74:\n\tbuffer_load_dword v112, v32, s[8:11], s41 offen sc1\n\ts_waitcnt vmcnt(0)\n\t"
      "76:\n\ts_memrealtime vcc\n\tv_max_u32_dpp v112, v112, v112 row_ror:8 row_mask:0xf bank_mask:0xf\n\ts_nop 1\n\t"
      "v_max_u32_dpp v112, v112, v112 row_ror:4 row_mask:0xf bank_mask:0xf\n\ts_nop 1\n\tv_max_u32_dpp v112, v112, v112 row_ror:2 row_mask:0xf bank_mask:0xf\n\ts_nop 1\n\t"
      "v_max_u32_dpp v112, v112, v112 row_ror:1 row_mask:0xf bank_mask:0xf\n\ts_nop 0\n\tv_readlane_b32 s52, v112, 0\n\tv_readlane_b32 s48, v112, 16\n\t"
      "s_max_u32 s52, s52, s48\n\ts_cmp_lg_u32 s52, -1\n\ts_cbranch_scc1 75f\n\ts_waitcnt lgkmcnt(0)\n\ts_cmp_eq_u32 s18, 0\n\ts_cbranch_scc1 75f\n\ts_sub_u32 s18, s18, 1\n\t"
      "s_branch 74b\n\t"
      "75:\n\ts_add_u32 s47, s52, s27\n\t"
      STC("s_waitcnt lgkmcnt(0)\n\ts_mov_b32 s58, vcc_lo\n\ts_sub_u32 s56, s57, s18\n\ts_lshl_b32 s56, s56, 24\n\t")
      TK1
      "s_waitcnt lgkmcnt(0)\n\ts_lshl2_add_u32 vcc_lo, vcc_lo, 10\n\ts_branch 70f\n\t"
      "71:\n\ts_memrealtime vcc\n\ts_waitcnt lgkmcnt(0)\n\ts_lshl_b32 vcc_lo, vcc_lo, 2\n\t"
      "70:\n\ts_sub_u32 s48, s47, vcc_lo\n\ts_cmp_gt_u32 s48, 4000\n\ts_cbranch_scc1 73f\n\ts_cmp_gt_u32 s48, 28\n\ts_cbranch_scc1 71b\n\ts_mul_i32 s48, s48, 45\n\t"
      "s_lshr_b32 s48, s48, 5\n\ts_lshl_b32 s48, s48, 2\n\ts_getpc_b64 vcc\n\t"
      "72:\n\ts_sub_u32 s48, 73f-72b, s48\n\ts_add_u32 vcc_lo, vcc_lo, s48\n\ts_addc_u32 vcc_hi, vcc_hi, 0\n\ts_setpc_b64 vcc\n\t.rept 40\n\ts_nop 0\n\t.endr\n\t"
      "73:\n\t"
      "50:\n\t"
      TK2
      "buffer_load_dwordx4 v[176:179], v4, s[8:11], s41 offen offset:0 sc1\n\tbuffer_load_dwordx4 v[144:147], v8, s[8:11], s43 offen offset:0 sc1\n\t"
      "buffer_load_dwordx4 v[148:151], v8, s[8:11], s43 offen offset:1024 sc1\n\tbuffer_load_dwordx4 v[192:195], v0, s[8:11], s41 offen offset:0 sc1\n\t"
      "buffer_load_dwordx4 v[196:199], v0, s[8:11], s41 offen offset:1024 sc1\n\tbuffer_load_dwordx4 v[180:183], v5, s[8:11], s41 offen offset:0 sc1\n\t"
      "buffer_load_dwordx4 v[200:203], v0, s[8:11], s41 offen offset:2048 sc1\n\tbuffer_load_dwordx4 v[204:207], v0, s[8:11], s41 offen offset:3072 sc1\n\t"
      "buffer_load_dwordx4 v[152:155], v9, s[8:11], s43 offen offset:0 sc1\n\tbuffer_load_dwordx4 v[156:159], v9, s[8:11], s43 offen offset:1024 sc1\n\t"
      "buffer_load_dwordx4 v[208:211], v1, s[8:11], s41 offen offset:0 sc1\n\tbuffer_load_dwordx4 v[212:215], v1, s[8:11], s41 offen offset:1024 sc1\n\t"
      "buffer_load_dwordx4 v[184:187], v6, s[8:11], s41 offen offset:0 sc1\n\tbuffer_load_dwordx4 v[216:219], v1, s[8:11], s41 offen offset:2048 sc1\n\t"
      "buffer_load_dwordx4 v[220:223], v1, s[8:11], s41 offen offset:3072 sc1\n\tbuffer_load_dwordx4 v[160:163], v10, s[8:11], s43 offen offset:0 sc1\n\t"
      "buffer_load_dwordx4 v[164:167], v10, s[8:11], s43 offen offset:1024 sc1\n\tbuffer_load_dwordx4 v[224:227], v2, s[8:11], s41 offen offset:0 sc1\n\t"
      "buffer_load_dwordx4 v[228:231], v2, s[8:11], s41 offen offset:1024 sc1\n\tbuffer_load_dwordx4 v[188:191], v7, s[8:11], s41 offen offset:0 sc1\n\t"
      "buffer_load_dwordx4 v[232:235], v2, s[8:11], s41 offen offset:2048 sc1\n\tbuffer_load_dwordx4 v[236:239], v2, s[8:11], s41 offen offset:3072 sc1\n\t"
      "buffer_load_dwordx4 v[168:171], v11, s[8:11], s43 offen offset:0 sc1\n\tbuffer_load_dwordx4 v[172:175], v11, s[8:11], s43 offen offset:1024 sc1\n\t"
      "buffer_load_dwordx4 v[240:243], v3, s[8:11], s41 offen offset:0 sc1\n\tbuffer_load_dwordx4 v[244:247], v3, s[8:11], s41 offen offset:1024 sc1\n\t"
      "buffer_load_dwordx4 v[248:251], v3, s[8:11], s41 offen offset:2048 sc1\n\tbuffer_load_dwordx4 v[252:255], v3, s[8:11], s41 offen offset:3072 sc1\n\t"
      "s_mov_b32 vcc_lo, 0x00ff00ff\n\ts_mov_b32 vcc_hi, 0x00ff00ff\n\t"
      TK3
      "s_waitcnt vmcnt(27)\n\t"
      TK4
      "v_cndmask_b32_dpp v128, v178, v176, vcc row_ror:8 row_mask:0xf bank_mask:0xf\n\tv_cndmask_b32_dpp v129, v179, v177, vcc row_ror:8 row_mask:0xf bank_mask:0xf\n\t"
      "s_not_b64 vcc, vcc\n\tv_cndmask_b32_dpp v130, v176, v178, vcc row_ror:8 row_mask:0xf bank_mask:0xf\n\ts_waitcnt vmcnt(24)\n\t"
      "v_smfmac_f32_16x16x64_bf16 v[120:123], v[192:195], a[0:7], v22\n\ts_waitcnt vmcnt(23)\n\tv_cndmask_b32_dpp v131, v177, v179, vcc row_ror:8 row_mask:0xf bank_mask:0xf\n\t"
      "v_smfmac_f32_16x16x64_bf16 v[120:123], v[196:199], a[8:15], v22\n\ts_nop 0\n\tv_smfmac_f32_16x16x64_bf16 v[120:123], v[128:131], v[144:151], v22\n\t"
      "s_waitcnt vmcnt(21)\n\tv_cndmask_b32_dpp v134, v180, v182, vcc row_ror:8 row_mask:0xf bank_mask:0xf\n\t"
      "v_smfmac_f32_16x16x64_bf16 v[120:123], v[200:203], a[16:23], v22\n\ts_waitcnt vmcnt(20)\n\t"
      "v_cndmask_b32_dpp v135, v181, v183, vcc row_ror:8 row_mask:0xf bank_mask:0xf\n\tv_smfmac_f32_16x16x64_bf16 v[120:123], v[204:207], a[24:31], v22\n\t"
      "s_waitcnt vmcnt(17)\n\ts_not_b64 vcc, vcc\n\tv_cndmask_b32_dpp v132, v182, v180, vcc row_ror:8 row_mask:0xf bank_mask:0xf\n\t"
      "v_smfmac_f32_16x16x64_bf16 v[120:123], v[208:211], a[32:39], v22\n\ts_waitcnt vmcnt(16)\n\t"
      "v_cndmask_b32_dpp v133, v183, v181, vcc row_ror:8 row_mask:0xf bank_mask:0xf\n\tv_smfmac_f32_16x16x64_bf16 v[120:123], v[212:215], a[40:47], v22\n\ts_nop 0\n\t"
      "v_smfmac_f32_16x16x64_bf16 v[120:123], v[132:135], v[152:159], v22\n\ts_waitcnt vmcnt(14)\n\t"
      "v_cndmask_b32_dpp v136, v186, v184, vcc row_ror:8 row_mask:0xf bank_mask:0xf\n\tv_smfmac_f32_16x16x64_bf16 v[120:123], v[216:219], a[48:55], v22\n\t"
      "s_waitcnt vmcnt(13)\n\tv_cndmask_b32_dpp v137, v187, v185, vcc row_ror:8 row_mask:0xf bank_mask:0xf\n\t"
      "v_smfmac_f32_16x16x64_bf16 v[120:123], v[220:223], a[56:63], v22\n\ts_waitcnt vmcnt(10)\n\ts_not_b64 vcc, vcc\n\t"
      "v_cndmask_b32_dpp v138, v184, v186, vcc row_ror:8 row_mask:0xf bank_mask:0xf\n\tv_smfmac_f32_16x16x64_bf16 v[120:123], v[224:227], a[64:71], v22\n\ts_waitcnt vmcnt(9)\n\t"
      "v_cndmask_b32_dpp v139, v185, v187, vcc row_ror:8 row_mask:0xf bank_mask:0xf\n\tv_smfmac_f32_16x16x64_bf16 v[120:123], v[228:231], a[72:79], v22\n\ts_nop 0\n\t"
      "v_smfmac_f32_16x16x64_bf16 v[120:123], v[136:139], v[160:167], v22\n\ts_waitcnt vmcnt(7)\n\t"
      "v_cndmask_b32_dpp v142, v188, v190, vcc row_ror:8 row_mask:0xf bank_mask:0xf\n\tv_smfmac_f32_16x16x64_bf16 v[120:123], v[232:235], a[80:87], v22\n\ts_waitcnt vmcnt(6)\n\t"
      "v_cndmask_b32_dpp v143, v189, v191, vcc row_ror:8 row_mask:0xf bank_mask:0xf\n\tv_smfmac_f32_16x16x64_bf16 v[120:123], v[236:239], a[88:95], v22\n\ts_waitcnt vmcnt(3)\n\t"
      "s_not_b64 vcc, vcc\n\tv_cndmask_b32_dpp v140, v190, v188, vcc row_ror:8 row_mask:0xf bank_mask:0xf\n\t"
      "v_smfmac_f32_16x16x64_bf16 v[120:123], v[240:243], a[96:103], v22\n\ts_waitcnt vmcnt(2)\n\t"
      "v_cndmask_b32_dpp v141, v191, v189, vcc row_ror:8 row_mask:0xf bank_mask:0xf\n\tv_smfmac_f32_16x16x64_bf16 v[120:123], v[244:247], a[104:111], v22\n\ts_nop 0\n\t"
      "v_smfmac_f32_16x16x64_bf16 v[120:123], v[140:143], v[168:175], v22\n\ts_waitcnt vmcnt(1)\n\tv_smfmac_f32_16x16x64_bf16 v[120:123], v[248:251], a[112:119], v22\n\t"
      "s_waitcnt vmcnt(0)\n\tv_smfmac_f32_16x16x64_bf16 v[120:123], v[252:255], a[120:127], v22\n\t"
      TK5
      "s_nop 7\n\tv_permlane32_swap_b32 v120, v122\n\tv_permlane32_swap_b32 v121, v123\n\tv_add_f32 v124, v120, v122\n\tv_add_f32 v125, v121, v123\n\t"
      "v_cvt_pk_bf16_f32 v126, v124, v125\n\tv_cmp_u_f32 vcc, v124, v125\n\ts_nop 0\n\tv_mov_b32_dpp v127, v126 quad_perm:[1,0,3,2] row_mask:0xf bank_mask:0xf\n\t"
      "v_perm_b32 v126, v127, v126, v21\n\ts_cbranch_vccnz 60f\n\t"
      "51:\n\tbuffer_store_dword v126, v20, s[8:11], s40 offen sc1\n\t"
      TK6
      STS("s_memrealtime s[54:55]\n\t")
      "s_add_u32 s49, s17, 1\n\tv_mov_b32 v26, s49\n\tds_write_b32 v24, v26 offset:16\n\ts_sub_u32 s49, s17, 3\n\t"
      "22:\n\tds_read_b32 v25, v24 offset:8\n\ts_waitcnt lgkmcnt(0)\n\tv_readfirstlane_b32 s48, v25\n\ts_cmp_gt_i32 s48, s49\n\ts_cbranch_scc1 23f\n\ts_cmp_eq_u32 s18, 0\n\t"
      "s_cbranch_scc1 23f\n\ts_sub_u32 s18, s18, 1\n\ts_sleep 1\n\ts_branch 22b\n\t"
      "23:\n\t"
      "24:\n\tds_read_b32 v25, v24 offset:12\n\ts_waitcnt lgkmcnt(0)\n\tv_readfirstlane_b32 s48, v25\n\ts_cmp_gt_i32 s48, s49\n\ts_cbranch_scc1 25f\n\ts_cmp_eq_u32 s18, 0\n\t"
      "s_cbranch_scc1 25f\n\ts_sub_u32 s18, s18, 1\n\ts_sleep 1\n\ts_branch 24b\n\t"
      "25:\n\tv_add_u32 v28, s31, v16\n\tv_add_u32 v29, s31, v17\n\tv_add_u32 v30, s31, v18\n\tv_add_u32 v31, s31, v19\n\tds_write_b128 v28, v[144:147] offset:0\n\t"
      "ds_write_b128 v28, v[148:151] offset:1024\n\tds_write_b128 v29, v[152:155] offset:0\n\tds_write_b128 v29, v[156:159] offset:1024\n\t"
      "ds_write_b128 v30, v[160:163] offset:0\n\tds_write_b128 v30, v[164:167] offset:1024\n\tds_write_b128 v31, v[168:171] offset:0\n\t"
      "ds_write_b128 v31, v[172:175] offset:1024\n\ts_add_u32 s31, s31, 0x2000\n\ts_cmp_eq_u32 s31, 0x6000\n\ts_cselect_b32 s31, 0, s31\n\tds_write_b32 v24, v26 offset:4\n\t"
      STC("s_lshl_b32 s48, s17, 4\n\ts_add_u32 s48, s48, 161056\n\tv_mov_b32 v112, s48\n\ts_sub_u32 s48, s52, s19\n\tv_mov_b32 v116, s48\n\tv_mov_b32 v117, s54\n\tv_mov_b32 v118, s56\n\ts_lshl_b32 s58, s58, 2\n\ts_sub_u32 s58, s52, s58\n\tv_mov_b32 v119, s58\n\tds_write_b128 v112, v[116:119]\n\ts_lshl_b32 s48, s54, 2\n\ts_sub_u32 s48, s48, s52\n\ts_add_u32 s51, s51, s48\n\t")
      "s_sub_u32 s48, s52, s19\n\ts_sub_u32 s48, s48, s62\n\ts_cmp_gt_i32 s48, s72\n\ts_cselect_b32 s19, s73, 0\n\ts_cmp_gt_i32 s48, 40\n\ts_cselect_b32 s48, 40, s48\n\t"
      "s_ashr_i32 s48, s48, 2\n\ts_add_i32 s62, s62, s48\n\ts_min_u32 s62, s62, 1000\n\ts_mul_i32 s63, s62, 3\n\ts_lshr_b32 s63, s63, 3\n\ts_add_u32 s19, s19, s52\n\t"
      "s_add_u32 s19, s19, s23\n\ts_add_u32 s19, s19, s63\n\t"
      TK7
      STS("s_cmp_lt_i32 s17, 8\n\ts_cselect_b32 s50, 0, s50\n\ts_cselect_b32 s51, 0, s51\n\ts_cselect_b32 s61, 0, s61\n\t")
      TKS("s_waitcnt lgkmcnt(0)\n\ts_cmp_lt_i32 s17, 0\n\ts_cbranch_scc1 98f\n\ts_cmp_lg_u32 s16, s91\n\ts_cbranch_scc1 98f\n\ts_mov_b64 exec, 1\n\ts_lshl_b32 s48, s17, 4\n\ts_add_u32 s48, s48, 161056\n\tv_mov_b32 v25, s48\n\ts_sub_u32 s48, s74, s90\n\ts_min_u32 s48, s48, 0xffff\n\tv_mov_b32 v26, s48\n\tds_write_b16 v25, v26 offset:0\n\ts_sub_u32 s48, s76, s74\n\ts_min_u32 s48, s48, 0xffff\n\tv_mov_b32 v26, s48\n\tds_write_b16 v25, v26 offset:2\n\ts_sub_u32 s48, s78, s76\n\ts_min_u32 s48, s48, 0xffff\n\tv_mov_b32 v26, s48\n\tds_write_b16 v25, v26 offset:4\n\ts_sub_u32 s48, s80, s78\n\ts_min_u32 s48, s48, 0xffff\n\tv_mov_b32 v26, s48\n\tds_write_b16 v25, v26 offset:6\n\ts_sub_u32 s48, s82, s80\n\ts_min_u32 s48, s48, 0xffff\n\tv_mov_b32 v26, s48\n\tds_write_b16 v25, v26 offset:8\n\ts_sub_u32 s48, s84, s82\n\ts_min_u32 s48, s48, 0xffff\n\tv_mov_b32 v26, s48\n\tds_write_b16 v25, v26 offset:10\n\ts_sub_u32 s48, s86, s84\n\ts_min_u32 s48, s48, 0xffff\n\tv_mov_b32 v26, s48\n\tds_write_b16 v25, v26 offset:12\n\ts_sub_u32 s48, s88, s86\n\ts_min_u32 s48, s48, 0xffff\n\tv_mov_b32 v26, s48\n\tds_write_b16 v25, v26 offset:14\n\ts_mov_b64 exec, -1\n\t98:\n\ts_mov_b32 s90, s88\n\t")
      "s_add_u32 s17, s17, 1\n\ts_cmp_lt_u32 s17, 63\n\ts_cbranch_scc1 10b\n\ts_branch 40f\n\t"
      "60:\n\ts_cmp_eq_u32 s30, s17\n\ts_mov_b32 s30, s17\n\ts_cbranch_scc0 61f\n\tv_max3_u32 v115, v144, v145, v146\n\tv_max_u32 v115, v115, v147\n\t"
      "v_max3_u32 v115, v115, v148, v149\n\tv_max3_u32 v115, v115, v150, v151\n\tv_max3_u32 v115, v115, v152, v153\n\tv_max3_u32 v115, v115, v154, v155\n\t"
      "v_max3_u32 v115, v115, v156, v157\n\tv_max3_u32 v115, v115, v158, v159\n\tv_max3_u32 v115, v115, v160, v161\n\tv_max3_u32 v115, v115, v162, v163\n\t"
      "v_max3_u32 v115, v115, v164, v165\n\tv_max3_u32 v115, v115, v166, v167\n\tv_max3_u32 v115, v115, v168, v169\n\tv_max3_u32 v115, v115, v170, v171\n\t"
      "v_max3_u32 v115, v115, v172, v173\n\tv_max3_u32 v115, v115, v174, v175\n\tv_max3_u32 v115, v115, v176, v177\n\tv_max3_u32 v115, v115, v178, v179\n\t"
      "v_max3_u32 v115, v115, v180, v181\n\tv_max3_u32 v115, v115, v182, v183\n\tv_max3_u32 v115, v115, v184, v185\n\tv_max3_u32 v115, v115, v186, v187\n\t"
      "v_max3_u32 v115, v115, v188, v189\n\tv_max3_u32 v115, v115, v190, v191\n\tv_max3_u32 v115, v115, v192, v193\n\tv_max3_u32 v115, v115, v194, v195\n\t"
      "v_max3_u32 v115, v115, v196, v197\n\tv_max3_u32 v115, v115, v198, v199\n\tv_max3_u32 v115, v115, v200, v201\n\tv_max3_u32 v115, v115, v202, v203\n\t"
      "v_max3_u32 v115, v115, v204, v205\n\tv_max3_u32 v115, v115, v206, v207\n\tv_max3_u32 v115, v115, v208, v209\n\tv_max3_u32 v115, v115, v210, v211\n\t"
      "v_max3_u32 v115, v115, v212, v213\n\tv_max3_u32 v115, v115, v214, v215\n\tv_max3_u32 v115, v115, v216, v217\n\tv_max3_u32 v115, v115, v218, v219\n\t"
      "v_max3_u32 v115, v115, v220, v221\n\tv_max3_u32 v115, v115, v222, v223\n\tv_max3_u32 v115, v115, v224, v225\n\tv_max3_u32 v115, v115, v226, v227\n\t"
      "v_max3_u32 v115, v115, v228, v229\n\tv_max3_u32 v115, v115, v230, v231\n\tv_max3_u32 v115, v115, v232, v233\n\tv_max3_u32 v115, v115, v234, v235\n\t"
      "v_max3_u32 v115, v115, v236, v237\n\tv_max3_u32 v115, v115, v238, v239\n\tv_max3_u32 v115, v115, v240, v241\n\tv_max3_u32 v115, v115, v242, v243\n\t"
      "v_max3_u32 v115, v115, v244, v245\n\tv_max3_u32 v115, v115, v246, v247\n\tv_max3_u32 v115, v115, v248, v249\n\tv_max3_u32 v115, v115, v250, v251\n\t"
      "v_max3_u32 v115, v115, v252, v253\n\tv_max3_u32 v115, v115, v254, v255\n\tv_cmp_eq_u32 vcc, -1, v115\n\ts_cbranch_vccz 51b\n\ts_cmp_eq_u32 s18, 0\n\t"
      "s_cbranch_scc1 51b\n\ts_sub_u32 s18, s18, 1\n\t"
      "61:\n\tv_mov_b64 v[116:117], 0\n\tv_mov_b64 v[118:119], 0\n\tv_mov_b64 v[120:121], 0\n\tv_mov_b64 v[122:123], 0\n\t"
      STS("s_add_u32 s50, s50, 1\n\t")
      STC("s_add_u32 s56, s56, 1\n\t")
      "s_branch 50b\n\t"
      "40:\n\t"
      ".Lmk_end6:\n\ts_waitcnt vmcnt(0) lgkmcnt(0)\n\t"
      TKS("s_mul_i32 s48, s16, 72\n\ts_add_u32 s48, s48, 162080\n\tv_mov_b32 v224, s48\n\tds_read_b32 v225, v224 offset:0\n\ts_waitcnt lgkmcnt(0)\n\tv_readfirstlane_b32 s74, v225\n\tds_read_b32 v225, v224 offset:4\n\ts_waitcnt lgkmcnt(0)\n\tv_readfirstlane_b32 s75, v225\n\tds_read_b32 v225, v224 offset:8\n\ts_waitcnt lgkmcnt(0)\n\tv_readfirstlane_b32 s76, v225\n\tds_read_b32 v225, v224 offset:12\n\ts_waitcnt lgkmcnt(0)\n\tv_readfirstlane_b32 s77, v225\n\tds_read_b32 v225, v224 offset:16\n\ts_waitcnt lgkmcnt(0)\n\tv_readfirstlane_b32 s78, v225\n\tds_read_b32 v225, v224 offset:20\n\ts_waitcnt lgkmcnt(0)\n\tv_readfirstlane_b32 s79, v225\n\tds_read_b32 v225, v224 offset:24\n\ts_waitcnt lgkmcnt(0)\n\tv_readfirstlane_b32 s80, v225\n\tds_read_b32 v225, v224 offset:28\n\ts_waitcnt lgkmcnt(0)\n\tv_readfirstlane_b32 s81, v225\n\tds_read_b32 v225, v224 offset:32\n\ts_waitcnt lgkmcnt(0)\n\tv_readfirstlane_b32 s82, v225\n\tds_read_b32 v225, v224 offset:36\n\ts_waitcnt lgkmcnt(0)\n\tv_readfirstlane_b32 s83, v225\n\tds_read_b32 v225, v224 offset:40\n\ts_waitcnt lgkmcnt(0)\n\tv_readfirstlane_b32 s84, v225\n\tds_read_b32 v225, v224 offset:44\n\ts_waitcnt lgkmcnt(0)\n\tv_readfirstlane_b32 s85, v225\n\tds_read_b32 v225, v224 offset:48\n\ts_waitcnt lgkmcnt(0)\n\tv_readfirstlane_b32 s86, v225\n\tds_read_b32 v225, v224 offset:52\n\ts_waitcnt lgkmcnt(0)\n\tv_readfirstlane_b32 s87, v225\n\tds_read_b32 v225, v224 offset:56\n\ts_waitcnt lgkmcnt(0)\n\tv_readfirstlane_b32 s88, v225\n\tds_read_b32 v225, v224 offset:60\n\ts_waitcnt lgkmcnt(0)\n\tv_readfirstlane_b32 s89, v225\n\tds_read_b32 v225, v224 offset:64\n\ts_waitcnt lgkmcnt(0)\n\tv_readfirstlane_b32 s90, v225\n\tds_read_b32 v225, v224 offset:68\n\ts_waitcnt lgkmcnt(0)\n\tv_readfirstlane_b32 s91, v225\n\t")
      STS("s_lshl_b32 s48, s16, 2\n\ts_add_u32 s48, s48, 162944\n\tv_mov_b32 v0, s48\n\tv_mov_b32 v1, s50\n\tds_write_b32 v0, v1\n\ts_cmp_lg_u32 s16, 0\n\ts_cbranch_scc1 97f\n\tv_mov_b32 v1, s51\n\tds_write_b32 v0, v1 offset:16\n\t97:\n\ts_cmp_lg_u32 s16, 0\n\ts_cbranch_scc1 95f\n\tv_mov_b32 v1, s61\n\tds_write_b32 v0, v1 offset:20\n\tv_mov_b32 v1, s21\n\tds_write_b32 v0, v1 offset:24\n\tv_mov_b32 v1, s22\n\tds_write_b32 v0, v1 offset:28\n\t95:\n\t")
      "s_cmp_lg_u32 s18, 0\n\ts_cbranch_scc1 96f\n\tv_mov_b32 v1, 1\n\tbuffer_store_dword v1, off, s[12:15], 0 offset:3328 sc1\n\t"
      "96:\n\t"
      : "+{v[0:31]}"(k), "+{v[160:191]}"(s0), "=&{v[32:39]}"(x3), "=&{v[112:127]}"(x0), "=&{v[128:159]}"(x[0]), "=&{v[192:223]}"(x[1]), "=&{v[224:255]}"(x[2]),
        "=&{a[0:31]}"(z[0]), "=&{a[32:63]}"(z[1]), "=&{a[64:95]}"(z[2]), "=&{a[96:127]}"(z[3])
      :: "memory", "vcc", "scc", S_CLOB TK_CLOB);
  } else if (w < 3) {
    v16u k = {(unsigned)(par * 8192 + lane * 16) + ((HACK & 32) << 25), (unsigned)(UL + lane * 16), (unsigned)(ZR + (w == 2 ? lane * 16 : (g >> 1) * 1024 + (g & 1) * 256 + cc * 16)),
              (unsigned)((16 * (w - 1) + cc) * 4096 + (16 * y + 4 * g) * 2) | ((HACK & 1) << 30), CW};
    v32u s0 = {RSRCS, (unsigned)w, 0, c.budget, 0, plo(p->out), phi(p->out), 2048 * 2048 * 2, 0x00020000, 0, 0};
    v32u x[6];
#define TOK_REGS "+{v[0:15]}"(k), "+{v[160:191]}"(s0), "=&{v[32:63]}"(x[0]), "=&{v[64:95]}"(x[1]), "=&{v[96:127]}"(x[2]), "=&{v[128:159]}"(x[3]), "=&{v[192:223]}"(x[4]), "=&{v[224:255]}"(x[5])
    if (w == 1) asm volatile(
      "s_nop 0\n\tv_readfirstlane_b32 s8, v160\n\tv_readfirstlane_b32 s9, v161\n\tv_readfirstlane_b32 s10, v162\n\tv_readfirstlane_b32 s11, v163\n\t"
      "v_readfirstlane_b32 s12, v164\n\tv_readfirstlane_b32 s13, v165\n\tv_readfirstlane_b32 s14, v166\n\tv_readfirstlane_b32 s15, v167\n\tv_readfirstlane_b32 s16, v168\n\t"
      "v_readfirstlane_b32 s17, v169\n\tv_readfirstlane_b32 s18, v170\n\tv_readfirstlane_b32 s19, v171\n\tv_readfirstlane_b32 s20, v172\n\tv_readfirstlane_b32 s21, v173\n\t"
      "v_readfirstlane_b32 s22, v174\n\tv_readfirstlane_b32 s23, v175\n\tv_readfirstlane_b32 s24, v176\n\tv_readfirstlane_b32 s25, v177\n\ts_nop 4\n\t"
      TKS("s_mul_i32 s48, s16, 72\n\ts_add_u32 s48, s48, 162080\n\tv_mov_b32 v224, s48\n\tv_mov_b32 v225, s74\n\tds_write_b32 v224, v225 offset:0\n\tv_mov_b32 v225, s75\n\tds_write_b32 v224, v225 offset:4\n\tv_mov_b32 v225, s76\n\tds_write_b32 v224, v225 offset:8\n\tv_mov_b32 v225, s77\n\tds_write_b32 v224, v225 offset:12\n\tv_mov_b32 v225, s78\n\tds_write_b32 v224, v225 offset:16\n\tv_mov_b32 v225, s79\n\tds_write_b32 v224, v225 offset:20\n\tv_mov_b32 v225, s80\n\tds_write_b32 v224, v225 offset:24\n\tv_mov_b32 v225, s81\n\tds_write_b32 v224, v225 offset:28\n\tv_mov_b32 v225, s82\n\tds_write_b32 v224, v225 offset:32\n\tv_mov_b32 v225, s83\n\tds_write_b32 v224, v225 offset:36\n\tv_mov_b32 v225, s84\n\tds_write_b32 v224, v225 offset:40\n\tv_mov_b32 v225, s85\n\tds_write_b32 v224, v225 offset:44\n\tv_mov_b32 v225, s86\n\tds_write_b32 v224, v225 offset:48\n\tv_mov_b32 v225, s87\n\tds_write_b32 v224, v225 offset:52\n\tv_mov_b32 v225, s88\n\tds_write_b32 v224, v225 offset:56\n\tv_mov_b32 v225, s89\n\tds_write_b32 v224, v225 offset:60\n\tv_mov_b32 v225, s90\n\tds_write_b32 v224, v225 offset:64\n\tv_mov_b32 v225, s91\n\tds_write_b32 v224, v225 offset:68\n\t")
      TKS("s_mov_b32 s91, 0\n\t")
      "10:\n\ts_and_b32 s48, s17, 7\n\ts_lshl_b32 s43, s48, 17\n\ts_add_u32 s43, s43, 0x300000\n\ts_and_b32 s48, s17, 3\n\ts_lshl_b32 s48, s48, 15\n\tv_add_u32 v8, s48, v2\n\t"
      "v_add_u32 v7, s24, v1\n\ts_add_u32 s49, s17, 1\n\ts_min_u32 s49, s49, 62\n\t"
      "20:\n\tds_read_b32 v5, v4 offset:4\n\ts_waitcnt lgkmcnt(0)\n\tv_readfirstlane_b32 s48, v5\n\ts_cmp_gt_i32 s48, s17\n\ts_cbranch_scc1 21f\n\ts_cmp_eq_u32 s18, 0\n\t"
      "s_cbranch_scc1 21f\n\ts_sub_u32 s18, s18, 1\n\ts_sleep 1\n\ts_branch 20b\n\t"
      "21:\n\t"
      "22:\n\tds_read_b32 v5, v4 offset:0\n\ts_waitcnt lgkmcnt(0)\n\tv_readfirstlane_b32 s48, v5\n\ts_cmp_gt_i32 s48, s17\n\ts_cbranch_scc1 23f\n\ts_cmp_eq_u32 s18, 0\n\t"
      "s_cbranch_scc1 23f\n\ts_sub_u32 s18, s18, 1\n\ts_sleep 1\n\ts_branch 22b\n\t"
      "23:\n\t"
      "24:\n\tds_read_b32 v5, v4 offset:16\n\ts_waitcnt lgkmcnt(0)\n\tv_readfirstlane_b32 s48, v5\n\ts_cmp_gt_i32 s48, s49\n\ts_cbranch_scc1 25f\n\ts_cmp_eq_u32 s18, 0\n\t"
      "s_cbranch_scc1 25f\n\ts_sub_u32 s18, s18, 1\n\ts_sleep 1\n\ts_branch 24b\n\t"
      "25:\n\t"
      TK0
      "s_cmp_gt_u32 s17, 60\n\ts_cbranch_scc1 50f\n\ts_sleep 27\n\t"
      "50:\n\t"
      TK1
      "s_add_u32 s26, s43, 98304\n\tbuffer_load_dwordx4 v[64:67], v0, s[8:11], s26 offen offset:0 sc1\n\ts_nop 2\n\t"
      "buffer_load_dwordx4 v[68:71], v0, s[8:11], s26 offen offset:1024 sc1\n\ts_nop 2\n\tbuffer_load_dwordx4 v[72:75], v0, s[8:11], s26 offen offset:2048 sc1\n\ts_nop 2\n\t"
      "buffer_load_dwordx4 v[76:79], v0, s[8:11], s26 offen offset:3072 sc1\n\ts_nop 2\n\ts_add_u32 s26, s43, 102400\n\t"
      "buffer_load_dwordx4 v[80:83], v0, s[8:11], s26 offen offset:0 sc1\n\ts_nop 2\n\tbuffer_load_dwordx4 v[84:87], v0, s[8:11], s26 offen offset:1024 sc1\n\ts_nop 2\n\t"
      "buffer_load_dwordx4 v[88:91], v0, s[8:11], s26 offen offset:2048 sc1\n\ts_nop 2\n\tbuffer_load_dwordx4 v[92:95], v0, s[8:11], s26 offen offset:3072 sc1\n\ts_nop 2\n\t"
      "s_add_u32 s26, s43, 0\n\tbuffer_load_dwordx4 v[96:99], v0, s[8:11], s26 offen offset:0 sc1\n\ts_nop 2\n\t"
      "buffer_load_dwordx4 v[100:103], v0, s[8:11], s26 offen offset:1024 sc1\n\ts_nop 2\n\tbuffer_load_dwordx4 v[104:107], v0, s[8:11], s26 offen offset:2048 sc1\n\t"
      "s_nop 2\n\tbuffer_load_dwordx4 v[108:111], v0, s[8:11], s26 offen offset:3072 sc1\n\ts_nop 2\n\ts_add_u32 s26, s43, 4096\n\t"
      "buffer_load_dwordx4 v[112:115], v0, s[8:11], s26 offen offset:0 sc1\n\ts_nop 2\n\tbuffer_load_dwordx4 v[116:119], v0, s[8:11], s26 offen offset:1024 sc1\n\ts_nop 2\n\t"
      "buffer_load_dwordx4 v[120:123], v0, s[8:11], s26 offen offset:2048 sc1\n\ts_nop 2\n\tbuffer_load_dwordx4 v[124:127], v0, s[8:11], s26 offen offset:3072 sc1\n\t"
      "s_nop 2\n\ts_add_u32 s26, s43, 16384\n\tbuffer_load_dwordx4 v[128:131], v0, s[8:11], s26 offen offset:0 sc1\n\ts_nop 2\n\t"
      "buffer_load_dwordx4 v[132:135], v0, s[8:11], s26 offen offset:1024 sc1\n\ts_nop 2\n\tbuffer_load_dwordx4 v[136:139], v0, s[8:11], s26 offen offset:2048 sc1\n\t"
      "s_nop 2\n\tbuffer_load_dwordx4 v[140:143], v0, s[8:11], s26 offen offset:3072 sc1\n\ts_nop 2\n\t"
      TK2
      "ds_read_b128 v[32:35], v7 offset:0\n\tds_read_b128 v[36:39], v7 offset:1024\n\tds_read_b128 v[40:43], v7 offset:2048\n\ts_add_u32 s26, s43, 20480\n\t"
      "buffer_load_dwordx4 v[144:147], v0, s[8:11], s26 offen offset:0 sc1\n\tds_read_b128 v[44:47], v7 offset:3072\n\ts_waitcnt vmcnt(20) lgkmcnt(3)\n\t"
      TK3
      "v_mfma_f32_16x16x32_bf16 v[48:51], v[32:35], v[64:67], 0\n\tbuffer_load_dwordx4 v[148:151], v0, s[8:11], s26 offen offset:1024 sc1\n\t"
      "ds_read_b128 v[32:35], v7 offset:4096\n\ts_waitcnt vmcnt(20) lgkmcnt(3)\n\tv_mfma_f32_16x16x32_bf16 v[52:55], v[36:39], v[68:71], 0\n\t"
      "buffer_load_dwordx4 v[152:155], v0, s[8:11], s26 offen offset:2048 sc1\n\tds_read_b128 v[36:39], v7 offset:5120\n\ts_waitcnt vmcnt(20) lgkmcnt(3)\n\t"
      "v_mfma_f32_16x16x32_bf16 v[48:51], v[40:43], v[72:75], v[48:51]\n\tbuffer_load_dwordx4 v[156:159], v0, s[8:11], s26 offen offset:3072 sc1\n\t"
      "ds_read_b128 v[40:43], v7 offset:6144\n\ts_waitcnt vmcnt(20) lgkmcnt(3)\n\t"
      TK4
      "v_mfma_f32_16x16x32_bf16 v[52:55], v[44:47], v[76:79], v[52:55]\n\tds_read_b128 v[44:47], v7 offset:7168\n\ts_waitcnt vmcnt(19) lgkmcnt(3)\n\t"
      "v_mfma_f32_16x16x32_bf16 v[48:51], v[32:35], v[80:83], v[48:51]\n\tds_read2st64_b64 v[32:35], v8 offset0:0 offset1:1\n\ts_waitcnt vmcnt(18) lgkmcnt(3)\n\t"
      "v_mfma_f32_16x16x32_bf16 v[52:55], v[36:39], v[84:87], v[52:55]\n\tds_read2st64_b64 v[36:39], v8 offset0:4 offset1:5\n\ts_waitcnt vmcnt(17) lgkmcnt(3)\n\t"
      "v_mfma_f32_16x16x32_bf16 v[48:51], v[40:43], v[88:91], v[48:51]\n\tds_read2st64_b64 v[40:43], v8 offset0:8 offset1:9\n\ts_waitcnt vmcnt(16) lgkmcnt(3)\n\t"
      "v_mfma_f32_16x16x32_bf16 v[52:55], v[44:47], v[92:95], v[52:55]\n\tds_read2st64_b64 v[44:47], v8 offset0:12 offset1:13\n\ts_waitcnt vmcnt(15) lgkmcnt(3)\n\t"
      "v_mfma_f32_16x16x32_bf16 v[48:51], v[32:35], v[96:99], v[48:51]\n\tds_read2st64_b64 v[32:35], v8 offset0:16 offset1:17\n\ts_waitcnt vmcnt(14) lgkmcnt(3)\n\t"
      "v_mfma_f32_16x16x32_bf16 v[52:55], v[36:39], v[100:103], v[52:55]\n\tds_read2st64_b64 v[36:39], v8 offset0:20 offset1:21\n\ts_waitcnt vmcnt(13) lgkmcnt(3)\n\t"
      "v_mfma_f32_16x16x32_bf16 v[48:51], v[40:43], v[104:107], v[48:51]\n\tds_read2st64_b64 v[40:43], v8 offset0:24 offset1:25\n\ts_waitcnt vmcnt(12) lgkmcnt(3)\n\t"
      "v_mfma_f32_16x16x32_bf16 v[52:55], v[44:47], v[108:111], v[52:55]\n\tds_read2st64_b64 v[44:47], v8 offset0:28 offset1:29\n\ts_waitcnt vmcnt(11) lgkmcnt(3)\n\t"
      "v_mfma_f32_16x16x32_bf16 v[48:51], v[32:35], v[112:115], v[48:51]\n\tds_read2st64_b64 v[32:35], v8 offset0:32 offset1:33\n\ts_waitcnt vmcnt(10) lgkmcnt(3)\n\t"
      "v_mfma_f32_16x16x32_bf16 v[52:55], v[36:39], v[116:119], v[52:55]\n\tds_read2st64_b64 v[36:39], v8 offset0:36 offset1:37\n\ts_waitcnt vmcnt(9) lgkmcnt(3)\n\t"
      "v_mfma_f32_16x16x32_bf16 v[48:51], v[40:43], v[120:123], v[48:51]\n\tds_read2st64_b64 v[40:43], v8 offset0:40 offset1:41\n\ts_waitcnt vmcnt(8) lgkmcnt(3)\n\t"
      "v_mfma_f32_16x16x32_bf16 v[52:55], v[44:47], v[124:127], v[52:55]\n\tds_read2st64_b64 v[44:47], v8 offset0:44 offset1:45\n\ts_waitcnt vmcnt(7) lgkmcnt(3)\n\t"
      "v_mfma_f32_16x16x32_bf16 v[48:51], v[32:35], v[128:131], v[48:51]\n\tds_read2st64_b64 v[32:35], v8 offset0:48 offset1:49\n\ts_waitcnt vmcnt(6) lgkmcnt(3)\n\t"
      "v_mfma_f32_16x16x32_bf16 v[52:55], v[36:39], v[132:135], v[52:55]\n\tds_read2st64_b64 v[36:39], v8 offset0:52 offset1:53\n\ts_waitcnt vmcnt(5) lgkmcnt(3)\n\t"
      "v_mfma_f32_16x16x32_bf16 v[48:51], v[40:43], v[136:139], v[48:51]\n\tds_read2st64_b64 v[40:43], v8 offset0:56 offset1:57\n\ts_waitcnt vmcnt(4) lgkmcnt(3)\n\t"
      "v_mfma_f32_16x16x32_bf16 v[52:55], v[44:47], v[140:143], v[52:55]\n\tds_read2st64_b64 v[44:47], v8 offset0:60 offset1:61\n\ts_waitcnt vmcnt(3) lgkmcnt(3)\n\t"
      "v_mfma_f32_16x16x32_bf16 v[48:51], v[32:35], v[144:147], v[48:51]\n\ts_waitcnt vmcnt(2) lgkmcnt(2)\n\t"
      "v_mfma_f32_16x16x32_bf16 v[52:55], v[36:39], v[148:151], v[52:55]\n\ts_waitcnt vmcnt(1) lgkmcnt(1)\n\t"
      "v_mfma_f32_16x16x32_bf16 v[48:51], v[40:43], v[152:155], v[48:51]\n\ts_waitcnt vmcnt(0) lgkmcnt(0)\n\t"
      TK5
      "v_mfma_f32_16x16x32_bf16 v[52:55], v[44:47], v[156:159], v[52:55]\n\ts_add_u32 s49, s17, 1\n\tv_mov_b32 v6, s49\n\ts_nop 5\n\t"
      "v_pk_add_f32 v[48:49], v[48:49], v[52:53]\n\tv_pk_add_f32 v[50:51], v[50:51], v[54:55]\n\tv_cmp_u_f32 vcc, v48, v49\n\tv_cvt_pk_bf16_f32 v56, v48, v49\n\t"
      "v_cvt_pk_bf16_f32 v57, v50, v51\n\ts_cbranch_vccnz 60f\n\t"
      "51:\n\tds_write_b32 v4, v6 offset:8\n\tbuffer_store_dwordx2 v[56:57], v3, s[20:23], s25 offen \n\t"
      TK6
      TK7
      STS("s_cmp_lt_i32 s17, 8\n\ts_cselect_b32 s50, 0, s50\n\ts_cselect_b32 s51, 0, s51\n\ts_cselect_b32 s61, 0, s61\n\t")
      TKS("s_waitcnt lgkmcnt(0)\n\ts_cmp_lt_i32 s17, 0\n\ts_cbranch_scc1 98f\n\ts_cmp_lg_u32 s16, s91\n\ts_cbranch_scc1 98f\n\ts_mov_b64 exec, 1\n\ts_lshl_b32 s48, s17, 4\n\ts_add_u32 s48, s48, 161056\n\tv_mov_b32 v5, s48\n\ts_sub_u32 s48, s74, s90\n\ts_min_u32 s48, s48, 0xffff\n\tv_mov_b32 v6, s48\n\tds_write_b16 v5, v6 offset:0\n\ts_sub_u32 s48, s76, s74\n\ts_min_u32 s48, s48, 0xffff\n\tv_mov_b32 v6, s48\n\tds_write_b16 v5, v6 offset:2\n\ts_sub_u32 s48, s78, s76\n\ts_min_u32 s48, s48, 0xffff\n\tv_mov_b32 v6, s48\n\tds_write_b16 v5, v6 offset:4\n\ts_sub_u32 s48, s80, s78\n\ts_min_u32 s48, s48, 0xffff\n\tv_mov_b32 v6, s48\n\tds_write_b16 v5, v6 offset:6\n\ts_sub_u32 s48, s82, s80\n\ts_min_u32 s48, s48, 0xffff\n\tv_mov_b32 v6, s48\n\tds_write_b16 v5, v6 offset:8\n\ts_sub_u32 s48, s84, s82\n\ts_min_u32 s48, s48, 0xffff\n\tv_mov_b32 v6, s48\n\tds_write_b16 v5, v6 offset:10\n\ts_sub_u32 s48, s86, s84\n\ts_min_u32 s48, s48, 0xffff\n\tv_mov_b32 v6, s48\n\tds_write_b16 v5, v6 offset:12\n\ts_sub_u32 s48, s88, s86\n\ts_min_u32 s48, s48, 0xffff\n\tv_mov_b32 v6, s48\n\tds_write_b16 v5, v6 offset:14\n\ts_mov_b64 exec, -1\n\t98:\n\ts_mov_b32 s90, s88\n\t")
      "s_add_u32 s25, s25, 0x20000\n\ts_add_u32 s24, s24, 0x2000\n\ts_cmp_eq_u32 s24, 0x6000\n\ts_cselect_b32 s24, 0, s24\n\ts_add_u32 s17, s17, 1\n\ts_cmp_lt_u32 s17, 64\n\t"
      "s_cbranch_scc1 10b\n\ts_branch 40f\n\t"
      "60:\n\tv_max3_u32 v5, v64, v65, v66\n\tv_max_u32 v5, v5, v67\n\tv_max3_u32 v5, v5, v68, v69\n\tv_max3_u32 v5, v5, v70, v71\n\tv_max3_u32 v5, v5, v72, v73\n\t"
      "v_max3_u32 v5, v5, v74, v75\n\tv_max3_u32 v5, v5, v76, v77\n\tv_max3_u32 v5, v5, v78, v79\n\tv_max3_u32 v5, v5, v80, v81\n\tv_max3_u32 v5, v5, v82, v83\n\t"
      "v_max3_u32 v5, v5, v84, v85\n\tv_max3_u32 v5, v5, v86, v87\n\tv_max3_u32 v5, v5, v88, v89\n\tv_max3_u32 v5, v5, v90, v91\n\tv_max3_u32 v5, v5, v92, v93\n\t"
      "v_max3_u32 v5, v5, v94, v95\n\tv_max3_u32 v5, v5, v96, v97\n\tv_max3_u32 v5, v5, v98, v99\n\tv_max3_u32 v5, v5, v100, v101\n\tv_max3_u32 v5, v5, v102, v103\n\t"
      "v_max3_u32 v5, v5, v104, v105\n\tv_max3_u32 v5, v5, v106, v107\n\tv_max3_u32 v5, v5, v108, v109\n\tv_max3_u32 v5, v5, v110, v111\n\tv_max3_u32 v5, v5, v112, v113\n\t"
      "v_max3_u32 v5, v5, v114, v115\n\tv_max3_u32 v5, v5, v116, v117\n\tv_max3_u32 v5, v5, v118, v119\n\tv_max3_u32 v5, v5, v120, v121\n\tv_max3_u32 v5, v5, v122, v123\n\t"
      "v_max3_u32 v5, v5, v124, v125\n\tv_max3_u32 v5, v5, v126, v127\n\tv_max3_u32 v5, v5, v128, v129\n\tv_max3_u32 v5, v5, v130, v131\n\tv_max3_u32 v5, v5, v132, v133\n\t"
      "v_max3_u32 v5, v5, v134, v135\n\tv_max3_u32 v5, v5, v136, v137\n\tv_max3_u32 v5, v5, v138, v139\n\tv_max3_u32 v5, v5, v140, v141\n\tv_max3_u32 v5, v5, v142, v143\n\t"
      "v_max3_u32 v5, v5, v144, v145\n\tv_max3_u32 v5, v5, v146, v147\n\tv_max3_u32 v5, v5, v148, v149\n\tv_max3_u32 v5, v5, v150, v151\n\tv_max3_u32 v5, v5, v152, v153\n\t"
      "v_max3_u32 v5, v5, v154, v155\n\tv_max3_u32 v5, v5, v156, v157\n\tv_max3_u32 v5, v5, v158, v159\n\tv_cmp_eq_u32 vcc, -1, v5\n\ts_cbranch_vccz 51b\n\t"
      "s_cmp_eq_u32 s18, 0\n\ts_cbranch_scc1 51b\n\ts_sub_u32 s18, s18, 1\n\ts_branch 50b\n\t"
      "40:\n\t"
      ".Lmk_end0:\n\ts_waitcnt vmcnt(0) lgkmcnt(0)\n\t"
      TKS("s_mul_i32 s48, s16, 72\n\ts_add_u32 s48, s48, 162080\n\tv_mov_b32 v224, s48\n\tds_read_b32 v225, v224 offset:0\n\ts_waitcnt lgkmcnt(0)\n\tv_readfirstlane_b32 s74, v225\n\tds_read_b32 v225, v224 offset:4\n\ts_waitcnt lgkmcnt(0)\n\tv_readfirstlane_b32 s75, v225\n\tds_read_b32 v225, v224 offset:8\n\ts_waitcnt lgkmcnt(0)\n\tv_readfirstlane_b32 s76, v225\n\tds_read_b32 v225, v224 offset:12\n\ts_waitcnt lgkmcnt(0)\n\tv_readfirstlane_b32 s77, v225\n\tds_read_b32 v225, v224 offset:16\n\ts_waitcnt lgkmcnt(0)\n\tv_readfirstlane_b32 s78, v225\n\tds_read_b32 v225, v224 offset:20\n\ts_waitcnt lgkmcnt(0)\n\tv_readfirstlane_b32 s79, v225\n\tds_read_b32 v225, v224 offset:24\n\ts_waitcnt lgkmcnt(0)\n\tv_readfirstlane_b32 s80, v225\n\tds_read_b32 v225, v224 offset:28\n\ts_waitcnt lgkmcnt(0)\n\tv_readfirstlane_b32 s81, v225\n\tds_read_b32 v225, v224 offset:32\n\ts_waitcnt lgkmcnt(0)\n\tv_readfirstlane_b32 s82, v225\n\tds_read_b32 v225, v224 offset:36\n\ts_waitcnt lgkmcnt(0)\n\tv_readfirstlane_b32 s83, v225\n\tds_read_b32 v225, v224 offset:40\n\ts_waitcnt lgkmcnt(0)\n\tv_readfirstlane_b32 s84, v225\n\tds_read_b32 v225, v224 offset:44\n\ts_waitcnt lgkmcnt(0)\n\tv_readfirstlane_b32 s85, v225\n\tds_read_b32 v225, v224 offset:48\n\ts_waitcnt lgkmcnt(0)\n\tv_readfirstlane_b32 s86, v225\n\tds_read_b32 v225, v224 offset:52\n\ts_waitcnt lgkmcnt(0)\n\tv_readfirstlane_b32 s87, v225\n\tds_read_b32 v225, v224 offset:56\n\ts_waitcnt lgkmcnt(0)\n\tv_readfirstlane_b32 s88, v225\n\tds_read_b32 v225, v224 offset:60\n\ts_waitcnt lgkmcnt(0)\n\tv_readfirstlane_b32 s89, v225\n\tds_read_b32 v225, v224 offset:64\n\ts_waitcnt lgkmcnt(0)\n\tv_readfirstlane_b32 s90, v225\n\tds_read_b32 v225, v224 offset:68\n\ts_waitcnt lgkmcnt(0)\n\tv_readfirstlane_b32 s91, v225\n\t")
      STS("s_lshl_b32 s48, s16, 2\n\ts_add_u32 s48, s48, 162944\n\tv_mov_b32 v0, s48\n\tv_mov_b32 v1, s50\n\tds_write_b32 v0, v1\n\ts_cmp_lg_u32 s16, 0\n\ts_cbranch_scc1 97f\n\tv_mov_b32 v1, s51\n\tds_write_b32 v0, v1 offset:16\n\t97:\n\ts_cmp_lg_u32 s16, -1\n\ts_cbranch_scc1 95f\n\tv_mov_b32 v1, s61\n\tds_write_b32 v0, v1 offset:24\n\tv_mov_b32 v1, s21\n\tds_write_b32 v0, v1 offset:28\n\tv_mov_b32 v1, s22\n\tds_write_b32 v0, v1 offset:32\n\t95:\n\t")
      "s_cmp_lg_u32 s18, 0\n\ts_cbranch_scc1 96f\n\tv_mov_b32 v1, 1\n\tbuffer_store_dword v1, off, s[12:15], 0 offset:3328 sc1\n\t"
      "96:\n\t"
      : TOK_REGS :: "memory", "vcc", "scc", S_CLOB TK_CLOB);
    else asm volatile(
      "s_nop 0\n\tv_readfirstlane_b32 s8, v160\n\tv_readfirstlane_b32 s9, v161\n\tv_readfirstlane_b32 s10, v162\n\tv_readfirstlane_b32 s11, v163\n\t"
      "v_readfirstlane_b32 s12, v164\n\tv_readfirstlane_b32 s13, v165\n\tv_readfirstlane_b32 s14, v166\n\tv_readfirstlane_b32 s15, v167\n\tv_readfirstlane_b32 s16, v168\n\t"
      "v_readfirstlane_b32 s17, v169\n\tv_readfirstlane_b32 s18, v170\n\tv_readfirstlane_b32 s19, v171\n\tv_readfirstlane_b32 s20, v172\n\tv_readfirstlane_b32 s21, v173\n\t"
      "v_readfirstlane_b32 s22, v174\n\tv_readfirstlane_b32 s23, v175\n\tv_readfirstlane_b32 s24, v176\n\tv_readfirstlane_b32 s25, v177\n\ts_nop 4\n\t"
      TKS("s_mul_i32 s48, s16, 72\n\ts_add_u32 s48, s48, 162080\n\tv_mov_b32 v224, s48\n\tv_mov_b32 v225, s74\n\tds_write_b32 v224, v225 offset:0\n\tv_mov_b32 v225, s75\n\tds_write_b32 v224, v225 offset:4\n\tv_mov_b32 v225, s76\n\tds_write_b32 v224, v225 offset:8\n\tv_mov_b32 v225, s77\n\tds_write_b32 v224, v225 offset:12\n\tv_mov_b32 v225, s78\n\tds_write_b32 v224, v225 offset:16\n\tv_mov_b32 v225, s79\n\tds_write_b32 v224, v225 offset:20\n\tv_mov_b32 v225, s80\n\tds_write_b32 v224, v225 offset:24\n\tv_mov_b32 v225, s81\n\tds_write_b32 v224, v225 offset:28\n\tv_mov_b32 v225, s82\n\tds_write_b32 v224, v225 offset:32\n\tv_mov_b32 v225, s83\n\tds_write_b32 v224, v225 offset:36\n\tv_mov_b32 v225, s84\n\tds_write_b32 v224, v225 offset:40\n\tv_mov_b32 v225, s85\n\tds_write_b32 v224, v225 offset:44\n\tv_mov_b32 v225, s86\n\tds_write_b32 v224, v225 offset:48\n\tv_mov_b32 v225, s87\n\tds_write_b32 v224, v225 offset:52\n\tv_mov_b32 v225, s88\n\tds_write_b32 v224, v225 offset:56\n\tv_mov_b32 v225, s89\n\tds_write_b32 v224, v225 offset:60\n\tv_mov_b32 v225, s90\n\tds_write_b32 v224, v225 offset:64\n\tv_mov_b32 v225, s91\n\tds_write_b32 v224, v225 offset:68\n\t")
      TKS("s_mov_b32 s91, 0\n\t")
      "10:\n\ts_and_b32 s48, s17, 7\n\ts_lshl_b32 s43, s48, 17\n\ts_add_u32 s43, s43, 0x300000\n\ts_and_b32 s48, s17, 3\n\ts_lshl_b32 s48, s48, 15\n\tv_add_u32 v8, s48, v2\n\t"
      "v_add_u32 v7, s24, v1\n\ts_add_u32 s49, s17, 1\n\ts_min_u32 s49, s49, 62\n\t"
      "20:\n\tds_read_b32 v5, v4 offset:4\n\ts_waitcnt lgkmcnt(0)\n\tv_readfirstlane_b32 s48, v5\n\ts_cmp_gt_i32 s48, s17\n\ts_cbranch_scc1 21f\n\ts_cmp_eq_u32 s18, 0\n\t"
      "s_cbranch_scc1 21f\n\ts_sub_u32 s18, s18, 1\n\ts_sleep 1\n\ts_branch 20b\n\t"
      "21:\n\t"
      "22:\n\tds_read_b32 v5, v4 offset:0\n\ts_waitcnt lgkmcnt(0)\n\tv_readfirstlane_b32 s48, v5\n\ts_cmp_gt_i32 s48, s17\n\ts_cbranch_scc1 23f\n\ts_cmp_eq_u32 s18, 0\n\t"
      "s_cbranch_scc1 23f\n\ts_sub_u32 s18, s18, 1\n\ts_sleep 1\n\ts_branch 22b\n\t"
      "23:\n\t"
      "24:\n\tds_read_b32 v5, v4 offset:16\n\ts_waitcnt lgkmcnt(0)\n\tv_readfirstlane_b32 s48, v5\n\ts_cmp_gt_i32 s48, s49\n\ts_cbranch_scc1 25f\n\ts_cmp_eq_u32 s18, 0\n\t"
      "s_cbranch_scc1 25f\n\ts_sub_u32 s18, s18, 1\n\ts_sleep 1\n\ts_branch 24b\n\t"
      "25:\n\t"
      TK0
      "50:\n\t"
      TK1
      "s_add_u32 s26, s43, 114688\n\tbuffer_load_dwordx4 v[64:67], v0, s[8:11], s26 offen offset:0 sc1\n\ts_nop 4\n\t"
      "buffer_load_dwordx4 v[68:71], v0, s[8:11], s26 offen offset:1024 sc1\n\ts_nop 4\n\tbuffer_load_dwordx4 v[72:75], v0, s[8:11], s26 offen offset:2048 sc1\n\ts_nop 4\n\t"
      "buffer_load_dwordx4 v[76:79], v0, s[8:11], s26 offen offset:3072 sc1\n\ts_nop 4\n\ts_add_u32 s26, s43, 118784\n\t"
      "buffer_load_dwordx4 v[80:83], v0, s[8:11], s26 offen offset:0 sc1\n\ts_nop 4\n\tbuffer_load_dwordx4 v[84:87], v0, s[8:11], s26 offen offset:1024 sc1\n\ts_nop 4\n\t"
      "buffer_load_dwordx4 v[88:91], v0, s[8:11], s26 offen offset:2048 sc1\n\ts_nop 4\n\tbuffer_load_dwordx4 v[92:95], v0, s[8:11], s26 offen offset:3072 sc1\n\ts_nop 4\n\t"
      "s_add_u32 s26, s43, 32768\n\tbuffer_load_dwordx4 v[96:99], v0, s[8:11], s26 offen offset:0 sc1\n\ts_nop 4\n\t"
      "buffer_load_dwordx4 v[100:103], v0, s[8:11], s26 offen offset:1024 sc1\n\ts_nop 4\n\tbuffer_load_dwordx4 v[104:107], v0, s[8:11], s26 offen offset:2048 sc1\n\t"
      "s_nop 4\n\tbuffer_load_dwordx4 v[108:111], v0, s[8:11], s26 offen offset:3072 sc1\n\ts_nop 4\n\ts_add_u32 s26, s43, 36864\n\t"
      "buffer_load_dwordx4 v[112:115], v0, s[8:11], s26 offen offset:0 sc1\n\ts_nop 4\n\tbuffer_load_dwordx4 v[116:119], v0, s[8:11], s26 offen offset:1024 sc1\n\ts_nop 4\n\t"
      "buffer_load_dwordx4 v[120:123], v0, s[8:11], s26 offen offset:2048 sc1\n\ts_nop 4\n\tbuffer_load_dwordx4 v[124:127], v0, s[8:11], s26 offen offset:3072 sc1\n\t"
      "s_nop 4\n\ts_add_u32 s26, s43, 49152\n\tbuffer_load_dwordx4 v[128:131], v0, s[8:11], s26 offen offset:0 sc1\n\ts_nop 4\n\t"
      "buffer_load_dwordx4 v[132:135], v0, s[8:11], s26 offen offset:1024 sc1\n\ts_nop 4\n\tbuffer_load_dwordx4 v[136:139], v0, s[8:11], s26 offen offset:2048 sc1\n\t"
      "s_nop 4\n\tbuffer_load_dwordx4 v[140:143], v0, s[8:11], s26 offen offset:3072 sc1\n\ts_nop 4\n\t"
      TK2
      "ds_read_b128 v[32:35], v7 offset:0\n\tds_read_b128 v[36:39], v7 offset:1024\n\tds_read_b128 v[40:43], v7 offset:2048\n\ts_add_u32 s26, s43, 53248\n\t"
      "buffer_load_dwordx4 v[144:147], v0, s[8:11], s26 offen offset:0 sc1\n\tds_read_b128 v[44:47], v7 offset:3072\n\ts_waitcnt vmcnt(20) lgkmcnt(3)\n\t"
      TK3
      "v_mfma_f32_16x16x32_bf16 v[48:51], v[32:35], v[64:67], 0\n\tbuffer_load_dwordx4 v[148:151], v0, s[8:11], s26 offen offset:1024 sc1\n\t"
      "ds_read_b128 v[32:35], v7 offset:4096\n\ts_waitcnt vmcnt(20) lgkmcnt(3)\n\tv_mfma_f32_16x16x32_bf16 v[52:55], v[36:39], v[68:71], 0\n\t"
      "buffer_load_dwordx4 v[152:155], v0, s[8:11], s26 offen offset:2048 sc1\n\tds_read_b128 v[36:39], v7 offset:5120\n\ts_waitcnt vmcnt(20) lgkmcnt(3)\n\t"
      "v_mfma_f32_16x16x32_bf16 v[48:51], v[40:43], v[72:75], v[48:51]\n\tbuffer_load_dwordx4 v[156:159], v0, s[8:11], s26 offen offset:3072 sc1\n\t"
      "ds_read_b128 v[40:43], v7 offset:6144\n\ts_waitcnt vmcnt(20) lgkmcnt(3)\n\tv_mfma_f32_16x16x32_bf16 v[52:55], v[44:47], v[76:79], v[52:55]\n\t"
      "s_add_u32 s26, s43, 65536\n\tbuffer_load_dwordx4 v[160:163], v0, s[8:11], s26 offen offset:0 sc1\n\tds_read_b128 v[44:47], v7 offset:7168\n\t"
      "s_waitcnt vmcnt(20) lgkmcnt(3)\n\tv_mfma_f32_16x16x32_bf16 v[48:51], v[32:35], v[80:83], v[48:51]\n\t"
      "buffer_load_dwordx4 v[164:167], v0, s[8:11], s26 offen offset:1024 sc1\n\tds_read_b128 v[32:35], v8 offset:0\n\ts_waitcnt vmcnt(20) lgkmcnt(3)\n\t"
      "v_mfma_f32_16x16x32_bf16 v[52:55], v[36:39], v[84:87], v[52:55]\n\tbuffer_load_dwordx4 v[168:171], v0, s[8:11], s26 offen offset:2048 sc1\n\t"
      "ds_read_b128 v[36:39], v8 offset:1024\n\ts_waitcnt vmcnt(20) lgkmcnt(3)\n\tv_mfma_f32_16x16x32_bf16 v[48:51], v[40:43], v[88:91], v[48:51]\n\t"
      "buffer_load_dwordx4 v[172:175], v0, s[8:11], s26 offen offset:3072 sc1\n\tds_read_b128 v[40:43], v8 offset:2048\n\ts_waitcnt vmcnt(20) lgkmcnt(3)\n\t"
      "v_mfma_f32_16x16x32_bf16 v[52:55], v[44:47], v[92:95], v[52:55]\n\ts_add_u32 s26, s43, 69632\n\tbuffer_load_dwordx4 v[176:179], v0, s[8:11], s26 offen offset:0 sc1\n\t"
      "ds_read_b128 v[44:47], v8 offset:3072\n\ts_waitcnt vmcnt(20) lgkmcnt(3)\n\tv_mfma_f32_16x16x32_bf16 v[48:51], v[32:35], v[96:99], v[48:51]\n\t"
      "buffer_load_dwordx4 v[180:183], v0, s[8:11], s26 offen offset:1024 sc1\n\tds_read_b128 v[32:35], v8 offset:4096\n\ts_waitcnt vmcnt(20) lgkmcnt(3)\n\t"
      "v_mfma_f32_16x16x32_bf16 v[52:55], v[36:39], v[100:103], v[52:55]\n\tbuffer_load_dwordx4 v[184:187], v0, s[8:11], s26 offen offset:2048 sc1\n\t"
      "ds_read_b128 v[36:39], v8 offset:5120\n\ts_waitcnt vmcnt(20) lgkmcnt(3)\n\tv_mfma_f32_16x16x32_bf16 v[48:51], v[40:43], v[104:107], v[48:51]\n\t"
      "buffer_load_dwordx4 v[188:191], v0, s[8:11], s26 offen offset:3072 sc1\n\tds_read_b128 v[40:43], v8 offset:6144\n\ts_waitcnt vmcnt(20) lgkmcnt(3)\n\t"
      "v_mfma_f32_16x16x32_bf16 v[52:55], v[44:47], v[108:111], v[52:55]\n\ts_add_u32 s26, s43, 81920\n\tbuffer_load_dwordx4 v[192:195], v0, s[8:11], s26 offen offset:0 sc1\n\t"
      "ds_read_b128 v[44:47], v8 offset:7168\n\ts_waitcnt vmcnt(20) lgkmcnt(3)\n\tv_mfma_f32_16x16x32_bf16 v[48:51], v[32:35], v[112:115], v[48:51]\n\t"
      "buffer_load_dwordx4 v[196:199], v0, s[8:11], s26 offen offset:1024 sc1\n\tds_read_b128 v[32:35], v8 offset:8192\n\ts_waitcnt vmcnt(20) lgkmcnt(3)\n\t"
      "v_mfma_f32_16x16x32_bf16 v[52:55], v[36:39], v[116:119], v[52:55]\n\tbuffer_load_dwordx4 v[200:203], v0, s[8:11], s26 offen offset:2048 sc1\n\t"
      "ds_read_b128 v[36:39], v8 offset:9216\n\ts_waitcnt vmcnt(20) lgkmcnt(3)\n\tv_mfma_f32_16x16x32_bf16 v[48:51], v[40:43], v[120:123], v[48:51]\n\t"
      "buffer_load_dwordx4 v[204:207], v0, s[8:11], s26 offen offset:3072 sc1\n\tds_read_b128 v[40:43], v8 offset:10240\n\ts_waitcnt vmcnt(20) lgkmcnt(3)\n\t"
      "v_mfma_f32_16x16x32_bf16 v[52:55], v[44:47], v[124:127], v[52:55]\n\ts_add_u32 s26, s43, 86016\n\tbuffer_load_dwordx4 v[208:211], v0, s[8:11], s26 offen offset:0 sc1\n\t"
      "ds_read_b128 v[44:47], v8 offset:11264\n\ts_waitcnt vmcnt(20) lgkmcnt(3)\n\tv_mfma_f32_16x16x32_bf16 v[48:51], v[32:35], v[128:131], v[48:51]\n\t"
      "buffer_load_dwordx4 v[212:215], v0, s[8:11], s26 offen offset:1024 sc1\n\tds_read_b128 v[32:35], v8 offset:12288\n\ts_waitcnt vmcnt(20) lgkmcnt(3)\n\t"
      "v_mfma_f32_16x16x32_bf16 v[52:55], v[36:39], v[132:135], v[52:55]\n\tbuffer_load_dwordx4 v[216:219], v0, s[8:11], s26 offen offset:2048 sc1\n\t"
      "ds_read_b128 v[36:39], v8 offset:13312\n\ts_waitcnt vmcnt(20) lgkmcnt(3)\n\tv_mfma_f32_16x16x32_bf16 v[48:51], v[40:43], v[136:139], v[48:51]\n\t"
      "buffer_load_dwordx4 v[220:223], v0, s[8:11], s26 offen offset:3072 sc1\n\tds_read_b128 v[40:43], v8 offset:14336\n\ts_waitcnt vmcnt(20) lgkmcnt(3)\n\t"
      TK4
      "v_mfma_f32_16x16x32_bf16 v[52:55], v[44:47], v[140:143], v[52:55]\n\tds_read_b128 v[44:47], v8 offset:15360\n\ts_waitcnt vmcnt(19) lgkmcnt(3)\n\t"
      "v_mfma_f32_16x16x32_bf16 v[48:51], v[32:35], v[144:147], v[48:51]\n\tds_read_b128 v[32:35], v8 offset:16384\n\ts_waitcnt vmcnt(18) lgkmcnt(3)\n\t"
      "v_mfma_f32_16x16x32_bf16 v[52:55], v[36:39], v[148:151], v[52:55]\n\tds_read_b128 v[36:39], v8 offset:17408\n\ts_waitcnt vmcnt(17) lgkmcnt(3)\n\t"
      "v_mfma_f32_16x16x32_bf16 v[48:51], v[40:43], v[152:155], v[48:51]\n\tds_read_b128 v[40:43], v8 offset:18432\n\ts_waitcnt vmcnt(16) lgkmcnt(3)\n\t"
      "v_mfma_f32_16x16x32_bf16 v[52:55], v[44:47], v[156:159], v[52:55]\n\tds_read_b128 v[44:47], v8 offset:19456\n\ts_waitcnt vmcnt(15) lgkmcnt(3)\n\t"
      "v_mfma_f32_16x16x32_bf16 v[48:51], v[32:35], v[160:163], v[48:51]\n\tds_read_b128 v[32:35], v8 offset:20480\n\ts_waitcnt vmcnt(14) lgkmcnt(3)\n\t"
      "v_mfma_f32_16x16x32_bf16 v[52:55], v[36:39], v[164:167], v[52:55]\n\tds_read_b128 v[36:39], v8 offset:21504\n\ts_waitcnt vmcnt(13) lgkmcnt(3)\n\t"
      "v_mfma_f32_16x16x32_bf16 v[48:51], v[40:43], v[168:171], v[48:51]\n\tds_read_b128 v[40:43], v8 offset:22528\n\ts_waitcnt vmcnt(12) lgkmcnt(3)\n\t"
      "v_mfma_f32_16x16x32_bf16 v[52:55], v[44:47], v[172:175], v[52:55]\n\tds_read_b128 v[44:47], v8 offset:23552\n\ts_waitcnt vmcnt(11) lgkmcnt(3)\n\t"
      "v_mfma_f32_16x16x32_bf16 v[48:51], v[32:35], v[176:179], v[48:51]\n\tds_read_b128 v[32:35], v8 offset:24576\n\ts_waitcnt vmcnt(10) lgkmcnt(3)\n\t"
      "v_mfma_f32_16x16x32_bf16 v[52:55], v[36:39], v[180:183], v[52:55]\n\tds_read_b128 v[36:39], v8 offset:25600\n\ts_waitcnt vmcnt(9) lgkmcnt(3)\n\t"
      "v_mfma_f32_16x16x32_bf16 v[48:51], v[40:43], v[184:187], v[48:51]\n\tds_read_b128 v[40:43], v8 offset:26624\n\ts_waitcnt vmcnt(8) lgkmcnt(3)\n\t"
      "v_mfma_f32_16x16x32_bf16 v[52:55], v[44:47], v[188:191], v[52:55]\n\tds_read_b128 v[44:47], v8 offset:27648\n\ts_waitcnt vmcnt(7) lgkmcnt(3)\n\t"
      "v_mfma_f32_16x16x32_bf16 v[48:51], v[32:35], v[192:195], v[48:51]\n\tds_read_b128 v[32:35], v8 offset:28672\n\ts_waitcnt vmcnt(6) lgkmcnt(3)\n\t"
      "v_mfma_f32_16x16x32_bf16 v[52:55], v[36:39], v[196:199], v[52:55]\n\tds_read_b128 v[36:39], v8 offset:29696\n\ts_waitcnt vmcnt(5) lgkmcnt(3)\n\t"
      "v_mfma_f32_16x16x32_bf16 v[48:51], v[40:43], v[200:203], v[48:51]\n\tds_read_b128 v[40:43], v8 offset:30720\n\ts_waitcnt vmcnt(4) lgkmcnt(3)\n\t"
      "v_mfma_f32_16x16x32_bf16 v[52:55], v[44:47], v[204:207], v[52:55]\n\tds_read_b128 v[44:47], v8 offset:31744\n\ts_waitcnt vmcnt(3) lgkmcnt(3)\n\t"
      "v_mfma_f32_16x16x32_bf16 v[48:51], v[32:35], v[208:211], v[48:51]\n\ts_waitcnt vmcnt(2) lgkmcnt(2)\n\t"
      "v_mfma_f32_16x16x32_bf16 v[52:55], v[36:39], v[212:215], v[52:55]\n\ts_waitcnt vmcnt(1) lgkmcnt(1)\n\t"
      "v_mfma_f32_16x16x32_bf16 v[48:51], v[40:43], v[216:219], v[48:51]\n\ts_waitcnt vmcnt(0) lgkmcnt(0)\n\t"
      TK5
      "v_mfma_f32_16x16x32_bf16 v[52:55], v[44:47], v[220:223], v[52:55]\n\ts_add_u32 s49, s17, 1\n\tv_mov_b32 v6, s49\n\ts_nop 5\n\t"
      "v_pk_add_f32 v[48:49], v[48:49], v[52:53]\n\tv_pk_add_f32 v[50:51], v[50:51], v[54:55]\n\tv_cmp_u_f32 vcc, v48, v49\n\tv_cvt_pk_bf16_f32 v56, v48, v49\n\t"
      "v_cvt_pk_bf16_f32 v57, v50, v51\n\ts_cbranch_vccnz 60f\n\t"
      "51:\n\tds_write_b32 v4, v6 offset:12\n\tbuffer_store_dwordx2 v[56:57], v3, s[20:23], s25 offen \n\t"
      TK6
      TK7
      STS("s_cmp_lt_i32 s17, 8\n\ts_cselect_b32 s50, 0, s50\n\ts_cselect_b32 s51, 0, s51\n\ts_cselect_b32 s61, 0, s61\n\t")
      TKS("s_waitcnt lgkmcnt(0)\n\ts_cmp_lt_i32 s17, 0\n\ts_cbranch_scc1 98f\n\ts_cmp_lg_u32 s16, s91\n\ts_cbranch_scc1 98f\n\ts_mov_b64 exec, 1\n\ts_lshl_b32 s48, s17, 4\n\ts_add_u32 s48, s48, 161056\n\tv_mov_b32 v5, s48\n\ts_sub_u32 s48, s74, s90\n\ts_min_u32 s48, s48, 0xffff\n\tv_mov_b32 v6, s48\n\tds_write_b16 v5, v6 offset:0\n\ts_sub_u32 s48, s76, s74\n\ts_min_u32 s48, s48, 0xffff\n\tv_mov_b32 v6, s48\n\tds_write_b16 v5, v6 offset:2\n\ts_sub_u32 s48, s78, s76\n\ts_min_u32 s48, s48, 0xffff\n\tv_mov_b32 v6, s48\n\tds_write_b16 v5, v6 offset:4\n\ts_sub_u32 s48, s80, s78\n\ts_min_u32 s48, s48, 0xffff\n\tv_mov_b32 v6, s48\n\tds_write_b16 v5, v6 offset:6\n\ts_sub_u32 s48, s82, s80\n\ts_min_u32 s48, s48, 0xffff\n\tv_mov_b32 v6, s48\n\tds_write_b16 v5, v6 offset:8\n\ts_sub_u32 s48, s84, s82\n\ts_min_u32 s48, s48, 0xffff\n\tv_mov_b32 v6, s48\n\tds_write_b16 v5, v6 offset:10\n\ts_sub_u32 s48, s86, s84\n\ts_min_u32 s48, s48, 0xffff\n\tv_mov_b32 v6, s48\n\tds_write_b16 v5, v6 offset:12\n\ts_sub_u32 s48, s88, s86\n\ts_min_u32 s48, s48, 0xffff\n\tv_mov_b32 v6, s48\n\tds_write_b16 v5, v6 offset:14\n\ts_mov_b64 exec, -1\n\t98:\n\ts_mov_b32 s90, s88\n\t")
      "s_add_u32 s25, s25, 0x20000\n\ts_add_u32 s24, s24, 0x2000\n\ts_cmp_eq_u32 s24, 0x6000\n\ts_cselect_b32 s24, 0, s24\n\ts_add_u32 s17, s17, 1\n\ts_cmp_lt_u32 s17, 64\n\t"
      "s_cbranch_scc1 10b\n\ts_branch 40f\n\t"
      "60:\n\tv_max3_u32 v5, v64, v65, v66\n\tv_max_u32 v5, v5, v67\n\tv_max3_u32 v5, v5, v68, v69\n\tv_max3_u32 v5, v5, v70, v71\n\tv_max3_u32 v5, v5, v72, v73\n\t"
      "v_max3_u32 v5, v5, v74, v75\n\tv_max3_u32 v5, v5, v76, v77\n\tv_max3_u32 v5, v5, v78, v79\n\tv_max3_u32 v5, v5, v80, v81\n\tv_max3_u32 v5, v5, v82, v83\n\t"
      "v_max3_u32 v5, v5, v84, v85\n\tv_max3_u32 v5, v5, v86, v87\n\tv_max3_u32 v5, v5, v88, v89\n\tv_max3_u32 v5, v5, v90, v91\n\tv_max3_u32 v5, v5, v92, v93\n\t"
      "v_max3_u32 v5, v5, v94, v95\n\tv_max3_u32 v5, v5, v96, v97\n\tv_max3_u32 v5, v5, v98, v99\n\tv_max3_u32 v5, v5, v100, v101\n\tv_max3_u32 v5, v5, v102, v103\n\t"
      "v_max3_u32 v5, v5, v104, v105\n\tv_max3_u32 v5, v5, v106, v107\n\tv_max3_u32 v5, v5, v108, v109\n\tv_max3_u32 v5, v5, v110, v111\n\tv_max3_u32 v5, v5, v112, v113\n\t"
      "v_max3_u32 v5, v5, v114, v115\n\tv_max3_u32 v5, v5, v116, v117\n\tv_max3_u32 v5, v5, v118, v119\n\tv_max3_u32 v5, v5, v120, v121\n\tv_max3_u32 v5, v5, v122, v123\n\t"
      "v_max3_u32 v5, v5, v124, v125\n\tv_max3_u32 v5, v5, v126, v127\n\tv_max3_u32 v5, v5, v128, v129\n\tv_max3_u32 v5, v5, v130, v131\n\tv_max3_u32 v5, v5, v132, v133\n\t"
      "v_max3_u32 v5, v5, v134, v135\n\tv_max3_u32 v5, v5, v136, v137\n\tv_max3_u32 v5, v5, v138, v139\n\tv_max3_u32 v5, v5, v140, v141\n\tv_max3_u32 v5, v5, v142, v143\n\t"
      "v_max3_u32 v5, v5, v144, v145\n\tv_max3_u32 v5, v5, v146, v147\n\tv_max3_u32 v5, v5, v148, v149\n\tv_max3_u32 v5, v5, v150, v151\n\tv_max3_u32 v5, v5, v152, v153\n\t"
      "v_max3_u32 v5, v5, v154, v155\n\tv_max3_u32 v5, v5, v156, v157\n\tv_max3_u32 v5, v5, v158, v159\n\tv_max3_u32 v5, v5, v160, v161\n\tv_max3_u32 v5, v5, v162, v163\n\t"
      "v_max3_u32 v5, v5, v164, v165\n\tv_max3_u32 v5, v5, v166, v167\n\tv_max3_u32 v5, v5, v168, v169\n\tv_max3_u32 v5, v5, v170, v171\n\tv_max3_u32 v5, v5, v172, v173\n\t"
      "v_max3_u32 v5, v5, v174, v175\n\tv_max3_u32 v5, v5, v176, v177\n\tv_max3_u32 v5, v5, v178, v179\n\tv_max3_u32 v5, v5, v180, v181\n\tv_max3_u32 v5, v5, v182, v183\n\t"
      "v_max3_u32 v5, v5, v184, v185\n\tv_max3_u32 v5, v5, v186, v187\n\tv_max3_u32 v5, v5, v188, v189\n\tv_max3_u32 v5, v5, v190, v191\n\tv_max3_u32 v5, v5, v192, v193\n\t"
      "v_max3_u32 v5, v5, v194, v195\n\tv_max3_u32 v5, v5, v196, v197\n\tv_max3_u32 v5, v5, v198, v199\n\tv_max3_u32 v5, v5, v200, v201\n\tv_max3_u32 v5, v5, v202, v203\n\t"
      "v_max3_u32 v5, v5, v204, v205\n\tv_max3_u32 v5, v5, v206, v207\n\tv_max3_u32 v5, v5, v208, v209\n\tv_max3_u32 v5, v5, v210, v211\n\tv_max3_u32 v5, v5, v212, v213\n\t"
      "v_max3_u32 v5, v5, v214, v215\n\tv_max3_u32 v5, v5, v216, v217\n\tv_max3_u32 v5, v5, v218, v219\n\tv_max3_u32 v5, v5, v220, v221\n\tv_max3_u32 v5, v5, v222, v223\n\t"
      "v_cmp_eq_u32 vcc, -1, v5\n\ts_cbranch_vccz 51b\n\ts_cmp_eq_u32 s18, 0\n\ts_cbranch_scc1 51b\n\ts_sub_u32 s18, s18, 1\n\ts_branch 50b\n\t"
      "40:\n\t"
      ".Lmk_end1:\n\ts_waitcnt vmcnt(0) lgkmcnt(0)\n\t"
      TKS("s_mul_i32 s48, s16, 72\n\ts_add_u32 s48, s48, 162080\n\tv_mov_b32 v224, s48\n\tds_read_b32 v225, v224 offset:0\n\ts_waitcnt lgkmcnt(0)\n\tv_readfirstlane_b32 s74, v225\n\tds_read_b32 v225, v224 offset:4\n\ts_waitcnt lgkmcnt(0)\n\tv_readfirstlane_b32 s75, v225\n\tds_read_b32 v225, v224 offset:8\n\ts_waitcnt lgkmcnt(0)\n\tv_readfirstlane_b32 s76, v225\n\tds_read_b32 v225, v224 offset:12\n\ts_waitcnt lgkmcnt(0)\n\tv_readfirstlane_b32 s77, v225\n\tds_read_b32 v225, v224 offset:16\n\ts_waitcnt lgkmcnt(0)\n\tv_readfirstlane_b32 s78, v225\n\tds_read_b32 v225, v224 offset:20\n\ts_waitcnt lgkmcnt(0)\n\tv_readfirstlane_b32 s79, v225\n\tds_read_b32 v225, v224 offset:24\n\ts_waitcnt lgkmcnt(0)\n\tv_readfirstlane_b32 s80, v225\n\tds_read_b32 v225, v224 offset:28\n\ts_waitcnt lgkmcnt(0)\n\tv_readfirstlane_b32 s81, v225\n\tds_read_b32 v225, v224 offset:32\n\ts_waitcnt lgkmcnt(0)\n\tv_readfirstlane_b32 s82, v225\n\tds_read_b32 v225, v224 offset:36\n\ts_waitcnt lgkmcnt(0)\n\tv_readfirstlane_b32 s83, v225\n\tds_read_b32 v225, v224 offset:40\n\ts_waitcnt lgkmcnt(0)\n\tv_readfirstlane_b32 s84, v225\n\tds_read_b32 v225, v224 offset:44\n\ts_waitcnt lgkmcnt(0)\n\tv_readfirstlane_b32 s85, v225\n\tds_read_b32 v225, v224 offset:48\n\ts_waitcnt lgkmcnt(0)\n\tv_readfirstlane_b32 s86, v225\n\tds_read_b32 v225, v224 offset:52\n\ts_waitcnt lgkmcnt(0)\n\tv_readfirstlane_b32 s87, v225\n\tds_read_b32 v225, v224 offset:56\n\ts_waitcnt lgkmcnt(0)\n\tv_readfirstlane_b32 s88, v225\n\tds_read_b32 v225, v224 offset:60\n\ts_waitcnt lgkmcnt(0)\n\tv_readfirstlane_b32 s89, v225\n\tds_read_b32 v225, v224 offset:64\n\ts_waitcnt lgkmcnt(0)\n\tv_readfirstlane_b32 s90, v225\n\tds_read_b32 v225, v224 offset:68\n\ts_waitcnt lgkmcnt(0)\n\tv_readfirstlane_b32 s91, v225\n\t")
      STS("s_lshl_b32 s48, s16, 2\n\ts_add_u32 s48, s48, 162944\n\tv_mov_b32 v0, s48\n\tv_mov_b32 v1, s50\n\tds_write_b32 v0, v1\n\ts_cmp_lg_u32 s16, 0\n\ts_cbranch_scc1 97f\n\tv_mov_b32 v1, s51\n\tds_write_b32 v0, v1 offset:16\n\t97:\n\ts_cmp_lg_u32 s16, -1\n\ts_cbranch_scc1 95f\n\tv_mov_b32 v1, s61\n\tds_write_b32 v0, v1 offset:24\n\tv_mov_b32 v1, s21\n\tds_write_b32 v0, v1 offset:28\n\tv_mov_b32 v1, s22\n\tds_write_b32 v0, v1 offset:32\n\t95:\n\t")
      "s_cmp_lg_u32 s18, 0\n\ts_cbranch_scc1 96f\n\tv_mov_b32 v1, 1\n\tbuffer_store_dword v1, off, s[12:15], 0 offset:3328 sc1\n\t"
      "96:\n\t"
      : TOK_REGS :: "memory", "vcc", "scc", S_CLOB TK_CLOB);
  } else {
    unsigned seenZ = 0;
    gu32* f = c.ctrl + (C_FLAG + (F_Z + (y >> 4) * 32 + (lane & 31)) * 128) / 4;
    for (int n = 0; n < NSEG; ++n) {
      const int s = n; (void)s;
      if (n >= 4) { lds_wait(TDONE, n - 3); lds_wait(TDONE + 1, n - 3); }
      if (n >= 2) lds_wait(PUB, n - 1);
      const unsigned t0 = (unsigned)__builtin_amdgcn_s_memtime();
#pragma unroll 1
      while (seenZ < (unsigned)n + 1u) {
        unsigned v = __hip_atomic_load(f, RLX_AGENT);
#pragma unroll
        for (int o = 1; o < 32; o <<= 1) v = min(v, (unsigned)__shfl_xor((int)v, o));
        seenZ = __builtin_amdgcn_readfirstlane(v);
        if (seenZ >= (unsigned)n + 1u || spend(c)) break;
        __builtin_amdgcn_s_sleep(2);
      }
      if (n >= 2) {
#pragma unroll 1
        while ((unsigned)__builtin_amdgcn_s_memtime() - t0 < ZDLY * 64u) {}
      }
      unsigned so;
      asm volatile(
      "s_add_u32 m0, %[m], 0\n\ts_add_u32 %[so], %[zb], 0\n\tbuffer_load_dwordx4 %[o], %[rs], %[so] offen offset:0 sc1 lds\n\ts_nop 4\n\t"
      "buffer_load_dwordx4 %[o], %[rs], %[so] offen offset:1024 sc1 lds\n\ts_nop 4\n\tbuffer_load_dwordx4 %[o], %[rs], %[so] offen offset:2048 sc1 lds\n\ts_nop 4\n\t"
      "buffer_load_dwordx4 %[o], %[rs], %[so] offen offset:3072 sc1 lds\n\ts_nop 4\n\ts_add_u32 m0, %[m], 4096\n\ts_add_u32 %[so], %[zb], 4096\n\t"
      "buffer_load_dwordx4 %[o], %[rs], %[so] offen offset:0 sc1 lds\n\ts_nop 4\n\tbuffer_load_dwordx4 %[o], %[rs], %[so] offen offset:1024 sc1 lds\n\ts_nop 4\n\t"
      "buffer_load_dwordx4 %[o], %[rs], %[so] offen offset:2048 sc1 lds\n\ts_nop 4\n\tbuffer_load_dwordx4 %[o], %[rs], %[so] offen offset:3072 sc1 lds\n\ts_nop 4\n\t"
      "s_add_u32 m0, %[m], 8192\n\ts_add_u32 %[so], %[zb], 16384\n\tbuffer_load_dwordx4 %[o], %[rs], %[so] offen offset:0 sc1 lds\n\ts_nop 4\n\t"
      "buffer_load_dwordx4 %[o], %[rs], %[so] offen offset:1024 sc1 lds\n\ts_nop 4\n\tbuffer_load_dwordx4 %[o], %[rs], %[so] offen offset:2048 sc1 lds\n\ts_nop 4\n\t"
      "buffer_load_dwordx4 %[o], %[rs], %[so] offen offset:3072 sc1 lds\n\ts_nop 4\n\ts_add_u32 m0, %[m], 12288\n\ts_add_u32 %[so], %[zb], 20480\n\t"
      "buffer_load_dwordx4 %[o], %[rs], %[so] offen offset:0 sc1 lds\n\ts_nop 4\n\tbuffer_load_dwordx4 %[o], %[rs], %[so] offen offset:1024 sc1 lds\n\ts_nop 4\n\t"
      "buffer_load_dwordx4 %[o], %[rs], %[so] offen offset:2048 sc1 lds\n\ts_nop 4\n\tbuffer_load_dwordx4 %[o], %[rs], %[so] offen offset:3072 sc1 lds\n\ts_nop 4\n\t"
      "s_add_u32 m0, %[m], 16384\n\ts_add_u32 %[so], %[zb], 32768\n\tbuffer_load_dwordx4 %[o], %[rs], %[so] offen offset:0 sc1 lds\n\ts_nop 4\n\t"
      "buffer_load_dwordx4 %[o], %[rs], %[so] offen offset:1024 sc1 lds\n\ts_nop 4\n\tbuffer_load_dwordx4 %[o], %[rs], %[so] offen offset:2048 sc1 lds\n\ts_nop 4\n\t"
      "buffer_load_dwordx4 %[o], %[rs], %[so] offen offset:3072 sc1 lds\n\ts_nop 4\n\ts_add_u32 m0, %[m], 20480\n\ts_add_u32 %[so], %[zb], 36864\n\t"
      "buffer_load_dwordx4 %[o], %[rs], %[so] offen offset:0 sc1 lds\n\ts_nop 4\n\tbuffer_load_dwordx4 %[o], %[rs], %[so] offen offset:1024 sc1 lds\n\ts_nop 4\n\t"
      "buffer_load_dwordx4 %[o], %[rs], %[so] offen offset:2048 sc1 lds\n\ts_nop 4\n\tbuffer_load_dwordx4 %[o], %[rs], %[so] offen offset:3072 sc1 lds\n\ts_nop 4\n\t"
      "s_add_u32 m0, %[m], 24576\n\ts_add_u32 %[so], %[zb], 49152\n\tbuffer_load_dwordx4 %[o], %[rs], %[so] offen offset:0 sc1 lds\n\ts_nop 4\n\t"
      "buffer_load_dwordx4 %[o], %[rs], %[so] offen offset:1024 sc1 lds\n\ts_nop 4\n\tbuffer_load_dwordx4 %[o], %[rs], %[so] offen offset:2048 sc1 lds\n\ts_nop 4\n\t"
      "buffer_load_dwordx4 %[o], %[rs], %[so] offen offset:3072 sc1 lds\n\ts_nop 4\n\ts_add_u32 m0, %[m], 28672\n\ts_add_u32 %[so], %[zb], 53248\n\t"
      "buffer_load_dwordx4 %[o], %[rs], %[so] offen offset:0 sc1 lds\n\ts_nop 4\n\tbuffer_load_dwordx4 %[o], %[rs], %[so] offen offset:1024 sc1 lds\n\ts_nop 4\n\t"
      "buffer_load_dwordx4 %[o], %[rs], %[so] offen offset:2048 sc1 lds\n\ts_nop 4\n\tbuffer_load_dwordx4 %[o], %[rs], %[so] offen offset:3072 sc1 lds\n\ts_nop 4\n\t"
        : [so] "=&s"(so) : [o] "v"(lane * 16 + ((HACK & 8) << 27)), [rs] "s"(c.rs), [m] "s"(ZR + (n & 3) * 32768), [zb] "s"(R_Z + ((n % ZRN) * 64 + y) * 65536 + ((para ^ (y >> 5)) << 13)) : "memory", "scc");
      VMWAIT(0);
      lds_post(ZFULL, n + 1);
    }
    constexpr int s = NSEG - 1;
    const int uo = y * 16384 + para * 8192 + lane * 16, ub = R_U + (s & 1) * (64 * 16384);
    lds_wait(GO, s); lds_wait(TDONE, s - 2); lds_wait(TDONE + 1, s - 2);
    v4u u[8];
#pragma unroll 1
    for (;;) {
      asm volatile("" ::: "memory");
#pragma unroll
      for (int i = 0; i < 8; ++i) u[i] = ld16s(c.rs, uo + (i & 3) * 1024, ub + (i >> 2) * 4096);
      SCHED();
      unsigned t = 0;
#pragma unroll
      for (int i = 0; i < 8; ++i) t = umax(t, top(u[i]));
      if (allok(t) || spend(c)) break;
    }
#pragma unroll
    for (int i = 0; i < 8; ++i) sts16(c.L, UL + (s % 3) * 8192 + i * 1024 + lane * 16, u[i]);
    lds_post(GO, s + 1);
  }
  TICK_OUT();
  if (c.budget == 0u) __hip_atomic_store(c.ctrl + C_TMO / 4, 1u, RLX_AGENT);
}

DEV void g_role(Ctx& c, int mat, int h) {
  const KArg p = enter(c);
  c.budget = SPIN_CAP;
  const int w = c.w, lane = fresh_lane(), g = lane >> 4, h2 = lane >> 5, r32 = lane & 31, q = (lane >> 2) & 3, pp = lane & 3;
  v32u k;
#pragma unroll
  for (int i = 0; i < 8; ++i) {
    k[i] = r32 * 2048 + (((2 * i + h2) ^ (r32 & 15)) << 4); k[8 + i] = k[i] + 65536;
    k[16 + i] = (8 * w + i) * 2048 + ((lane ^ ((8 * w + i) & 15)) << 4) + ((i & 1) && (HACK & 128) ? 1 << 30 : 0);
  }
  k[24] = (4 * w + 2 * h2) * 512 + r32 * 16 + ((HACK & 4) << 28);
  k[25] = (((h & 7) * 4 + w) * 16 + (lane & 15)) * 128;
  k[26] = 0; k[27] = (lane | w) ? 1 << 30 : 0; k[28] = 0;
  k[29] = (16 * w + g) * 8192 + h * 256 + (((lane & 15) ^ ((g & 1) | (h2 << 3))) << 4);
  k[30] = 65536 + (8 * h2 + q) * 256 + (((8 * (w >> 1) + 4 * (g & 1) + 2 * (pp >> 1) + (w & 1)) ^ ((q & 1) | ((q >> 1) << 3))) << 4) + 8 * (pp & 1);
  k[31] = k[30] + 65536;
  const u16* W = mat == 0 ? p->Wq : mat == 1 ? p->Wk : p->Wv;
  v32u s0 = {RSRCS, (unsigned)w, 0, c.budget, (unsigned)(R_QX + mat * (R_KX - R_QX) + hblock(h, mat == 2 ? 8192 : QKFAR) + (GRN - 1) * 16384), 0, 4 << 16, (unsigned)(C_FLAG + (mat * 32 + h) * 128), mat == 0, mat != 2, 0, 0, mat == 2 ? GRN - 1 : L0, mat == 2 ? 0 : LA};
  v32u s1 = {plo(W), phi(W), 1024 * 4096 * 2, 0x00020000, plo(p->x), phi(p->x), 2048 * 2048, 0x00020000, 0, 0, (unsigned)(w * 16384), (unsigned)(65536 + w * 4096), 0, 0, 0, 0,
             0, 0, 0, 0, ENTRY, 0, 0, 0, 0, HELLO};
  v32u wa[8], wv[5];
  asm volatile(
      "s_nop 0\n\tv_readfirstlane_b32 s8, v160\n\tv_readfirstlane_b32 s9, v161\n\tv_readfirstlane_b32 s10, v162\n\tv_readfirstlane_b32 s11, v163\n\t"
      "v_readfirstlane_b32 s12, v164\n\tv_readfirstlane_b32 s13, v165\n\tv_readfirstlane_b32 s14, v166\n\tv_readfirstlane_b32 s15, v167\n\tv_readfirstlane_b32 s16, v168\n\t"
      "v_readfirstlane_b32 s17, v169\n\tv_readfirstlane_b32 s18, v170\n\tv_readfirstlane_b32 s19, v171\n\tv_readfirstlane_b32 s20, v172\n\tv_readfirstlane_b32 s21, v173\n\t"
      "v_readfirstlane_b32 s22, v174\n\tv_readfirstlane_b32 s23, v175\n\tv_readfirstlane_b32 s24, v176\n\tv_readfirstlane_b32 s27, v179\n\tv_readfirstlane_b32 s28, v180\n\t"
      "v_readfirstlane_b32 s40, v192\n\tv_readfirstlane_b32 s41, v193\n\tv_readfirstlane_b32 s42, v194\n\tv_readfirstlane_b32 s43, v195\n\tv_readfirstlane_b32 s44, v196\n\t"
      "v_readfirstlane_b32 s45, v197\n\tv_readfirstlane_b32 s46, v198\n\tv_readfirstlane_b32 s47, v199\n\tv_readfirstlane_b32 s50, v202\n\tv_readfirstlane_b32 s51, v203\n\t"
      "v_readfirstlane_b32 s60, v212\n\tv_readfirstlane_b32 s61, v213\n\tv_readfirstlane_b32 s62, v214\n\tv_readfirstlane_b32 s63, v215\n\tv_readfirstlane_b32 s68, v220\n\t"
      "v_readfirstlane_b32 s69, v221\n\tv_readfirstlane_b32 s70, v222\n\ts_nop 4\n\t"
      TKS("s_mul_i32 s48, s16, 72\n\ts_add_u32 s48, s48, 162080\n\tv_mov_b32 v224, s48\n\tv_mov_b32 v225, s74\n\tds_write_b32 v224, v225 offset:0\n\tv_mov_b32 v225, s75\n\tds_write_b32 v224, v225 offset:4\n\tv_mov_b32 v225, s76\n\tds_write_b32 v224, v225 offset:8\n\tv_mov_b32 v225, s77\n\tds_write_b32 v224, v225 offset:12\n\tv_mov_b32 v225, s78\n\tds_write_b32 v224, v225 offset:16\n\tv_mov_b32 v225, s79\n\tds_write_b32 v224, v225 offset:20\n\tv_mov_b32 v225, s80\n\tds_write_b32 v224, v225 offset:24\n\tv_mov_b32 v225, s81\n\tds_write_b32 v224, v225 offset:28\n\tv_mov_b32 v225, s82\n\tds_write_b32 v224, v225 offset:32\n\tv_mov_b32 v225, s83\n\tds_write_b32 v224, v225 offset:36\n\tv_mov_b32 v225, s84\n\tds_write_b32 v224, v225 offset:40\n\tv_mov_b32 v225, s85\n\tds_write_b32 v224, v225 offset:44\n\tv_mov_b32 v225, s86\n\tds_write_b32 v224, v225 offset:48\n\tv_mov_b32 v225, s87\n\tds_write_b32 v224, v225 offset:52\n\tv_mov_b32 v225, s88\n\tds_write_b32 v224, v225 offset:56\n\tv_mov_b32 v225, s89\n\tds_write_b32 v224, v225 offset:60\n\tv_mov_b32 v225, s90\n\tds_write_b32 v224, v225 offset:64\n\tv_mov_b32 v225, s91\n\tds_write_b32 v224, v225 offset:68\n\t")
      "s_buffer_load_dword s71, s[12:15], s69 glc\n\t"
      "s_cmp_eq_u32 s23, 0\n\ts_cbranch_scc1 8f\n\ts_waitcnt lgkmcnt(0)\n\ts_bitcmp0_b32 s68, 0\n\ts_cbranch_scc1 5f\n\ts_add_u32 s71, s71, 1\n\ts_lshl_b32 s48, s69, 4\n\t"
      "s_add_u32 s48, s48, 36864\n\tv_mov_b32 v28, s71\n\ts_mov_b64 exec, 1\n\tbuffer_store_dword v28, off, s[12:15], s48 sc1\n\ts_mov_b64 exec, -1\n\t"
      "5:\n\ts_cmp_lt_u32 s68, 1\n\ts_cbranch_scc1 7f\n\ts_cmp_eq_u32 s68, 2\n\ts_cbranch_scc1 6f\n\t"
      "5:\n\ts_buffer_load_dword s48, s[12:15], s69 glc\n\ts_waitcnt lgkmcnt(0)\n\ts_cmp_eq_u32 s48, s71\n\ts_cbranch_scc1 6f\n\ts_cmp_eq_u32 s18, 0\n\ts_cbranch_scc1 6f\n\t"
      "s_sub_u32 s18, s18, 1\n\ts_branch 5b\n\t"
      "6:\n\tv_mov_b32 v28, 1\n\ts_mov_b64 exec, 1\n\tbuffer_atomic_add v28, off, s[12:15], s70\n\ts_mov_b64 exec, -1\n\ts_mov_b32 s68, 0\n\t"
      "7:\n\ts_sleep 100\n\t"
      "8:\n\ts_add_u32 m0, s51, 0\n\ts_mov_b32 s48, 0x0\n\tbuffer_load_dwordx4 v29, s[40:43], s48 offen  lds\n\ts_add_u32 m0, s51, 1024\n\ts_mov_b32 s48, 0x8000\n\t"
      "buffer_load_dwordx4 v29, s[40:43], s48 offen  lds\n\ts_add_u32 m0, s51, 2048\n\ts_mov_b32 s48, 0x10000\n\tbuffer_load_dwordx4 v29, s[40:43], s48 offen  lds\n\t"
      "s_add_u32 m0, s51, 3072\n\ts_mov_b32 s48, 0x18000\n\tbuffer_load_dwordx4 v29, s[40:43], s48 offen  lds\n\ts_add_u32 m0, s51, 16384\n\ts_mov_b32 s48, 0x80000\n\t"
      "buffer_load_dwordx4 v29, s[40:43], s48 offen  lds\n\ts_add_u32 m0, s51, 17408\n\ts_mov_b32 s48, 0x88000\n\tbuffer_load_dwordx4 v29, s[40:43], s48 offen  lds\n\t"
      "s_add_u32 m0, s51, 18432\n\ts_mov_b32 s48, 0x90000\n\tbuffer_load_dwordx4 v29, s[40:43], s48 offen  lds\n\ts_add_u32 m0, s51, 19456\n\ts_mov_b32 s48, 0x98000\n\t"
      "buffer_load_dwordx4 v29, s[40:43], s48 offen  lds\n\ts_waitcnt lgkmcnt(0)\n\ts_bitcmp0_b32 s68, 0\n\ts_cbranch_scc1 5f\n\ts_add_u32 s71, s71, 1\n\t"
      "s_lshl_b32 s48, s69, 4\n\ts_add_u32 s48, s48, 36864\n\tv_mov_b32 v28, s71\n\ts_mov_b64 exec, 1\n\tbuffer_store_dword v28, off, s[12:15], s48 sc1\n\ts_mov_b64 exec, -1\n\t"
      "5:\n\ts_add_u32 m0, s51, 32768\n\ts_mov_b32 s48, 0x100000\n\tbuffer_load_dwordx4 v29, s[40:43], s48 offen  lds\n\ts_add_u32 m0, s51, 33792\n\ts_mov_b32 s48, 0x108000\n\t"
      "buffer_load_dwordx4 v29, s[40:43], s48 offen  lds\n\ts_add_u32 m0, s51, 34816\n\ts_mov_b32 s48, 0x110000\n\tbuffer_load_dwordx4 v29, s[40:43], s48 offen  lds\n\t"
      "s_add_u32 m0, s51, 35840\n\ts_mov_b32 s48, 0x118000\n\tbuffer_load_dwordx4 v29, s[40:43], s48 offen  lds\n\ts_add_u32 m0, s51, 49152\n\ts_mov_b32 s48, 0x180000\n\t"
      "buffer_load_dwordx4 v29, s[40:43], s48 offen  lds\n\ts_add_u32 m0, s51, 50176\n\ts_mov_b32 s48, 0x188000\n\tbuffer_load_dwordx4 v29, s[40:43], s48 offen  lds\n\t"
      "s_add_u32 m0, s51, 51200\n\ts_mov_b32 s48, 0x190000\n\tbuffer_load_dwordx4 v29, s[40:43], s48 offen  lds\n\ts_add_u32 m0, s51, 52224\n\ts_mov_b32 s48, 0x198000\n\t"
      "buffer_load_dwordx4 v29, s[40:43], s48 offen  lds\n\ts_cmp_lt_u32 s68, 1\n\ts_cbranch_scc1 7f\n\ts_cmp_eq_u32 s68, 2\n\ts_cbranch_scc1 6f\n\t"
      "5:\n\ts_buffer_load_dword s48, s[12:15], s69 glc\n\ts_waitcnt lgkmcnt(0)\n\ts_cmp_eq_u32 s48, s71\n\ts_cbranch_scc1 6f\n\ts_cmp_lg_u32 s68, 3\n\ts_cbranch_scc1 7f\n\t"
      "s_cmp_eq_u32 s18, 0\n\ts_cbranch_scc1 6f\n\ts_sub_u32 s18, s18, 1\n\ts_branch 5b\n\t"
      "6:\n\tv_mov_b32 v28, 1\n\ts_mov_b64 exec, 1\n\tbuffer_atomic_add v28, off, s[12:15], s70\n\ts_mov_b64 exec, -1\n\ts_mov_b32 s68, 0\n\t"
      "7:\n\ts_waitcnt vmcnt(12)\n\ts_barrier\n\tds_read_b64_tr_b16 a[0:1], v30 offset:0\n\tds_read_b64_tr_b16 a[2:3], v30 offset:1024\n\t"
      "ds_read_b64_tr_b16 a[4:5], v30 offset:4096\n\tds_read_b64_tr_b16 a[6:7], v30 offset:5120\n\tds_read_b64_tr_b16 a[8:9], v30 offset:8192\n\t"
      "ds_read_b64_tr_b16 a[10:11], v30 offset:9216\n\tds_read_b64_tr_b16 a[12:13], v30 offset:12288\n\tds_read_b64_tr_b16 a[14:15], v30 offset:13312\n\ts_waitcnt lgkmcnt(0)\n\t"
      "s_add_u32 m0, s51, 65536\n\ts_mov_b32 s48, 0x200000\n\tbuffer_load_dwordx4 v29, s[40:43], s48 offen  lds\n\ts_add_u32 m0, s51, 66560\n\ts_mov_b32 s48, 0x208000\n\t"
      "buffer_load_dwordx4 v29, s[40:43], s48 offen  lds\n\ts_add_u32 m0, s51, 67584\n\ts_mov_b32 s48, 0x210000\n\tbuffer_load_dwordx4 v29, s[40:43], s48 offen  lds\n\t"
      "s_add_u32 m0, s51, 68608\n\ts_mov_b32 s48, 0x218000\n\tbuffer_load_dwordx4 v29, s[40:43], s48 offen  lds\n\ts_cmp_lt_u32 s68, 1\n\ts_cbranch_scc1 7f\n\t"
      "s_cmp_eq_u32 s68, 2\n\ts_cbranch_scc1 6f\n\t"
      "5:\n\ts_buffer_load_dword s48, s[12:15], s69 glc\n\ts_waitcnt lgkmcnt(0)\n\ts_cmp_eq_u32 s48, s71\n\ts_cbranch_scc0 7f\n\t"
      "6:\n\tv_mov_b32 v28, 1\n\ts_mov_b64 exec, 1\n\tbuffer_atomic_add v28, off, s[12:15], s70\n\ts_mov_b64 exec, -1\n\ts_mov_b32 s68, 0\n\t"
      "7:\n\ts_waitcnt vmcnt(12)\n\ts_barrier\n\tds_read_b64_tr_b16 a[16:17], v30 offset:16384\n\tds_read_b64_tr_b16 a[18:19], v30 offset:17408\n\t"
      "ds_read_b64_tr_b16 a[20:21], v30 offset:20480\n\tds_read_b64_tr_b16 a[22:23], v30 offset:21504\n\tds_read_b64_tr_b16 a[24:25], v30 offset:24576\n\t"
      "ds_read_b64_tr_b16 a[26:27], v30 offset:25600\n\tds_read_b64_tr_b16 a[28:29], v30 offset:28672\n\tds_read_b64_tr_b16 a[30:31], v30 offset:29696\n\t"
      "s_waitcnt lgkmcnt(0)\n\ts_add_u32 m0, s51, 0\n\ts_mov_b32 s48, 0x280000\n\tbuffer_load_dwordx4 v29, s[40:43], s48 offen  lds\n\ts_add_u32 m0, s51, 1024\n\t"
      "s_mov_b32 s48, 0x288000\n\tbuffer_load_dwordx4 v29, s[40:43], s48 offen  lds\n\ts_add_u32 m0, s51, 2048\n\ts_mov_b32 s48, 0x290000\n\t"
      "buffer_load_dwordx4 v29, s[40:43], s48 offen  lds\n\ts_add_u32 m0, s51, 3072\n\ts_mov_b32 s48, 0x298000\n\tbuffer_load_dwordx4 v29, s[40:43], s48 offen  lds\n\t"
      "s_cmp_lt_u32 s68, 1\n\ts_cbranch_scc1 7f\n\ts_cmp_eq_u32 s68, 2\n\ts_cbranch_scc1 6f\n\t"
      "5:\n\ts_buffer_load_dword s48, s[12:15], s69 glc\n\ts_waitcnt lgkmcnt(0)\n\ts_cmp_eq_u32 s48, s71\n\ts_cbranch_scc0 7f\n\t"
      "6:\n\tv_mov_b32 v28, 1\n\ts_mov_b64 exec, 1\n\tbuffer_atomic_add v28, off, s[12:15], s70\n\ts_mov_b64 exec, -1\n\ts_mov_b32 s68, 0\n\t"
      "7:\n\ts_waitcnt vmcnt(12)\n\ts_barrier\n\tds_read_b64_tr_b16 a[32:33], v30 offset:32768\n\tds_read_b64_tr_b16 a[34:35], v30 offset:33792\n\t"
      "ds_read_b64_tr_b16 a[36:37], v30 offset:36864\n\tds_read_b64_tr_b16 a[38:39], v30 offset:37888\n\tds_read_b64_tr_b16 a[40:41], v30 offset:40960\n\t"
      "ds_read_b64_tr_b16 a[42:43], v30 offset:41984\n\tds_read_b64_tr_b16 a[44:45], v30 offset:45056\n\tds_read_b64_tr_b16 a[46:47], v30 offset:46080\n\t"
      "s_waitcnt lgkmcnt(0)\n\ts_add_u32 m0, s51, 16384\n\ts_mov_b32 s48, 0x300000\n\tbuffer_load_dwordx4 v29, s[40:43], s48 offen  lds\n\ts_add_u32 m0, s51, 17408\n\t"
      "s_mov_b32 s48, 0x308000\n\tbuffer_load_dwordx4 v29, s[40:43], s48 offen  lds\n\ts_add_u32 m0, s51, 18432\n\ts_mov_b32 s48, 0x310000\n\t"
      "buffer_load_dwordx4 v29, s[40:43], s48 offen  lds\n\ts_add_u32 m0, s51, 19456\n\ts_mov_b32 s48, 0x318000\n\tbuffer_load_dwordx4 v29, s[40:43], s48 offen  lds\n\t"
      "s_cmp_lt_u32 s68, 1\n\ts_cbranch_scc1 7f\n\ts_cmp_eq_u32 s68, 2\n\ts_cbranch_scc1 6f\n\t"
      "5:\n\ts_buffer_load_dword s48, s[12:15], s69 glc\n\ts_waitcnt lgkmcnt(0)\n\ts_cmp_eq_u32 s48, s71\n\ts_cbranch_scc0 7f\n\t"
      "6:\n\tv_mov_b32 v28, 1\n\ts_mov_b64 exec, 1\n\tbuffer_atomic_add v28, off, s[12:15], s70\n\ts_mov_b64 exec, -1\n\ts_mov_b32 s68, 0\n\t"
      "7:\n\ts_waitcnt vmcnt(12)\n\ts_barrier\n\tds_read_b64_tr_b16 a[48:49], v30 offset:49152\n\tds_read_b64_tr_b16 a[50:51], v30 offset:50176\n\t"
      "ds_read_b64_tr_b16 a[52:53], v30 offset:53248\n\tds_read_b64_tr_b16 a[54:55], v30 offset:54272\n\tds_read_b64_tr_b16 a[56:57], v30 offset:57344\n\t"
      "ds_read_b64_tr_b16 a[58:59], v30 offset:58368\n\tds_read_b64_tr_b16 a[60:61], v30 offset:61440\n\tds_read_b64_tr_b16 a[62:63], v30 offset:62464\n\t"
      "s_waitcnt lgkmcnt(0)\n\ts_add_u32 m0, s51, 32768\n\ts_mov_b32 s48, 0x380000\n\tbuffer_load_dwordx4 v29, s[40:43], s48 offen  lds\n\ts_add_u32 m0, s51, 33792\n\t"
      "s_mov_b32 s48, 0x388000\n\tbuffer_load_dwordx4 v29, s[40:43], s48 offen  lds\n\ts_add_u32 m0, s51, 34816\n\ts_mov_b32 s48, 0x390000\n\t"
      "buffer_load_dwordx4 v29, s[40:43], s48 offen  lds\n\ts_add_u32 m0, s51, 35840\n\ts_mov_b32 s48, 0x398000\n\tbuffer_load_dwordx4 v29, s[40:43], s48 offen  lds\n\t"
      "s_add_u32 m0, s50, 0\n\ts_nop 0\n\tbuffer_load_dwordx4 v16, s[44:47], 0 offen offset:0  lds\n\ts_add_u32 m0, s50, 0\n\ts_nop 0\n\t"
      "buffer_load_dwordx4 v16, s[44:47], 0 offen offset:1024  lds\n\ts_add_u32 m0, s50, 2048\n\ts_nop 0\n\tbuffer_load_dwordx4 v17, s[44:47], 0 offen offset:0  lds\n\t"
      "s_add_u32 m0, s50, 2048\n\ts_nop 0\n\tbuffer_load_dwordx4 v17, s[44:47], 0 offen offset:1024  lds\n\ts_cmp_lt_u32 s68, 1\n\ts_cbranch_scc1 7f\n\ts_cmp_eq_u32 s68, 2\n\t"
      "s_cbranch_scc1 6f\n\t"
      "5:\n\ts_buffer_load_dword s48, s[12:15], s69 glc\n\ts_waitcnt lgkmcnt(0)\n\ts_cmp_eq_u32 s48, s71\n\ts_cbranch_scc0 7f\n\t"
      "6:\n\tv_mov_b32 v28, 1\n\ts_mov_b64 exec, 1\n\tbuffer_atomic_add v28, off, s[12:15], s70\n\ts_mov_b64 exec, -1\n\ts_mov_b32 s68, 0\n\t"
      "7:\n\ts_waitcnt vmcnt(16)\n\ts_barrier\n\tds_read_b64_tr_b16 a[64:65], v31 offset:0\n\tds_read_b64_tr_b16 a[66:67], v31 offset:1024\n\t"
      "ds_read_b64_tr_b16 a[68:69], v31 offset:4096\n\tds_read_b64_tr_b16 a[70:71], v31 offset:5120\n\tds_read_b64_tr_b16 a[72:73], v31 offset:8192\n\t"
      "ds_read_b64_tr_b16 a[74:75], v31 offset:9216\n\tds_read_b64_tr_b16 a[76:77], v31 offset:12288\n\tds_read_b64_tr_b16 a[78:79], v31 offset:13312\n\ts_waitcnt lgkmcnt(0)\n\t"
      "s_add_u32 m0, s51, 49152\n\ts_mov_b32 s48, 0x400000\n\tbuffer_load_dwordx4 v29, s[40:43], s48 offen  lds\n\ts_add_u32 m0, s51, 50176\n\ts_mov_b32 s48, 0x408000\n\t"
      "buffer_load_dwordx4 v29, s[40:43], s48 offen  lds\n\ts_add_u32 m0, s51, 51200\n\ts_mov_b32 s48, 0x410000\n\tbuffer_load_dwordx4 v29, s[40:43], s48 offen  lds\n\t"
      "s_add_u32 m0, s51, 52224\n\ts_mov_b32 s48, 0x418000\n\tbuffer_load_dwordx4 v29, s[40:43], s48 offen  lds\n\ts_add_u32 m0, s50, 4096\n\ts_nop 0\n\t"
      "buffer_load_dwordx4 v18, s[44:47], 0 offen offset:0  lds\n\ts_add_u32 m0, s50, 4096\n\ts_nop 0\n\tbuffer_load_dwordx4 v18, s[44:47], 0 offen offset:1024  lds\n\t"
      "s_add_u32 m0, s50, 6144\n\ts_nop 0\n\tbuffer_load_dwordx4 v19, s[44:47], 0 offen offset:0  lds\n\ts_add_u32 m0, s50, 6144\n\ts_nop 0\n\t"
      "buffer_load_dwordx4 v19, s[44:47], 0 offen offset:1024  lds\n\ts_cmp_lt_u32 s68, 1\n\ts_cbranch_scc1 7f\n\ts_cmp_eq_u32 s68, 2\n\ts_cbranch_scc1 6f\n\t"
      "5:\n\ts_buffer_load_dword s48, s[12:15], s69 glc\n\ts_waitcnt lgkmcnt(0)\n\ts_cmp_eq_u32 s48, s71\n\ts_cbranch_scc0 7f\n\t"
      "6:\n\tv_mov_b32 v28, 1\n\ts_mov_b64 exec, 1\n\tbuffer_atomic_add v28, off, s[12:15], s70\n\ts_mov_b64 exec, -1\n\ts_mov_b32 s68, 0\n\t"
      "7:\n\ts_waitcnt vmcnt(20)\n\ts_barrier\n\tds_read_b64_tr_b16 a[80:81], v30 offset:0\n\tds_read_b64_tr_b16 a[82:83], v30 offset:1024\n\t"
      "ds_read_b64_tr_b16 a[84:85], v30 offset:4096\n\tds_read_b64_tr_b16 a[86:87], v30 offset:5120\n\tds_read_b64_tr_b16 a[88:89], v30 offset:8192\n\t"
      "ds_read_b64_tr_b16 a[90:91], v30 offset:9216\n\tds_read_b64_tr_b16 a[92:93], v30 offset:12288\n\tds_read_b64_tr_b16 a[94:95], v30 offset:13312\n\ts_waitcnt lgkmcnt(0)\n\t"
      "s_add_u32 m0, s51, 65536\n\ts_mov_b32 s48, 0x480000\n\tbuffer_load_dwordx4 v29, s[40:43], s48 offen  lds\n\ts_add_u32 m0, s51, 66560\n\ts_mov_b32 s48, 0x488000\n\t"
      "buffer_load_dwordx4 v29, s[40:43], s48 offen  lds\n\ts_add_u32 m0, s51, 67584\n\ts_mov_b32 s48, 0x490000\n\tbuffer_load_dwordx4 v29, s[40:43], s48 offen  lds\n\t"
      "s_add_u32 m0, s51, 68608\n\ts_mov_b32 s48, 0x498000\n\tbuffer_load_dwordx4 v29, s[40:43], s48 offen  lds\n\ts_add_u32 m0, s50, 8192\n\ts_nop 0\n\t"
      "buffer_load_dwordx4 v20, s[44:47], 0 offen offset:0  lds\n\ts_add_u32 m0, s50, 8192\n\ts_nop 0\n\tbuffer_load_dwordx4 v20, s[44:47], 0 offen offset:1024  lds\n\t"
      "s_add_u32 m0, s50, 10240\n\ts_nop 0\n\tbuffer_load_dwordx4 v21, s[44:47], 0 offen offset:0  lds\n\ts_add_u32 m0, s50, 10240\n\ts_nop 0\n\t"
      "buffer_load_dwordx4 v21, s[44:47], 0 offen offset:1024  lds\n\ts_cmp_lt_u32 s68, 1\n\ts_cbranch_scc1 7f\n\ts_cmp_eq_u32 s68, 2\n\ts_cbranch_scc1 6f\n\t"
      "5:\n\ts_buffer_load_dword s48, s[12:15], s69 glc\n\ts_waitcnt lgkmcnt(0)\n\ts_cmp_eq_u32 s48, s71\n\ts_cbranch_scc0 7f\n\t"
      "6:\n\tv_mov_b32 v28, 1\n\ts_mov_b64 exec, 1\n\tbuffer_atomic_add v28, off, s[12:15], s70\n\ts_mov_b64 exec, -1\n\ts_mov_b32 s68, 0\n\t"
      "7:\n\ts_waitcnt vmcnt(24)\n\ts_barrier\n\tds_read_b64_tr_b16 a[96:97], v30 offset:16384\n\tds_read_b64_tr_b16 a[98:99], v30 offset:17408\n\t"
      "ds_read_b64_tr_b16 a[100:101], v30 offset:20480\n\tds_read_b64_tr_b16 a[102:103], v30 offset:21504\n\tds_read_b64_tr_b16 a[104:105], v30 offset:24576\n\t"
      "ds_read_b64_tr_b16 a[106:107], v30 offset:25600\n\tds_read_b64_tr_b16 a[108:109], v30 offset:28672\n\tds_read_b64_tr_b16 a[110:111], v30 offset:29696\n\t"
      "s_waitcnt lgkmcnt(0)\n\ts_add_u32 m0, s51, 0\n\ts_mov_b32 s48, 0x500000\n\tbuffer_load_dwordx4 v29, s[40:43], s48 offen  lds\n\ts_add_u32 m0, s51, 1024\n\t"
      "s_mov_b32 s48, 0x508000\n\tbuffer_load_dwordx4 v29, s[40:43], s48 offen  lds\n\ts_add_u32 m0, s51, 2048\n\ts_mov_b32 s48, 0x510000\n\t"
      "buffer_load_dwordx4 v29, s[40:43], s48 offen  lds\n\ts_add_u32 m0, s51, 3072\n\ts_mov_b32 s48, 0x518000\n\tbuffer_load_dwordx4 v29, s[40:43], s48 offen  lds\n\t"
      "s_add_u32 m0, s50, 12288\n\ts_nop 0\n\tbuffer_load_dwordx4 v22, s[44:47], 0 offen offset:0  lds\n\ts_add_u32 m0, s50, 12288\n\ts_nop 0\n\t"
      "buffer_load_dwordx4 v22, s[44:47], 0 offen offset:1024  lds\n\ts_add_u32 m0, s50, 14336\n\ts_nop 0\n\tbuffer_load_dwordx4 v23, s[44:47], 0 offen offset:0  lds\n\t"
      "s_add_u32 m0, s50, 14336\n\ts_nop 0\n\tbuffer_load_dwordx4 v23, s[44:47], 0 offen offset:1024  lds\n\ts_cmp_lt_u32 s68, 1\n\ts_cbranch_scc1 7f\n\ts_cmp_eq_u32 s68, 2\n\t"
      "s_cbranch_scc1 6f\n\t"
      "5:\n\ts_buffer_load_dword s48, s[12:15], s69 glc\n\ts_waitcnt lgkmcnt(0)\n\ts_cmp_eq_u32 s48, s71\n\ts_cbranch_scc0 7f\n\t"
      "6:\n\tv_mov_b32 v28, 1\n\ts_mov_b64 exec, 1\n\tbuffer_atomic_add v28, off, s[12:15], s70\n\ts_mov_b64 exec, -1\n\ts_mov_b32 s68, 0\n\t"
      "7:\n\ts_waitcnt vmcnt(28)\n\ts_barrier\n\tds_read_b64_tr_b16 a[112:113], v30 offset:32768\n\tds_read_b64_tr_b16 a[114:115], v30 offset:33792\n\t"
      "ds_read_b64_tr_b16 a[116:117], v30 offset:36864\n\tds_read_b64_tr_b16 a[118:119], v30 offset:37888\n\tds_read_b64_tr_b16 a[120:121], v30 offset:40960\n\t"
      "ds_read_b64_tr_b16 a[122:123], v30 offset:41984\n\tds_read_b64_tr_b16 a[124:125], v30 offset:45056\n\tds_read_b64_tr_b16 a[126:127], v30 offset:46080\n\t"
      "s_waitcnt lgkmcnt(0)\n\ts_add_u32 m0, s51, 16384\n\ts_mov_b32 s48, 0x580000\n\tbuffer_load_dwordx4 v29, s[40:43], s48 offen  lds\n\ts_add_u32 m0, s51, 17408\n\t"
      "s_mov_b32 s48, 0x588000\n\tbuffer_load_dwordx4 v29, s[40:43], s48 offen  lds\n\ts_add_u32 m0, s51, 18432\n\ts_mov_b32 s48, 0x590000\n\t"
      "buffer_load_dwordx4 v29, s[40:43], s48 offen  lds\n\ts_add_u32 m0, s51, 19456\n\ts_mov_b32 s48, 0x598000\n\tbuffer_load_dwordx4 v29, s[40:43], s48 offen  lds\n\t"
      "s_mov_b32 s48, 0x10000\n\tbuffer_load_dword v26, v25, s[44:47], s48 offen\n\ts_mov_b32 s48, 0x20000\n\tbuffer_load_dword v26, v25, s[44:47], s48 offen\n\t"
      "s_mov_b32 s48, 0x30000\n\tbuffer_load_dword v26, v25, s[44:47], s48 offen\n\ts_cmp_lt_u32 s68, 1\n\ts_cbranch_scc1 7f\n\ts_cmp_eq_u32 s68, 2\n\ts_cbranch_scc1 6f\n\t"
      "5:\n\ts_buffer_load_dword s48, s[12:15], s69 glc\n\ts_waitcnt lgkmcnt(0)\n\ts_cmp_eq_u32 s48, s71\n\ts_cbranch_scc0 7f\n\t"
      "6:\n\tv_mov_b32 v28, 1\n\ts_mov_b64 exec, 1\n\tbuffer_atomic_add v28, off, s[12:15], s70\n\ts_mov_b64 exec, -1\n\ts_mov_b32 s68, 0\n\t"
      "7:\n\ts_waitcnt vmcnt(27)\n\ts_barrier\n\tds_read_b64_tr_b16 a[128:129], v30 offset:49152\n\tds_read_b64_tr_b16 a[130:131], v30 offset:50176\n\t"
      "ds_read_b64_tr_b16 a[132:133], v30 offset:53248\n\tds_read_b64_tr_b16 a[134:135], v30 offset:54272\n\tds_read_b64_tr_b16 a[136:137], v30 offset:57344\n\t"
      "ds_read_b64_tr_b16 a[138:139], v30 offset:58368\n\tds_read_b64_tr_b16 a[140:141], v30 offset:61440\n\tds_read_b64_tr_b16 a[142:143], v30 offset:62464\n\t"
      "s_waitcnt lgkmcnt(0)\n\ts_add_u32 m0, s51, 32768\n\ts_mov_b32 s48, 0x600000\n\tbuffer_load_dwordx4 v29, s[40:43], s48 offen  lds\n\ts_add_u32 m0, s51, 33792\n\t"
      "s_mov_b32 s48, 0x608000\n\tbuffer_load_dwordx4 v29, s[40:43], s48 offen  lds\n\ts_add_u32 m0, s51, 34816\n\ts_mov_b32 s48, 0x610000\n\t"
      "buffer_load_dwordx4 v29, s[40:43], s48 offen  lds\n\ts_add_u32 m0, s51, 35840\n\ts_mov_b32 s48, 0x618000\n\tbuffer_load_dwordx4 v29, s[40:43], s48 offen  lds\n\t"
      "s_cmp_lt_u32 s68, 1\n\ts_cbranch_scc1 7f\n\ts_cmp_eq_u32 s68, 2\n\ts_cbranch_scc1 6f\n\t"
      "5:\n\ts_buffer_load_dword s48, s[12:15], s69 glc\n\ts_waitcnt lgkmcnt(0)\n\ts_cmp_eq_u32 s48, s71\n\ts_cbranch_scc0 7f\n\t"
      "6:\n\tv_mov_b32 v28, 1\n\ts_mov_b64 exec, 1\n\tbuffer_atomic_add v28, off, s[12:15], s70\n\ts_mov_b64 exec, -1\n\ts_mov_b32 s68, 0\n\t"
      "7:\n\ts_waitcnt vmcnt(23)\n\ts_barrier\n\tds_read_b64_tr_b16 a[144:145], v31 offset:0\n\tds_read_b64_tr_b16 a[146:147], v31 offset:1024\n\t"
      "ds_read_b64_tr_b16 a[148:149], v31 offset:4096\n\tds_read_b64_tr_b16 a[150:151], v31 offset:5120\n\tds_read_b64_tr_b16 a[152:153], v31 offset:8192\n\t"
      "ds_read_b64_tr_b16 a[154:155], v31 offset:9216\n\tds_read_b64_tr_b16 a[156:157], v31 offset:12288\n\tds_read_b64_tr_b16 a[158:159], v31 offset:13312\n\t"
      "s_waitcnt lgkmcnt(0)\n\ts_add_u32 m0, s51, 49152\n\ts_mov_b32 s48, 0x680000\n\tbuffer_load_dwordx4 v29, s[40:43], s48 offen  lds\n\ts_add_u32 m0, s51, 50176\n\t"
      "s_mov_b32 s48, 0x688000\n\tbuffer_load_dwordx4 v29, s[40:43], s48 offen  lds\n\ts_add_u32 m0, s51, 51200\n\ts_mov_b32 s48, 0x690000\n\t"
      "buffer_load_dwordx4 v29, s[40:43], s48 offen  lds\n\ts_add_u32 m0, s51, 52224\n\ts_mov_b32 s48, 0x698000\n\tbuffer_load_dwordx4 v29, s[40:43], s48 offen  lds\n\t"
      "s_cmp_lt_u32 s68, 1\n\ts_cbranch_scc1 7f\n\ts_cmp_eq_u32 s68, 2\n\ts_cbranch_scc1 6f\n\t"
      "5:\n\ts_buffer_load_dword s48, s[12:15], s69 glc\n\ts_waitcnt lgkmcnt(0)\n\ts_cmp_eq_u32 s48, s71\n\ts_cbranch_scc0 7f\n\t"
      "6:\n\tv_mov_b32 v28, 1\n\ts_mov_b64 exec, 1\n\tbuffer_atomic_add v28, off, s[12:15], s70\n\ts_mov_b64 exec, -1\n\ts_mov_b32 s68, 0\n\t"
      "7:\n\ts_waitcnt vmcnt(19)\n\ts_barrier\n\tds_read_b64_tr_b16 a[160:161], v30 offset:0\n\tds_read_b64_tr_b16 a[162:163], v30 offset:1024\n\t"
      "ds_read_b64_tr_b16 a[164:165], v30 offset:4096\n\tds_read_b64_tr_b16 a[166:167], v30 offset:5120\n\tds_read_b64_tr_b16 a[168:169], v30 offset:8192\n\t"
      "ds_read_b64_tr_b16 a[170:171], v30 offset:9216\n\tds_read_b64_tr_b16 a[172:173], v30 offset:12288\n\tds_read_b64_tr_b16 a[174:175], v30 offset:13312\n\t"
      "s_waitcnt lgkmcnt(0)\n\ts_add_u32 m0, s51, 65536\n\ts_mov_b32 s48, 0x700000\n\tbuffer_load_dwordx4 v29, s[40:43], s48 offen  lds\n\ts_add_u32 m0, s51, 66560\n\t"
      "s_mov_b32 s48, 0x708000\n\tbuffer_load_dwordx4 v29, s[40:43], s48 offen  lds\n\ts_add_u32 m0, s51, 67584\n\ts_mov_b32 s48, 0x710000\n\t"
      "buffer_load_dwordx4 v29, s[40:43], s48 offen  lds\n\ts_add_u32 m0, s51, 68608\n\ts_mov_b32 s48, 0x718000\n\tbuffer_load_dwordx4 v29, s[40:43], s48 offen  lds\n\t"
      "s_cmp_lt_u32 s68, 1\n\ts_cbranch_scc1 7f\n\ts_cmp_eq_u32 s68, 2\n\ts_cbranch_scc1 6f\n\t"
      "5:\n\ts_buffer_load_dword s48, s[12:15], s69 glc\n\ts_waitcnt lgkmcnt(0)\n\ts_cmp_eq_u32 s48, s71\n\ts_cbranch_scc0 7f\n\t"
      "6:\n\tv_mov_b32 v28, 1\n\ts_mov_b64 exec, 1\n\tbuffer_atomic_add v28, off, s[12:15], s70\n\ts_mov_b64 exec, -1\n\ts_mov_b32 s68, 0\n\t"
      "7:\n\ts_waitcnt vmcnt(15)\n\ts_barrier\n\tds_read_b64_tr_b16 a[176:177], v30 offset:16384\n\tds_read_b64_tr_b16 a[178:179], v30 offset:17408\n\t"
      "ds_read_b64_tr_b16 a[180:181], v30 offset:20480\n\tds_read_b64_tr_b16 a[182:183], v30 offset:21504\n\tds_read_b64_tr_b16 a[184:185], v30 offset:24576\n\t"
      "ds_read_b64_tr_b16 a[186:187], v30 offset:25600\n\tds_read_b64_tr_b16 a[188:189], v30 offset:28672\n\tds_read_b64_tr_b16 a[190:191], v30 offset:29696\n\t"
      "s_waitcnt lgkmcnt(0)\n\ts_add_u32 m0, s51, 0\n\ts_mov_b32 s48, 0x780000\n\tbuffer_load_dwordx4 v29, s[40:43], s48 offen  lds\n\ts_add_u32 m0, s51, 1024\n\t"
      "s_mov_b32 s48, 0x788000\n\tbuffer_load_dwordx4 v29, s[40:43], s48 offen  lds\n\ts_add_u32 m0, s51, 2048\n\ts_mov_b32 s48, 0x790000\n\t"
      "buffer_load_dwordx4 v29, s[40:43], s48 offen  lds\n\ts_add_u32 m0, s51, 3072\n\ts_mov_b32 s48, 0x798000\n\tbuffer_load_dwordx4 v29, s[40:43], s48 offen  lds\n\t"
      "s_waitcnt vmcnt(12)\n\ts_barrier\n\tds_read_b64_tr_b16 a[192:193], v30 offset:32768\n\tds_read_b64_tr_b16 a[194:195], v30 offset:33792\n\t"
      "ds_read_b64_tr_b16 a[196:197], v30 offset:36864\n\tds_read_b64_tr_b16 a[198:199], v30 offset:37888\n\tds_read_b64_tr_b16 a[200:201], v30 offset:40960\n\t"
      "ds_read_b64_tr_b16 a[202:203], v30 offset:41984\n\tds_read_b64_tr_b16 a[204:205], v30 offset:45056\n\tds_read_b64_tr_b16 a[206:207], v30 offset:46080\n\t"
      "s_waitcnt lgkmcnt(0)\n\ts_waitcnt vmcnt(8)\n\ts_barrier\n\tds_read_b64_tr_b16 a[208:209], v30 offset:49152\n\tds_read_b64_tr_b16 a[210:211], v30 offset:50176\n\t"
      "ds_read_b64_tr_b16 a[212:213], v30 offset:53248\n\tds_read_b64_tr_b16 a[214:215], v30 offset:54272\n\tds_read_b64_tr_b16 a[216:217], v30 offset:57344\n\t"
      "ds_read_b64_tr_b16 a[218:219], v30 offset:58368\n\tds_read_b64_tr_b16 a[220:221], v30 offset:61440\n\tds_read_b64_tr_b16 a[222:223], v30 offset:62464\n\t"
      "s_waitcnt lgkmcnt(0)\n\ts_waitcnt vmcnt(4)\n\ts_barrier\n\tds_read_b64_tr_b16 a[224:225], v31 offset:0\n\tds_read_b64_tr_b16 a[226:227], v31 offset:1024\n\t"
      "ds_read_b64_tr_b16 a[228:229], v31 offset:4096\n\tds_read_b64_tr_b16 a[230:231], v31 offset:5120\n\tds_read_b64_tr_b16 a[232:233], v31 offset:8192\n\t"
      "ds_read_b64_tr_b16 a[234:235], v31 offset:9216\n\tds_read_b64_tr_b16 a[236:237], v31 offset:12288\n\tds_read_b64_tr_b16 a[238:239], v31 offset:13312\n\t"
      "s_waitcnt lgkmcnt(0)\n\ts_cmp_lt_u32 s68, 1\n\ts_cbranch_scc1 7f\n\ts_cmp_eq_u32 s68, 2\n\ts_cbranch_scc1 6f\n\t"
      "5:\n\ts_buffer_load_dword s48, s[12:15], s69 glc\n\ts_waitcnt lgkmcnt(0)\n\ts_cmp_eq_u32 s48, s71\n\ts_cbranch_scc1 6f\n\ts_cmp_eq_u32 s18, 0\n\ts_cbranch_scc1 6f\n\t"
      "s_sub_u32 s18, s18, 1\n\ts_branch 5b\n\t"
      "6:\n\tv_mov_b32 v28, 1\n\ts_mov_b64 exec, 1\n\tbuffer_atomic_add v28, off, s[12:15], s70\n\ts_mov_b64 exec, -1\n\ts_mov_b32 s68, 0\n\t"
      "7:\n\ts_waitcnt vmcnt(0)\n\ts_barrier\n\tds_read_b64_tr_b16 a[240:241], v30 offset:0\n\tds_read_b64_tr_b16 a[242:243], v30 offset:1024\n\t"
      "ds_read_b64_tr_b16 a[244:245], v30 offset:4096\n\tds_read_b64_tr_b16 a[246:247], v30 offset:5120\n\tds_read_b64_tr_b16 a[248:249], v30 offset:8192\n\t"
      "ds_read_b64_tr_b16 a[250:251], v30 offset:9216\n\tds_read_b64_tr_b16 a[252:253], v30 offset:12288\n\tds_read_b64_tr_b16 a[254:255], v30 offset:13312\n\t"
      "s_waitcnt lgkmcnt(0)\n\t"
      STS("s_memrealtime vcc\n\ts_waitcnt lgkmcnt(0)\n\tv_mov_b32 v95, vcc_lo\n\tv_mov_b32 v94, 161024\n\tds_write_b32 v94, v95\n\ts_memtime vcc\n\ts_waitcnt lgkmcnt(0)\n\tv_mov_b32 v95, vcc_lo\n\tds_write_b32 v94, v95 offset:400\n\ts_waitcnt lgkmcnt(0)\n\t")
      TKS("s_mov_b32 s91, 1\n\t")
      "s_mov_b32 s23, 0\n\ts_mov_b32 s26, s27\n\ts_mov_b32 s25, 0\n\tv_mov_b32 v90, 0x40000000\n\tv_mov_b32 v91, 0x40000000\n\tv_mov_b32 v29, 0x40000000\n\t"
      "v_mov_b32 v88, v27\n\tv_mov_b32 v27, 0x40000000\n\tds_read_b128 v[64:67], v0 offset:0\n\tds_read_b128 v[68:71], v1 offset:0\n\tds_read_b128 v[72:75], v2 offset:0\n\t"
      "10:\n\ts_waitcnt lgkmcnt(2)\n\tv_mfma_f32_32x32x16_bf16 v[32:47], a[0:3], v[64:67], 0\n\t"
      TK0
      "s_add_u32 s48, s17, 1\n\ts_and_b32 s48, s48, 63\n\ts_lshl_b32 s20, s48, 16\n\tds_read_b128 v[76:79], v3 offset:0\n\ts_waitcnt lgkmcnt(2)\n\t"
      "v_mfma_f32_32x32x16_bf16 v[32:47], a[4:7], v[68:71], v[32:47]\n\tds_read_b128 v[64:67], v4 offset:0\n\ts_barrier\n\tbuffer_load_dword v26, v25, s[44:47], s21 offen \n\t"
      "s_waitcnt lgkmcnt(2)\n\tv_mfma_f32_32x32x16_bf16 v[32:47], a[8:11], v[72:75], v[32:47]\n\tds_read_b128 v[68:71], v5 offset:0\n\ts_add_u32 m0, s50, 65536\n\t"
      "s_add_u32 s23, s23, s26\n\ts_cmp_gt_u32 s17, s23\n\ts_cbranch_scc1 24f\n\t"
      "14:\n\ts_waitcnt lgkmcnt(2)\n\tv_mfma_f32_32x32x16_bf16 v[32:47], a[12:15], v[76:79], v[32:47]\n\tds_read_b128 v[72:75], v6 offset:0\n\t"
      "buffer_load_dwordx4 v16, s[44:47], s20 offen offset:0  lds\n\ts_waitcnt lgkmcnt(2)\n\tv_mfma_f32_32x32x16_bf16 v[32:47], a[16:19], v[64:67], v[32:47]\n\t"
      "ds_read_b128 v[76:79], v7 offset:0\n\ts_add_u32 m0, s50, 65536\n\tv_cvt_pk_bf16_f32 v80, v48, v52\n\tv_cvt_pk_bf16_f32 v81, v56, v60\n\t"
      "v_cvt_pk_bf16_f32 v82, v49, v53\n\tv_cvt_pk_bf16_f32 v83, v57, v61\n\ts_waitcnt lgkmcnt(2)\n\tv_mfma_f32_32x32x16_bf16 v[32:47], a[20:23], v[68:71], v[32:47]\n\t"
      "ds_read_b128 v[64:67], v0 offset:256\n\tbuffer_load_dwordx4 v16, s[44:47], s20 offen offset:1024  lds\n\ts_waitcnt lgkmcnt(2)\n\t"
      "v_mfma_f32_32x32x16_bf16 v[32:47], a[24:27], v[72:75], v[32:47]\n\tds_read_b128 v[68:71], v1 offset:256\n\t"
      "buffer_store_dwordx4 v[80:83], v90, s[8:11], s19 offen offset:0 sc1\n\ts_add_u32 m0, s50, 67584\n\ts_waitcnt lgkmcnt(2)\n\t"
      "v_mfma_f32_32x32x16_bf16 v[32:47], a[28:31], v[76:79], v[32:47]\n\tds_read_b128 v[72:75], v2 offset:256\n\tbuffer_load_dwordx4 v17, s[44:47], s20 offen offset:0  lds\n\t"
      "s_waitcnt lgkmcnt(2)\n\tv_mfma_f32_32x32x16_bf16 v[32:47], a[32:35], v[64:67], v[32:47]\n\tds_read_b128 v[76:79], v3 offset:256\n\ts_add_u32 m0, s50, 67584\n\t"
      "v_cvt_pk_bf16_f32 v84, v50, v54\n\tv_cvt_pk_bf16_f32 v85, v58, v62\n\tv_cvt_pk_bf16_f32 v86, v51, v55\n\tv_cvt_pk_bf16_f32 v87, v59, v63\n\ts_waitcnt lgkmcnt(2)\n\t"
      "v_mfma_f32_32x32x16_bf16 v[32:47], a[36:39], v[68:71], v[32:47]\n\tds_read_b128 v[64:67], v4 offset:256\n\t"
      "buffer_load_dwordx4 v17, s[44:47], s20 offen offset:1024  lds\n\ts_waitcnt lgkmcnt(2)\n\tv_mfma_f32_32x32x16_bf16 v[32:47], a[40:43], v[72:75], v[32:47]\n\t"
      "ds_read_b128 v[68:71], v5 offset:256\n\tbuffer_store_dwordx4 v[84:87], v90, s[8:11], s19 offen offset:512 sc1\n\ts_add_u32 m0, s50, 69632\n\ts_waitcnt lgkmcnt(2)\n\t"
      "v_mfma_f32_32x32x16_bf16 v[32:47], a[44:47], v[76:79], v[32:47]\n\tds_read_b128 v[72:75], v6 offset:256\n\tbuffer_load_dwordx4 v18, s[44:47], s20 offen offset:0  lds\n\t"
      "s_waitcnt lgkmcnt(2)\n\tv_mfma_f32_32x32x16_bf16 v[32:47], a[48:51], v[64:67], v[32:47]\n\tds_read_b128 v[76:79], v7 offset:256\n\ts_add_u32 m0, s50, 69632\n\t"
      "s_sub_u32 s48, s17, s25\n\tv_mov_b32 v28, s48\n\ts_waitcnt lgkmcnt(2)\n\tv_mfma_f32_32x32x16_bf16 v[32:47], a[52:55], v[68:71], v[32:47]\n\t"
      "ds_read_b128 v[64:67], v0 offset:512\n\tbuffer_load_dwordx4 v18, s[44:47], s20 offen offset:1024  lds\n\ts_waitcnt lgkmcnt(2)\n\t"
      "v_mfma_f32_32x32x16_bf16 v[32:47], a[56:59], v[72:75], v[32:47]\n\tds_read_b128 v[68:71], v1 offset:512\n\ts_add_u32 m0, s50, 71680\n\ts_waitcnt lgkmcnt(2)\n\t"
      "v_mfma_f32_32x32x16_bf16 v[32:47], a[60:63], v[76:79], v[32:47]\n\tds_read_b128 v[72:75], v2 offset:512\n\tbuffer_load_dwordx4 v19, s[44:47], s20 offen offset:0  lds\n\t"
      "s_waitcnt lgkmcnt(2)\n\tv_mfma_f32_32x32x16_bf16 v[32:47], a[64:67], v[64:67], v[32:47]\n\tds_read_b128 v[76:79], v3 offset:512\n\ts_add_u32 m0, s50, 71680\n\t"
      "s_waitcnt lgkmcnt(2)\n\tv_mfma_f32_32x32x16_bf16 v[32:47], a[68:71], v[68:71], v[32:47]\n\tds_read_b128 v[64:67], v4 offset:512\n\t"
      "buffer_load_dwordx4 v19, s[44:47], s20 offen offset:1024  lds\n\ts_waitcnt lgkmcnt(2)\n\tv_mfma_f32_32x32x16_bf16 v[32:47], a[72:75], v[72:75], v[32:47]\n\t"
      "ds_read_b128 v[68:71], v5 offset:512\n\ts_add_u32 m0, s50, 73728\n\ts_waitcnt lgkmcnt(2)\n\tv_mfma_f32_32x32x16_bf16 v[32:47], a[76:79], v[76:79], v[32:47]\n\t"
      "ds_read_b128 v[72:75], v6 offset:512\n\tbuffer_load_dwordx4 v20, s[44:47], s20 offen offset:0  lds\n\ts_waitcnt lgkmcnt(2)\n\t"
      "v_mfma_f32_32x32x16_bf16 v[32:47], a[80:83], v[64:67], v[32:47]\n\tds_read_b128 v[76:79], v7 offset:512\n\ts_add_u32 m0, s50, 73728\n\ts_waitcnt lgkmcnt(2)\n\t"
      "v_mfma_f32_32x32x16_bf16 v[32:47], a[84:87], v[68:71], v[32:47]\n\tds_read_b128 v[64:67], v0 offset:768\n\t"
      "buffer_load_dwordx4 v20, s[44:47], s20 offen offset:1024  lds\n\ts_waitcnt lgkmcnt(2)\n\tv_mfma_f32_32x32x16_bf16 v[32:47], a[88:91], v[72:75], v[32:47]\n\t"
      "ds_read_b128 v[68:71], v1 offset:768\n\ts_add_u32 m0, s50, 75776\n\ts_waitcnt lgkmcnt(2)\n\tv_mfma_f32_32x32x16_bf16 v[32:47], a[92:95], v[76:79], v[32:47]\n\t"
      "ds_read_b128 v[72:75], v2 offset:768\n\tbuffer_load_dwordx4 v21, s[44:47], s20 offen offset:0  lds\n\ts_waitcnt lgkmcnt(2)\n\t"
      "v_mfma_f32_32x32x16_bf16 v[32:47], a[96:99], v[64:67], v[32:47]\n\tds_read_b128 v[76:79], v3 offset:768\n\ts_add_u32 m0, s50, 75776\n\ts_waitcnt lgkmcnt(2)\n\t"
      "v_mfma_f32_32x32x16_bf16 v[32:47], a[100:103], v[68:71], v[32:47]\n\tds_read_b128 v[64:67], v4 offset:768\n\t"
      "buffer_load_dwordx4 v21, s[44:47], s20 offen offset:1024  lds\n\ts_waitcnt lgkmcnt(2)\n\tv_mfma_f32_32x32x16_bf16 v[32:47], a[104:107], v[72:75], v[32:47]\n\t"
      "ds_read_b128 v[68:71], v5 offset:768\n\ts_add_u32 m0, s50, 77824\n\ts_waitcnt lgkmcnt(2)\n\tv_mfma_f32_32x32x16_bf16 v[32:47], a[108:111], v[76:79], v[32:47]\n\t"
      "ds_read_b128 v[72:75], v6 offset:768\n\tbuffer_load_dwordx4 v22, s[44:47], s20 offen offset:0  lds\n\ts_waitcnt lgkmcnt(2)\n\t"
      "v_mfma_f32_32x32x16_bf16 v[32:47], a[112:115], v[64:67], v[32:47]\n\tds_read_b128 v[76:79], v7 offset:768\n\ts_add_u32 m0, s50, 77824\n\ts_waitcnt lgkmcnt(2)\n\t"
      "v_mfma_f32_32x32x16_bf16 v[32:47], a[116:119], v[68:71], v[32:47]\n\tds_read_b128 v[64:67], v0 offset:1024\n\t"
      "buffer_load_dwordx4 v22, s[44:47], s20 offen offset:1024  lds\n\ts_waitcnt lgkmcnt(2)\n\tv_mfma_f32_32x32x16_bf16 v[32:47], a[120:123], v[72:75], v[32:47]\n\t"
      "ds_read_b128 v[68:71], v1 offset:1024\n\ts_add_u32 m0, s50, 79872\n\ts_waitcnt lgkmcnt(2)\n\tv_mfma_f32_32x32x16_bf16 v[32:47], a[124:127], v[76:79], v[32:47]\n\t"
      "ds_read_b128 v[72:75], v2 offset:1024\n\tbuffer_load_dwordx4 v23, s[44:47], s20 offen offset:0  lds\n\ts_waitcnt lgkmcnt(2)\n\t"
      "v_mfma_f32_32x32x16_bf16 v[32:47], a[128:131], v[64:67], v[32:47]\n\tds_read_b128 v[76:79], v3 offset:1024\n\ts_add_u32 m0, s50, 79872\n\t"
      TK1
      "s_waitcnt vmcnt(11)\n\t"
      TK2
      "s_barrier\n\t"
      TK3
      "s_waitcnt lgkmcnt(2)\n\tv_mfma_f32_32x32x16_bf16 v[32:47], a[132:135], v[68:71], v[32:47]\n\tds_read_b128 v[64:67], v4 offset:1024\n\t"
      "buffer_load_dwordx4 v23, s[44:47], s20 offen offset:1024  lds\n\ts_waitcnt lgkmcnt(2)\n\tv_mfma_f32_32x32x16_bf16 v[32:47], a[136:139], v[72:75], v[32:47]\n\t"
      "ds_read_b128 v[68:71], v5 offset:1024\n\tbuffer_store_dword v28, v27, s[12:15], s22 offen sc1\n\ts_waitcnt lgkmcnt(2)\n\t"
      "v_mfma_f32_32x32x16_bf16 v[32:47], a[140:143], v[76:79], v[32:47]\n\tds_read_b128 v[72:75], v6 offset:1024\n\t"
      "buffer_store_dwordx4 v[80:83], v91, s[8:11], s19 offen offset:0 sc1\n\ts_waitcnt lgkmcnt(2)\n\tv_mfma_f32_32x32x16_bf16 v[32:47], a[144:147], v[64:67], v[32:47]\n\t"
      "ds_read_b128 v[76:79], v7 offset:1024\n\tbuffer_store_dwordx4 v[84:87], v91, s[8:11], s19 offen offset:512 sc1\n\ts_waitcnt lgkmcnt(2)\n\t"
      "v_mfma_f32_32x32x16_bf16 v[32:47], a[148:151], v[68:71], v[32:47]\n\tds_read_b128 v[64:67], v0 offset:1280\n\ts_add_u32 s48, s17, 5\n\ts_min_u32 s48, s48, 63\n\t"
      "s_lshl_b32 s21, s48, 16\n\ts_waitcnt lgkmcnt(2)\n\tv_mfma_f32_32x32x16_bf16 v[32:47], a[152:155], v[72:75], v[32:47]\n\tds_read_b128 v[68:71], v1 offset:1280\n\t"
      "s_waitcnt lgkmcnt(2)\n\tv_mfma_f32_32x32x16_bf16 v[32:47], a[156:159], v[76:79], v[32:47]\n\tds_read_b128 v[72:75], v2 offset:1280\n\ts_waitcnt lgkmcnt(2)\n\t"
      "v_mfma_f32_32x32x16_bf16 v[32:47], a[160:163], v[64:67], v[32:47]\n\tds_read_b128 v[76:79], v3 offset:1280\n\ts_waitcnt lgkmcnt(2)\n\t"
      "v_mfma_f32_32x32x16_bf16 v[32:47], a[164:167], v[68:71], v[32:47]\n\tds_read_b128 v[64:67], v4 offset:1280\n\ts_waitcnt lgkmcnt(2)\n\t"
      "v_mfma_f32_32x32x16_bf16 v[32:47], a[168:171], v[72:75], v[32:47]\n\tds_read_b128 v[68:71], v5 offset:1280\n\t"
      "s_mul_i32 s26, s17, s28\n\ts_lshr_b32 s26, s26, 6\n\ts_add_u32 s26, s26, s27\n\ts_waitcnt lgkmcnt(2)\n\t"
      "v_mfma_f32_32x32x16_bf16 v[32:47], a[172:175], v[76:79], v[32:47]\n\tds_read_b128 v[72:75], v6 offset:1280\n\ts_waitcnt lgkmcnt(2)\n\t"
      "v_mfma_f32_32x32x16_bf16 v[32:47], a[176:179], v[64:67], v[32:47]\n\tds_read_b128 v[76:79], v7 offset:1280\n\ts_waitcnt lgkmcnt(2)\n\t"
      "v_mfma_f32_32x32x16_bf16 v[32:47], a[180:183], v[68:71], v[32:47]\n\tds_read_b128 v[64:67], v0 offset:1536\n\ts_waitcnt lgkmcnt(2)\n\t"
      "v_mfma_f32_32x32x16_bf16 v[32:47], a[184:187], v[72:75], v[32:47]\n\tds_read_b128 v[68:71], v1 offset:1536\n\ts_waitcnt lgkmcnt(2)\n\t"
      "v_mfma_f32_32x32x16_bf16 v[32:47], a[188:191], v[76:79], v[32:47]\n\tds_read_b128 v[72:75], v2 offset:1536\n\ts_waitcnt lgkmcnt(2)\n\t"
      "v_mfma_f32_32x32x16_bf16 v[32:47], a[192:195], v[64:67], v[32:47]\n\tds_read_b128 v[76:79], v3 offset:1536\n\ts_waitcnt lgkmcnt(2)\n\t"
      "v_mfma_f32_32x32x16_bf16 v[32:47], a[196:199], v[68:71], v[32:47]\n\tds_read_b128 v[64:67], v4 offset:1536\n\ts_waitcnt lgkmcnt(2)\n\t"
      "v_mfma_f32_32x32x16_bf16 v[32:47], a[200:203], v[72:75], v[32:47]\n\tds_read_b128 v[68:71], v5 offset:1536\n\ts_waitcnt lgkmcnt(2)\n\t"
      "v_mfma_f32_32x32x16_bf16 v[32:47], a[204:207], v[76:79], v[32:47]\n\tds_read_b128 v[72:75], v6 offset:1536\n\ts_waitcnt lgkmcnt(2)\n\t"
      "v_mfma_f32_32x32x16_bf16 v[32:47], a[208:211], v[64:67], v[32:47]\n\tds_read_b128 v[76:79], v7 offset:1536\n\ts_waitcnt lgkmcnt(2)\n\t"
      "v_mfma_f32_32x32x16_bf16 v[32:47], a[212:215], v[68:71], v[32:47]\n\tds_read_b128 v[64:67], v0 offset:1792\n\ts_waitcnt lgkmcnt(2)\n\t"
      "v_mfma_f32_32x32x16_bf16 v[32:47], a[216:219], v[72:75], v[32:47]\n\tds_read_b128 v[68:71], v1 offset:1792\n\ts_add_u32 s48, s19, 0x4000\n\t"
      "s_and_b32 s48, s48, 0x3c000\n\ts_andn2_b32 s19, s19, 0x3c000\n\ts_or_b32 s19, s19, s48\n\ts_waitcnt lgkmcnt(2)\n\t"
      "v_mfma_f32_32x32x16_bf16 v[32:47], a[220:223], v[76:79], v[32:47]\n\tds_read_b128 v[72:75], v2 offset:1792\n\ts_waitcnt lgkmcnt(2)\n\t"
      "v_mfma_f32_32x32x16_bf16 v[32:47], a[224:227], v[64:67], v[32:47]\n\tds_read_b128 v[76:79], v3 offset:1792\n\ts_cmp_ge_u32 s17, 16\n\ts_cselect_b32 s25, s24, 0\n\t"
      "s_lshl_b32 s48, s25, 30\n\tv_or_b32 v90, s48, v29\n\ts_waitcnt lgkmcnt(2)\n\tv_mfma_f32_32x32x16_bf16 v[32:47], a[228:231], v[68:71], v[32:47]\n\t"
      "ds_read_b128 v[64:67], v4 offset:1792\n\ts_xor_b32 s48, s48, 0x40000000\n\tv_or_b32 v91, s48, v29\n\ts_waitcnt lgkmcnt(2)\n\t"
      "v_mfma_f32_32x32x16_bf16 v[32:47], a[232:235], v[72:75], v[32:47]\n\tds_read_b128 v[68:71], v5 offset:1792\n\ts_waitcnt lgkmcnt(2)\n\t"
      "v_mfma_f32_32x32x16_bf16 v[32:47], a[236:239], v[76:79], v[32:47]\n\tds_read_b128 v[72:75], v6 offset:1792\n\ts_waitcnt lgkmcnt(2)\n\t"
      "v_mfma_f32_32x32x16_bf16 v[32:47], a[240:243], v[64:67], v[32:47]\n\tds_read_b128 v[76:79], v7 offset:1792\n\ts_waitcnt lgkmcnt(2)\n\t"
      "v_mfma_f32_32x32x16_bf16 v[32:47], a[244:247], v[68:71], v[32:47]\n\t"
      TK4
      "s_waitcnt vmcnt(3)\n\t"
      TK5
      "s_barrier\n\t"
      TK6
      "ds_read_b128 v[64:67], v8 offset:0\n\ts_waitcnt lgkmcnt(2)\n\tv_mfma_f32_32x32x16_bf16 v[32:47], a[248:251], v[72:75], v[32:47]\n\tds_read_b128 v[68:71], v9 offset:0\n\t"
      "v_readfirstlane_b32 s23, v89\n\ts_waitcnt lgkmcnt(2)\n\tv_mfma_f32_32x32x16_bf16 v[32:47], a[252:255], v[76:79], v[32:47]\n\tds_read_b128 v[72:75], v10 offset:0\n\t"
      "buffer_load_dword v89, off, s[12:15], 0 offset:3840 sc1\n\t"
      STF("s_memrealtime vcc\n\ts_waitcnt lgkmcnt(0)\n\tv_mov_b32 v95, vcc_lo\n\ts_lshl_b32 vcc_hi, s17, 2\n\ts_add_u32 vcc_hi, vcc_hi, 161056\n\tv_mov_b32 v94, vcc_hi\n\tds_write_b32 v94, v95\n\ts_memtime vcc\n\ts_waitcnt lgkmcnt(0)\n\tv_mov_b32 v95, vcc_lo\n\tds_write_b32 v94, v95 offset:400\n\ts_waitcnt lgkmcnt(0)\n\t")
      TK7
      TKS("s_waitcnt lgkmcnt(0)\n\ts_cmp_lt_i32 s17, 0\n\ts_cbranch_scc1 98f\n\ts_cmp_lg_u32 s16, s91\n\ts_cbranch_scc1 98f\n\ts_mov_b64 exec, 1\n\ts_lshl_b32 s48, s17, 4\n\ts_add_u32 s48, s48, 161056\n\tv_mov_b32 v92, s48\n\ts_sub_u32 s48, s74, s90\n\ts_min_u32 s48, s48, 0xffff\n\tv_mov_b32 v93, s48\n\tds_write_b16 v92, v93 offset:0\n\ts_sub_u32 s48, s76, s74\n\ts_min_u32 s48, s48, 0xffff\n\tv_mov_b32 v93, s48\n\tds_write_b16 v92, v93 offset:2\n\ts_sub_u32 s48, s78, s76\n\ts_min_u32 s48, s48, 0xffff\n\tv_mov_b32 v93, s48\n\tds_write_b16 v92, v93 offset:4\n\ts_sub_u32 s48, s80, s78\n\ts_min_u32 s48, s48, 0xffff\n\tv_mov_b32 v93, s48\n\tds_write_b16 v92, v93 offset:6\n\ts_sub_u32 s48, s82, s80\n\ts_min_u32 s48, s48, 0xffff\n\tv_mov_b32 v93, s48\n\tds_write_b16 v92, v93 offset:8\n\ts_sub_u32 s48, s84, s82\n\ts_min_u32 s48, s48, 0xffff\n\tv_mov_b32 v93, s48\n\tds_write_b16 v92, v93 offset:10\n\ts_sub_u32 s48, s86, s84\n\ts_min_u32 s48, s48, 0xffff\n\tv_mov_b32 v93, s48\n\tds_write_b16 v92, v93 offset:12\n\ts_sub_u32 s48, s88, s86\n\ts_min_u32 s48, s48, 0xffff\n\tv_mov_b32 v93, s48\n\tds_write_b16 v92, v93 offset:14\n\ts_mov_b64 exec, -1\n\t98:\n\ts_mov_b32 s90, s88\n\t")
      "s_add_u32 s17, s17, 1\n\ts_cmp_eq_u32 s17, 1\n\ts_cbranch_scc1 20f\n\t"
      "11:\n\ts_waitcnt lgkmcnt(2)\n\tv_mfma_f32_32x32x16_bf16 v[48:63], a[0:3], v[64:67], 0\n\t"
      TK0
      "s_add_u32 s48, s17, 1\n\ts_and_b32 s48, s48, 63\n\ts_lshl_b32 s20, s48, 16\n\tds_read_b128 v[76:79], v11 offset:0\n\ts_waitcnt lgkmcnt(2)\n\t"
      "v_mfma_f32_32x32x16_bf16 v[48:63], a[4:7], v[68:71], v[48:63]\n\tds_read_b128 v[64:67], v12 offset:0\n\ts_barrier\n\tbuffer_load_dword v26, v25, s[44:47], s21 offen \n\t"
      "s_waitcnt lgkmcnt(2)\n\tv_mfma_f32_32x32x16_bf16 v[48:63], a[8:11], v[72:75], v[48:63]\n\tds_read_b128 v[68:71], v13 offset:0\n\ts_add_u32 m0, s50, 0\n\t"
      "s_add_u32 s23, s23, s26\n\ts_cmp_gt_u32 s17, s23\n\ts_cbranch_scc1 25f\n\t"
      "15:\n\ts_waitcnt lgkmcnt(2)\n\tv_mfma_f32_32x32x16_bf16 v[48:63], a[12:15], v[76:79], v[48:63]\n\tds_read_b128 v[72:75], v14 offset:0\n\t"
      "buffer_load_dwordx4 v16, s[44:47], s20 offen offset:0  lds\n\ts_waitcnt lgkmcnt(2)\n\tv_mfma_f32_32x32x16_bf16 v[48:63], a[16:19], v[64:67], v[48:63]\n\t"
      "ds_read_b128 v[76:79], v15 offset:0\n\ts_add_u32 m0, s50, 0\n\tv_cvt_pk_bf16_f32 v80, v32, v36\n\tv_cvt_pk_bf16_f32 v81, v40, v44\n\tv_cvt_pk_bf16_f32 v82, v33, v37\n\t"
      "v_cvt_pk_bf16_f32 v83, v41, v45\n\ts_waitcnt lgkmcnt(2)\n\tv_mfma_f32_32x32x16_bf16 v[48:63], a[20:23], v[68:71], v[48:63]\n\tds_read_b128 v[64:67], v8 offset:256\n\t"
      "buffer_load_dwordx4 v16, s[44:47], s20 offen offset:1024  lds\n\ts_waitcnt lgkmcnt(2)\n\tv_mfma_f32_32x32x16_bf16 v[48:63], a[24:27], v[72:75], v[48:63]\n\t"
      "ds_read_b128 v[68:71], v9 offset:256\n\tbuffer_store_dwordx4 v[80:83], v90, s[8:11], s19 offen offset:0 sc1\n\ts_add_u32 m0, s50, 2048\n\ts_waitcnt lgkmcnt(2)\n\t"
      "v_mfma_f32_32x32x16_bf16 v[48:63], a[28:31], v[76:79], v[48:63]\n\tds_read_b128 v[72:75], v10 offset:256\n\tbuffer_load_dwordx4 v17, s[44:47], s20 offen offset:0  lds\n\t"
      "s_waitcnt lgkmcnt(2)\n\tv_mfma_f32_32x32x16_bf16 v[48:63], a[32:35], v[64:67], v[48:63]\n\tds_read_b128 v[76:79], v11 offset:256\n\ts_add_u32 m0, s50, 2048\n\t"
      "v_cvt_pk_bf16_f32 v84, v34, v38\n\tv_cvt_pk_bf16_f32 v85, v42, v46\n\tv_cvt_pk_bf16_f32 v86, v35, v39\n\tv_cvt_pk_bf16_f32 v87, v43, v47\n\ts_waitcnt lgkmcnt(2)\n\t"
      "v_mfma_f32_32x32x16_bf16 v[48:63], a[36:39], v[68:71], v[48:63]\n\tds_read_b128 v[64:67], v12 offset:256\n\t"
      "buffer_load_dwordx4 v17, s[44:47], s20 offen offset:1024  lds\n\ts_waitcnt lgkmcnt(2)\n\tv_mfma_f32_32x32x16_bf16 v[48:63], a[40:43], v[72:75], v[48:63]\n\t"
      "ds_read_b128 v[68:71], v13 offset:256\n\tbuffer_store_dwordx4 v[84:87], v90, s[8:11], s19 offen offset:512 sc1\n\ts_add_u32 m0, s50, 4096\n\ts_waitcnt lgkmcnt(2)\n\t"
      "v_mfma_f32_32x32x16_bf16 v[48:63], a[44:47], v[76:79], v[48:63]\n\tds_read_b128 v[72:75], v14 offset:256\n\tbuffer_load_dwordx4 v18, s[44:47], s20 offen offset:0  lds\n\t"
      "s_waitcnt lgkmcnt(2)\n\tv_mfma_f32_32x32x16_bf16 v[48:63], a[48:51], v[64:67], v[48:63]\n\tds_read_b128 v[76:79], v15 offset:256\n\ts_add_u32 m0, s50, 4096\n\t"
      "s_sub_u32 s48, s17, s25\n\tv_mov_b32 v28, s48\n\ts_waitcnt lgkmcnt(2)\n\tv_mfma_f32_32x32x16_bf16 v[48:63], a[52:55], v[68:71], v[48:63]\n\t"
      "ds_read_b128 v[64:67], v8 offset:512\n\tbuffer_load_dwordx4 v18, s[44:47], s20 offen offset:1024  lds\n\ts_waitcnt lgkmcnt(2)\n\t"
      "v_mfma_f32_32x32x16_bf16 v[48:63], a[56:59], v[72:75], v[48:63]\n\tds_read_b128 v[68:71], v9 offset:512\n\ts_add_u32 m0, s50, 6144\n\ts_waitcnt lgkmcnt(2)\n\t"
      "v_mfma_f32_32x32x16_bf16 v[48:63], a[60:63], v[76:79], v[48:63]\n\tds_read_b128 v[72:75], v10 offset:512\n\tbuffer_load_dwordx4 v19, s[44:47], s20 offen offset:0  lds\n\t"
      "s_waitcnt lgkmcnt(2)\n\tv_mfma_f32_32x32x16_bf16 v[48:63], a[64:67], v[64:67], v[48:63]\n\tds_read_b128 v[76:79], v11 offset:512\n\ts_add_u32 m0, s50, 6144\n\t"
      "s_waitcnt lgkmcnt(2)\n\tv_mfma_f32_32x32x16_bf16 v[48:63], a[68:71], v[68:71], v[48:63]\n\tds_read_b128 v[64:67], v12 offset:512\n\t"
      "buffer_load_dwordx4 v19, s[44:47], s20 offen offset:1024  lds\n\ts_waitcnt lgkmcnt(2)\n\tv_mfma_f32_32x32x16_bf16 v[48:63], a[72:75], v[72:75], v[48:63]\n\t"
      "ds_read_b128 v[68:71], v13 offset:512\n\ts_add_u32 m0, s50, 8192\n\ts_waitcnt lgkmcnt(2)\n\tv_mfma_f32_32x32x16_bf16 v[48:63], a[76:79], v[76:79], v[48:63]\n\t"
      "ds_read_b128 v[72:75], v14 offset:512\n\tbuffer_load_dwordx4 v20, s[44:47], s20 offen offset:0  lds\n\ts_waitcnt lgkmcnt(2)\n\t"
      "v_mfma_f32_32x32x16_bf16 v[48:63], a[80:83], v[64:67], v[48:63]\n\tds_read_b128 v[76:79], v15 offset:512\n\ts_add_u32 m0, s50, 8192\n\ts_waitcnt lgkmcnt(2)\n\t"
      "v_mfma_f32_32x32x16_bf16 v[48:63], a[84:87], v[68:71], v[48:63]\n\tds_read_b128 v[64:67], v8 offset:768\n\t"
      "buffer_load_dwordx4 v20, s[44:47], s20 offen offset:1024  lds\n\ts_waitcnt lgkmcnt(2)\n\tv_mfma_f32_32x32x16_bf16 v[48:63], a[88:91], v[72:75], v[48:63]\n\t"
      "ds_read_b128 v[68:71], v9 offset:768\n\ts_add_u32 m0, s50, 10240\n\ts_waitcnt lgkmcnt(2)\n\tv_mfma_f32_32x32x16_bf16 v[48:63], a[92:95], v[76:79], v[48:63]\n\t"
      "ds_read_b128 v[72:75], v10 offset:768\n\tbuffer_load_dwordx4 v21, s[44:47], s20 offen offset:0  lds\n\ts_waitcnt lgkmcnt(2)\n\t"
      "v_mfma_f32_32x32x16_bf16 v[48:63], a[96:99], v[64:67], v[48:63]\n\tds_read_b128 v[76:79], v11 offset:768\n\ts_add_u32 m0, s50, 10240\n\ts_waitcnt lgkmcnt(2)\n\t"
      "v_mfma_f32_32x32x16_bf16 v[48:63], a[100:103], v[68:71], v[48:63]\n\tds_read_b128 v[64:67], v12 offset:768\n\t"
      "buffer_load_dwordx4 v21, s[44:47], s20 offen offset:1024  lds\n\ts_waitcnt lgkmcnt(2)\n\tv_mfma_f32_32x32x16_bf16 v[48:63], a[104:107], v[72:75], v[48:63]\n\t"
      "ds_read_b128 v[68:71], v13 offset:768\n\ts_add_u32 m0, s50, 12288\n\ts_waitcnt lgkmcnt(2)\n\tv_mfma_f32_32x32x16_bf16 v[48:63], a[108:111], v[76:79], v[48:63]\n\t"
      "ds_read_b128 v[72:75], v14 offset:768\n\tbuffer_load_dwordx4 v22, s[44:47], s20 offen offset:0  lds\n\ts_waitcnt lgkmcnt(2)\n\t"
      "v_mfma_f32_32x32x16_bf16 v[48:63], a[112:115], v[64:67], v[48:63]\n\tds_read_b128 v[76:79], v15 offset:768\n\ts_add_u32 m0, s50, 12288\n\ts_waitcnt lgkmcnt(2)\n\t"
      "v_mfma_f32_32x32x16_bf16 v[48:63], a[116:119], v[68:71], v[48:63]\n\tds_read_b128 v[64:67], v8 offset:1024\n\t"
      "buffer_load_dwordx4 v22, s[44:47], s20 offen offset:1024  lds\n\ts_waitcnt lgkmcnt(2)\n\tv_mfma_f32_32x32x16_bf16 v[48:63], a[120:123], v[72:75], v[48:63]\n\t"
      "ds_read_b128 v[68:71], v9 offset:1024\n\ts_add_u32 m0, s50, 14336\n\ts_waitcnt lgkmcnt(2)\n\tv_mfma_f32_32x32x16_bf16 v[48:63], a[124:127], v[76:79], v[48:63]\n\t"
      "ds_read_b128 v[72:75], v10 offset:1024\n\tbuffer_load_dwordx4 v23, s[44:47], s20 offen offset:0  lds\n\ts_waitcnt lgkmcnt(2)\n\t"
      "v_mfma_f32_32x32x16_bf16 v[48:63], a[128:131], v[64:67], v[48:63]\n\tds_read_b128 v[76:79], v11 offset:1024\n\ts_add_u32 m0, s50, 14336\n\t"
      TK1
      "s_waitcnt vmcnt(11)\n\t"
      TK2
      "s_barrier\n\t"
      TK3
      "s_waitcnt lgkmcnt(2)\n\tv_mfma_f32_32x32x16_bf16 v[48:63], a[132:135], v[68:71], v[48:63]\n\tds_read_b128 v[64:67], v12 offset:1024\n\t"
      "buffer_load_dwordx4 v23, s[44:47], s20 offen offset:1024  lds\n\ts_waitcnt lgkmcnt(2)\n\tv_mfma_f32_32x32x16_bf16 v[48:63], a[136:139], v[72:75], v[48:63]\n\t"
      "ds_read_b128 v[68:71], v13 offset:1024\n\tbuffer_store_dword v28, v27, s[12:15], s22 offen sc1\n\ts_waitcnt lgkmcnt(2)\n\t"
      "v_mfma_f32_32x32x16_bf16 v[48:63], a[140:143], v[76:79], v[48:63]\n\tds_read_b128 v[72:75], v14 offset:1024\n\t"
      "buffer_store_dwordx4 v[80:83], v91, s[8:11], s19 offen offset:0 sc1\n\ts_waitcnt lgkmcnt(2)\n\tv_mfma_f32_32x32x16_bf16 v[48:63], a[144:147], v[64:67], v[48:63]\n\t"
      "ds_read_b128 v[76:79], v15 offset:1024\n\tbuffer_store_dwordx4 v[84:87], v91, s[8:11], s19 offen offset:512 sc1\n\ts_waitcnt lgkmcnt(2)\n\t"
      "v_mfma_f32_32x32x16_bf16 v[48:63], a[148:151], v[68:71], v[48:63]\n\tds_read_b128 v[64:67], v8 offset:1280\n\ts_add_u32 s48, s17, 5\n\ts_min_u32 s48, s48, 63\n\t"
      "s_lshl_b32 s21, s48, 16\n\ts_waitcnt lgkmcnt(2)\n\tv_mfma_f32_32x32x16_bf16 v[48:63], a[152:155], v[72:75], v[48:63]\n\tds_read_b128 v[68:71], v9 offset:1280\n\t"
      "s_waitcnt lgkmcnt(2)\n\tv_mfma_f32_32x32x16_bf16 v[48:63], a[156:159], v[76:79], v[48:63]\n\tds_read_b128 v[72:75], v10 offset:1280\n\ts_waitcnt lgkmcnt(2)\n\t"
      "v_mfma_f32_32x32x16_bf16 v[48:63], a[160:163], v[64:67], v[48:63]\n\tds_read_b128 v[76:79], v11 offset:1280\n\ts_waitcnt lgkmcnt(2)\n\t"
      "v_mfma_f32_32x32x16_bf16 v[48:63], a[164:167], v[68:71], v[48:63]\n\tds_read_b128 v[64:67], v12 offset:1280\n\ts_waitcnt lgkmcnt(2)\n\t"
      "v_mfma_f32_32x32x16_bf16 v[48:63], a[168:171], v[72:75], v[48:63]\n\tds_read_b128 v[68:71], v13 offset:1280\n\t"
      "s_mul_i32 s26, s17, s28\n\ts_lshr_b32 s26, s26, 6\n\ts_add_u32 s26, s26, s27\n\ts_waitcnt lgkmcnt(2)\n\t"
      "v_mfma_f32_32x32x16_bf16 v[48:63], a[172:175], v[76:79], v[48:63]\n\tds_read_b128 v[72:75], v14 offset:1280\n\ts_waitcnt lgkmcnt(2)\n\t"
      "v_mfma_f32_32x32x16_bf16 v[48:63], a[176:179], v[64:67], v[48:63]\n\tds_read_b128 v[76:79], v15 offset:1280\n\ts_waitcnt lgkmcnt(2)\n\t"
      "v_mfma_f32_32x32x16_bf16 v[48:63], a[180:183], v[68:71], v[48:63]\n\tds_read_b128 v[64:67], v8 offset:1536\n\ts_waitcnt lgkmcnt(2)\n\t"
      "v_mfma_f32_32x32x16_bf16 v[48:63], a[184:187], v[72:75], v[48:63]\n\tds_read_b128 v[68:71], v9 offset:1536\n\ts_waitcnt lgkmcnt(2)\n\t"
      "v_mfma_f32_32x32x16_bf16 v[48:63], a[188:191], v[76:79], v[48:63]\n\tds_read_b128 v[72:75], v10 offset:1536\n\ts_waitcnt lgkmcnt(2)\n\t"
      "v_mfma_f32_32x32x16_bf16 v[48:63], a[192:195], v[64:67], v[48:63]\n\tds_read_b128 v[76:79], v11 offset:1536\n\ts_waitcnt lgkmcnt(2)\n\t"
      "v_mfma_f32_32x32x16_bf16 v[48:63], a[196:199], v[68:71], v[48:63]\n\tds_read_b128 v[64:67], v12 offset:1536\n\ts_waitcnt lgkmcnt(2)\n\t"
      "v_mfma_f32_32x32x16_bf16 v[48:63], a[200:203], v[72:75], v[48:63]\n\tds_read_b128 v[68:71], v13 offset:1536\n\ts_waitcnt lgkmcnt(2)\n\t"
      "v_mfma_f32_32x32x16_bf16 v[48:63], a[204:207], v[76:79], v[48:63]\n\tds_read_b128 v[72:75], v14 offset:1536\n\ts_waitcnt lgkmcnt(2)\n\t"
      "v_mfma_f32_32x32x16_bf16 v[48:63], a[208:211], v[64:67], v[48:63]\n\tds_read_b128 v[76:79], v15 offset:1536\n\ts_waitcnt lgkmcnt(2)\n\t"
      "v_mfma_f32_32x32x16_bf16 v[48:63], a[212:215], v[68:71], v[48:63]\n\tds_read_b128 v[64:67], v8 offset:1792\n\ts_waitcnt lgkmcnt(2)\n\t"
      "v_mfma_f32_32x32x16_bf16 v[48:63], a[216:219], v[72:75], v[48:63]\n\tds_read_b128 v[68:71], v9 offset:1792\n\ts_add_u32 s48, s19, 0x4000\n\t"
      "s_and_b32 s48, s48, 0x3c000\n\ts_andn2_b32 s19, s19, 0x3c000\n\ts_or_b32 s19, s19, s48\n\ts_waitcnt lgkmcnt(2)\n\t"
      "v_mfma_f32_32x32x16_bf16 v[48:63], a[220:223], v[76:79], v[48:63]\n\tds_read_b128 v[72:75], v10 offset:1792\n\tv_mov_b32 v29, v24\n\ts_waitcnt lgkmcnt(2)\n\t"
      "v_mfma_f32_32x32x16_bf16 v[48:63], a[224:227], v[64:67], v[48:63]\n\tds_read_b128 v[76:79], v11 offset:1792\n\ts_cmp_ge_u32 s17, 16\n\ts_cselect_b32 s25, s24, 0\n\t"
      "s_lshl_b32 s48, s25, 30\n\tv_or_b32 v90, s48, v29\n\ts_waitcnt lgkmcnt(2)\n\tv_mfma_f32_32x32x16_bf16 v[48:63], a[228:231], v[68:71], v[48:63]\n\t"
      "ds_read_b128 v[64:67], v12 offset:1792\n\ts_xor_b32 s48, s48, 0x40000000\n\tv_or_b32 v91, s48, v29\n\ts_waitcnt lgkmcnt(2)\n\t"
      "v_mfma_f32_32x32x16_bf16 v[48:63], a[232:235], v[72:75], v[48:63]\n\tds_read_b128 v[68:71], v13 offset:1792\n\ts_waitcnt lgkmcnt(2)\n\t"
      "v_mfma_f32_32x32x16_bf16 v[48:63], a[236:239], v[76:79], v[48:63]\n\tds_read_b128 v[72:75], v14 offset:1792\n\ts_waitcnt lgkmcnt(2)\n\t"
      "v_mfma_f32_32x32x16_bf16 v[48:63], a[240:243], v[64:67], v[48:63]\n\tds_read_b128 v[76:79], v15 offset:1792\n\ts_waitcnt lgkmcnt(2)\n\t"
      "v_mfma_f32_32x32x16_bf16 v[48:63], a[244:247], v[68:71], v[48:63]\n\t"
      TK4
      "s_waitcnt vmcnt(3)\n\t"
      TK5
      "s_barrier\n\t"
      TK6
      "ds_read_b128 v[64:67], v0 offset:0\n\ts_waitcnt lgkmcnt(2)\n\tv_mfma_f32_32x32x16_bf16 v[48:63], a[248:251], v[72:75], v[48:63]\n\tds_read_b128 v[68:71], v1 offset:0\n\t"
      "v_readfirstlane_b32 s23, v89\n\ts_waitcnt lgkmcnt(2)\n\tv_mfma_f32_32x32x16_bf16 v[48:63], a[252:255], v[76:79], v[48:63]\n\tds_read_b128 v[72:75], v2 offset:0\n\t"
      "buffer_load_dword v89, off, s[12:15], 0 offset:3840 sc1\n\t"
      STF("s_memrealtime vcc\n\ts_waitcnt lgkmcnt(0)\n\tv_mov_b32 v95, vcc_lo\n\ts_lshl_b32 vcc_hi, s17, 2\n\ts_add_u32 vcc_hi, vcc_hi, 161056\n\tv_mov_b32 v94, vcc_hi\n\tds_write_b32 v94, v95\n\ts_memtime vcc\n\ts_waitcnt lgkmcnt(0)\n\tv_mov_b32 v95, vcc_lo\n\tds_write_b32 v94, v95 offset:400\n\ts_waitcnt lgkmcnt(0)\n\t")
      TK7
      TKS("s_waitcnt lgkmcnt(0)\n\ts_cmp_lt_i32 s17, 0\n\ts_cbranch_scc1 98f\n\ts_cmp_lg_u32 s16, s91\n\ts_cbranch_scc1 98f\n\ts_mov_b64 exec, 1\n\ts_lshl_b32 s48, s17, 4\n\ts_add_u32 s48, s48, 161056\n\tv_mov_b32 v92, s48\n\ts_sub_u32 s48, s74, s90\n\ts_min_u32 s48, s48, 0xffff\n\tv_mov_b32 v93, s48\n\tds_write_b16 v92, v93 offset:0\n\ts_sub_u32 s48, s76, s74\n\ts_min_u32 s48, s48, 0xffff\n\tv_mov_b32 v93, s48\n\tds_write_b16 v92, v93 offset:2\n\ts_sub_u32 s48, s78, s76\n\ts_min_u32 s48, s48, 0xffff\n\tv_mov_b32 v93, s48\n\tds_write_b16 v92, v93 offset:4\n\ts_sub_u32 s48, s80, s78\n\ts_min_u32 s48, s48, 0xffff\n\tv_mov_b32 v93, s48\n\tds_write_b16 v92, v93 offset:6\n\ts_sub_u32 s48, s82, s80\n\ts_min_u32 s48, s48, 0xffff\n\tv_mov_b32 v93, s48\n\tds_write_b16 v92, v93 offset:8\n\ts_sub_u32 s48, s84, s82\n\ts_min_u32 s48, s48, 0xffff\n\tv_mov_b32 v93, s48\n\tds_write_b16 v92, v93 offset:10\n\ts_sub_u32 s48, s86, s84\n\ts_min_u32 s48, s48, 0xffff\n\tv_mov_b32 v93, s48\n\tds_write_b16 v92, v93 offset:12\n\ts_sub_u32 s48, s88, s86\n\ts_min_u32 s48, s48, 0xffff\n\tv_mov_b32 v93, s48\n\tds_write_b16 v92, v93 offset:14\n\ts_mov_b64 exec, -1\n\t98:\n\ts_mov_b32 s90, s88\n\t")
      "s_add_u32 s17, s17, 1\n\ts_cmp_lt_u32 s17, 64\n\ts_cbranch_scc1 10b\n\ts_branch 26f\n\t"
      "16:\n\ts_nop 11\n\tv_cvt_pk_bf16_f32 v80, v48, v52\n\tv_cvt_pk_bf16_f32 v81, v56, v60\n\tv_cvt_pk_bf16_f32 v82, v49, v53\n\tv_cvt_pk_bf16_f32 v83, v57, v61\n\t"
      "v_cvt_pk_bf16_f32 v84, v50, v54\n\tv_cvt_pk_bf16_f32 v85, v58, v62\n\tv_cvt_pk_bf16_f32 v86, v51, v55\n\tv_cvt_pk_bf16_f32 v87, v59, v63\n\t"
      "buffer_store_dwordx4 v[80:83], v24, s[8:11], s19 offen offset:0 sc1\n\tbuffer_store_dwordx4 v[84:87], v24, s[8:11], s19 offen offset:512 sc1\n\tv_mov_b32 v28, 64\n\t"
      "s_waitcnt vmcnt(0) lgkmcnt(0)\n\ts_barrier\n\tbuffer_store_dword v28, v27, s[12:15], s22 offen sc1\n\ts_branch 30f\n\t"
      "24:\n\tbuffer_load_dword v89, off, s[12:15], 0 offset:3840 sc1\n\ts_waitcnt vmcnt(0)\n\tv_readfirstlane_b32 s23, v89\n\ts_add_u32 s23, s23, s26\n\t"
      "s_cmp_le_u32 s17, s23\n\ts_cbranch_scc1 14b\n\ts_cmp_eq_u32 s18, 0\n\ts_cbranch_scc1 14b\n\ts_sub_u32 s18, s18, 1\n\ts_sleep 8\n\ts_branch 24b\n\t"
      "25:\n\tbuffer_load_dword v89, off, s[12:15], 0 offset:3840 sc1\n\ts_waitcnt vmcnt(0)\n\tv_readfirstlane_b32 s23, v89\n\ts_add_u32 s23, s23, s26\n\t"
      "s_cmp_le_u32 s17, s23\n\ts_cbranch_scc1 15b\n\ts_cmp_eq_u32 s18, 0\n\ts_cbranch_scc1 15b\n\ts_sub_u32 s18, s18, 1\n\ts_sleep 8\n\ts_branch 25b\n\t"
      "26:\n\tbuffer_load_dword v89, off, s[12:15], 0 offset:3840 sc1\n\ts_waitcnt vmcnt(0)\n\tv_readfirstlane_b32 s23, v89\n\ts_add_u32 s23, s23, s26\n\t"
      "s_cmp_le_u32 s17, s23\n\ts_cbranch_scc1 16b\n\ts_cmp_eq_u32 s18, 0\n\ts_cbranch_scc1 16b\n\ts_sub_u32 s18, s18, 1\n\ts_sleep 8\n\ts_branch 26b\n\t"
      "20:\n\t"
      "1:\n\tbuffer_load_dword v26, off, s[12:15], s63 sc1\n\ts_waitcnt vmcnt(0)\n\tv_readfirstlane_b32 s49, v26\n\ts_bitcmp1_b32 s49, 0\n\ts_cbranch_scc1 2f\n\t"
      "s_cmp_eq_u32 s18, 0\n\ts_cbranch_scc1 2f\n\ts_sub_u32 s18, s18, 1\n\ts_sleep 2\n\ts_branch 1b\n\t"
      "2:\n\ts_and_b32 s48, s49, 3\n\ts_cmp_lg_u32 s48, 3\n\ts_cbranch_scc1 4f\n\ts_cmp_eq_u32 s62, 0\n\ts_cbranch_scc1 4f\n\ts_waitcnt lgkmcnt(0)\n\ts_barrier\n\t"
      "s_mov_b64 s[0:1], s[60:61]\n\ts_mov_b32 s2, s62\n\ts_lshl_b32 s48, s16, 6\n\tv_mbcnt_lo_u32_b32 v0, -1, 0\n\tv_mbcnt_hi_u32_b32 v0, -1, v0\n\tv_add_u32 v0, s48, v0\n\t"
      "s_getpc_b64 s[4:5]\n\t"
      "3:\n\ts_sub_u32 s4, s4, 3b-mk_mega\n\ts_subb_u32 s5, s5, 0\n\ts_setpc_b64 s[4:5]\n\t"
      "4:\n\ts_lshl_b32 s49, s49, 11\n\ts_and_b32 s49, s49, 0x2000\n\ts_xor_b32 s19, s19, s49\n\ts_nop 11\n\tv_cvt_pk_bf16_f32 v80, v32, v36\n\t"
      "v_cvt_pk_bf16_f32 v81, v40, v44\n\tv_cvt_pk_bf16_f32 v82, v33, v37\n\tv_cvt_pk_bf16_f32 v83, v41, v45\n\tv_cvt_pk_bf16_f32 v84, v34, v38\n\t"
      "v_cvt_pk_bf16_f32 v85, v42, v46\n\tv_cvt_pk_bf16_f32 v86, v35, v39\n\tv_cvt_pk_bf16_f32 v87, v43, v47\n\t"
      "buffer_store_dwordx4 v[80:83], v24, s[8:11], s19 offen offset:0 sc1\n\tbuffer_store_dwordx4 v[84:87], v24, s[8:11], s19 offen offset:512 sc1\n\ts_mov_b32 s23, 0\n\t"
      "v_mov_b32 v89, 0\n\tv_mov_b32 v27, v88\n\tv_mov_b32 v28, 1\n\ts_waitcnt vmcnt(0)\n\ts_barrier\n\tbuffer_store_dword v28, v27, s[12:15], s22 offen sc1\n\ts_branch 11b\n\t"
      "30:\n\t"
      ".Lmk_end2:\n\ts_waitcnt vmcnt(0) lgkmcnt(0)\n\t"
      TKS("s_mul_i32 s48, s16, 72\n\ts_add_u32 s48, s48, 162080\n\tv_mov_b32 v224, s48\n\tds_read_b32 v225, v224 offset:0\n\ts_waitcnt lgkmcnt(0)\n\tv_readfirstlane_b32 s74, v225\n\tds_read_b32 v225, v224 offset:4\n\ts_waitcnt lgkmcnt(0)\n\tv_readfirstlane_b32 s75, v225\n\tds_read_b32 v225, v224 offset:8\n\ts_waitcnt lgkmcnt(0)\n\tv_readfirstlane_b32 s76, v225\n\tds_read_b32 v225, v224 offset:12\n\ts_waitcnt lgkmcnt(0)\n\tv_readfirstlane_b32 s77, v225\n\tds_read_b32 v225, v224 offset:16\n\ts_waitcnt lgkmcnt(0)\n\tv_readfirstlane_b32 s78, v225\n\tds_read_b32 v225, v224 offset:20\n\ts_waitcnt lgkmcnt(0)\n\tv_readfirstlane_b32 s79, v225\n\tds_read_b32 v225, v224 offset:24\n\ts_waitcnt lgkmcnt(0)\n\tv_readfirstlane_b32 s80, v225\n\tds_read_b32 v225, v224 offset:28\n\ts_waitcnt lgkmcnt(0)\n\tv_readfirstlane_b32 s81, v225\n\tds_read_b32 v225, v224 offset:32\n\ts_waitcnt lgkmcnt(0)\n\tv_readfirstlane_b32 s82, v225\n\tds_read_b32 v225, v224 offset:36\n\ts_waitcnt lgkmcnt(0)\n\tv_readfirstlane_b32 s83, v225\n\tds_read_b32 v225, v224 offset:40\n\ts_waitcnt lgkmcnt(0)\n\tv_readfirstlane_b32 s84, v225\n\tds_read_b32 v225, v224 offset:44\n\ts_waitcnt lgkmcnt(0)\n\tv_readfirstlane_b32 s85, v225\n\tds_read_b32 v225, v224 offset:48\n\ts_waitcnt lgkmcnt(0)\n\tv_readfirstlane_b32 s86, v225\n\tds_read_b32 v225, v224 offset:52\n\ts_waitcnt lgkmcnt(0)\n\tv_readfirstlane_b32 s87, v225\n\tds_read_b32 v225, v224 offset:56\n\ts_waitcnt lgkmcnt(0)\n\tv_readfirstlane_b32 s88, v225\n\tds_read_b32 v225, v224 offset:60\n\ts_waitcnt lgkmcnt(0)\n\tv_readfirstlane_b32 s89, v225\n\tds_read_b32 v225, v224 offset:64\n\ts_waitcnt lgkmcnt(0)\n\tv_readfirstlane_b32 s90, v225\n\tds_read_b32 v225, v224 offset:68\n\ts_waitcnt lgkmcnt(0)\n\tv_readfirstlane_b32 s91, v225\n\t")
      STS("s_lshl_b32 s48, s16, 2\n\ts_add_u32 s48, s48, 162944\n\tv_mov_b32 v0, s48\n\tv_mov_b32 v1, s50\n\tds_write_b32 v0, v1\n\ts_cmp_lg_u32 s16, 0\n\ts_cbranch_scc1 97f\n\tv_mov_b32 v1, s51\n\tds_write_b32 v0, v1 offset:16\n\t97:\n\ts_cmp_lg_u32 s16, 0\n\ts_cbranch_scc1 95f\n\tv_mov_b32 v1, s61\n\tds_write_b32 v0, v1 offset:20\n\tv_mov_b32 v1, s21\n\tds_write_b32 v0, v1 offset:24\n\tv_mov_b32 v1, s22\n\tds_write_b32 v0, v1 offset:28\n\t95:\n\t")
      "s_cmp_lg_u32 s18, 0\n\ts_cbranch_scc1 96f\n\tv_mov_b32 v1, 1\n\tbuffer_store_dword v1, off, s[12:15], 0 offset:3328 sc1\n\t"
      "96:\n\t"
      : "+{v[0:31]}"(k), "+{v[160:191]}"(s0), "+{v[192:223]}"(s1), ALL_REGS :: "memory", "vcc", "scc", S_CLOB TK_CLOB);
}

DEV void zc_role(Ctx& c, int h) {
  const KArg p = enter(c);
  c.budget = SPIN_CAP;
  const int w = c.w, lane = fresh_lane(), g = lane >> 4, h2 = lane >> 5, q = (lane >> 2) & 3, pp = lane & 3;
  v32u k;
#pragma unroll
  for (int i = 0; i < 32; ++i) k[i] = 0;
  k[0] = lane * 16 + (w && (HACK & 64) ? 1 << 30 : 0);
  k[1] = (16 * w + (g & 1)) * 65536 + amap(h * 1024, w >> 1) + h2 * 256 + (lane & 15) * 16 + ((HACK & 2) << 29);
  k[6] = lane == 0 ? C_FLAG + (F_V + h) * 128 : C_PROG;
  k[3] = lane ? 1 << 30 : 0;
#pragma unroll
  for (int i = 0; i < 2; ++i) k[8 + i] = (h * 128 + 16 * h2) * 2048 + w * 512 + (((lane & 31) ^ ((2 * i + h2) << 2)) << 4);
#pragma unroll
  for (int i = 0; i < 4; ++i) k[10 + i] = w * 32768 + (8 * h2 + q) * 512 + (((4 * i + 2 * (g & 1) + (pp >> 1)) ^ (q << 2)) << 4) + 8 * (pp & 1);
  v32u s0 = {RSRCS, (unsigned)w, 0, c.budget, (unsigned)(R_VX + hblock(h, 8192)), R_Z, (unsigned)(C_FLAG + (F_V + h) * 128), (unsigned)(C_FLAG + (F_Z + w * 32 + h) * 128)};
  v32u s1 = {plo(p->Wo), phi(p->Wo), 1024 * 4096 * 2, 0x00020000, 0, 0, 0, 0, 0, 0, 0, (unsigned)(w * 32768), 0, 0, 0, 0, 0, 0, 0, 0, ENTRY, 0, 0, 0, 0, HELLO};
  v32u wa[8], wv[5];
  asm volatile(
      "s_nop 0\n\tv_readfirstlane_b32 s8, v160\n\tv_readfirstlane_b32 s9, v161\n\tv_readfirstlane_b32 s10, v162\n\tv_readfirstlane_b32 s11, v163\n\t"
      "v_readfirstlane_b32 s12, v164\n\tv_readfirstlane_b32 s13, v165\n\tv_readfirstlane_b32 s14, v166\n\tv_readfirstlane_b32 s15, v167\n\tv_readfirstlane_b32 s16, v168\n\t"
      "v_readfirstlane_b32 s17, v169\n\tv_readfirstlane_b32 s18, v170\n\tv_readfirstlane_b32 s19, v171\n\tv_readfirstlane_b32 s20, v172\n\tv_readfirstlane_b32 s21, v173\n\t"
      "v_readfirstlane_b32 s22, v174\n\tv_readfirstlane_b32 s40, v192\n\tv_readfirstlane_b32 s41, v193\n\tv_readfirstlane_b32 s42, v194\n\tv_readfirstlane_b32 s43, v195\n\t"
      "v_readfirstlane_b32 s51, v203\n\tv_readfirstlane_b32 s60, v212\n\tv_readfirstlane_b32 s61, v213\n\tv_readfirstlane_b32 s62, v214\n\tv_readfirstlane_b32 s63, v215\n\t"
      "v_readfirstlane_b32 s68, v220\n\tv_readfirstlane_b32 s69, v221\n\tv_readfirstlane_b32 s70, v222\n\ts_nop 4\n\t"
      TKS("s_mul_i32 s48, s16, 72\n\ts_add_u32 s48, s48, 162080\n\tv_mov_b32 v224, s48\n\tv_mov_b32 v225, s74\n\tds_write_b32 v224, v225 offset:0\n\tv_mov_b32 v225, s75\n\tds_write_b32 v224, v225 offset:4\n\tv_mov_b32 v225, s76\n\tds_write_b32 v224, v225 offset:8\n\tv_mov_b32 v225, s77\n\tds_write_b32 v224, v225 offset:12\n\tv_mov_b32 v225, s78\n\tds_write_b32 v224, v225 offset:16\n\tv_mov_b32 v225, s79\n\tds_write_b32 v224, v225 offset:20\n\tv_mov_b32 v225, s80\n\tds_write_b32 v224, v225 offset:24\n\tv_mov_b32 v225, s81\n\tds_write_b32 v224, v225 offset:28\n\tv_mov_b32 v225, s82\n\tds_write_b32 v224, v225 offset:32\n\tv_mov_b32 v225, s83\n\tds_write_b32 v224, v225 offset:36\n\tv_mov_b32 v225, s84\n\tds_write_b32 v224, v225 offset:40\n\tv_mov_b32 v225, s85\n\tds_write_b32 v224, v225 offset:44\n\tv_mov_b32 v225, s86\n\tds_write_b32 v224, v225 offset:48\n\tv_mov_b32 v225, s87\n\tds_write_b32 v224, v225 offset:52\n\tv_mov_b32 v225, s88\n\tds_write_b32 v224, v225 offset:56\n\tv_mov_b32 v225, s89\n\tds_write_b32 v224, v225 offset:60\n\tv_mov_b32 v225, s90\n\tds_write_b32 v224, v225 offset:64\n\tv_mov_b32 v225, s91\n\tds_write_b32 v224, v225 offset:68\n\t")
      "s_buffer_load_dword s71, s[12:15], s69 glc\n\ts_add_u32 m0, s51, 0\n\ts_mov_b32 s48, 0\n\tbuffer_load_dwordx4 v8, s[40:43], s48 offen  lds\n\ts_add_u32 m0, s51, 1024\n\t"
      "s_mov_b32 s48, 65536\n\tbuffer_load_dwordx4 v9, s[40:43], s48 offen  lds\n\ts_add_u32 m0, s51, 2048\n\ts_mov_b32 s48, 2048\n\t"
      "buffer_load_dwordx4 v8, s[40:43], s48 offen  lds\n\ts_add_u32 m0, s51, 3072\n\ts_mov_b32 s48, 67584\n\tbuffer_load_dwordx4 v9, s[40:43], s48 offen  lds\n\t"
      "s_add_u32 m0, s51, 4096\n\ts_mov_b32 s48, 4096\n\tbuffer_load_dwordx4 v8, s[40:43], s48 offen  lds\n\ts_add_u32 m0, s51, 5120\n\ts_mov_b32 s48, 69632\n\t"
      "buffer_load_dwordx4 v9, s[40:43], s48 offen  lds\n\ts_add_u32 m0, s51, 6144\n\ts_mov_b32 s48, 6144\n\tbuffer_load_dwordx4 v8, s[40:43], s48 offen  lds\n\t"
      "s_add_u32 m0, s51, 7168\n\ts_mov_b32 s48, 71680\n\tbuffer_load_dwordx4 v9, s[40:43], s48 offen  lds\n\ts_waitcnt lgkmcnt(0)\n\ts_bitcmp0_b32 s68, 0\n\t"
      "s_cbranch_scc1 5f\n\ts_add_u32 s71, s71, 1\n\ts_lshl_b32 s48, s69, 4\n\ts_add_u32 s48, s48, 36864\n\tv_mov_b32 v5, s71\n\ts_mov_b64 exec, 1\n\t"
      "buffer_store_dword v5, off, s[12:15], s48 sc1\n\ts_mov_b64 exec, -1\n\t"
      "5:\n\ts_add_u32 m0, s51, 8192\n\ts_mov_b32 s48, 8192\n\tbuffer_load_dwordx4 v8, s[40:43], s48 offen  lds\n\ts_add_u32 m0, s51, 9216\n\ts_mov_b32 s48, 73728\n\t"
      "buffer_load_dwordx4 v9, s[40:43], s48 offen  lds\n\ts_add_u32 m0, s51, 10240\n\ts_mov_b32 s48, 10240\n\tbuffer_load_dwordx4 v8, s[40:43], s48 offen  lds\n\t"
      "s_add_u32 m0, s51, 11264\n\ts_mov_b32 s48, 75776\n\tbuffer_load_dwordx4 v9, s[40:43], s48 offen  lds\n\ts_add_u32 m0, s51, 12288\n\ts_mov_b32 s48, 12288\n\t"
      "buffer_load_dwordx4 v8, s[40:43], s48 offen  lds\n\ts_add_u32 m0, s51, 13312\n\ts_mov_b32 s48, 77824\n\tbuffer_load_dwordx4 v9, s[40:43], s48 offen  lds\n\t"
      "s_add_u32 m0, s51, 14336\n\ts_mov_b32 s48, 14336\n\tbuffer_load_dwordx4 v8, s[40:43], s48 offen  lds\n\ts_add_u32 m0, s51, 15360\n\ts_mov_b32 s48, 79872\n\t"
      "buffer_load_dwordx4 v9, s[40:43], s48 offen  lds\n\ts_add_u32 m0, s51, 16384\n\ts_mov_b32 s48, 16384\n\tbuffer_load_dwordx4 v8, s[40:43], s48 offen  lds\n\t"
      "s_add_u32 m0, s51, 17408\n\ts_mov_b32 s48, 81920\n\tbuffer_load_dwordx4 v9, s[40:43], s48 offen  lds\n\ts_add_u32 m0, s51, 18432\n\ts_mov_b32 s48, 18432\n\t"
      "buffer_load_dwordx4 v8, s[40:43], s48 offen  lds\n\ts_add_u32 m0, s51, 19456\n\ts_mov_b32 s48, 83968\n\tbuffer_load_dwordx4 v9, s[40:43], s48 offen  lds\n\t"
      "s_add_u32 m0, s51, 20480\n\ts_mov_b32 s48, 20480\n\tbuffer_load_dwordx4 v8, s[40:43], s48 offen  lds\n\ts_add_u32 m0, s51, 21504\n\ts_mov_b32 s48, 86016\n\t"
      "buffer_load_dwordx4 v9, s[40:43], s48 offen  lds\n\ts_add_u32 m0, s51, 22528\n\ts_mov_b32 s48, 22528\n\tbuffer_load_dwordx4 v8, s[40:43], s48 offen  lds\n\t"
      "s_add_u32 m0, s51, 23552\n\ts_mov_b32 s48, 88064\n\tbuffer_load_dwordx4 v9, s[40:43], s48 offen  lds\n\ts_cmp_lt_u32 s68, 1\n\ts_cbranch_scc1 7f\n\t"
      "s_cmp_eq_u32 s68, 2\n\ts_cbranch_scc1 6f\n\t"
      "5:\n\ts_buffer_load_dword s48, s[12:15], s69 glc\n\ts_waitcnt lgkmcnt(0)\n\ts_cmp_eq_u32 s48, s71\n\ts_cbranch_scc1 6f\n\ts_cmp_lg_u32 s68, 3\n\ts_cbranch_scc1 7f\n\t"
      "s_cmp_eq_u32 s18, 0\n\ts_cbranch_scc1 6f\n\ts_sub_u32 s18, s18, 1\n\ts_branch 5b\n\t"
      "6:\n\tv_mov_b32 v5, 1\n\ts_mov_b64 exec, 1\n\tbuffer_atomic_add v5, off, s[12:15], s70\n\ts_mov_b64 exec, -1\n\ts_mov_b32 s68, 0\n\t"
      "7:\n\ts_waitcnt vmcnt(16)\n\tds_read_b64_tr_b16 a[0:1], v10 offset:0\n\tds_read_b64_tr_b16 a[2:3], v10 offset:2048\n\tds_read_b64_tr_b16 a[32:33], v11 offset:0\n\t"
      "ds_read_b64_tr_b16 a[34:35], v11 offset:2048\n\tds_read_b64_tr_b16 a[64:65], v12 offset:0\n\tds_read_b64_tr_b16 a[66:67], v12 offset:2048\n\t"
      "ds_read_b64_tr_b16 a[96:97], v13 offset:0\n\tds_read_b64_tr_b16 a[98:99], v13 offset:2048\n\tds_read_b64_tr_b16 a[128:129], v10 offset:256\n\t"
      "ds_read_b64_tr_b16 a[130:131], v10 offset:2304\n\tds_read_b64_tr_b16 a[160:161], v11 offset:256\n\tds_read_b64_tr_b16 a[162:163], v11 offset:2304\n\t"
      "ds_read_b64_tr_b16 a[192:193], v12 offset:256\n\tds_read_b64_tr_b16 a[194:195], v12 offset:2304\n\tds_read_b64_tr_b16 a[224:225], v13 offset:256\n\t"
      "ds_read_b64_tr_b16 a[226:227], v13 offset:2304\n\ts_waitcnt lgkmcnt(0)\n\ts_add_u32 m0, s51, 24576\n\ts_mov_b32 s48, 24576\n\t"
      "buffer_load_dwordx4 v8, s[40:43], s48 offen  lds\n\ts_add_u32 m0, s51, 25600\n\ts_mov_b32 s48, 90112\n\tbuffer_load_dwordx4 v9, s[40:43], s48 offen  lds\n\t"
      "s_add_u32 m0, s51, 26624\n\ts_mov_b32 s48, 26624\n\tbuffer_load_dwordx4 v8, s[40:43], s48 offen  lds\n\ts_add_u32 m0, s51, 27648\n\ts_mov_b32 s48, 92160\n\t"
      "buffer_load_dwordx4 v9, s[40:43], s48 offen  lds\n\ts_add_u32 m0, s51, 28672\n\ts_mov_b32 s48, 28672\n\tbuffer_load_dwordx4 v8, s[40:43], s48 offen  lds\n\t"
      "s_add_u32 m0, s51, 29696\n\ts_mov_b32 s48, 94208\n\tbuffer_load_dwordx4 v9, s[40:43], s48 offen  lds\n\ts_add_u32 m0, s51, 30720\n\ts_mov_b32 s48, 30720\n\t"
      "buffer_load_dwordx4 v8, s[40:43], s48 offen  lds\n\ts_add_u32 m0, s51, 31744\n\ts_mov_b32 s48, 96256\n\tbuffer_load_dwordx4 v9, s[40:43], s48 offen  lds\n\t"
      "s_cmp_lt_u32 s68, 1\n\ts_cbranch_scc1 7f\n\ts_cmp_eq_u32 s68, 2\n\ts_cbranch_scc1 6f\n\t"
      "5:\n\ts_buffer_load_dword s48, s[12:15], s69 glc\n\ts_waitcnt lgkmcnt(0)\n\ts_cmp_eq_u32 s48, s71\n\ts_cbranch_scc0 7f\n\t"
      "6:\n\tv_mov_b32 v5, 1\n\ts_mov_b64 exec, 1\n\tbuffer_atomic_add v5, off, s[12:15], s70\n\ts_mov_b64 exec, -1\n\ts_mov_b32 s68, 0\n\t"
      "7:\n\ts_waitcnt vmcnt(16)\n\tds_read_b64_tr_b16 a[4:5], v10 offset:8192\n\tds_read_b64_tr_b16 a[6:7], v10 offset:10240\n\tds_read_b64_tr_b16 a[36:37], v11 offset:8192\n\t"
      "ds_read_b64_tr_b16 a[38:39], v11 offset:10240\n\tds_read_b64_tr_b16 a[68:69], v12 offset:8192\n\tds_read_b64_tr_b16 a[70:71], v12 offset:10240\n\t"
      "ds_read_b64_tr_b16 a[100:101], v13 offset:8192\n\tds_read_b64_tr_b16 a[102:103], v13 offset:10240\n\tds_read_b64_tr_b16 a[132:133], v10 offset:8448\n\t"
      "ds_read_b64_tr_b16 a[134:135], v10 offset:10496\n\tds_read_b64_tr_b16 a[164:165], v11 offset:8448\n\tds_read_b64_tr_b16 a[166:167], v11 offset:10496\n\t"
      "ds_read_b64_tr_b16 a[196:197], v12 offset:8448\n\tds_read_b64_tr_b16 a[198:199], v12 offset:10496\n\tds_read_b64_tr_b16 a[228:229], v13 offset:8448\n\t"
      "ds_read_b64_tr_b16 a[230:231], v13 offset:10496\n\ts_waitcnt lgkmcnt(0)\n\ts_add_u32 m0, s51, 0\n\ts_mov_b32 s48, 131072\n\t"
      "buffer_load_dwordx4 v8, s[40:43], s48 offen  lds\n\ts_add_u32 m0, s51, 1024\n\ts_mov_b32 s48, 196608\n\tbuffer_load_dwordx4 v9, s[40:43], s48 offen  lds\n\t"
      "s_add_u32 m0, s51, 2048\n\ts_mov_b32 s48, 133120\n\tbuffer_load_dwordx4 v8, s[40:43], s48 offen  lds\n\ts_add_u32 m0, s51, 3072\n\ts_mov_b32 s48, 198656\n\t"
      "buffer_load_dwordx4 v9, s[40:43], s48 offen  lds\n\ts_add_u32 m0, s51, 4096\n\ts_mov_b32 s48, 135168\n\tbuffer_load_dwordx4 v8, s[40:43], s48 offen  lds\n\t"
      "s_add_u32 m0, s51, 5120\n\ts_mov_b32 s48, 200704\n\tbuffer_load_dwordx4 v9, s[40:43], s48 offen  lds\n\ts_add_u32 m0, s51, 6144\n\ts_mov_b32 s48, 137216\n\t"
      "buffer_load_dwordx4 v8, s[40:43], s48 offen  lds\n\ts_add_u32 m0, s51, 7168\n\ts_mov_b32 s48, 202752\n\tbuffer_load_dwordx4 v9, s[40:43], s48 offen  lds\n\t"
      "s_cmp_lt_u32 s68, 1\n\ts_cbranch_scc1 7f\n\ts_cmp_eq_u32 s68, 2\n\ts_cbranch_scc1 6f\n\t"
      "5:\n\ts_buffer_load_dword s48, s[12:15], s69 glc\n\ts_waitcnt lgkmcnt(0)\n\ts_cmp_eq_u32 s48, s71\n\ts_cbranch_scc0 7f\n\t"
      "6:\n\tv_mov_b32 v5, 1\n\ts_mov_b64 exec, 1\n\tbuffer_atomic_add v5, off, s[12:15], s70\n\ts_mov_b64 exec, -1\n\ts_mov_b32 s68, 0\n\t"
      "7:\n\ts_waitcnt vmcnt(16)\n\tds_read_b64_tr_b16 a[8:9], v10 offset:16384\n\tds_read_b64_tr_b16 a[10:11], v10 offset:18432\n\t"
      "ds_read_b64_tr_b16 a[40:41], v11 offset:16384\n\tds_read_b64_tr_b16 a[42:43], v11 offset:18432\n\tds_read_b64_tr_b16 a[72:73], v12 offset:16384\n\t"
      "ds_read_b64_tr_b16 a[74:75], v12 offset:18432\n\tds_read_b64_tr_b16 a[104:105], v13 offset:16384\n\tds_read_b64_tr_b16 a[106:107], v13 offset:18432\n\t"
      "ds_read_b64_tr_b16 a[136:137], v10 offset:16640\n\tds_read_b64_tr_b16 a[138:139], v10 offset:18688\n\tds_read_b64_tr_b16 a[168:169], v11 offset:16640\n\t"
      "ds_read_b64_tr_b16 a[170:171], v11 offset:18688\n\tds_read_b64_tr_b16 a[200:201], v12 offset:16640\n\tds_read_b64_tr_b16 a[202:203], v12 offset:18688\n\t"
      "ds_read_b64_tr_b16 a[232:233], v13 offset:16640\n\tds_read_b64_tr_b16 a[234:235], v13 offset:18688\n\ts_waitcnt lgkmcnt(0)\n\ts_add_u32 m0, s51, 8192\n\t"
      "s_mov_b32 s48, 139264\n\tbuffer_load_dwordx4 v8, s[40:43], s48 offen  lds\n\ts_add_u32 m0, s51, 9216\n\ts_mov_b32 s48, 204800\n\t"
      "buffer_load_dwordx4 v9, s[40:43], s48 offen  lds\n\ts_add_u32 m0, s51, 10240\n\ts_mov_b32 s48, 141312\n\tbuffer_load_dwordx4 v8, s[40:43], s48 offen  lds\n\t"
      "s_add_u32 m0, s51, 11264\n\ts_mov_b32 s48, 206848\n\tbuffer_load_dwordx4 v9, s[40:43], s48 offen  lds\n\ts_add_u32 m0, s51, 12288\n\ts_mov_b32 s48, 143360\n\t"
      "buffer_load_dwordx4 v8, s[40:43], s48 offen  lds\n\ts_add_u32 m0, s51, 13312\n\ts_mov_b32 s48, 208896\n\tbuffer_load_dwordx4 v9, s[40:43], s48 offen  lds\n\t"
      "s_add_u32 m0, s51, 14336\n\ts_mov_b32 s48, 145408\n\tbuffer_load_dwordx4 v8, s[40:43], s48 offen  lds\n\ts_add_u32 m0, s51, 15360\n\ts_mov_b32 s48, 210944\n\t"
      "buffer_load_dwordx4 v9, s[40:43], s48 offen  lds\n\ts_cmp_lt_u32 s68, 1\n\ts_cbranch_scc1 7f\n\ts_cmp_eq_u32 s68, 2\n\ts_cbranch_scc1 6f\n\t"
      "5:\n\ts_buffer_load_dword s48, s[12:15], s69 glc\n\ts_waitcnt lgkmcnt(0)\n\ts_cmp_eq_u32 s48, s71\n\ts_cbranch_scc0 7f\n\t"
      "6:\n\tv_mov_b32 v5, 1\n\ts_mov_b64 exec, 1\n\tbuffer_atomic_add v5, off, s[12:15], s70\n\ts_mov_b64 exec, -1\n\ts_mov_b32 s68, 0\n\t"
      "7:\n\ts_waitcnt vmcnt(16)\n\tds_read_b64_tr_b16 a[12:13], v10 offset:24576\n\tds_read_b64_tr_b16 a[14:15], v10 offset:26624\n\t"
      "ds_read_b64_tr_b16 a[44:45], v11 offset:24576\n\tds_read_b64_tr_b16 a[46:47], v11 offset:26624\n\tds_read_b64_tr_b16 a[76:77], v12 offset:24576\n\t"
      "ds_read_b64_tr_b16 a[78:79], v12 offset:26624\n\tds_read_b64_tr_b16 a[108:109], v13 offset:24576\n\tds_read_b64_tr_b16 a[110:111], v13 offset:26624\n\t"
      "ds_read_b64_tr_b16 a[140:141], v10 offset:24832\n\tds_read_b64_tr_b16 a[142:143], v10 offset:26880\n\tds_read_b64_tr_b16 a[172:173], v11 offset:24832\n\t"
      "ds_read_b64_tr_b16 a[174:175], v11 offset:26880\n\tds_read_b64_tr_b16 a[204:205], v12 offset:24832\n\tds_read_b64_tr_b16 a[206:207], v12 offset:26880\n\t"
      "ds_read_b64_tr_b16 a[236:237], v13 offset:24832\n\tds_read_b64_tr_b16 a[238:239], v13 offset:26880\n\ts_waitcnt lgkmcnt(0)\n\ts_add_u32 m0, s51, 16384\n\t"
      "s_mov_b32 s48, 147456\n\tbuffer_load_dwordx4 v8, s[40:43], s48 offen  lds\n\ts_add_u32 m0, s51, 17408\n\ts_mov_b32 s48, 212992\n\t"
      "buffer_load_dwordx4 v9, s[40:43], s48 offen  lds\n\ts_add_u32 m0, s51, 18432\n\ts_mov_b32 s48, 149504\n\tbuffer_load_dwordx4 v8, s[40:43], s48 offen  lds\n\t"
      "s_add_u32 m0, s51, 19456\n\ts_mov_b32 s48, 215040\n\tbuffer_load_dwordx4 v9, s[40:43], s48 offen  lds\n\ts_add_u32 m0, s51, 20480\n\ts_mov_b32 s48, 151552\n\t"
      "buffer_load_dwordx4 v8, s[40:43], s48 offen  lds\n\ts_add_u32 m0, s51, 21504\n\ts_mov_b32 s48, 217088\n\tbuffer_load_dwordx4 v9, s[40:43], s48 offen  lds\n\t"
      "s_add_u32 m0, s51, 22528\n\ts_mov_b32 s48, 153600\n\tbuffer_load_dwordx4 v8, s[40:43], s48 offen  lds\n\ts_add_u32 m0, s51, 23552\n\ts_mov_b32 s48, 219136\n\t"
      "buffer_load_dwordx4 v9, s[40:43], s48 offen  lds\n\ts_cmp_lt_u32 s68, 1\n\ts_cbranch_scc1 7f\n\ts_cmp_eq_u32 s68, 2\n\ts_cbranch_scc1 6f\n\t"
      "5:\n\ts_buffer_load_dword s48, s[12:15], s69 glc\n\ts_waitcnt lgkmcnt(0)\n\ts_cmp_eq_u32 s48, s71\n\ts_cbranch_scc0 7f\n\t"
      "6:\n\tv_mov_b32 v5, 1\n\ts_mov_b64 exec, 1\n\tbuffer_atomic_add v5, off, s[12:15], s70\n\ts_mov_b64 exec, -1\n\ts_mov_b32 s68, 0\n\t"
      "7:\n\ts_waitcnt vmcnt(16)\n\tds_read_b64_tr_b16 a[16:17], v10 offset:0\n\tds_read_b64_tr_b16 a[18:19], v10 offset:2048\n\tds_read_b64_tr_b16 a[48:49], v11 offset:0\n\t"
      "ds_read_b64_tr_b16 a[50:51], v11 offset:2048\n\tds_read_b64_tr_b16 a[80:81], v12 offset:0\n\tds_read_b64_tr_b16 a[82:83], v12 offset:2048\n\t"
      "ds_read_b64_tr_b16 a[112:113], v13 offset:0\n\tds_read_b64_tr_b16 a[114:115], v13 offset:2048\n\tds_read_b64_tr_b16 a[144:145], v10 offset:256\n\t"
      "ds_read_b64_tr_b16 a[146:147], v10 offset:2304\n\tds_read_b64_tr_b16 a[176:177], v11 offset:256\n\tds_read_b64_tr_b16 a[178:179], v11 offset:2304\n\t"
      "ds_read_b64_tr_b16 a[208:209], v12 offset:256\n\tds_read_b64_tr_b16 a[210:211], v12 offset:2304\n\tds_read_b64_tr_b16 a[240:241], v13 offset:256\n\t"
      "ds_read_b64_tr_b16 a[242:243], v13 offset:2304\n\ts_waitcnt lgkmcnt(0)\n\ts_add_u32 m0, s51, 24576\n\ts_mov_b32 s48, 155648\n\t"
      "buffer_load_dwordx4 v8, s[40:43], s48 offen  lds\n\ts_add_u32 m0, s51, 25600\n\ts_mov_b32 s48, 221184\n\tbuffer_load_dwordx4 v9, s[40:43], s48 offen  lds\n\t"
      "s_add_u32 m0, s51, 26624\n\ts_mov_b32 s48, 157696\n\tbuffer_load_dwordx4 v8, s[40:43], s48 offen  lds\n\ts_add_u32 m0, s51, 27648\n\ts_mov_b32 s48, 223232\n\t"
      "buffer_load_dwordx4 v9, s[40:43], s48 offen  lds\n\ts_add_u32 m0, s51, 28672\n\ts_mov_b32 s48, 159744\n\tbuffer_load_dwordx4 v8, s[40:43], s48 offen  lds\n\t"
      "s_add_u32 m0, s51, 29696\n\ts_mov_b32 s48, 225280\n\tbuffer_load_dwordx4 v9, s[40:43], s48 offen  lds\n\ts_add_u32 m0, s51, 30720\n\ts_mov_b32 s48, 161792\n\t"
      "buffer_load_dwordx4 v8, s[40:43], s48 offen  lds\n\ts_add_u32 m0, s51, 31744\n\ts_mov_b32 s48, 227328\n\tbuffer_load_dwordx4 v9, s[40:43], s48 offen  lds\n\t"
      "s_cmp_lt_u32 s68, 1\n\ts_cbranch_scc1 7f\n\ts_cmp_eq_u32 s68, 2\n\ts_cbranch_scc1 6f\n\t"
      "5:\n\ts_buffer_load_dword s48, s[12:15], s69 glc\n\ts_waitcnt lgkmcnt(0)\n\ts_cmp_eq_u32 s48, s71\n\ts_cbranch_scc0 7f\n\t"
      "6:\n\tv_mov_b32 v5, 1\n\ts_mov_b64 exec, 1\n\tbuffer_atomic_add v5, off, s[12:15], s70\n\ts_mov_b64 exec, -1\n\ts_mov_b32 s68, 0\n\t"
      "7:\n\ts_waitcnt vmcnt(16)\n\tds_read_b64_tr_b16 a[20:21], v10 offset:8192\n\tds_read_b64_tr_b16 a[22:23], v10 offset:10240\n\t"
      "ds_read_b64_tr_b16 a[52:53], v11 offset:8192\n\tds_read_b64_tr_b16 a[54:55], v11 offset:10240\n\tds_read_b64_tr_b16 a[84:85], v12 offset:8192\n\t"
      "ds_read_b64_tr_b16 a[86:87], v12 offset:10240\n\tds_read_b64_tr_b16 a[116:117], v13 offset:8192\n\tds_read_b64_tr_b16 a[118:119], v13 offset:10240\n\t"
      "ds_read_b64_tr_b16 a[148:149], v10 offset:8448\n\tds_read_b64_tr_b16 a[150:151], v10 offset:10496\n\tds_read_b64_tr_b16 a[180:181], v11 offset:8448\n\t"
      "ds_read_b64_tr_b16 a[182:183], v11 offset:10496\n\tds_read_b64_tr_b16 a[212:213], v12 offset:8448\n\tds_read_b64_tr_b16 a[214:215], v12 offset:10496\n\t"
      "ds_read_b64_tr_b16 a[244:245], v13 offset:8448\n\tds_read_b64_tr_b16 a[246:247], v13 offset:10496\n\ts_waitcnt lgkmcnt(0)\n\ts_cmp_lt_u32 s68, 1\n\ts_cbranch_scc1 7f\n\t"
      "s_cmp_eq_u32 s68, 2\n\ts_cbranch_scc1 6f\n\t"
      "5:\n\ts_buffer_load_dword s48, s[12:15], s69 glc\n\ts_waitcnt lgkmcnt(0)\n\ts_cmp_eq_u32 s48, s71\n\ts_cbranch_scc0 7f\n\t"
      "6:\n\tv_mov_b32 v5, 1\n\ts_mov_b64 exec, 1\n\tbuffer_atomic_add v5, off, s[12:15], s70\n\ts_mov_b64 exec, -1\n\ts_mov_b32 s68, 0\n\t"
      "7:\n\ts_waitcnt vmcnt(8)\n\tds_read_b64_tr_b16 a[24:25], v10 offset:16384\n\tds_read_b64_tr_b16 a[26:27], v10 offset:18432\n\t"
      "ds_read_b64_tr_b16 a[56:57], v11 offset:16384\n\tds_read_b64_tr_b16 a[58:59], v11 offset:18432\n\tds_read_b64_tr_b16 a[88:89], v12 offset:16384\n\t"
      "ds_read_b64_tr_b16 a[90:91], v12 offset:18432\n\tds_read_b64_tr_b16 a[120:121], v13 offset:16384\n\tds_read_b64_tr_b16 a[122:123], v13 offset:18432\n\t"
      "ds_read_b64_tr_b16 a[152:153], v10 offset:16640\n\tds_read_b64_tr_b16 a[154:155], v10 offset:18688\n\tds_read_b64_tr_b16 a[184:185], v11 offset:16640\n\t"
      "ds_read_b64_tr_b16 a[186:187], v11 offset:18688\n\tds_read_b64_tr_b16 a[216:217], v12 offset:16640\n\tds_read_b64_tr_b16 a[218:219], v12 offset:18688\n\t"
      "ds_read_b64_tr_b16 a[248:249], v13 offset:16640\n\tds_read_b64_tr_b16 a[250:251], v13 offset:18688\n\ts_waitcnt lgkmcnt(0)\n\ts_cmp_lt_u32 s68, 1\n\ts_cbranch_scc1 7f\n\t"
      "s_cmp_eq_u32 s68, 2\n\ts_cbranch_scc1 6f\n\t"
      "5:\n\ts_buffer_load_dword s48, s[12:15], s69 glc\n\ts_waitcnt lgkmcnt(0)\n\ts_cmp_eq_u32 s48, s71\n\ts_cbranch_scc1 6f\n\ts_cmp_eq_u32 s18, 0\n\ts_cbranch_scc1 6f\n\t"
      "s_sub_u32 s18, s18, 1\n\ts_branch 5b\n\t"
      "6:\n\tv_mov_b32 v5, 1\n\ts_mov_b64 exec, 1\n\tbuffer_atomic_add v5, off, s[12:15], s70\n\ts_mov_b64 exec, -1\n\ts_mov_b32 s68, 0\n\t"
      "7:\n\ts_waitcnt vmcnt(0)\n\tds_read_b64_tr_b16 a[28:29], v10 offset:24576\n\tds_read_b64_tr_b16 a[30:31], v10 offset:26624\n\t"
      "ds_read_b64_tr_b16 a[60:61], v11 offset:24576\n\tds_read_b64_tr_b16 a[62:63], v11 offset:26624\n\tds_read_b64_tr_b16 a[92:93], v12 offset:24576\n\t"
      "ds_read_b64_tr_b16 a[94:95], v12 offset:26624\n\tds_read_b64_tr_b16 a[124:125], v13 offset:24576\n\tds_read_b64_tr_b16 a[126:127], v13 offset:26624\n\t"
      "ds_read_b64_tr_b16 a[156:157], v10 offset:24832\n\tds_read_b64_tr_b16 a[158:159], v10 offset:26880\n\tds_read_b64_tr_b16 a[188:189], v11 offset:24832\n\t"
      "ds_read_b64_tr_b16 a[190:191], v11 offset:26880\n\tds_read_b64_tr_b16 a[220:221], v12 offset:24832\n\tds_read_b64_tr_b16 a[222:223], v12 offset:26880\n\t"
      "ds_read_b64_tr_b16 a[252:253], v13 offset:24832\n\tds_read_b64_tr_b16 a[254:255], v13 offset:26880\n\ts_waitcnt lgkmcnt(0)\n\t"
      STS("s_memrealtime vcc\n\ts_waitcnt lgkmcnt(0)\n\tv_mov_b32 v95, vcc_lo\n\tv_mov_b32 v94, 161024\n\tds_write_b32 v94, v95\n\ts_memtime vcc\n\ts_waitcnt lgkmcnt(0)\n\tv_mov_b32 v95, vcc_lo\n\tds_write_b32 v94, v95 offset:400\n\ts_waitcnt lgkmcnt(0)\n\t")
      "s_barrier\n\t"
      "1:\n\tbuffer_load_dword v5, off, s[12:15], s63 sc1\n\ts_waitcnt vmcnt(0)\n\tv_readfirstlane_b32 s49, v5\n\ts_bitcmp1_b32 s49, 0\n\ts_cbranch_scc1 2f\n\t"
      "s_cmp_eq_u32 s18, 0\n\ts_cbranch_scc1 2f\n\ts_sub_u32 s18, s18, 1\n\ts_sleep 2\n\ts_branch 1b\n\t"
      "2:\n\ts_and_b32 s48, s49, 3\n\ts_cmp_lg_u32 s48, 3\n\ts_cbranch_scc1 4f\n\ts_cmp_eq_u32 s62, 0\n\ts_cbranch_scc1 4f\n\ts_waitcnt lgkmcnt(0)\n\ts_barrier\n\t"
      "s_mov_b64 s[0:1], s[60:61]\n\ts_mov_b32 s2, s62\n\ts_lshl_b32 s48, s16, 6\n\tv_mbcnt_lo_u32_b32 v0, -1, 0\n\tv_mbcnt_hi_u32_b32 v0, -1, v0\n\tv_add_u32 v0, s48, v0\n\t"
      "s_getpc_b64 s[4:5]\n\t"
      "3:\n\ts_sub_u32 s4, s4, 3b-mk_mega\n\ts_subb_u32 s5, s5, 0\n\ts_setpc_b64 s[4:5]\n\t"
      "4:\n\ts_lshl_b32 s49, s49, 11\n\ts_and_b32 s49, s49, 0x2000\n\ts_xor_b32 s19, s19, s49\n\tv_xor_b32 v1, s49, v1\n\t"
      TKS("s_mov_b32 s91, 1\n\t")
      "s_cmp_eq_u32 s16, 0\n\ts_cselect_b32 s27, s10, 0\n\tv_mov_b32 v15, 16384\n\tv_add_u32 v7, s51, v15\n\ts_mov_b64 s[56:57], s[8:9]\n\ts_mov_b32 s59, s11\n\t"
      "s_mov_b32 s23, 0\n\ts_mov_b32 s25, 0\n\tv_mov_b32 v2, 0x40000000\n\ts_branch 43f\n\t"
      "10:\n\tv_mfma_f32_32x32x16_bf16 v[32:47], v[96:99], a[0:3], 0\n\ts_nop 4\n\t"
      TK0
      "v_mfma_f32_32x32x16_bf16 v[32:47], v[100:103], a[4:7], v[32:47]\n\ts_add_u32 s48, s19, 0x4000\n\ts_and_b32 s48, s48, 0x3c000\n\ts_andn2_b32 s19, s19, 0x3c000\n\t"
      "s_or_b32 s19, s19, s48\n\ts_add_u32 s24, s19, 0x1000\n\tv_mfma_f32_32x32x16_bf16 v[32:47], v[104:107], a[8:11], v[32:47]\n\tv_cvt_pk_bf16_f32 v80, v48, v49\n\t"
      "v_cvt_pk_bf16_f32 v81, v50, v51\n\tv_cvt_pk_bf16_f32 v82, v56, v57\n\tv_cvt_pk_bf16_f32 v83, v58, v59\n\t"
      "v_mfma_f32_32x32x16_bf16 v[32:47], v[108:111], a[12:15], v[32:47]\n\tbuffer_store_dwordx4 v[80:83], v2, s[8:11], s25 offen offset:0 sc1\n\t"
      "v_mfma_f32_32x32x16_bf16 v[32:47], v[112:115], a[16:19], v[32:47]\n\tv_cvt_pk_bf16_f32 v84, v52, v53\n\tv_cvt_pk_bf16_f32 v85, v54, v55\n\t"
      "v_cvt_pk_bf16_f32 v86, v60, v61\n\tv_cvt_pk_bf16_f32 v87, v62, v63\n\tv_mfma_f32_32x32x16_bf16 v[32:47], v[116:119], a[20:23], v[32:47]\n\t"
      "buffer_store_dwordx4 v[84:87], v2, s[8:11], s25 offen offset:512 sc1\n\tv_mfma_f32_32x32x16_bf16 v[32:47], v[120:123], a[24:27], v[32:47]\n\ts_add_u32 s25, s20, 0x0\n\t"
      "v_mfma_f32_32x32x16_bf16 v[32:47], v[124:127], a[28:31], v[32:47]\n\tv_mov_b32 v4, s17\n\tv_mov_b32 v2, v1\n\t"
      TK1
      "s_waitcnt vmcnt(8)\n\t"
      TK2
      "v_readlane_b32 s23, v5, 0\n\tv_readlane_b32 s48, v5, 1\n\ts_add_u32 s48, s48, 14\n\ts_min_u32 s23, s23, s48\n\t"
      "v_mfma_f32_32x32x16_bf16 v[48:63], v[96:99], a[32:35], 0\n\t"
      "s_add_u32 s48, s17, 1\n\ts_cmp_gt_u32 s23, s48\n\ts_cselect_b32 s58, s27, 0\n\ts_add_u32 m0, s51, 0\n\tv_mov_b32 v16, s58\n\t"
      "v_mfma_f32_32x32x16_bf16 v[48:63], v[100:103], a[36:39], v[48:63]\n\tds_write_b32 v7, v16 offset:0\n\ts_nop 3\n\t"
      "v_mfma_f32_32x32x16_bf16 v[48:63], v[104:107], a[40:43], v[48:63]\n\tv_cvt_pk_bf16_f32 v80, v32, v33\n\tv_cvt_pk_bf16_f32 v81, v34, v35\n\t"
      "v_cvt_pk_bf16_f32 v82, v40, v41\n\tv_cvt_pk_bf16_f32 v83, v42, v43\n\tbuffer_load_dwordx4 v0, s[56:59], s19 offen offset:0 sc1 lds\n\t"
      "v_mfma_f32_32x32x16_bf16 v[48:63], v[108:111], a[44:47], v[48:63]\n\tbuffer_store_dwordx4 v[80:83], v1, s[8:11], s25 offen offset:0 sc1\n\t"
      "v_mfma_f32_32x32x16_bf16 v[48:63], v[112:115], a[48:51], v[48:63]\n\tv_cvt_pk_bf16_f32 v84, v36, v37\n\tv_cvt_pk_bf16_f32 v85, v38, v39\n\t"
      "v_cvt_pk_bf16_f32 v86, v44, v45\n\tv_cvt_pk_bf16_f32 v87, v46, v47\n\tbuffer_load_dwordx4 v0, s[56:59], s19 offen offset:1024 sc1 lds\n\t"
      "v_mfma_f32_32x32x16_bf16 v[48:63], v[116:119], a[52:55], v[48:63]\n\tbuffer_store_dwordx4 v[84:87], v1, s[8:11], s25 offen offset:512 sc1\n\t"
      "v_mfma_f32_32x32x16_bf16 v[48:63], v[120:123], a[56:59], v[48:63]\n\ts_add_u32 s25, s20, 0x20000\n\tbuffer_load_dwordx4 v0, s[56:59], s19 offen offset:2048 sc1 lds\n\t"
      "v_mfma_f32_32x32x16_bf16 v[48:63], v[124:127], a[60:63], v[48:63]\n\tbuffer_load_dwordx4 v0, s[56:59], s19 offen offset:3072 sc1 lds\n\t"
      "v_mfma_f32_32x32x16_bf16 v[32:47], v[96:99], a[64:67], 0\n\ts_add_u32 m0, s51, 4096\n\ts_nop 3\n\tv_mfma_f32_32x32x16_bf16 v[32:47], v[100:103], a[68:71], v[32:47]\n\t"
      "s_nop 4\n\tv_mfma_f32_32x32x16_bf16 v[32:47], v[104:107], a[72:75], v[32:47]\n\tv_cvt_pk_bf16_f32 v80, v48, v49\n\tv_cvt_pk_bf16_f32 v81, v50, v51\n\t"
      "v_cvt_pk_bf16_f32 v82, v56, v57\n\tv_cvt_pk_bf16_f32 v83, v58, v59\n\tbuffer_load_dwordx4 v0, s[56:59], s24 offen offset:0 sc1 lds\n\t"
      "v_mfma_f32_32x32x16_bf16 v[32:47], v[108:111], a[76:79], v[32:47]\n\tbuffer_store_dwordx4 v[80:83], v1, s[8:11], s25 offen offset:0 sc1\n\t"
      "v_mfma_f32_32x32x16_bf16 v[32:47], v[112:115], a[80:83], v[32:47]\n\tv_cvt_pk_bf16_f32 v84, v52, v53\n\tv_cvt_pk_bf16_f32 v85, v54, v55\n\t"
      "v_cvt_pk_bf16_f32 v86, v60, v61\n\tv_cvt_pk_bf16_f32 v87, v62, v63\n\tbuffer_load_dwordx4 v0, s[56:59], s24 offen offset:1024 sc1 lds\n\t"
      "v_mfma_f32_32x32x16_bf16 v[32:47], v[116:119], a[84:87], v[32:47]\n\tbuffer_store_dwordx4 v[84:87], v1, s[8:11], s25 offen offset:512 sc1\n\t"
      "v_mfma_f32_32x32x16_bf16 v[32:47], v[120:123], a[88:91], v[32:47]\n\ts_add_u32 s25, s20, 0x40000\n\tbuffer_load_dwordx4 v0, s[56:59], s24 offen offset:2048 sc1 lds\n\t"
      "v_mfma_f32_32x32x16_bf16 v[32:47], v[124:127], a[92:95], v[32:47]\n\tbuffer_load_dwordx4 v0, s[56:59], s24 offen offset:3072 sc1 lds\n\t"
      "v_mfma_f32_32x32x16_bf16 v[48:63], v[96:99], a[96:99], 0\n\ts_nop 4\n\tv_mfma_f32_32x32x16_bf16 v[48:63], v[100:103], a[100:103], v[48:63]\n\ts_nop 4\n\t"
      "v_mfma_f32_32x32x16_bf16 v[48:63], v[104:107], a[104:107], v[48:63]\n\tv_cvt_pk_bf16_f32 v80, v32, v33\n\tv_cvt_pk_bf16_f32 v81, v34, v35\n\t"
      "v_cvt_pk_bf16_f32 v82, v40, v41\n\tv_cvt_pk_bf16_f32 v83, v42, v43\n\tv_mfma_f32_32x32x16_bf16 v[48:63], v[108:111], a[108:111], v[48:63]\n\t"
      "buffer_store_dwordx4 v[80:83], v1, s[8:11], s25 offen offset:0 sc1\n\tv_mfma_f32_32x32x16_bf16 v[48:63], v[112:115], a[112:115], v[48:63]\n\t"
      "v_cvt_pk_bf16_f32 v84, v36, v37\n\tv_cvt_pk_bf16_f32 v85, v38, v39\n\tv_cvt_pk_bf16_f32 v86, v44, v45\n\tv_cvt_pk_bf16_f32 v87, v46, v47\n\t"
      "v_mfma_f32_32x32x16_bf16 v[48:63], v[116:119], a[116:119], v[48:63]\n\tbuffer_store_dwordx4 v[84:87], v1, s[8:11], s25 offen offset:512 sc1\n\t"
      "v_mfma_f32_32x32x16_bf16 v[48:63], v[120:123], a[120:123], v[48:63]\n\ts_add_u32 s25, s20, 0x60000\n\t"
      "v_mfma_f32_32x32x16_bf16 v[48:63], v[124:127], a[124:127], v[48:63]\n\tv_mfma_f32_32x32x16_bf16 v[32:47], v[96:99], a[128:131], 0\n\ts_nop 4\n\t"
      "v_mfma_f32_32x32x16_bf16 v[32:47], v[100:103], a[132:135], v[32:47]\n\ts_nop 4\n\tv_mfma_f32_32x32x16_bf16 v[32:47], v[104:107], a[136:139], v[32:47]\n\t"
      "v_cvt_pk_bf16_f32 v80, v48, v49\n\tv_cvt_pk_bf16_f32 v81, v50, v51\n\tv_cvt_pk_bf16_f32 v82, v56, v57\n\tv_cvt_pk_bf16_f32 v83, v58, v59\n\t"
      "v_mfma_f32_32x32x16_bf16 v[32:47], v[108:111], a[140:143], v[32:47]\n\tbuffer_store_dwordx4 v[80:83], v1, s[8:11], s25 offen offset:0 sc1\n\t"
      "v_mfma_f32_32x32x16_bf16 v[32:47], v[112:115], a[144:147], v[32:47]\n\tv_cvt_pk_bf16_f32 v84, v52, v53\n\tv_cvt_pk_bf16_f32 v85, v54, v55\n\t"
      "v_cvt_pk_bf16_f32 v86, v60, v61\n\tv_cvt_pk_bf16_f32 v87, v62, v63\n\tv_mfma_f32_32x32x16_bf16 v[32:47], v[116:119], a[148:151], v[32:47]\n\t"
      "buffer_store_dwordx4 v[84:87], v1, s[8:11], s25 offen offset:512 sc1\n\tv_mfma_f32_32x32x16_bf16 v[32:47], v[120:123], a[152:155], v[32:47]\n\t"
      "s_add_u32 s25, s20, 0x80000\n\tv_mfma_f32_32x32x16_bf16 v[32:47], v[124:127], a[156:159], v[32:47]\n\t"
      TK3
      "s_waitcnt vmcnt(16)\n\t"
      TK4
      "buffer_store_dword v4, v3, s[12:15], s22 offen sc1\n\tv_mfma_f32_32x32x16_bf16 v[48:63], v[96:99], a[160:163], 0\n\tbuffer_load_dword v5, v6, s[12:15], 0 offen sc1\n\t"
      "s_nop 3\n\tv_mfma_f32_32x32x16_bf16 v[48:63], v[100:103], a[164:167], v[48:63]\n\ts_nop 4\n\tv_mfma_f32_32x32x16_bf16 v[48:63], v[104:107], a[168:171], v[48:63]\n\t"
      "v_cvt_pk_bf16_f32 v80, v32, v33\n\tv_cvt_pk_bf16_f32 v81, v34, v35\n\tv_cvt_pk_bf16_f32 v82, v40, v41\n\tv_cvt_pk_bf16_f32 v83, v42, v43\n\t"
      "v_mfma_f32_32x32x16_bf16 v[48:63], v[108:111], a[172:175], v[48:63]\n\tbuffer_store_dwordx4 v[80:83], v1, s[8:11], s25 offen offset:0 sc1\n\t"
      "v_mfma_f32_32x32x16_bf16 v[48:63], v[112:115], a[176:179], v[48:63]\n\tv_cvt_pk_bf16_f32 v84, v36, v37\n\tv_cvt_pk_bf16_f32 v85, v38, v39\n\t"
      "v_cvt_pk_bf16_f32 v86, v44, v45\n\tv_cvt_pk_bf16_f32 v87, v46, v47\n\tv_mfma_f32_32x32x16_bf16 v[48:63], v[116:119], a[180:183], v[48:63]\n\t"
      "buffer_store_dwordx4 v[84:87], v1, s[8:11], s25 offen offset:512 sc1\n\tv_mfma_f32_32x32x16_bf16 v[48:63], v[120:123], a[184:187], v[48:63]\n\t"
      "s_add_u32 s25, s20, 0xa0000\n\tv_mfma_f32_32x32x16_bf16 v[48:63], v[124:127], a[188:191], v[48:63]\n\tv_mfma_f32_32x32x16_bf16 v[32:47], v[96:99], a[192:195], 0\n\t"
      "s_nop 4\n\tv_mfma_f32_32x32x16_bf16 v[32:47], v[100:103], a[196:199], v[32:47]\n\ts_nop 4\n\tv_mfma_f32_32x32x16_bf16 v[32:47], v[104:107], a[200:203], v[32:47]\n\t"
      "v_cvt_pk_bf16_f32 v80, v48, v49\n\tv_cvt_pk_bf16_f32 v81, v50, v51\n\tv_cvt_pk_bf16_f32 v82, v56, v57\n\tv_cvt_pk_bf16_f32 v83, v58, v59\n\t"
      "v_mfma_f32_32x32x16_bf16 v[32:47], v[108:111], a[204:207], v[32:47]\n\tbuffer_store_dwordx4 v[80:83], v1, s[8:11], s25 offen offset:0 sc1\n\t"
      "v_mfma_f32_32x32x16_bf16 v[32:47], v[112:115], a[208:211], v[32:47]\n\tv_cvt_pk_bf16_f32 v84, v52, v53\n\tv_cvt_pk_bf16_f32 v85, v54, v55\n\t"
      "v_cvt_pk_bf16_f32 v86, v60, v61\n\tv_cvt_pk_bf16_f32 v87, v62, v63\n\tv_mfma_f32_32x32x16_bf16 v[32:47], v[116:119], a[212:215], v[32:47]\n\t"
      "buffer_store_dwordx4 v[84:87], v1, s[8:11], s25 offen offset:512 sc1\n\tv_mfma_f32_32x32x16_bf16 v[32:47], v[120:123], a[216:219], v[32:47]\n\t"
      "s_add_u32 s25, s20, 0xc0000\n\tv_mfma_f32_32x32x16_bf16 v[32:47], v[124:127], a[220:223], v[32:47]\n\t"
      TK5
      "s_waitcnt vmcnt(10) lgkmcnt(0)\n\t"
      TK6
      "s_barrier\n\tds_read_b32 v16, v15 offset:0\n\tv_mfma_f32_32x32x16_bf16 v[48:63], v[96:99], a[224:227], 0\n\tds_read_b128 v[128:131], v0 offset:0\n\t"
      "ds_read_b128 v[132:135], v0 offset:1024\n\tds_read_b128 v[136:139], v0 offset:2048\n\tds_read_b128 v[140:143], v0 offset:3072\n\ts_nop 0\n\t"
      "v_mfma_f32_32x32x16_bf16 v[48:63], v[100:103], a[228:231], v[48:63]\n\tds_read_b128 v[144:147], v0 offset:4096\n\tds_read_b128 v[148:151], v0 offset:5120\n\t"
      "ds_read_b128 v[152:155], v0 offset:6144\n\tds_read_b128 v[156:159], v0 offset:7168\n\ts_nop 0\n\tv_mfma_f32_32x32x16_bf16 v[48:63], v[104:107], a[232:235], v[48:63]\n\t"
      "v_cvt_pk_bf16_f32 v80, v32, v33\n\tv_cvt_pk_bf16_f32 v81, v34, v35\n\tv_cvt_pk_bf16_f32 v82, v40, v41\n\tv_cvt_pk_bf16_f32 v83, v42, v43\n\t"
      "v_mfma_f32_32x32x16_bf16 v[48:63], v[108:111], a[236:239], v[48:63]\n\tbuffer_store_dwordx4 v[80:83], v1, s[8:11], s25 offen offset:0 sc1\n\t"
      "v_mfma_f32_32x32x16_bf16 v[48:63], v[112:115], a[240:243], v[48:63]\n\tv_cvt_pk_bf16_f32 v84, v36, v37\n\tv_cvt_pk_bf16_f32 v85, v38, v39\n\t"
      "v_cvt_pk_bf16_f32 v86, v44, v45\n\tv_cvt_pk_bf16_f32 v87, v46, v47\n\tv_mfma_f32_32x32x16_bf16 v[48:63], v[116:119], a[244:247], v[48:63]\n\t"
      "buffer_store_dwordx4 v[84:87], v1, s[8:11], s25 offen offset:512 sc1\n\tv_mfma_f32_32x32x16_bf16 v[48:63], v[120:123], a[248:251], v[48:63]\n\t"
      "s_add_u32 s25, s20, 0xe0000\n\ts_waitcnt lgkmcnt(0)\n\tv_readfirstlane_b32 s28, v16\n\tv_mfma_f32_32x32x16_bf16 v[48:63], v[124:127], a[252:255], v[48:63]\n\t"
      "s_add_u32 s17, s17, 1\n\ts_add_u32 s20, s20, 0x400000\n\ts_cmp_eq_u32 s20, 0x5400000\n\ts_cselect_b32 s20, 0x1c00000, s20\n\ts_cmp_eq_u32 s28, 0\n\ts_cbranch_scc1 40f\n\t"
      "11:\n\t"
      STF("s_memrealtime vcc\n\ts_waitcnt lgkmcnt(0)\n\tv_mov_b32 v95, vcc_lo\n\ts_lshl_b32 vcc_hi, s17, 2\n\ts_add_u32 vcc_hi, vcc_hi, 161052\n\tv_mov_b32 v94, vcc_hi\n\tds_write_b32 v94, v95\n\ts_memtime vcc\n\ts_waitcnt lgkmcnt(0)\n\tv_mov_b32 v95, vcc_lo\n\tds_write_b32 v94, v95 offset:400\n\ts_waitcnt lgkmcnt(0)\n\t")
      TK7
      TKS("s_waitcnt lgkmcnt(0)\n\ts_cmp_lt_i32 s17, 0\n\ts_cbranch_scc1 98f\n\ts_cmp_lg_u32 s16, s91\n\ts_cbranch_scc1 98f\n\ts_mov_b64 exec, 1\n\ts_lshl_b32 s48, s17, 4\n\ts_add_u32 s48, s48, 161056\n\tv_mov_b32 v92, s48\n\ts_sub_u32 s48, s74, s90\n\ts_min_u32 s48, s48, 0xffff\n\tv_mov_b32 v93, s48\n\tds_write_b16 v92, v93 offset:0\n\ts_sub_u32 s48, s76, s74\n\ts_min_u32 s48, s48, 0xffff\n\tv_mov_b32 v93, s48\n\tds_write_b16 v92, v93 offset:2\n\ts_sub_u32 s48, s78, s76\n\ts_min_u32 s48, s48, 0xffff\n\tv_mov_b32 v93, s48\n\tds_write_b16 v92, v93 offset:4\n\ts_sub_u32 s48, s80, s78\n\ts_min_u32 s48, s48, 0xffff\n\tv_mov_b32 v93, s48\n\tds_write_b16 v92, v93 offset:6\n\ts_sub_u32 s48, s82, s80\n\ts_min_u32 s48, s48, 0xffff\n\tv_mov_b32 v93, s48\n\tds_write_b16 v92, v93 offset:8\n\ts_sub_u32 s48, s84, s82\n\ts_min_u32 s48, s48, 0xffff\n\tv_mov_b32 v93, s48\n\tds_write_b16 v92, v93 offset:10\n\ts_sub_u32 s48, s86, s84\n\ts_min_u32 s48, s48, 0xffff\n\tv_mov_b32 v93, s48\n\tds_write_b16 v92, v93 offset:12\n\ts_sub_u32 s48, s88, s86\n\ts_min_u32 s48, s48, 0xffff\n\tv_mov_b32 v93, s48\n\tds_write_b16 v92, v93 offset:14\n\ts_mov_b64 exec, -1\n\t98:\n\ts_mov_b32 s90, s88\n\t")
      "v_mfma_f32_32x32x16_bf16 v[32:47], v[128:131], a[0:3], 0\n\ts_nop 4\n\t"
      TK0
      "v_mfma_f32_32x32x16_bf16 v[32:47], v[132:135], a[4:7], v[32:47]\n\ts_add_u32 s48, s19, 0x4000\n\ts_and_b32 s48, s48, 0x3c000\n\ts_andn2_b32 s19, s19, 0x3c000\n\t"
      "s_or_b32 s19, s19, s48\n\ts_add_u32 s24, s19, 0x1000\n\tv_mfma_f32_32x32x16_bf16 v[32:47], v[136:139], a[8:11], v[32:47]\n\tv_cvt_pk_bf16_f32 v80, v48, v49\n\t"
      "v_cvt_pk_bf16_f32 v81, v50, v51\n\tv_cvt_pk_bf16_f32 v82, v56, v57\n\tv_cvt_pk_bf16_f32 v83, v58, v59\n\t"
      "v_mfma_f32_32x32x16_bf16 v[32:47], v[140:143], a[12:15], v[32:47]\n\tbuffer_store_dwordx4 v[80:83], v2, s[8:11], s25 offen offset:0 sc1\n\t"
      "v_mfma_f32_32x32x16_bf16 v[32:47], v[144:147], a[16:19], v[32:47]\n\tv_cvt_pk_bf16_f32 v84, v52, v53\n\tv_cvt_pk_bf16_f32 v85, v54, v55\n\t"
      "v_cvt_pk_bf16_f32 v86, v60, v61\n\tv_cvt_pk_bf16_f32 v87, v62, v63\n\tv_mfma_f32_32x32x16_bf16 v[32:47], v[148:151], a[20:23], v[32:47]\n\t"
      "buffer_store_dwordx4 v[84:87], v2, s[8:11], s25 offen offset:512 sc1\n\tv_mfma_f32_32x32x16_bf16 v[32:47], v[152:155], a[24:27], v[32:47]\n\ts_add_u32 s25, s20, 0x0\n\t"
      "v_mfma_f32_32x32x16_bf16 v[32:47], v[156:159], a[28:31], v[32:47]\n\tv_mov_b32 v4, s17\n\tv_mov_b32 v2, v1\n\t"
      TK1
      "s_waitcnt vmcnt(8)\n\t"
      TK2
      "v_readlane_b32 s23, v5, 0\n\tv_readlane_b32 s48, v5, 1\n\ts_add_u32 s48, s48, 14\n\ts_min_u32 s23, s23, s48\n\t"
      "v_mfma_f32_32x32x16_bf16 v[48:63], v[128:131], a[32:35], 0\n\t"
      "s_add_u32 s48, s17, 1\n\ts_cmp_gt_u32 s23, s48\n\ts_cselect_b32 s58, s27, 0\n\ts_add_u32 m0, s51, 8192\n\tv_mov_b32 v16, s58\n\t"
      "v_mfma_f32_32x32x16_bf16 v[48:63], v[132:135], a[36:39], v[48:63]\n\tds_write_b32 v7, v16 offset:4\n\ts_nop 3\n\t"
      "v_mfma_f32_32x32x16_bf16 v[48:63], v[136:139], a[40:43], v[48:63]\n\tv_cvt_pk_bf16_f32 v80, v32, v33\n\tv_cvt_pk_bf16_f32 v81, v34, v35\n\t"
      "v_cvt_pk_bf16_f32 v82, v40, v41\n\tv_cvt_pk_bf16_f32 v83, v42, v43\n\tbuffer_load_dwordx4 v0, s[56:59], s19 offen offset:0 sc1 lds\n\t"
      "v_mfma_f32_32x32x16_bf16 v[48:63], v[140:143], a[44:47], v[48:63]\n\tbuffer_store_dwordx4 v[80:83], v1, s[8:11], s25 offen offset:0 sc1\n\t"
      "v_mfma_f32_32x32x16_bf16 v[48:63], v[144:147], a[48:51], v[48:63]\n\tv_cvt_pk_bf16_f32 v84, v36, v37\n\tv_cvt_pk_bf16_f32 v85, v38, v39\n\t"
      "v_cvt_pk_bf16_f32 v86, v44, v45\n\tv_cvt_pk_bf16_f32 v87, v46, v47\n\tbuffer_load_dwordx4 v0, s[56:59], s19 offen offset:1024 sc1 lds\n\t"
      "v_mfma_f32_32x32x16_bf16 v[48:63], v[148:151], a[52:55], v[48:63]\n\tbuffer_store_dwordx4 v[84:87], v1, s[8:11], s25 offen offset:512 sc1\n\t"
      "v_mfma_f32_32x32x16_bf16 v[48:63], v[152:155], a[56:59], v[48:63]\n\ts_add_u32 s25, s20, 0x20000\n\tbuffer_load_dwordx4 v0, s[56:59], s19 offen offset:2048 sc1 lds\n\t"
      "v_mfma_f32_32x32x16_bf16 v[48:63], v[156:159], a[60:63], v[48:63]\n\tbuffer_load_dwordx4 v0, s[56:59], s19 offen offset:3072 sc1 lds\n\t"
      "v_mfma_f32_32x32x16_bf16 v[32:47], v[128:131], a[64:67], 0\n\ts_add_u32 m0, s51, 12288\n\ts_nop 3\n\tv_mfma_f32_32x32x16_bf16 v[32:47], v[132:135], a[68:71], v[32:47]\n\t"
      "s_nop 4\n\tv_mfma_f32_32x32x16_bf16 v[32:47], v[136:139], a[72:75], v[32:47]\n\tv_cvt_pk_bf16_f32 v80, v48, v49\n\tv_cvt_pk_bf16_f32 v81, v50, v51\n\t"
      "v_cvt_pk_bf16_f32 v82, v56, v57\n\tv_cvt_pk_bf16_f32 v83, v58, v59\n\tbuffer_load_dwordx4 v0, s[56:59], s24 offen offset:0 sc1 lds\n\t"
      "v_mfma_f32_32x32x16_bf16 v[32:47], v[140:143], a[76:79], v[32:47]\n\tbuffer_store_dwordx4 v[80:83], v1, s[8:11], s25 offen offset:0 sc1\n\t"
      "v_mfma_f32_32x32x16_bf16 v[32:47], v[144:147], a[80:83], v[32:47]\n\tv_cvt_pk_bf16_f32 v84, v52, v53\n\tv_cvt_pk_bf16_f32 v85, v54, v55\n\t"
      "v_cvt_pk_bf16_f32 v86, v60, v61\n\tv_cvt_pk_bf16_f32 v87, v62, v63\n\tbuffer_load_dwordx4 v0, s[56:59], s24 offen offset:1024 sc1 lds\n\t"
      "v_mfma_f32_32x32x16_bf16 v[32:47], v[148:151], a[84:87], v[32:47]\n\tbuffer_store_dwordx4 v[84:87], v1, s[8:11], s25 offen offset:512 sc1\n\t"
      "v_mfma_f32_32x32x16_bf16 v[32:47], v[152:155], a[88:91], v[32:47]\n\ts_add_u32 s25, s20, 0x40000\n\tbuffer_load_dwordx4 v0, s[56:59], s24 offen offset:2048 sc1 lds\n\t"
      "v_mfma_f32_32x32x16_bf16 v[32:47], v[156:159], a[92:95], v[32:47]\n\tbuffer_load_dwordx4 v0, s[56:59], s24 offen offset:3072 sc1 lds\n\t"
      "v_mfma_f32_32x32x16_bf16 v[48:63], v[128:131], a[96:99], 0\n\ts_nop 4\n\tv_mfma_f32_32x32x16_bf16 v[48:63], v[132:135], a[100:103], v[48:63]\n\ts_nop 4\n\t"
      "v_mfma_f32_32x32x16_bf16 v[48:63], v[136:139], a[104:107], v[48:63]\n\tv_cvt_pk_bf16_f32 v80, v32, v33\n\tv_cvt_pk_bf16_f32 v81, v34, v35\n\t"
      "v_cvt_pk_bf16_f32 v82, v40, v41\n\tv_cvt_pk_bf16_f32 v83, v42, v43\n\tv_mfma_f32_32x32x16_bf16 v[48:63], v[140:143], a[108:111], v[48:63]\n\t"
      "buffer_store_dwordx4 v[80:83], v1, s[8:11], s25 offen offset:0 sc1\n\tv_mfma_f32_32x32x16_bf16 v[48:63], v[144:147], a[112:115], v[48:63]\n\t"
      "v_cvt_pk_bf16_f32 v84, v36, v37\n\tv_cvt_pk_bf16_f32 v85, v38, v39\n\tv_cvt_pk_bf16_f32 v86, v44, v45\n\tv_cvt_pk_bf16_f32 v87, v46, v47\n\t"
      "v_mfma_f32_32x32x16_bf16 v[48:63], v[148:151], a[116:119], v[48:63]\n\tbuffer_store_dwordx4 v[84:87], v1, s[8:11], s25 offen offset:512 sc1\n\t"
      "v_mfma_f32_32x32x16_bf16 v[48:63], v[152:155], a[120:123], v[48:63]\n\ts_add_u32 s25, s20, 0x60000\n\t"
      "v_mfma_f32_32x32x16_bf16 v[48:63], v[156:159], a[124:127], v[48:63]\n\tv_mfma_f32_32x32x16_bf16 v[32:47], v[128:131], a[128:131], 0\n\ts_nop 4\n\t"
      "v_mfma_f32_32x32x16_bf16 v[32:47], v[132:135], a[132:135], v[32:47]\n\ts_nop 4\n\tv_mfma_f32_32x32x16_bf16 v[32:47], v[136:139], a[136:139], v[32:47]\n\t"
      "v_cvt_pk_bf16_f32 v80, v48, v49\n\tv_cvt_pk_bf16_f32 v81, v50, v51\n\tv_cvt_pk_bf16_f32 v82, v56, v57\n\tv_cvt_pk_bf16_f32 v83, v58, v59\n\t"
      "v_mfma_f32_32x32x16_bf16 v[32:47], v[140:143], a[140:143], v[32:47]\n\tbuffer_store_dwordx4 v[80:83], v1, s[8:11], s25 offen offset:0 sc1\n\t"
      "v_mfma_f32_32x32x16_bf16 v[32:47], v[144:147], a[144:147], v[32:47]\n\tv_cvt_pk_bf16_f32 v84, v52, v53\n\tv_cvt_pk_bf16_f32 v85, v54, v55\n\t"
      "v_cvt_pk_bf16_f32 v86, v60, v61\n\tv_cvt_pk_bf16_f32 v87, v62, v63\n\tv_mfma_f32_32x32x16_bf16 v[32:47], v[148:151], a[148:151], v[32:47]\n\t"
      "buffer_store_dwordx4 v[84:87], v1, s[8:11], s25 offen offset:512 sc1\n\tv_mfma_f32_32x32x16_bf16 v[32:47], v[152:155], a[152:155], v[32:47]\n\t"
      "s_add_u32 s25, s20, 0x80000\n\tv_mfma_f32_32x32x16_bf16 v[32:47], v[156:159], a[156:159], v[32:47]\n\t"
      TK3
      "s_waitcnt vmcnt(16)\n\t"
      TK4
      "buffer_store_dword v4, v3, s[12:15], s22 offen sc1\n\tv_mfma_f32_32x32x16_bf16 v[48:63], v[128:131], a[160:163], 0\n\tbuffer_load_dword v5, v6, s[12:15], 0 offen sc1\n\t"
      "s_nop 3\n\tv_mfma_f32_32x32x16_bf16 v[48:63], v[132:135], a[164:167], v[48:63]\n\ts_nop 4\n\tv_mfma_f32_32x32x16_bf16 v[48:63], v[136:139], a[168:171], v[48:63]\n\t"
      "v_cvt_pk_bf16_f32 v80, v32, v33\n\tv_cvt_pk_bf16_f32 v81, v34, v35\n\tv_cvt_pk_bf16_f32 v82, v40, v41\n\tv_cvt_pk_bf16_f32 v83, v42, v43\n\t"
      "v_mfma_f32_32x32x16_bf16 v[48:63], v[140:143], a[172:175], v[48:63]\n\tbuffer_store_dwordx4 v[80:83], v1, s[8:11], s25 offen offset:0 sc1\n\t"
      "v_mfma_f32_32x32x16_bf16 v[48:63], v[144:147], a[176:179], v[48:63]\n\tv_cvt_pk_bf16_f32 v84, v36, v37\n\tv_cvt_pk_bf16_f32 v85, v38, v39\n\t"
      "v_cvt_pk_bf16_f32 v86, v44, v45\n\tv_cvt_pk_bf16_f32 v87, v46, v47\n\tv_mfma_f32_32x32x16_bf16 v[48:63], v[148:151], a[180:183], v[48:63]\n\t"
      "buffer_store_dwordx4 v[84:87], v1, s[8:11], s25 offen offset:512 sc1\n\tv_mfma_f32_32x32x16_bf16 v[48:63], v[152:155], a[184:187], v[48:63]\n\t"
      "s_add_u32 s25, s20, 0xa0000\n\tv_mfma_f32_32x32x16_bf16 v[48:63], v[156:159], a[188:191], v[48:63]\n\tv_mfma_f32_32x32x16_bf16 v[32:47], v[128:131], a[192:195], 0\n\t"
      "s_nop 4\n\tv_mfma_f32_32x32x16_bf16 v[32:47], v[132:135], a[196:199], v[32:47]\n\ts_nop 4\n\tv_mfma_f32_32x32x16_bf16 v[32:47], v[136:139], a[200:203], v[32:47]\n\t"
      "v_cvt_pk_bf16_f32 v80, v48, v49\n\tv_cvt_pk_bf16_f32 v81, v50, v51\n\tv_cvt_pk_bf16_f32 v82, v56, v57\n\tv_cvt_pk_bf16_f32 v83, v58, v59\n\t"
      "v_mfma_f32_32x32x16_bf16 v[32:47], v[140:143], a[204:207], v[32:47]\n\tbuffer_store_dwordx4 v[80:83], v1, s[8:11], s25 offen offset:0 sc1\n\t"
      "v_mfma_f32_32x32x16_bf16 v[32:47], v[144:147], a[208:211], v[32:47]\n\tv_cvt_pk_bf16_f32 v84, v52, v53\n\tv_cvt_pk_bf16_f32 v85, v54, v55\n\t"
      "v_cvt_pk_bf16_f32 v86, v60, v61\n\tv_cvt_pk_bf16_f32 v87, v62, v63\n\tv_mfma_f32_32x32x16_bf16 v[32:47], v[148:151], a[212:215], v[32:47]\n\t"
      "buffer_store_dwordx4 v[84:87], v1, s[8:11], s25 offen offset:512 sc1\n\tv_mfma_f32_32x32x16_bf16 v[32:47], v[152:155], a[216:219], v[32:47]\n\t"
      "s_add_u32 s25, s20, 0xc0000\n\tv_mfma_f32_32x32x16_bf16 v[32:47], v[156:159], a[220:223], v[32:47]\n\t"
      TK5
      "s_waitcnt vmcnt(10) lgkmcnt(0)\n\t"
      TK6
      "s_barrier\n\tds_read_b32 v16, v15 offset:4\n\tv_mfma_f32_32x32x16_bf16 v[48:63], v[128:131], a[224:227], 0\n\tds_read_b128 v[96:99], v0 offset:8192\n\t"
      "ds_read_b128 v[100:103], v0 offset:9216\n\tds_read_b128 v[104:107], v0 offset:10240\n\tds_read_b128 v[108:111], v0 offset:11264\n\ts_nop 0\n\t"
      "v_mfma_f32_32x32x16_bf16 v[48:63], v[132:135], a[228:231], v[48:63]\n\tds_read_b128 v[112:115], v0 offset:12288\n\tds_read_b128 v[116:119], v0 offset:13312\n\t"
      "ds_read_b128 v[120:123], v0 offset:14336\n\tds_read_b128 v[124:127], v0 offset:15360\n\ts_nop 0\n\tv_mfma_f32_32x32x16_bf16 v[48:63], v[136:139], a[232:235], v[48:63]\n\t"
      "v_cvt_pk_bf16_f32 v80, v32, v33\n\tv_cvt_pk_bf16_f32 v81, v34, v35\n\tv_cvt_pk_bf16_f32 v82, v40, v41\n\tv_cvt_pk_bf16_f32 v83, v42, v43\n\t"
      "v_mfma_f32_32x32x16_bf16 v[48:63], v[140:143], a[236:239], v[48:63]\n\tbuffer_store_dwordx4 v[80:83], v1, s[8:11], s25 offen offset:0 sc1\n\t"
      "v_mfma_f32_32x32x16_bf16 v[48:63], v[144:147], a[240:243], v[48:63]\n\tv_cvt_pk_bf16_f32 v84, v36, v37\n\tv_cvt_pk_bf16_f32 v85, v38, v39\n\t"
      "v_cvt_pk_bf16_f32 v86, v44, v45\n\tv_cvt_pk_bf16_f32 v87, v46, v47\n\tv_mfma_f32_32x32x16_bf16 v[48:63], v[148:151], a[244:247], v[48:63]\n\t"
      "buffer_store_dwordx4 v[84:87], v1, s[8:11], s25 offen offset:512 sc1\n\tv_mfma_f32_32x32x16_bf16 v[48:63], v[152:155], a[248:251], v[48:63]\n\t"
      "s_add_u32 s25, s20, 0xe0000\n\ts_waitcnt lgkmcnt(0)\n\tv_readfirstlane_b32 s28, v16\n\tv_mfma_f32_32x32x16_bf16 v[48:63], v[156:159], a[252:255], v[48:63]\n\t"
      "s_add_u32 s17, s17, 1\n\ts_add_u32 s20, s20, 0x400000\n\ts_cmp_eq_u32 s20, 0x5400000\n\ts_cselect_b32 s20, 0x1c00000, s20\n\ts_cmp_eq_u32 s28, 0\n\ts_cbranch_scc1 42f\n\t"
      "12:\n\t"
      STF("s_memrealtime vcc\n\ts_waitcnt lgkmcnt(0)\n\tv_mov_b32 v95, vcc_lo\n\ts_lshl_b32 vcc_hi, s17, 2\n\ts_add_u32 vcc_hi, vcc_hi, 161052\n\tv_mov_b32 v94, vcc_hi\n\tds_write_b32 v94, v95\n\ts_memtime vcc\n\ts_waitcnt lgkmcnt(0)\n\tv_mov_b32 v95, vcc_lo\n\tds_write_b32 v94, v95 offset:400\n\ts_waitcnt lgkmcnt(0)\n\t")
      TK7
      TKS("s_waitcnt lgkmcnt(0)\n\ts_cmp_lt_i32 s17, 0\n\ts_cbranch_scc1 98f\n\ts_cmp_lg_u32 s16, s91\n\ts_cbranch_scc1 98f\n\ts_mov_b64 exec, 1\n\ts_lshl_b32 s48, s17, 4\n\ts_add_u32 s48, s48, 161056\n\tv_mov_b32 v92, s48\n\ts_sub_u32 s48, s74, s90\n\ts_min_u32 s48, s48, 0xffff\n\tv_mov_b32 v93, s48\n\tds_write_b16 v92, v93 offset:0\n\ts_sub_u32 s48, s76, s74\n\ts_min_u32 s48, s48, 0xffff\n\tv_mov_b32 v93, s48\n\tds_write_b16 v92, v93 offset:2\n\ts_sub_u32 s48, s78, s76\n\ts_min_u32 s48, s48, 0xffff\n\tv_mov_b32 v93, s48\n\tds_write_b16 v92, v93 offset:4\n\ts_sub_u32 s48, s80, s78\n\ts_min_u32 s48, s48, 0xffff\n\tv_mov_b32 v93, s48\n\tds_write_b16 v92, v93 offset:6\n\ts_sub_u32 s48, s82, s80\n\ts_min_u32 s48, s48, 0xffff\n\tv_mov_b32 v93, s48\n\tds_write_b16 v92, v93 offset:8\n\ts_sub_u32 s48, s84, s82\n\ts_min_u32 s48, s48, 0xffff\n\tv_mov_b32 v93, s48\n\tds_write_b16 v92, v93 offset:10\n\ts_sub_u32 s48, s86, s84\n\ts_min_u32 s48, s48, 0xffff\n\tv_mov_b32 v93, s48\n\tds_write_b16 v92, v93 offset:12\n\ts_sub_u32 s48, s88, s86\n\ts_min_u32 s48, s48, 0xffff\n\tv_mov_b32 v93, s48\n\tds_write_b16 v92, v93 offset:14\n\ts_mov_b64 exec, -1\n\t98:\n\ts_mov_b32 s90, s88\n\t")
      "s_branch 10b\n\t"
      "40:\n\ts_waitcnt vmcnt(6)\n\tv_readlane_b32 s23, v5, 0\n\tv_readlane_b32 s48, v5, 1\n\ts_add_u32 s48, s48, 14\n\ts_min_u32 s23, s23, s48\n\ts_cmp_gt_u32 s23, s17\n\t"
      "s_cbranch_scc0 47f\n\tbuffer_load_dwordx4 v[128:131], v0, s[8:11], s19 offen offset:0 sc1\n\tbuffer_load_dwordx4 v[132:135], v0, s[8:11], s19 offen offset:1024 sc1\n\t"
      "buffer_load_dwordx4 v[136:139], v0, s[8:11], s19 offen offset:2048 sc1\n\tbuffer_load_dwordx4 v[140:143], v0, s[8:11], s19 offen offset:3072 sc1\n\t"
      "buffer_load_dwordx4 v[144:147], v0, s[8:11], s24 offen offset:0 sc1\n\tbuffer_load_dwordx4 v[148:151], v0, s[8:11], s24 offen offset:1024 sc1\n\t"
      "buffer_load_dwordx4 v[152:155], v0, s[8:11], s24 offen offset:2048 sc1\n\tbuffer_load_dwordx4 v[156:159], v0, s[8:11], s24 offen offset:3072 sc1\n\t"
      "v_cvt_pk_bf16_f32 v80, v48, v49\n\tv_cvt_pk_bf16_f32 v81, v50, v51\n\tv_cvt_pk_bf16_f32 v82, v56, v57\n\tv_cvt_pk_bf16_f32 v83, v58, v59\n\t"
      "v_cvt_pk_bf16_f32 v84, v52, v53\n\tv_cvt_pk_bf16_f32 v85, v54, v55\n\tv_cvt_pk_bf16_f32 v86, v60, v61\n\tv_cvt_pk_bf16_f32 v87, v62, v63\n\t"
      "buffer_store_dwordx4 v[80:83], v1, s[8:11], s25 offen offset:0 sc1\n\tbuffer_store_dwordx4 v[84:87], v1, s[8:11], s25 offen offset:512 sc1\n\tv_mov_b32 v4, s17\n\t"
      "buffer_load_dword v5, v6, s[12:15], 0 offen sc1\n\ts_waitcnt vmcnt(0)\n\tbuffer_store_dword v4, v3, s[12:15], s22 offen sc1\n\ts_branch 11b\n\t"
      "47:\n\ts_nop 5\n\tv_cvt_pk_bf16_f32 v80, v48, v49\n\tv_cvt_pk_bf16_f32 v81, v50, v51\n\tv_cvt_pk_bf16_f32 v82, v56, v57\n\tv_cvt_pk_bf16_f32 v83, v58, v59\n\t"
      "v_cvt_pk_bf16_f32 v84, v52, v53\n\tv_cvt_pk_bf16_f32 v85, v54, v55\n\tv_cvt_pk_bf16_f32 v86, v60, v61\n\tv_cvt_pk_bf16_f32 v87, v62, v63\n\t"
      "buffer_store_dwordx4 v[80:83], v1, s[8:11], s25 offen offset:0 sc1\n\tbuffer_store_dwordx4 v[84:87], v1, s[8:11], s25 offen offset:512 sc1\n\tv_mov_b32 v4, s17\n\t"
      "buffer_load_dword v5, v6, s[12:15], 0 offen sc1\n\ts_waitcnt vmcnt(0)\n\tbuffer_store_dword v4, v3, s[12:15], s22 offen sc1\n\tv_readlane_b32 s23, v5, 0\n\t"
      "v_readlane_b32 s48, v5, 1\n\ts_add_u32 s48, s48, 14\n\ts_min_u32 s23, s23, s48\n\ts_cmp_eq_u32 s17, 64\n\ts_cbranch_scc1 50f\n\t"
      "41:\n\t"
      "45:\n\ts_cmp_gt_u32 s23, s17\n\ts_cbranch_scc1 46f\n\tbuffer_load_dword v5, v6, s[12:15], 0 offen sc1\n\ts_waitcnt vmcnt(0)\n\tv_readlane_b32 s23, v5, 0\n\t"
      "v_readlane_b32 s48, v5, 1\n\ts_add_u32 s48, s48, 14\n\ts_min_u32 s23, s23, s48\n\ts_cmp_eq_u32 s18, 0\n\ts_cbranch_scc1 46f\n\ts_sub_u32 s18, s18, 1\n\ts_sleep 2\n\t"
      "s_branch 45b\n\t"
      "46:\n\ts_add_u32 s24, s19, 0x1000\n\tbuffer_load_dwordx4 v[128:131], v0, s[8:11], s19 offen offset:0 sc1\n\t"
      "buffer_load_dwordx4 v[132:135], v0, s[8:11], s19 offen offset:1024 sc1\n\tbuffer_load_dwordx4 v[136:139], v0, s[8:11], s19 offen offset:2048 sc1\n\t"
      "buffer_load_dwordx4 v[140:143], v0, s[8:11], s19 offen offset:3072 sc1\n\tbuffer_load_dwordx4 v[144:147], v0, s[8:11], s24 offen offset:0 sc1\n\t"
      "buffer_load_dwordx4 v[148:151], v0, s[8:11], s24 offen offset:1024 sc1\n\tbuffer_load_dwordx4 v[152:155], v0, s[8:11], s24 offen offset:2048 sc1\n\t"
      "buffer_load_dwordx4 v[156:159], v0, s[8:11], s24 offen offset:3072 sc1\n\tbuffer_load_dword v5, v6, s[12:15], 0 offen sc1\n\ts_waitcnt vmcnt(0)\n\ts_branch 11b\n\t"
      "42:\n\ts_waitcnt vmcnt(6)\n\tv_readlane_b32 s23, v5, 0\n\tv_readlane_b32 s48, v5, 1\n\ts_add_u32 s48, s48, 14\n\ts_min_u32 s23, s23, s48\n\ts_cmp_gt_u32 s23, s17\n\t"
      "s_cbranch_scc0 47f\n\tbuffer_load_dwordx4 v[96:99], v0, s[8:11], s19 offen offset:0 sc1\n\tbuffer_load_dwordx4 v[100:103], v0, s[8:11], s19 offen offset:1024 sc1\n\t"
      "buffer_load_dwordx4 v[104:107], v0, s[8:11], s19 offen offset:2048 sc1\n\tbuffer_load_dwordx4 v[108:111], v0, s[8:11], s19 offen offset:3072 sc1\n\t"
      "buffer_load_dwordx4 v[112:115], v0, s[8:11], s24 offen offset:0 sc1\n\tbuffer_load_dwordx4 v[116:119], v0, s[8:11], s24 offen offset:1024 sc1\n\t"
      "buffer_load_dwordx4 v[120:123], v0, s[8:11], s24 offen offset:2048 sc1\n\tbuffer_load_dwordx4 v[124:127], v0, s[8:11], s24 offen offset:3072 sc1\n\t"
      "v_cvt_pk_bf16_f32 v80, v48, v49\n\tv_cvt_pk_bf16_f32 v81, v50, v51\n\tv_cvt_pk_bf16_f32 v82, v56, v57\n\tv_cvt_pk_bf16_f32 v83, v58, v59\n\t"
      "v_cvt_pk_bf16_f32 v84, v52, v53\n\tv_cvt_pk_bf16_f32 v85, v54, v55\n\tv_cvt_pk_bf16_f32 v86, v60, v61\n\tv_cvt_pk_bf16_f32 v87, v62, v63\n\t"
      "buffer_store_dwordx4 v[80:83], v1, s[8:11], s25 offen offset:0 sc1\n\tbuffer_store_dwordx4 v[84:87], v1, s[8:11], s25 offen offset:512 sc1\n\tv_mov_b32 v4, s17\n\t"
      "buffer_load_dword v5, v6, s[12:15], 0 offen sc1\n\ts_waitcnt vmcnt(0)\n\tbuffer_store_dword v4, v3, s[12:15], s22 offen sc1\n\ts_branch 12b\n\t"
      "47:\n\ts_nop 5\n\tv_cvt_pk_bf16_f32 v80, v48, v49\n\tv_cvt_pk_bf16_f32 v81, v50, v51\n\tv_cvt_pk_bf16_f32 v82, v56, v57\n\tv_cvt_pk_bf16_f32 v83, v58, v59\n\t"
      "v_cvt_pk_bf16_f32 v84, v52, v53\n\tv_cvt_pk_bf16_f32 v85, v54, v55\n\tv_cvt_pk_bf16_f32 v86, v60, v61\n\tv_cvt_pk_bf16_f32 v87, v62, v63\n\t"
      "buffer_store_dwordx4 v[80:83], v1, s[8:11], s25 offen offset:0 sc1\n\tbuffer_store_dwordx4 v[84:87], v1, s[8:11], s25 offen offset:512 sc1\n\tv_mov_b32 v4, s17\n\t"
      "buffer_load_dword v5, v6, s[12:15], 0 offen sc1\n\ts_waitcnt vmcnt(0)\n\tbuffer_store_dword v4, v3, s[12:15], s22 offen sc1\n\tv_readlane_b32 s23, v5, 0\n\t"
      "v_readlane_b32 s48, v5, 1\n\ts_add_u32 s48, s48, 14\n\ts_min_u32 s23, s23, s48\n\ts_cmp_eq_u32 s17, 64\n\ts_cbranch_scc1 50f\n\t"
      "43:\n\t"
      "45:\n\ts_cmp_gt_u32 s23, s17\n\ts_cbranch_scc1 46f\n\tbuffer_load_dword v5, v6, s[12:15], 0 offen sc1\n\ts_waitcnt vmcnt(0)\n\tv_readlane_b32 s23, v5, 0\n\t"
      "v_readlane_b32 s48, v5, 1\n\ts_add_u32 s48, s48, 14\n\ts_min_u32 s23, s23, s48\n\ts_cmp_eq_u32 s18, 0\n\ts_cbranch_scc1 46f\n\ts_sub_u32 s18, s18, 1\n\ts_sleep 2\n\t"
      "s_branch 45b\n\t"
      "46:\n\ts_add_u32 s24, s19, 0x1000\n\tbuffer_load_dwordx4 v[96:99], v0, s[8:11], s19 offen offset:0 sc1\n\t"
      "buffer_load_dwordx4 v[100:103], v0, s[8:11], s19 offen offset:1024 sc1\n\tbuffer_load_dwordx4 v[104:107], v0, s[8:11], s19 offen offset:2048 sc1\n\t"
      "buffer_load_dwordx4 v[108:111], v0, s[8:11], s19 offen offset:3072 sc1\n\tbuffer_load_dwordx4 v[112:115], v0, s[8:11], s24 offen offset:0 sc1\n\t"
      "buffer_load_dwordx4 v[116:119], v0, s[8:11], s24 offen offset:1024 sc1\n\tbuffer_load_dwordx4 v[120:123], v0, s[8:11], s24 offen offset:2048 sc1\n\t"
      "buffer_load_dwordx4 v[124:127], v0, s[8:11], s24 offen offset:3072 sc1\n\tbuffer_load_dword v5, v6, s[12:15], 0 offen sc1\n\ts_waitcnt vmcnt(0)\n\ts_branch 12b\n\t"
      "50:\n\t"
      STF("s_memrealtime vcc\n\ts_waitcnt lgkmcnt(0)\n\tv_mov_b32 v95, vcc_lo\n\ts_lshl_b32 vcc_hi, s17, 2\n\ts_add_u32 vcc_hi, vcc_hi, 161052\n\tv_mov_b32 v94, vcc_hi\n\tds_write_b32 v94, v95\n\ts_memtime vcc\n\ts_waitcnt lgkmcnt(0)\n\tv_mov_b32 v95, vcc_lo\n\tds_write_b32 v94, v95 offset:400\n\ts_waitcnt lgkmcnt(0)\n\t")
      ".Lmk_end3:\n\ts_waitcnt vmcnt(0) lgkmcnt(0)\n\t"
      TKS("s_mul_i32 s48, s16, 72\n\ts_add_u32 s48, s48, 162080\n\tv_mov_b32 v224, s48\n\tds_read_b32 v225, v224 offset:0\n\ts_waitcnt lgkmcnt(0)\n\tv_readfirstlane_b32 s74, v225\n\tds_read_b32 v225, v224 offset:4\n\ts_waitcnt lgkmcnt(0)\n\tv_readfirstlane_b32 s75, v225\n\tds_read_b32 v225, v224 offset:8\n\ts_waitcnt lgkmcnt(0)\n\tv_readfirstlane_b32 s76, v225\n\tds_read_b32 v225, v224 offset:12\n\ts_waitcnt lgkmcnt(0)\n\tv_readfirstlane_b32 s77, v225\n\tds_read_b32 v225, v224 offset:16\n\ts_waitcnt lgkmcnt(0)\n\tv_readfirstlane_b32 s78, v225\n\tds_read_b32 v225, v224 offset:20\n\ts_waitcnt lgkmcnt(0)\n\tv_readfirstlane_b32 s79, v225\n\tds_read_b32 v225, v224 offset:24\n\ts_waitcnt lgkmcnt(0)\n\tv_readfirstlane_b32 s80, v225\n\tds_read_b32 v225, v224 offset:28\n\ts_waitcnt lgkmcnt(0)\n\tv_readfirstlane_b32 s81, v225\n\tds_read_b32 v225, v224 offset:32\n\ts_waitcnt lgkmcnt(0)\n\tv_readfirstlane_b32 s82, v225\n\tds_read_b32 v225, v224 offset:36\n\ts_waitcnt lgkmcnt(0)\n\tv_readfirstlane_b32 s83, v225\n\tds_read_b32 v225, v224 offset:40\n\ts_waitcnt lgkmcnt(0)\n\tv_readfirstlane_b32 s84, v225\n\tds_read_b32 v225, v224 offset:44\n\ts_waitcnt lgkmcnt(0)\n\tv_readfirstlane_b32 s85, v225\n\tds_read_b32 v225, v224 offset:48\n\ts_waitcnt lgkmcnt(0)\n\tv_readfirstlane_b32 s86, v225\n\tds_read_b32 v225, v224 offset:52\n\ts_waitcnt lgkmcnt(0)\n\tv_readfirstlane_b32 s87, v225\n\tds_read_b32 v225, v224 offset:56\n\ts_waitcnt lgkmcnt(0)\n\tv_readfirstlane_b32 s88, v225\n\tds_read_b32 v225, v224 offset:60\n\ts_waitcnt lgkmcnt(0)\n\tv_readfirstlane_b32 s89, v225\n\tds_read_b32 v225, v224 offset:64\n\ts_waitcnt lgkmcnt(0)\n\tv_readfirstlane_b32 s90, v225\n\tds_read_b32 v225, v224 offset:68\n\ts_waitcnt lgkmcnt(0)\n\tv_readfirstlane_b32 s91, v225\n\t")
      STS("s_lshl_b32 s48, s16, 2\n\ts_add_u32 s48, s48, 162944\n\tv_mov_b32 v0, s48\n\tv_mov_b32 v1, s50\n\tds_write_b32 v0, v1\n\ts_cmp_lg_u32 s16, 0\n\ts_cbranch_scc1 97f\n\tv_mov_b32 v1, s51\n\tds_write_b32 v0, v1 offset:16\n\t97:\n\ts_cmp_lg_u32 s16, 0\n\ts_cbranch_scc1 95f\n\tv_mov_b32 v1, s61\n\tds_write_b32 v0, v1 offset:20\n\tv_mov_b32 v1, s21\n\tds_write_b32 v0, v1 offset:24\n\tv_mov_b32 v1, s22\n\tds_write_b32 v0, v1 offset:28\n\t95:\n\t")
      "s_cmp_lg_u32 s18, 0\n\ts_cbranch_scc1 96f\n\tv_mov_b32 v1, 1\n\tbuffer_store_dword v1, off, s[12:15], 0 offset:3328 sc1\n\t"
      "96:\n\t"
      : "+{v[0:31]}"(k), "+{v[160:191]}"(s0), "+{v[192:223]}"(s1), ALL_REGS :: "memory", "vcc", "scc", S_CLOB TK_CLOB);
}

DEV void conv_role(Ctx& c, int cg) {
  const KArg p = enter(c);
  constexpr int CH = 96 * 80, U8 = 0;
  constexpr int CP = 4160, FC = 8 * CH;
  static_assert(FC + 16 * CP <= 160000, "lds");
  c.budget = SPIN_CAP;
  const int w = c.w, lane = c.lane = fresh_lane(), tid = w * 64 + lane;
  const int d0 = 8 * cg + 2 * w;
  const int de = (cg >> 3) * 64 + (tid & 7) * 8, t0 = (cg & 7) * 256 + (tid >> 3) * 8;
  auto group_wait = [&](int phase, unsigned need) {
    gu32* cnt = c.ctrl + (C_FLAG + (F_C + phase * 16 + (cg >> 3)) * 128) / 4;
    VMWAIT(0);
    BAR();
    if (w == 0) {
      if ((phase || !KV_EARLY) && lane == 0) __hip_atomic_fetch_add(cnt, 1u, RLX_AGENT);
#pragma unroll 1
      while (__builtin_amdgcn_readfirstlane(__hip_atomic_load(cnt, RLX_AGENT)) < need) { if (spend(c)) break; __builtin_amdgcn_s_sleep(4); }
    }
    BAR();
  };
  if (ZWAIT && w == 0) wait_flag(c, F_Z + (cg >> 5) * 8 + (cg & 7), ZWAIT);
  const int va = vote_a(__builtin_amdgcn_readfirstlane(__hip_atomic_load(c.ctrl + (C_VERDICT + c.att * 128) / 4, RLX_AGENT))), far = va ^ ((cg >> 5) & 1) ^ 1;
  v4u fd[2][4], qr[8]; v2u fp[2][4];
  unsigned warm = 0;
  const unsigned dd = *(const GAS unsigned*)(p->ssmD + d0);
  auto build = [&](int k) {
#pragma unroll
    for (int i = 0; i < 4; ++i) {
      const v4u d = fd[k][i]; const v2u q = fp[k][i];
      const unsigned o0 = __builtin_amdgcn_alignbyte(q.y, q.x, 2), o1 = __builtin_amdgcn_alignbyte(d.x, q.y, 2), o2 = __builtin_amdgcn_alignbyte(d.y, d.x, 2),
                     o3 = __builtin_amdgcn_alignbyte(d.z, d.y, 2), o4 = __builtin_amdgcn_alignbyte(d.w, d.z, 2);
      const int at = FC + 4 * w * CP + 64 + i * 1024 + lane * 16;
      sts16(c.L, at, d); sts16(c.L, at + CP, v4u{o1, o2, o3, o4}); sts16(c.L, at + 2 * CP, v4u{q.y, d.x, d.y, d.z}); sts16(c.L, at + 3 * CP, v4u{o0, o1, o2, o3});
    }
  };
  {
    v4u u[8];
    BAR();
    KvIn in;
    kv_load(p, in, 4 * cg + w, lane);
#pragma unroll
    for (int k = 0; k < 2; ++k) {
      const rsrc_t rf = mkrs(p->ssmk + (d0 + k) * 2048, 4096);
#pragma unroll
      for (int i = 0; i < 4; ++i) { fd[k][i] = ld16n(rf, i * 1024 + lane * 16, 0); fp[k][i] = ld8n(rf, i * 1024 + lane * 16 - 8, 0); }
      if (k == 0 && KV_EARLY) {
#pragma unroll
        for (int i = 0; i < 8; ++i) u[i] = ld16s(c.rs, (i & 3) * 1024 + lane * 16, R_KVT + amap((d0 + (i >> 2)) * 4096, far));
      }
    }
    const rsrc_t rq = mkrs(p->bq, 2048 * 2048);
#pragma unroll
    for (int i = 0; i < 8; ++i) qr[i] = ld16n(rq, (t0 + i) * 2048 + de * 2, 0);
    if ((cg & 31) < 16) {
      const int n = (cg & 15) * 128 + (w & 1) * 64 + lane, zc = (cg >> 5) * 256 + 192;
      warm = __builtin_amdgcn_raw_buffer_load_b32(mkrs(w < 2 ? p->bq : p->ssmk, 2048 * 2048), w < 2 ? n * 2048 + zc * 2 : zc * 4096 + n * 128, 0, 0);
      if (!KV_EARLY) warm += __builtin_amdgcn_raw_buffer_load_b32(mkrs(w < 2 ? p->bk : p->bv, 2048 * 2048), n * 2048 + zc * 2, 0, 0);
    }
    SCHED();
    kv_make(c, in, FC + 4 * w * CP, lane);
    kv_store(c, FC + 4 * w * CP, 4 * cg + w, lane, va);
    LGKM(0);
#pragma unroll
    for (int i = 0; i < 5; ++i) { const int z = i * 64 + lane; sts16(c.L, U8 + (2 * w + z / 160) * CH + (z % 160) * 16, v4u{0, 0, 0, 0}); }
    if (lane < 16) sts16(c.L, FC + (4 * w + (lane >> 2)) * CP + (lane & 3) * 16, v4u{0, 0, 0, 0});
    build(0);
    group_wait(0, 8);
#pragma unroll
    for (int i = 0; i < 8; ++i) u[i] = ld16s(c.rs, (i & 3) * 1024 + lane * 16, R_KVT + amap((d0 + (i >> 2)) * 4096, far));
#pragma unroll
    for (int i = 0; i < 8; ++i) sts16(c.L, U8 + (2 * w + (i >> 2)) * CH + (32 + (i & 3) * 16 + (lane >> 2)) * 80 + (lane & 3) * 16, u[i]);
  }
  STAMP(5);
  const int h2 = lane >> 5, r = lane & 31;
#pragma unroll
  for (int k = 0; k < 2; ++k) {
    if (k == 1) build(1);
    f32x16 acc[2]; v32u x0, x1;
    const int ub = U8 + (2 * w + k) * CH;
    asm volatile("s_waitcnt lgkmcnt(0)\n\t"
      "ds_read_b64_tr_b16 v[160:161], %[fa] offset:40\n\tds_read_b64_tr_b16 v[162:163], %[fa] offset:32\n\tds_read_b128 v[168:171], %[ua] offset:2480\n\t"
      "ds_read_b128 v[172:175], %[ua] offset:5040\n\tds_read_b64_tr_b16 v[164:165], %[fa] offset:2088\n\tds_read_b64_tr_b16 v[166:167], %[fa] offset:2080\n\t"
      "ds_read_b64_tr_b16 v[176:177], %[fa] offset:8\n\tds_read_b64_tr_b16 v[178:179], %[fa] offset:0\n\tds_read_b128 v[184:187], %[ua] offset:2512\n\t"
      "ds_read_b128 v[188:191], %[ua] offset:5072\n\tds_read_b64_tr_b16 v[180:181], %[fa] offset:2056\n\tds_read_b64_tr_b16 v[182:183], %[fa] offset:2048\n\t"
      "s_waitcnt lgkmcnt(6)\n\tv_mfma_f32_32x32x16_bf16 v[128:143], v[160:163], v[168:171], 0\n\tds_read_b64_tr_b16 v[192:193], %[fa] offset:104\n\t"
      "ds_read_b64_tr_b16 v[194:195], %[fa] offset:96\n\tv_mfma_f32_32x32x16_bf16 v[144:159], v[160:163], v[172:175], 0\n\tds_read_b128 v[200:203], %[ua] offset:2400\n\t"
      "ds_read_b128 v[204:207], %[ua] offset:4960\n\tv_mfma_f32_32x32x16_bf16 v[144:159], v[164:167], v[168:171], v[144:159]\n\t"
      "ds_read_b64_tr_b16 v[196:197], %[fa] offset:2152\n\tds_read_b64_tr_b16 v[198:199], %[fa] offset:2144\n\ts_waitcnt lgkmcnt(6)\n\t"
      "v_mfma_f32_32x32x16_bf16 v[128:143], v[176:179], v[184:187], v[128:143]\n\tds_read_b64_tr_b16 v[208:209], %[fa] offset:72\n\t"
      "ds_read_b64_tr_b16 v[210:211], %[fa] offset:64\n\tv_mfma_f32_32x32x16_bf16 v[144:159], v[176:179], v[188:191], v[144:159]\n\t"
      "ds_read_b128 v[216:219], %[ua] offset:2432\n\tds_read_b128 v[220:223], %[ua] offset:4992\n\tv_mfma_f32_32x32x16_bf16 v[144:159], v[180:183], v[184:187], v[144:159]\n\t"
      "ds_read_b64_tr_b16 v[212:213], %[fa] offset:2120\n\tds_read_b64_tr_b16 v[214:215], %[fa] offset:2112\n\ts_waitcnt lgkmcnt(6)\n\t"
      "v_mfma_f32_32x32x16_bf16 v[128:143], v[192:195], v[200:203], v[128:143]\n\tds_read_b64_tr_b16 v[160:161], %[fa] offset:168\n\t"
      "ds_read_b64_tr_b16 v[162:163], %[fa] offset:160\n\tv_mfma_f32_32x32x16_bf16 v[144:159], v[192:195], v[204:207], v[144:159]\n\t"
      "ds_read_b128 v[168:171], %[ua] offset:2320\n\tds_read_b128 v[172:175], %[ua] offset:4880\n\tv_mfma_f32_32x32x16_bf16 v[144:159], v[196:199], v[200:203], v[144:159]\n\t"
      "ds_read_b64_tr_b16 v[164:165], %[fa] offset:2216\n\tds_read_b64_tr_b16 v[166:167], %[fa] offset:2208\n\ts_waitcnt lgkmcnt(6)\n\t"
      "v_mfma_f32_32x32x16_bf16 v[128:143], v[208:211], v[216:219], v[128:143]\n\tds_read_b64_tr_b16 v[176:177], %[fa] offset:136\n\t"
      "ds_read_b64_tr_b16 v[178:179], %[fa] offset:128\n\tv_mfma_f32_32x32x16_bf16 v[144:159], v[208:211], v[220:223], v[144:159]\n\t"
      "ds_read_b128 v[184:187], %[ua] offset:2352\n\tds_read_b128 v[188:191], %[ua] offset:4912\n\tv_mfma_f32_32x32x16_bf16 v[144:159], v[212:215], v[216:219], v[144:159]\n\t"
      "ds_read_b64_tr_b16 v[180:181], %[fa] offset:2184\n\tds_read_b64_tr_b16 v[182:183], %[fa] offset:2176\n\ts_waitcnt lgkmcnt(6)\n\t"
      "v_mfma_f32_32x32x16_bf16 v[128:143], v[160:163], v[168:171], v[128:143]\n\tds_read_b64_tr_b16 v[192:193], %[fa] offset:232\n\t"
      "ds_read_b64_tr_b16 v[194:195], %[fa] offset:224\n\tv_mfma_f32_32x32x16_bf16 v[144:159], v[160:163], v[172:175], v[144:159]\n\t"
      "ds_read_b128 v[200:203], %[ua] offset:2240\n\tds_read_b128 v[204:207], %[ua] offset:4800\n\tv_mfma_f32_32x32x16_bf16 v[144:159], v[164:167], v[168:171], v[144:159]\n\t"
      "ds_read_b64_tr_b16 v[196:197], %[fa] offset:2280\n\tds_read_b64_tr_b16 v[198:199], %[fa] offset:2272\n\ts_waitcnt lgkmcnt(6)\n\t"
      "v_mfma_f32_32x32x16_bf16 v[128:143], v[176:179], v[184:187], v[128:143]\n\tds_read_b64_tr_b16 v[208:209], %[fa] offset:200\n\t"
      "ds_read_b64_tr_b16 v[210:211], %[fa] offset:192\n\tv_mfma_f32_32x32x16_bf16 v[144:159], v[176:179], v[188:191], v[144:159]\n\t"
      "ds_read_b128 v[216:219], %[ua] offset:2272\n\tds_read_b128 v[220:223], %[ua] offset:4832\n\tv_mfma_f32_32x32x16_bf16 v[144:159], v[180:183], v[184:187], v[144:159]\n\t"
      "ds_read_b64_tr_b16 v[212:213], %[fa] offset:2248\n\tds_read_b64_tr_b16 v[214:215], %[fa] offset:2240\n\ts_waitcnt lgkmcnt(6)\n\t"
      "v_mfma_f32_32x32x16_bf16 v[128:143], v[192:195], v[200:203], v[128:143]\n\tds_read_b64_tr_b16 v[160:161], %[fa] offset:296\n\t"
      "ds_read_b64_tr_b16 v[162:163], %[fa] offset:288\n\tv_mfma_f32_32x32x16_bf16 v[144:159], v[192:195], v[204:207], v[144:159]\n\t"
      "ds_read_b128 v[168:171], %[ua] offset:2160\n\tds_read_b128 v[172:175], %[ua] offset:4720\n\tv_mfma_f32_32x32x16_bf16 v[144:159], v[196:199], v[200:203], v[144:159]\n\t"
      "ds_read_b64_tr_b16 v[164:165], %[fa] offset:2344\n\tds_read_b64_tr_b16 v[166:167], %[fa] offset:2336\n\ts_waitcnt lgkmcnt(6)\n\t"
      "v_mfma_f32_32x32x16_bf16 v[128:143], v[208:211], v[216:219], v[128:143]\n\tds_read_b64_tr_b16 v[176:177], %[fa] offset:264\n\t"
      "ds_read_b64_tr_b16 v[178:179], %[fa] offset:256\n\tv_mfma_f32_32x32x16_bf16 v[144:159], v[208:211], v[220:223], v[144:159]\n\t"
      "ds_read_b128 v[184:187], %[ua] offset:2192\n\tds_read_b128 v[188:191], %[ua] offset:4752\n\tv_mfma_f32_32x32x16_bf16 v[144:159], v[212:215], v[216:219], v[144:159]\n\t"
      "ds_read_b64_tr_b16 v[180:181], %[fa] offset:2312\n\tds_read_b64_tr_b16 v[182:183], %[fa] offset:2304\n\ts_waitcnt lgkmcnt(6)\n\t"
      "v_mfma_f32_32x32x16_bf16 v[128:143], v[160:163], v[168:171], v[128:143]\n\tds_read_b64_tr_b16 v[192:193], %[fa] offset:360\n\t"
      "ds_read_b64_tr_b16 v[194:195], %[fa] offset:352\n\tv_mfma_f32_32x32x16_bf16 v[144:159], v[160:163], v[172:175], v[144:159]\n\t"
      "ds_read_b128 v[200:203], %[ua] offset:2080\n\tds_read_b128 v[204:207], %[ua] offset:4640\n\tv_mfma_f32_32x32x16_bf16 v[144:159], v[164:167], v[168:171], v[144:159]\n\t"
      "ds_read_b64_tr_b16 v[196:197], %[fa] offset:2408\n\tds_read_b64_tr_b16 v[198:199], %[fa] offset:2400\n\ts_waitcnt lgkmcnt(6)\n\t"
      "v_mfma_f32_32x32x16_bf16 v[128:143], v[176:179], v[184:187], v[128:143]\n\tds_read_b64_tr_b16 v[208:209], %[fa] offset:328\n\t"
      "ds_read_b64_tr_b16 v[210:211], %[fa] offset:320\n\tv_mfma_f32_32x32x16_bf16 v[144:159], v[176:179], v[188:191], v[144:159]\n\t"
      "ds_read_b128 v[216:219], %[ua] offset:2112\n\tds_read_b128 v[220:223], %[ua] offset:4672\n\tv_mfma_f32_32x32x16_bf16 v[144:159], v[180:183], v[184:187], v[144:159]\n\t"
      "ds_read_b64_tr_b16 v[212:213], %[fa] offset:2376\n\tds_read_b64_tr_b16 v[214:215], %[fa] offset:2368\n\ts_waitcnt lgkmcnt(6)\n\t"
      "v_mfma_f32_32x32x16_bf16 v[128:143], v[192:195], v[200:203], v[128:143]\n\tds_read_b64_tr_b16 v[160:161], %[fa] offset:424\n\t"
      "ds_read_b64_tr_b16 v[162:163], %[fa] offset:416\n\tv_mfma_f32_32x32x16_bf16 v[144:159], v[192:195], v[204:207], v[144:159]\n\t"
      "ds_read_b128 v[168:171], %[ua] offset:2000\n\tds_read_b128 v[172:175], %[ua] offset:4560\n\tv_mfma_f32_32x32x16_bf16 v[144:159], v[196:199], v[200:203], v[144:159]\n\t"
      "ds_read_b64_tr_b16 v[164:165], %[fa] offset:2472\n\tds_read_b64_tr_b16 v[166:167], %[fa] offset:2464\n\ts_waitcnt lgkmcnt(6)\n\t"
      "v_mfma_f32_32x32x16_bf16 v[128:143], v[208:211], v[216:219], v[128:143]\n\tds_read_b64_tr_b16 v[176:177], %[fa] offset:392\n\t"
      "ds_read_b64_tr_b16 v[178:179], %[fa] offset:384\n\tv_mfma_f32_32x32x16_bf16 v[144:159], v[208:211], v[220:223], v[144:159]\n\t"
      "ds_read_b128 v[184:187], %[ua] offset:2032\n\tds_read_b128 v[188:191], %[ua] offset:4592\n\tv_mfma_f32_32x32x16_bf16 v[144:159], v[212:215], v[216:219], v[144:159]\n\t"
      "ds_read_b64_tr_b16 v[180:181], %[fa] offset:2440\n\tds_read_b64_tr_b16 v[182:183], %[fa] offset:2432\n\ts_waitcnt lgkmcnt(6)\n\t"
      "v_mfma_f32_32x32x16_bf16 v[128:143], v[160:163], v[168:171], v[128:143]\n\tds_read_b64_tr_b16 v[192:193], %[fa] offset:488\n\t"
      "ds_read_b64_tr_b16 v[194:195], %[fa] offset:480\n\tv_mfma_f32_32x32x16_bf16 v[144:159], v[160:163], v[172:175], v[144:159]\n\t"
      "ds_read_b128 v[200:203], %[ua] offset:1920\n\tds_read_b128 v[204:207], %[ua] offset:4480\n\tv_mfma_f32_32x32x16_bf16 v[144:159], v[164:167], v[168:171], v[144:159]\n\t"
      "ds_read_b64_tr_b16 v[196:197], %[fa] offset:2536\n\tds_read_b64_tr_b16 v[198:199], %[fa] offset:2528\n\ts_waitcnt lgkmcnt(6)\n\t"
      "v_mfma_f32_32x32x16_bf16 v[128:143], v[176:179], v[184:187], v[128:143]\n\tds_read_b64_tr_b16 v[208:209], %[fa] offset:456\n\t"
      "ds_read_b64_tr_b16 v[210:211], %[fa] offset:448\n\tv_mfma_f32_32x32x16_bf16 v[144:159], v[176:179], v[188:191], v[144:159]\n\t"
      "ds_read_b128 v[216:219], %[ua] offset:1952\n\tds_read_b128 v[220:223], %[ua] offset:4512\n\tv_mfma_f32_32x32x16_bf16 v[144:159], v[180:183], v[184:187], v[144:159]\n\t"
      "ds_read_b64_tr_b16 v[212:213], %[fa] offset:2504\n\tds_read_b64_tr_b16 v[214:215], %[fa] offset:2496\n\ts_waitcnt lgkmcnt(6)\n\t"
      "v_mfma_f32_32x32x16_bf16 v[128:143], v[192:195], v[200:203], v[128:143]\n\tds_read_b64_tr_b16 v[160:161], %[fa] offset:552\n\t"
      "ds_read_b64_tr_b16 v[162:163], %[fa] offset:544\n\tv_mfma_f32_32x32x16_bf16 v[144:159], v[192:195], v[204:207], v[144:159]\n\t"
      "ds_read_b128 v[168:171], %[ua] offset:1840\n\tds_read_b128 v[172:175], %[ua] offset:4400\n\tv_mfma_f32_32x32x16_bf16 v[144:159], v[196:199], v[200:203], v[144:159]\n\t"
      "ds_read_b64_tr_b16 v[164:165], %[fa] offset:2600\n\tds_read_b64_tr_b16 v[166:167], %[fa] offset:2592\n\ts_waitcnt lgkmcnt(6)\n\t"
      "v_mfma_f32_32x32x16_bf16 v[128:143], v[208:211], v[216:219], v[128:143]\n\tds_read_b64_tr_b16 v[176:177], %[fa] offset:520\n\t"
      "ds_read_b64_tr_b16 v[178:179], %[fa] offset:512\n\tv_mfma_f32_32x32x16_bf16 v[144:159], v[208:211], v[220:223], v[144:159]\n\t"
      "ds_read_b128 v[184:187], %[ua] offset:1872\n\tds_read_b128 v[188:191], %[ua] offset:4432\n\tv_mfma_f32_32x32x16_bf16 v[144:159], v[212:215], v[216:219], v[144:159]\n\t"
      "ds_read_b64_tr_b16 v[180:181], %[fa] offset:2568\n\tds_read_b64_tr_b16 v[182:183], %[fa] offset:2560\n\ts_waitcnt lgkmcnt(6)\n\t"
      "v_mfma_f32_32x32x16_bf16 v[128:143], v[160:163], v[168:171], v[128:143]\n\tds_read_b64_tr_b16 v[192:193], %[fa] offset:616\n\t"
      "ds_read_b64_tr_b16 v[194:195], %[fa] offset:608\n\tv_mfma_f32_32x32x16_bf16 v[144:159], v[160:163], v[172:175], v[144:159]\n\t"
      "ds_read_b128 v[200:203], %[ua] offset:1760\n\tds_read_b128 v[204:207], %[ua] offset:4320\n\tv_mfma_f32_32x32x16_bf16 v[144:159], v[164:167], v[168:171], v[144:159]\n\t"
      "ds_read_b64_tr_b16 v[196:197], %[fa] offset:2664\n\tds_read_b64_tr_b16 v[198:199], %[fa] offset:2656\n\ts_waitcnt lgkmcnt(6)\n\t"
      "v_mfma_f32_32x32x16_bf16 v[128:143], v[176:179], v[184:187], v[128:143]\n\tds_read_b64_tr_b16 v[208:209], %[fa] offset:584\n\t"
      "ds_read_b64_tr_b16 v[210:211], %[fa] offset:576\n\tv_mfma_f32_32x32x16_bf16 v[144:159], v[176:179], v[188:191], v[144:159]\n\t"
      "ds_read_b128 v[216:219], %[ua] offset:1792\n\tds_read_b128 v[220:223], %[ua] offset:4352\n\tv_mfma_f32_32x32x16_bf16 v[144:159], v[180:183], v[184:187], v[144:159]\n\t"
      "ds_read_b64_tr_b16 v[212:213], %[fa] offset:2632\n\tds_read_b64_tr_b16 v[214:215], %[fa] offset:2624\n\ts_waitcnt lgkmcnt(6)\n\t"
      "v_mfma_f32_32x32x16_bf16 v[128:143], v[192:195], v[200:203], v[128:143]\n\tds_read_b64_tr_b16 v[160:161], %[fa] offset:680\n\t"
      "ds_read_b64_tr_b16 v[162:163], %[fa] offset:672\n\tv_mfma_f32_32x32x16_bf16 v[144:159], v[192:195], v[204:207], v[144:159]\n\t"
      "ds_read_b128 v[168:171], %[ua] offset:1680\n\tds_read_b128 v[172:175], %[ua] offset:4240\n\tv_mfma_f32_32x32x16_bf16 v[144:159], v[196:199], v[200:203], v[144:159]\n\t"
      "ds_read_b64_tr_b16 v[164:165], %[fa] offset:2728\n\tds_read_b64_tr_b16 v[166:167], %[fa] offset:2720\n\ts_waitcnt lgkmcnt(6)\n\t"
      "v_mfma_f32_32x32x16_bf16 v[128:143], v[208:211], v[216:219], v[128:143]\n\tds_read_b64_tr_b16 v[176:177], %[fa] offset:648\n\t"
      "ds_read_b64_tr_b16 v[178:179], %[fa] offset:640\n\tv_mfma_f32_32x32x16_bf16 v[144:159], v[208:211], v[220:223], v[144:159]\n\t"
      "ds_read_b128 v[184:187], %[ua] offset:1712\n\tds_read_b128 v[188:191], %[ua] offset:4272\n\tv_mfma_f32_32x32x16_bf16 v[144:159], v[212:215], v[216:219], v[144:159]\n\t"
      "ds_read_b64_tr_b16 v[180:181], %[fa] offset:2696\n\tds_read_b64_tr_b16 v[182:183], %[fa] offset:2688\n\ts_waitcnt lgkmcnt(6)\n\t"
      "v_mfma_f32_32x32x16_bf16 v[128:143], v[160:163], v[168:171], v[128:143]\n\tds_read_b64_tr_b16 v[192:193], %[fa] offset:744\n\t"
      "ds_read_b64_tr_b16 v[194:195], %[fa] offset:736\n\tv_mfma_f32_32x32x16_bf16 v[144:159], v[160:163], v[172:175], v[144:159]\n\t"
      "ds_read_b128 v[200:203], %[ua] offset:1600\n\tds_read_b128 v[204:207], %[ua] offset:4160\n\tv_mfma_f32_32x32x16_bf16 v[144:159], v[164:167], v[168:171], v[144:159]\n\t"
      "ds_read_b64_tr_b16 v[196:197], %[fa] offset:2792\n\tds_read_b64_tr_b16 v[198:199], %[fa] offset:2784\n\ts_waitcnt lgkmcnt(6)\n\t"
      "v_mfma_f32_32x32x16_bf16 v[128:143], v[176:179], v[184:187], v[128:143]\n\tds_read_b64_tr_b16 v[208:209], %[fa] offset:712\n\t"
      "ds_read_b64_tr_b16 v[210:211], %[fa] offset:704\n\tv_mfma_f32_32x32x16_bf16 v[144:159], v[176:179], v[188:191], v[144:159]\n\t"
      "ds_read_b128 v[216:219], %[ua] offset:1632\n\tds_read_b128 v[220:223], %[ua] offset:4192\n\tv_mfma_f32_32x32x16_bf16 v[144:159], v[180:183], v[184:187], v[144:159]\n\t"
      "ds_read_b64_tr_b16 v[212:213], %[fa] offset:2760\n\tds_read_b64_tr_b16 v[214:215], %[fa] offset:2752\n\ts_waitcnt lgkmcnt(6)\n\t"
      "v_mfma_f32_32x32x16_bf16 v[128:143], v[192:195], v[200:203], v[128:143]\n\tds_read_b64_tr_b16 v[160:161], %[fa] offset:808\n\t"
      "ds_read_b64_tr_b16 v[162:163], %[fa] offset:800\n\tv_mfma_f32_32x32x16_bf16 v[144:159], v[192:195], v[204:207], v[144:159]\n\t"
      "ds_read_b128 v[168:171], %[ua] offset:1520\n\tds_read_b128 v[172:175], %[ua] offset:4080\n\tv_mfma_f32_32x32x16_bf16 v[144:159], v[196:199], v[200:203], v[144:159]\n\t"
      "ds_read_b64_tr_b16 v[164:165], %[fa] offset:2856\n\tds_read_b64_tr_b16 v[166:167], %[fa] offset:2848\n\ts_waitcnt lgkmcnt(6)\n\t"
      "v_mfma_f32_32x32x16_bf16 v[128:143], v[208:211], v[216:219], v[128:143]\n\tds_read_b64_tr_b16 v[176:177], %[fa] offset:776\n\t"
      "ds_read_b64_tr_b16 v[178:179], %[fa] offset:768\n\tv_mfma_f32_32x32x16_bf16 v[144:159], v[208:211], v[220:223], v[144:159]\n\t"
      "ds_read_b128 v[184:187], %[ua] offset:1552\n\tds_read_b128 v[188:191], %[ua] offset:4112\n\tv_mfma_f32_32x32x16_bf16 v[144:159], v[212:215], v[216:219], v[144:159]\n\t"
      "ds_read_b64_tr_b16 v[180:181], %[fa] offset:2824\n\tds_read_b64_tr_b16 v[182:183], %[fa] offset:2816\n\ts_waitcnt lgkmcnt(6)\n\t"
      "v_mfma_f32_32x32x16_bf16 v[128:143], v[160:163], v[168:171], v[128:143]\n\tds_read_b64_tr_b16 v[192:193], %[fa] offset:872\n\t"
      "ds_read_b64_tr_b16 v[194:195], %[fa] offset:864\n\tv_mfma_f32_32x32x16_bf16 v[144:159], v[160:163], v[172:175], v[144:159]\n\t"
      "ds_read_b128 v[200:203], %[ua] offset:1440\n\tds_read_b128 v[204:207], %[ua] offset:4000\n\tv_mfma_f32_32x32x16_bf16 v[144:159], v[164:167], v[168:171], v[144:159]\n\t"
      "ds_read_b64_tr_b16 v[196:197], %[fa] offset:2920\n\tds_read_b64_tr_b16 v[198:199], %[fa] offset:2912\n\ts_waitcnt lgkmcnt(6)\n\t"
      "v_mfma_f32_32x32x16_bf16 v[128:143], v[176:179], v[184:187], v[128:143]\n\tds_read_b64_tr_b16 v[208:209], %[fa] offset:840\n\t"
      "ds_read_b64_tr_b16 v[210:211], %[fa] offset:832\n\tv_mfma_f32_32x32x16_bf16 v[144:159], v[176:179], v[188:191], v[144:159]\n\t"
      "ds_read_b128 v[216:219], %[ua] offset:1472\n\tds_read_b128 v[220:223], %[ua] offset:4032\n\tv_mfma_f32_32x32x16_bf16 v[144:159], v[180:183], v[184:187], v[144:159]\n\t"
      "ds_read_b64_tr_b16 v[212:213], %[fa] offset:2888\n\tds_read_b64_tr_b16 v[214:215], %[fa] offset:2880\n\ts_waitcnt lgkmcnt(6)\n\t"
      "v_mfma_f32_32x32x16_bf16 v[128:143], v[192:195], v[200:203], v[128:143]\n\tds_read_b64_tr_b16 v[160:161], %[fa] offset:936\n\t"
      "ds_read_b64_tr_b16 v[162:163], %[fa] offset:928\n\tv_mfma_f32_32x32x16_bf16 v[144:159], v[192:195], v[204:207], v[144:159]\n\t"
      "ds_read_b128 v[168:171], %[ua] offset:1360\n\tds_read_b128 v[172:175], %[ua] offset:3920\n\tv_mfma_f32_32x32x16_bf16 v[144:159], v[196:199], v[200:203], v[144:159]\n\t"
      "ds_read_b64_tr_b16 v[164:165], %[fa] offset:2984\n\tds_read_b64_tr_b16 v[166:167], %[fa] offset:2976\n\ts_waitcnt lgkmcnt(6)\n\t"
      "v_mfma_f32_32x32x16_bf16 v[128:143], v[208:211], v[216:219], v[128:143]\n\tds_read_b64_tr_b16 v[176:177], %[fa] offset:904\n\t"
      "ds_read_b64_tr_b16 v[178:179], %[fa] offset:896\n\tv_mfma_f32_32x32x16_bf16 v[144:159], v[208:211], v[220:223], v[144:159]\n\t"
      "ds_read_b128 v[184:187], %[ua] offset:1392\n\tds_read_b128 v[188:191], %[ua] offset:3952\n\tv_mfma_f32_32x32x16_bf16 v[144:159], v[212:215], v[216:219], v[144:159]\n\t"
      "ds_read_b64_tr_b16 v[180:181], %[fa] offset:2952\n\tds_read_b64_tr_b16 v[182:183], %[fa] offset:2944\n\ts_waitcnt lgkmcnt(6)\n\t"
      "v_mfma_f32_32x32x16_bf16 v[128:143], v[160:163], v[168:171], v[128:143]\n\tds_read_b64_tr_b16 v[192:193], %[fa] offset:1000\n\t"
      "ds_read_b64_tr_b16 v[194:195], %[fa] offset:992\n\tv_mfma_f32_32x32x16_bf16 v[144:159], v[160:163], v[172:175], v[144:159]\n\t"
      "ds_read_b128 v[200:203], %[ua] offset:1280\n\tds_read_b128 v[204:207], %[ua] offset:3840\n\tv_mfma_f32_32x32x16_bf16 v[144:159], v[164:167], v[168:171], v[144:159]\n\t"
      "ds_read_b64_tr_b16 v[196:197], %[fa] offset:3048\n\tds_read_b64_tr_b16 v[198:199], %[fa] offset:3040\n\ts_waitcnt lgkmcnt(6)\n\t"
      "v_mfma_f32_32x32x16_bf16 v[128:143], v[176:179], v[184:187], v[128:143]\n\tds_read_b64_tr_b16 v[208:209], %[fa] offset:968\n\t"
      "ds_read_b64_tr_b16 v[210:211], %[fa] offset:960\n\tv_mfma_f32_32x32x16_bf16 v[144:159], v[176:179], v[188:191], v[144:159]\n\t"
      "ds_read_b128 v[216:219], %[ua] offset:1312\n\tds_read_b128 v[220:223], %[ua] offset:3872\n\tv_mfma_f32_32x32x16_bf16 v[144:159], v[180:183], v[184:187], v[144:159]\n\t"
      "ds_read_b64_tr_b16 v[212:213], %[fa] offset:3016\n\tds_read_b64_tr_b16 v[214:215], %[fa] offset:3008\n\ts_waitcnt lgkmcnt(6)\n\t"
      "v_mfma_f32_32x32x16_bf16 v[128:143], v[192:195], v[200:203], v[128:143]\n\tds_read_b64_tr_b16 v[160:161], %[fa] offset:1064\n\t"
      "ds_read_b64_tr_b16 v[162:163], %[fa] offset:1056\n\tv_mfma_f32_32x32x16_bf16 v[144:159], v[192:195], v[204:207], v[144:159]\n\t"
      "ds_read_b128 v[168:171], %[ua] offset:1200\n\tds_read_b128 v[172:175], %[ua] offset:3760\n\tv_mfma_f32_32x32x16_bf16 v[144:159], v[196:199], v[200:203], v[144:159]\n\t"
      "ds_read_b64_tr_b16 v[164:165], %[fa] offset:3112\n\tds_read_b64_tr_b16 v[166:167], %[fa] offset:3104\n\ts_waitcnt lgkmcnt(6)\n\t"
      "v_mfma_f32_32x32x16_bf16 v[128:143], v[208:211], v[216:219], v[128:143]\n\tds_read_b64_tr_b16 v[176:177], %[fa] offset:1032\n\t"
      "ds_read_b64_tr_b16 v[178:179], %[fa] offset:1024\n\tv_mfma_f32_32x32x16_bf16 v[144:159], v[208:211], v[220:223], v[144:159]\n\t"
      "ds_read_b128 v[184:187], %[ua] offset:1232\n\tds_read_b128 v[188:191], %[ua] offset:3792\n\tv_mfma_f32_32x32x16_bf16 v[144:159], v[212:215], v[216:219], v[144:159]\n\t"
      "ds_read_b64_tr_b16 v[180:181], %[fa] offset:3080\n\tds_read_b64_tr_b16 v[182:183], %[fa] offset:3072\n\ts_waitcnt lgkmcnt(6)\n\t"
      "v_mfma_f32_32x32x16_bf16 v[128:143], v[160:163], v[168:171], v[128:143]\n\tds_read_b64_tr_b16 v[192:193], %[fa] offset:1128\n\t"
      "ds_read_b64_tr_b16 v[194:195], %[fa] offset:1120\n\tv_mfma_f32_32x32x16_bf16 v[144:159], v[160:163], v[172:175], v[144:159]\n\t"
      "ds_read_b128 v[200:203], %[ua] offset:1120\n\tds_read_b128 v[204:207], %[ua] offset:3680\n\tv_mfma_f32_32x32x16_bf16 v[144:159], v[164:167], v[168:171], v[144:159]\n\t"
      "ds_read_b64_tr_b16 v[196:197], %[fa] offset:3176\n\tds_read_b64_tr_b16 v[198:199], %[fa] offset:3168\n\ts_waitcnt lgkmcnt(6)\n\t"
      "v_mfma_f32_32x32x16_bf16 v[128:143], v[176:179], v[184:187], v[128:143]\n\tds_read_b64_tr_b16 v[208:209], %[fa] offset:1096\n\t"
      "ds_read_b64_tr_b16 v[210:211], %[fa] offset:1088\n\tv_mfma_f32_32x32x16_bf16 v[144:159], v[176:179], v[188:191], v[144:159]\n\t"
      "ds_read_b128 v[216:219], %[ua] offset:1152\n\tds_read_b128 v[220:223], %[ua] offset:3712\n\tv_mfma_f32_32x32x16_bf16 v[144:159], v[180:183], v[184:187], v[144:159]\n\t"
      "ds_read_b64_tr_b16 v[212:213], %[fa] offset:3144\n\tds_read_b64_tr_b16 v[214:215], %[fa] offset:3136\n\ts_waitcnt lgkmcnt(6)\n\t"
      "v_mfma_f32_32x32x16_bf16 v[128:143], v[192:195], v[200:203], v[128:143]\n\tds_read_b64_tr_b16 v[160:161], %[fa] offset:1192\n\t"
      "ds_read_b64_tr_b16 v[162:163], %[fa] offset:1184\n\tv_mfma_f32_32x32x16_bf16 v[144:159], v[192:195], v[204:207], v[144:159]\n\t"
      "ds_read_b128 v[168:171], %[ua] offset:1040\n\tds_read_b128 v[172:175], %[ua] offset:3600\n\tv_mfma_f32_32x32x16_bf16 v[144:159], v[196:199], v[200:203], v[144:159]\n\t"
      "ds_read_b64_tr_b16 v[164:165], %[fa] offset:3240\n\tds_read_b64_tr_b16 v[166:167], %[fa] offset:3232\n\ts_waitcnt lgkmcnt(6)\n\t"
      "v_mfma_f32_32x32x16_bf16 v[128:143], v[208:211], v[216:219], v[128:143]\n\tds_read_b64_tr_b16 v[176:177], %[fa] offset:1160\n\t"
      "ds_read_b64_tr_b16 v[178:179], %[fa] offset:1152\n\tv_mfma_f32_32x32x16_bf16 v[144:159], v[208:211], v[220:223], v[144:159]\n\t"
      "ds_read_b128 v[184:187], %[ua] offset:1072\n\tds_read_b128 v[188:191], %[ua] offset:3632\n\tv_mfma_f32_32x32x16_bf16 v[144:159], v[212:215], v[216:219], v[144:159]\n\t"
      "ds_read_b64_tr_b16 v[180:181], %[fa] offset:3208\n\tds_read_b64_tr_b16 v[182:183], %[fa] offset:3200\n\ts_waitcnt lgkmcnt(6)\n\t"
      "v_mfma_f32_32x32x16_bf16 v[128:143], v[160:163], v[168:171], v[128:143]\n\tds_read_b64_tr_b16 v[192:193], %[fa] offset:1256\n\t"
      "ds_read_b64_tr_b16 v[194:195], %[fa] offset:1248\n\tv_mfma_f32_32x32x16_bf16 v[144:159], v[160:163], v[172:175], v[144:159]\n\t"
      "ds_read_b128 v[200:203], %[ua] offset:960\n\tds_read_b128 v[204:207], %[ua] offset:3520\n\tv_mfma_f32_32x32x16_bf16 v[144:159], v[164:167], v[168:171], v[144:159]\n\t"
      "ds_read_b64_tr_b16 v[196:197], %[fa] offset:3304\n\tds_read_b64_tr_b16 v[198:199], %[fa] offset:3296\n\ts_waitcnt lgkmcnt(6)\n\t"
      "v_mfma_f32_32x32x16_bf16 v[128:143], v[176:179], v[184:187], v[128:143]\n\tds_read_b64_tr_b16 v[208:209], %[fa] offset:1224\n\t"
      "ds_read_b64_tr_b16 v[210:211], %[fa] offset:1216\n\tv_mfma_f32_32x32x16_bf16 v[144:159], v[176:179], v[188:191], v[144:159]\n\t"
      "ds_read_b128 v[216:219], %[ua] offset:992\n\tds_read_b128 v[220:223], %[ua] offset:3552\n\tv_mfma_f32_32x32x16_bf16 v[144:159], v[180:183], v[184:187], v[144:159]\n\t"
      "ds_read_b64_tr_b16 v[212:213], %[fa] offset:3272\n\tds_read_b64_tr_b16 v[214:215], %[fa] offset:3264\n\ts_waitcnt lgkmcnt(6)\n\t"
      "v_mfma_f32_32x32x16_bf16 v[128:143], v[192:195], v[200:203], v[128:143]\n\tds_read_b64_tr_b16 v[160:161], %[fa] offset:1320\n\t"
      "ds_read_b64_tr_b16 v[162:163], %[fa] offset:1312\n\tv_mfma_f32_32x32x16_bf16 v[144:159], v[192:195], v[204:207], v[144:159]\n\t"
      "ds_read_b128 v[168:171], %[ua] offset:880\n\tds_read_b128 v[172:175], %[ua] offset:3440\n\tv_mfma_f32_32x32x16_bf16 v[144:159], v[196:199], v[200:203], v[144:159]\n\t"
      "ds_read_b64_tr_b16 v[164:165], %[fa] offset:3368\n\tds_read_b64_tr_b16 v[166:167], %[fa] offset:3360\n\ts_waitcnt lgkmcnt(6)\n\t"
      "v_mfma_f32_32x32x16_bf16 v[128:143], v[208:211], v[216:219], v[128:143]\n\tds_read_b64_tr_b16 v[176:177], %[fa] offset:1288\n\t"
      "ds_read_b64_tr_b16 v[178:179], %[fa] offset:1280\n\tv_mfma_f32_32x32x16_bf16 v[144:159], v[208:211], v[220:223], v[144:159]\n\t"
      "ds_read_b128 v[184:187], %[ua] offset:912\n\tds_read_b128 v[188:191], %[ua] offset:3472\n\tv_mfma_f32_32x32x16_bf16 v[144:159], v[212:215], v[216:219], v[144:159]\n\t"
      "ds_read_b64_tr_b16 v[180:181], %[fa] offset:3336\n\tds_read_b64_tr_b16 v[182:183], %[fa] offset:3328\n\ts_waitcnt lgkmcnt(6)\n\t"
      "v_mfma_f32_32x32x16_bf16 v[128:143], v[160:163], v[168:171], v[128:143]\n\tds_read_b64_tr_b16 v[192:193], %[fa] offset:1384\n\t"
      "ds_read_b64_tr_b16 v[194:195], %[fa] offset:1376\n\tv_mfma_f32_32x32x16_bf16 v[144:159], v[160:163], v[172:175], v[144:159]\n\t"
      "ds_read_b128 v[200:203], %[ua] offset:800\n\tds_read_b128 v[204:207], %[ua] offset:3360\n\tv_mfma_f32_32x32x16_bf16 v[144:159], v[164:167], v[168:171], v[144:159]\n\t"
      "ds_read_b64_tr_b16 v[196:197], %[fa] offset:3432\n\tds_read_b64_tr_b16 v[198:199], %[fa] offset:3424\n\ts_waitcnt lgkmcnt(6)\n\t"
      "v_mfma_f32_32x32x16_bf16 v[128:143], v[176:179], v[184:187], v[128:143]\n\tds_read_b64_tr_b16 v[208:209], %[fa] offset:1352\n\t"
      "ds_read_b64_tr_b16 v[210:211], %[fa] offset:1344\n\tv_mfma_f32_32x32x16_bf16 v[144:159], v[176:179], v[188:191], v[144:159]\n\t"
      "ds_read_b128 v[216:219], %[ua] offset:832\n\tds_read_b128 v[220:223], %[ua] offset:3392\n\tv_mfma_f32_32x32x16_bf16 v[144:159], v[180:183], v[184:187], v[144:159]\n\t"
      "ds_read_b64_tr_b16 v[212:213], %[fa] offset:3400\n\tds_read_b64_tr_b16 v[214:215], %[fa] offset:3392\n\ts_waitcnt lgkmcnt(6)\n\t"
      "v_mfma_f32_32x32x16_bf16 v[128:143], v[192:195], v[200:203], v[128:143]\n\tds_read_b64_tr_b16 v[160:161], %[fa] offset:1448\n\t"
      "ds_read_b64_tr_b16 v[162:163], %[fa] offset:1440\n\tv_mfma_f32_32x32x16_bf16 v[144:159], v[192:195], v[204:207], v[144:159]\n\t"
      "ds_read_b128 v[168:171], %[ua] offset:720\n\tds_read_b128 v[172:175], %[ua] offset:3280\n\tv_mfma_f32_32x32x16_bf16 v[144:159], v[196:199], v[200:203], v[144:159]\n\t"
      "ds_read_b64_tr_b16 v[164:165], %[fa] offset:3496\n\tds_read_b64_tr_b16 v[166:167], %[fa] offset:3488\n\ts_waitcnt lgkmcnt(6)\n\t"
      "v_mfma_f32_32x32x16_bf16 v[128:143], v[208:211], v[216:219], v[128:143]\n\tds_read_b64_tr_b16 v[176:177], %[fa] offset:1416\n\t"
      "ds_read_b64_tr_b16 v[178:179], %[fa] offset:1408\n\tv_mfma_f32_32x32x16_bf16 v[144:159], v[208:211], v[220:223], v[144:159]\n\t"
      "ds_read_b128 v[184:187], %[ua] offset:752\n\tds_read_b128 v[188:191], %[ua] offset:3312\n\tv_mfma_f32_32x32x16_bf16 v[144:159], v[212:215], v[216:219], v[144:159]\n\t"
      "ds_read_b64_tr_b16 v[180:181], %[fa] offset:3464\n\tds_read_b64_tr_b16 v[182:183], %[fa] offset:3456\n\ts_waitcnt lgkmcnt(6)\n\t"
      "v_mfma_f32_32x32x16_bf16 v[128:143], v[160:163], v[168:171], v[128:143]\n\tds_read_b64_tr_b16 v[192:193], %[fa] offset:1512\n\t"
      "ds_read_b64_tr_b16 v[194:195], %[fa] offset:1504\n\tv_mfma_f32_32x32x16_bf16 v[144:159], v[160:163], v[172:175], v[144:159]\n\t"
      "ds_read_b128 v[200:203], %[ua] offset:640\n\tds_read_b128 v[204:207], %[ua] offset:3200\n\tv_mfma_f32_32x32x16_bf16 v[144:159], v[164:167], v[168:171], v[144:159]\n\t"
      "ds_read_b64_tr_b16 v[196:197], %[fa] offset:3560\n\tds_read_b64_tr_b16 v[198:199], %[fa] offset:3552\n\ts_waitcnt lgkmcnt(6)\n\t"
      "v_mfma_f32_32x32x16_bf16 v[128:143], v[176:179], v[184:187], v[128:143]\n\tds_read_b64_tr_b16 v[208:209], %[fa] offset:1480\n\t"
      "ds_read_b64_tr_b16 v[210:211], %[fa] offset:1472\n\tv_mfma_f32_32x32x16_bf16 v[144:159], v[176:179], v[188:191], v[144:159]\n\t"
      "ds_read_b128 v[216:219], %[ua] offset:672\n\tds_read_b128 v[220:223], %[ua] offset:3232\n\tv_mfma_f32_32x32x16_bf16 v[144:159], v[180:183], v[184:187], v[144:159]\n\t"
      "ds_read_b64_tr_b16 v[212:213], %[fa] offset:3528\n\tds_read_b64_tr_b16 v[214:215], %[fa] offset:3520\n\ts_waitcnt lgkmcnt(6)\n\t"
      "v_mfma_f32_32x32x16_bf16 v[128:143], v[192:195], v[200:203], v[128:143]\n\tds_read_b64_tr_b16 v[160:161], %[fa] offset:1576\n\t"
      "ds_read_b64_tr_b16 v[162:163], %[fa] offset:1568\n\tv_mfma_f32_32x32x16_bf16 v[144:159], v[192:195], v[204:207], v[144:159]\n\t"
      "ds_read_b128 v[168:171], %[ua] offset:560\n\tds_read_b128 v[172:175], %[ua] offset:3120\n\tv_mfma_f32_32x32x16_bf16 v[144:159], v[196:199], v[200:203], v[144:159]\n\t"
      "ds_read_b64_tr_b16 v[164:165], %[fa] offset:3624\n\tds_read_b64_tr_b16 v[166:167], %[fa] offset:3616\n\ts_waitcnt lgkmcnt(6)\n\t"
      "v_mfma_f32_32x32x16_bf16 v[128:143], v[208:211], v[216:219], v[128:143]\n\tds_read_b64_tr_b16 v[176:177], %[fa] offset:1544\n\t"
      "ds_read_b64_tr_b16 v[178:179], %[fa] offset:1536\n\tv_mfma_f32_32x32x16_bf16 v[144:159], v[208:211], v[220:223], v[144:159]\n\t"
      "ds_read_b128 v[184:187], %[ua] offset:592\n\tds_read_b128 v[188:191], %[ua] offset:3152\n\tv_mfma_f32_32x32x16_bf16 v[144:159], v[212:215], v[216:219], v[144:159]\n\t"
      "ds_read_b64_tr_b16 v[180:181], %[fa] offset:3592\n\tds_read_b64_tr_b16 v[182:183], %[fa] offset:3584\n\ts_waitcnt lgkmcnt(6)\n\t"
      "v_mfma_f32_32x32x16_bf16 v[128:143], v[160:163], v[168:171], v[128:143]\n\tds_read_b64_tr_b16 v[192:193], %[fa] offset:1640\n\t"
      "ds_read_b64_tr_b16 v[194:195], %[fa] offset:1632\n\tv_mfma_f32_32x32x16_bf16 v[144:159], v[160:163], v[172:175], v[144:159]\n\t"
      "ds_read_b128 v[200:203], %[ua] offset:480\n\tds_read_b128 v[204:207], %[ua] offset:3040\n\tv_mfma_f32_32x32x16_bf16 v[144:159], v[164:167], v[168:171], v[144:159]\n\t"
      "ds_read_b64_tr_b16 v[196:197], %[fa] offset:3688\n\tds_read_b64_tr_b16 v[198:199], %[fa] offset:3680\n\ts_waitcnt lgkmcnt(6)\n\t"
      "v_mfma_f32_32x32x16_bf16 v[128:143], v[176:179], v[184:187], v[128:143]\n\tds_read_b64_tr_b16 v[208:209], %[fa] offset:1608\n\t"
      "ds_read_b64_tr_b16 v[210:211], %[fa] offset:1600\n\tv_mfma_f32_32x32x16_bf16 v[144:159], v[176:179], v[188:191], v[144:159]\n\t"
      "ds_read_b128 v[216:219], %[ua] offset:512\n\tds_read_b128 v[220:223], %[ua] offset:3072\n\tv_mfma_f32_32x32x16_bf16 v[144:159], v[180:183], v[184:187], v[144:159]\n\t"
      "ds_read_b64_tr_b16 v[212:213], %[fa] offset:3656\n\tds_read_b64_tr_b16 v[214:215], %[fa] offset:3648\n\ts_waitcnt lgkmcnt(6)\n\t"
      "v_mfma_f32_32x32x16_bf16 v[128:143], v[192:195], v[200:203], v[128:143]\n\tds_read_b64_tr_b16 v[160:161], %[fa] offset:1704\n\t"
      "ds_read_b64_tr_b16 v[162:163], %[fa] offset:1696\n\tv_mfma_f32_32x32x16_bf16 v[144:159], v[192:195], v[204:207], v[144:159]\n\t"
      "ds_read_b128 v[168:171], %[ua] offset:400\n\tds_read_b128 v[172:175], %[ua] offset:2960\n\tv_mfma_f32_32x32x16_bf16 v[144:159], v[196:199], v[200:203], v[144:159]\n\t"
      "ds_read_b64_tr_b16 v[164:165], %[fa] offset:3752\n\tds_read_b64_tr_b16 v[166:167], %[fa] offset:3744\n\ts_waitcnt lgkmcnt(6)\n\t"
      "v_mfma_f32_32x32x16_bf16 v[128:143], v[208:211], v[216:219], v[128:143]\n\tds_read_b64_tr_b16 v[176:177], %[fa] offset:1672\n\t"
      "ds_read_b64_tr_b16 v[178:179], %[fa] offset:1664\n\tv_mfma_f32_32x32x16_bf16 v[144:159], v[208:211], v[220:223], v[144:159]\n\t"
      "ds_read_b128 v[184:187], %[ua] offset:432\n\tds_read_b128 v[188:191], %[ua] offset:2992\n\tv_mfma_f32_32x32x16_bf16 v[144:159], v[212:215], v[216:219], v[144:159]\n\t"
      "ds_read_b64_tr_b16 v[180:181], %[fa] offset:3720\n\tds_read_b64_tr_b16 v[182:183], %[fa] offset:3712\n\ts_waitcnt lgkmcnt(6)\n\t"
      "v_mfma_f32_32x32x16_bf16 v[128:143], v[160:163], v[168:171], v[128:143]\n\tds_read_b64_tr_b16 v[192:193], %[fa] offset:1768\n\t"
      "ds_read_b64_tr_b16 v[194:195], %[fa] offset:1760\n\tv_mfma_f32_32x32x16_bf16 v[144:159], v[160:163], v[172:175], v[144:159]\n\t"
      "ds_read_b128 v[200:203], %[ua] offset:320\n\tds_read_b128 v[204:207], %[ua] offset:2880\n\tv_mfma_f32_32x32x16_bf16 v[144:159], v[164:167], v[168:171], v[144:159]\n\t"
      "ds_read_b64_tr_b16 v[196:197], %[fa] offset:3816\n\tds_read_b64_tr_b16 v[198:199], %[fa] offset:3808\n\ts_waitcnt lgkmcnt(6)\n\t"
      "v_mfma_f32_32x32x16_bf16 v[128:143], v[176:179], v[184:187], v[128:143]\n\tds_read_b64_tr_b16 v[208:209], %[fa] offset:1736\n\t"
      "ds_read_b64_tr_b16 v[210:211], %[fa] offset:1728\n\tv_mfma_f32_32x32x16_bf16 v[144:159], v[176:179], v[188:191], v[144:159]\n\t"
      "ds_read_b128 v[216:219], %[ua] offset:352\n\tds_read_b128 v[220:223], %[ua] offset:2912\n\tv_mfma_f32_32x32x16_bf16 v[144:159], v[180:183], v[184:187], v[144:159]\n\t"
      "ds_read_b64_tr_b16 v[212:213], %[fa] offset:3784\n\tds_read_b64_tr_b16 v[214:215], %[fa] offset:3776\n\ts_waitcnt lgkmcnt(6)\n\t"
      "v_mfma_f32_32x32x16_bf16 v[128:143], v[192:195], v[200:203], v[128:143]\n\tds_read_b64_tr_b16 v[160:161], %[fa] offset:1832\n\t"
      "ds_read_b64_tr_b16 v[162:163], %[fa] offset:1824\n\tv_mfma_f32_32x32x16_bf16 v[144:159], v[192:195], v[204:207], v[144:159]\n\t"
      "ds_read_b128 v[168:171], %[ua] offset:240\n\tds_read_b128 v[172:175], %[ua] offset:2800\n\tv_mfma_f32_32x32x16_bf16 v[144:159], v[196:199], v[200:203], v[144:159]\n\t"
      "ds_read_b64_tr_b16 v[164:165], %[fa] offset:3880\n\tds_read_b64_tr_b16 v[166:167], %[fa] offset:3872\n\ts_waitcnt lgkmcnt(6)\n\t"
      "v_mfma_f32_32x32x16_bf16 v[128:143], v[208:211], v[216:219], v[128:143]\n\tds_read_b64_tr_b16 v[176:177], %[fa] offset:1800\n\t"
      "ds_read_b64_tr_b16 v[178:179], %[fa] offset:1792\n\tv_mfma_f32_32x32x16_bf16 v[144:159], v[208:211], v[220:223], v[144:159]\n\t"
      "ds_read_b128 v[184:187], %[ua] offset:272\n\tds_read_b128 v[188:191], %[ua] offset:2832\n\tv_mfma_f32_32x32x16_bf16 v[144:159], v[212:215], v[216:219], v[144:159]\n\t"
      "ds_read_b64_tr_b16 v[180:181], %[fa] offset:3848\n\tds_read_b64_tr_b16 v[182:183], %[fa] offset:3840\n\ts_waitcnt lgkmcnt(6)\n\t"
      "v_mfma_f32_32x32x16_bf16 v[128:143], v[160:163], v[168:171], v[128:143]\n\tds_read_b64_tr_b16 v[192:193], %[fa] offset:1896\n\t"
      "ds_read_b64_tr_b16 v[194:195], %[fa] offset:1888\n\tv_mfma_f32_32x32x16_bf16 v[144:159], v[160:163], v[172:175], v[144:159]\n\t"
      "ds_read_b128 v[200:203], %[ua] offset:160\n\tds_read_b128 v[204:207], %[ua] offset:2720\n\tv_mfma_f32_32x32x16_bf16 v[144:159], v[164:167], v[168:171], v[144:159]\n\t"
      "ds_read_b64_tr_b16 v[196:197], %[fa] offset:3944\n\tds_read_b64_tr_b16 v[198:199], %[fa] offset:3936\n\ts_waitcnt lgkmcnt(6)\n\t"
      "v_mfma_f32_32x32x16_bf16 v[128:143], v[176:179], v[184:187], v[128:143]\n\tds_read_b64_tr_b16 v[208:209], %[fa] offset:1864\n\t"
      "ds_read_b64_tr_b16 v[210:211], %[fa] offset:1856\n\tv_mfma_f32_32x32x16_bf16 v[144:159], v[176:179], v[188:191], v[144:159]\n\t"
      "ds_read_b128 v[216:219], %[ua] offset:192\n\tds_read_b128 v[220:223], %[ua] offset:2752\n\tv_mfma_f32_32x32x16_bf16 v[144:159], v[180:183], v[184:187], v[144:159]\n\t"
      "ds_read_b64_tr_b16 v[212:213], %[fa] offset:3912\n\tds_read_b64_tr_b16 v[214:215], %[fa] offset:3904\n\ts_waitcnt lgkmcnt(6)\n\t"
      "v_mfma_f32_32x32x16_bf16 v[128:143], v[192:195], v[200:203], v[128:143]\n\tds_read_b64_tr_b16 v[160:161], %[fa] offset:1960\n\t"
      "ds_read_b64_tr_b16 v[162:163], %[fa] offset:1952\n\tv_mfma_f32_32x32x16_bf16 v[144:159], v[192:195], v[204:207], v[144:159]\n\t"
      "ds_read_b128 v[168:171], %[ua] offset:80\n\tds_read_b128 v[172:175], %[ua] offset:2640\n\tv_mfma_f32_32x32x16_bf16 v[144:159], v[196:199], v[200:203], v[144:159]\n\t"
      "ds_read_b64_tr_b16 v[164:165], %[fa] offset:4008\n\tds_read_b64_tr_b16 v[166:167], %[fa] offset:4000\n\ts_waitcnt lgkmcnt(6)\n\t"
      "v_mfma_f32_32x32x16_bf16 v[128:143], v[208:211], v[216:219], v[128:143]\n\tds_read_b64_tr_b16 v[176:177], %[fa] offset:1928\n\t"
      "ds_read_b64_tr_b16 v[178:179], %[fa] offset:1920\n\tv_mfma_f32_32x32x16_bf16 v[144:159], v[208:211], v[220:223], v[144:159]\n\t"
      "ds_read_b128 v[184:187], %[ua] offset:112\n\tds_read_b128 v[188:191], %[ua] offset:2672\n\tv_mfma_f32_32x32x16_bf16 v[144:159], v[212:215], v[216:219], v[144:159]\n\t"
      "ds_read_b64_tr_b16 v[180:181], %[fa] offset:3976\n\tds_read_b64_tr_b16 v[182:183], %[fa] offset:3968\n\ts_waitcnt lgkmcnt(6)\n\t"
      "v_mfma_f32_32x32x16_bf16 v[128:143], v[160:163], v[168:171], v[128:143]\n\tds_read_b64_tr_b16 v[192:193], %[fa] offset:2024\n\t"
      "ds_read_b64_tr_b16 v[194:195], %[fa] offset:2016\n\tv_mfma_f32_32x32x16_bf16 v[144:159], v[160:163], v[172:175], v[144:159]\n\tds_read_b128 v[200:203], %[ua] offset:0\n\t"
      "ds_read_b128 v[204:207], %[ua] offset:2560\n\tv_mfma_f32_32x32x16_bf16 v[144:159], v[164:167], v[168:171], v[144:159]\n\t"
      "ds_read_b64_tr_b16 v[196:197], %[fa] offset:4072\n\tds_read_b64_tr_b16 v[198:199], %[fa] offset:4064\n\ts_waitcnt lgkmcnt(6)\n\t"
      "v_mfma_f32_32x32x16_bf16 v[128:143], v[176:179], v[184:187], v[128:143]\n\tds_read_b64_tr_b16 v[208:209], %[fa] offset:1992\n\t"
      "ds_read_b64_tr_b16 v[210:211], %[fa] offset:1984\n\tv_mfma_f32_32x32x16_bf16 v[144:159], v[176:179], v[188:191], v[144:159]\n\t"
      "ds_read_b128 v[216:219], %[ua] offset:32\n\tds_read_b128 v[220:223], %[ua] offset:2592\n\tv_mfma_f32_32x32x16_bf16 v[144:159], v[180:183], v[184:187], v[144:159]\n\t"
      "ds_read_b64_tr_b16 v[212:213], %[fa] offset:4040\n\tds_read_b64_tr_b16 v[214:215], %[fa] offset:4032\n\ts_waitcnt lgkmcnt(6)\n\t"
      "v_mfma_f32_32x32x16_bf16 v[128:143], v[192:195], v[200:203], v[128:143]\n\tv_mfma_f32_32x32x16_bf16 v[144:159], v[192:195], v[204:207], v[144:159]\n\t"
      "v_mfma_f32_32x32x16_bf16 v[144:159], v[196:199], v[200:203], v[144:159]\n\ts_waitcnt lgkmcnt(0)\n\t"
      "v_mfma_f32_32x32x16_bf16 v[128:143], v[208:211], v[216:219], v[128:143]\n\tv_mfma_f32_32x32x16_bf16 v[144:159], v[208:211], v[220:223], v[144:159]\n\t"
      "v_mfma_f32_32x32x16_bf16 v[144:159], v[212:215], v[216:219], v[144:159]\n\ts_nop 11\n\t"
      : "=&{v[128:143]}"(acc[0]), "=&{v[144:159]}"(acc[1]), "=&{v[160:191]}"(x0), "=&{v[192:223]}"(x1)
      : [fa] "v"(FC + (4 * w + ((lane >> 2) & 3)) * CP + 24 + (lane & 16) * 2 + (lane & 3) * 8 - h2 * 16), [ua] "v"(ub + (r + 1) * 80 + h2 * 16) : "memory");
    const float dv = k ? bhi(dd) : blo(dd);
#pragma unroll
    for (int i = 0; i < 8; ++i) {
      LAS v2u* at = (LAS v2u*)(c.L + ub + (32 + (i >> 2) * 32 + r) * 80 + (i & 3) * 16 + h2 * 8);
      const v2u uu = *at;
      const f32x16& a = acc[i >> 2];
      const int m = 4 * (i & 3);
      const f32x2 y0 = rb2(f32x2{a[m], a[m + 1]}) + rb2(f32x2{blo(uu.x), bhi(uu.x)} * dv), y1 = rb2(f32x2{a[m + 2], a[m + 3]}) + rb2(f32x2{blo(uu.y), bhi(uu.y)} * dv);
      *at = v2u{pk(y0.x, y0.y), pk(y1.x, y1.y)};
    }
    LGKM(0);
#pragma unroll
    for (int i = 0; i < 4; ++i) st16s(c.rs, i * 1024 + lane * 16, R_YT + amap((d0 + k) * 4096, far), lds16(c.L, ub + (32 + i * 16 + (lane >> 2)) * 80 + (lane & 3) * 16));
  }
  STAMP(6);
  {
    const rsrc_t ro = mkrs(p->out, 2048 * 2048 * 2);
    v4u yv[8];
    asm volatile("" :: "v"(warm));
    group_wait(1, 8);
#pragma unroll
    for (int ch = 0; ch < 8; ++ch) yv[ch] = ld16s(c.rs, seq_off(4 * cg + w, lane, va), R_YT + amap(ch * 4096, 0));
    const int at = FC + 4 * w * CP;
#pragma unroll
    for (int ch = 0; ch < 8; ++ch) sts16(c.L, at + ch * 1152 + (lane & 7) * 16 + (lane >> 3) * 144, yv[ch]);
    LGKM(0);
#pragma unroll
    for (int ch = 0; ch < 8; ++ch) yv[ch] = lds16(c.L, at + ch * 1152 + (lane >> 3) * 16 + (lane & 7) * 144);
#pragma unroll
    for (int i = 0; i < 8; ++i) {
      float o[8];
#pragma unroll
      for (int ch = 0; ch < 8; ++ch) o[ch] = ((ch & 1) ? bhi(qr[i][ch >> 1]) : blo(qr[i][ch >> 1])) * ((i & 1) ? bhi(yv[ch][i >> 1]) : blo(yv[ch][i >> 1]));
      __builtin_amdgcn_raw_buffer_store_b128(v4u{pk(o[0], o[1]), pk(o[2], o[3]), pk(o[4], o[5]), pk(o[6], o[7])}, ro, (t0 + i) * 4096 + (1024 + de) * 2, 0, BAUX);
    }
  }
  if (c.budget == 0u) __hip_atomic_store(c.ctrl + C_TMO / 4, 1u, RLX_AGENT);
}

extern "C" __global__ void __launch_bounds__(256, 1) mk_mega(Params p) {
  extern __shared__ __attribute__((aligned(16))) unsigned char lds_raw[];
  Ctx c;
  c.ctrl = (gu32*)p.ctrl;
  c.L = (lds_t)lds_raw;
  c.w = __builtin_amdgcn_readfirstlane(threadIdx.x >> 6);
  c.lane = threadIdx.x & 63;
  c.b = (int)(blockIdx.x & 255u);
  c.att = (int)(blockIdx.x >> 8);
  c.ask = 0;
  if (c.w == 3) {
    unsigned long long pc; unsigned len;
    asm volatile("s_getpc_b64 %0\n1:\n\ts_mov_b32 %1, 0\n\ts_max_i32 %1, %1, .Lmk_end0-1b\n\ts_max_i32 %1, %1, .Lmk_end1-1b\n\ts_max_i32 %1, %1, .Lmk_end2-1b\n\ts_max_i32 %1, %1, .Lmk_end3-1b\n\t"
                 "s_max_i32 %1, %1, .Lmk_end4-1b\n\ts_max_i32 %1, %1, .Lmk_end5-1b\n\ts_max_i32 %1, %1, .Lmk_end6-1b" : "=s"(pc), "=s"(len) :: "scc");
    const unsigned t = __builtin_amdgcn_raw_buffer_load_b32(mkrs((const void*)pc, len), ((c.b >> 3) + 32 * c.lane) * 128, 0, 0);
    asm volatile("" :: "v"(t));
  }
  c.slot = (c.att ? (c.b & 7) : (int)((unsigned)__builtin_amdgcn_s_getreg(20 | (3 << 11)) & 7u)) * 32 + (c.b >> 3);
  if (c.b == SERVER && c.w == 3) serve(c);
  const int x = c.slot >> 5, t = c.slot & 31;
  if (x < 4) {
#define QKM 0
    const int qk = QKM ? (QKM >> x) & 1 : t < 8, h = QKM ? __builtin_popcount((qk ? QKM : ~QKM) & ((1 << x) - 1)) * 16 + t : x * 8 + (t & 7);
    if (t < 16 && qk) qk_role(c, h);
    else if (t < 16) vo_role(c, h);
    else y_role(c, (t & 12) * 4 + x * 4 + (t & 3));
  } else {
    if (t < 24) g_role(c, t >> 3, (x - 4) * 8 + (t & 7));
    else zc_role(c, (x - 4) * 8 + t - 24);
    STAMP(2);
    conv_role(c, (x - 4) * 32 + t);
  }
  STAMP(3);
  STAMP(7);
}

}

extern "C" void kernel_launch(void* const* d_in, const int* in_sizes, int n_in, void* d_out, int out_size, void* d_ws, size_t ws_size,
                              hipStream_t stream) {
  using namespace mk;
  (void)in_sizes; (void)n_in; (void)out_size; (void)ws_size;
  Params p;
  p.x = (const u16*)d_in[0]; p.mem0 = (const u16*)d_in[1];
  p.Wq = (const u16*)d_in[2]; p.Wk = (const u16*)d_in[3]; p.Wv = (const u16*)d_in[4]; p.Wo = (const u16*)d_in[5];
  p.bq = (const u16*)d_in[7]; p.bk = (const u16*)d_in[8]; p.bv = (const u16*)d_in[9];
  p.shift = (const u16*)d_in[10]; p.ssmk = (const u16*)d_in[11]; p.ssmD = (const u16*)d_in[12];
  p.out = (u16*)d_out;
  p.ctrl = (unsigned char*)d_ws;
  p.data = (unsigned char*)(((size_t)d_ws + ws_size - R_END - MB) & ~(size_t)((2u << 20) - 1));
  (void)hipFuncSetAttribute((const void*)mk_mega, hipFuncAttributeMaxDynamicSharedMemorySize, LDS_BYTES);
  hipLaunchKernelGGL(mk_mega, dim3(256), dim3(256), LDS_BYTES, stream, p);
}
```

```cpp
#include <hip/hip_runtime.h>
#include <hip/hip_bf16.h>

namespace mk {

#define GAS __attribute__((address_space(1)))
#define LAS __attribute__((address_space(3)))
#define DEV __device__ __forceinline__
#define RLX_AGENT __ATOMIC_RELAXED, __HIP_MEMORY_SCOPE_AGENT
typedef unsigned short u16;
typedef unsigned v2u __attribute__((ext_vector_type(2)));
typedef unsigned v4u __attribute__((ext_vector_type(4)));
typedef unsigned v8u __attribute__((ext_vector_type(8)));
typedef unsigned v16u __attribute__((ext_vector_type(16)));
typedef unsigned v32u __attribute__((ext_vector_type(32)));
typedef float f32x2 __attribute__((ext_vector_type(2)));
typedef float f32x4 __attribute__((ext_vector_type(4)));
typedef float f32x16 __attribute__((ext_vector_type(16)));
typedef __bf16 bf16x2 __attribute__((ext_vector_type(2)));
typedef __bf16 bf16x8 __attribute__((ext_vector_type(8)));
typedef __bf16 bf16x16 __attribute__((ext_vector_type(16)));
typedef GAS unsigned gu32;
typedef __amdgpu_buffer_rsrc_t rsrc_t;
typedef LAS unsigned char* lds_t;

constexpr int NSEG = 64, SERVER = 130;
constexpr unsigned SENT = 0xFFFFFFFFu;
constexpr int AUX_SC1 = 16, AUX_NT = 2;
constexpr int T_LAG = 61;
constexpr int G_AHEAD = 4;
#define SPINS 21
constexpr unsigned SPIN_CAP = 1u << SPINS;
constexpr int LDS_BYTES = 163840;

constexpr int C_CLAIM = 1024, C_VERDICT = 3072, C_TMO = 3328, C_CONV = 4096, C_ASK = C_CONV + 32 * 128;
static_assert(C_TMO == 3328, "known to the generated code");
constexpr int C_ECHO = C_ASK + 256 * 128, CTRL_BYTES = C_ECHO + 256 * 128;
constexpr int MB = 1 << 20;
constexpr int R_U = 0;
constexpr int R_MEM = 2 * MB;
constexpr int R_FQ = R_MEM + 393216, R_FK = R_FQ + 4096, R_FV = R_FQ + 8192, R_FP = R_FV + 4096, R_FZ = R_MEM + 131072;
constexpr int R_PT = 3 * MB;
#define ZRN 14
#define GRN 16
constexpr int R_QX = 4 * MB, R_KX = R_QX + GRN * MB / 2, R_VX = R_QX + GRN * MB, R_Z = R_QX + GRN * MB * 3 / 2, R_END = R_Z + ZRN * 4 * MB;
constexpr int R_KVT = R_Z, R_YT = R_Z + 8 * MB;
#define QKFAR 8192
#define HMAP 1
DEV int head(int h) { return HMAP == 0 ? h : HMAP == 1 ? 4 * (h & 7) + (h >> 3) : (h & 1) + 2 * (h >> 3) + 8 * ((h & 7) >> 1); }
DEV int hblock(int h, int flip) { return h * GRN * 16384 + ((((h * GRN) << 6) ^ flip) & 8192); }

struct Params {
  const u16 *x, *mem0, *Wq, *Wk, *Wv, *Wo, *bq, *bk, *bv, *shift, *ssmk, *ssmD;
  u16* out;
  unsigned char* ctrl;
  unsigned char* data;
};

typedef const __attribute__((address_space(4))) Params* KArg;
DEV KArg args() {
  KArg a = (KArg)__builtin_amdgcn_kernarg_segment_ptr(); asm volatile("" : "+s"(a)); return a;
}
DEV rsrc_t mkrs(const void* p, unsigned bytes) { return __builtin_amdgcn_make_buffer_rsrc((void*)p, 0, bytes, 0x00020000); }
DEV v4u ld16s(rsrc_t rs, int v, int s) { return __builtin_amdgcn_raw_buffer_load_b128(rs, v, s, AUX_SC1); }
DEV v2u ld8s(rsrc_t rs, int v, int s) { return __builtin_amdgcn_raw_buffer_load_b64(rs, v, s, AUX_SC1); }
DEV v4u ld16(rsrc_t rs, int v, int s) { return __builtin_amdgcn_raw_buffer_load_b128(rs, v, s, 0); }
DEV v4u ld16n(rsrc_t rs, int v, int s) { return __builtin_amdgcn_raw_buffer_load_b128(rs, v, s, AUX_NT); }
DEV v2u ld8n(rsrc_t rs, int v, int s) { return __builtin_amdgcn_raw_buffer_load_b64(rs, v, s, AUX_NT); }
DEV v2u ld8(rsrc_t rs, int v, int s) { return __builtin_amdgcn_raw_buffer_load_b64(rs, v, s, 0); }
DEV void st16s(rsrc_t rs, int v, int s, v4u d) { __builtin_amdgcn_raw_buffer_store_b128(d, rs, v, s, AUX_SC1); }
DEV void st8s(rsrc_t rs, int v, int s, v2u d) { __builtin_amdgcn_raw_buffer_store_b64(d, rs, v, s, AUX_SC1); }
DEV void st4s(rsrc_t rs, int v, int s, unsigned d) { __builtin_amdgcn_raw_buffer_store_b32(d, rs, v, s, AUX_SC1); }
DEV int amap(int o, int par) { return ((o >> 13) << 14) | (par << 13) | (o & 8191); }
DEV unsigned umax(unsigned a, unsigned b) { return a > b ? a : b; }
DEV unsigned top(v4u v) { return umax(umax(v.x, v.y), umax(v.z, v.w)); }
DEV unsigned pk(float a, float b) { bf16x2 v = {(__bf16)a, (__bf16)b}; return __builtin_bit_cast(unsigned, v); }
DEV unsigned pks(float a, float b) { unsigned u = pk(a, b); return u < SENT - 1u ? u : SENT - 1u; }
DEV float blo(unsigned u) { return __builtin_bit_cast(float, u << 16); }
DEV float bhi(unsigned u) { return __builtin_bit_cast(float, u & 0xFFFF0000u); }
DEV float rb(float a) { return blo(pk(a, a)); }
DEV f32x2 rb2(f32x2 a) { const unsigned u = pk(a.x, a.y); return f32x2{blo(u), bhi(u)}; }
DEV unsigned fu(float f) { return __builtin_bit_cast(unsigned, f); }
DEV float uf(unsigned u) { return __builtin_bit_cast(float, u); }
DEV f32x4 mfma16(v4u a, v4u b, f32x4 c) {
  return __builtin_amdgcn_mfma_f32_16x16x32_bf16(__builtin_bit_cast(bf16x8, a), __builtin_bit_cast(bf16x8, b), c, 0, 0, 0);
}
DEV f32x16 mfma32(v4u a, v4u b, f32x16 c) {
  return __builtin_amdgcn_mfma_f32_32x32x16_bf16(__builtin_bit_cast(bf16x8, a), __builtin_bit_cast(bf16x8, b), c, 0, 0, 0);
}
DEV f32x4 smf(v4u a, v8u b, f32x4 c, int idx) {
  return __builtin_amdgcn_smfmac_f32_16x16x64_bf16(__builtin_bit_cast(bf16x8, a), __builtin_bit_cast(bf16x16, b), c, idx, 0, 0);
}
DEV v8u cat(v4u a, v4u b) { return __builtin_shufflevector(a, b, 0, 1, 2, 3, 4, 5, 6, 7); }
DEV v4u lo4(v8u a) { return __builtin_shufflevector(a, a, 0, 1, 2, 3); }
DEV v4u hi4(v8u a) { return __builtin_shufflevector(a, a, 4, 5, 6, 7); }
DEV v4u lds16(lds_t L, int o) { return *(LAS v4u*)(L + o); }
DEV void sts16(lds_t L, int o, v4u v) { *(LAS v4u*)(L + o) = v; }
DEV void sts8(lds_t L, int o, v2u v) { *(LAS v2u*)(L + o) = v; }
#define SCHED() __builtin_amdgcn_sched_barrier(0)
#define PIN_A(x) asm volatile("" : "+a"(x))
#define PIN_V(x) asm volatile("" : "+v"(x))
DEV int opq(int v) { asm volatile("" : "+v"(v)); return v; }
DEV int fresh_lane() {
  int l; asm volatile("v_mbcnt_lo_u32_b32 %0, -1, 0\n\tv_mbcnt_hi_u32_b32 %0, -1, %0" : "=v"(l)); return l;
}
DEV f32x4 fresh_zero() {
  f32x2 a, b;
  asm volatile("v_mov_b64 %0, 0" : "=v"(a));
  asm volatile("v_mov_b64 %0, 0" : "=v"(b));
  return __builtin_shufflevector(a, b, 0, 1, 2, 3);
}
DEV unsigned fresh_sent() { unsigned z; asm volatile("v_mov_b32 %0, -1" : "=v"(z)); return z; }
#define SMF "v_smfmac_f32_16x16x64_bf16 "
#define MF16 "v_mfma_f32_16x16x32_bf16 "
#define SMF1(cls, d, a, b) asm volatile(SMF "%0, %1, %2, %3" : "+v"(d) : "v"(a), cls(b), "v"(idx))
#define SMF1F(cls, lo, hi, d, a, b) asm volatile(SMF "v[" #lo ":" #hi "], %1, %2, %3" : "+{v[" #lo ":" #hi "]}"(d) : "v"(a), cls(b), "v"(idx))
#define AT_HOME(M, j) do { const int j_ = (j); if (j_ == 0) M("{v[32:39]}"); if (j_ == 1) M("{v[40:47]}"); if (j_ == 2) M("{v[48:55]}"); if (j_ == 3) M("{v[56:63]}"); if (j_ == 4) M("{v[64:71]}"); if (j_ == 5) M("{v[72:79]}"); if (j_ == 6) M("{v[80:87]}"); if (j_ == 7) M("{v[88:95]}"); if (j_ == 8) M("{v[96:103]}"); if (j_ == 9) M("{v[104:111]}"); if (j_ == 10) M("{v[112:119]}"); if (j_ == 11) M("{v[120:127]}"); if (j_ == 12) M("{v[128:135]}"); if (j_ == 13) M("{v[136:143]}"); if (j_ == 14) M("{v[144:151]}"); if (j_ == 15) M("{v[152:159]}"); } while (0)
#define MF1(d, a, b) asm volatile(MF16 "%0, %1, %2, %0" : "+v"(d) : "v"(a), "v"(b))
#define MF1Z(d, a, b) asm volatile(MF16 "%0, %1, %2, 0" : "=&v"(d) : "v"(a), "v"(b))
#define LGKM(n) asm volatile("s_waitcnt lgkmcnt(" #n ")" ::: "memory")
#define RAWBAR() do { __builtin_amdgcn_s_barrier(); asm volatile("" ::: "memory"); } while (0)
DEV void settle3(f32x4& s0, f32x4& s1, f32x4& s2) { asm volatile("s_nop 7" : "+v"(s0), "+v"(s1), "+v"(s2)); }
DEV void settle4(f32x4 (&d)[4]) { asm volatile("s_nop 7" : "+v"(d[0]), "+v"(d[1]), "+v"(d[2]), "+v"(d[3])); }
#define BAR() do { asm volatile("s_waitcnt lgkmcnt(0)" ::: "memory"); __builtin_amdgcn_s_barrier(); asm volatile("" ::: "memory"); } while (0)
#define VMWAIT(n) asm volatile("s_waitcnt vmcnt(" #n ")" ::: "memory")
DEV float add1(float a, float b) {
  float o; asm("v_add_f32 %0, %1, %2" : "=v"(o) : "v"(a), "v"(b)); return o;
}
DEV float xmax(float v) {
  auto r = __builtin_amdgcn_permlane32_swap(fu(v), fu(v), false, false); v = fmaxf(uf(r[0]), uf(r[1]));
  auto q = __builtin_amdgcn_permlane16_swap(fu(v), fu(v), false, false); return fmaxf(uf(q[0]), uf(q[1]));
}
DEV float xsum(float v) {
  auto r = __builtin_amdgcn_permlane32_swap(fu(v), fu(v), false, false); v = uf(r[0]) + uf(r[1]);
  auto q = __builtin_amdgcn_permlane16_swap(fu(v), fu(v), false, false); return uf(q[0]) + uf(q[1]);
}

DEV float ror8(float v) { return uf(__builtin_amdgcn_update_dpp(0u, fu(v), 0x128, 0xf, 0xf, true)); }
DEV float fmax2(float a, float b) { return __builtin_amdgcn_fmed3f(a, b, __builtin_inff()); }
DEV float fmax3(float a, float b, float c) { float r; asm("v_max3_f32 %0, %1, %2, %3" : "=v"(r) : "v"(a), "v"(b), "v"(c)); return r; }
DEV bool allok(unsigned t) { return __all(t != SENT); }

template <int N> struct IC { static constexpr int value = N; };
struct Ctx {
  gu32* ctrl;
  rsrc_t rs;
  lds_t L;
  int w, lane;
  int b, slot, att;
  unsigned ask;
  unsigned budget;
};
DEV KArg enter(Ctx& c) {
  const KArg a = args();
  c.ctrl = (gu32*)a->ctrl;
  c.rs = mkrs(a->data, R_END);
  asm volatile("" : "+s"(c.w), "+s"(c.b), "+s"(c.att), "+s"(c.slot));
  return a;
}
DEV bool spend(Ctx& c) {
  if (c.budget == 0u) return true;
  c.budget -= 1u;
  return false;
}
DEV void claim(Ctx& c, unsigned votes) {
  if (c.w != 0) return;
  if (c.att == 0) {
#pragma unroll 1
    while (__builtin_amdgcn_readfirstlane(__hip_atomic_load(c.ctrl + (C_ECHO + c.b * 128) / 4, RLX_AGENT)) != c.ask) {
      if (spend(c)) break;
      __builtin_amdgcn_s_sleep(2);
    }
  }
  if (fresh_lane() == 0) __hip_atomic_fetch_add(c.ctrl + (C_CLAIM + c.att * 1024) / 4 + c.slot, 1u + votes, RLX_AGENT);
}
DEV unsigned wait_verdict(Ctx& c) {
  unsigned v;
#pragma unroll 1
  for (;;) {
    v = __builtin_amdgcn_readfirstlane(__hip_atomic_load(c.ctrl + (C_VERDICT + c.att * 128) / 4, RLX_AGENT));
    if ((v & 1u) || spend(c)) break;
    __builtin_amdgcn_s_sleep(2);
  }
  __builtin_amdgcn_fence(__ATOMIC_ACQUIRE, "wavefront");
  if ((v & 3u) == 3u && c.att == 0) {
    asm volatile("s_waitcnt vmcnt(0) lgkmcnt(0)\n\ts_barrier\n\ts_mov_b64 s[0:1], %0\n\ts_mov_b32 s2, %1\n\tv_mov_b32 v0, %2\n\t"
                 "s_getpc_b64 s[4:5]\n1:\n\ts_sub_u32 s4, s4, 1b-mk_mega\n\ts_subb_u32 s5, s5, 0\n\ts_setpc_b64 s[4:5]"
                 :: "s"(__builtin_amdgcn_kernarg_segment_ptr()), "s"(c.b + 256), "v"(c.w * 64 + fresh_lane()) : "s0", "s1", "s2", "s4", "s5", "v0", "scc", "memory");
    __builtin_unreachable();
  }
  return v;
}
DEV void serve(Ctx& c) {
  c.budget = SPIN_CAP;
  const int lane = fresh_lane();
  const rsrc_t rc = mkrs((const void*)c.ctrl, CTRL_BYTES);
  if (c.att == 0) {
#pragma unroll
    for (int i = C_CLAIM / 1024; i < C_ASK / 1024; ++i) st16s(rc, lane * 16, i * 1024, v4u{0, 0, 0, 0});
    VMWAIT(0);
  }
  v4u said = {0, 0, 0, 0};
  bool first = true;
#pragma unroll 1
  for (;;) {
    asm volatile("" ::: "memory");
    const v4u cl = ld16s(rc, lane * 16, C_CLAIM + c.att * 1024);
    if (c.att == 0) {
      v4u e;
#pragma unroll
      for (int i = 0; i < 4; ++i) e[i] = __builtin_amdgcn_raw_buffer_load_b32(rc, lane * 128, C_ASK + i * 8192, AUX_SC1);
#pragma unroll
      for (int i = 0; i < 4; ++i) st4s(rc, (first || e[i] != said[i]) ? lane * 128 : 1 << 30, C_ECHO + i * 8192, e[i]);
      said = e; first = false;
    }
    unsigned n = 0, one = 1, va = 0, vb = 0;
#pragma unroll
    for (int i = 0; i < 4; ++i) { n += cl[i] & 0xFFFFu; one &= (cl[i] & 0xFFFFu) == 1u; va += (cl[i] >> 16) & 1u; vb += (cl[i] >> 24) & 1u; }
    unsigned sum = n | (va << 12) | (vb << 22);
#pragma unroll
    for (int o = 1; o < 64; o <<= 1) sum += (unsigned)__shfl_xor((int)sum, o);
    sum = __builtin_amdgcn_readfirstlane(sum);
    if ((sum & 0xFFFu) >= 256u) {
      int da = (int)((sum >> 12) & 0x3FFu) - 32, db = (int)(sum >> 22) - 32;
      if (abs(da) < 12 || abs(db) < 12) { da -= db; db = -da; }
      const unsigned v = 1u | (__all(one) ? 0u : 2u) | (da > 0 ? 4u : 0u) | (db > 0 ? 8u : 0u);
      if (lane == 0) __hip_atomic_store(c.ctrl + (C_VERDICT + c.att * 128) / 4, v, RLX_AGENT);
      break;
    }
    if (spend(c)) break;
  }
}
DEV int vote_a(unsigned v) { return (int)((v >> 2) & 1u); }
DEV int vote_b(unsigned v) { return (int)((v >> 3) & 1u); }

#define STAMP(slot) do {} while (0)
#define TICK_DECL
#define TICK(i) do {} while (0)
#define TICK_SUM(n) do {} while (0)
#define TICK_OUT() do {} while (0)
#define TKS(x) ""
#define TK_CLOB
#define TK0 TKS("s_memtime s[74:75]\n\t")
#define TK1 TKS("s_memtime s[76:77]\n\t")
#define TK2 TKS("s_memtime s[78:79]\n\t")
#define TK3 TKS("s_memtime s[80:81]\n\t")
#define TK4 TKS("s_memtime s[82:83]\n\t")
#define TK5 TKS("s_memtime s[84:85]\n\t")
#define TK6 TKS("s_memtime s[86:87]\n\t")
#define TK7 TKS("s_memtime s[88:89]\n\t")
#define DIAG(slot, v) do {} while (0)
#define STS(x) ""
#define STC(x) ""
#define STF(x) ""

DEV constexpr int krow(int rho8, int i) { return 32 * (rho8 >> 2) + 8 * (rho8 & 1) + 2 * ((rho8 >> 1) & 1) + i; }
DEV int krow_lane(int g) { return 16 * (g >> 1) + 4 * (g & 1); }
DEV unsigned prm(unsigned hi, unsigned lo, int odd) { return __builtin_amdgcn_perm(hi, lo, odd ? 0x07060302u : 0x05040100u); }

#define ISBAD "v_cmp_eq_u32 vcc, -1, %[t]\n\t"
#define SPEND(lbl) "s_cmp_eq_u32 %[bd], 0\n\ts_cbranch_scc1 " lbl "\n\ts_sub_u32 %[bd], %[bd], 1\n\t"
#define KNOB(i, d) ((unsigned)(d))
#define CQ0 300
#define CV0 200
#define D1 30
#define D2 105
#define C1 KNOB(0, D1)
#define C2 KNOB(1, D2)
#define SKEW ((c.slot & 32) ? KNOB(2, 0) : 0u)
#define KLT KNOB(4, 28)
#define KLP KNOB(3, 90)
#define KH KNOB(5, 92)
#define ZDLY KNOB(7, 2)
#define ZRING 63
#define HACK KNOB(6, 0)
#define NLK 1
#define BAUX 0
#define NPROBE 4
#define HAUX 0
#define CWARM 0
#define KV_EARLY 0
#define XWARM 1
#define CAUX AUX_NT
#define L0 5u
#define LA 11u
#define ZWAIT 60
static_assert(L0 + LA <= GRN, "ring");
#define GATE 0
#define GATED 0
#define TLBW 1
#define DQ 500
#define DK 200
#define FAHEAD 1u
#define PACE 0, 0, C2, KLT, KLP, 0
DEV unsigned uni(unsigned v) { return __builtin_amdgcn_readfirstlane(v); }
#define ALL_REGS "=&{a[0:31]}"(wa[0]), "=&{a[32:63]}"(wa[1]), "=&{a[64:95]}"(wa[2]), "=&{a[96:127]}"(wa[3]), "=&{a[128:159]}"(wa[4]), "=&{a[160:191]}"(wa[5]), "=&{a[192:223]}"(wa[6]), \
    "=&{a[224:255]}"(wa[7]), "=&{v[32:63]}"(wv[0]), "=&{v[64:95]}"(wv[1]), "=&{v[96:127]}"(wv[2]), "=&{v[128:159]}"(wv[3]), "=&{v[224:255]}"(wv[4])
#define S_CLOB "s8", "s9", "s10", "s11", "s12", "s13", "s14", "s15", "s16", "s17", "s18", "s19", "s20", "s21", "s22", "s23", "s24", "s25", "s26", "s27", "s28", "s29", "s30", "s31", "s33", "s34", "s35", "s36", "s37", "s38", "s39", "s40", "s41", "s42", "s43", "s44", "s45", "s46", "s47", "s48", "s49", "s50", "s51", "s52", "s53", "s54", "s55", "s56", "s57", "s58", "s59", "s60", "s61", "s62", "s63", "s64", "s65", "s66", "s67", "s68", "s69", "s70", "s71", "s72", "s73"
#define ROFF 0
#define ROTS(x) (unsigned)((((x) + ROFF) & 3) << 19), (unsigned)((((x) + ROFF + 1) & 3) << 19), (unsigned)((((x) + ROFF + 2) & 3) << 19), (unsigned)((((x) + ROFF + 3) & 3) << 19)
#define HELLO c.w ? 0u : c.att ? 2u : c.b == SERVER ? 3u : 1u, (unsigned)(C_ECHO + c.b * 128), (unsigned)opq(C_CLAIM + c.att * 1024 + c.slot * 4)
#define ENTRY plo((const void*)p), phi((const void*)p), c.att ? 0u : (unsigned)c.b + 256u, (unsigned)(C_VERDICT + c.att * 128)
#define RSRCS plo(p->data), phi(p->data), (unsigned)opq(R_END), 0x00020000, plo(p->ctrl), phi(p->ctrl), CTRL_BYTES, 0x00020000
DEV unsigned plo(const void* q) { return (unsigned)(size_t)q; }
DEV unsigned phi(const void* q) { return (unsigned)((size_t)q >> 32); }

DEV constexpr int qk_r(int j) { return j < 4 ? 0 : (j - 4) % 3 + 1; }
DEV constexpr int qk_i(int j) { return j < 4 ? j : (j - 4) / 3; }
DEV void qk_role(Ctx& c, int h) {
  const KArg p = enter(c);
  constexpr int WL = 0, MEMS = 131072, KXL = 147456, QKX = 155648, QROW = 272, CTL = 160000;
  c.budget = SPIN_CAP;
  const int w = c.w, lane = fresh_lane(), g = lane >> 4, cc = lane & 15, q = (lane >> 2) & 3, pp = lane & 3;
  const int prow = (4 * (g & 1) + (g & 2) + (cc & 1)) * 2 + ((cc >> 1) & 1);
  v32u k;
#pragma unroll
  for (int i = 0; i < 32; ++i) k[i] = 0;
  k[0] = amap(w * 4096 + lane * 16, 0);
  k[1] = MEMS + w * 4096 + lane * 16;
#pragma unroll
  for (int r = 0; r < 3; ++r) k[2 + r] = MEMS + ((w + r + 1) & 3) * 4096 + lane * 16;
  k[5] = WL + w * 32768 + lane * 16;
  k[6] = (lane & 1) ? 0xEEEE : 0x4444;
  k[7] = w < 2 ? QKX + (4 * w + (cc >> 2)) * 256 + g * 64 + (cc & 3) * 4 : QKX + (8 + 2 * g) * QROW + (64 * (w & 1) + 4 * cc) * 2;
  k[8] = QKX + g * 16 + (8 + (cc & 7)) * QROW;
  k[9] = KXL + g * 512 + cc * 16;
  k[10] = w == 0 ? amap((h >> 1) * 1024 + ((2 * (h & 1) + (cc >> 3)) * 16 + (prow >> 1) + 8 * (prow & 1)) * 16 + ((cc >> 2) & 1) * 8, 0)
        : w == 1 ? amap(h * 512 + (g & 1) * 256 + cc * 16 + (g >> 1) * 8, 0) : w == 2 ? amap(16384 + h * 1024 + lane * 16, 0) : lane * 16;
  k[11] = w == 0 ? (cc < 8 ? amap(16384 + h * 128 + prow * 8 + (cc >> 2) * 4, 0) : 1 << 30)
        : w < 3 ? (g < 2 ? amap(49152 + ((w - 1) * 32 + h) * 256 + cc * 16 + g * 8, 0) : 1 << 30) : lane * 128;
  k[12] = w == 3 ? (((lane >> 2) | h) ? 1 << 30 : R_FP + lane * 128) : g * 512 + (16 * ((w + 1) & 1) + cc) * 16;
  k[13] = fu((w == 0 ? cc < 8 : g < 2) ? 1.0f : 0.0f);
  k[14] = (lane >= 4 * NLK || w) ? 1 << 30 : amap(24576 + lane * 4096 + h * 128, 0);
  k[15] = amap(24576 + (c.slot >> 5) * 4096 + (lane & 31) * 128, 0);
  k[16] = CTL;
#pragma unroll
  for (int i = 0; i < 2; ++i) k[20 + i] = (lane >> 3) * 8192 + (head(h) * 128 + 64 * (w & 1)) * 2 + 16 * ((lane & 7) ^ (4 * i + 2 * (lane >> 5)));
#pragma unroll
  for (int t = 0; t < 4; ++t) k[22 + t] = WL + w * 32768 + (8 * (q >> 1) + (q & 1) + krow_lane(g)) * 128 + 16 * ((2 * t + (pp >> 1)) ^ (4 * (q >> 1) + 2 * (g & 1))) + 8 * (pp & 1);
  const u16* W = (w >> 1) ? p->Wk : p->Wq;
  constexpr float SC = 0.08838834764831845f, CE = 1.4426950408889634f * SC;
  v32u s0 = {RSRCS, (unsigned)w, ~0u, c.budget, SKEW, 260 * 16, 0, (unsigned)(R_QX + hblock(h, QKFAR) + (GRN - 1) * 16384), C2, KLT, KLP, 0, 0, fu(CE), fu(CE), fu(-1e9f / SC), (unsigned)(R_KX + hblock(h, QKFAR))};
#pragma unroll
  for (int i = 0; i < 4; ++i) { const unsigned long long m = __ballot(4 * g + i <= cc); s0[24 + 2 * i] = (unsigned)m; s0[25 + 2 * i] = (unsigned)(m >> 32); }
  v32u s1 = {plo(W), phi(W), 1024 * 4096 * 2, 0x00020000, 0, (unsigned)(R_FQ + h * 128), 0, 0, 0, (unsigned)(WL + w * 32768), 0, 0, 0, 0, 0, 0,
             (unsigned)(w << 21), (unsigned)(((w + 1) & 3) << 21), (unsigned)(((w + 2) & 3) << 21), (unsigned)(((w + 3) & 3) << 21), plo((const void*)p), phi((const void*)p),
             c.att ? 0u : (unsigned)c.b + 256u, (unsigned)(C_VERDICT + c.att * 128), ROTS(c.slot >> 5), HELLO};
  v32u wa[8], wv[5];
  asm volatile(
      "s_nop 0\n\tv_readfirstlane_b32 s8, v160\n\tv_readfirstlane_b32 s9, v161\n\tv_readfirstlane_b32 s10, v162\n\tv_readfirstlane_b32 s11, v163\n\t"
      "v_readfirstlane_b32 s12, v164\n\tv_readfirstlane_b32 s13, v165\n\tv_readfirstlane_b32 s14, v166\n\tv_readfirstlane_b32 s15, v167\n\tv_readfirstlane_b32 s16, v168\n\t"
      "v_readfirstlane_b32 s17, v169\n\tv_readfirstlane_b32 s18, v170\n\tv_readfirstlane_b32 s19, v171\n\tv_readfirstlane_b32 s20, v172\n\tv_readfirstlane_b32 s21, v173\n\t"
      "v_readfirstlane_b32 s22, v174\n\tv_readfirstlane_b32 s23, v175\n\tv_readfirstlane_b32 s24, v176\n\tv_readfirstlane_b32 s25, v177\n\tv_readfirstlane_b32 s26, v178\n\t"
      "v_readfirstlane_b32 s27, v179\n\tv_readfirstlane_b32 s28, v180\n\tv_readfirstlane_b32 s29, v181\n\tv_readfirstlane_b32 s30, v182\n\tv_readfirstlane_b32 s31, v183\n\t"
      "v_readfirstlane_b32 s32, v184\n\tv_readfirstlane_b32 s33, v185\n\tv_readfirstlane_b32 s34, v186\n\tv_readfirstlane_b32 s35, v187\n\tv_readfirstlane_b32 s36, v188\n\t"
      "v_readfirstlane_b32 s37, v189\n\tv_readfirstlane_b32 s38, v190\n\tv_readfirstlane_b32 s39, v191\n\tv_readfirstlane_b32 s40, v192\n\tv_readfirstlane_b32 s41, v193\n\t"
      "v_readfirstlane_b32 s42, v194\n\tv_readfirstlane_b32 s43, v195\n\tv_readfirstlane_b32 s45, v197\n\tv_readfirstlane_b32 s49, v201\n\tv_readfirstlane_b32 s54, v206\n\t"
      "v_readfirstlane_b32 s55, v207\n\tv_readfirstlane_b32 s56, v208\n\tv_readfirstlane_b32 s57, v209\n\tv_readfirstlane_b32 s58, v210\n\tv_readfirstlane_b32 s59, v211\n\t"
      "v_readfirstlane_b32 s60, v212\n\tv_readfirstlane_b32 s61, v213\n\tv_readfirstlane_b32 s62, v214\n\tv_readfirstlane_b32 s63, v215\n\tv_readfirstlane_b32 s64, v216\n\t"
      "v_readfirstlane_b32 s65, v217\n\tv_readfirstlane_b32 s66, v218\n\tv_readfirstlane_b32 s67, v219\n\tv_readfirstlane_b32 s68, v220\n\tv_readfirstlane_b32 s69, v221\n\t"
      "v_readfirstlane_b32 s70, v222\n\ts_nop 4\n\t"
      TKS("s_mul_i32 s48, s16, 72\n\ts_add_u32 s48, s48, 162080\n\tv_mov_b32 v224, s48\n\tv_mov_b32 v225, s74\n\tds_write_b32 v224, v225 offset:0\n\tv_mov_b32 v225, s75\n\tds_write_b32 v224, v225 offset:4\n\tv_mov_b32 v225, s76\n\tds_write_b32 v224, v225 offset:8\n\tv_mov_b32 v225, s77\n\tds_write_b32 v224, v225 offset:12\n\tv_mov_b32 v225, s78\n\tds_write_b32 v224, v225 offset:16\n\tv_mov_b32 v225, s79\n\tds_write_b32 v224, v225 offset:20\n\tv_mov_b32 v225, s80\n\tds_write_b32 v224, v225 offset:24\n\tv_mov_b32 v225, s81\n\tds_write_b32 v224, v225 offset:28\n\tv_mov_b32 v225, s82\n\tds_write_b32 v224, v225 offset:32\n\tv_mov_b32 v225, s83\n\tds_write_b32 v224, v225 offset:36\n\tv_mov_b32 v225, s84\n\tds_write_b32 v224, v225 offset:40\n\tv_mov_b32 v225, s85\n\tds_write_b32 v224, v225 offset:44\n\tv_mov_b32 v225, s86\n\tds_write_b32 v224, v225 offset:48\n\tv_mov_b32 v225, s87\n\tds_write_b32 v224, v225 offset:52\n\tv_mov_b32 v225, s88\n\tds_write_b32 v224, v225 offset:56\n\tv_mov_b32 v225, s89\n\tds_write_b32 v224, v225 offset:60\n\tv_mov_b32 v225, s90\n\tds_write_b32 v224, v225 offset:64\n\tv_mov_b32 v225, s91\n\tds_write_b32 v224, v225 offset:68\n\t")
      "s_buffer_load_dword s71, s[12:15], s69 glc\n\ts_add_u32 s50, s56, s64\n\ts_add_u32 m0, s49, 0\n\ts_add_u32 s48, s50, 0x0\n\t"
      "buffer_load_dwordx4 v20, s[40:43], s48 offen  lds\n\ts_add_u32 m0, s49, 1024\n\ts_add_u32 s48, s50, 0x10000\n\tbuffer_load_dwordx4 v21, s[40:43], s48 offen  lds\n\t"
      "s_add_u32 m0, s49, 2048\n\ts_add_u32 s48, s50, 0x20000\n\tbuffer_load_dwordx4 v20, s[40:43], s48 offen  lds\n\ts_add_u32 m0, s49, 3072\n\ts_add_u32 s48, s50, 0x30000\n\t"
      "buffer_load_dwordx4 v21, s[40:43], s48 offen  lds\n\ts_add_u32 m0, s49, 4096\n\ts_add_u32 s48, s50, 0x40000\n\tbuffer_load_dwordx4 v20, s[40:43], s48 offen  lds\n\t"
      "s_add_u32 m0, s49, 5120\n\ts_add_u32 s48, s50, 0x50000\n\tbuffer_load_dwordx4 v21, s[40:43], s48 offen  lds\n\ts_add_u32 m0, s49, 6144\n\ts_add_u32 s48, s50, 0x60000\n\t"
      "buffer_load_dwordx4 v20, s[40:43], s48 offen  lds\n\ts_add_u32 m0, s49, 7168\n\ts_add_u32 s48, s50, 0x70000\n\tbuffer_load_dwordx4 v21, s[40:43], s48 offen  lds\n\t"
      "s_waitcnt lgkmcnt(0)\n\ts_bitcmp0_b32 s68, 0\n\ts_cbranch_scc1 5f\n\ts_add_u32 s71, s71, 1\n\ts_sub_u32 s48, s69, 32768\n\tv_mov_b32 v17, s71\n\ts_mov_b64 exec, 1\n\t"
      "buffer_store_dword v17, off, s[12:15], s48 sc1\n\ts_mov_b64 exec, -1\n\t"
      "5:\n\ts_add_u32 s50, s56, s65\n\ts_add_u32 m0, s49, 8192\n\ts_add_u32 s48, s50, 0x0\n\tbuffer_load_dwordx4 v20, s[40:43], s48 offen  lds\n\ts_add_u32 m0, s49, 9216\n\t"
      "s_add_u32 s48, s50, 0x10000\n\tbuffer_load_dwordx4 v21, s[40:43], s48 offen  lds\n\ts_add_u32 m0, s49, 10240\n\ts_add_u32 s48, s50, 0x20000\n\t"
      "buffer_load_dwordx4 v20, s[40:43], s48 offen  lds\n\ts_add_u32 m0, s49, 11264\n\ts_add_u32 s48, s50, 0x30000\n\tbuffer_load_dwordx4 v21, s[40:43], s48 offen  lds\n\t"
      "s_add_u32 m0, s49, 12288\n\ts_add_u32 s48, s50, 0x40000\n\tbuffer_load_dwordx4 v20, s[40:43], s48 offen  lds\n\ts_add_u32 m0, s49, 13312\n\t"
      "s_add_u32 s48, s50, 0x50000\n\tbuffer_load_dwordx4 v21, s[40:43], s48 offen  lds\n\ts_add_u32 m0, s49, 14336\n\ts_add_u32 s48, s50, 0x60000\n\t"
      "buffer_load_dwordx4 v20, s[40:43], s48 offen  lds\n\ts_add_u32 m0, s49, 15360\n\ts_add_u32 s48, s50, 0x70000\n\tbuffer_load_dwordx4 v21, s[40:43], s48 offen  lds\n\t"
      "s_add_u32 s50, s56, s66\n\ts_add_u32 m0, s49, 16384\n\ts_add_u32 s48, s50, 0x0\n\tbuffer_load_dwordx4 v20, s[40:43], s48 offen  lds\n\ts_add_u32 m0, s49, 17408\n\t"
      "s_add_u32 s48, s50, 0x10000\n\tbuffer_load_dwordx4 v21, s[40:43], s48 offen  lds\n\ts_add_u32 m0, s49, 18432\n\ts_add_u32 s48, s50, 0x20000\n\t"
      "buffer_load_dwordx4 v20, s[40:43], s48 offen  lds\n\ts_add_u32 m0, s49, 19456\n\ts_add_u32 s48, s50, 0x30000\n\tbuffer_load_dwordx4 v21, s[40:43], s48 offen  lds\n\t"
      "s_add_u32 m0, s49, 20480\n\ts_add_u32 s48, s50, 0x40000\n\tbuffer_load_dwordx4 v20, s[40:43], s48 offen  lds\n\ts_add_u32 m0, s49, 21504\n\t"
      "s_add_u32 s48, s50, 0x50000\n\tbuffer_load_dwordx4 v21, s[40:43], s48 offen  lds\n\ts_add_u32 m0, s49, 22528\n\ts_add_u32 s48, s50, 0x60000\n\t"
      "buffer_load_dwordx4 v20, s[40:43], s48 offen  lds\n\ts_add_u32 m0, s49, 23552\n\ts_add_u32 s48, s50, 0x70000\n\tbuffer_load_dwordx4 v21, s[40:43], s48 offen  lds\n\t"
      "s_cmp_lt_u32 s68, 1\n\ts_cbranch_scc1 7f\n\ts_cmp_eq_u32 s68, 2\n\ts_cbranch_scc1 6f\n\t"
      "5:\n\ts_buffer_load_dword s48, s[12:15], s69 glc\n\ts_waitcnt lgkmcnt(0)\n\ts_cmp_eq_u32 s48, s71\n\ts_cbranch_scc1 6f\n\ts_cmp_lg_u32 s68, 3\n\ts_cbranch_scc1 7f\n\t"
      "s_cmp_eq_u32 s18, 0\n\ts_cbranch_scc1 6f\n\ts_sub_u32 s18, s18, 1\n\ts_branch 5b\n\t"
      "6:\n\tv_mov_b32 v17, 1\n\ts_mov_b64 exec, 1\n\tbuffer_atomic_add v17, off, s[12:15], s70\n\ts_mov_b64 exec, -1\n\ts_mov_b32 s68, 0\n\t"
      "7:\n\ts_waitcnt vmcnt(16)\n\tds_read_b64_tr_b16 a[0:1], v22 offset:0\n\tds_read_b64_tr_b16 a[2:3], v22 offset:256\n\tds_read_b64_tr_b16 a[4:5], v22 offset:4096\n\t"
      "ds_read_b64_tr_b16 a[6:7], v22 offset:4352\n\tds_read_b64_tr_b16 a[8:9], v23 offset:0\n\tds_read_b64_tr_b16 a[10:11], v23 offset:256\n\t"
      "ds_read_b64_tr_b16 a[12:13], v23 offset:4096\n\tds_read_b64_tr_b16 a[14:15], v23 offset:4352\n\tds_read_b64_tr_b16 v[32:33], v24 offset:0\n\t"
      "ds_read_b64_tr_b16 v[34:35], v24 offset:256\n\tds_read_b64_tr_b16 v[36:37], v24 offset:4096\n\tds_read_b64_tr_b16 v[38:39], v24 offset:4352\n\t"
      "ds_read_b64_tr_b16 v[152:153], v25 offset:0\n\tds_read_b64_tr_b16 v[154:155], v25 offset:256\n\tds_read_b64_tr_b16 v[156:157], v25 offset:4096\n\t"
      "ds_read_b64_tr_b16 v[158:159], v25 offset:4352\n\ts_waitcnt lgkmcnt(0)\n\ts_add_u32 s50, s56, s67\n\ts_add_u32 m0, s49, 24576\n\ts_add_u32 s48, s50, 0x0\n\t"
      "buffer_load_dwordx4 v20, s[40:43], s48 offen  lds\n\ts_add_u32 m0, s49, 25600\n\ts_add_u32 s48, s50, 0x10000\n\tbuffer_load_dwordx4 v21, s[40:43], s48 offen  lds\n\t"
      "s_add_u32 m0, s49, 26624\n\ts_add_u32 s48, s50, 0x20000\n\tbuffer_load_dwordx4 v20, s[40:43], s48 offen  lds\n\ts_add_u32 m0, s49, 27648\n\t"
      "s_add_u32 s48, s50, 0x30000\n\tbuffer_load_dwordx4 v21, s[40:43], s48 offen  lds\n\ts_add_u32 m0, s49, 28672\n\ts_add_u32 s48, s50, 0x40000\n\t"
      "buffer_load_dwordx4 v20, s[40:43], s48 offen  lds\n\ts_add_u32 m0, s49, 29696\n\ts_add_u32 s48, s50, 0x50000\n\tbuffer_load_dwordx4 v21, s[40:43], s48 offen  lds\n\t"
      "s_add_u32 m0, s49, 30720\n\ts_add_u32 s48, s50, 0x60000\n\tbuffer_load_dwordx4 v20, s[40:43], s48 offen  lds\n\ts_add_u32 m0, s49, 31744\n\t"
      "s_add_u32 s48, s50, 0x70000\n\tbuffer_load_dwordx4 v21, s[40:43], s48 offen  lds\n\ts_cmp_lt_u32 s68, 1\n\ts_cbranch_scc1 7f\n\ts_cmp_eq_u32 s68, 2\n\t"
      "s_cbranch_scc1 6f\n\t"
      "5:\n\ts_buffer_load_dword s48, s[12:15], s69 glc\n\ts_waitcnt lgkmcnt(0)\n\ts_cmp_eq_u32 s48, s71\n\ts_cbranch_scc0 7f\n\t"
      "6:\n\tv_mov_b32 v17, 1\n\ts_mov_b64 exec, 1\n\tbuffer_atomic_add v17, off, s[12:15], s70\n\ts_mov_b64 exec, -1\n\ts_mov_b32 s68, 0\n\t"
      "7:\n\ts_waitcnt vmcnt(16)\n\tds_read_b64_tr_b16 a[16:17], v22 offset:8192\n\tds_read_b64_tr_b16 a[18:19], v22 offset:8448\n\t"
      "ds_read_b64_tr_b16 a[20:21], v22 offset:12288\n\tds_read_b64_tr_b16 a[22:23], v22 offset:12544\n\tds_read_b64_tr_b16 a[24:25], v23 offset:8192\n\t"
      "ds_read_b64_tr_b16 a[26:27], v23 offset:8448\n\tds_read_b64_tr_b16 a[28:29], v23 offset:12288\n\tds_read_b64_tr_b16 a[30:31], v23 offset:12544\n\t"
      "ds_read_b64_tr_b16 v[40:41], v24 offset:8192\n\tds_read_b64_tr_b16 v[42:43], v24 offset:8448\n\tds_read_b64_tr_b16 v[44:45], v24 offset:12288\n\t"
      "ds_read_b64_tr_b16 v[46:47], v24 offset:12544\n\tds_read_b64_tr_b16 v[160:161], v25 offset:8192\n\tds_read_b64_tr_b16 v[162:163], v25 offset:8448\n\t"
      "ds_read_b64_tr_b16 v[164:165], v25 offset:12288\n\tds_read_b64_tr_b16 v[166:167], v25 offset:12544\n\ts_waitcnt lgkmcnt(0)\n\ts_add_u32 s50, s57, s64\n\t"
      "s_add_u32 m0, s49, 0\n\ts_add_u32 s48, s50, 0x0\n\tbuffer_load_dwordx4 v20, s[40:43], s48 offen  lds\n\ts_add_u32 m0, s49, 1024\n\ts_add_u32 s48, s50, 0x10000\n\t"
      "buffer_load_dwordx4 v21, s[40:43], s48 offen  lds\n\ts_add_u32 m0, s49, 2048\n\ts_add_u32 s48, s50, 0x20000\n\tbuffer_load_dwordx4 v20, s[40:43], s48 offen  lds\n\t"
      "s_add_u32 m0, s49, 3072\n\ts_add_u32 s48, s50, 0x30000\n\tbuffer_load_dwordx4 v21, s[40:43], s48 offen  lds\n\ts_add_u32 m0, s49, 4096\n\ts_add_u32 s48, s50, 0x40000\n\t"
      "buffer_load_dwordx4 v20, s[40:43], s48 offen  lds\n\ts_add_u32 m0, s49, 5120\n\ts_add_u32 s48, s50, 0x50000\n\tbuffer_load_dwordx4 v21, s[40:43], s48 offen  lds\n\t"
      "s_add_u32 m0, s49, 6144\n\ts_add_u32 s48, s50, 0x60000\n\tbuffer_load_dwordx4 v20, s[40:43], s48 offen  lds\n\ts_add_u32 m0, s49, 7168\n\ts_add_u32 s48, s50, 0x70000\n\t"
      "buffer_load_dwordx4 v21, s[40:43], s48 offen  lds\n\ts_cmp_lt_u32 s68, 1\n\ts_cbranch_scc1 7f\n\ts_cmp_eq_u32 s68, 2\n\ts_cbranch_scc1 6f\n\t"
      "5:\n\ts_buffer_load_dword s48, s[12:15], s69 glc\n\ts_waitcnt lgkmcnt(0)\n\ts_cmp_eq_u32 s48, s71\n\ts_cbranch_scc0 7f\n\t"
      "6:\n\tv_mov_b32 v17, 1\n\ts_mov_b64 exec, 1\n\tbuffer_atomic_add v17, off, s[12:15], s70\n\ts_mov_b64 exec, -1\n\ts_mov_b32 s68, 0\n\t"
      "7:\n\ts_waitcnt vmcnt(16)\n\tds_read_b64_tr_b16 a[32:33], v22 offset:16384\n\tds_read_b64_tr_b16 a[34:35], v22 offset:16640\n\t"
      "ds_read_b64_tr_b16 a[36:37], v22 offset:20480\n\tds_read_b64_tr_b16 a[38:39], v22 offset:20736\n\tds_read_b64_tr_b16 a[40:41], v23 offset:16384\n\t"
      "ds_read_b64_tr_b16 a[42:43], v23 offset:16640\n\tds_read_b64_tr_b16 a[44:45], v23 offset:20480\n\tds_read_b64_tr_b16 a[46:47], v23 offset:20736\n\t"
      "ds_read_b64_tr_b16 v[48:49], v24 offset:16384\n\tds_read_b64_tr_b16 v[50:51], v24 offset:16640\n\tds_read_b64_tr_b16 v[52:53], v24 offset:20480\n\t"
      "ds_read_b64_tr_b16 v[54:55], v24 offset:20736\n\tds_read_b64_tr_b16 v[168:169], v25 offset:16384\n\tds_read_b64_tr_b16 v[170:171], v25 offset:16640\n\t"
      "ds_read_b64_tr_b16 v[172:173], v25 offset:20480\n\tds_read_b64_tr_b16 v[174:175], v25 offset:20736\n\ts_waitcnt lgkmcnt(0)\n\ts_add_u32 s50, s58, s64\n\t"
      "s_add_u32 m0, s49, 8192\n\ts_add_u32 s48, s50, 0x0\n\tbuffer_load_dwordx4 v20, s[40:43], s48 offen  lds\n\ts_add_u32 m0, s49, 9216\n\ts_add_u32 s48, s50, 0x10000\n\t"
      "buffer_load_dwordx4 v21, s[40:43], s48 offen  lds\n\ts_add_u32 m0, s49, 10240\n\ts_add_u32 s48, s50, 0x20000\n\tbuffer_load_dwordx4 v20, s[40:43], s48 offen  lds\n\t"
      "s_add_u32 m0, s49, 11264\n\ts_add_u32 s48, s50, 0x30000\n\tbuffer_load_dwordx4 v21, s[40:43], s48 offen  lds\n\ts_add_u32 m0, s49, 12288\n\t"
      "s_add_u32 s48, s50, 0x40000\n\tbuffer_load_dwordx4 v20, s[40:43], s48 offen  lds\n\ts_add_u32 m0, s49, 13312\n\ts_add_u32 s48, s50, 0x50000\n\t"
      "buffer_load_dwordx4 v21, s[40:43], s48 offen  lds\n\ts_add_u32 m0, s49, 14336\n\ts_add_u32 s48, s50, 0x60000\n\tbuffer_load_dwordx4 v20, s[40:43], s48 offen  lds\n\t"
      "s_add_u32 m0, s49, 15360\n\ts_add_u32 s48, s50, 0x70000\n\tbuffer_load_dwordx4 v21, s[40:43], s48 offen  lds\n\ts_cmp_lt_u32 s68, 1\n\ts_cbranch_scc1 7f\n\t"
      "s_cmp_eq_u32 s68, 2\n\ts_cbranch_scc1 6f\n\t"
      "5:\n\ts_buffer_load_dword s48, s[12:15], s69 glc\n\ts_waitcnt lgkmcnt(0)\n\ts_cmp_eq_u32 s48, s71\n\ts_cbranch_scc0 7f\n\t"
      "6:\n\tv_mov_b32 v17, 1\n\ts_mov_b64 exec, 1\n\tbuffer_atomic_add v17, off, s[12:15], s70\n\ts_mov_b64 exec, -1\n\ts_mov_b32 s68, 0\n\t"
      "7:\n\ts_waitcnt vmcnt(16)\n\tds_read_b64_tr_b16 a[48:49], v22 offset:24576\n\tds_read_b64_tr_b16 a[50:51], v22 offset:24832\n\t"
      "ds_read_b64_tr_b16 a[52:53], v22 offset:28672\n\tds_read_b64_tr_b16 a[54:55], v22 offset:28928\n\tds_read_b64_tr_b16 a[56:57], v23 offset:24576\n\t"
      "ds_read_b64_tr_b16 a[58:59], v23 offset:24832\n\tds_read_b64_tr_b16 a[60:61], v23 offset:28672\n\tds_read_b64_tr_b16 a[62:63], v23 offset:28928\n\t"
      "ds_read_b64_tr_b16 v[56:57], v24 offset:24576\n\tds_read_b64_tr_b16 v[58:59], v24 offset:24832\n\tds_read_b64_tr_b16 v[60:61], v24 offset:28672\n\t"
      "ds_read_b64_tr_b16 v[62:63], v24 offset:28928\n\tds_read_b64_tr_b16 v[176:177], v25 offset:24576\n\tds_read_b64_tr_b16 v[178:179], v25 offset:24832\n\t"
      "ds_read_b64_tr_b16 v[180:181], v25 offset:28672\n\tds_read_b64_tr_b16 v[182:183], v25 offset:28928\n\ts_waitcnt lgkmcnt(0)\n\ts_add_u32 s50, s59, s64\n\t"
      "s_add_u32 m0, s49, 16384\n\ts_add_u32 s48, s50, 0x0\n\tbuffer_load_dwordx4 v20, s[40:43], s48 offen  lds\n\ts_add_u32 m0, s49, 17408\n\ts_add_u32 s48, s50, 0x10000\n\t"
      "buffer_load_dwordx4 v21, s[40:43], s48 offen  lds\n\ts_add_u32 m0, s49, 18432\n\ts_add_u32 s48, s50, 0x20000\n\tbuffer_load_dwordx4 v20, s[40:43], s48 offen  lds\n\t"
      "s_add_u32 m0, s49, 19456\n\ts_add_u32 s48, s50, 0x30000\n\tbuffer_load_dwordx4 v21, s[40:43], s48 offen  lds\n\ts_add_u32 m0, s49, 20480\n\t"
      "s_add_u32 s48, s50, 0x40000\n\tbuffer_load_dwordx4 v20, s[40:43], s48 offen  lds\n\ts_add_u32 m0, s49, 21504\n\ts_add_u32 s48, s50, 0x50000\n\t"
      "buffer_load_dwordx4 v21, s[40:43], s48 offen  lds\n\ts_add_u32 m0, s49, 22528\n\ts_add_u32 s48, s50, 0x60000\n\tbuffer_load_dwordx4 v20, s[40:43], s48 offen  lds\n\t"
      "s_add_u32 m0, s49, 23552\n\ts_add_u32 s48, s50, 0x70000\n\tbuffer_load_dwordx4 v21, s[40:43], s48 offen  lds\n\ts_cmp_lt_u32 s68, 1\n\ts_cbranch_scc1 7f\n\t"
      "s_cmp_eq_u32 s68, 2\n\ts_cbranch_scc1 6f\n\t"
      "5:\n\ts_buffer_load_dword s48, s[12:15], s69 glc\n\ts_waitcnt lgkmcnt(0)\n\ts_cmp_eq_u32 s48, s71\n\ts_cbranch_scc0 7f\n\t"
      "6:\n\tv_mov_b32 v17, 1\n\ts_mov_b64 exec, 1\n\tbuffer_atomic_add v17, off, s[12:15], s70\n\ts_mov_b64 exec, -1\n\ts_mov_b32 s68, 0\n\t"
      "7:\n\ts_waitcnt vmcnt(16)\n\tds_read_b64_tr_b16 a[64:65], v22 offset:0\n\tds_read_b64_tr_b16 a[66:67], v22 offset:256\n\tds_read_b64_tr_b16 a[68:69], v22 offset:4096\n\t"
      "ds_read_b64_tr_b16 a[70:71], v22 offset:4352\n\tds_read_b64_tr_b16 a[72:73], v23 offset:0\n\tds_read_b64_tr_b16 a[74:75], v23 offset:256\n\t"
      "ds_read_b64_tr_b16 a[76:77], v23 offset:4096\n\tds_read_b64_tr_b16 a[78:79], v23 offset:4352\n\tds_read_b64_tr_b16 v[64:65], v24 offset:0\n\t"
      "ds_read_b64_tr_b16 v[66:67], v24 offset:256\n\tds_read_b64_tr_b16 v[68:69], v24 offset:4096\n\tds_read_b64_tr_b16 v[70:71], v24 offset:4352\n\t"
      "ds_read_b64_tr_b16 v[184:185], v25 offset:0\n\tds_read_b64_tr_b16 v[186:187], v25 offset:256\n\tds_read_b64_tr_b16 v[188:189], v25 offset:4096\n\t"
      "ds_read_b64_tr_b16 v[190:191], v25 offset:4352\n\ts_waitcnt lgkmcnt(0)\n\ts_add_u32 s50, s57, s65\n\ts_add_u32 m0, s49, 24576\n\ts_add_u32 s48, s50, 0x0\n\t"
      "buffer_load_dwordx4 v20, s[40:43], s48 offen  lds\n\ts_add_u32 m0, s49, 25600\n\ts_add_u32 s48, s50, 0x10000\n\tbuffer_load_dwordx4 v21, s[40:43], s48 offen  lds\n\t"
      "s_add_u32 m0, s49, 26624\n\ts_add_u32 s48, s50, 0x20000\n\tbuffer_load_dwordx4 v20, s[40:43], s48 offen  lds\n\ts_add_u32 m0, s49, 27648\n\t"
      "s_add_u32 s48, s50, 0x30000\n\tbuffer_load_dwordx4 v21, s[40:43], s48 offen  lds\n\ts_add_u32 m0, s49, 28672\n\ts_add_u32 s48, s50, 0x40000\n\t"
      "buffer_load_dwordx4 v20, s[40:43], s48 offen  lds\n\ts_add_u32 m0, s49, 29696\n\ts_add_u32 s48, s50, 0x50000\n\tbuffer_load_dwordx4 v21, s[40:43], s48 offen  lds\n\t"
      "s_add_u32 m0, s49, 30720\n\ts_add_u32 s48, s50, 0x60000\n\tbuffer_load_dwordx4 v20, s[40:43], s48 offen  lds\n\ts_add_u32 m0, s49, 31744\n\t"
      "s_add_u32 s48, s50, 0x70000\n\tbuffer_load_dwordx4 v21, s[40:43], s48 offen  lds\n\ts_cmp_lt_u32 s68, 1\n\ts_cbranch_scc1 7f\n\ts_cmp_eq_u32 s68, 2\n\t"
      "s_cbranch_scc1 6f\n\t"
      "5:\n\ts_buffer_load_dword s48, s[12:15], s69 glc\n\ts_waitcnt lgkmcnt(0)\n\ts_cmp_eq_u32 s48, s71\n\ts_cbranch_scc0 7f\n\t"
      "6:\n\tv_mov_b32 v17, 1\n\ts_mov_b64 exec, 1\n\tbuffer_atomic_add v17, off, s[12:15], s70\n\ts_mov_b64 exec, -1\n\ts_mov_b32 s68, 0\n\t"
      "7:\n\ts_waitcnt vmcnt(16)\n\tds_read_b64_tr_b16 a[80:81], v22 offset:8192\n\tds_read_b64_tr_b16 a[82:83], v22 offset:8448\n\t"
      "ds_read_b64_tr_b16 a[84:85], v22 offset:12288\n\tds_read_b64_tr_b16 a[86:87], v22 offset:12544\n\tds_read_b64_tr_b16 a[88:89], v23 offset:8192\n\t"
      "ds_read_b64_tr_b16 a[90:91], v23 offset:8448\n\tds_read_b64_tr_b16 a[92:93], v23 offset:12288\n\tds_read_b64_tr_b16 a[94:95], v23 offset:12544\n\t"
      "ds_read_b64_tr_b16 v[72:73], v24 offset:8192\n\tds_read_b64_tr_b16 v[74:75], v24 offset:8448\n\tds_read_b64_tr_b16 v[76:77], v24 offset:12288\n\t"
      "ds_read_b64_tr_b16 v[78:79], v24 offset:12544\n\tds_read_b64_tr_b16 v[192:193], v25 offset:8192\n\tds_read_b64_tr_b16 v[194:195], v25 offset:8448\n\t"
      "ds_read_b64_tr_b16 v[196:197], v25 offset:12288\n\tds_read_b64_tr_b16 v[198:199], v25 offset:12544\n\ts_waitcnt lgkmcnt(0)\n\ts_add_u32 s50, s58, s65\n\t"
      "s_add_u32 m0, s49, 0\n\ts_add_u32 s48, s50, 0x0\n\tbuffer_load_dwordx4 v20, s[40:43], s48 offen  lds\n\ts_add_u32 m0, s49, 1024\n\ts_add_u32 s48, s50, 0x10000\n\t"
      "buffer_load_dwordx4 v21, s[40:43], s48 offen  lds\n\ts_add_u32 m0, s49, 2048\n\ts_add_u32 s48, s50, 0x20000\n\tbuffer_load_dwordx4 v20, s[40:43], s48 offen  lds\n\t"
      "s_add_u32 m0, s49, 3072\n\ts_add_u32 s48, s50, 0x30000\n\tbuffer_load_dwordx4 v21, s[40:43], s48 offen  lds\n\ts_add_u32 m0, s49, 4096\n\ts_add_u32 s48, s50, 0x40000\n\t"
      "buffer_load_dwordx4 v20, s[40:43], s48 offen  lds\n\ts_add_u32 m0, s49, 5120\n\ts_add_u32 s48, s50, 0x50000\n\tbuffer_load_dwordx4 v21, s[40:43], s48 offen  lds\n\t"
      "s_add_u32 m0, s49, 6144\n\ts_add_u32 s48, s50, 0x60000\n\tbuffer_load_dwordx4 v20, s[40:43], s48 offen  lds\n\ts_add_u32 m0, s49, 7168\n\ts_add_u32 s48, s50, 0x70000\n\t"
      "buffer_load_dwordx4 v21, s[40:43], s48 offen  lds\n\ts_cmp_lt_u32 s68, 1\n\ts_cbranch_scc1 7f\n\ts_cmp_eq_u32 s68, 2\n\ts_cbranch_scc1 6f\n\t"
      "5:\n\ts_buffer_load_dword s48, s[12:15], s69 glc\n\ts_waitcnt lgkmcnt(0)\n\ts_cmp_eq_u32 s48, s71\n\ts_cbranch_scc0 7f\n\t"
      "6:\n\tv_mov_b32 v17, 1\n\ts_mov_b64 exec, 1\n\tbuffer_atomic_add v17, off, s[12:15], s70\n\ts_mov_b64 exec, -1\n\ts_mov_b32 s68, 0\n\t"
      "7:\n\ts_waitcnt vmcnt(16)\n\tds_read_b64_tr_b16 a[96:97], v22 offset:16384\n\tds_read_b64_tr_b16 a[98:99], v22 offset:16640\n\t"
      "ds_read_b64_tr_b16 a[100:101], v22 offset:20480\n\tds_read_b64_tr_b16 a[102:103], v22 offset:20736\n\tds_read_b64_tr_b16 a[104:105], v23 offset:16384\n\t"
      "ds_read_b64_tr_b16 a[106:107], v23 offset:16640\n\tds_read_b64_tr_b16 a[108:109], v23 offset:20480\n\tds_read_b64_tr_b16 a[110:111], v23 offset:20736\n\t"
      "ds_read_b64_tr_b16 v[80:81], v24 offset:16384\n\tds_read_b64_tr_b16 v[82:83], v24 offset:16640\n\tds_read_b64_tr_b16 v[84:85], v24 offset:20480\n\t"
      "ds_read_b64_tr_b16 v[86:87], v24 offset:20736\n\tds_read_b64_tr_b16 v[200:201], v25 offset:16384\n\tds_read_b64_tr_b16 v[202:203], v25 offset:16640\n\t"
      "ds_read_b64_tr_b16 v[204:205], v25 offset:20480\n\tds_read_b64_tr_b16 v[206:207], v25 offset:20736\n\ts_waitcnt lgkmcnt(0)\n\ts_add_u32 s50, s59, s65\n\t"
      "s_add_u32 m0, s49, 8192\n\ts_add_u32 s48, s50, 0x0\n\tbuffer_load_dwordx4 v20, s[40:43], s48 offen  lds\n\ts_add_u32 m0, s49, 9216\n\ts_add_u32 s48, s50, 0x10000\n\t"
      "buffer_load_dwordx4 v21, s[40:43], s48 offen  lds\n\ts_add_u32 m0, s49, 10240\n\ts_add_u32 s48, s50, 0x20000\n\tbuffer_load_dwordx4 v20, s[40:43], s48 offen  lds\n\t"
      "s_add_u32 m0, s49, 11264\n\ts_add_u32 s48, s50, 0x30000\n\tbuffer_load_dwordx4 v21, s[40:43], s48 offen  lds\n\ts_add_u32 m0, s49, 12288\n\t"
      "s_add_u32 s48, s50, 0x40000\n\tbuffer_load_dwordx4 v20, s[40:43], s48 offen  lds\n\ts_add_u32 m0, s49, 13312\n\ts_add_u32 s48, s50, 0x50000\n\t"
      "buffer_load_dwordx4 v21, s[40:43], s48 offen  lds\n\ts_add_u32 m0, s49, 14336\n\ts_add_u32 s48, s50, 0x60000\n\tbuffer_load_dwordx4 v20, s[40:43], s48 offen  lds\n\t"
      "s_add_u32 m0, s49, 15360\n\ts_add_u32 s48, s50, 0x70000\n\tbuffer_load_dwordx4 v21, s[40:43], s48 offen  lds\n\ts_cmp_lt_u32 s68, 1\n\ts_cbranch_scc1 7f\n\t"
      "s_cmp_eq_u32 s68, 2\n\ts_cbranch_scc1 6f\n\t"
      "5:\n\ts_buffer_load_dword s48, s[12:15], s69 glc\n\ts_waitcnt lgkmcnt(0)\n\ts_cmp_eq_u32 s48, s71\n\ts_cbranch_scc0 7f\n\t"
      "6:\n\tv_mov_b32 v17, 1\n\ts_mov_b64 exec, 1\n\tbuffer_atomic_add v17, off, s[12:15], s70\n\ts_mov_b64 exec, -1\n\ts_mov_b32 s68, 0\n\t"
      "7:\n\ts_waitcnt vmcnt(16)\n\tds_read_b64_tr_b16 a[112:113], v22 offset:24576\n\tds_read_b64_tr_b16 a[114:115], v22 offset:24832\n\t"
      "ds_read_b64_tr_b16 a[116:117], v22 offset:28672\n\tds_read_b64_tr_b16 a[118:119], v22 offset:28928\n\tds_read_b64_tr_b16 a[120:121], v23 offset:24576\n\t"
      "ds_read_b64_tr_b16 a[122:123], v23 offset:24832\n\tds_read_b64_tr_b16 a[124:125], v23 offset:28672\n\tds_read_b64_tr_b16 a[126:127], v23 offset:28928\n\t"
      "ds_read_b64_tr_b16 v[88:89], v24 offset:24576\n\tds_read_b64_tr_b16 v[90:91], v24 offset:24832\n\tds_read_b64_tr_b16 v[92:93], v24 offset:28672\n\t"
      "ds_read_b64_tr_b16 v[94:95], v24 offset:28928\n\tds_read_b64_tr_b16 v[208:209], v25 offset:24576\n\tds_read_b64_tr_b16 v[210:211], v25 offset:24832\n\t"
      "ds_read_b64_tr_b16 v[212:213], v25 offset:28672\n\tds_read_b64_tr_b16 v[214:215], v25 offset:28928\n\ts_waitcnt lgkmcnt(0)\n\ts_add_u32 s50, s57, s66\n\t"
      "s_add_u32 m0, s49, 16384\n\ts_add_u32 s48, s50, 0x0\n\tbuffer_load_dwordx4 v20, s[40:43], s48 offen  lds\n\ts_add_u32 m0, s49, 17408\n\ts_add_u32 s48, s50, 0x10000\n\t"
      "buffer_load_dwordx4 v21, s[40:43], s48 offen  lds\n\ts_add_u32 m0, s49, 18432\n\ts_add_u32 s48, s50, 0x20000\n\tbuffer_load_dwordx4 v20, s[40:43], s48 offen  lds\n\t"
      "s_add_u32 m0, s49, 19456\n\ts_add_u32 s48, s50, 0x30000\n\tbuffer_load_dwordx4 v21, s[40:43], s48 offen  lds\n\ts_add_u32 m0, s49, 20480\n\t"
      "s_add_u32 s48, s50, 0x40000\n\tbuffer_load_dwordx4 v20, s[40:43], s48 offen  lds\n\ts_add_u32 m0, s49, 21504\n\ts_add_u32 s48, s50, 0x50000\n\t"
      "buffer_load_dwordx4 v21, s[40:43], s48 offen  lds\n\ts_add_u32 m0, s49, 22528\n\ts_add_u32 s48, s50, 0x60000\n\tbuffer_load_dwordx4 v20, s[40:43], s48 offen  lds\n\t"
      "s_add_u32 m0, s49, 23552\n\ts_add_u32 s48, s50, 0x70000\n\tbuffer_load_dwordx4 v21, s[40:43], s48 offen  lds\n\ts_cmp_lt_u32 s68, 1\n\ts_cbranch_scc1 7f\n\t"
      "s_cmp_eq_u32 s68, 2\n\ts_cbranch_scc1 6f\n\t"
      "5:\n\ts_buffer_load_dword s48, s[12:15], s69 glc\n\ts_waitcnt lgkmcnt(0)\n\ts_cmp_eq_u32 s48, s71\n\ts_cbranch_scc0 7f\n\t"
      "6:\n\tv_mov_b32 v17, 1\n\ts_mov_b64 exec, 1\n\tbuffer_atomic_add v17, off, s[12:15], s70\n\ts_mov_b64 exec, -1\n\ts_mov_b32 s68, 0\n\t"
      "7:\n\ts_waitcnt vmcnt(16)\n\tds_read_b64_tr_b16 a[128:129], v22 offset:0\n\tds_read_b64_tr_b16 a[130:131], v22 offset:256\n\t"
      "ds_read_b64_tr_b16 a[132:133], v22 offset:4096\n\tds_read_b64_tr_b16 a[134:135], v22 offset:4352\n\tds_read_b64_tr_b16 a[136:137], v23 offset:0\n\t"
      "ds_read_b64_tr_b16 a[138:139], v23 offset:256\n\tds_read_b64_tr_b16 a[140:141], v23 offset:4096\n\tds_read_b64_tr_b16 a[142:143], v23 offset:4352\n\t"
      "ds_read_b64_tr_b16 v[96:97], v24 offset:0\n\tds_read_b64_tr_b16 v[98:99], v24 offset:256\n\tds_read_b64_tr_b16 v[100:101], v24 offset:4096\n\t"
      "ds_read_b64_tr_b16 v[102:103], v24 offset:4352\n\tds_read_b64_tr_b16 v[216:217], v25 offset:0\n\tds_read_b64_tr_b16 v[218:219], v25 offset:256\n\t"
      "ds_read_b64_tr_b16 v[220:221], v25 offset:4096\n\tds_read_b64_tr_b16 v[222:223], v25 offset:4352\n\ts_waitcnt lgkmcnt(0)\n\ts_add_u32 s50, s58, s66\n\t"
      "s_add_u32 m0, s49, 24576\n\ts_add_u32 s48, s50, 0x0\n\tbuffer_load_dwordx4 v20, s[40:43], s48 offen  lds\n\ts_add_u32 m0, s49, 25600\n\ts_add_u32 s48, s50, 0x10000\n\t"
      "buffer_load_dwordx4 v21, s[40:43], s48 offen  lds\n\ts_add_u32 m0, s49, 26624\n\ts_add_u32 s48, s50, 0x20000\n\tbuffer_load_dwordx4 v20, s[40:43], s48 offen  lds\n\t"
      "s_add_u32 m0, s49, 27648\n\ts_add_u32 s48, s50, 0x30000\n\tbuffer_load_dwordx4 v21, s[40:43], s48 offen  lds\n\ts_add_u32 m0, s49, 28672\n\t"
      "s_add_u32 s48, s50, 0x40000\n\tbuffer_load_dwordx4 v20, s[40:43], s48 offen  lds\n\ts_add_u32 m0, s49, 29696\n\ts_add_u32 s48, s50, 0x50000\n\t"
      "buffer_load_dwordx4 v21, s[40:43], s48 offen  lds\n\ts_add_u32 m0, s49, 30720\n\ts_add_u32 s48, s50, 0x60000\n\tbuffer_load_dwordx4 v20, s[40:43], s48 offen  lds\n\t"
      "s_add_u32 m0, s49, 31744\n\ts_add_u32 s48, s50, 0x70000\n\tbuffer_load_dwordx4 v21, s[40:43], s48 offen  lds\n\ts_cmp_lt_u32 s68, 1\n\ts_cbranch_scc1 7f\n\t"
      "s_cmp_eq_u32 s68, 2\n\ts_cbranch_scc1 6f\n\t"
      "5:\n\ts_buffer_load_dword s48, s[12:15], s69 glc\n\ts_waitcnt lgkmcnt(0)\n\ts_cmp_eq_u32 s48, s71\n\ts_cbranch_scc0 7f\n\t"
      "6:\n\tv_mov_b32 v17, 1\n\ts_mov_b64 exec, 1\n\tbuffer_atomic_add v17, off, s[12:15], s70\n\ts_mov_b64 exec, -1\n\ts_mov_b32 s68, 0\n\t"
      "7:\n\ts_waitcnt vmcnt(16)\n\tds_read_b64_tr_b16 a[144:145], v22 offset:8192\n\tds_read_b64_tr_b16 a[146:147], v22 offset:8448\n\t"
      "ds_read_b64_tr_b16 a[148:149], v22 offset:12288\n\tds_read_b64_tr_b16 a[150:151], v22 offset:12544\n\tds_read_b64_tr_b16 a[152:153], v23 offset:8192\n\t"
      "ds_read_b64_tr_b16 a[154:155], v23 offset:8448\n\tds_read_b64_tr_b16 a[156:157], v23 offset:12288\n\tds_read_b64_tr_b16 a[158:159], v23 offset:12544\n\t"
      "ds_read_b64_tr_b16 v[104:105], v24 offset:8192\n\tds_read_b64_tr_b16 v[106:107], v24 offset:8448\n\tds_read_b64_tr_b16 v[108:109], v24 offset:12288\n\t"
      "ds_read_b64_tr_b16 v[110:111], v24 offset:12544\n\tds_read_b64_tr_b16 v[224:225], v25 offset:8192\n\tds_read_b64_tr_b16 v[226:227], v25 offset:8448\n\t"
      "ds_read_b64_tr_b16 v[228:229], v25 offset:12288\n\tds_read_b64_tr_b16 v[230:231], v25 offset:12544\n\ts_waitcnt lgkmcnt(0)\n\ts_add_u32 s50, s59, s66\n\t"
      "s_add_u32 m0, s49, 0\n\ts_add_u32 s48, s50, 0x0\n\tbuffer_load_dwordx4 v20, s[40:43], s48 offen  lds\n\ts_add_u32 m0, s49, 1024\n\ts_add_u32 s48, s50, 0x10000\n\t"
      "buffer_load_dwordx4 v21, s[40:43], s48 offen  lds\n\ts_add_u32 m0, s49, 2048\n\ts_add_u32 s48, s50, 0x20000\n\tbuffer_load_dwordx4 v20, s[40:43], s48 offen  lds\n\t"
      "s_add_u32 m0, s49, 3072\n\ts_add_u32 s48, s50, 0x30000\n\tbuffer_load_dwordx4 v21, s[40:43], s48 offen  lds\n\ts_add_u32 m0, s49, 4096\n\ts_add_u32 s48, s50, 0x40000\n\t"
      "buffer_load_dwordx4 v20, s[40:43], s48 offen  lds\n\ts_add_u32 m0, s49, 5120\n\ts_add_u32 s48, s50, 0x50000\n\tbuffer_load_dwordx4 v21, s[40:43], s48 offen  lds\n\t"
      "s_add_u32 m0, s49, 6144\n\ts_add_u32 s48, s50, 0x60000\n\tbuffer_load_dwordx4 v20, s[40:43], s48 offen  lds\n\ts_add_u32 m0, s49, 7168\n\ts_add_u32 s48, s50, 0x70000\n\t"
      "buffer_load_dwordx4 v21, s[40:43], s48 offen  lds\n\ts_waitcnt vmcnt(16)\n\tds_read_b64_tr_b16 a[160:161], v22 offset:16384\n\t"
      "ds_read_b64_tr_b16 a[162:163], v22 offset:16640\n\tds_read_b64_tr_b16 a[164:165], v22 offset:20480\n\tds_read_b64_tr_b16 a[166:167], v22 offset:20736\n\t"
      "ds_read_b64_tr_b16 a[168:169], v23 offset:16384\n\tds_read_b64_tr_b16 a[170:171], v23 offset:16640\n\tds_read_b64_tr_b16 a[172:173], v23 offset:20480\n\t"
      "ds_read_b64_tr_b16 a[174:175], v23 offset:20736\n\tds_read_b64_tr_b16 v[112:113], v24 offset:16384\n\tds_read_b64_tr_b16 v[114:115], v24 offset:16640\n\t"
      "ds_read_b64_tr_b16 v[116:117], v24 offset:20480\n\tds_read_b64_tr_b16 v[118:119], v24 offset:20736\n\tds_read_b64_tr_b16 v[232:233], v25 offset:16384\n\t"
      "ds_read_b64_tr_b16 v[234:235], v25 offset:16640\n\tds_read_b64_tr_b16 v[236:237], v25 offset:20480\n\tds_read_b64_tr_b16 v[238:239], v25 offset:20736\n\t"
      "s_waitcnt lgkmcnt(0)\n\ts_add_u32 s50, s57, s67\n\ts_add_u32 m0, s49, 8192\n\ts_add_u32 s48, s50, 0x0\n\tbuffer_load_dwordx4 v20, s[40:43], s48 offen  lds\n\t"
      "s_add_u32 m0, s49, 9216\n\ts_add_u32 s48, s50, 0x10000\n\tbuffer_load_dwordx4 v21, s[40:43], s48 offen  lds\n\ts_add_u32 m0, s49, 10240\n\ts_add_u32 s48, s50, 0x20000\n\t"
      "buffer_load_dwordx4 v20, s[40:43], s48 offen  lds\n\ts_add_u32 m0, s49, 11264\n\ts_add_u32 s48, s50, 0x30000\n\tbuffer_load_dwordx4 v21, s[40:43], s48 offen  lds\n\t"
      "s_add_u32 m0, s49, 12288\n\ts_add_u32 s48, s50, 0x40000\n\tbuffer_load_dwordx4 v20, s[40:43], s48 offen  lds\n\ts_add_u32 m0, s49, 13312\n\t"
      "s_add_u32 s48, s50, 0x50000\n\tbuffer_load_dwordx4 v21, s[40:43], s48 offen  lds\n\ts_add_u32 m0, s49, 14336\n\ts_add_u32 s48, s50, 0x60000\n\t"
      "buffer_load_dwordx4 v20, s[40:43], s48 offen  lds\n\ts_add_u32 m0, s49, 15360\n\ts_add_u32 s48, s50, 0x70000\n\tbuffer_load_dwordx4 v21, s[40:43], s48 offen  lds\n\t"
      "s_waitcnt vmcnt(16)\n\tds_read_b64_tr_b16 a[176:177], v22 offset:24576\n\tds_read_b64_tr_b16 a[178:179], v22 offset:24832\n\t"
      "ds_read_b64_tr_b16 a[180:181], v22 offset:28672\n\tds_read_b64_tr_b16 a[182:183], v22 offset:28928\n\tds_read_b64_tr_b16 a[184:185], v23 offset:24576\n\t"
      "ds_read_b64_tr_b16 a[186:187], v23 offset:24832\n\tds_read_b64_tr_b16 a[188:189], v23 offset:28672\n\tds_read_b64_tr_b16 a[190:191], v23 offset:28928\n\t"
      "ds_read_b64_tr_b16 v[120:121], v24 offset:24576\n\tds_read_b64_tr_b16 v[122:123], v24 offset:24832\n\tds_read_b64_tr_b16 v[124:125], v24 offset:28672\n\t"
      "ds_read_b64_tr_b16 v[126:127], v24 offset:28928\n\tds_read_b64_tr_b16 v[240:241], v25 offset:24576\n\tds_read_b64_tr_b16 v[242:243], v25 offset:24832\n\t"
      "ds_read_b64_tr_b16 v[244:245], v25 offset:28672\n\tds_read_b64_tr_b16 v[246:247], v25 offset:28928\n\ts_waitcnt lgkmcnt(0)\n\ts_add_u32 s50, s58, s67\n\t"
      "s_add_u32 m0, s49, 16384\n\ts_add_u32 s48, s50, 0x0\n\tbuffer_load_dwordx4 v20, s[40:43], s48 offen  lds\n\ts_add_u32 m0, s49, 17408\n\ts_add_u32 s48, s50, 0x10000\n\t"
      "buffer_load_dwordx4 v21, s[40:43], s48 offen  lds\n\ts_add_u32 m0, s49, 18432\n\ts_add_u32 s48, s50, 0x20000\n\tbuffer_load_dwordx4 v20, s[40:43], s48 offen  lds\n\t"
      "s_add_u32 m0, s49, 19456\n\ts_add_u32 s48, s50, 0x30000\n\tbuffer_load_dwordx4 v21, s[40:43], s48 offen  lds\n\ts_add_u32 m0, s49, 20480\n\t"
      "s_add_u32 s48, s50, 0x40000\n\tbuffer_load_dwordx4 v20, s[40:43], s48 offen  lds\n\ts_add_u32 m0, s49, 21504\n\ts_add_u32 s48, s50, 0x50000\n\t"
      "buffer_load_dwordx4 v21, s[40:43], s48 offen  lds\n\ts_add_u32 m0, s49, 22528\n\ts_add_u32 s48, s50, 0x60000\n\tbuffer_load_dwordx4 v20, s[40:43], s48 offen  lds\n\t"
      "s_add_u32 m0, s49, 23552\n\ts_add_u32 s48, s50, 0x70000\n\tbuffer_load_dwordx4 v21, s[40:43], s48 offen  lds\n\ts_waitcnt vmcnt(16)\n\t"
      "ds_read_b64_tr_b16 a[192:193], v22 offset:0\n\tds_read_b64_tr_b16 a[194:195], v22 offset:256\n\tds_read_b64_tr_b16 a[196:197], v22 offset:4096\n\t"
      "ds_read_b64_tr_b16 a[198:199], v22 offset:4352\n\tds_read_b64_tr_b16 a[200:201], v23 offset:0\n\tds_read_b64_tr_b16 a[202:203], v23 offset:256\n\t"
      "ds_read_b64_tr_b16 a[204:205], v23 offset:4096\n\tds_read_b64_tr_b16 a[206:207], v23 offset:4352\n\tds_read_b64_tr_b16 v[128:129], v24 offset:0\n\t"
      "ds_read_b64_tr_b16 v[130:131], v24 offset:256\n\tds_read_b64_tr_b16 v[132:133], v24 offset:4096\n\tds_read_b64_tr_b16 v[134:135], v24 offset:4352\n\t"
      "ds_read_b64_tr_b16 v[248:249], v25 offset:0\n\tds_read_b64_tr_b16 v[250:251], v25 offset:256\n\tds_read_b64_tr_b16 v[252:253], v25 offset:4096\n\t"
      "ds_read_b64_tr_b16 v[254:255], v25 offset:4352\n\ts_waitcnt lgkmcnt(0)\n\tds_write_b128 v5, v[152:155] offset:0\n\tds_write_b128 v5, v[156:159] offset:1024\n\t"
      "ds_write_b128 v5, v[160:163] offset:2048\n\tds_write_b128 v5, v[164:167] offset:3072\n\tds_write_b128 v5, v[168:171] offset:4096\n\t"
      "ds_write_b128 v5, v[172:175] offset:5120\n\tds_write_b128 v5, v[176:179] offset:6144\n\tds_write_b128 v5, v[180:183] offset:7168\n\ts_add_u32 s50, s59, s67\n\t"
      "s_add_u32 m0, s49, 24576\n\ts_add_u32 s48, s50, 0x0\n\tbuffer_load_dwordx4 v20, s[40:43], s48 offen  lds\n\ts_add_u32 m0, s49, 25600\n\ts_add_u32 s48, s50, 0x10000\n\t"
      "buffer_load_dwordx4 v21, s[40:43], s48 offen  lds\n\ts_add_u32 m0, s49, 26624\n\ts_add_u32 s48, s50, 0x20000\n\tbuffer_load_dwordx4 v20, s[40:43], s48 offen  lds\n\t"
      "s_add_u32 m0, s49, 27648\n\ts_add_u32 s48, s50, 0x30000\n\tbuffer_load_dwordx4 v21, s[40:43], s48 offen  lds\n\ts_add_u32 m0, s49, 28672\n\t"
      "s_add_u32 s48, s50, 0x40000\n\tbuffer_load_dwordx4 v20, s[40:43], s48 offen  lds\n\ts_add_u32 m0, s49, 29696\n\ts_add_u32 s48, s50, 0x50000\n\t"
      "buffer_load_dwordx4 v21, s[40:43], s48 offen  lds\n\ts_add_u32 m0, s49, 30720\n\ts_add_u32 s48, s50, 0x60000\n\tbuffer_load_dwordx4 v20, s[40:43], s48 offen  lds\n\t"
      "s_add_u32 m0, s49, 31744\n\ts_add_u32 s48, s50, 0x70000\n\tbuffer_load_dwordx4 v21, s[40:43], s48 offen  lds\n\ts_waitcnt vmcnt(16)\n\t"
      "ds_read_b64_tr_b16 a[208:209], v22 offset:8192\n\tds_read_b64_tr_b16 a[210:211], v22 offset:8448\n\tds_read_b64_tr_b16 a[212:213], v22 offset:12288\n\t"
      "ds_read_b64_tr_b16 a[214:215], v22 offset:12544\n\tds_read_b64_tr_b16 a[216:217], v23 offset:8192\n\tds_read_b64_tr_b16 a[218:219], v23 offset:8448\n\t"
      "ds_read_b64_tr_b16 a[220:221], v23 offset:12288\n\tds_read_b64_tr_b16 a[222:223], v23 offset:12544\n\tds_read_b64_tr_b16 v[136:137], v24 offset:8192\n\t"
      "ds_read_b64_tr_b16 v[138:139], v24 offset:8448\n\tds_read_b64_tr_b16 v[140:141], v24 offset:12288\n\tds_read_b64_tr_b16 v[142:143], v24 offset:12544\n\t"
      "ds_read_b64_tr_b16 v[160:161], v25 offset:8192\n\tds_read_b64_tr_b16 v[162:163], v25 offset:8448\n\tds_read_b64_tr_b16 v[164:165], v25 offset:12288\n\t"
      "ds_read_b64_tr_b16 v[166:167], v25 offset:12544\n\ts_waitcnt lgkmcnt(0)\n\tds_write_b128 v5, v[184:187] offset:8192\n\tds_write_b128 v5, v[188:191] offset:9216\n\t"
      "ds_write_b128 v5, v[192:195] offset:10240\n\tds_write_b128 v5, v[196:199] offset:11264\n\tds_write_b128 v5, v[200:203] offset:12288\n\t"
      "ds_write_b128 v5, v[204:207] offset:13312\n\tds_write_b128 v5, v[208:211] offset:14336\n\tds_write_b128 v5, v[212:215] offset:15360\n\ts_waitcnt vmcnt(8)\n\t"
      "ds_read_b64_tr_b16 a[224:225], v22 offset:16384\n\tds_read_b64_tr_b16 a[226:227], v22 offset:16640\n\tds_read_b64_tr_b16 a[228:229], v22 offset:20480\n\t"
      "ds_read_b64_tr_b16 a[230:231], v22 offset:20736\n\tds_read_b64_tr_b16 a[232:233], v23 offset:16384\n\tds_read_b64_tr_b16 a[234:235], v23 offset:16640\n\t"
      "ds_read_b64_tr_b16 a[236:237], v23 offset:20480\n\tds_read_b64_tr_b16 a[238:239], v23 offset:20736\n\tds_read_b64_tr_b16 v[144:145], v24 offset:16384\n\t"
      "ds_read_b64_tr_b16 v[146:147], v24 offset:16640\n\tds_read_b64_tr_b16 v[148:149], v24 offset:20480\n\tds_read_b64_tr_b16 v[150:151], v24 offset:20736\n\t"
      "ds_read_b64_tr_b16 v[168:169], v25 offset:16384\n\tds_read_b64_tr_b16 v[170:171], v25 offset:16640\n\tds_read_b64_tr_b16 v[172:173], v25 offset:20480\n\t"
      "ds_read_b64_tr_b16 v[174:175], v25 offset:20736\n\ts_waitcnt lgkmcnt(0)\n\tds_write_b128 v5, v[216:219] offset:16384\n\tds_write_b128 v5, v[220:223] offset:17408\n\t"
      "ds_write_b128 v5, v[224:227] offset:18432\n\tds_write_b128 v5, v[228:231] offset:19456\n\tds_write_b128 v5, v[232:235] offset:20480\n\t"
      "ds_write_b128 v5, v[236:239] offset:21504\n\tds_write_b128 v5, v[240:243] offset:22528\n\tds_write_b128 v5, v[244:247] offset:23552\n\ts_cmp_lt_u32 s68, 1\n\t"
      "s_cbranch_scc1 7f\n\ts_cmp_eq_u32 s68, 2\n\ts_cbranch_scc1 6f\n\t"
      "5:\n\ts_buffer_load_dword s48, s[12:15], s69 glc\n\ts_waitcnt lgkmcnt(0)\n\ts_cmp_eq_u32 s48, s71\n\ts_cbranch_scc1 6f\n\ts_cmp_eq_u32 s18, 0\n\ts_cbranch_scc1 6f\n\t"
      "s_sub_u32 s18, s18, 1\n\ts_branch 5b\n\t"
      "6:\n\tv_mov_b32 v17, 1\n\ts_mov_b64 exec, 1\n\tbuffer_atomic_add v17, off, s[12:15], s70\n\ts_mov_b64 exec, -1\n\ts_mov_b32 s68, 0\n\t"
      "7:\n\ts_waitcnt vmcnt(0)\n\tds_read_b64_tr_b16 a[240:241], v22 offset:24576\n\tds_read_b64_tr_b16 a[242:243], v22 offset:24832\n\t"
      "ds_read_b64_tr_b16 a[244:245], v22 offset:28672\n\tds_read_b64_tr_b16 a[246:247], v22 offset:28928\n\tds_read_b64_tr_b16 a[248:249], v23 offset:24576\n\t"
      "ds_read_b64_tr_b16 a[250:251], v23 offset:24832\n\tds_read_b64_tr_b16 a[252:253], v23 offset:28672\n\tds_read_b64_tr_b16 a[254:255], v23 offset:28928\n\t"
      "ds_read_b64_tr_b16 v[152:153], v24 offset:24576\n\tds_read_b64_tr_b16 v[154:155], v24 offset:24832\n\tds_read_b64_tr_b16 v[156:157], v24 offset:28672\n\t"
      "ds_read_b64_tr_b16 v[158:159], v24 offset:28928\n\tds_read_b64_tr_b16 v[176:177], v25 offset:24576\n\tds_read_b64_tr_b16 v[178:179], v25 offset:24832\n\t"
      "ds_read_b64_tr_b16 v[180:181], v25 offset:28672\n\tds_read_b64_tr_b16 v[182:183], v25 offset:28928\n\ts_waitcnt lgkmcnt(0)\n\t"
      "ds_write_b128 v5, v[248:251] offset:24576\n\tds_write_b128 v5, v[252:255] offset:25600\n\tds_write_b128 v5, v[160:163] offset:26624\n\t"
      "ds_write_b128 v5, v[164:167] offset:27648\n\tds_write_b128 v5, v[168:171] offset:28672\n\tds_write_b128 v5, v[172:175] offset:29696\n\t"
      "ds_write_b128 v5, v[176:179] offset:30720\n\tds_write_b128 v5, v[180:183] offset:31744\n\t"
      "v_mov_b64 v[30:31], 0\n\tv_mov_b32 v29, 160000\n\tds_write_b64 v29, v[30:31]\n\t"
      TKS("s_lshr_b32 s91, s62, 3\n\ts_and_b32 s91, s91, 3\n\t")
      "s_waitcnt lgkmcnt(0)\n\ts_barrier\n\t"
      STS("s_memrealtime vcc\n\ts_waitcnt lgkmcnt(0)\n\tv_mov_b32 v31, vcc_lo\n\tv_mov_b32 v30, 161024\n\tds_write_b32 v30, v31\n\t")
      "1:\n\tbuffer_load_dword v29, off, s[12:15], s63 sc1\n\ts_waitcnt vmcnt(0)\n\tv_readfirstlane_b32 s49, v29\n\ts_bitcmp1_b32 s49, 0\n\ts_cbranch_scc1 2f\n\t"
      "s_cmp_eq_u32 s18, 0\n\ts_cbranch_scc1 2f\n\ts_sub_u32 s18, s18, 1\n\ts_sleep 2\n\ts_branch 1b\n\t"
      "2:\n\ts_and_b32 s48, s49, 3\n\ts_cmp_lg_u32 s48, 3\n\ts_cbranch_scc1 4f\n\ts_cmp_eq_u32 s62, 0\n\ts_cbranch_scc1 4f\n\t"
      "s_waitcnt lgkmcnt(0)\n\ts_barrier\n\ts_mov_b64 s[0:1], s[60:61]\n\ts_mov_b32 s2, s62\n\ts_lshl_b32 s48, s16, 6\n\tv_mbcnt_lo_u32_b32 v0, -1, 0\n\t"
      "v_mbcnt_hi_u32_b32 v0, -1, v0\n\tv_add_u32 v0, s48, v0\n\ts_getpc_b64 s[4:5]\n\t"
      "3:\n\ts_sub_u32 s4, s4, 3b-mk_mega\n\ts_subb_u32 s5, s5, 0\n\ts_setpc_b64 s[4:5]\n\t"
      "4:\n\ts_lshr_b32 s64, s64, 9\n\ts_lshr_b32 s65, s65, 9\n\ts_lshr_b32 s66, s66, 9\n\ts_lshr_b32 s67, s67, 9\n\ts_lshl_b32 s48, s49, 10\n\ts_and_b32 s48, s48, 0x2000\n\t"
      "v_or_b32 v0, s48, v0\n\tv_or_b32 v14, s48, v14\n\tv_or_b32 v15, s48, v15\n\ts_mov_b64 s[72:73], s[24:25]\n\ts_mov_b64 s[56:57], s[8:9]\n\ts_mov_b32 s59, s11\n\t"
      "s_mov_b64 s[24:25], s[8:9]\n\ts_mov_b32 s27, s11\n\ts_add_u32 s60, s16, 1\n\ts_bitcmp1_b32 s60, 1\n\ts_cselect_b32 s60, s10, 0\n\ts_lshl_b32 s49, s49, 11\n\t"
      "s_and_b32 s49, s49, 0x2000\n\ts_xor_b32 s22, s22, s49\n\ts_xor_b32 s31, s31, s49\n\ts_or_b32 s45, s45, s48\n\ts_cmp_eq_u32 s16, 3\n\ts_cselect_b32 s49, s48, 0\n\t"
      "v_xor_b32 v12, s49, v12\n\ts_cbranch_scc1 9f\n\tv_or_b32 v10, s48, v10\n\tv_or_b32 v11, s48, v11\n\ts_cmp_lg_u32 s16, 0\n\ts_cbranch_scc1 69f\n\t"
      "9:\n\ts_mov_b32 s40, s19\n\ts_memtime vcc\n\ts_memrealtime s[32:33]\n\ts_nop 1\n\ts_memrealtime s[34:35]\n\ts_nop 1\n\ts_memrealtime s[36:37]\n\ts_nop 1\n\t"
      "s_memrealtime s[38:39]\n\ts_nop 1\n\ts_memrealtime s[46:47]\n\ts_nop 1\n\ts_memrealtime s[52:53]\n\ts_nop 1\n\ts_memrealtime s[68:69]\n\ts_nop 1\n\t"
      "s_memrealtime s[70:71]\n\ts_nop 1\n\ts_waitcnt lgkmcnt(0)\n\ts_add_u32 s34, s34, s32\n\ts_add_u32 s34, s34, s36\n\ts_add_u32 s34, s34, s38\n\ts_add_u32 s34, s34, s46\n\t"
      "s_add_u32 s34, s34, s52\n\ts_add_u32 s34, s34, s68\n\ts_add_u32 s34, s34, s70\n\ts_lshl_b32 s36, s32, 3\n\ts_sub_u32 s34, s34, s36\n\ts_lshr_b32 s34, s34, 1\n\t"
      "s_lshl2_add_u32 s34, s32, s34\n\ts_add_u32 s34, s34, s40\n\ts_sub_u32 s36, s34, s43\n\ts_sub_u32 s37, vcc_lo, s44\n\ts_mov_b32 s43, s34\n\ts_mov_b32 s44, vcc_lo\n\t"
      "v_cvt_f32_u32 v17, s36\n\tv_cvt_f32_u32 v18, s37\n\tv_rcp_f32 v17, v17\n\tv_mul_f32 v18, 0x45800000, v18\n\tv_mul_f32 v17, v17, v18\n\tv_cvt_u32_f32 v17, v17\n\t"
      "s_cmp_lt_u32 s36, 300\n\tv_readfirstlane_b32 s34, v17\n\ts_cselect_b32 s34, 0, s34\n\ts_sub_u32 s36, s34, 10240\n\ts_cmp_gt_u32 s36, 16400\n\t"
      "s_cselect_b32 s34, s30, s34\n\ts_add_u32 s30, s30, s34\n\ts_lshr_b32 s30, s30, 1\n\ts_mov_b32 s30, 23040\n\ts_sub_u32 s19, s43, 0x100000\n\ts_mov_b32 s62, 440\n\t"
      "s_mov_b32 s63, 0\n\ts_cmp_eq_u32 s16, 3\n\ts_cbranch_scc1 65f\n\ts_mov_b32 s30, 11650\n\ts_mov_b32 s58, 0\n\ts_branch 66f\n\t"
      "10:\n\ts_cmp_eq_u32 s16, 3\n\ts_cbranch_scc1 8f\n\tds_read_b128 v[160:163], v5 offset:0\n\tds_read_b128 v[164:167], v5 offset:1024\n\t"
      "ds_read_b128 v[168:171], v5 offset:2048\n\tds_read_b128 v[172:175], v5 offset:3072\n\tds_read_b128 v[176:179], v5 offset:4096\n\t"
      "ds_read_b128 v[180:183], v5 offset:5120\n\tds_read_b128 v[184:187], v5 offset:6144\n\tds_read_b128 v[188:191], v5 offset:7168\n\t"
      "ds_read_b128 v[192:195], v5 offset:8192\n\tds_read_b128 v[196:199], v5 offset:9216\n\tds_read_b128 v[200:203], v5 offset:10240\n\t"
      "ds_read_b128 v[204:207], v5 offset:11264\n\tds_read_b128 v[208:211], v5 offset:12288\n\tds_read_b128 v[212:215], v5 offset:13312\n\t"
      "ds_read_b128 v[216:219], v5 offset:14336\n\tds_read_b128 v[220:223], v5 offset:15360\n\tv_mov_b64 v[224:225], 0\n\tv_mov_b64 v[226:227], 0\n\tv_mov_b64 v[228:229], 0\n\t"
      "v_mov_b64 v[230:231], 0\n\tv_mov_b64 v[232:233], 0\n\tv_mov_b64 v[234:235], 0\n\tv_mov_b64 v[236:237], 0\n\tv_mov_b64 v[238:239], 0\n\ts_branch 9f\n\t"
      "8:\n\ts_waitcnt lgkmcnt(0)\n\t"
      "78:\n\ts_memrealtime vcc\n\ts_waitcnt lgkmcnt(0)\n\ts_lshl2_add_u32 vcc_lo, vcc_lo, 110\n\ts_sub_u32 s48, s19, vcc_lo\n\ts_cmp_lt_u32 s48, 4000\n\ts_cbranch_scc1 78b\n\t"
      "s_memtime vcc\n\ts_memrealtime s[32:33]\n\ts_nop 1\n\ts_memrealtime s[34:35]\n\ts_nop 1\n\ts_memrealtime s[36:37]\n\ts_nop 1\n\ts_memrealtime s[38:39]\n\ts_nop 1\n\t"
      "s_memrealtime s[46:47]\n\ts_nop 1\n\ts_memrealtime s[52:53]\n\ts_nop 1\n\ts_memrealtime s[68:69]\n\ts_nop 1\n\ts_memrealtime s[70:71]\n\ts_nop 1\n\t"
      "s_waitcnt lgkmcnt(0)\n\ts_add_u32 s34, s34, s32\n\ts_add_u32 s34, s34, s36\n\ts_add_u32 s34, s34, s38\n\ts_add_u32 s34, s34, s46\n\ts_add_u32 s34, s34, s52\n\t"
      "s_add_u32 s34, s34, s68\n\ts_add_u32 s34, s34, s70\n\ts_lshl_b32 s36, s32, 3\n\ts_sub_u32 s34, s34, s36\n\ts_lshr_b32 s34, s34, 1\n\ts_lshl2_add_u32 s34, s32, s34\n\t"
      "s_add_u32 s34, s34, s40\n\ts_sub_u32 s36, s34, s43\n\ts_sub_u32 s37, vcc_lo, s44\n\ts_mov_b32 s43, s34\n\ts_mov_b32 s44, vcc_lo\n\tv_cvt_f32_u32 v17, s36\n\t"
      "v_cvt_f32_u32 v18, s37\n\tv_rcp_f32 v17, v17\n\tv_mul_f32 v18, 0x45800000, v18\n\tv_mul_f32 v17, v17, v18\n\tv_cvt_u32_f32 v17, v17\n\ts_cmp_lt_u32 s36, 300\n\t"
      "v_readfirstlane_b32 s34, v17\n\ts_cselect_b32 s34, 0, s34\n\ts_sub_u32 s36, s34, 10240\n\ts_cmp_gt_u32 s36, 16400\n\ts_cselect_b32 s34, s30, s34\n\t"
      "s_add_u32 s30, s30, s34\n\ts_lshr_b32 s30, s30, 1\n\t"
      "9:\n\ts_and_b32 s48, s17, 3\n\ts_lshl_b32 s49, s48, 18\n\ts_add_u32 s49, s49, 0x200000\n\ts_lshl_b32 s41, s48, 18\n\ts_add_u32 s41, s41, 0x208000\n\t"
      "s_xor_b32 s48, s48, 2\n\ts_lshl_b32 s42, s48, 18\n\ts_add_u32 s42, s42, 0x208000\n\ts_add_u32 s68, s49, s64\n\ts_add_u32 s69, s49, s65\n\ts_add_u32 s70, s49, s66\n\t"
      "s_add_u32 s71, s49, s67\n\ts_add_u32 s49, s22, 0x1000\n\ts_waitcnt lgkmcnt(0)\n\ts_cmp_lg_u32 s16, 0\n\ts_cbranch_scc1 9f\n\ts_waitcnt vmcnt(0)\n\t"
      "ds_write_b64 v16, v[28:29]\n\tv_readfirstlane_b32 s54, v28\n\tv_readfirstlane_b32 s55, v29\n\ts_lshr_b32 s48, s20, 4\n\tv_mov_b32 v18, s48\n\ts_waitcnt lgkmcnt(0)\n\t"
      "s_barrier\n\t"
      TK0
      "buffer_load_dwordx4 v[240:243], v0, s[8:11], s68 offen sc1\n\ts_nop 10\n\ts_memtime vcc\n\tbuffer_load_dwordx4 v[244:247], v0, s[8:11], s69 offen sc1\n\t"
      "s_memrealtime s[32:33]\n\ts_nop 1\n\ts_memrealtime s[34:35]\n\ts_nop 1\n\ts_memrealtime s[36:37]\n\ts_nop 1\n\ts_memrealtime s[38:39]\n\ts_nop 1\n\t"
      "buffer_load_dwordx4 v[248:251], v0, s[8:11], s70 offen sc1\n\ts_memrealtime s[46:47]\n\ts_nop 1\n\ts_memrealtime s[52:53]\n\ts_nop 1\n\ts_memrealtime s[24:25]\n\t"
      "s_nop 1\n\ts_memrealtime s[26:27]\n\ts_nop 1\n\tbuffer_load_dwordx4 v[252:255], v0, s[8:11], s71 offen sc1\n\ts_waitcnt lgkmcnt(0)\n\ts_add_u32 s34, s34, s32\n\t"
      "s_add_u32 s34, s34, s36\n\ts_add_u32 s34, s34, s38\n\ts_add_u32 s34, s34, s46\n\ts_add_u32 s34, s34, s52\n\ts_add_u32 s34, s34, s24\n\ts_add_u32 s34, s34, s26\n\t"
      "s_lshl_b32 s36, s32, 3\n\ts_sub_u32 s34, s34, s36\n\ts_lshr_b32 s34, s34, 1\n\ts_lshl2_add_u32 s34, s32, s34\n\ts_add_u32 s34, s34, s40\n\ts_mov_b32 s35, vcc_lo\n\t"
      "s_branch 15f\n\t"
      "9:\n\ts_cmp_lg_u32 s16, 3\n\ts_cbranch_scc1 73f\n\ts_sub_u32 s48, s19, s43\n\ts_cmp_gt_u32 s48, 4000\n\ts_cselect_b32 s48, 0, s48\n\ts_mul_i32 s48, s48, s30\n\t"
      "s_lshr_b32 s48, s48, 12\n\ts_add_u32 s32, s48, s44\n\t"
      "71:\n\ts_memtime vcc\n\ts_waitcnt lgkmcnt(0)\n\t"
      "70:\n\ts_sub_u32 s48, s32, vcc_lo\n\ts_cmp_gt_i32 s48, 160\n\ts_cbranch_scc1 71b\n\ts_max_i32 s48, s48, 0\n\ts_and_b32 s48, s48, -4\n\ts_getpc_b64 vcc\n\t"
      "72:\n\ts_sub_u32 s48, 73f-72b, s48\n\ts_add_u32 vcc_lo, vcc_lo, s48\n\ts_addc_u32 vcc_hi, vcc_hi, 0\n\ts_setpc_b64 vcc\n\t.rept 40\n\ts_nop 0\n\t.endr\n\t"
      "73:\n\ts_barrier\n\t"
      TK0
      "buffer_load_dwordx4 v[240:243], v0, s[8:11], s68 offen sc1\n\ts_nop 11\n\tbuffer_load_dwordx4 v[244:247], v0, s[8:11], s69 offen sc1\n\ts_nop 11\n\t"
      "buffer_load_dwordx4 v[248:251], v0, s[8:11], s70 offen sc1\n\ts_nop 11\n\tbuffer_load_dwordx4 v[252:255], v0, s[8:11], s71 offen sc1\n\t"
      "ds_read_b64 v[18:19], v16\n\ts_waitcnt lgkmcnt(0)\n\tv_readfirstlane_b32 s48, v18\n\tv_readfirstlane_b32 s55, v19\n\ts_max_u32 s54, s54, s48\n\ts_cmp_gt_u32 s55, s17\n\t"
      "s_cselect_b32 s58, s60, 0\n\ts_min_u32 s48, s55, s54\n\ts_sub_u32 s48, s48, 1\n\ts_cmp_gt_i32 s48, s17\n\ts_cselect_b32 s26, s10, 0\n\ts_andn2_b32 s26, s26, s60\n\t"
      "s_sub_u32 s53, s31, 0x800000\n\t"
      "11:\n\ts_cmp_lt_u32 s16, 2\n\ts_cbranch_scc1 15f\n\ts_waitcnt vmcnt(3)\n\t"
      TK1
      "v_smfmac_f32_16x16x64_bf16 v[224:227], v[240:243], a[0:7], v6\n\tv_max3_u32 v17, v240, v241, v242\n\tv_smfmac_f32_16x16x64_bf16 v[228:231], v[240:243], a[8:15], v6\n\t"
      "v_max_u32 v17, v17, v243\n\tv_smfmac_f32_16x16x64_bf16 v[232:235], v[240:243], v[32:39], v6\n\tds_write_b128 v1, v[240:243] offset:0\n\t"
      "v_smfmac_f32_16x16x64_bf16 v[236:239], v[240:243], v[160:167], v6\n\ts_waitcnt vmcnt(2)\n\tv_cmp_eq_u32 vcc, -1, v17\n\t"
      "v_smfmac_f32_16x16x64_bf16 v[224:227], v[244:247], a[16:23], v6\n\tv_max3_u32 v17, v244, v245, v246\n\tv_smfmac_f32_16x16x64_bf16 v[228:231], v[244:247], a[24:31], v6\n\t"
      "s_cbranch_vccnz 86f\n\t"
      "24:\n\tv_max_u32 v17, v17, v247\n\tv_smfmac_f32_16x16x64_bf16 v[232:235], v[244:247], v[40:47], v6\n\tds_write_b128 v1, v[244:247] offset:1024\n\t"
      "v_smfmac_f32_16x16x64_bf16 v[236:239], v[244:247], v[168:175], v6\n\ts_waitcnt lgkmcnt(1)\n\ts_barrier\n\ts_waitcnt vmcnt(1)\n\tv_cmp_eq_u32 vcc, -1, v17\n\t"
      "v_smfmac_f32_16x16x64_bf16 v[224:227], v[248:251], a[32:39], v6\n\tv_max3_u32 v17, v248, v249, v250\n\tv_smfmac_f32_16x16x64_bf16 v[228:231], v[248:251], a[40:47], v6\n\t"
      "s_cbranch_vccnz 87f\n\t"
      "25:\n\tv_max_u32 v17, v17, v251\n\tv_smfmac_f32_16x16x64_bf16 v[232:235], v[248:251], v[48:55], v6\n\tds_write_b128 v1, v[248:251] offset:2048\n\t"
      "v_smfmac_f32_16x16x64_bf16 v[236:239], v[248:251], v[176:183], v6\n\ts_waitcnt vmcnt(0)\n\tv_cmp_eq_u32 vcc, -1, v17\n\t"
      "v_smfmac_f32_16x16x64_bf16 v[224:227], v[252:255], a[48:55], v6\n\tds_read_b128 v[20:23], v2 offset:0\n\tds_read_b128 v[24:27], v3 offset:0\n\t"
      "v_max3_u32 v17, v252, v253, v254\n\tv_smfmac_f32_16x16x64_bf16 v[228:231], v[252:255], a[56:63], v6\n\ts_cbranch_vccnz 88f\n\t"
      "26:\n\tv_max_u32 v17, v17, v255\n\tv_smfmac_f32_16x16x64_bf16 v[232:235], v[252:255], v[56:63], v6\n\tds_write_b128 v1, v[252:255] offset:3072\n\t"
      "v_smfmac_f32_16x16x64_bf16 v[236:239], v[252:255], v[184:191], v6\n\ts_waitcnt lgkmcnt(2)\n\t"
      TK2
      "v_cmp_eq_u32 vcc, -1, v17\n\tv_smfmac_f32_16x16x64_bf16 v[224:227], v[20:23], a[64:71], v6\n\tds_read_b128 v[28:31], v4 offset:0\n\t"
      "v_smfmac_f32_16x16x64_bf16 v[228:231], v[20:23], a[72:79], v6\n\ts_cbranch_vccnz 89f\n\t"
      "27:\n\tv_smfmac_f32_16x16x64_bf16 v[232:235], v[20:23], v[64:71], v6\n\ts_barrier\n\tv_smfmac_f32_16x16x64_bf16 v[236:239], v[20:23], v[192:199], v6\n\t"
      "s_waitcnt lgkmcnt(1)\n\ts_barrier\n\t"
      TK3
      "v_smfmac_f32_16x16x64_bf16 v[224:227], v[24:27], a[80:87], v6\n\tds_read_b128 v[20:23], v2 offset:1024\n\t"
      "v_smfmac_f32_16x16x64_bf16 v[228:231], v[24:27], a[88:95], v6\n\tds_read_b128 v[160:163], v5 offset:16384\n\tds_read_b128 v[164:167], v5 offset:17408\n\t"
      "v_smfmac_f32_16x16x64_bf16 v[232:235], v[24:27], v[72:79], v6\n\tv_smfmac_f32_16x16x64_bf16 v[236:239], v[24:27], v[200:207], v6\n\ts_waitcnt lgkmcnt(3)\n\t"
      "v_smfmac_f32_16x16x64_bf16 v[224:227], v[28:31], a[96:103], v6\n\tds_read_b128 v[24:27], v3 offset:1024\n\t"
      "v_smfmac_f32_16x16x64_bf16 v[228:231], v[28:31], a[104:111], v6\n\tds_read_b128 v[168:171], v5 offset:18432\n\tds_read_b128 v[172:175], v5 offset:19456\n\t"
      "v_smfmac_f32_16x16x64_bf16 v[232:235], v[28:31], v[80:87], v6\n\tv_smfmac_f32_16x16x64_bf16 v[236:239], v[28:31], v[208:215], v6\n\ts_waitcnt lgkmcnt(5)\n\t"
      "v_smfmac_f32_16x16x64_bf16 v[224:227], v[20:23], a[112:119], v6\n\tds_read_b128 v[28:31], v4 offset:1024\n\t"
      "v_smfmac_f32_16x16x64_bf16 v[228:231], v[20:23], a[120:127], v6\n\tds_read_b128 v[176:179], v5 offset:20480\n\tds_read_b128 v[180:183], v5 offset:21504\n\t"
      "v_smfmac_f32_16x16x64_bf16 v[232:235], v[20:23], v[88:95], v6\n\tbuffer_load_dwordx4 v[240:243], v12, s[56:59], s22 offen offset:0 sc1\n\t"
      "v_smfmac_f32_16x16x64_bf16 v[236:239], v[20:23], v[216:223], v6\n\ts_waitcnt lgkmcnt(5)\n\tv_smfmac_f32_16x16x64_bf16 v[224:227], v[24:27], a[128:135], v6\n\t"
      "ds_read_b128 v[20:23], v2 offset:2048\n\tv_smfmac_f32_16x16x64_bf16 v[228:231], v[24:27], a[136:143], v6\n\tds_read_b128 v[184:187], v5 offset:22528\n\t"
      "ds_read_b128 v[188:191], v5 offset:23552\n\tv_smfmac_f32_16x16x64_bf16 v[232:235], v[24:27], v[96:103], v6\n\t"
      "buffer_load_dwordx4 v[244:247], v12, s[56:59], s22 offen offset:2048 sc1\n\tv_smfmac_f32_16x16x64_bf16 v[236:239], v[24:27], v[160:167], v6\n\ts_waitcnt lgkmcnt(5)\n\t"
      "v_smfmac_f32_16x16x64_bf16 v[224:227], v[28:31], a[144:151], v6\n\tds_read_b128 v[24:27], v3 offset:2048\n\t"
      "v_smfmac_f32_16x16x64_bf16 v[228:231], v[28:31], a[152:159], v6\n\tds_read_b128 v[192:195], v5 offset:24576\n\tds_read_b128 v[196:199], v5 offset:25600\n\t"
      "v_smfmac_f32_16x16x64_bf16 v[232:235], v[28:31], v[104:111], v6\n\tbuffer_load_dwordx4 v[248:251], v12, s[56:59], s49 offen offset:0 sc1\n\t"
      "v_smfmac_f32_16x16x64_bf16 v[236:239], v[28:31], v[168:175], v6\n\ts_waitcnt lgkmcnt(5)\n\tv_smfmac_f32_16x16x64_bf16 v[224:227], v[20:23], a[160:167], v6\n\t"
      "ds_read_b128 v[28:31], v4 offset:2048\n\tv_smfmac_f32_16x16x64_bf16 v[228:231], v[20:23], a[168:175], v6\n\tds_read_b128 v[200:203], v5 offset:26624\n\t"
      "ds_read_b128 v[204:207], v5 offset:27648\n\tv_smfmac_f32_16x16x64_bf16 v[232:235], v[20:23], v[112:119], v6\n\t"
      "buffer_load_dwordx4 v[252:255], v12, s[56:59], s49 offen offset:2048 sc1\n\tv_smfmac_f32_16x16x64_bf16 v[236:239], v[20:23], v[176:183], v6\n\ts_waitcnt lgkmcnt(5)\n\t"
      "ds_read_b128 v[160:163], v9 offset:0\n\tv_smfmac_f32_16x16x64_bf16 v[224:227], v[24:27], a[176:183], v6\n\tds_read_b128 v[20:23], v2 offset:3072\n\t"
      "v_smfmac_f32_16x16x64_bf16 v[228:231], v[24:27], a[184:191], v6\n\tds_read_b128 v[208:211], v5 offset:28672\n\tds_read_b128 v[212:215], v5 offset:29696\n\t"
      "v_smfmac_f32_16x16x64_bf16 v[232:235], v[24:27], v[120:127], v6\n\tbuffer_load_dword v19, v11, s[24:27], s31 offen sc1\n\t"
      "v_smfmac_f32_16x16x64_bf16 v[236:239], v[24:27], v[184:191], v6\n\ts_waitcnt lgkmcnt(6)\n\tv_smfmac_f32_16x16x64_bf16 v[224:227], v[28:31], a[192:199], v6\n\t"
      "ds_read_b128 v[24:27], v3 offset:3072\n\tv_smfmac_f32_16x16x64_bf16 v[228:231], v[28:31], a[200:207], v6\n\tds_read_b128 v[216:219], v5 offset:30720\n\t"
      "ds_read_b128 v[220:223], v5 offset:31744\n\tv_smfmac_f32_16x16x64_bf16 v[232:235], v[28:31], v[128:135], v6\n\ts_cmp_eq_u32 s16, 2\n\t"
      "v_smfmac_f32_16x16x64_bf16 v[236:239], v[28:31], v[192:199], v6\n\ts_waitcnt lgkmcnt(5)\n\tds_read_b128 v[164:167], v9 offset:2048\n\t"
      "v_smfmac_f32_16x16x64_bf16 v[224:227], v[20:23], a[208:215], v6\n\tds_read_b128 v[28:31], v4 offset:3072\n\t"
      "v_smfmac_f32_16x16x64_bf16 v[228:231], v[20:23], a[216:223], v6\n\tds_read_b128 v[168:171], v9 offset:4096\n\tds_read_b128 v[172:175], v9 offset:6144\n\t"
      "v_smfmac_f32_16x16x64_bf16 v[232:235], v[20:23], v[136:143], v6\n\tbuffer_load_dword v19, v11, s[24:27], s53 offen sc1\n\t"
      "v_smfmac_f32_16x16x64_bf16 v[236:239], v[20:23], v[200:207], v6\n\ts_waitcnt lgkmcnt(6)\n\tv_smfmac_f32_16x16x64_bf16 v[224:227], v[24:27], a[224:231], v6\n\t"
      "ds_read_b128 v[176:179], v9 offset:256\n\tds_read_b128 v[180:183], v9 offset:2304\n\tv_smfmac_f32_16x16x64_bf16 v[228:231], v[24:27], a[232:239], v6\n\t"
      "s_waitcnt lgkmcnt(4)\n\tv_smfmac_f32_16x16x64_bf16 v[224:227], v[28:31], a[240:247], v6\n\tds_read_b128 v[184:187], v9 offset:4352\n\t"
      "ds_read_b128 v[188:191], v9 offset:6400\n\ts_nop 0\n\tv_smfmac_f32_16x16x64_bf16 v[228:231], v[28:31], a[248:255], v6\n\ts_nop 2\n\t"
      "v_smfmac_f32_16x16x64_bf16 v[232:235], v[24:27], v[144:151], v6\n\tv_add_f32 v224, v224, v225\n\tv_smfmac_f32_16x16x64_bf16 v[232:235], v[28:31], v[152:159], v6\n\t"
      "s_nop 0\n\tv_add_f32 v226, v226, v227\n\tv_smfmac_f32_16x16x64_bf16 v[236:239], v[24:27], v[208:215], v6\n\tv_add_f32 v228, v228, v229\n\t"
      "v_smfmac_f32_16x16x64_bf16 v[236:239], v[28:31], v[216:223], v6\n\t"
      TK4
      "v_add_f32 v230, v230, v231\n\tv_cvt_pk_bf16_f32 v224, v224, v228\n\tv_cvt_pk_bf16_f32 v226, v226, v230\n\tv_add_f32 v232, v232, v233\n\tv_add_f32 v234, v234, v235\n\t"
      "s_nop 2\n\tv_add_f32 v236, v236, v237\n\tv_add_f32 v238, v238, v239\n\tv_cvt_pk_bf16_f32 v225, v232, v236\n\tv_cvt_pk_bf16_f32 v227, v234, v238\n\t"
      "ds_write2_b64 v7, v[224:225], v[226:227] offset1:34\n\ts_waitcnt lgkmcnt(0)\n\ts_barrier\n\ts_cbranch_scc1 40f\n\ts_branch 60f\n\t"
      "15:\n\ts_waitcnt vmcnt(3)\n\t"
      TK1
      "v_smfmac_f32_16x16x64_bf16 v[224:227], v[240:243], a[0:7], v6\n\tds_write_b128 v1, v[240:243] offset:0\n\t"
      "v_smfmac_f32_16x16x64_bf16 v[228:231], v[240:243], a[8:15], v6\n\tv_max3_u32 v17, v240, v241, v242\n\tv_smfmac_f32_16x16x64_bf16 v[232:235], v[240:243], v[32:39], v6\n\t"
      "v_max_u32 v17, v17, v243\n\tv_smfmac_f32_16x16x64_bf16 v[236:239], v[240:243], v[160:167], v6\n\ts_waitcnt vmcnt(2)\n\tv_cmp_eq_u32 vcc, -1, v17\n\t"
      "v_smfmac_f32_16x16x64_bf16 v[224:227], v[244:247], a[16:23], v6\n\tds_write_b128 v1, v[244:247] offset:1024\n\t"
      "v_smfmac_f32_16x16x64_bf16 v[228:231], v[244:247], a[24:31], v6\n\ts_cbranch_vccnz 86f\n\t"
      "20:\n\tv_max3_u32 v17, v244, v245, v246\n\tv_smfmac_f32_16x16x64_bf16 v[232:235], v[244:247], v[40:47], v6\n\tv_max_u32 v17, v17, v247\n\t"
      "v_smfmac_f32_16x16x64_bf16 v[236:239], v[244:247], v[168:175], v6\n\ts_waitcnt lgkmcnt(1)\n\ts_barrier\n\ts_waitcnt vmcnt(1)\n\tv_cmp_eq_u32 vcc, -1, v17\n\t"
      "v_smfmac_f32_16x16x64_bf16 v[224:227], v[248:251], a[32:39], v6\n\tds_write_b128 v1, v[248:251] offset:2048\n\t"
      "v_smfmac_f32_16x16x64_bf16 v[228:231], v[248:251], a[40:47], v6\n\ts_cbranch_vccnz 87f\n\t"
      "21:\n\tv_max3_u32 v17, v248, v249, v250\n\tv_smfmac_f32_16x16x64_bf16 v[232:235], v[248:251], v[48:55], v6\n\tv_max_u32 v17, v17, v251\n\t"
      "v_smfmac_f32_16x16x64_bf16 v[236:239], v[248:251], v[176:183], v6\n\tds_read_b128 v[20:23], v2 offset:0\n\tds_read_b128 v[24:27], v3 offset:0\n\ts_waitcnt vmcnt(0)\n\t"
      "v_cmp_eq_u32 vcc, -1, v17\n\tv_smfmac_f32_16x16x64_bf16 v[224:227], v[252:255], a[48:55], v6\n\tds_write_b128 v1, v[252:255] offset:3072\n\t"
      "v_smfmac_f32_16x16x64_bf16 v[228:231], v[252:255], a[56:63], v6\n\ts_cbranch_vccnz 88f\n\t"
      "22:\n\tv_max3_u32 v17, v252, v253, v254\n\tv_smfmac_f32_16x16x64_bf16 v[232:235], v[252:255], v[56:63], v6\n\tv_max_u32 v17, v17, v255\n\t"
      "v_smfmac_f32_16x16x64_bf16 v[236:239], v[252:255], v[184:191], v6\n\tds_read_b128 v[28:31], v4 offset:0\n\ts_waitcnt lgkmcnt(3)\n\t"
      TK2
      "v_cmp_eq_u32 vcc, -1, v17\n\tv_smfmac_f32_16x16x64_bf16 v[224:227], v[20:23], a[64:71], v6\n\tv_smfmac_f32_16x16x64_bf16 v[228:231], v[20:23], a[72:79], v6\n\t"
      "s_cbranch_vccnz 89f\n\t"
      "23:\n\tv_smfmac_f32_16x16x64_bf16 v[232:235], v[20:23], v[64:71], v6\n\ts_barrier\n\ts_memtime s[46:47]\n\t"
      "v_smfmac_f32_16x16x64_bf16 v[236:239], v[20:23], v[192:199], v6\n\ts_waitcnt lgkmcnt(1)\n\ts_barrier\n\t"
      TK3
      "v_smfmac_f32_16x16x64_bf16 v[224:227], v[24:27], a[80:87], v6\n\tv_smfmac_f32_16x16x64_bf16 v[228:231], v[24:27], a[88:95], v6\n\ts_waitcnt lgkmcnt(0)\n\t"
      "s_sub_u32 s47, s46, s35\n\ts_mul_i32 s47, s47, s30\n\tv_smfmac_f32_16x16x64_bf16 v[232:235], v[24:27], v[72:79], v6\n\tds_read_b128 v[20:23], v2 offset:1024\n\t"
      "s_lshr_b32 s47, s47, 16\n\ts_add_u32 s21, s47, s34\n\tv_add_u32 v18, s21, v18\n\tv_smfmac_f32_16x16x64_bf16 v[236:239], v[24:27], v[200:207], v6\n\t"
      "ds_read_b128 v[160:163], v5 offset:16384\n\tds_read_b128 v[164:167], v5 offset:17408\n\tv_and_b32 v18, -2, v18\n\t"
      "v_smfmac_f32_16x16x64_bf16 v[224:227], v[28:31], a[96:103], v6\n\tbuffer_store_dword v18, v14, s[8:11], s41 offen sc1\n\t"
      "v_smfmac_f32_16x16x64_bf16 v[228:231], v[28:31], a[104:111], v6\n\tds_read_b128 v[168:171], v5 offset:18432\n\tds_read_b128 v[172:175], v5 offset:19456\n\t"
      "v_smfmac_f32_16x16x64_bf16 v[232:235], v[28:31], v[80:87], v6\n\tds_read_b128 v[24:27], v3 offset:1024\n\t"
      "v_smfmac_f32_16x16x64_bf16 v[236:239], v[28:31], v[208:215], v6\n\ts_waitcnt lgkmcnt(5)\n\tv_smfmac_f32_16x16x64_bf16 v[224:227], v[20:23], a[112:119], v6\n\t"
      "ds_read_b128 v[28:31], v4 offset:1024\n\tv_smfmac_f32_16x16x64_bf16 v[228:231], v[20:23], a[120:127], v6\n\tds_read_b128 v[176:179], v5 offset:20480\n\t"
      "ds_read_b128 v[180:183], v5 offset:21504\n\tv_smfmac_f32_16x16x64_bf16 v[232:235], v[20:23], v[88:95], v6\n\t"
      "buffer_load_dwordx4 v[240:243], v12, s[56:59], s22 offen offset:0 sc1\n\tv_smfmac_f32_16x16x64_bf16 v[236:239], v[20:23], v[216:223], v6\n\ts_waitcnt lgkmcnt(3)\n\t"
      "v_smfmac_f32_16x16x64_bf16 v[224:227], v[24:27], a[128:135], v6\n\tds_read_b128 v[20:23], v2 offset:2048\n\t"
      "v_smfmac_f32_16x16x64_bf16 v[228:231], v[24:27], a[136:143], v6\n\tds_read_b128 v[184:187], v5 offset:22528\n\tds_read_b128 v[188:191], v5 offset:23552\n\t"
      "v_smfmac_f32_16x16x64_bf16 v[232:235], v[24:27], v[96:103], v6\n\tbuffer_load_dwordx4 v[244:247], v12, s[56:59], s22 offen offset:2048 sc1\n\t"
      "v_smfmac_f32_16x16x64_bf16 v[236:239], v[24:27], v[160:167], v6\n\ts_waitcnt lgkmcnt(5)\n\tv_smfmac_f32_16x16x64_bf16 v[224:227], v[28:31], a[144:151], v6\n\t"
      "ds_read_b128 v[24:27], v3 offset:2048\n\tv_smfmac_f32_16x16x64_bf16 v[228:231], v[28:31], a[152:159], v6\n\tds_read_b128 v[192:195], v5 offset:24576\n\t"
      "ds_read_b128 v[196:199], v5 offset:25600\n\tv_smfmac_f32_16x16x64_bf16 v[232:235], v[28:31], v[104:111], v6\n\t"
      "buffer_load_dwordx4 v[248:251], v12, s[56:59], s49 offen offset:0 sc1\n\tv_smfmac_f32_16x16x64_bf16 v[236:239], v[28:31], v[168:175], v6\n\ts_waitcnt lgkmcnt(5)\n\t"
      "v_smfmac_f32_16x16x64_bf16 v[224:227], v[20:23], a[160:167], v6\n\tds_read_b128 v[28:31], v4 offset:2048\n\t"
      "v_smfmac_f32_16x16x64_bf16 v[228:231], v[20:23], a[168:175], v6\n\tds_read_b128 v[200:203], v5 offset:26624\n\tds_read_b128 v[204:207], v5 offset:27648\n\t"
      "v_smfmac_f32_16x16x64_bf16 v[232:235], v[20:23], v[112:119], v6\n\tbuffer_load_dwordx4 v[252:255], v12, s[56:59], s49 offen offset:2048 sc1\n\t"
      "v_smfmac_f32_16x16x64_bf16 v[236:239], v[20:23], v[176:183], v6\n\ts_waitcnt lgkmcnt(5)\n\tds_read_b128 v[160:163], v9 offset:0\n\t"
      "v_smfmac_f32_16x16x64_bf16 v[224:227], v[24:27], a[176:183], v6\n\tds_read_b128 v[20:23], v2 offset:3072\n\t"
      "v_smfmac_f32_16x16x64_bf16 v[228:231], v[24:27], a[184:191], v6\n\tds_read_b128 v[208:211], v5 offset:28672\n\tds_read_b128 v[212:215], v5 offset:29696\n\t"
      "v_smfmac_f32_16x16x64_bf16 v[232:235], v[24:27], v[120:127], v6\n\tv_smfmac_f32_16x16x64_bf16 v[236:239], v[24:27], v[184:191], v6\n\ts_waitcnt lgkmcnt(6)\n\t"
      "v_smfmac_f32_16x16x64_bf16 v[224:227], v[28:31], a[192:199], v6\n\tds_read_b128 v[24:27], v3 offset:3072\n\t"
      "v_smfmac_f32_16x16x64_bf16 v[228:231], v[28:31], a[200:207], v6\n\tds_read_b128 v[216:219], v5 offset:30720\n\tds_read_b128 v[220:223], v5 offset:31744\n\t"
      "v_smfmac_f32_16x16x64_bf16 v[232:235], v[28:31], v[128:135], v6\n\ts_cmp_eq_u32 s16, 0\n\tv_smfmac_f32_16x16x64_bf16 v[236:239], v[28:31], v[192:199], v6\n\t"
      "s_waitcnt lgkmcnt(5)\n\tds_read_b128 v[164:167], v9 offset:2048\n\tv_smfmac_f32_16x16x64_bf16 v[224:227], v[20:23], a[208:215], v6\n\t"
      "ds_read_b128 v[28:31], v4 offset:3072\n\tv_smfmac_f32_16x16x64_bf16 v[228:231], v[20:23], a[216:223], v6\n\tds_read_b128 v[168:171], v9 offset:4096\n\t"
      "ds_read_b128 v[172:175], v9 offset:6144\n\tv_smfmac_f32_16x16x64_bf16 v[232:235], v[20:23], v[136:143], v6\n\t"
      "v_smfmac_f32_16x16x64_bf16 v[236:239], v[20:23], v[200:207], v6\n\ts_waitcnt lgkmcnt(6)\n\tv_smfmac_f32_16x16x64_bf16 v[224:227], v[24:27], a[224:231], v6\n\t"
      "ds_read_b128 v[176:179], v9 offset:256\n\tds_read_b128 v[180:183], v9 offset:2304\n\tv_smfmac_f32_16x16x64_bf16 v[228:231], v[24:27], a[232:239], v6\n\t"
      "s_waitcnt lgkmcnt(4)\n\tv_smfmac_f32_16x16x64_bf16 v[224:227], v[28:31], a[240:247], v6\n\tds_read_b128 v[184:187], v9 offset:4352\n\t"
      "ds_read_b128 v[188:191], v9 offset:6400\n\ts_nop 0\n\tv_smfmac_f32_16x16x64_bf16 v[228:231], v[28:31], a[248:255], v6\n\ts_nop 2\n\t"
      "v_smfmac_f32_16x16x64_bf16 v[232:235], v[24:27], v[144:151], v6\n\tv_add_f32 v224, v224, v225\n\tv_smfmac_f32_16x16x64_bf16 v[232:235], v[28:31], v[152:159], v6\n\t"
      "s_nop 0\n\tv_add_f32 v226, v226, v227\n\tv_smfmac_f32_16x16x64_bf16 v[236:239], v[24:27], v[208:215], v6\n\tv_add_f32 v228, v228, v229\n\t"
      "v_smfmac_f32_16x16x64_bf16 v[236:239], v[28:31], v[216:223], v6\n\t"
      TK4
      "v_add_f32 v230, v230, v231\n\tv_cvt_pk_bf16_f32 v224, v224, v228\n\tv_cvt_pk_bf16_f32 v226, v226, v230\n\tv_add_f32 v232, v232, v233\n\tv_add_f32 v234, v234, v235\n\t"
      "s_nop 2\n\tv_add_f32 v236, v236, v237\n\tv_add_f32 v238, v238, v239\n\tv_cvt_pk_bf16_f32 v225, v232, v236\n\tv_cvt_pk_bf16_f32 v227, v234, v238\n\t"
      "ds_write2_b32 v7, v224, v225 offset1:4\n\tds_write2_b32 v7, v226, v227 offset0:8 offset1:12\n\ts_cbranch_scc0 39f\n\t"
      "14:\n\tv_mov_b64 v[228:229], 0\n\tv_mov_b64 v[230:231], 0\n\tv_mov_b64 v[232:233], 0\n\tv_mov_b64 v[234:235], 0\n\tv_mov_b64 v[236:237], 0\n\tv_mov_b64 v[238:239], 0\n\t"
      "s_mov_b32 vcc_lo, 0x55555555\n\ts_mov_b32 vcc_hi, 0x55555555\n\ts_waitcnt lgkmcnt(0)\n\ts_barrier\n\t"
      TK5
      "ds_read_b128 v[20:23], v1 offset:24576\n\tds_read_b128 v[24:27], v1 offset:25600\n\tds_read_b128 v[208:211], v8 offset:0\n\tds_read_b128 v[212:215], v8 offset:64\n\t"
      "ds_read_b128 v[216:219], v8 offset:128\n\tds_read_b128 v[220:223], v8 offset:192\n\ts_waitcnt lgkmcnt(5)\n\t"
      "v_smfmac_f32_16x16x64_bf16 v[232:235], v[20:23], v[160:167], v6\n\tv_smfmac_f32_16x16x64_bf16 v[236:239], v[20:23], v[176:183], v6\n\ts_waitcnt lgkmcnt(4)\n\t"
      "v_smfmac_f32_16x16x64_bf16 v[232:235], v[24:27], v[168:175], v6\n\tv_smfmac_f32_16x16x64_bf16 v[236:239], v[24:27], v[184:191], v6\n\ts_nop 1\n\ts_waitcnt lgkmcnt(2)\n\t"
      "v_smfmac_f32_16x16x64_bf16 v[228:231], v[20:23], v[208:215], v6\n\ts_nop 1\n\ts_waitcnt lgkmcnt(0)\n\tv_smfmac_f32_16x16x64_bf16 v[228:231], v[24:27], v[216:223], v6\n\t"
      "v_add_f32 v232, v232, v233\n\tv_add_f32 v234, v234, v235\n\tv_add_f32 v236, v236, v237\n\tv_add_f32 v238, v238, v239\n\t"
      "v_cndmask_b32_dpp v194, v234, v232, vcc quad_perm:[1,0,3,2] row_mask:0xf bank_mask:0xf\n\ts_not_b64 vcc, vcc\n\t"
      "v_cndmask_b32_dpp v195, v232, v234, vcc quad_perm:[1,0,3,2] row_mask:0xf bank_mask:0xf\n\t"
      "v_cndmask_b32_dpp v197, v236, v238, vcc quad_perm:[1,0,3,2] row_mask:0xf bank_mask:0xf\n\tv_add_f32 v228, v228, v229\n\tv_add_f32 v230, v230, v231\n\t"
      "v_max3_f32 v198, v194, v195, v197\n\tv_pk_mul_f32 v[194:195], v[194:195], s[28:29]\n\t"
      "v_cndmask_b32_dpp v193, v228, v230, vcc quad_perm:[1,0,3,2] row_mask:0xf bank_mask:0xf\n\ts_not_b64 vcc, vcc\n\t"
      "v_cndmask_b32_dpp v196, v238, v236, vcc quad_perm:[1,0,3,2] row_mask:0xf bank_mask:0xf\n\t"
      "v_cndmask_b32_dpp v192, v230, v228, vcc quad_perm:[1,0,3,2] row_mask:0xf bank_mask:0xf\n\tv_max3_f32 v198, v198, v196, v193\n\tv_max_f32 v198, v198, v192\n\t"
      "v_pk_mul_f32 v[196:197], v[196:197], s[28:29]\n\tv_pk_mul_f32 v[192:193], v[192:193], s[28:29]\n\tv_max_f32_dpp v198, v198, v198 row_ror:8 row_mask:0xf bank_mask:0xf\n\t"
      "s_nop 1\n\tv_max_f32_dpp v198, v198, v198 row_ror:4 row_mask:0xf bank_mask:0xf\n\ts_nop 1\n\tv_max_f32_dpp v198, v198, v198 row_ror:2 row_mask:0xf bank_mask:0xf\n\t"
      "v_mul_f32 v198, s28, v198\n\tv_pk_add_f32 v[192:193], v[192:193], v[198:199] op_sel_hi:[1,0] neg_lo:[0,1] neg_hi:[0,1]\n\t"
      "v_pk_add_f32 v[194:195], v[194:195], v[198:199] op_sel_hi:[1,0] neg_lo:[0,1] neg_hi:[0,1]\n\t"
      "v_pk_add_f32 v[196:197], v[196:197], v[198:199] op_sel_hi:[1,0] neg_lo:[0,1] neg_hi:[0,1]\n\tv_exp_f32 v192, v192\n\tv_exp_f32 v193, v193\n\tv_exp_f32 v194, v194\n\t"
      "v_exp_f32 v195, v195\n\tv_exp_f32 v196, v196\n\tv_pk_fma_f32 v[198:199], v[192:193], v[12:13], v[194:195] op_sel:[0,1,0]\n\tv_exp_f32 v197, v197\n\t"
      "v_add_f32 v198, v198, v196\n\tv_add_f32 v199, v199, v197\n\tv_add_f32 v198, v198, v199\n\ts_nop 1\n\t"
      "v_add_f32_dpp v198, v198, v198 row_ror:8 row_mask:0xf bank_mask:0xf\n\ts_nop 1\n\tv_add_f32_dpp v198, v198, v198 row_ror:4 row_mask:0xf bank_mask:0xf\n\ts_nop 1\n\t"
      "v_add_f32_dpp v198, v198, v198 row_ror:2 row_mask:0xf bank_mask:0xf\n\tv_rcp_f32 v198, v198\n\ts_nop 0\n\t"
      "v_pk_mul_f32 v[194:195], v[194:195], v[198:199] op_sel_hi:[1,0]\n\tv_pk_mul_f32 v[196:197], v[196:197], v[198:199] op_sel_hi:[1,0]\n\t"
      "v_cvt_pk_bf16_f32 v200, v194, v195\n\tv_cvt_pk_bf16_f32 v201, v196, v197\n\tv_pk_mul_f32 v[192:193], v[192:193], v[198:199] op_sel_hi:[1,0]\n\t"
      "buffer_store_dwordx2 v[200:201], v10, s[8:11], s41 offen sc1\n\tv_cvt_pk_bf16_f32 v202, v192, v193\n\tbuffer_store_dword v202, v11, s[8:11], s41 offen sc1\n\t"
      TK6
      "s_memtime vcc\n\ts_memrealtime s[32:33]\n\ts_nop 1\n\ts_memrealtime s[34:35]\n\ts_nop 1\n\ts_memrealtime s[36:37]\n\ts_nop 1\n\ts_memrealtime s[38:39]\n\ts_nop 1\n\t"
      "s_memrealtime s[46:47]\n\ts_nop 1\n\ts_memrealtime s[52:53]\n\ts_nop 1\n\ts_memrealtime s[68:69]\n\ts_nop 1\n\ts_memrealtime s[70:71]\n\ts_nop 1\n\t"
      "s_waitcnt lgkmcnt(0)\n\ts_add_u32 s34, s34, s32\n\ts_add_u32 s34, s34, s36\n\ts_add_u32 s34, s34, s38\n\ts_add_u32 s34, s34, s46\n\ts_add_u32 s34, s34, s52\n\t"
      "s_add_u32 s34, s34, s68\n\ts_add_u32 s34, s34, s70\n\ts_lshl_b32 s36, s32, 3\n\ts_sub_u32 s34, s34, s36\n\ts_lshr_b32 s34, s34, 1\n\ts_lshl2_add_u32 s34, s32, s34\n\t"
      "s_add_u32 s34, s34, s40\n\ts_sub_u32 s36, s34, s43\n\ts_sub_u32 s37, vcc_lo, s44\n\ts_mov_b32 s43, s34\n\ts_mov_b32 s44, vcc_lo\n\tv_cvt_f32_u32 v17, s37\n\t"
      "v_cvt_f32_u32 v18, s36\n\tv_rcp_f32 v17, v17\n\tv_mul_f32 v18, 0x47800000, v18\n\tv_mul_f32 v17, v17, v18\n\tv_cvt_u32_f32 v17, v17\n\ts_cmp_lt_u32 s36, 300\n\t"
      "v_readfirstlane_b32 s34, v17\n\ts_cselect_b32 s34, 0, s34\n\ts_sub_u32 s36, s34, 10000\n\ts_cmp_gt_u32 s36, 16400\n\ts_cselect_b32 s34, s30, s34\n\t"
      "s_add_u32 s30, s30, s34\n\ts_lshr_b32 s30, s30, 1\n\ts_sub_u32 s48, s43, s21\n\ts_min_u32 s48, s48, 800\n\t"
      STS("s_add_u32 s51, s51, s48\n\t")
      STC("s_lshl_b32 s32, s17, 3\n\ts_add_u32 s32, s32, 161056\n\tv_mov_b32 v17, s32\n\ts_lshr_b32 s32, s20, 4\n\ts_sub_u32 s32, s48, s32\n\tv_mov_b32 v18, s32\n\tds_write_b32 v17, v18\n\ts_waitcnt vmcnt(0)\n\ts_memtime s[32:33]\n\ts_waitcnt lgkmcnt(0)\n\ts_sub_u32 s32, s32, s44\n\tv_mov_b32 v18, s32\n\tds_write_b32 v17, v18 offset:4\n\t")
      "s_lshl_b32 s48, s48, 4\n\ts_sub_i32 s48, s48, s20\n\ts_ashr_i32 s48, s48, 2\n\ts_add_i32 s20, s20, s48\n\tv_mov_b64 v[204:205], -1\n\t"
      "buffer_store_dwordx2 v[204:205], v10, s[8:11], s42 offen sc1\n\tbuffer_store_dword v204, v11, s[8:11], s42 offen sc1\n\t"
      "buffer_store_dword v204, v14, s[8:11], s42 offen sc1\n\t"
      "66:\n\tv_mov_b32 v28, s54\n\tv_mov_b32 v29, s55\n\ts_min_u32 s48, s55, s54\n\ts_add_u32 s49, s17, 2\n\ts_cmp_gt_u32 s48, s49\n\ts_cbranch_scc1 69f\n\t"
      "s_add_u32 s48, s45, 0x1000\n\tbuffer_load_dword v28, off, s[8:11], s48 sc1\n\tbuffer_load_dword v29, off, s[8:11], s45 sc1\n\ts_branch 69f\n\t"
      "39:\n\ts_waitcnt lgkmcnt(0)\n\ts_barrier\n\t"
      "40:\n\t"
      TK5
      "s_sleep 2\n\tds_read_b128 v[208:211], v8 offset:0\n\tds_read_b128 v[212:215], v8 offset:64\n\tds_read_b128 v[216:219], v8 offset:128\n\t"
      "ds_read_b128 v[220:223], v8 offset:192\n\ts_and_b32 s48, s17, 7\n\ts_lshl_b32 s43, s48, 17\n\ts_add_u32 s43, s43, 0x300000\n\ts_add_u32 s48, s17, 3\n\t"
      "s_and_b32 s48, s48, 7\n\ts_lshl_b32 s44, s48, 17\n\ts_add_u32 s44, s44, 0x300000\n\ts_cmp_lg_u32 s58, 0\n\ts_cbranch_scc1 43f\n\t"
      "41:\n\ts_cmp_gt_u32 s55, s17\n\ts_cbranch_scc1 42f\n\tbuffer_load_dword v17, off, s[8:11], s45 sc1\n\ts_waitcnt vmcnt(0)\n\tv_readfirstlane_b32 s55, v17\n\t"
      "s_cmp_eq_u32 s18, 0\n\ts_cbranch_scc1 42f\n\ts_sub_u32 s18, s18, 1\n\ts_sleep 1\n\ts_branch 41b\n\t"
      "42:\n\tbuffer_load_dwordx4 v[240:243], v12, s[8:11], s22 offen offset:0 sc1\n\tbuffer_load_dwordx4 v[244:247], v12, s[8:11], s22 offen offset:2048 sc1\n\t"
      "buffer_load_dwordx4 v[248:251], v12, s[8:11], s49 offen offset:0 sc1\n\tbuffer_load_dwordx4 v[252:255], v12, s[8:11], s49 offen offset:2048 sc1\n\t"
      "43:\n\ts_waitcnt vmcnt(0)\n\tv_mov_b64 v[204:205], -1\n\tv_mov_b64 v[206:207], -1\n\tv_mov_b32 v203, s30\n\ts_cmp_eq_u32 s16, 2\n\ts_cbranch_scc1 50f\n\t"
      "v_mfma_f32_16x16x32_bf16 v[228:231], v[160:163], v[240:243], 0\n\tv_mfma_f32_16x16x32_bf16 v[228:231], v[164:167], v[244:247], v[228:231]\n\t"
      "v_mfma_f32_16x16x32_bf16 v[228:231], v[168:171], v[248:251], v[228:231]\n\tv_mfma_f32_16x16x32_bf16 v[228:231], v[172:175], v[252:255], v[228:231]\n\t"
      "s_waitcnt lgkmcnt(0)\n\tv_mfma_f32_16x16x32_bf16 v[224:227], v[208:211], v[240:243], 0\n\tv_mfma_f32_16x16x32_bf16 v[224:227], v[212:215], v[244:247], v[224:227]\n\t"
      "v_mfma_f32_16x16x32_bf16 v[224:227], v[216:219], v[248:251], v[224:227]\n\tv_mfma_f32_16x16x32_bf16 v[224:227], v[220:223], v[252:255], v[224:227]\n\ts_nop 2\n\t"
      "v_cndmask_b32 v228, v203, v228, s[32:33]\n\tv_cndmask_b32 v229, v203, v229, s[34:35]\n\tv_cndmask_b32 v230, v203, v230, s[36:37]\n\t"
      "v_cndmask_b32 v231, v203, v231, s[38:39]\n\tv_max3_f32 v192, v228, v229, v230\n\tv_max3_f32 v193, v231, v224, v225\n\tv_max3_f32 v192, v192, v226, v227\n\t"
      "v_max_f32 v192, v192, v193\n\tv_mov_b32 v193, v192\n\ts_nop 1\n\tv_permlane32_swap_b32 v192, v193\n\tv_max_f32 v192, v192, v193\n\tv_mov_b32 v193, v192\n\ts_nop 1\n\t"
      "v_permlane16_swap_b32 v192, v193\n\tv_max_f32 v192, v192, v193\n\tv_mov_b32 v193, v192\n\tv_pk_add_f32 v[224:225], v[224:225], v[192:193] neg_lo:[0,1] neg_hi:[0,1]\n\t"
      "v_pk_add_f32 v[226:227], v[226:227], v[192:193] neg_lo:[0,1] neg_hi:[0,1]\n\tv_pk_add_f32 v[228:229], v[228:229], v[192:193] neg_lo:[0,1] neg_hi:[0,1]\n\t"
      "v_pk_add_f32 v[230:231], v[230:231], v[192:193] neg_lo:[0,1] neg_hi:[0,1]\n\tv_pk_mul_f32 v[224:225], v[224:225], s[28:29]\n\t"
      "v_pk_mul_f32 v[226:227], v[226:227], s[28:29]\n\tv_pk_mul_f32 v[228:229], v[228:229], s[28:29]\n\tv_pk_mul_f32 v[230:231], v[230:231], s[28:29]\n\t"
      "v_exp_f32 v224, v224\n\tv_exp_f32 v225, v225\n\tv_exp_f32 v226, v226\n\tv_exp_f32 v227, v227\n\tv_exp_f32 v228, v228\n\tv_exp_f32 v229, v229\n\tv_exp_f32 v230, v230\n\t"
      "v_exp_f32 v231, v231\n\tv_pk_add_f32 v[192:193], v[224:225], v[226:227]\n\tv_pk_add_f32 v[194:195], v[228:229], v[230:231]\n\tv_add_f32 v192, v192, v193\n\t"
      "v_add_f32 v194, v194, v195\n\tv_fma_f32 v192, v192, v13, v194\n\tv_mov_b32 v193, v192\n\ts_nop 1\n\tv_permlane32_swap_b32 v192, v193\n\tv_add_f32 v192, v192, v193\n\t"
      "v_mov_b32 v193, v192\n\ts_nop 1\n\tv_permlane16_swap_b32 v192, v193\n\tv_add_f32 v192, v192, v193\n\tv_rcp_f32 v192, v192\n\ts_nop 0\n\tv_mov_b32 v193, v192\n\t"
      "v_pk_mul_f32 v[224:225], v[224:225], v[192:193]\n\tv_pk_mul_f32 v[226:227], v[226:227], v[192:193]\n\tv_pk_mul_f32 v[228:229], v[228:229], v[192:193]\n\t"
      "v_pk_mul_f32 v[230:231], v[230:231], v[192:193]\n\tv_cvt_pk_bf16_f32 v194, v224, v225\n\tv_cvt_pk_bf16_f32 v195, v226, v227\n\tv_cvt_pk_bf16_f32 v196, v228, v229\n\t"
      "v_cvt_pk_bf16_f32 v197, v230, v231\n\tbuffer_store_dwordx2 v[196:197], v10, s[8:11], s43 offen sc1\n\tbuffer_store_dwordx2 v[194:195], v11, s[8:11], s43 offen sc1\n\t"
      "buffer_store_dwordx2 v[204:205], v10, s[8:11], s44 offen sc1\n\tbuffer_store_dwordx2 v[204:205], v11, s[8:11], s44 offen sc1\n\t"
      TK6
      "s_branch 69f\n\t"
      "50:\n\tv_mfma_f32_16x16x32_bf16 v[228:231], v[160:163], v[240:243], 0\n\tv_mfma_f32_16x16x32_bf16 v[232:235], v[176:179], v[240:243], 0\n\t"
      "v_mfma_f32_16x16x32_bf16 v[228:231], v[164:167], v[244:247], v[228:231]\n\tv_mfma_f32_16x16x32_bf16 v[232:235], v[180:183], v[244:247], v[232:235]\n\t"
      "v_mfma_f32_16x16x32_bf16 v[228:231], v[168:171], v[248:251], v[228:231]\n\tv_mfma_f32_16x16x32_bf16 v[232:235], v[184:187], v[248:251], v[232:235]\n\t"
      "v_mfma_f32_16x16x32_bf16 v[228:231], v[172:175], v[252:255], v[228:231]\n\tv_mfma_f32_16x16x32_bf16 v[232:235], v[188:191], v[252:255], v[232:235]\n\t"
      "s_waitcnt lgkmcnt(0)\n\tv_mfma_f32_16x16x32_bf16 v[224:227], v[208:211], v[240:243], 0\n\tv_mfma_f32_16x16x32_bf16 v[224:227], v[212:215], v[244:247], v[224:227]\n\t"
      "v_mfma_f32_16x16x32_bf16 v[224:227], v[216:219], v[248:251], v[224:227]\n\tv_mfma_f32_16x16x32_bf16 v[224:227], v[220:223], v[252:255], v[224:227]\n\ts_nop 2\n\t"
      "v_cndmask_b32 v232, v203, v232, s[32:33]\n\tv_cndmask_b32 v233, v203, v233, s[34:35]\n\tv_cndmask_b32 v234, v203, v234, s[36:37]\n\t"
      "v_cndmask_b32 v235, v203, v235, s[38:39]\n\tv_max3_f32 v192, v228, v229, v230\n\tv_max3_f32 v193, v231, v224, v225\n\tv_max3_f32 v192, v192, v226, v227\n\t"
      "v_max3_f32 v193, v193, v232, v233\n\tv_max3_f32 v192, v192, v234, v235\n\tv_max_f32 v192, v192, v193\n\tv_mov_b32 v193, v192\n\ts_nop 1\n\t"
      "v_permlane32_swap_b32 v192, v193\n\tv_max_f32 v192, v192, v193\n\tv_mov_b32 v193, v192\n\ts_nop 1\n\tv_permlane16_swap_b32 v192, v193\n\tv_max_f32 v192, v192, v193\n\t"
      "v_mov_b32 v193, v192\n\tv_pk_add_f32 v[224:225], v[224:225], v[192:193] neg_lo:[0,1] neg_hi:[0,1]\n\t"
      "v_pk_add_f32 v[226:227], v[226:227], v[192:193] neg_lo:[0,1] neg_hi:[0,1]\n\tv_pk_add_f32 v[228:229], v[228:229], v[192:193] neg_lo:[0,1] neg_hi:[0,1]\n\t"
      "v_pk_add_f32 v[230:231], v[230:231], v[192:193] neg_lo:[0,1] neg_hi:[0,1]\n\tv_pk_add_f32 v[232:233], v[232:233], v[192:193] neg_lo:[0,1] neg_hi:[0,1]\n\t"
      "v_pk_add_f32 v[234:235], v[234:235], v[192:193] neg_lo:[0,1] neg_hi:[0,1]\n\tv_pk_mul_f32 v[224:225], v[224:225], s[28:29]\n\t"
      "v_pk_mul_f32 v[226:227], v[226:227], s[28:29]\n\tv_pk_mul_f32 v[228:229], v[228:229], s[28:29]\n\tv_pk_mul_f32 v[230:231], v[230:231], s[28:29]\n\t"
      "v_pk_mul_f32 v[232:233], v[232:233], s[28:29]\n\tv_pk_mul_f32 v[234:235], v[234:235], s[28:29]\n\tv_exp_f32 v224, v224\n\tv_exp_f32 v225, v225\n\tv_exp_f32 v226, v226\n\t"
      "v_exp_f32 v227, v227\n\tv_exp_f32 v228, v228\n\tv_exp_f32 v229, v229\n\tv_exp_f32 v230, v230\n\tv_exp_f32 v231, v231\n\tv_exp_f32 v232, v232\n\tv_exp_f32 v233, v233\n\t"
      "v_exp_f32 v234, v234\n\tv_exp_f32 v235, v235\n\tv_pk_add_f32 v[192:193], v[224:225], v[226:227]\n\tv_pk_add_f32 v[194:195], v[228:229], v[230:231]\n\t"
      "v_pk_add_f32 v[196:197], v[232:233], v[234:235]\n\tv_pk_add_f32 v[194:195], v[194:195], v[196:197]\n\tv_add_f32 v192, v192, v193\n\tv_add_f32 v194, v194, v195\n\t"
      "v_fma_f32 v192, v192, v13, v194\n\tv_mov_b32 v193, v192\n\ts_nop 1\n\tv_permlane32_swap_b32 v192, v193\n\tv_add_f32 v192, v192, v193\n\tv_mov_b32 v193, v192\n\t"
      "s_nop 1\n\tv_permlane16_swap_b32 v192, v193\n\tv_add_f32 v192, v192, v193\n\tv_rcp_f32 v192, v192\n\ts_nop 0\n\tv_mov_b32 v193, v192\n\t"
      "v_pk_mul_f32 v[224:225], v[224:225], v[192:193]\n\tv_pk_mul_f32 v[226:227], v[226:227], v[192:193]\n\tv_pk_mul_f32 v[228:229], v[228:229], v[192:193]\n\t"
      "v_pk_mul_f32 v[230:231], v[230:231], v[192:193]\n\tv_pk_mul_f32 v[232:233], v[232:233], v[192:193]\n\tv_pk_mul_f32 v[234:235], v[234:235], v[192:193]\n\t"
      "v_cvt_pk_bf16_f32 v194, v224, v225\n\tv_cvt_pk_bf16_f32 v195, v226, v227\n\tv_cvt_pk_bf16_f32 v196, v228, v229\n\tv_cvt_pk_bf16_f32 v197, v230, v231\n\t"
      "v_cvt_pk_bf16_f32 v198, v232, v233\n\tv_cvt_pk_bf16_f32 v199, v234, v235\n\tbuffer_store_dwordx4 v[196:199], v10, s[8:11], s43 offen sc1\n\t"
      "buffer_store_dwordx2 v[194:195], v11, s[8:11], s43 offen sc1\n\tbuffer_store_dwordx4 v[204:207], v10, s[8:11], s44 offen sc1\n\t"
      "buffer_store_dwordx2 v[204:205], v11, s[8:11], s44 offen sc1\n\t"
      TK6
      "s_branch 69f\n\t"
      "60:\n\t"
      TK5
      "s_sleep 3\n\tds_read_b128 v[160:163], v5 offset:0\n\tds_read_b128 v[164:167], v5 offset:1024\n\tds_read_b128 v[168:171], v5 offset:2048\n\t"
      "ds_read_b128 v[172:175], v5 offset:3072\n\tds_read_b128 v[176:179], v5 offset:4096\n\tds_read_b128 v[180:183], v5 offset:5120\n\t"
      "ds_read_b128 v[184:187], v5 offset:6144\n\tds_read_b128 v[188:191], v5 offset:7168\n\tds_read_b128 v[192:195], v5 offset:8192\n\t"
      "ds_read_b128 v[196:199], v5 offset:9216\n\tds_read_b128 v[200:203], v5 offset:10240\n\tds_read_b128 v[204:207], v5 offset:11264\n\t"
      "ds_read_b128 v[208:211], v5 offset:12288\n\tds_read_b128 v[212:215], v5 offset:13312\n\tds_read_b128 v[216:219], v5 offset:14336\n\t"
      "ds_read_b128 v[220:223], v5 offset:15360\n\tv_mov_b64 v[224:225], 0\n\tv_mov_b64 v[226:227], 0\n\tv_mov_b64 v[228:229], 0\n\tv_mov_b64 v[230:231], 0\n\t"
      "v_mov_b64 v[232:233], 0\n\tv_mov_b64 v[234:235], 0\n\tv_mov_b64 v[236:237], 0\n\tv_mov_b64 v[238:239], 0\n\t"
      "s_sleep 2\n\tv_mov_b32 v17, s17\n\tbuffer_store_dword v17, v12, s[8:11], 0 offen sc1\n\tbuffer_load_dword v18, v15, s[8:11], s41 offen sc1\n\t"
      "64:\n\ts_add_u32 s49, s17, 1\n\ts_cmp_eq_u32 s49, 64\n\ts_cbranch_scc1 69f\n\ts_add_u32 s48, s45, 0x1000\n\t"
      "61:\n\ts_cmp_gt_u32 s54, s49\n\ts_cbranch_scc1 62f\n\tbuffer_load_dword v17, off, s[8:11], s48 sc1\n\ts_waitcnt vmcnt(0)\n\tv_readfirstlane_b32 s54, v17\n\t"
      "s_cmp_eq_u32 s18, 0\n\ts_cbranch_scc1 62f\n\ts_sub_u32 s18, s18, 1\n\ts_sleep 1\n\ts_branch 61b\n\t"
      "62:\n\ts_add_u32 s46, s31, 0x1000\n\ts_mov_b32 m0, 147456\n\ts_nop 0\n\tbuffer_load_dwordx4 v10, s[8:11], s31 offen offset:0 sc1 lds\n\t"
      "buffer_load_dwordx4 v10, s[8:11], s31 offen offset:1024 sc1 lds\n\tbuffer_load_dwordx4 v10, s[8:11], s31 offen offset:2048 sc1 lds\n\t"
      "buffer_load_dwordx4 v10, s[8:11], s31 offen offset:3072 sc1 lds\n\ts_mov_b32 m0, 151552\n\ts_nop 0\n\tbuffer_load_dwordx4 v10, s[8:11], s46 offen offset:0 sc1 lds\n\t"
      "buffer_load_dwordx4 v10, s[8:11], s46 offen offset:1024 sc1 lds\n\tbuffer_load_dwordx4 v10, s[8:11], s46 offen offset:2048 sc1 lds\n\t"
      "buffer_load_dwordx4 v10, s[8:11], s46 offen offset:3072 sc1 lds\n\ts_cmp_lt_i32 s17, 0\n\ts_cbranch_scc1 69f\n\t"
      TK6
      "s_waitcnt vmcnt(8)\n\ts_branch 76f\n\t"
      "74:\n\tbuffer_load_dword v18, v15, s[8:11], s41 offen sc1\n\ts_waitcnt vmcnt(0)\n\t"
      "76:\n\tv_max_u32_dpp v18, v18, v18 row_ror:8 row_mask:0xf bank_mask:0xf\n\ts_nop 1\n\tv_max_u32_dpp v18, v18, v18 row_ror:4 row_mask:0xf bank_mask:0xf\n\ts_nop 1\n\t"
      "v_max_u32_dpp v18, v18, v18 row_ror:2 row_mask:0xf bank_mask:0xf\n\ts_nop 1\n\tv_max_u32_dpp v18, v18, v18 row_ror:1 row_mask:0xf bank_mask:0xf\n\ts_nop 0\n\t"
      "v_readlane_b32 s52, v18, 0\n\tv_readlane_b32 s48, v18, 16\n\ts_max_u32 s52, s52, s48\n\ts_cmp_lg_u32 s52, -1\n\ts_cbranch_scc1 75f\n\ts_cmp_eq_u32 s18, 0\n\t"
      "s_cbranch_scc1 75f\n\ts_sub_u32 s18, s18, 1\n\ts_branch 74b\n\t"
      "75:\n\ts_sub_u32 s48, s52, s19\n\ts_sub_u32 s48, s48, s62\n\ts_cmp_gt_i32 s48, s72\n\ts_cselect_b32 s19, s73, 0\n\ts_cmp_gt_i32 s48, 100\n\t"
      "s_cselect_b32 s19, 40, s19\n\ts_cmp_gt_i32 s48, 40\n\ts_cselect_b32 s48, 24, s48\n\ts_ashr_i32 s48, s48, 2\n\ts_add_i32 s62, s62, s48\n\ts_min_u32 s62, s62, 1000\n\t"
      "s_mul_i32 s63, s62, 3\n\ts_lshr_b32 s63, s63, 3\n\ts_add_u32 s19, s19, s52\n\ts_add_u32 s19, s19, s23\n\ts_add_u32 s19, s19, s63\n\ts_branch 69f\n\t"
      "65:\n\tds_read_b128 v[160:163], v5 offset:0\n\tds_read_b128 v[164:167], v5 offset:1024\n\tds_read_b128 v[168:171], v5 offset:2048\n\t"
      "ds_read_b128 v[172:175], v5 offset:3072\n\tds_read_b128 v[176:179], v5 offset:4096\n\tds_read_b128 v[180:183], v5 offset:5120\n\t"
      "ds_read_b128 v[184:187], v5 offset:6144\n\tds_read_b128 v[188:191], v5 offset:7168\n\tds_read_b128 v[192:195], v5 offset:8192\n\t"
      "ds_read_b128 v[196:199], v5 offset:9216\n\tds_read_b128 v[200:203], v5 offset:10240\n\tds_read_b128 v[204:207], v5 offset:11264\n\t"
      "ds_read_b128 v[208:211], v5 offset:12288\n\tds_read_b128 v[212:215], v5 offset:13312\n\tds_read_b128 v[216:219], v5 offset:14336\n\t"
      "ds_read_b128 v[220:223], v5 offset:15360\n\tv_mov_b64 v[224:225], 0\n\tv_mov_b64 v[226:227], 0\n\tv_mov_b64 v[228:229], 0\n\tv_mov_b64 v[230:231], 0\n\t"
      "v_mov_b64 v[232:233], 0\n\tv_mov_b64 v[234:235], 0\n\tv_mov_b64 v[236:237], 0\n\tv_mov_b64 v[238:239], 0\n\ts_branch 64b\n\t"
      "86:\n\t"
      STS("s_add_u32 s50, s50, 1\n\t")
      "s_waitcnt lgkmcnt(0)\n\tbuffer_load_dwordx4 v[240:243], v0, s[8:11], s68 offen sc1\n\tbuffer_load_dwordx4 v[244:247], v0, s[8:11], s69 offen sc1\n\t"
      "buffer_load_dwordx4 v[248:251], v0, s[8:11], s70 offen sc1\n\tbuffer_load_dwordx4 v[252:255], v0, s[8:11], s71 offen sc1\n\ts_nop 3\n\tv_mov_b64 v[224:225], 0\n\t"
      "v_mov_b64 v[226:227], 0\n\tv_mov_b64 v[228:229], 0\n\tv_mov_b64 v[230:231], 0\n\tv_mov_b64 v[232:233], 0\n\tv_mov_b64 v[234:235], 0\n\tv_mov_b64 v[236:237], 0\n\t"
      "v_mov_b64 v[238:239], 0\n\ts_nop 1\n\ts_branch 84f\n\t"
      "83:\n\tbuffer_load_dwordx4 v[240:243], v0, s[8:11], s68 offen sc1\n\tbuffer_load_dwordx4 v[244:247], v0, s[8:11], s69 offen sc1\n\t"
      "buffer_load_dwordx4 v[248:251], v0, s[8:11], s70 offen sc1\n\tbuffer_load_dwordx4 v[252:255], v0, s[8:11], s71 offen sc1\n\t"
      "84:\n\ts_waitcnt vmcnt(0)\n\tv_max3_u32 v17, v240, v241, v242\n\tv_max_u32 v17, v17, v243\n\tv_max3_u32 v17, v17, v244, v245\n\tv_max3_u32 v17, v17, v246, v247\n\t"
      "v_max3_u32 v17, v17, v248, v249\n\tv_max3_u32 v17, v17, v250, v251\n\tv_max3_u32 v17, v17, v252, v253\n\tv_max3_u32 v17, v17, v254, v255\n\tv_cmp_eq_u32 vcc, -1, v17\n\t"
      "s_cbranch_vccz 85f\n\ts_cmp_eq_u32 s18, 0\n\ts_cbranch_scc1 85f\n\ts_sub_u32 s18, s18, 1\n\ts_branch 83b\n\t"
      "85:\n\tv_smfmac_f32_16x16x64_bf16 v[224:227], v[240:243], a[0:7], v6\n\tv_smfmac_f32_16x16x64_bf16 v[228:231], v[240:243], a[8:15], v6\n\t"
      "v_smfmac_f32_16x16x64_bf16 v[232:235], v[240:243], v[32:39], v6\n\tv_smfmac_f32_16x16x64_bf16 v[236:239], v[240:243], v[160:167], v6\n\t"
      "v_smfmac_f32_16x16x64_bf16 v[224:227], v[244:247], a[16:23], v6\n\tv_smfmac_f32_16x16x64_bf16 v[228:231], v[244:247], a[24:31], v6\n\t"
      "ds_write_b128 v1, v[240:243] offset:0\n\tds_write_b128 v1, v[244:247] offset:1024\n\tds_write_b128 v1, v[248:251] offset:2048\n\t"
      "ds_write_b128 v1, v[252:255] offset:3072\n\tv_mov_b32 v17, 0\n\ts_mov_b64 vcc, 0\n\ts_waitcnt lgkmcnt(0)\n\ts_cmp_lt_u32 s16, 2\n\ts_cbranch_scc1 20b\n\ts_branch 24b\n\t"
      "87:\n\t"
      STS("s_add_u32 s50, s50, 1\n\t")
      "s_waitcnt lgkmcnt(0)\n\tbuffer_load_dwordx4 v[244:247], v0, s[8:11], s69 offen sc1\n\tbuffer_load_dwordx4 v[248:251], v0, s[8:11], s70 offen sc1\n\t"
      "buffer_load_dwordx4 v[252:255], v0, s[8:11], s71 offen sc1\n\ts_nop 3\n\tv_mov_b64 v[224:225], 0\n\tv_mov_b64 v[226:227], 0\n\tv_mov_b64 v[228:229], 0\n\t"
      "v_mov_b64 v[230:231], 0\n\tv_mov_b64 v[232:233], 0\n\tv_mov_b64 v[234:235], 0\n\tv_mov_b64 v[236:237], 0\n\tv_mov_b64 v[238:239], 0\n\ts_nop 1\n\t"
      "v_smfmac_f32_16x16x64_bf16 v[224:227], v[240:243], a[0:7], v6\n\tv_smfmac_f32_16x16x64_bf16 v[228:231], v[240:243], a[8:15], v6\n\t"
      "v_smfmac_f32_16x16x64_bf16 v[232:235], v[240:243], v[32:39], v6\n\tv_smfmac_f32_16x16x64_bf16 v[236:239], v[240:243], v[160:167], v6\n\ts_branch 84f\n\t"
      "83:\n\tbuffer_load_dwordx4 v[244:247], v0, s[8:11], s69 offen sc1\n\tbuffer_load_dwordx4 v[248:251], v0, s[8:11], s70 offen sc1\n\t"
      "buffer_load_dwordx4 v[252:255], v0, s[8:11], s71 offen sc1\n\t"
      "84:\n\ts_waitcnt vmcnt(0)\n\tv_max3_u32 v17, v244, v245, v246\n\tv_max_u32 v17, v17, v247\n\tv_max3_u32 v17, v17, v248, v249\n\tv_max3_u32 v17, v17, v250, v251\n\t"
      "v_max3_u32 v17, v17, v252, v253\n\tv_max3_u32 v17, v17, v254, v255\n\tv_cmp_eq_u32 vcc, -1, v17\n\ts_cbranch_vccz 85f\n\ts_cmp_eq_u32 s18, 0\n\ts_cbranch_scc1 85f\n\t"
      "s_sub_u32 s18, s18, 1\n\ts_branch 83b\n\t"
      "85:\n\tv_smfmac_f32_16x16x64_bf16 v[224:227], v[244:247], a[16:23], v6\n\tv_smfmac_f32_16x16x64_bf16 v[228:231], v[244:247], a[24:31], v6\n\t"
      "v_smfmac_f32_16x16x64_bf16 v[232:235], v[244:247], v[40:47], v6\n\tv_smfmac_f32_16x16x64_bf16 v[236:239], v[244:247], v[168:175], v6\n\t"
      "v_smfmac_f32_16x16x64_bf16 v[224:227], v[248:251], a[32:39], v6\n\tv_smfmac_f32_16x16x64_bf16 v[228:231], v[248:251], a[40:47], v6\n\t"
      "ds_write_b128 v1, v[244:247] offset:1024\n\tds_write_b128 v1, v[248:251] offset:2048\n\tds_write_b128 v1, v[252:255] offset:3072\n\tv_mov_b32 v17, 0\n\t"
      "s_mov_b64 vcc, 0\n\ts_waitcnt lgkmcnt(0)\n\ts_cmp_lt_u32 s16, 2\n\ts_cbranch_scc1 21b\n\ts_branch 25b\n\t"
      "88:\n\t"
      STS("s_add_u32 s50, s50, 1\n\t")
      "s_waitcnt lgkmcnt(0)\n\tbuffer_load_dwordx4 v[248:251], v0, s[8:11], s70 offen sc1\n\tbuffer_load_dwordx4 v[252:255], v0, s[8:11], s71 offen sc1\n\ts_nop 3\n\t"
      "v_mov_b64 v[224:225], 0\n\tv_mov_b64 v[226:227], 0\n\tv_mov_b64 v[228:229], 0\n\tv_mov_b64 v[230:231], 0\n\tv_mov_b64 v[232:233], 0\n\tv_mov_b64 v[234:235], 0\n\t"
      "v_mov_b64 v[236:237], 0\n\tv_mov_b64 v[238:239], 0\n\ts_nop 1\n\tv_smfmac_f32_16x16x64_bf16 v[224:227], v[240:243], a[0:7], v6\n\t"
      "v_smfmac_f32_16x16x64_bf16 v[228:231], v[240:243], a[8:15], v6\n\tv_smfmac_f32_16x16x64_bf16 v[232:235], v[240:243], v[32:39], v6\n\t"
      "v_smfmac_f32_16x16x64_bf16 v[236:239], v[240:243], v[160:167], v6\n\tv_smfmac_f32_16x16x64_bf16 v[224:227], v[244:247], a[16:23], v6\n\t"
      "v_smfmac_f32_16x16x64_bf16 v[228:231], v[244:247], a[24:31], v6\n\tv_smfmac_f32_16x16x64_bf16 v[232:235], v[244:247], v[40:47], v6\n\t"
      "v_smfmac_f32_16x16x64_bf16 v[236:239], v[244:247], v[168:175], v6\n\ts_branch 84f\n\t"
      "83:\n\tbuffer_load_dwordx4 v[248:251], v0, s[8:11], s70 offen sc1\n\tbuffer_load_dwordx4 v[252:255], v0, s[8:11], s71 offen sc1\n\t"
      "84:\n\ts_waitcnt vmcnt(0)\n\tv_max3_u32 v17, v248, v249, v250\n\tv_max_u32 v17, v17, v251\n\tv_max3_u32 v17, v17, v252, v253\n\tv_max3_u32 v17, v17, v254, v255\n\t"
      "v_cmp_eq_u32 vcc, -1, v17\n\ts_cbranch_vccz 85f\n\ts_cmp_eq_u32 s18, 0\n\ts_cbranch_scc1 85f\n\ts_sub_u32 s18, s18, 1\n\ts_branch 83b\n\t"
      "85:\n\tv_smfmac_f32_16x16x64_bf16 v[224:227], v[248:251], a[32:39], v6\n\tv_smfmac_f32_16x16x64_bf16 v[228:231], v[248:251], a[40:47], v6\n\t"
      "v_smfmac_f32_16x16x64_bf16 v[232:235], v[248:251], v[48:55], v6\n\tv_smfmac_f32_16x16x64_bf16 v[236:239], v[248:251], v[176:183], v6\n\t"
      "v_smfmac_f32_16x16x64_bf16 v[224:227], v[252:255], a[48:55], v6\n\tv_smfmac_f32_16x16x64_bf16 v[228:231], v[252:255], a[56:63], v6\n\t"
      "ds_write_b128 v1, v[248:251] offset:2048\n\tds_write_b128 v1, v[252:255] offset:3072\n\tv_mov_b32 v17, 0\n\ts_mov_b64 vcc, 0\n\ts_waitcnt lgkmcnt(0)\n\t"
      "s_cmp_lt_u32 s16, 2\n\ts_cbranch_scc1 22b\n\ts_branch 26b\n\t"
      "89:\n\t"
      STS("s_add_u32 s50, s50, 1\n\t")
      "s_waitcnt lgkmcnt(0)\n\tbuffer_load_dwordx4 v[252:255], v0, s[8:11], s71 offen sc1\n\ts_nop 3\n\tv_mov_b64 v[224:225], 0\n\tv_mov_b64 v[226:227], 0\n\t"
      "v_mov_b64 v[228:229], 0\n\tv_mov_b64 v[230:231], 0\n\tv_mov_b64 v[232:233], 0\n\tv_mov_b64 v[234:235], 0\n\tv_mov_b64 v[236:237], 0\n\tv_mov_b64 v[238:239], 0\n\t"
      "s_nop 1\n\tv_smfmac_f32_16x16x64_bf16 v[224:227], v[240:243], a[0:7], v6\n\tv_smfmac_f32_16x16x64_bf16 v[228:231], v[240:243], a[8:15], v6\n\t"
      "v_smfmac_f32_16x16x64_bf16 v[232:235], v[240:243], v[32:39], v6\n\tv_smfmac_f32_16x16x64_bf16 v[236:239], v[240:243], v[160:167], v6\n\t"
      "v_smfmac_f32_16x16x64_bf16 v[224:227], v[244:247], a[16:23], v6\n\tv_smfmac_f32_16x16x64_bf16 v[228:231], v[244:247], a[24:31], v6\n\t"
      "v_smfmac_f32_16x16x64_bf16 v[232:235], v[244:247], v[40:47], v6\n\tv_smfmac_f32_16x16x64_bf16 v[236:239], v[244:247], v[168:175], v6\n\t"
      "v_smfmac_f32_16x16x64_bf16 v[224:227], v[248:251], a[32:39], v6\n\tv_smfmac_f32_16x16x64_bf16 v[228:231], v[248:251], a[40:47], v6\n\t"
      "v_smfmac_f32_16x16x64_bf16 v[232:235], v[248:251], v[48:55], v6\n\tv_smfmac_f32_16x16x64_bf16 v[236:239], v[248:251], v[176:183], v6\n\ts_branch 84f\n\t"
      "83:\n\tbuffer_load_dwordx4 v[252:255], v0, s[8:11], s71 offen sc1\n\t"
      "84:\n\ts_waitcnt vmcnt(0)\n\tv_max3_u32 v17, v252, v253, v254\n\tv_max_u32 v17, v17, v255\n\tv_cmp_eq_u32 vcc, -1, v17\n\ts_cbranch_vccz 85f\n\ts_cmp_eq_u32 s18, 0\n\t"
      "s_cbranch_scc1 85f\n\ts_sub_u32 s18, s18, 1\n\ts_branch 83b\n\t"
      "85:\n\tv_smfmac_f32_16x16x64_bf16 v[224:227], v[252:255], a[48:55], v6\n\tv_smfmac_f32_16x16x64_bf16 v[228:231], v[252:255], a[56:63], v6\n\t"
      "v_smfmac_f32_16x16x64_bf16 v[232:235], v[252:255], v[56:63], v6\n\tv_smfmac_f32_16x16x64_bf16 v[236:239], v[252:255], v[184:191], v6\n\t"
      "v_smfmac_f32_16x16x64_bf16 v[224:227], v[20:23], a[64:71], v6\n\tv_smfmac_f32_16x16x64_bf16 v[228:231], v[20:23], a[72:79], v6\n\t"
      "ds_write_b128 v1, v[252:255] offset:3072\n\tv_mov_b32 v17, 0\n\ts_mov_b64 vcc, 0\n\ts_waitcnt lgkmcnt(0)\n\ts_cmp_lt_u32 s16, 2\n\ts_cbranch_scc1 23b\n\ts_branch 27b\n\t"
      "69:\n\t"
      TK7
      STS("s_cmp_lt_i32 s17, 8\n\ts_cselect_b32 s50, 0, s50\n\ts_cselect_b32 s51, 0, s51\n\ts_cselect_b32 s61, 0, s61\n\t")
      TKS("s_waitcnt lgkmcnt(0)\n\ts_cmp_lt_i32 s17, 0\n\ts_cbranch_scc1 98f\n\ts_cmp_lg_u32 s16, s91\n\ts_cbranch_scc1 98f\n\ts_mov_b64 exec, 1\n\ts_lshl_b32 s48, s17, 4\n\ts_add_u32 s48, s48, 161056\n\tv_mov_b32 v17, s48\n\ts_sub_u32 s48, s74, s90\n\ts_min_u32 s48, s48, 0xffff\n\tv_mov_b32 v18, s48\n\tds_write_b16 v17, v18 offset:0\n\ts_sub_u32 s48, s76, s74\n\ts_min_u32 s48, s48, 0xffff\n\tv_mov_b32 v18, s48\n\tds_write_b16 v17, v18 offset:2\n\ts_sub_u32 s48, s78, s76\n\ts_min_u32 s48, s48, 0xffff\n\tv_mov_b32 v18, s48\n\tds_write_b16 v17, v18 offset:4\n\ts_sub_u32 s48, s80, s78\n\ts_min_u32 s48, s48, 0xffff\n\tv_mov_b32 v18, s48\n\tds_write_b16 v17, v18 offset:6\n\ts_sub_u32 s48, s82, s80\n\ts_min_u32 s48, s48, 0xffff\n\tv_mov_b32 v18, s48\n\tds_write_b16 v17, v18 offset:8\n\ts_sub_u32 s48, s84, s82\n\ts_min_u32 s48, s48, 0xffff\n\tv_mov_b32 v18, s48\n\tds_write_b16 v17, v18 offset:10\n\ts_sub_u32 s48, s86, s84\n\ts_min_u32 s48, s48, 0xffff\n\tv_mov_b32 v18, s48\n\tds_write_b16 v17, v18 offset:12\n\ts_sub_u32 s48, s88, s86\n\ts_min_u32 s48, s48, 0xffff\n\tv_mov_b32 v18, s48\n\tds_write_b16 v17, v18 offset:14\n\ts_mov_b64 exec, -1\n\t98:\n\ts_mov_b32 s90, s88\n\t")
      "s_add_u32 s48, s22, 0x4000\n\ts_and_b32 s48, s48, 0x3c000\n\ts_andn2_b32 s22, s22, 0x3c000\n\ts_or_b32 s22, s22, s48\n\ts_add_u32 s48, s31, 0x4000\n\t"
      "s_and_b32 s48, s48, 0x3c000\n\ts_andn2_b32 s31, s31, 0x3c000\n\ts_or_b32 s31, s31, s48\n\ts_add_u32 s17, s17, 1\n\ts_cmp_lt_u32 s17, 64\n\ts_cbranch_scc1 10b\n\t"
      ".Lmk_end4:\n\ts_waitcnt vmcnt(0) lgkmcnt(0)\n\t"
      TKS("s_mul_i32 s48, s16, 72\n\ts_add_u32 s48, s48, 162080\n\tv_mov_b32 v224, s48\n\tds_read_b32 v225, v224 offset:0\n\ts_waitcnt lgkmcnt(0)\n\tv_readfirstlane_b32 s74, v225\n\tds_read_b32 v225, v224 offset:4\n\ts_waitcnt lgkmcnt(0)\n\tv_readfirstlane_b32 s75, v225\n\tds_read_b32 v225, v224 offset:8\n\ts_waitcnt lgkmcnt(0)\n\tv_readfirstlane_b32 s76, v225\n\tds_read_b32 v225, v224 offset:12\n\ts_waitcnt lgkmcnt(0)\n\tv_readfirstlane_b32 s77, v225\n\tds_read_b32 v225, v224 offset:16\n\ts_waitcnt lgkmcnt(0)\n\tv_readfirstlane_b32 s78, v225\n\tds_read_b32 v225, v224 offset:20\n\ts_waitcnt lgkmcnt(0)\n\tv_readfirstlane_b32 s79, v225\n\tds_read_b32 v225, v224 offset:24\n\ts_waitcnt lgkmcnt(0)\n\tv_readfirstlane_b32 s80, v225\n\tds_read_b32 v225, v224 offset:28\n\ts_waitcnt lgkmcnt(0)\n\tv_readfirstlane_b32 s81, v225\n\tds_read_b32 v225, v224 offset:32\n\ts_waitcnt lgkmcnt(0)\n\tv_readfirstlane_b32 s82, v225\n\tds_read_b32 v225, v224 offset:36\n\ts_waitcnt lgkmcnt(0)\n\tv_readfirstlane_b32 s83, v225\n\tds_read_b32 v225, v224 offset:40\n\ts_waitcnt lgkmcnt(0)\n\tv_readfirstlane_b32 s84, v225\n\tds_read_b32 v225, v224 offset:44\n\ts_waitcnt lgkmcnt(0)\n\tv_readfirstlane_b32 s85, v225\n\tds_read_b32 v225, v224 offset:48\n\ts_waitcnt lgkmcnt(0)\n\tv_readfirstlane_b32 s86, v225\n\tds_read_b32 v225, v224 offset:52\n\ts_waitcnt lgkmcnt(0)\n\tv_readfirstlane_b32 s87, v225\n\tds_read_b32 v225, v224 offset:56\n\ts_waitcnt lgkmcnt(0)\n\tv_readfirstlane_b32 s88, v225\n\tds_read_b32 v225, v224 offset:60\n\ts_waitcnt lgkmcnt(0)\n\tv_readfirstlane_b32 s89, v225\n\tds_read_b32 v225, v224 offset:64\n\ts_waitcnt lgkmcnt(0)\n\tv_readfirstlane_b32 s90, v225\n\tds_read_b32 v225, v224 offset:68\n\ts_waitcnt lgkmcnt(0)\n\tv_readfirstlane_b32 s91, v225\n\t")
      STS("s_lshl_b32 s48, s16, 2\n\ts_add_u32 s48, s48, 162944\n\tv_mov_b32 v0, s48\n\tv_mov_b32 v1, s50\n\tds_write_b32 v0, v1\n\ts_cmp_lg_u32 s16, 0\n\ts_cbranch_scc1 97f\n\tv_mov_b32 v1, s51\n\tds_write_b32 v0, v1 offset:16\n\t97:\n\ts_cmp_lg_u32 s16, 3\n\ts_cbranch_scc1 95f\n\tv_mov_b32 v1, s61\n\tds_write_b32 v0, v1 offset:8\n\tv_mov_b32 v1, s21\n\tds_write_b32 v0, v1 offset:12\n\tv_mov_b32 v1, s22\n\tds_write_b32 v0, v1 offset:16\n\t95:\n\t")
      "s_cmp_lg_u32 s18, 0\n\ts_cbranch_scc1 96f\n\tv_mov_b32 v1, 1\n\tbuffer_store_dword v1, off, s[12:15], 0 offset:3328 sc1\n\t"
      "96:\n\t"
      : "+{v[0:31]}"(k), "+{v[160:191]}"(s0), "+{v[192:223]}"(s1), ALL_REGS :: "memory", "vcc", "scc", S_CLOB TK_CLOB);
}

DEV void vo_role(Ctx& c, int h) {
  const KArg p = enter(c);
  constexpr int WL = 0, MEMS = 131072, VM = 147456, VROW = 272;
  c.budget = SPIN_CAP;
  const int w = c.w, lane = fresh_lane(), g = lane >> 4, cc = lane & 15, q = (lane >> 2) & 3, pp = lane & 3;
  auto grp = [&](int i) { return ((h >> 3) - ROFF - i) & 3; };
  v32u k;
  k[0] = amap(w * 4096 + lane * 16, 0);
  k[1] = MEMS + w * 4096 + lane * 16;
#pragma unroll
  for (int r = 0; r < 3; ++r) k[2 + r] = MEMS + ((w + r + 1) & 3) * 4096 + lane * 16;
  k[5] = WL + w * 32768 + lane * 16;
  k[6] = (lane & 1) ? 0xEEEE : 0x4444;
  k[7] = VM + 2 * g * VROW + (32 * w + 2 * cc) * 2;
  k[8] = VM + (cc & 7) * VROW + 16 * g;
  k[9] = R_U + (16 * w + (g >> 1)) * 16384 + h * 256 + cc * 16 + (g & 1) * 8;
  k[10] = 0;
  k[11] = amap(24576 + (c.slot >> 5) * 4096 + (lane & 31) * 128, 0);
  k[12] = (lane & 8) ? 0xEEEE : 0x4444;
#pragma unroll
  for (int i = 0; i < 4; ++i) k[14 + i] = g * 8192 + head(h) * 256 + 16 * (cc ^ (2 * (g & 1) + 4 * i));
#pragma unroll
  for (int i = 0; i < 2; ++i) { const int ch = cc ^ (4 * (g >> 1) + 8 * i); k[18 + i] = g * 2048 + w * 512 + grp(2 + (ch >> 3)) * 128 + 16 * (ch & 7); }
#pragma unroll
  for (int i = 0; i < 8; ++i)
    k[20 + i] = WL + ((w + (i >> 1)) & 3) * 32768 + (8 * (q >> 1) + (q & 1) + krow_lane(g)) * 256 + 16 * ((4 * w + 2 * (i & 1) + (pp >> 1)) ^ (2 * (q & 1) + 4 * (g & 1) + 8 * (q >> 1))) + 8 * (pp & 1);
#pragma unroll
  for (int i = 0; i < 4; ++i) k[28 + i] = WL + w * 32768 + (16 * (q & 1) + 2 * g) * 256 + 16 * ((4 * i + 2 * (q >> 1) + (pp >> 1)) ^ (4 * (g & 1) + 8 * (q & 1))) + 8 * (pp & 1);
  k[13] = 0;
  v32u s0 = {RSRCS, (unsigned)w, 0, c.budget, SKEW, 0, PACE, (unsigned)grp(0) << 16, (unsigned)grp(1) << 16, (unsigned)grp(2) << 16, (unsigned)grp(3) << 16};
  v32u s1 = {plo(p->Wv), phi(p->Wv), 1024 * 4096 * 2, 0x00020000, plo(p->Wo), phi(p->Wo), 1024 * 4096 * 2, 0x00020000, 0, (unsigned)(WL + w * 32768), 0, 0, 0, 0, 0, 0,
             (unsigned)(w << 21), (unsigned)(head(h) << 18), 0, 0, plo((const void*)p), phi((const void*)p), c.att ? 0u : (unsigned)c.b + 256u, (unsigned)(C_VERDICT + c.att * 128), ROTS(c.slot >> 5), HELLO};
  v32u wa[8], wv[5];
  asm volatile(
      "s_nop 0\n\tv_readfirstlane_b32 s8, v160\n\tv_readfirstlane_b32 s9, v161\n\tv_readfirstlane_b32 s10, v162\n\tv_readfirstlane_b32 s11, v163\n\t"
      "v_readfirstlane_b32 s12, v164\n\tv_readfirstlane_b32 s13, v165\n\tv_readfirstlane_b32 s14, v166\n\tv_readfirstlane_b32 s15, v167\n\tv_readfirstlane_b32 s16, v168\n\t"
      "v_readfirstlane_b32 s17, v169\n\tv_readfirstlane_b32 s18, v170\n\tv_readfirstlane_b32 s19, v171\n\tv_readfirstlane_b32 s20, v172\n\tv_readfirstlane_b32 s21, v173\n\t"
      "v_readfirstlane_b32 s22, v174\n\tv_readfirstlane_b32 s23, v175\n\tv_readfirstlane_b32 s24, v176\n\tv_readfirstlane_b32 s25, v177\n\tv_readfirstlane_b32 s26, v178\n\t"
      "v_readfirstlane_b32 s27, v179\n\tv_readfirstlane_b32 s28, v180\n\tv_readfirstlane_b32 s29, v181\n\tv_readfirstlane_b32 s30, v182\n\tv_readfirstlane_b32 s40, v192\n\t"
      "v_readfirstlane_b32 s41, v193\n\tv_readfirstlane_b32 s42, v194\n\tv_readfirstlane_b32 s43, v195\n\tv_readfirstlane_b32 s44, v196\n\tv_readfirstlane_b32 s45, v197\n\t"
      "v_readfirstlane_b32 s46, v198\n\tv_readfirstlane_b32 s47, v199\n\tv_readfirstlane_b32 s49, v201\n\tv_readfirstlane_b32 s56, v208\n\tv_readfirstlane_b32 s57, v209\n\t"
      "v_readfirstlane_b32 s60, v212\n\tv_readfirstlane_b32 s61, v213\n\tv_readfirstlane_b32 s62, v214\n\tv_readfirstlane_b32 s63, v215\n\tv_readfirstlane_b32 s64, v216\n\t"
      "v_readfirstlane_b32 s65, v217\n\tv_readfirstlane_b32 s66, v218\n\tv_readfirstlane_b32 s67, v219\n\tv_readfirstlane_b32 s68, v220\n\tv_readfirstlane_b32 s69, v221\n\t"
      "v_readfirstlane_b32 s70, v222\n\ts_nop 4\n\t"
      TKS("s_mul_i32 s48, s16, 72\n\ts_add_u32 s48, s48, 162080\n\tv_mov_b32 v224, s48\n\tv_mov_b32 v225, s74\n\tds_write_b32 v224, v225 offset:0\n\tv_mov_b32 v225, s75\n\tds_write_b32 v224, v225 offset:4\n\tv_mov_b32 v225, s76\n\tds_write_b32 v224, v225 offset:8\n\tv_mov_b32 v225, s77\n\tds_write_b32 v224, v225 offset:12\n\tv_mov_b32 v225, s78\n\tds_write_b32 v224, v225 offset:16\n\tv_mov_b32 v225, s79\n\tds_write_b32 v224, v225 offset:20\n\tv_mov_b32 v225, s80\n\tds_write_b32 v224, v225 offset:24\n\tv_mov_b32 v225, s81\n\tds_write_b32 v224, v225 offset:28\n\tv_mov_b32 v225, s82\n\tds_write_b32 v224, v225 offset:32\n\tv_mov_b32 v225, s83\n\tds_write_b32 v224, v225 offset:36\n\tv_mov_b32 v225, s84\n\tds_write_b32 v224, v225 offset:40\n\tv_mov_b32 v225, s85\n\tds_write_b32 v224, v225 offset:44\n\tv_mov_b32 v225, s86\n\tds_write_b32 v224, v225 offset:48\n\tv_mov_b32 v225, s87\n\tds_write_b32 v224, v225 offset:52\n\tv_mov_b32 v225, s88\n\tds_write_b32 v224, v225 offset:56\n\tv_mov_b32 v225, s89\n\tds_write_b32 v224, v225 offset:60\n\tv_mov_b32 v225, s90\n\tds_write_b32 v224, v225 offset:64\n\tv_mov_b32 v225, s91\n\tds_write_b32 v224, v225 offset:68\n\t")
      "s_buffer_load_dword s71, s[12:15], s69 glc\n\ts_add_u32 s50, s56, s64\n\ts_add_u32 m0, s49, 0\n\ts_add_u32 s48, s50, 0x0\n\t"
      "buffer_load_dwordx4 v14, s[40:43], s48 offen  lds\n\ts_add_u32 m0, s49, 1024\n\ts_add_u32 s48, s50, 0x8000\n\tbuffer_load_dwordx4 v15, s[40:43], s48 offen  lds\n\t"
      "s_add_u32 m0, s49, 2048\n\ts_add_u32 s48, s50, 0x10000\n\tbuffer_load_dwordx4 v16, s[40:43], s48 offen  lds\n\ts_add_u32 m0, s49, 3072\n\ts_add_u32 s48, s50, 0x18000\n\t"
      "buffer_load_dwordx4 v17, s[40:43], s48 offen  lds\n\ts_add_u32 m0, s49, 4096\n\ts_add_u32 s48, s50, 0x20000\n\tbuffer_load_dwordx4 v14, s[40:43], s48 offen  lds\n\t"
      "s_add_u32 m0, s49, 5120\n\ts_add_u32 s48, s50, 0x28000\n\tbuffer_load_dwordx4 v15, s[40:43], s48 offen  lds\n\ts_add_u32 m0, s49, 6144\n\ts_add_u32 s48, s50, 0x30000\n\t"
      "buffer_load_dwordx4 v16, s[40:43], s48 offen  lds\n\ts_add_u32 m0, s49, 7168\n\ts_add_u32 s48, s50, 0x38000\n\tbuffer_load_dwordx4 v17, s[40:43], s48 offen  lds\n\t"
      "s_waitcnt lgkmcnt(0)\n\ts_bitcmp0_b32 s68, 0\n\ts_cbranch_scc1 5f\n\ts_add_u32 s71, s71, 1\n\ts_sub_u32 s48, s69, 32768\n\tv_mov_b32 v13, s71\n\ts_mov_b64 exec, 1\n\t"
      "buffer_store_dword v13, off, s[12:15], s48 sc1\n\ts_mov_b64 exec, -1\n\t"
      "5:\n\ts_add_u32 s50, s56, s64\n\ts_add_u32 m0, s49, 8192\n\ts_add_u32 s48, s50, 0x40000\n\tbuffer_load_dwordx4 v14, s[40:43], s48 offen  lds\n\t"
      "s_add_u32 m0, s49, 9216\n\ts_add_u32 s48, s50, 0x48000\n\tbuffer_load_dwordx4 v15, s[40:43], s48 offen  lds\n\ts_add_u32 m0, s49, 10240\n\ts_add_u32 s48, s50, 0x50000\n\t"
      "buffer_load_dwordx4 v16, s[40:43], s48 offen  lds\n\ts_add_u32 m0, s49, 11264\n\ts_add_u32 s48, s50, 0x58000\n\tbuffer_load_dwordx4 v17, s[40:43], s48 offen  lds\n\t"
      "s_add_u32 m0, s49, 12288\n\ts_add_u32 s48, s50, 0x60000\n\tbuffer_load_dwordx4 v14, s[40:43], s48 offen  lds\n\ts_add_u32 m0, s49, 13312\n\t"
      "s_add_u32 s48, s50, 0x68000\n\tbuffer_load_dwordx4 v15, s[40:43], s48 offen  lds\n\ts_add_u32 m0, s49, 14336\n\ts_add_u32 s48, s50, 0x70000\n\t"
      "buffer_load_dwordx4 v16, s[40:43], s48 offen  lds\n\ts_add_u32 m0, s49, 15360\n\ts_add_u32 s48, s50, 0x78000\n\tbuffer_load_dwordx4 v17, s[40:43], s48 offen  lds\n\t"
      "s_add_u32 s50, s56, s65\n\ts_add_u32 m0, s49, 16384\n\ts_add_u32 s48, s50, 0x0\n\tbuffer_load_dwordx4 v14, s[40:43], s48 offen  lds\n\ts_add_u32 m0, s49, 17408\n\t"
      "s_add_u32 s48, s50, 0x8000\n\tbuffer_load_dwordx4 v15, s[40:43], s48 offen  lds\n\ts_add_u32 m0, s49, 18432\n\ts_add_u32 s48, s50, 0x10000\n\t"
      "buffer_load_dwordx4 v16, s[40:43], s48 offen  lds\n\ts_add_u32 m0, s49, 19456\n\ts_add_u32 s48, s50, 0x18000\n\tbuffer_load_dwordx4 v17, s[40:43], s48 offen  lds\n\t"
      "s_add_u32 m0, s49, 20480\n\ts_add_u32 s48, s50, 0x20000\n\tbuffer_load_dwordx4 v14, s[40:43], s48 offen  lds\n\ts_add_u32 m0, s49, 21504\n\t"
      "s_add_u32 s48, s50, 0x28000\n\tbuffer_load_dwordx4 v15, s[40:43], s48 offen  lds\n\ts_add_u32 m0, s49, 22528\n\ts_add_u32 s48, s50, 0x30000\n\t"
      "buffer_load_dwordx4 v16, s[40:43], s48 offen  lds\n\ts_add_u32 m0, s49, 23552\n\ts_add_u32 s48, s50, 0x38000\n\tbuffer_load_dwordx4 v17, s[40:43], s48 offen  lds\n\t"
      "s_cmp_lt_u32 s68, 1\n\ts_cbranch_scc1 7f\n\ts_cmp_eq_u32 s68, 2\n\ts_cbranch_scc1 6f\n\t"
      "5:\n\ts_buffer_load_dword s48, s[12:15], s69 glc\n\ts_waitcnt lgkmcnt(0)\n\ts_cmp_eq_u32 s48, s71\n\ts_cbranch_scc1 6f\n\ts_cmp_lg_u32 s68, 3\n\ts_cbranch_scc1 7f\n\t"
      "s_cmp_eq_u32 s18, 0\n\ts_cbranch_scc1 6f\n\ts_sub_u32 s18, s18, 1\n\ts_branch 5b\n\t"
      "6:\n\tv_mov_b32 v13, 1\n\ts_mov_b64 exec, 1\n\tbuffer_atomic_add v13, off, s[12:15], s70\n\ts_mov_b64 exec, -1\n\ts_mov_b32 s68, 0\n\t"
      "7:\n\ts_waitcnt vmcnt(16)\n\ts_barrier\n\tds_read_b64_tr_b16 a[0:1], v20 offset:0\n\tds_read_b64_tr_b16 a[2:3], v20 offset:512\n\t"
      "ds_read_b64_tr_b16 a[8:9], v21 offset:0\n\tds_read_b64_tr_b16 a[10:11], v21 offset:512\n\tds_read_b64_tr_b16 a[48:49], v22 offset:0\n\t"
      "ds_read_b64_tr_b16 a[50:51], v22 offset:512\n\tds_read_b64_tr_b16 a[56:57], v23 offset:0\n\tds_read_b64_tr_b16 a[58:59], v23 offset:512\n\t"
      "ds_read_b64_tr_b16 a[64:65], v24 offset:0\n\tds_read_b64_tr_b16 a[66:67], v24 offset:512\n\tds_read_b64_tr_b16 v[208:209], v25 offset:0\n\t"
      "ds_read_b64_tr_b16 v[210:211], v25 offset:512\n\tds_read_b64_tr_b16 a[72:73], v26 offset:0\n\tds_read_b64_tr_b16 a[74:75], v26 offset:512\n\t"
      "ds_read_b64_tr_b16 a[80:81], v27 offset:0\n\tds_read_b64_tr_b16 a[82:83], v27 offset:512\n\ts_waitcnt lgkmcnt(0)\n\ts_add_u32 s50, s56, s65\n\t"
      "s_add_u32 m0, s49, 24576\n\ts_add_u32 s48, s50, 0x40000\n\tbuffer_load_dwordx4 v14, s[40:43], s48 offen  lds\n\ts_add_u32 m0, s49, 25600\n\t"
      "s_add_u32 s48, s50, 0x48000\n\tbuffer_load_dwordx4 v15, s[40:43], s48 offen  lds\n\ts_add_u32 m0, s49, 26624\n\ts_add_u32 s48, s50, 0x50000\n\t"
      "buffer_load_dwordx4 v16, s[40:43], s48 offen  lds\n\ts_add_u32 m0, s49, 27648\n\ts_add_u32 s48, s50, 0x58000\n\tbuffer_load_dwordx4 v17, s[40:43], s48 offen  lds\n\t"
      "s_add_u32 m0, s49, 28672\n\ts_add_u32 s48, s50, 0x60000\n\tbuffer_load_dwordx4 v14, s[40:43], s48 offen  lds\n\ts_add_u32 m0, s49, 29696\n\t"
      "s_add_u32 s48, s50, 0x68000\n\tbuffer_load_dwordx4 v15, s[40:43], s48 offen  lds\n\ts_add_u32 m0, s49, 30720\n\ts_add_u32 s48, s50, 0x70000\n\t"
      "buffer_load_dwordx4 v16, s[40:43], s48 offen  lds\n\ts_add_u32 m0, s49, 31744\n\ts_add_u32 s48, s50, 0x78000\n\tbuffer_load_dwordx4 v17, s[40:43], s48 offen  lds\n\t"
      "s_cmp_lt_u32 s68, 1\n\ts_cbranch_scc1 7f\n\ts_cmp_eq_u32 s68, 2\n\ts_cbranch_scc1 6f\n\t"
      "5:\n\ts_buffer_load_dword s48, s[12:15], s69 glc\n\ts_waitcnt lgkmcnt(0)\n\ts_cmp_eq_u32 s48, s71\n\ts_cbranch_scc0 7f\n\t"
      "6:\n\tv_mov_b32 v13, 1\n\ts_mov_b64 exec, 1\n\tbuffer_atomic_add v13, off, s[12:15], s70\n\ts_mov_b64 exec, -1\n\ts_mov_b32 s68, 0\n\t"
      "7:\n\ts_waitcnt vmcnt(16)\n\ts_barrier\n\tds_read_b64_tr_b16 a[4:5], v20 offset:8192\n\tds_read_b64_tr_b16 a[6:7], v20 offset:8704\n\t"
      "ds_read_b64_tr_b16 a[12:13], v21 offset:8192\n\tds_read_b64_tr_b16 a[14:15], v21 offset:8704\n\tds_read_b64_tr_b16 a[52:53], v22 offset:8192\n\t"
      "ds_read_b64_tr_b16 a[54:55], v22 offset:8704\n\tds_read_b64_tr_b16 a[60:61], v23 offset:8192\n\tds_read_b64_tr_b16 a[62:63], v23 offset:8704\n\t"
      "ds_read_b64_tr_b16 a[68:69], v24 offset:8192\n\tds_read_b64_tr_b16 a[70:71], v24 offset:8704\n\tds_read_b64_tr_b16 v[212:213], v25 offset:8192\n\t"
      "ds_read_b64_tr_b16 v[214:215], v25 offset:8704\n\tds_read_b64_tr_b16 a[76:77], v26 offset:8192\n\tds_read_b64_tr_b16 a[78:79], v26 offset:8704\n\t"
      "ds_read_b64_tr_b16 a[84:85], v27 offset:8192\n\tds_read_b64_tr_b16 a[86:87], v27 offset:8704\n\ts_waitcnt lgkmcnt(0)\n\ts_add_u32 s50, s56, s66\n\t"
      "s_add_u32 m0, s49, 0\n\ts_add_u32 s48, s50, 0x0\n\tbuffer_load_dwordx4 v14, s[40:43], s48 offen  lds\n\ts_add_u32 m0, s49, 1024\n\ts_add_u32 s48, s50, 0x8000\n\t"
      "buffer_load_dwordx4 v15, s[40:43], s48 offen  lds\n\ts_add_u32 m0, s49, 2048\n\ts_add_u32 s48, s50, 0x10000\n\tbuffer_load_dwordx4 v16, s[40:43], s48 offen  lds\n\t"
      "s_add_u32 m0, s49, 3072\n\ts_add_u32 s48, s50, 0x18000\n\tbuffer_load_dwordx4 v17, s[40:43], s48 offen  lds\n\ts_add_u32 m0, s49, 4096\n\ts_add_u32 s48, s50, 0x20000\n\t"
      "buffer_load_dwordx4 v14, s[40:43], s48 offen  lds\n\ts_add_u32 m0, s49, 5120\n\ts_add_u32 s48, s50, 0x28000\n\tbuffer_load_dwordx4 v15, s[40:43], s48 offen  lds\n\t"
      "s_add_u32 m0, s49, 6144\n\ts_add_u32 s48, s50, 0x30000\n\tbuffer_load_dwordx4 v16, s[40:43], s48 offen  lds\n\ts_add_u32 m0, s49, 7168\n\ts_add_u32 s48, s50, 0x38000\n\t"
      "buffer_load_dwordx4 v17, s[40:43], s48 offen  lds\n\ts_cmp_lt_u32 s68, 1\n\ts_cbranch_scc1 7f\n\ts_cmp_eq_u32 s68, 2\n\ts_cbranch_scc1 6f\n\t"
      "5:\n\ts_buffer_load_dword s48, s[12:15], s69 glc\n\ts_waitcnt lgkmcnt(0)\n\ts_cmp_eq_u32 s48, s71\n\ts_cbranch_scc0 7f\n\t"
      "6:\n\tv_mov_b32 v13, 1\n\ts_mov_b64 exec, 1\n\tbuffer_atomic_add v13, off, s[12:15], s70\n\ts_mov_b64 exec, -1\n\ts_mov_b32 s68, 0\n\t"
      "7:\n\ts_waitcnt vmcnt(16)\n\ts_barrier\n\tds_read_b64_tr_b16 a[16:17], v20 offset:16384\n\tds_read_b64_tr_b16 a[18:19], v20 offset:16896\n\t"
      "ds_read_b64_tr_b16 v[192:193], v21 offset:16384\n\tds_read_b64_tr_b16 v[194:195], v21 offset:16896\n\tds_read_b64_tr_b16 a[88:89], v22 offset:16384\n\t"
      "ds_read_b64_tr_b16 a[90:91], v22 offset:16896\n\tds_read_b64_tr_b16 v[216:217], v23 offset:16384\n\tds_read_b64_tr_b16 v[218:219], v23 offset:16896\n\t"
      "ds_read_b64_tr_b16 a[96:97], v24 offset:16384\n\tds_read_b64_tr_b16 a[98:99], v24 offset:16896\n\tds_read_b64_tr_b16 a[104:105], v25 offset:16384\n\t"
      "ds_read_b64_tr_b16 a[106:107], v25 offset:16896\n\tds_read_b64_tr_b16 a[112:113], v26 offset:16384\n\tds_read_b64_tr_b16 a[114:115], v26 offset:16896\n\t"
      "ds_read_b64_tr_b16 v[224:225], v27 offset:16384\n\tds_read_b64_tr_b16 v[226:227], v27 offset:16896\n\ts_waitcnt lgkmcnt(0)\n\ts_add_u32 s50, s56, s66\n\t"
      "s_add_u32 m0, s49, 8192\n\ts_add_u32 s48, s50, 0x40000\n\tbuffer_load_dwordx4 v14, s[40:43], s48 offen  lds\n\ts_add_u32 m0, s49, 9216\n\ts_add_u32 s48, s50, 0x48000\n\t"
      "buffer_load_dwordx4 v15, s[40:43], s48 offen  lds\n\ts_add_u32 m0, s49, 10240\n\ts_add_u32 s48, s50, 0x50000\n\tbuffer_load_dwordx4 v16, s[40:43], s48 offen  lds\n\t"
      "s_add_u32 m0, s49, 11264\n\ts_add_u32 s48, s50, 0x58000\n\tbuffer_load_dwordx4 v17, s[40:43], s48 offen  lds\n\ts_add_u32 m0, s49, 12288\n\t"
      "s_add_u32 s48, s50, 0x60000\n\tbuffer_load_dwordx4 v14, s[40:43], s48 offen  lds\n\ts_add_u32 m0, s49, 13312\n\ts_add_u32 s48, s50, 0x68000\n\t"
      "buffer_load_dwordx4 v15, s[40:43], s48 offen  lds\n\ts_add_u32 m0, s49, 14336\n\ts_add_u32 s48, s50, 0x70000\n\tbuffer_load_dwordx4 v16, s[40:43], s48 offen  lds\n\t"
      "s_add_u32 m0, s49, 15360\n\ts_add_u32 s48, s50, 0x78000\n\tbuffer_load_dwordx4 v17, s[40:43], s48 offen  lds\n\ts_cmp_lt_u32 s68, 1\n\ts_cbranch_scc1 7f\n\t"
      "s_cmp_eq_u32 s68, 2\n\ts_cbranch_scc1 6f\n\t"
      "5:\n\ts_buffer_load_dword s48, s[12:15], s69 glc\n\ts_waitcnt lgkmcnt(0)\n\ts_cmp_eq_u32 s48, s71\n\ts_cbranch_scc0 7f\n\t"
      "6:\n\tv_mov_b32 v13, 1\n\ts_mov_b64 exec, 1\n\tbuffer_atomic_add v13, off, s[12:15], s70\n\ts_mov_b64 exec, -1\n\ts_mov_b32 s68, 0\n\t"
      "7:\n\ts_waitcnt vmcnt(16)\n\ts_barrier\n\tds_read_b64_tr_b16 a[20:21], v20 offset:24576\n\tds_read_b64_tr_b16 a[22:23], v20 offset:25088\n\t"
      "ds_read_b64_tr_b16 v[196:197], v21 offset:24576\n\tds_read_b64_tr_b16 v[198:199], v21 offset:25088\n\tds_read_b64_tr_b16 a[92:93], v22 offset:24576\n\t"
      "ds_read_b64_tr_b16 a[94:95], v22 offset:25088\n\tds_read_b64_tr_b16 v[220:221], v23 offset:24576\n\tds_read_b64_tr_b16 v[222:223], v23 offset:25088\n\t"
      "ds_read_b64_tr_b16 a[100:101], v24 offset:24576\n\tds_read_b64_tr_b16 a[102:103], v24 offset:25088\n\tds_read_b64_tr_b16 a[108:109], v25 offset:24576\n\t"
      "ds_read_b64_tr_b16 a[110:111], v25 offset:25088\n\tds_read_b64_tr_b16 a[116:117], v26 offset:24576\n\tds_read_b64_tr_b16 a[118:119], v26 offset:25088\n\t"
      "ds_read_b64_tr_b16 v[228:229], v27 offset:24576\n\tds_read_b64_tr_b16 v[230:231], v27 offset:25088\n\ts_waitcnt lgkmcnt(0)\n\ts_add_u32 s50, s56, s67\n\t"
      "s_add_u32 m0, s49, 16384\n\ts_add_u32 s48, s50, 0x0\n\tbuffer_load_dwordx4 v14, s[40:43], s48 offen  lds\n\ts_add_u32 m0, s49, 17408\n\ts_add_u32 s48, s50, 0x8000\n\t"
      "buffer_load_dwordx4 v15, s[40:43], s48 offen  lds\n\ts_add_u32 m0, s49, 18432\n\ts_add_u32 s48, s50, 0x10000\n\tbuffer_load_dwordx4 v16, s[40:43], s48 offen  lds\n\t"
      "s_add_u32 m0, s49, 19456\n\ts_add_u32 s48, s50, 0x18000\n\tbuffer_load_dwordx4 v17, s[40:43], s48 offen  lds\n\ts_add_u32 m0, s49, 20480\n\t"
      "s_add_u32 s48, s50, 0x20000\n\tbuffer_load_dwordx4 v14, s[40:43], s48 offen  lds\n\ts_add_u32 m0, s49, 21504\n\ts_add_u32 s48, s50, 0x28000\n\t"
      "buffer_load_dwordx4 v15, s[40:43], s48 offen  lds\n\ts_add_u32 m0, s49, 22528\n\ts_add_u32 s48, s50, 0x30000\n\tbuffer_load_dwordx4 v16, s[40:43], s48 offen  lds\n\t"
      "s_add_u32 m0, s49, 23552\n\ts_add_u32 s48, s50, 0x38000\n\tbuffer_load_dwordx4 v17, s[40:43], s48 offen  lds\n\ts_cmp_lt_u32 s68, 1\n\ts_cbranch_scc1 7f\n\t"
      "s_cmp_eq_u32 s68, 2\n\ts_cbranch_scc1 6f\n\t"
      "5:\n\ts_buffer_load_dword s48, s[12:15], s69 glc\n\ts_waitcnt lgkmcnt(0)\n\ts_cmp_eq_u32 s48, s71\n\ts_cbranch_scc0 7f\n\t"
      "6:\n\tv_mov_b32 v13, 1\n\ts_mov_b64 exec, 1\n\tbuffer_atomic_add v13, off, s[12:15], s70\n\ts_mov_b64 exec, -1\n\ts_mov_b32 s68, 0\n\t"
      "7:\n\ts_waitcnt vmcnt(16)\n\ts_barrier\n\tds_read_b64_tr_b16 a[24:25], v20 offset:0\n\tds_read_b64_tr_b16 a[26:27], v20 offset:512\n\t"
      "ds_read_b64_tr_b16 a[32:33], v21 offset:0\n\tds_read_b64_tr_b16 a[34:35], v21 offset:512\n\tds_read_b64_tr_b16 a[120:121], v22 offset:0\n\t"
      "ds_read_b64_tr_b16 a[122:123], v22 offset:512\n\tds_read_b64_tr_b16 a[128:129], v23 offset:0\n\tds_read_b64_tr_b16 a[130:131], v23 offset:512\n\t"
      "ds_read_b64_tr_b16 a[136:137], v24 offset:0\n\tds_read_b64_tr_b16 a[138:139], v24 offset:512\n\tds_read_b64_tr_b16 v[232:233], v25 offset:0\n\t"
      "ds_read_b64_tr_b16 v[234:235], v25 offset:512\n\tds_read_b64_tr_b16 a[144:145], v26 offset:0\n\tds_read_b64_tr_b16 a[146:147], v26 offset:512\n\t"
      "ds_read_b64_tr_b16 a[152:153], v27 offset:0\n\tds_read_b64_tr_b16 a[154:155], v27 offset:512\n\ts_waitcnt lgkmcnt(0)\n\ts_add_u32 s50, s56, s67\n\t"
      "s_add_u32 m0, s49, 24576\n\ts_add_u32 s48, s50, 0x40000\n\tbuffer_load_dwordx4 v14, s[40:43], s48 offen  lds\n\ts_add_u32 m0, s49, 25600\n\t"
      "s_add_u32 s48, s50, 0x48000\n\tbuffer_load_dwordx4 v15, s[40:43], s48 offen  lds\n\ts_add_u32 m0, s49, 26624\n\ts_add_u32 s48, s50, 0x50000\n\t"
      "buffer_load_dwordx4 v16, s[40:43], s48 offen  lds\n\ts_add_u32 m0, s49, 27648\n\ts_add_u32 s48, s50, 0x58000\n\tbuffer_load_dwordx4 v17, s[40:43], s48 offen  lds\n\t"
      "s_add_u32 m0, s49, 28672\n\ts_add_u32 s48, s50, 0x60000\n\tbuffer_load_dwordx4 v14, s[40:43], s48 offen  lds\n\ts_add_u32 m0, s49, 29696\n\t"
      "s_add_u32 s48, s50, 0x68000\n\tbuffer_load_dwordx4 v15, s[40:43], s48 offen  lds\n\ts_add_u32 m0, s49, 30720\n\ts_add_u32 s48, s50, 0x70000\n\t"
      "buffer_load_dwordx4 v16, s[40:43], s48 offen  lds\n\ts_add_u32 m0, s49, 31744\n\ts_add_u32 s48, s50, 0x78000\n\tbuffer_load_dwordx4 v17, s[40:43], s48 offen  lds\n\t"
      "s_cmp_lt_u32 s68, 1\n\ts_cbranch_scc1 7f\n\ts_cmp_eq_u32 s68, 2\n\ts_cbranch_scc1 6f\n\t"
      "5:\n\ts_buffer_load_dword s48, s[12:15], s69 glc\n\ts_waitcnt lgkmcnt(0)\n\ts_cmp_eq_u32 s48, s71\n\ts_cbranch_scc0 7f\n\t"
      "6:\n\tv_mov_b32 v13, 1\n\ts_mov_b64 exec, 1\n\tbuffer_atomic_add v13, off, s[12:15], s70\n\ts_mov_b64 exec, -1\n\ts_mov_b32 s68, 0\n\t"
      "7:\n\ts_waitcnt vmcnt(16)\n\ts_barrier\n\tds_read_b64_tr_b16 a[28:29], v20 offset:8192\n\tds_read_b64_tr_b16 a[30:31], v20 offset:8704\n\t"
      "ds_read_b64_tr_b16 a[36:37], v21 offset:8192\n\tds_read_b64_tr_b16 a[38:39], v21 offset:8704\n\tds_read_b64_tr_b16 a[124:125], v22 offset:8192\n\t"
      "ds_read_b64_tr_b16 a[126:127], v22 offset:8704\n\tds_read_b64_tr_b16 a[132:133], v23 offset:8192\n\tds_read_b64_tr_b16 a[134:135], v23 offset:8704\n\t"
      "ds_read_b64_tr_b16 a[140:141], v24 offset:8192\n\tds_read_b64_tr_b16 a[142:143], v24 offset:8704\n\tds_read_b64_tr_b16 v[236:237], v25 offset:8192\n\t"
      "ds_read_b64_tr_b16 v[238:239], v25 offset:8704\n\tds_read_b64_tr_b16 a[148:149], v26 offset:8192\n\tds_read_b64_tr_b16 a[150:151], v26 offset:8704\n\t"
      "ds_read_b64_tr_b16 a[156:157], v27 offset:8192\n\tds_read_b64_tr_b16 a[158:159], v27 offset:8704\n\ts_waitcnt lgkmcnt(0)\n\ts_add_u32 m0, s49, 0\n\t"
      "s_add_u32 s48, s57, 0x0\n\tbuffer_load_dwordx4 v18, s[44:47], s48 offen  lds\n\ts_add_u32 m0, s49, 1024\n\ts_add_u32 s48, s57, 0x2000\n\t"
      "buffer_load_dwordx4 v18, s[44:47], s48 offen  lds\n\ts_add_u32 m0, s49, 2048\n\ts_add_u32 s48, s57, 0x4000\n\tbuffer_load_dwordx4 v18, s[44:47], s48 offen  lds\n\t"
      "s_add_u32 m0, s49, 3072\n\ts_add_u32 s48, s57, 0x6000\n\tbuffer_load_dwordx4 v18, s[44:47], s48 offen  lds\n\ts_add_u32 m0, s49, 4096\n\ts_add_u32 s48, s57, 0x8000\n\t"
      "buffer_load_dwordx4 v19, s[44:47], s48 offen  lds\n\ts_add_u32 m0, s49, 5120\n\ts_add_u32 s48, s57, 0xa000\n\tbuffer_load_dwordx4 v19, s[44:47], s48 offen  lds\n\t"
      "s_add_u32 m0, s49, 6144\n\ts_add_u32 s48, s57, 0xc000\n\tbuffer_load_dwordx4 v19, s[44:47], s48 offen  lds\n\ts_add_u32 m0, s49, 7168\n\ts_add_u32 s48, s57, 0xe000\n\t"
      "buffer_load_dwordx4 v19, s[44:47], s48 offen  lds\n\ts_cmp_lt_u32 s68, 1\n\ts_cbranch_scc1 7f\n\ts_cmp_eq_u32 s68, 2\n\ts_cbranch_scc1 6f\n\t"
      "5:\n\ts_buffer_load_dword s48, s[12:15], s69 glc\n\ts_waitcnt lgkmcnt(0)\n\ts_cmp_eq_u32 s48, s71\n\ts_cbranch_scc0 7f\n\t"
      "6:\n\tv_mov_b32 v13, 1\n\ts_mov_b64 exec, 1\n\tbuffer_atomic_add v13, off, s[12:15], s70\n\ts_mov_b64 exec, -1\n\ts_mov_b32 s68, 0\n\t"
      "7:\n\ts_waitcnt vmcnt(16)\n\ts_barrier\n\tds_read_b64_tr_b16 a[40:41], v20 offset:16384\n\tds_read_b64_tr_b16 a[42:43], v20 offset:16896\n\t"
      "ds_read_b64_tr_b16 v[200:201], v21 offset:16384\n\tds_read_b64_tr_b16 v[202:203], v21 offset:16896\n\tds_read_b64_tr_b16 a[160:161], v22 offset:16384\n\t"
      "ds_read_b64_tr_b16 a[162:163], v22 offset:16896\n\tds_read_b64_tr_b16 v[240:241], v23 offset:16384\n\tds_read_b64_tr_b16 v[242:243], v23 offset:16896\n\t"
      "ds_read_b64_tr_b16 a[168:169], v24 offset:16384\n\tds_read_b64_tr_b16 a[170:171], v24 offset:16896\n\tds_read_b64_tr_b16 a[176:177], v25 offset:16384\n\t"
      "ds_read_b64_tr_b16 a[178:179], v25 offset:16896\n\tds_read_b64_tr_b16 a[184:185], v26 offset:16384\n\tds_read_b64_tr_b16 a[186:187], v26 offset:16896\n\t"
      "ds_read_b64_tr_b16 v[248:249], v27 offset:16384\n\tds_read_b64_tr_b16 v[250:251], v27 offset:16896\n\ts_waitcnt lgkmcnt(0)\n\ts_add_u32 m0, s49, 8192\n\t"
      "s_add_u32 s48, s57, 0x10000\n\tbuffer_load_dwordx4 v18, s[44:47], s48 offen  lds\n\ts_add_u32 m0, s49, 9216\n\ts_add_u32 s48, s57, 0x12000\n\t"
      "buffer_load_dwordx4 v18, s[44:47], s48 offen  lds\n\ts_add_u32 m0, s49, 10240\n\ts_add_u32 s48, s57, 0x14000\n\tbuffer_load_dwordx4 v18, s[44:47], s48 offen  lds\n\t"
      "s_add_u32 m0, s49, 11264\n\ts_add_u32 s48, s57, 0x16000\n\tbuffer_load_dwordx4 v18, s[44:47], s48 offen  lds\n\ts_add_u32 m0, s49, 12288\n\t"
      "s_add_u32 s48, s57, 0x18000\n\tbuffer_load_dwordx4 v19, s[44:47], s48 offen  lds\n\ts_add_u32 m0, s49, 13312\n\ts_add_u32 s48, s57, 0x1a000\n\t"
      "buffer_load_dwordx4 v19, s[44:47], s48 offen  lds\n\ts_add_u32 m0, s49, 14336\n\ts_add_u32 s48, s57, 0x1c000\n\tbuffer_load_dwordx4 v19, s[44:47], s48 offen  lds\n\t"
      "s_add_u32 m0, s49, 15360\n\ts_add_u32 s48, s57, 0x1e000\n\tbuffer_load_dwordx4 v19, s[44:47], s48 offen  lds\n\ts_cmp_lt_u32 s68, 1\n\ts_cbranch_scc1 7f\n\t"
      "s_cmp_eq_u32 s68, 2\n\ts_cbranch_scc1 6f\n\t"
      "5:\n\ts_buffer_load_dword s48, s[12:15], s69 glc\n\ts_waitcnt lgkmcnt(0)\n\ts_cmp_eq_u32 s48, s71\n\ts_cbranch_scc0 7f\n\t"
      "6:\n\tv_mov_b32 v13, 1\n\ts_mov_b64 exec, 1\n\tbuffer_atomic_add v13, off, s[12:15], s70\n\ts_mov_b64 exec, -1\n\ts_mov_b32 s68, 0\n\t"
      "7:\n\ts_waitcnt vmcnt(16)\n\ts_barrier\n\tds_read_b64_tr_b16 a[44:45], v20 offset:24576\n\tds_read_b64_tr_b16 a[46:47], v20 offset:25088\n\t"
      "ds_read_b64_tr_b16 v[204:205], v21 offset:24576\n\tds_read_b64_tr_b16 v[206:207], v21 offset:25088\n\tds_read_b64_tr_b16 a[164:165], v22 offset:24576\n\t"
      "ds_read_b64_tr_b16 a[166:167], v22 offset:25088\n\tds_read_b64_tr_b16 v[244:245], v23 offset:24576\n\tds_read_b64_tr_b16 v[246:247], v23 offset:25088\n\t"
      "ds_read_b64_tr_b16 a[172:173], v24 offset:24576\n\tds_read_b64_tr_b16 a[174:175], v24 offset:25088\n\tds_read_b64_tr_b16 a[180:181], v25 offset:24576\n\t"
      "ds_read_b64_tr_b16 a[182:183], v25 offset:25088\n\tds_read_b64_tr_b16 a[188:189], v26 offset:24576\n\tds_read_b64_tr_b16 a[190:191], v26 offset:25088\n\t"
      "ds_read_b64_tr_b16 v[252:253], v27 offset:24576\n\tds_read_b64_tr_b16 v[254:255], v27 offset:25088\n\ts_waitcnt lgkmcnt(0)\n\ts_add_u32 m0, s49, 16384\n\t"
      "s_add_u32 s48, s57, 0x20000\n\tbuffer_load_dwordx4 v18, s[44:47], s48 offen  lds\n\ts_add_u32 m0, s49, 17408\n\ts_add_u32 s48, s57, 0x22000\n\t"
      "buffer_load_dwordx4 v18, s[44:47], s48 offen  lds\n\ts_add_u32 m0, s49, 18432\n\ts_add_u32 s48, s57, 0x24000\n\tbuffer_load_dwordx4 v18, s[44:47], s48 offen  lds\n\t"
      "s_add_u32 m0, s49, 19456\n\ts_add_u32 s48, s57, 0x26000\n\tbuffer_load_dwordx4 v18, s[44:47], s48 offen  lds\n\ts_add_u32 m0, s49, 20480\n\t"
      "s_add_u32 s48, s57, 0x28000\n\tbuffer_load_dwordx4 v19, s[44:47], s48 offen  lds\n\ts_add_u32 m0, s49, 21504\n\ts_add_u32 s48, s57, 0x2a000\n\t"
      "buffer_load_dwordx4 v19, s[44:47], s48 offen  lds\n\ts_add_u32 m0, s49, 22528\n\ts_add_u32 s48, s57, 0x2c000\n\tbuffer_load_dwordx4 v19, s[44:47], s48 offen  lds\n\t"
      "s_add_u32 m0, s49, 23552\n\ts_add_u32 s48, s57, 0x2e000\n\tbuffer_load_dwordx4 v19, s[44:47], s48 offen  lds\n\ts_cmp_lt_u32 s68, 1\n\ts_cbranch_scc1 7f\n\t"
      "s_cmp_eq_u32 s68, 2\n\ts_cbranch_scc1 6f\n\t"
      "5:\n\ts_buffer_load_dword s48, s[12:15], s69 glc\n\ts_waitcnt lgkmcnt(0)\n\ts_cmp_eq_u32 s48, s71\n\ts_cbranch_scc0 7f\n\t"
      "6:\n\tv_mov_b32 v13, 1\n\ts_mov_b64 exec, 1\n\tbuffer_atomic_add v13, off, s[12:15], s70\n\ts_mov_b64 exec, -1\n\ts_mov_b32 s68, 0\n\t"
      "7:\n\ts_waitcnt vmcnt(16)\n\ts_barrier\n\tds_read_b64_tr_b16 a[192:193], v28 offset:0\n\tds_read_b64_tr_b16 a[194:195], v28 offset:256\n\t"
      "ds_read_b64_tr_b16 a[196:197], v28 offset:2048\n\tds_read_b64_tr_b16 a[198:199], v28 offset:2304\n\tds_read_b64_tr_b16 a[224:225], v29 offset:0\n\t"
      "ds_read_b64_tr_b16 a[226:227], v29 offset:256\n\tds_read_b64_tr_b16 a[228:229], v29 offset:2048\n\tds_read_b64_tr_b16 a[230:231], v29 offset:2304\n\t"
      "ds_read_b64_tr_b16 v[56:57], v30 offset:0\n\tds_read_b64_tr_b16 v[58:59], v30 offset:256\n\tds_read_b64_tr_b16 v[60:61], v30 offset:2048\n\t"
      "ds_read_b64_tr_b16 v[62:63], v30 offset:2304\n\tds_read_b64_tr_b16 v[160:161], v31 offset:0\n\tds_read_b64_tr_b16 v[162:163], v31 offset:256\n\t"
      "ds_read_b64_tr_b16 v[164:165], v31 offset:2048\n\tds_read_b64_tr_b16 v[166:167], v31 offset:2304\n\ts_waitcnt lgkmcnt(0)\n\ts_add_u32 m0, s49, 24576\n\t"
      "s_add_u32 s48, s57, 0x30000\n\tbuffer_load_dwordx4 v18, s[44:47], s48 offen  lds\n\ts_add_u32 m0, s49, 25600\n\ts_add_u32 s48, s57, 0x32000\n\t"
      "buffer_load_dwordx4 v18, s[44:47], s48 offen  lds\n\ts_add_u32 m0, s49, 26624\n\ts_add_u32 s48, s57, 0x34000\n\tbuffer_load_dwordx4 v18, s[44:47], s48 offen  lds\n\t"
      "s_add_u32 m0, s49, 27648\n\ts_add_u32 s48, s57, 0x36000\n\tbuffer_load_dwordx4 v18, s[44:47], s48 offen  lds\n\ts_add_u32 m0, s49, 28672\n\t"
      "s_add_u32 s48, s57, 0x38000\n\tbuffer_load_dwordx4 v19, s[44:47], s48 offen  lds\n\ts_add_u32 m0, s49, 29696\n\ts_add_u32 s48, s57, 0x3a000\n\t"
      "buffer_load_dwordx4 v19, s[44:47], s48 offen  lds\n\ts_add_u32 m0, s49, 30720\n\ts_add_u32 s48, s57, 0x3c000\n\tbuffer_load_dwordx4 v19, s[44:47], s48 offen  lds\n\t"
      "s_add_u32 m0, s49, 31744\n\ts_add_u32 s48, s57, 0x3e000\n\tbuffer_load_dwordx4 v19, s[44:47], s48 offen  lds\n\tv_xor_b32 v18, 0x100, v18\n\tv_xor_b32 v19, 0x100, v19\n\t"
      "s_cmp_lt_u32 s68, 1\n\ts_cbranch_scc1 7f\n\ts_cmp_eq_u32 s68, 2\n\ts_cbranch_scc1 6f\n\t"
      "5:\n\ts_buffer_load_dword s48, s[12:15], s69 glc\n\ts_waitcnt lgkmcnt(0)\n\ts_cmp_eq_u32 s48, s71\n\ts_cbranch_scc0 7f\n\t"
      "6:\n\tv_mov_b32 v13, 1\n\ts_mov_b64 exec, 1\n\tbuffer_atomic_add v13, off, s[12:15], s70\n\ts_mov_b64 exec, -1\n\ts_mov_b32 s68, 0\n\t"
      "7:\n\ts_waitcnt vmcnt(16)\n\tds_read_b64_tr_b16 a[200:201], v28 offset:8192\n\tds_read_b64_tr_b16 a[202:203], v28 offset:8448\n\t"
      "ds_read_b64_tr_b16 a[204:205], v28 offset:10240\n\tds_read_b64_tr_b16 a[206:207], v28 offset:10496\n\tds_read_b64_tr_b16 a[232:233], v29 offset:8192\n\t"
      "ds_read_b64_tr_b16 a[234:235], v29 offset:8448\n\tds_read_b64_tr_b16 a[236:237], v29 offset:10240\n\tds_read_b64_tr_b16 a[238:239], v29 offset:10496\n\t"
      "ds_read_b64_tr_b16 v[88:89], v30 offset:8192\n\tds_read_b64_tr_b16 v[90:91], v30 offset:8448\n\tds_read_b64_tr_b16 v[92:93], v30 offset:10240\n\t"
      "ds_read_b64_tr_b16 v[94:95], v30 offset:10496\n\tds_read_b64_tr_b16 v[168:169], v31 offset:8192\n\tds_read_b64_tr_b16 v[170:171], v31 offset:8448\n\t"
      "ds_read_b64_tr_b16 v[172:173], v31 offset:10240\n\tds_read_b64_tr_b16 v[174:175], v31 offset:10496\n\ts_waitcnt lgkmcnt(0)\n\ts_add_u32 m0, s49, 0\n\t"
      "s_add_u32 s48, s57, 0x0\n\tbuffer_load_dwordx4 v18, s[44:47], s48 offen  lds\n\ts_add_u32 m0, s49, 1024\n\ts_add_u32 s48, s57, 0x2000\n\t"
      "buffer_load_dwordx4 v18, s[44:47], s48 offen  lds\n\ts_add_u32 m0, s49, 2048\n\ts_add_u32 s48, s57, 0x4000\n\tbuffer_load_dwordx4 v18, s[44:47], s48 offen  lds\n\t"
      "s_add_u32 m0, s49, 3072\n\ts_add_u32 s48, s57, 0x6000\n\tbuffer_load_dwordx4 v18, s[44:47], s48 offen  lds\n\ts_add_u32 m0, s49, 4096\n\ts_add_u32 s48, s57, 0x8000\n\t"
      "buffer_load_dwordx4 v19, s[44:47], s48 offen  lds\n\ts_add_u32 m0, s49, 5120\n\ts_add_u32 s48, s57, 0xa000\n\tbuffer_load_dwordx4 v19, s[44:47], s48 offen  lds\n\t"
      "s_add_u32 m0, s49, 6144\n\ts_add_u32 s48, s57, 0xc000\n\tbuffer_load_dwordx4 v19, s[44:47], s48 offen  lds\n\ts_add_u32 m0, s49, 7168\n\ts_add_u32 s48, s57, 0xe000\n\t"
      "buffer_load_dwordx4 v19, s[44:47], s48 offen  lds\n\ts_waitcnt vmcnt(16)\n\tds_read_b64_tr_b16 a[208:209], v28 offset:16384\n\t"
      "ds_read_b64_tr_b16 a[210:211], v28 offset:16640\n\tds_read_b64_tr_b16 a[212:213], v28 offset:18432\n\tds_read_b64_tr_b16 a[214:215], v28 offset:18688\n\t"
      "ds_read_b64_tr_b16 a[240:241], v29 offset:16384\n\tds_read_b64_tr_b16 a[242:243], v29 offset:16640\n\tds_read_b64_tr_b16 a[244:245], v29 offset:18432\n\t"
      "ds_read_b64_tr_b16 a[246:247], v29 offset:18688\n\tds_read_b64_tr_b16 v[120:121], v30 offset:16384\n\tds_read_b64_tr_b16 v[122:123], v30 offset:16640\n\t"
      "ds_read_b64_tr_b16 v[124:125], v30 offset:18432\n\tds_read_b64_tr_b16 v[126:127], v30 offset:18688\n\tds_read_b64_tr_b16 v[176:177], v31 offset:16384\n\t"
      "ds_read_b64_tr_b16 v[178:179], v31 offset:16640\n\tds_read_b64_tr_b16 v[180:181], v31 offset:18432\n\tds_read_b64_tr_b16 v[182:183], v31 offset:18688\n\t"
      "s_waitcnt lgkmcnt(0)\n\ts_add_u32 m0, s49, 8192\n\ts_add_u32 s48, s57, 0x10000\n\tbuffer_load_dwordx4 v18, s[44:47], s48 offen  lds\n\ts_add_u32 m0, s49, 9216\n\t"
      "s_add_u32 s48, s57, 0x12000\n\tbuffer_load_dwordx4 v18, s[44:47], s48 offen  lds\n\ts_add_u32 m0, s49, 10240\n\ts_add_u32 s48, s57, 0x14000\n\t"
      "buffer_load_dwordx4 v18, s[44:47], s48 offen  lds\n\ts_add_u32 m0, s49, 11264\n\ts_add_u32 s48, s57, 0x16000\n\tbuffer_load_dwordx4 v18, s[44:47], s48 offen  lds\n\t"
      "s_add_u32 m0, s49, 12288\n\ts_add_u32 s48, s57, 0x18000\n\tbuffer_load_dwordx4 v19, s[44:47], s48 offen  lds\n\ts_add_u32 m0, s49, 13312\n\t"
      "s_add_u32 s48, s57, 0x1a000\n\tbuffer_load_dwordx4 v19, s[44:47], s48 offen  lds\n\ts_add_u32 m0, s49, 14336\n\ts_add_u32 s48, s57, 0x1c000\n\t"
      "buffer_load_dwordx4 v19, s[44:47], s48 offen  lds\n\ts_add_u32 m0, s49, 15360\n\ts_add_u32 s48, s57, 0x1e000\n\tbuffer_load_dwordx4 v19, s[44:47], s48 offen  lds\n\t"
      "s_waitcnt vmcnt(16)\n\tds_read_b64_tr_b16 a[216:217], v28 offset:24576\n\tds_read_b64_tr_b16 a[218:219], v28 offset:24832\n\t"
      "ds_read_b64_tr_b16 a[220:221], v28 offset:26624\n\tds_read_b64_tr_b16 a[222:223], v28 offset:26880\n\tds_read_b64_tr_b16 a[248:249], v29 offset:24576\n\t"
      "ds_read_b64_tr_b16 a[250:251], v29 offset:24832\n\tds_read_b64_tr_b16 a[252:253], v29 offset:26624\n\tds_read_b64_tr_b16 a[254:255], v29 offset:26880\n\t"
      "ds_read_b64_tr_b16 v[152:153], v30 offset:24576\n\tds_read_b64_tr_b16 v[154:155], v30 offset:24832\n\tds_read_b64_tr_b16 v[156:157], v30 offset:26624\n\t"
      "ds_read_b64_tr_b16 v[158:159], v30 offset:26880\n\tds_read_b64_tr_b16 v[184:185], v31 offset:24576\n\tds_read_b64_tr_b16 v[186:187], v31 offset:24832\n\t"
      "ds_read_b64_tr_b16 v[188:189], v31 offset:26624\n\tds_read_b64_tr_b16 v[190:191], v31 offset:26880\n\ts_waitcnt lgkmcnt(0)\n\ts_add_u32 m0, s49, 16384\n\t"
      "s_add_u32 s48, s57, 0x20000\n\tbuffer_load_dwordx4 v18, s[44:47], s48 offen  lds\n\ts_add_u32 m0, s49, 17408\n\ts_add_u32 s48, s57, 0x22000\n\t"
      "buffer_load_dwordx4 v18, s[44:47], s48 offen  lds\n\ts_add_u32 m0, s49, 18432\n\ts_add_u32 s48, s57, 0x24000\n\tbuffer_load_dwordx4 v18, s[44:47], s48 offen  lds\n\t"
      "s_add_u32 m0, s49, 19456\n\ts_add_u32 s48, s57, 0x26000\n\tbuffer_load_dwordx4 v18, s[44:47], s48 offen  lds\n\ts_add_u32 m0, s49, 20480\n\t"
      "s_add_u32 s48, s57, 0x28000\n\tbuffer_load_dwordx4 v19, s[44:47], s48 offen  lds\n\ts_add_u32 m0, s49, 21504\n\ts_add_u32 s48, s57, 0x2a000\n\t"
      "buffer_load_dwordx4 v19, s[44:47], s48 offen  lds\n\ts_add_u32 m0, s49, 22528\n\ts_add_u32 s48, s57, 0x2c000\n\tbuffer_load_dwordx4 v19, s[44:47], s48 offen  lds\n\t"
      "s_add_u32 m0, s49, 23552\n\ts_add_u32 s48, s57, 0x2e000\n\tbuffer_load_dwordx4 v19, s[44:47], s48 offen  lds\n\ts_waitcnt vmcnt(16)\n\t"
      "ds_read_b64_tr_b16 v[32:33], v28 offset:0\n\tds_read_b64_tr_b16 v[34:35], v28 offset:256\n\tds_read_b64_tr_b16 v[36:37], v28 offset:2048\n\t"
      "ds_read_b64_tr_b16 v[38:39], v28 offset:2304\n\tds_read_b64_tr_b16 v[64:65], v29 offset:0\n\tds_read_b64_tr_b16 v[66:67], v29 offset:256\n\t"
      "ds_read_b64_tr_b16 v[68:69], v29 offset:2048\n\tds_read_b64_tr_b16 v[70:71], v29 offset:2304\n\tds_read_b64_tr_b16 v[96:97], v30 offset:0\n\t"
      "ds_read_b64_tr_b16 v[98:99], v30 offset:256\n\tds_read_b64_tr_b16 v[100:101], v30 offset:2048\n\tds_read_b64_tr_b16 v[102:103], v30 offset:2304\n\t"
      "ds_read_b64_tr_b16 v[128:129], v31 offset:0\n\tds_read_b64_tr_b16 v[130:131], v31 offset:256\n\tds_read_b64_tr_b16 v[132:133], v31 offset:2048\n\t"
      "ds_read_b64_tr_b16 v[134:135], v31 offset:2304\n\ts_waitcnt lgkmcnt(0)\n\tds_write_b128 v5, v[56:59] offset:0\n\tds_write_b128 v5, v[60:63] offset:1024\n\t"
      "ds_write_b128 v5, v[88:91] offset:2048\n\tds_write_b128 v5, v[92:95] offset:3072\n\tds_write_b128 v5, v[120:123] offset:4096\n\t"
      "ds_write_b128 v5, v[124:127] offset:5120\n\tds_write_b128 v5, v[152:155] offset:6144\n\tds_write_b128 v5, v[156:159] offset:7168\n\ts_add_u32 m0, s49, 24576\n\t"
      "s_add_u32 s48, s57, 0x30000\n\tbuffer_load_dwordx4 v18, s[44:47], s48 offen  lds\n\ts_add_u32 m0, s49, 25600\n\ts_add_u32 s48, s57, 0x32000\n\t"
      "buffer_load_dwordx4 v18, s[44:47], s48 offen  lds\n\ts_add_u32 m0, s49, 26624\n\ts_add_u32 s48, s57, 0x34000\n\tbuffer_load_dwordx4 v18, s[44:47], s48 offen  lds\n\t"
      "s_add_u32 m0, s49, 27648\n\ts_add_u32 s48, s57, 0x36000\n\tbuffer_load_dwordx4 v18, s[44:47], s48 offen  lds\n\ts_add_u32 m0, s49, 28672\n\t"
      "s_add_u32 s48, s57, 0x38000\n\tbuffer_load_dwordx4 v19, s[44:47], s48 offen  lds\n\ts_add_u32 m0, s49, 29696\n\ts_add_u32 s48, s57, 0x3a000\n\t"
      "buffer_load_dwordx4 v19, s[44:47], s48 offen  lds\n\ts_add_u32 m0, s49, 30720\n\ts_add_u32 s48, s57, 0x3c000\n\tbuffer_load_dwordx4 v19, s[44:47], s48 offen  lds\n\t"
      "s_add_u32 m0, s49, 31744\n\ts_add_u32 s48, s57, 0x3e000\n\tbuffer_load_dwordx4 v19, s[44:47], s48 offen  lds\n\ts_waitcnt vmcnt(16)\n\t"
      "ds_read_b64_tr_b16 v[40:41], v28 offset:8192\n\tds_read_b64_tr_b16 v[42:43], v28 offset:8448\n\tds_read_b64_tr_b16 v[44:45], v28 offset:10240\n\t"
      "ds_read_b64_tr_b16 v[46:47], v28 offset:10496\n\tds_read_b64_tr_b16 v[72:73], v29 offset:8192\n\tds_read_b64_tr_b16 v[74:75], v29 offset:8448\n\t"
      "ds_read_b64_tr_b16 v[76:77], v29 offset:10240\n\tds_read_b64_tr_b16 v[78:79], v29 offset:10496\n\tds_read_b64_tr_b16 v[104:105], v30 offset:8192\n\t"
      "ds_read_b64_tr_b16 v[106:107], v30 offset:8448\n\tds_read_b64_tr_b16 v[108:109], v30 offset:10240\n\tds_read_b64_tr_b16 v[110:111], v30 offset:10496\n\t"
      "ds_read_b64_tr_b16 v[136:137], v31 offset:8192\n\tds_read_b64_tr_b16 v[138:139], v31 offset:8448\n\tds_read_b64_tr_b16 v[140:141], v31 offset:10240\n\t"
      "ds_read_b64_tr_b16 v[142:143], v31 offset:10496\n\ts_waitcnt lgkmcnt(0)\n\tds_write_b128 v5, v[160:163] offset:8192\n\tds_write_b128 v5, v[164:167] offset:9216\n\t"
      "ds_write_b128 v5, v[168:171] offset:10240\n\tds_write_b128 v5, v[172:175] offset:11264\n\tds_write_b128 v5, v[176:179] offset:12288\n\t"
      "ds_write_b128 v5, v[180:183] offset:13312\n\tds_write_b128 v5, v[184:187] offset:14336\n\tds_write_b128 v5, v[188:191] offset:15360\n\ts_waitcnt vmcnt(8)\n\t"
      "ds_read_b64_tr_b16 v[48:49], v28 offset:16384\n\tds_read_b64_tr_b16 v[50:51], v28 offset:16640\n\tds_read_b64_tr_b16 v[52:53], v28 offset:18432\n\t"
      "ds_read_b64_tr_b16 v[54:55], v28 offset:18688\n\tds_read_b64_tr_b16 v[80:81], v29 offset:16384\n\tds_read_b64_tr_b16 v[82:83], v29 offset:16640\n\t"
      "ds_read_b64_tr_b16 v[84:85], v29 offset:18432\n\tds_read_b64_tr_b16 v[86:87], v29 offset:18688\n\tds_read_b64_tr_b16 v[112:113], v30 offset:16384\n\t"
      "ds_read_b64_tr_b16 v[114:115], v30 offset:16640\n\tds_read_b64_tr_b16 v[116:117], v30 offset:18432\n\tds_read_b64_tr_b16 v[118:119], v30 offset:18688\n\t"
      "ds_read_b64_tr_b16 v[144:145], v31 offset:16384\n\tds_read_b64_tr_b16 v[146:147], v31 offset:16640\n\tds_read_b64_tr_b16 v[148:149], v31 offset:18432\n\t"
      "ds_read_b64_tr_b16 v[150:151], v31 offset:18688\n\ts_waitcnt lgkmcnt(0)\n\tds_write_b128 v5, v[192:195] offset:16384\n\tds_write_b128 v5, v[196:199] offset:17408\n\t"
      "ds_write_b128 v5, v[200:203] offset:18432\n\tds_write_b128 v5, v[204:207] offset:19456\n\tds_write_b128 v5, v[208:211] offset:20480\n\t"
      "ds_write_b128 v5, v[212:215] offset:21504\n\tds_write_b128 v5, v[216:219] offset:22528\n\tds_write_b128 v5, v[220:223] offset:23552\n\ts_cmp_lt_u32 s68, 1\n\t"
      "s_cbranch_scc1 7f\n\ts_cmp_eq_u32 s68, 2\n\ts_cbranch_scc1 6f\n\t"
      "5:\n\ts_buffer_load_dword s48, s[12:15], s69 glc\n\ts_waitcnt lgkmcnt(0)\n\ts_cmp_eq_u32 s48, s71\n\ts_cbranch_scc1 6f\n\ts_cmp_eq_u32 s18, 0\n\ts_cbranch_scc1 6f\n\t"
      "s_sub_u32 s18, s18, 1\n\ts_branch 5b\n\t"
      "6:\n\tv_mov_b32 v13, 1\n\ts_mov_b64 exec, 1\n\tbuffer_atomic_add v13, off, s[12:15], s70\n\ts_mov_b64 exec, -1\n\ts_mov_b32 s68, 0\n\t"
      "7:\n\ts_waitcnt vmcnt(0)\n\tds_read_b64_tr_b16 v[56:57], v28 offset:24576\n\tds_read_b64_tr_b16 v[58:59], v28 offset:24832\n\t"
      "ds_read_b64_tr_b16 v[60:61], v28 offset:26624\n\tds_read_b64_tr_b16 v[62:63], v28 offset:26880\n\tds_read_b64_tr_b16 v[88:89], v29 offset:24576\n\t"
      "ds_read_b64_tr_b16 v[90:91], v29 offset:24832\n\tds_read_b64_tr_b16 v[92:93], v29 offset:26624\n\tds_read_b64_tr_b16 v[94:95], v29 offset:26880\n\t"
      "ds_read_b64_tr_b16 v[120:121], v30 offset:24576\n\tds_read_b64_tr_b16 v[122:123], v30 offset:24832\n\tds_read_b64_tr_b16 v[124:125], v30 offset:26624\n\t"
      "ds_read_b64_tr_b16 v[126:127], v30 offset:26880\n\tds_read_b64_tr_b16 v[152:153], v31 offset:24576\n\tds_read_b64_tr_b16 v[154:155], v31 offset:24832\n\t"
      "ds_read_b64_tr_b16 v[156:157], v31 offset:26624\n\tds_read_b64_tr_b16 v[158:159], v31 offset:26880\n\ts_waitcnt lgkmcnt(0)\n\t"
      "ds_write_b128 v5, v[224:227] offset:24576\n\tds_write_b128 v5, v[228:231] offset:25600\n\tds_write_b128 v5, v[232:235] offset:26624\n\t"
      "ds_write_b128 v5, v[236:239] offset:27648\n\tds_write_b128 v5, v[240:243] offset:28672\n\tds_write_b128 v5, v[244:247] offset:29696\n\t"
      "ds_write_b128 v5, v[248:251] offset:30720\n\tds_write_b128 v5, v[252:255] offset:31744\n\t"
      "v_mov_b64 v[30:31], 0\n\tv_mov_b32 v29, 160000\n\tds_write_b64 v29, v[30:31]\n\t"
      TKS("s_lshr_b32 s91, s62, 3\n\ts_and_b32 s91, s91, 3\n\t")
      "s_waitcnt lgkmcnt(0)\n\ts_barrier\n\t"
      STS("s_memrealtime vcc\n\ts_waitcnt lgkmcnt(0)\n\tv_mov_b32 v31, vcc_lo\n\tv_mov_b32 v30, 161024\n\tds_write_b32 v30, v31\n\t")
      "1:\n\tbuffer_load_dword v29, off, s[12:15], s63 sc1\n\ts_waitcnt vmcnt(0)\n\tv_readfirstlane_b32 s49, v29\n\ts_bitcmp1_b32 s49, 0\n\ts_cbranch_scc1 2f\n\t"
      "s_cmp_eq_u32 s18, 0\n\ts_cbranch_scc1 2f\n\ts_sub_u32 s18, s18, 1\n\ts_sleep 2\n\ts_branch 1b\n\t"
      "2:\n\ts_and_b32 s48, s49, 3\n\ts_cmp_lg_u32 s48, 3\n\ts_cbranch_scc1 4f\n\ts_cmp_eq_u32 s62, 0\n\ts_cbranch_scc1 4f\n\t"
      "s_waitcnt lgkmcnt(0)\n\ts_barrier\n\ts_mov_b64 s[0:1], s[60:61]\n\ts_mov_b32 s2, s62\n\ts_lshl_b32 s48, s16, 6\n\tv_mbcnt_lo_u32_b32 v0, -1, 0\n\t"
      "v_mbcnt_hi_u32_b32 v0, -1, v0\n\tv_add_u32 v0, s48, v0\n\ts_getpc_b64 s[4:5]\n\t"
      "3:\n\ts_sub_u32 s4, s4, 3b-mk_mega\n\ts_subb_u32 s5, s5, 0\n\ts_setpc_b64 s[4:5]\n\t"
      "4:\n\ts_lshr_b32 s64, s64, 9\n\ts_lshr_b32 s65, s65, 9\n\ts_lshr_b32 s66, s66, 9\n\ts_lshr_b32 s67, s67, 9\n\ts_lshl_b32 s48, s49, 10\n\ts_and_b32 s48, s48, 0x2000\n\t"
      "s_mov_b64 s[72:73], s[24:25]\n\tv_or_b32 v0, s48, v0\n\tv_or_b32 v11, s48, v11\n\ts_lshl_b32 s48, s49, 11\n\ts_and_b32 s48, s48, 0x2000\n\tv_or_b32 v9, s48, v9\n\t"
      "s_mov_b32 s31, s19\n\ts_memtime vcc\n\ts_memrealtime s[32:33]\n\ts_nop 1\n\ts_memrealtime s[34:35]\n\ts_nop 1\n\ts_memrealtime s[36:37]\n\ts_nop 1\n\t"
      "s_memrealtime s[38:39]\n\ts_nop 1\n\ts_memrealtime s[46:47]\n\ts_nop 1\n\ts_memrealtime s[54:55]\n\ts_nop 1\n\ts_memrealtime s[56:57]\n\ts_nop 1\n\t"
      "s_memrealtime s[58:59]\n\ts_nop 1\n\ts_waitcnt lgkmcnt(0)\n\ts_add_u32 s34, s34, s32\n\ts_add_u32 s34, s34, s36\n\ts_add_u32 s34, s34, s38\n\ts_add_u32 s34, s34, s46\n\t"
      "s_add_u32 s34, s34, s54\n\ts_add_u32 s34, s34, s56\n\ts_add_u32 s34, s34, s58\n\ts_lshl_b32 s36, s32, 3\n\ts_sub_u32 s34, s34, s36\n\ts_lshr_b32 s34, s34, 1\n\t"
      "s_lshl2_add_u32 s34, s32, s34\n\ts_add_u32 s34, s34, s31\n\ts_sub_u32 s36, s34, s20\n\ts_sub_u32 s37, vcc_lo, s22\n\ts_mov_b32 s20, s34\n\ts_mov_b32 s22, vcc_lo\n\t"
      "v_cvt_f32_u32 v13, s36\n\tv_cvt_f32_u32 v14, s37\n\tv_rcp_f32 v13, v13\n\tv_mul_f32 v14, 0x45800000, v14\n\tv_mul_f32 v13, v13, v14\n\tv_cvt_u32_f32 v13, v13\n\t"
      "s_cmp_lt_u32 s36, 300\n\tv_readfirstlane_b32 s34, v13\n\ts_cselect_b32 s34, 0, s34\n\ts_sub_u32 s36, s34, 10240\n\ts_cmp_gt_u32 s36, 16400\n\t"
      "s_cselect_b32 s34, s26, s34\n\ts_add_u32 s26, s26, s34\n\ts_lshr_b32 s26, s26, 1\n\ts_mov_b32 s26, 23040\n\ts_sub_u32 s19, s20, 0x100000\n\ts_mov_b32 s62, 440\n\t"
      "s_mov_b32 s63, 0\n\t"
      "10:\n\tds_read_b128 v[160:163], v5 offset:16384\n\tds_read_b128 v[164:167], v5 offset:17408\n\tds_read_b128 v[168:171], v5 offset:18432\n\t"
      "ds_read_b128 v[172:175], v5 offset:19456\n\tds_read_b128 v[176:179], v5 offset:20480\n\tds_read_b128 v[180:183], v5 offset:21504\n\t"
      "ds_read_b128 v[184:187], v5 offset:22528\n\tds_read_b128 v[188:191], v5 offset:23552\n\tds_read_b128 v[192:195], v5 offset:24576\n\t"
      "ds_read_b128 v[196:199], v5 offset:25600\n\tds_read_b128 v[200:203], v5 offset:26624\n\tds_read_b128 v[204:207], v5 offset:27648\n\t"
      "ds_read_b128 v[208:211], v5 offset:28672\n\tds_read_b128 v[212:215], v5 offset:29696\n\tds_read_b128 v[216:219], v5 offset:30720\n\t"
      "ds_read_b128 v[220:223], v5 offset:31744\n\tv_mov_b64 v[16:17], 0\n\tv_mov_b64 v[18:19], 0\n\tv_mov_b64 v[20:21], 0\n\tv_mov_b64 v[22:23], 0\n\t"
      "12:\n\ts_and_b32 s48, s17, 3\n\ts_lshl_b32 s49, s48, 18\n\ts_add_u32 s49, s49, 0x200000\n\ts_lshl_b32 s41, s48, 18\n\ts_add_u32 s41, s41, 0x208000\n\t"
      "s_xor_b32 s48, s48, 2\n\ts_lshl_b32 s42, s48, 18\n\ts_add_u32 s42, s42, 0x208000\n\ts_add_u32 s68, s49, s64\n\ts_add_u32 s69, s49, s65\n\ts_add_u32 s70, s49, s66\n\t"
      "s_add_u32 s71, s49, s67\n\ts_and_b32 s43, s17, 1\n\ts_lshl_b32 s43, s43, 20\n\ts_add_u32 s44, s43, s27\n\ts_waitcnt lgkmcnt(0)\n\ts_cmp_lg_u32 s16, 1\n\t"
      "s_cbranch_scc1 73f\n\t"
      "78:\n\ts_memrealtime vcc\n\ts_waitcnt lgkmcnt(0)\n\ts_lshl2_add_u32 vcc_lo, vcc_lo, 110\n\ts_sub_u32 s48, s19, vcc_lo\n\ts_cmp_lt_u32 s48, 4000\n\ts_cbranch_scc1 78b\n\t"
      "s_memtime vcc\n\ts_memrealtime s[32:33]\n\ts_nop 1\n\ts_memrealtime s[34:35]\n\ts_nop 1\n\ts_memrealtime s[36:37]\n\ts_nop 1\n\ts_memrealtime s[38:39]\n\ts_nop 1\n\t"
      "s_memrealtime s[46:47]\n\ts_nop 1\n\ts_memrealtime s[54:55]\n\ts_nop 1\n\ts_memrealtime s[56:57]\n\ts_nop 1\n\ts_memrealtime s[58:59]\n\ts_nop 1\n\t"
      "s_waitcnt lgkmcnt(0)\n\ts_add_u32 s34, s34, s32\n\ts_add_u32 s34, s34, s36\n\ts_add_u32 s34, s34, s38\n\ts_add_u32 s34, s34, s46\n\ts_add_u32 s34, s34, s54\n\t"
      "s_add_u32 s34, s34, s56\n\ts_add_u32 s34, s34, s58\n\ts_lshl_b32 s36, s32, 3\n\ts_sub_u32 s34, s34, s36\n\ts_lshr_b32 s34, s34, 1\n\ts_lshl2_add_u32 s34, s32, s34\n\t"
      "s_add_u32 s34, s34, s31\n\ts_sub_u32 s36, s34, s20\n\ts_sub_u32 s37, vcc_lo, s22\n\ts_mov_b32 s20, s34\n\ts_mov_b32 s22, vcc_lo\n\tv_cvt_f32_u32 v13, s36\n\t"
      "v_cvt_f32_u32 v14, s37\n\tv_rcp_f32 v13, v13\n\tv_mul_f32 v14, 0x45800000, v14\n\tv_mul_f32 v13, v13, v14\n\tv_cvt_u32_f32 v13, v13\n\ts_cmp_lt_u32 s36, 300\n\t"
      "v_readfirstlane_b32 s34, v13\n\ts_cselect_b32 s34, 0, s34\n\ts_sub_u32 s36, s34, 10240\n\ts_cmp_gt_u32 s36, 16400\n\ts_cselect_b32 s34, s26, s34\n\t"
      "s_add_u32 s26, s26, s34\n\ts_lshr_b32 s26, s26, 1\n\ts_sub_u32 s48, s19, s20\n\ts_cmp_gt_u32 s48, 4000\n\ts_cselect_b32 s48, 0, s48\n\ts_mul_i32 s48, s48, s26\n\t"
      "s_lshr_b32 s48, s48, 12\n\ts_add_u32 s32, s48, s22\n\t"
      "71:\n\ts_memtime vcc\n\ts_waitcnt lgkmcnt(0)\n\t"
      "70:\n\ts_sub_u32 s48, s32, vcc_lo\n\ts_cmp_gt_i32 s48, 160\n\ts_cbranch_scc1 71b\n\ts_max_i32 s48, s48, 0\n\ts_and_b32 s48, s48, -4\n\ts_getpc_b64 vcc\n\t"
      "72:\n\ts_sub_u32 s48, 73f-72b, s48\n\ts_add_u32 vcc_lo, vcc_lo, s48\n\ts_addc_u32 vcc_hi, vcc_hi, 0\n\ts_setpc_b64 vcc\n\t.rept 40\n\ts_nop 0\n\t.endr\n\t"
      "73:\n\ts_barrier\n\t"
      TK0
      "buffer_load_dwordx4 v[240:243], v0, s[8:11], s68 offen sc1\n\ts_nop 11\n\tbuffer_load_dwordx4 v[244:247], v0, s[8:11], s69 offen sc1\n\ts_nop 11\n\t"
      "buffer_load_dwordx4 v[248:251], v0, s[8:11], s70 offen sc1\n\ts_nop 11\n\tbuffer_load_dwordx4 v[252:255], v0, s[8:11], s71 offen sc1\n\t"
      "11:\n\ts_waitcnt vmcnt(3)\n\t"
      TK1
      "ds_write_b128 v1, v[240:243] offset:0\n\tv_max3_u32 v13, v240, v241, v242\n\tv_max_u32 v13, v13, v243\n\tv_cmp_eq_u32 vcc, -1, v13\n\t"
      "v_smfmac_f32_16x16x64_bf16 v[16:19], v[240:243], a[0:7], v6\n\tv_smfmac_f32_16x16x64_bf16 v[20:23], v[240:243], a[8:15], v6\n\ts_cbranch_vccnz 86f\n\t"
      "20:\n\ts_waitcnt lgkmcnt(0)\n\ts_barrier\n\tds_read_b128 v[24:27], v2 offset:0\n\tds_read_b128 v[28:31], v3 offset:0\n\tds_read_b128 v[224:227], v4 offset:0\n\t"
      "s_waitcnt vmcnt(2)\n\tv_smfmac_f32_16x16x64_bf16 v[16:19], v[244:247], a[16:23], v6\n\tds_write_b128 v1, v[244:247] offset:1024\n\tv_max3_u32 v13, v244, v245, v246\n\t"
      "v_smfmac_f32_16x16x64_bf16 v[20:23], v[244:247], v[160:167], v6\n\ts_waitcnt vmcnt(1)\n\tv_max_u32 v13, v13, v247\n\t"
      "v_smfmac_f32_16x16x64_bf16 v[16:19], v[248:251], a[24:31], v6\n\tv_cmp_eq_u32 vcc, -1, v13\n\tv_smfmac_f32_16x16x64_bf16 v[20:23], v[248:251], a[32:39], v6\n\t"
      "s_waitcnt lgkmcnt(3)\n\tv_max3_u32 v13, v248, v249, v250\n\tv_smfmac_f32_16x16x64_bf16 v[16:19], v[24:27], a[48:55], v6\n\ts_cbranch_vccnz 87f\n\t"
      "21:\n\tv_max_u32 v13, v13, v251\n\tv_smfmac_f32_16x16x64_bf16 v[20:23], v[24:27], a[56:63], v6\n\ts_waitcnt lgkmcnt(2)\n\ts_waitcnt lgkmcnt(0)\n\ts_barrier\n\t"
      "s_waitcnt vmcnt(0)\n\tv_smfmac_f32_16x16x64_bf16 v[16:19], v[252:255], a[40:47], v6\n\tds_read_b128 v[228:231], v2 offset:1024\n\t"
      "ds_read_b128 v[232:235], v3 offset:1024\n\tv_max3_u32 v13, v13, v252, v253\n\tv_smfmac_f32_16x16x64_bf16 v[20:23], v[252:255], v[168:175], v6\n\t"
      "ds_read_b128 v[236:239], v4 offset:1024\n\tv_max3_u32 v13, v13, v254, v255\n\tv_smfmac_f32_16x16x64_bf16 v[16:19], v[28:31], a[64:71], v6\n\t"
      "ds_write_b128 v1, v[248:251] offset:2048\n\tv_smfmac_f32_16x16x64_bf16 v[20:23], v[28:31], v[176:183], v6\n\tv_cmp_eq_u32 vcc, -1, v13\n\t"
      TK2
      "v_smfmac_f32_16x16x64_bf16 v[16:19], v[224:227], a[72:79], v6\n\tv_smfmac_f32_16x16x64_bf16 v[20:23], v[224:227], a[80:87], v6\n\ts_waitcnt lgkmcnt(3)\n\t"
      "ds_write_b128 v1, v[252:255] offset:3072\n\tv_smfmac_f32_16x16x64_bf16 v[16:19], v[228:231], a[88:95], v6\n\ts_cbranch_vccnz 88f\n\t"
      "22:\n\tv_smfmac_f32_16x16x64_bf16 v[20:23], v[228:231], v[184:191], v6\n\ts_waitcnt lgkmcnt(3)\n\tv_smfmac_f32_16x16x64_bf16 v[16:19], v[232:235], a[96:103], v6\n\t"
      "v_smfmac_f32_16x16x64_bf16 v[20:23], v[232:235], a[104:111], v6\n\ts_waitcnt lgkmcnt(2)\n\ts_waitcnt lgkmcnt(0)\n\ts_barrier\n\t"
      "ds_read_b128 v[240:243], v2 offset:2048\n\t"
      TK3
      "v_smfmac_f32_16x16x64_bf16 v[16:19], v[236:239], a[112:119], v6\n\tds_read_b128 v[244:247], v3 offset:2048\n\tds_read_b128 v[248:251], v4 offset:2048\n\t"
      "v_smfmac_f32_16x16x64_bf16 v[20:23], v[236:239], v[192:199], v6\n\tds_read_b128 v[24:27], v2 offset:3072\n\ts_waitcnt lgkmcnt(3)\n\t"
      "v_smfmac_f32_16x16x64_bf16 v[16:19], v[240:243], a[120:127], v6\n\tds_read_b128 v[28:31], v3 offset:3072\n\tds_read_b128 v[224:227], v4 offset:3072\n\t"
      "v_smfmac_f32_16x16x64_bf16 v[20:23], v[240:243], a[128:135], v6\n\ts_waitcnt lgkmcnt(4)\n\tv_smfmac_f32_16x16x64_bf16 v[16:19], v[244:247], a[136:143], v6\n\t"
      "v_smfmac_f32_16x16x64_bf16 v[20:23], v[244:247], v[200:207], v6\n\ts_waitcnt lgkmcnt(3)\n\tv_smfmac_f32_16x16x64_bf16 v[16:19], v[248:251], a[144:151], v6\n\t"
      "ds_read_b128 v[160:163], v5 offset:0\n\tv_smfmac_f32_16x16x64_bf16 v[20:23], v[248:251], a[152:159], v6\n\ts_waitcnt lgkmcnt(3)\n\t"
      "v_smfmac_f32_16x16x64_bf16 v[16:19], v[24:27], a[160:167], v6\n\tds_read_b128 v[164:167], v5 offset:1024\n\t"
      "v_smfmac_f32_16x16x64_bf16 v[20:23], v[24:27], v[208:215], v6\n\ts_waitcnt lgkmcnt(3)\n\tv_smfmac_f32_16x16x64_bf16 v[16:19], v[28:31], a[168:175], v6\n\t"
      "ds_read_b128 v[168:171], v5 offset:2048\n\tv_smfmac_f32_16x16x64_bf16 v[20:23], v[28:31], a[176:183], v6\n\ts_waitcnt lgkmcnt(3)\n\t"
      "v_smfmac_f32_16x16x64_bf16 v[16:19], v[224:227], a[184:191], v6\n\tds_read_b128 v[172:175], v5 offset:3072\n\t"
      "v_smfmac_f32_16x16x64_bf16 v[20:23], v[224:227], v[216:223], v6\n\t"
      TK4
      "v_mov_b64 v[228:229], 0\n\tv_mov_b64 v[230:231], 0\n\tv_mov_b64 v[232:233], 0\n\tv_mov_b64 v[234:235], 0\n\tv_mov_b64 v[236:237], 0\n\tv_mov_b64 v[238:239], 0\n\t"
      "v_add_f32 v16, v16, v17\n\tv_add_f32 v18, v18, v19\n\tv_add_f32 v20, v20, v21\n\tv_add_f32 v22, v22, v23\n\tv_cvt_pk_bf16_f32 v16, v16, v20\n\t"
      "v_cvt_pk_bf16_f32 v18, v18, v22\n\tds_write2_b32 v7, v16, v18 offset1:68\n\tv_mov_b64 v[224:225], 0\n\tv_mov_b64 v[226:227], 0\n\tv_mov_b64 v[24:25], 0\n\t"
      "v_mov_b64 v[26:27], 0\n\tv_mov_b64 v[28:29], 0\n\tv_mov_b64 v[30:31], 0\n\ts_waitcnt lgkmcnt(0)\n\ts_barrier\n\t"
      TK5
      "ds_read_b128 v[240:243], v8 offset:0\n\tds_read_b128 v[244:247], v8 offset:64\n\tds_read_b128 v[248:251], v8 offset:128\n\tds_read_b128 v[252:255], v8 offset:192\n\t"
      "v_mov_b64 v[16:17], 0\n\tv_mov_b64 v[18:19], 0\n\tv_mov_b64 v[20:21], 0\n\tv_mov_b64 v[22:23], 0\n\ts_waitcnt lgkmcnt(3)\n\t"
      "v_smfmac_f32_16x16x64_bf16 v[224:227], v[240:243], v[32:39], v12\n\tds_read_b128 v[176:179], v5 offset:4096\n\t"
      "v_smfmac_f32_16x16x64_bf16 v[228:231], v[240:243], v[64:71], v12\n\ts_waitcnt lgkmcnt(3)\n\tds_read_b128 v[180:183], v5 offset:5120\n\t"
      "v_smfmac_f32_16x16x64_bf16 v[224:227], v[244:247], v[40:47], v12\n\tds_read_b128 v[184:187], v5 offset:6144\n\t"
      "v_smfmac_f32_16x16x64_bf16 v[228:231], v[244:247], v[72:79], v12\n\ts_waitcnt lgkmcnt(4)\n\tds_read_b128 v[188:191], v5 offset:7168\n\t"
      "v_smfmac_f32_16x16x64_bf16 v[224:227], v[248:251], v[48:55], v12\n\tds_read_b128 v[192:195], v5 offset:8192\n\t"
      "v_smfmac_f32_16x16x64_bf16 v[228:231], v[248:251], v[80:87], v12\n\ts_waitcnt lgkmcnt(5)\n\tds_read_b128 v[196:199], v5 offset:9216\n\t"
      "v_smfmac_f32_16x16x64_bf16 v[224:227], v[252:255], v[56:63], v12\n\tds_read_b128 v[200:203], v5 offset:10240\n\t"
      "v_smfmac_f32_16x16x64_bf16 v[228:231], v[252:255], v[88:95], v12\n\ts_nop 1\n\tv_smfmac_f32_16x16x64_bf16 v[232:235], v[240:243], v[96:103], v12\n\ts_nop 2\n\t"
      "v_smfmac_f32_16x16x64_bf16 v[236:239], v[240:243], v[128:135], v12\n\tds_read_b128 v[204:207], v5 offset:11264\n\tv_cvt_pk_bf16_f32 v224, v224, v225\n\t"
      "v_smfmac_f32_16x16x64_bf16 v[232:235], v[244:247], v[104:111], v12\n\tds_read_b128 v[208:211], v5 offset:12288\n\tv_cvt_pk_bf16_f32 v225, v226, v227\n\t"
      "v_smfmac_f32_16x16x64_bf16 v[236:239], v[244:247], v[136:143], v12\n\tbuffer_store_dwordx2 v[224:225], v9, s[8:11], s44 offen sc1\n\t"
      "v_smfmac_f32_16x16x64_bf16 v[232:235], v[248:251], v[112:119], v12\n\tds_read_b128 v[212:215], v5 offset:13312\n\ts_add_u32 s44, s44, 0x8000\n\t"
      "v_cvt_pk_bf16_f32 v228, v228, v229\n\tv_smfmac_f32_16x16x64_bf16 v[236:239], v[248:251], v[144:151], v12\n\tds_read_b128 v[216:219], v5 offset:14336\n\t"
      "v_cvt_pk_bf16_f32 v229, v230, v231\n\tv_smfmac_f32_16x16x64_bf16 v[232:235], v[252:255], v[120:127], v12\n\t"
      "buffer_store_dwordx2 v[228:229], v9, s[8:11], s44 offen sc1\n\tv_smfmac_f32_16x16x64_bf16 v[236:239], v[252:255], v[152:159], v12\n\ts_nop 1\n\t"
      "s_add_u32 s44, s43, s28\n\tv_smfmac_f32_16x16x64_bf16 v[24:27], v[240:243], a[192:199], v12\n\ts_nop 2\n\t"
      "v_smfmac_f32_16x16x64_bf16 v[28:31], v[240:243], a[224:231], v12\n\tds_read_b128 v[220:223], v5 offset:15360\n\tv_cvt_pk_bf16_f32 v232, v232, v233\n\t"
      "v_smfmac_f32_16x16x64_bf16 v[24:27], v[244:247], a[200:207], v12\n\tv_cvt_pk_bf16_f32 v233, v234, v235\n\t"
      "v_smfmac_f32_16x16x64_bf16 v[28:31], v[244:247], a[232:239], v12\n\tbuffer_store_dwordx2 v[232:233], v9, s[8:11], s44 offen sc1\n\t"
      "v_smfmac_f32_16x16x64_bf16 v[24:27], v[248:251], a[208:215], v12\n\ts_add_u32 s44, s44, 0x8000\n\tv_cvt_pk_bf16_f32 v236, v236, v237\n\t"
      "v_smfmac_f32_16x16x64_bf16 v[28:31], v[248:251], a[240:247], v12\n\tv_cvt_pk_bf16_f32 v237, v238, v239\n\t"
      "v_smfmac_f32_16x16x64_bf16 v[24:27], v[252:255], a[216:223], v12\n\tbuffer_store_dwordx2 v[236:237], v9, s[8:11], s44 offen sc1\n\t"
      "v_smfmac_f32_16x16x64_bf16 v[28:31], v[252:255], a[248:255], v12\n\ts_waitcnt lgkmcnt(0)\n\ts_nop 0\n\ts_add_u32 s44, s43, s29\n\t"
      "v_smfmac_f32_16x16x64_bf16 v[16:19], v[240:243], v[160:167], v12\n\ts_nop 2\n\tv_smfmac_f32_16x16x64_bf16 v[20:23], v[240:243], v[192:199], v12\n\t"
      "v_cvt_pk_bf16_f32 v24, v24, v25\n\tv_smfmac_f32_16x16x64_bf16 v[16:19], v[244:247], v[168:175], v12\n\tv_cvt_pk_bf16_f32 v25, v26, v27\n\t"
      "v_smfmac_f32_16x16x64_bf16 v[20:23], v[244:247], v[200:207], v12\n\tbuffer_store_dwordx2 v[24:25], v9, s[8:11], s44 offen sc1\n\t"
      "v_smfmac_f32_16x16x64_bf16 v[16:19], v[248:251], v[176:183], v12\n\ts_add_u32 s44, s44, 0x8000\n\tv_cvt_pk_bf16_f32 v28, v28, v29\n\t"
      "v_smfmac_f32_16x16x64_bf16 v[20:23], v[248:251], v[208:215], v12\n\tv_cvt_pk_bf16_f32 v29, v30, v31\n\t"
      "v_smfmac_f32_16x16x64_bf16 v[16:19], v[252:255], v[184:191], v12\n\tbuffer_store_dwordx2 v[28:29], v9, s[8:11], s44 offen sc1\n\t"
      "v_smfmac_f32_16x16x64_bf16 v[20:23], v[252:255], v[216:223], v12\n\ts_add_u32 s44, s43, s30\n\ts_nop 4\n\tv_cvt_pk_bf16_f32 v16, v16, v17\n\t"
      "v_cvt_pk_bf16_f32 v17, v18, v19\n\tv_cvt_pk_bf16_f32 v20, v20, v21\n\tv_cvt_pk_bf16_f32 v21, v22, v23\n\tbuffer_store_dwordx2 v[16:17], v9, s[8:11], s44 offen sc1\n\t"
      "s_add_u32 s44, s44, 0x8000\n\tbuffer_store_dwordx2 v[20:21], v9, s[8:11], s44 offen sc1\n\t"
      TK6
      "s_cmp_lg_u32 s16, 1\n\ts_cbranch_scc1 69f\n\t"
      STS("s_memrealtime s[46:47]\n\t")
      "buffer_load_dword v14, v11, s[8:11], s41 offen sc1\n\tds_read_b128 v[160:163], v5 offset:16384\n\tds_read_b128 v[164:167], v5 offset:17408\n\t"
      "ds_read_b128 v[168:171], v5 offset:18432\n\tds_read_b128 v[172:175], v5 offset:19456\n\tds_read_b128 v[176:179], v5 offset:20480\n\t"
      "ds_read_b128 v[180:183], v5 offset:21504\n\tds_read_b128 v[184:187], v5 offset:22528\n\tds_read_b128 v[188:191], v5 offset:23552\n\t"
      "ds_read_b128 v[192:195], v5 offset:24576\n\tds_read_b128 v[196:199], v5 offset:25600\n\tds_read_b128 v[200:203], v5 offset:26624\n\t"
      "ds_read_b128 v[204:207], v5 offset:27648\n\tds_read_b128 v[208:211], v5 offset:28672\n\tds_read_b128 v[212:215], v5 offset:29696\n\t"
      "ds_read_b128 v[216:219], v5 offset:30720\n\tds_read_b128 v[220:223], v5 offset:31744\n\tv_mov_b64 v[16:17], 0\n\tv_mov_b64 v[18:19], 0\n\tv_mov_b64 v[20:21], 0\n\t"
      "v_mov_b64 v[22:23], 0\n\ts_waitcnt vmcnt(0)\n\ts_branch 76f\n\t"
      "74:\n\tbuffer_load_dword v14, v11, s[8:11], s41 offen sc1\n\ts_waitcnt vmcnt(0)\n\t"
      "76:\n\tv_max_u32_dpp v14, v14, v14 row_ror:8 row_mask:0xf bank_mask:0xf\n\ts_nop 1\n\tv_max_u32_dpp v14, v14, v14 row_ror:4 row_mask:0xf bank_mask:0xf\n\ts_nop 1\n\t"
      "v_max_u32_dpp v14, v14, v14 row_ror:2 row_mask:0xf bank_mask:0xf\n\ts_nop 1\n\tv_max_u32_dpp v14, v14, v14 row_ror:1 row_mask:0xf bank_mask:0xf\n\ts_nop 0\n\t"
      "v_readlane_b32 s52, v14, 0\n\tv_readlane_b32 s48, v14, 16\n\ts_max_u32 s52, s52, s48\n\ts_cmp_lg_u32 s52, -1\n\ts_cbranch_scc1 75f\n\ts_cmp_eq_u32 s18, 0\n\t"
      "s_cbranch_scc1 75f\n\ts_sub_u32 s18, s18, 1\n\ts_branch 74b\n\t"
      "75:\n\ts_sub_u32 s48, s52, s19\n\ts_sub_u32 s48, s48, s62\n\ts_cmp_gt_i32 s48, s72\n\ts_cselect_b32 s19, s73, 0\n\ts_cmp_gt_i32 s48, 100\n\t"
      "s_cselect_b32 s19, 40, s19\n\ts_cmp_gt_i32 s48, 40\n\ts_cselect_b32 s48, 24, s48\n\ts_ashr_i32 s48, s48, 2\n\ts_add_i32 s62, s62, s48\n\ts_min_u32 s62, s62, 1000\n\t"
      "s_mul_i32 s63, s62, 3\n\ts_lshr_b32 s63, s63, 3\n\ts_add_u32 s19, s19, s52\n\ts_add_u32 s19, s19, s23\n\ts_add_u32 s19, s19, s63\n\t"
      STS("s_waitcnt lgkmcnt(0)\n\ts_lshl2_add_u32 s46, s46, s31\n\ts_sub_u32 s46, s52, s46\n\ts_add_u32 s61, s61, s46\n\ts_min_i32 s21, s21, s46\n\ts_cmp_lt_u32 s17, 8\n\ts_cselect_b32 s21, 1000, s21\n\t")
      "s_branch 69f\n\t"
      "86:\n\t"
      STS("s_add_u32 s50, s50, 1\n\t")
      "s_waitcnt lgkmcnt(0)\n\tbuffer_load_dwordx4 v[240:243], v0, s[8:11], s68 offen sc1\n\tbuffer_load_dwordx4 v[244:247], v0, s[8:11], s69 offen sc1\n\t"
      "buffer_load_dwordx4 v[248:251], v0, s[8:11], s70 offen sc1\n\tbuffer_load_dwordx4 v[252:255], v0, s[8:11], s71 offen sc1\n\ts_nop 3\n\tv_mov_b64 v[16:17], 0\n\t"
      "v_mov_b64 v[18:19], 0\n\tv_mov_b64 v[20:21], 0\n\tv_mov_b64 v[22:23], 0\n\ts_nop 1\n\ts_branch 84f\n\t"
      "83:\n\tbuffer_load_dwordx4 v[240:243], v0, s[8:11], s68 offen sc1\n\tbuffer_load_dwordx4 v[244:247], v0, s[8:11], s69 offen sc1\n\t"
      "buffer_load_dwordx4 v[248:251], v0, s[8:11], s70 offen sc1\n\tbuffer_load_dwordx4 v[252:255], v0, s[8:11], s71 offen sc1\n\t"
      "84:\n\ts_waitcnt vmcnt(0)\n\tv_max3_u32 v13, v240, v241, v242\n\tv_max_u32 v13, v13, v243\n\tv_max3_u32 v13, v13, v244, v245\n\tv_max3_u32 v13, v13, v246, v247\n\t"
      "v_max3_u32 v13, v13, v248, v249\n\tv_max3_u32 v13, v13, v250, v251\n\tv_max3_u32 v13, v13, v252, v253\n\tv_max3_u32 v13, v13, v254, v255\n\tv_cmp_eq_u32 vcc, -1, v13\n\t"
      "s_cbranch_vccz 85f\n\ts_cmp_eq_u32 s18, 0\n\ts_cbranch_scc1 85f\n\ts_sub_u32 s18, s18, 1\n\ts_branch 83b\n\t"
      "85:\n\tv_smfmac_f32_16x16x64_bf16 v[16:19], v[240:243], a[0:7], v6\n\tv_smfmac_f32_16x16x64_bf16 v[20:23], v[240:243], a[8:15], v6\n\t"
      "ds_write_b128 v1, v[240:243] offset:0\n\tds_write_b128 v1, v[244:247] offset:1024\n\tds_write_b128 v1, v[248:251] offset:2048\n\t"
      "ds_write_b128 v1, v[252:255] offset:3072\n\tv_mov_b32 v13, 0\n\ts_mov_b64 vcc, 0\n\ts_waitcnt lgkmcnt(0)\n\ts_branch 20b\n\t"
      "87:\n\t"
      STS("s_add_u32 s50, s50, 1\n\t")
      "s_waitcnt lgkmcnt(0)\n\tbuffer_load_dwordx4 v[244:247], v0, s[8:11], s69 offen sc1\n\tbuffer_load_dwordx4 v[248:251], v0, s[8:11], s70 offen sc1\n\t"
      "buffer_load_dwordx4 v[252:255], v0, s[8:11], s71 offen sc1\n\ts_nop 3\n\tv_mov_b64 v[16:17], 0\n\tv_mov_b64 v[18:19], 0\n\tv_mov_b64 v[20:21], 0\n\t"
      "v_mov_b64 v[22:23], 0\n\ts_nop 1\n\tv_smfmac_f32_16x16x64_bf16 v[16:19], v[240:243], a[0:7], v6\n\tv_smfmac_f32_16x16x64_bf16 v[20:23], v[240:243], a[8:15], v6\n\t"
      "s_branch 84f\n\t"
      "83:\n\tbuffer_load_dwordx4 v[244:247], v0, s[8:11], s69 offen sc1\n\tbuffer_load_dwordx4 v[248:251], v0, s[8:11], s70 offen sc1\n\t"
      "buffer_load_dwordx4 v[252:255], v0, s[8:11], s71 offen sc1\n\t"
      "84:\n\ts_waitcnt vmcnt(0)\n\tv_max3_u32 v13, v244, v245, v246\n\tv_max_u32 v13, v13, v247\n\tv_max3_u32 v13, v13, v248, v249\n\tv_max3_u32 v13, v13, v250, v251\n\t"
      "v_max3_u32 v13, v13, v252, v253\n\tv_max3_u32 v13, v13, v254, v255\n\tv_cmp_eq_u32 vcc, -1, v13\n\ts_cbranch_vccz 85f\n\ts_cmp_eq_u32 s18, 0\n\ts_cbranch_scc1 85f\n\t"
      "s_sub_u32 s18, s18, 1\n\ts_branch 83b\n\t"
      "85:\n\tv_smfmac_f32_16x16x64_bf16 v[16:19], v[244:247], a[16:23], v6\n\tv_smfmac_f32_16x16x64_bf16 v[20:23], v[244:247], v[160:167], v6\n\t"
      "v_smfmac_f32_16x16x64_bf16 v[16:19], v[248:251], a[24:31], v6\n\tv_smfmac_f32_16x16x64_bf16 v[20:23], v[248:251], a[32:39], v6\n\t"
      "v_smfmac_f32_16x16x64_bf16 v[16:19], v[24:27], a[48:55], v6\n\tds_write_b128 v1, v[244:247] offset:1024\n\tds_write_b128 v1, v[248:251] offset:2048\n\t"
      "ds_write_b128 v1, v[252:255] offset:3072\n\tv_mov_b32 v13, 0\n\ts_mov_b64 vcc, 0\n\ts_waitcnt lgkmcnt(0)\n\ts_branch 21b\n\t"
      "88:\n\t"
      STS("s_add_u32 s50, s50, 1\n\t")
      "s_waitcnt lgkmcnt(0)\n\tbuffer_load_dwordx4 v[248:251], v0, s[8:11], s70 offen sc1\n\tbuffer_load_dwordx4 v[252:255], v0, s[8:11], s71 offen sc1\n\ts_nop 3\n\t"
      "v_mov_b64 v[16:17], 0\n\tv_mov_b64 v[18:19], 0\n\tv_mov_b64 v[20:21], 0\n\tv_mov_b64 v[22:23], 0\n\ts_nop 1\n\t"
      "v_smfmac_f32_16x16x64_bf16 v[16:19], v[240:243], a[0:7], v6\n\tv_smfmac_f32_16x16x64_bf16 v[20:23], v[240:243], a[8:15], v6\n\t"
      "v_smfmac_f32_16x16x64_bf16 v[16:19], v[244:247], a[16:23], v6\n\tv_smfmac_f32_16x16x64_bf16 v[20:23], v[244:247], v[160:167], v6\n\ts_branch 84f\n\t"
      "83:\n\tbuffer_load_dwordx4 v[248:251], v0, s[8:11], s70 offen sc1\n\tbuffer_load_dwordx4 v[252:255], v0, s[8:11], s71 offen sc1\n\t"
      "84:\n\ts_waitcnt vmcnt(0)\n\tv_max3_u32 v13, v248, v249, v250\n\tv_max_u32 v13, v13, v251\n\tv_max3_u32 v13, v13, v252, v253\n\tv_max3_u32 v13, v13, v254, v255\n\t"
      "v_cmp_eq_u32 vcc, -1, v13\n\ts_cbranch_vccz 85f\n\ts_cmp_eq_u32 s18, 0\n\ts_cbranch_scc1 85f\n\ts_sub_u32 s18, s18, 1\n\ts_branch 83b\n\t"
      "85:\n\tv_smfmac_f32_16x16x64_bf16 v[16:19], v[248:251], a[24:31], v6\n\tv_smfmac_f32_16x16x64_bf16 v[20:23], v[248:251], a[32:39], v6\n\t"
      "v_smfmac_f32_16x16x64_bf16 v[16:19], v[24:27], a[48:55], v6\n\tv_smfmac_f32_16x16x64_bf16 v[20:23], v[24:27], a[56:63], v6\n\t"
      "v_smfmac_f32_16x16x64_bf16 v[16:19], v[252:255], a[40:47], v6\n\tv_smfmac_f32_16x16x64_bf16 v[20:23], v[252:255], v[168:175], v6\n\t"
      "v_smfmac_f32_16x16x64_bf16 v[16:19], v[28:31], a[64:71], v6\n\tv_smfmac_f32_16x16x64_bf16 v[20:23], v[28:31], v[176:183], v6\n\t"
      "v_smfmac_f32_16x16x64_bf16 v[16:19], v[224:227], a[72:79], v6\n\tv_smfmac_f32_16x16x64_bf16 v[20:23], v[224:227], a[80:87], v6\n\t"
      "v_smfmac_f32_16x16x64_bf16 v[16:19], v[228:231], a[88:95], v6\n\tds_write_b128 v1, v[248:251] offset:2048\n\tds_write_b128 v1, v[252:255] offset:3072\n\t"
      "v_mov_b32 v13, 0\n\ts_mov_b64 vcc, 0\n\ts_waitcnt lgkmcnt(0)\n\ts_branch 22b\n\t"
      "69:\n\t"
      TK7
      STS("s_cmp_lt_i32 s17, 8\n\ts_cselect_b32 s50, 0, s50\n\ts_cselect_b32 s51, 0, s51\n\ts_cselect_b32 s61, 0, s61\n\t")
      TKS("s_waitcnt lgkmcnt(0)\n\ts_cmp_lt_i32 s17, 0\n\ts_cbranch_scc1 98f\n\ts_cmp_lg_u32 s16, s91\n\ts_cbranch_scc1 98f\n\ts_mov_b64 exec, 1\n\ts_lshl_b32 s48, s17, 4\n\ts_add_u32 s48, s48, 161056\n\tv_mov_b32 v13, s48\n\ts_sub_u32 s48, s74, s90\n\ts_min_u32 s48, s48, 0xffff\n\tv_mov_b32 v14, s48\n\tds_write_b16 v13, v14 offset:0\n\ts_sub_u32 s48, s76, s74\n\ts_min_u32 s48, s48, 0xffff\n\tv_mov_b32 v14, s48\n\tds_write_b16 v13, v14 offset:2\n\ts_sub_u32 s48, s78, s76\n\ts_min_u32 s48, s48, 0xffff\n\tv_mov_b32 v14, s48\n\tds_write_b16 v13, v14 offset:4\n\ts_sub_u32 s48, s80, s78\n\ts_min_u32 s48, s48, 0xffff\n\tv_mov_b32 v14, s48\n\tds_write_b16 v13, v14 offset:6\n\ts_sub_u32 s48, s82, s80\n\ts_min_u32 s48, s48, 0xffff\n\tv_mov_b32 v14, s48\n\tds_write_b16 v13, v14 offset:8\n\ts_sub_u32 s48, s84, s82\n\ts_min_u32 s48, s48, 0xffff\n\tv_mov_b32 v14, s48\n\tds_write_b16 v13, v14 offset:10\n\ts_sub_u32 s48, s86, s84\n\ts_min_u32 s48, s48, 0xffff\n\tv_mov_b32 v14, s48\n\tds_write_b16 v13, v14 offset:12\n\ts_sub_u32 s48, s88, s86\n\ts_min_u32 s48, s48, 0xffff\n\tv_mov_b32 v14, s48\n\tds_write_b16 v13, v14 offset:14\n\ts_mov_b64 exec, -1\n\t98:\n\ts_mov_b32 s90, s88\n\t")
      "s_add_u32 s17, s17, 1\n\ts_cmp_eq_u32 s17, 64\n\ts_cbranch_scc1 9f\n\ts_cmp_eq_u32 s16, 1\n\ts_cbranch_scc1 12b\n\ts_branch 10b\n\t"
      "9:\n\t"
      ".Lmk_end5:\n\ts_waitcnt vmcnt(0) lgkmcnt(0)\n\t"
      TKS("s_mul_i32 s48, s16, 72\n\ts_add_u32 s48, s48, 162080\n\tv_mov_b32 v224, s48\n\tds_read_b32 v225, v224 offset:0\n\ts_waitcnt lgkmcnt(0)\n\tv_readfirstlane_b32 s74, v225\n\tds_read_b32 v225, v224 offset:4\n\ts_waitcnt lgkmcnt(0)\n\tv_readfirstlane_b32 s75, v225\n\tds_read_b32 v225, v224 offset:8\n\ts_waitcnt lgkmcnt(0)\n\tv_readfirstlane_b32 s76, v225\n\tds_read_b32 v225, v224 offset:12\n\ts_waitcnt lgkmcnt(0)\n\tv_readfirstlane_b32 s77, v225\n\tds_read_b32 v225, v224 offset:16\n\ts_waitcnt lgkmcnt(0)\n\tv_readfirstlane_b32 s78, v225\n\tds_read_b32 v225, v224 offset:20\n\ts_waitcnt lgkmcnt(0)\n\tv_readfirstlane_b32 s79, v225\n\tds_read_b32 v225, v224 offset:24\n\ts_waitcnt lgkmcnt(0)\n\tv_readfirstlane_b32 s80, v225\n\tds_read_b32 v225, v224 offset:28\n\ts_waitcnt lgkmcnt(0)\n\tv_readfirstlane_b32 s81, v225\n\tds_read_b32 v225, v224 offset:32\n\ts_waitcnt lgkmcnt(0)\n\tv_readfirstlane_b32 s82, v225\n\tds_read_b32 v225, v224 offset:36\n\ts_waitcnt lgkmcnt(0)\n\tv_readfirstlane_b32 s83, v225\n\tds_read_b32 v225, v224 offset:40\n\ts_waitcnt lgkmcnt(0)\n\tv_readfirstlane_b32 s84, v225\n\tds_read_b32 v225, v224 offset:44\n\ts_waitcnt lgkmcnt(0)\n\tv_readfirstlane_b32 s85, v225\n\tds_read_b32 v225, v224 offset:48\n\ts_waitcnt lgkmcnt(0)\n\tv_readfirstlane_b32 s86, v225\n\tds_read_b32 v225, v224 offset:52\n\ts_waitcnt lgkmcnt(0)\n\tv_readfirstlane_b32 s87, v225\n\tds_read_b32 v225, v224 offset:56\n\ts_waitcnt lgkmcnt(0)\n\tv_readfirstlane_b32 s88, v225\n\tds_read_b32 v225, v224 offset:60\n\ts_waitcnt lgkmcnt(0)\n\tv_readfirstlane_b32 s89, v225\n\tds_read_b32 v225, v224 offset:64\n\ts_waitcnt lgkmcnt(0)\n\tv_readfirstlane_b32 s90, v225\n\tds_read_b32 v225, v224 offset:68\n\ts_waitcnt lgkmcnt(0)\n\tv_readfirstlane_b32 s91, v225\n\t")
      STS("s_lshl_b32 s48, s16, 2\n\ts_add_u32 s48, s48, 162944\n\tv_mov_b32 v0, s48\n\tv_mov_b32 v1, s50\n\tds_write_b32 v0, v1\n\ts_cmp_lg_u32 s16, 0\n\ts_cbranch_scc1 97f\n\tv_mov_b32 v1, s51\n\tds_write_b32 v0, v1 offset:16\n\t97:\n\ts_cmp_lg_u32 s16, 1\n\ts_cbranch_scc1 95f\n\tv_mov_b32 v1, s61\n\tds_write_b32 v0, v1 offset:16\n\tv_mov_b32 v1, s21\n\tds_write_b32 v0, v1 offset:20\n\tv_mov_b32 v1, s22\n\tds_write_b32 v0, v1 offset:24\n\t95:\n\t")
      "s_cmp_lg_u32 s18, 0\n\ts_cbranch_scc1 96f\n\tv_mov_b32 v1, 1\n\tbuffer_store_dword v1, off, s[12:15], 0 offset:3328 sc1\n\t"
      "96:\n\t"
      : "+{v[0:31]}"(k), "+{v[160:191]}"(s0), "+{v[192:223]}"(s1), ALL_REGS :: "memory", "vcc", "scc", S_CLOB TK_CLOB);
}

struct KvIn { v4u kr[11], vr[8], sh[4]; };
DEV void kv_load(KArg p, KvIn& in, int u, int lane) {
  const rsrc_t rk = mkrs(p->bk, 2048 * 2048), rv = mkrs(p->bv, 2048 * 2048);
  const int de = (u >> 5) * 64 + (lane & 7) * 8, t0 = (u & 31) * 64 + (lane >> 3) * 8;
#pragma unroll
  for (int i = 0; i < 11; ++i) in.kr[i] = ld16n(rk, (t0 + i - 3) * 2048 + de * 2, 0);
#pragma unroll
  for (int i = 0; i < 8; ++i) in.vr[i] = ld16n(rv, (t0 + i) * 2048 + de * 2, 0);
#pragma unroll
  for (int i = 0; i < 4; ++i) in.sh[i] = *(const GAS v4u*)(p->shift + de * 4 + i * 8);
}
DEV f32x2 both(unsigned u) { return f32x2{blo(u), bhi(u)}; }
DEV void kv_make(Ctx& c, const KvIn& in, int at, int lane) {
#pragma unroll
  for (int cp = 0; cp < 4; ++cp) {
    const v4u sh = in.sh[cp];
    const f32x2 s0 = {blo(sh.x), blo(sh.z)}, s1 = {bhi(sh.x), bhi(sh.z)}, s2 = {blo(sh.y), blo(sh.w)}, s3 = {bhi(sh.y), bhi(sh.w)};
    f32x2 kk[11], o[8];
#pragma unroll
    for (int i = 0; i < 11; ++i) kk[i] = both(in.kr[i][cp]);
#pragma unroll
    for (int i = 0; i < 8; ++i)
      o[i] = rb2(__builtin_elementwise_fma(kk[i + 3], s0, __builtin_elementwise_fma(kk[i + 2], s1, __builtin_elementwise_fma(kk[i + 1], s2, kk[i] * s3)))) * both(in.vr[i][cp]);
#pragma unroll
    for (int e = 0; e < 2; ++e)
      sts16(c.L, at + (2 * cp + e) * 1152 + (lane >> 3) * 16 + (lane & 7) * 144, v4u{pk(o[0][e], o[1][e]), pk(o[2][e], o[3][e]), pk(o[4][e], o[5][e]), pk(o[6][e], o[7][e])});
  }
}
DEV int seq_off(int u, int lane, int va) { return amap(((u >> 5) * 64 + (lane >> 3) * 8) * 4096, va ^ ((u >> 7) & 1) ^ 1) + ((u & 31) * 64 + (lane & 7) * 8) * 2; }
DEV void kv_store(Ctx& c, int at, int u, int lane, int va) {
  LGKM(0);
#pragma unroll
  for (int ch = 0; ch < 8; ++ch) st16s(c.rs, seq_off(u, lane, va), R_KVT + amap(ch * 4096, 0), lds16(c.L, at + ch * 1152 + (lane & 7) * 16 + (lane >> 3) * 144));
}

DEV void y_role(Ctx& c, int y) {
  const KArg p = enter(c);
  constexpr int ZR = 0, UL = 131072, CW = 155648;
  c.budget = SPIN_CAP;
  const int w = c.w, lane = c.lane = fresh_lane(), g = lane >> 4, cc = lane & 15;
  const v4u sent4 = {SENT, SENT, SENT, SENT};
  unsigned m0 = 0, votes = 0;
  if (w == 0) {
    if (c.att == 0) {
      c.ask = __builtin_amdgcn_readfirstlane(__hip_atomic_load(c.ctrl + (C_ECHO + c.b * 128) / 4, RLX_AGENT)) + 1u;
      if (lane == 0) __hip_atomic_store(c.ctrl + (C_ASK + c.b * 128) / 4, c.ask, RLX_AGENT);
    }
    m0 = *(const GAS unsigned*)(p->mem0 + (lane >> 3) * 1024 + 16 * y + 2 * (lane & 7));
    m0 = m0 < SENT - 1u ? m0 : SENT - 1u;
    const int po = MB + y * 16384 + (y & 31) * 256 + (lane & 31) * 4;
    st4s(c.rs, po + (lane >> 5) * 8192, 0, SENT); st4s(c.rs, po + (lane >> 5) * 8192, 2 * MB, SENT);
    unsigned best[4] = {~0u, ~0u, ~0u, ~0u};
#pragma unroll
    for (int r = 0; r < NPROBE; ++r) {
      VMWAIT(0);
      const unsigned t0 = (unsigned)__builtin_amdgcn_s_memtime();
      LGKM(0);
      st4s(c.rs, po + ((r ^ y) & 1) * 8192, (r & 2) * MB, SENT);
      VMWAIT(0);
      const unsigned t1 = (unsigned)__builtin_amdgcn_s_memtime();
      LGKM(0);
      best[r & 3] = min(best[r & 3], t1 - t0);
    }
    votes = ((best[1] < best[0] ? 1u << 16 : 0u) | (best[3] < best[2] ? 1u << 24 : 0u)) ^ ((y & 1) ? 0x01010000u : 0u);
    if (lane < 8) *(LAS unsigned*)(c.L + CW + lane * 4) = 0u;
  }
  const bool srv = c.b == SERVER;
#pragma unroll 1
  for (int f = 0; f < 2; ++f) {
    if (srv ? w != 0 : w != f + 1) continue;
#pragma unroll
    for (int i = 0; i < 8; ++i) st16s(c.rs, y * 16384 + f * 8192 + lane * 16, R_U + i * 1024, sent4);
#pragma unroll
    for (int i = 0; i < 2; ++i) st16s(c.rs, f * 262144 + y * 2048 + lane * 16, R_MEM + i * 1024, sent4);
#pragma unroll
    for (int i = 0; i < 3; ++i) st16s(c.rs, (2 * y + f) * 3072 + lane * 16, R_PT + i * 1024, sent4);
    st16s(c.rs, (2 * y + f) * 1024 + lane * 16, R_FZ, v4u{0, 0, 0, 0});
    st16s(c.rs, lane < 8 ? (2 * y + f) * 128 + lane * 16 : 1 << 30, R_FQ, v4u{0, 0, 0, 0});
  }
  VMWAIT(0);
  if (!srv) BAR();
  claim(c, votes);
  STAMP(2);
  const int kvu = 8 * y + 2 * w, kva = 65536 + w * 18432;
  BAR();
  {
    const rsrc_t rx = mkrs(p->x, 2048 * 2048);
    unsigned t = 0;
#pragma unroll
    for (int i = 0; i < 2 * XWARM; ++i) t += __builtin_amdgcn_raw_buffer_load_b32(rx, (4 * y + w) * 8192 + lane * 128, i * 2 * MB, 0);
    if (TLBW && w == 1) {
      t += __builtin_amdgcn_raw_buffer_load_b32(mkrs(p->out, 2048 * 4096), lane * 131072 + y * 32, 0, 0);
      t += __builtin_amdgcn_raw_buffer_load_b32(c.rs, lane * 4 * MB + y * 65536, R_Z, 0);
    }
    asm volatile("" :: "v"(t));
  }
  const unsigned vote = wait_verdict(c);
  const int par = vote_b(vote), para = vote_a(vote);
  const int mo = amap(y * 256 + lane * 4, par);
  STAMP(0);
  if (w == 1) DIAG(6, (unsigned)__builtin_amdgcn_s_memrealtime());
  volatile LAS unsigned* const ZFULL = (volatile LAS unsigned*)(c.L + CW);
  volatile LAS unsigned* const GO = ZFULL + 1;
  volatile LAS unsigned* const TDONE = ZFULL + 2;
  volatile LAS unsigned* const PUB = ZFULL + 4;
  auto lds_wait = [&](volatile LAS unsigned* wd, unsigned need) {
#pragma unroll 1
    while (__builtin_amdgcn_readfirstlane(*wd) < need) { if (spend(c)) break; __builtin_amdgcn_s_sleep(1); }
    asm volatile("" ::: "memory");
  };
  auto lds_post = [&](volatile LAS unsigned* wd, unsigned v) { LGKM(0); *wd = v; };
  TICK_DECL;
  if (w == 0) {
    if (GATE) lds_wait(ZFULL, GATE);
#pragma unroll 1
    for (int i = 0; i < GATED; ++i) __builtin_amdgcn_s_sleep(16);
    st4s(c.rs, mo, R_MEM, m0);
    v32u k;
#pragma unroll
    for (int i = 0; i < 4; ++i) {
      const int o = (i + (y >> 2) + ROFF) & 3;
      k[i] = amap(o * 4096 + lane * 16, par);
      k[4 + i] = amap(16384 + o * 1024 + lane * 16, par);
      k[8 + i] = R_U + y * 16384 + para * 8192 + o * 2048 + lane * 16;
      k[12 + i] = ZR + o * 8192 + lane * 16;
      k[16 + i] = UL + o * 2048 + lane * 16;
    }
    k[20] = amap(y * 256 + (2 * g + (cc & 1)) * 32 + (cc >> 1) * 4, par);
    k[21] = (cc & 1) ? 0x03020706 : 0x05040100;
    k[22] = (lane & 8) ? 0xEEEE : 0x4444;
    k[23] = amap(24576 + ((y >> 2) & 3) * 4096 + (lane & 31) * 128, par);
    k[24] = CW;
#pragma unroll
    for (int i = 25; i < 32; ++i) k[i] = 0;
    v32u s0 = {RSRCS, 0, 0, c.budget, SKEW, 0, PACE, C1, 0, KH, ~0u, 0};
    v16u x0; v8u x3; v32u z[4], x[3];
    asm volatile(
      "v_mov_b32 v32, v23\n\tv_add_u32 v33, 0x8000, v23\n\tv_add_u32 v34, 0x10000, v23\n\ts_nop 0\n\tv_readfirstlane_b32 s8, v160\n\tv_readfirstlane_b32 s9, v161\n\t"
      "v_readfirstlane_b32 s10, v162\n\tv_readfirstlane_b32 s11, v163\n\tv_readfirstlane_b32 s12, v164\n\tv_readfirstlane_b32 s13, v165\n\tv_readfirstlane_b32 s14, v166\n\t"
      "v_readfirstlane_b32 s15, v167\n\tv_readfirstlane_b32 s16, v168\n\tv_readfirstlane_b32 s17, v169\n\tv_readfirstlane_b32 s18, v170\n\tv_readfirstlane_b32 s19, v171\n\t"
      "v_readfirstlane_b32 s20, v172\n\tv_readfirstlane_b32 s21, v173\n\tv_readfirstlane_b32 s22, v174\n\tv_readfirstlane_b32 s23, v175\n\tv_readfirstlane_b32 s24, v176\n\t"
      "v_readfirstlane_b32 s25, v177\n\tv_readfirstlane_b32 s26, v178\n\tv_readfirstlane_b32 s27, v179\n\tv_readfirstlane_b32 s28, v180\n\tv_readfirstlane_b32 s29, v181\n\t"
      "v_readfirstlane_b32 s30, v182\n\tv_readfirstlane_b32 s31, v183\n\ts_nop 4\n\t"
      TKS("s_mul_i32 s48, s16, 72\n\ts_add_u32 s48, s48, 162080\n\tv_mov_b32 v224, s48\n\tv_mov_b32 v225, s74\n\tds_write_b32 v224, v225 offset:0\n\tv_mov_b32 v225, s75\n\tds_write_b32 v224, v225 offset:4\n\tv_mov_b32 v225, s76\n\tds_write_b32 v224, v225 offset:8\n\tv_mov_b32 v225, s77\n\tds_write_b32 v224, v225 offset:12\n\tv_mov_b32 v225, s78\n\tds_write_b32 v224, v225 offset:16\n\tv_mov_b32 v225, s79\n\tds_write_b32 v224, v225 offset:20\n\tv_mov_b32 v225, s80\n\tds_write_b32 v224, v225 offset:24\n\tv_mov_b32 v225, s81\n\tds_write_b32 v224, v225 offset:28\n\tv_mov_b32 v225, s82\n\tds_write_b32 v224, v225 offset:32\n\tv_mov_b32 v225, s83\n\tds_write_b32 v224, v225 offset:36\n\tv_mov_b32 v225, s84\n\tds_write_b32 v224, v225 offset:40\n\tv_mov_b32 v225, s85\n\tds_write_b32 v224, v225 offset:44\n\tv_mov_b32 v225, s86\n\tds_write_b32 v224, v225 offset:48\n\tv_mov_b32 v225, s87\n\tds_write_b32 v224, v225 offset:52\n\tv_mov_b32 v225, s88\n\tds_write_b32 v224, v225 offset:56\n\tv_mov_b32 v225, s89\n\tds_write_b32 v224, v225 offset:60\n\tv_mov_b32 v225, s90\n\tds_write_b32 v224, v225 offset:64\n\tv_mov_b32 v225, s91\n\tds_write_b32 v224, v225 offset:68\n\t")
      "s_mov_b64 s[72:73], s[24:25]\n\t"
      TKS("s_mov_b32 s91, 0\n\t")
      "v_mov_b64 v[36:37], -1\n\tv_mov_b64 v[38:39], -1\n\ts_mov_b32 s42, 0x280000\n\ts_mov_b32 s43, 0x100000\n\ts_mov_b32 s21, s19\n\ts_memtime vcc\n\t"
      "s_memrealtime s[32:33]\n\ts_nop 1\n\ts_memrealtime s[34:35]\n\ts_nop 1\n\ts_memrealtime s[36:37]\n\ts_nop 1\n\ts_memrealtime s[38:39]\n\ts_nop 1\n\t"
      "s_memrealtime s[64:65]\n\ts_nop 1\n\ts_memrealtime s[66:67]\n\ts_nop 1\n\ts_memrealtime s[68:69]\n\ts_nop 1\n\ts_memrealtime s[70:71]\n\ts_nop 1\n\t"
      "s_waitcnt lgkmcnt(0)\n\ts_add_u32 s34, s34, s32\n\ts_add_u32 s34, s34, s36\n\ts_add_u32 s34, s34, s38\n\ts_add_u32 s34, s34, s64\n\ts_add_u32 s34, s34, s66\n\t"
      "s_add_u32 s34, s34, s68\n\ts_add_u32 s34, s34, s70\n\ts_lshl_b32 s36, s32, 3\n\ts_sub_u32 s34, s34, s36\n\ts_lshr_b32 s34, s34, 1\n\ts_lshl2_add_u32 s34, s32, s34\n\t"
      "s_add_u32 s34, s34, s21\n\ts_sub_u32 s36, s34, s20\n\ts_sub_u32 s37, vcc_lo, s22\n\ts_mov_b32 s20, s34\n\ts_mov_b32 s22, vcc_lo\n\tv_cvt_f32_u32 v25, s36\n\t"
      "v_cvt_f32_u32 v26, s37\n\tv_rcp_f32 v25, v25\n\tv_mul_f32 v26, 0x45800000, v26\n\tv_mul_f32 v25, v25, v26\n\tv_cvt_u32_f32 v25, v25\n\ts_cmp_lt_u32 s36, 300\n\t"
      "v_readfirstlane_b32 s34, v25\n\ts_cselect_b32 s34, 0, s34\n\ts_sub_u32 s36, s34, 10240\n\ts_cmp_gt_u32 s36, 16400\n\ts_cselect_b32 s34, s26, s34\n\t"
      "s_add_u32 s26, s26, s34\n\ts_lshr_b32 s26, s26, 1\n\ts_mov_b32 s26, 23040\n\ts_sub_u32 s19, s20, 0x100000\n\ts_mov_b32 s62, 440\n\ts_mov_b32 s63, 0\n\t"
      "10:\n\ts_and_b32 s48, s17, 3\n\ts_lshl_b32 s49, s48, 15\n\tv_add_u32 v28, s49, v12\n\tv_add_u32 v29, s49, v13\n\tv_add_u32 v30, s49, v14\n\tv_add_u32 v31, s49, v15\n\t"
      "ds_read_b32 v25, v24\n\ts_waitcnt lgkmcnt(0)\n\tv_readfirstlane_b32 s48, v25\n\ts_cmp_gt_i32 s48, s17\n\ts_cselect_b32 s28, 1, 0\n\ts_cbranch_scc0 30f\n\t"
      "ds_read_b128 a[0:3], v28 offset:0\n\tds_read_b128 a[4:7], v28 offset:1024\n\tds_read_b128 a[8:11], v28 offset:2048\n\tds_read_b128 a[12:15], v28 offset:3072\n\t"
      "ds_read_b128 a[16:19], v28 offset:4096\n\tds_read_b128 a[20:23], v28 offset:5120\n\tds_read_b128 a[24:27], v28 offset:6144\n\tds_read_b128 a[28:31], v28 offset:7168\n\t"
      "ds_read_b128 a[32:35], v29 offset:0\n\tds_read_b128 a[36:39], v29 offset:1024\n\tds_read_b128 a[40:43], v29 offset:2048\n\tds_read_b128 a[44:47], v29 offset:3072\n\t"
      "ds_read_b128 a[48:51], v29 offset:4096\n\tds_read_b128 a[52:55], v29 offset:5120\n\tds_read_b128 a[56:59], v29 offset:6144\n\t"
      "buffer_store_dword v36, v20, s[8:11], s42 offen sc1\n\tds_read_b128 a[60:63], v29 offset:7168\n\tds_read_b128 a[64:67], v30 offset:0\n\t"
      "buffer_store_dwordx4 v[36:39], v8, s[8:11], s43 offen offset:0 sc1\n\tds_read_b128 a[68:71], v30 offset:1024\n\tds_read_b128 a[72:75], v30 offset:2048\n\t"
      "buffer_store_dwordx4 v[36:39], v8, s[8:11], s43 offen offset:1024 sc1\n\tds_read_b128 a[76:79], v30 offset:3072\n\tds_read_b128 a[80:83], v30 offset:4096\n\t"
      "buffer_store_dwordx4 v[36:39], v9, s[8:11], s43 offen offset:0 sc1\n\tds_read_b128 a[84:87], v30 offset:5120\n\tds_read_b128 a[88:91], v30 offset:6144\n\t"
      "buffer_store_dwordx4 v[36:39], v9, s[8:11], s43 offen offset:1024 sc1\n\tds_read_b128 a[92:95], v30 offset:7168\n\tds_read_b128 a[96:99], v31 offset:0\n\t"
      "buffer_store_dwordx4 v[36:39], v10, s[8:11], s43 offen offset:0 sc1\n\tds_read_b128 a[100:103], v31 offset:1024\n\tds_read_b128 a[104:107], v31 offset:2048\n\t"
      "buffer_store_dwordx4 v[36:39], v10, s[8:11], s43 offen offset:1024 sc1\n\tds_read_b128 a[108:111], v31 offset:3072\n\tds_read_b128 a[112:115], v31 offset:4096\n\t"
      "buffer_store_dwordx4 v[36:39], v11, s[8:11], s43 offen offset:0 sc1\n\tds_read_b128 a[116:119], v31 offset:5120\n\tds_read_b128 a[120:123], v31 offset:6144\n\t"
      "buffer_store_dwordx4 v[36:39], v11, s[8:11], s43 offen offset:1024 sc1\n\tds_read_b128 a[124:127], v31 offset:7168\n\t"
      "31:\n\ts_and_b32 s48, s17, 3\n\ts_lshl_b32 s41, s48, 18\n\ts_add_u32 s41, s41, 0x208000\n\ts_add_u32 s48, s17, 1\n\ts_and_b32 s48, s48, 3\n\ts_lshl_b32 s40, s48, 18\n\t"
      "s_add_u32 s40, s40, 0x200000\n\ts_xor_b32 s42, s40, 0x80000\n\ts_and_b32 s43, s17, 1\n\ts_lshl_b32 s43, s43, 20\n\tv_mov_b64 v[116:117], 0\n\tv_mov_b64 v[118:119], 0\n\t"
      "v_mov_b64 v[120:121], 0\n\tv_mov_b64 v[122:123], 0\n\ts_add_u32 s46, s19, s29\n\ts_add_u32 s46, s46, s63\n\t"
      TK0
      STC("s_mov_b32 s57, s18\n\t")
      "s_sub_u32 s48, s46, s20\n\ts_cmp_gt_u32 s48, 4000\n\ts_cselect_b32 s48, 0, s48\n\ts_mul_i32 s48, s48, s26\n\ts_lshr_b32 s48, s48, 12\n\ts_add_u32 s32, s48, s22\n\t"
      "71:\n\ts_memtime vcc\n\ts_waitcnt lgkmcnt(0)\n\t"
      "70:\n\ts_sub_u32 s48, s32, vcc_lo\n\ts_cmp_gt_i32 s48, 160\n\ts_cbranch_scc1 71b\n\ts_max_i32 s48, s48, 0\n\ts_and_b32 s48, s48, -4\n\ts_getpc_b64 vcc\n\t"
      "72:\n\ts_sub_u32 s48, 73f-72b, s48\n\ts_add_u32 vcc_lo, vcc_lo, s48\n\ts_addc_u32 vcc_hi, vcc_hi, 0\n\ts_setpc_b64 vcc\n\t.rept 40\n\ts_nop 0\n\t.endr\n\t"
      "73:\n\tbuffer_load_dword v112, v32, s[8:11], s41 offen sc1\n\ts_cmp_eq_u32 s28, 0\n\ts_cbranch_scc1 32f\n\ts_sleep 4\n\t"
      "33:\n\ts_memtime vcc\n\ts_memrealtime s[32:33]\n\ts_nop 1\n\ts_memrealtime s[34:35]\n\ts_nop 1\n\ts_memrealtime s[36:37]\n\ts_nop 1\n\ts_memrealtime s[38:39]\n\t"
      "s_nop 1\n\ts_memrealtime s[64:65]\n\ts_nop 1\n\ts_memrealtime s[66:67]\n\ts_nop 1\n\ts_memrealtime s[68:69]\n\ts_nop 1\n\ts_memrealtime s[70:71]\n\ts_nop 1\n\t"
      "s_waitcnt lgkmcnt(0)\n\ts_add_u32 s34, s34, s32\n\ts_add_u32 s34, s34, s36\n\ts_add_u32 s34, s34, s38\n\ts_add_u32 s34, s34, s64\n\ts_add_u32 s34, s34, s66\n\t"
      "s_add_u32 s34, s34, s68\n\ts_add_u32 s34, s34, s70\n\ts_lshl_b32 s36, s32, 3\n\ts_sub_u32 s34, s34, s36\n\ts_lshr_b32 s34, s34, 1\n\ts_lshl2_add_u32 s34, s32, s34\n\t"
      "s_add_u32 s34, s34, s21\n\ts_sub_u32 s36, s34, s20\n\ts_sub_u32 s37, vcc_lo, s22\n\ts_mov_b32 s20, s34\n\ts_mov_b32 s22, vcc_lo\n\tv_cvt_f32_u32 v25, s36\n\t"
      "v_cvt_f32_u32 v26, s37\n\tv_rcp_f32 v25, v25\n\tv_mul_f32 v26, 0x45800000, v26\n\tv_mul_f32 v25, v25, v26\n\tv_cvt_u32_f32 v25, v25\n\ts_cmp_lt_u32 s36, 300\n\t"
      "v_readfirstlane_b32 s34, v25\n\ts_cselect_b32 s34, 0, s34\n\ts_sub_u32 s36, s34, 10240\n\ts_cmp_gt_u32 s36, 16400\n\ts_cselect_b32 s34, s26, s34\n\t"
      "s_add_u32 s26, s26, s34\n\ts_lshr_b32 s26, s26, 1\n\ts_waitcnt vmcnt(0)\n\ts_branch 76f\n\t"
      "74:\n\tbuffer_load_dword v112, v32, s[8:11], s41 offen sc1\n\ts_waitcnt vmcnt(0)\n\t"
      "76:\n\ts_memtime vcc\n\tv_max_u32_dpp v112, v112, v112 row_ror:8 row_mask:0xf bank_mask:0xf\n\ts_nop 1\n\t"
      "v_max_u32_dpp v112, v112, v112 row_ror:4 row_mask:0xf bank_mask:0xf\n\ts_nop 1\n\tv_max_u32_dpp v112, v112, v112 row_ror:2 row_mask:0xf bank_mask:0xf\n\ts_nop 1\n\t"
      "v_max_u32_dpp v112, v112, v112 row_ror:1 row_mask:0xf bank_mask:0xf\n\ts_nop 0\n\tv_readlane_b32 s52, v112, 0\n\tv_readlane_b32 s48, v112, 16\n\t"
      "s_max_u32 s52, s52, s48\n\ts_cmp_lg_u32 s52, -1\n\ts_cbranch_scc1 75f\n\ts_waitcnt lgkmcnt(0)\n\ts_cmp_eq_u32 s18, 0\n\ts_cbranch_scc1 75f\n\ts_sub_u32 s18, s18, 1\n\t"
      "s_branch 74b\n\t"
      "75:\n\ts_add_u32 s47, s52, s27\n\t"
      STC("s_waitcnt lgkmcnt(0)\n\ts_sub_u32 s58, vcc_lo, s22\n\ts_sub_u32 s56, s57, s18\n\ts_lshl_b32 s56, s56, 24\n\t")
      TK1
      "s_sub_u32 s48, s47, s20\n\ts_cmp_gt_u32 s48, 4000\n\ts_cselect_b32 s48, 0, s48\n\ts_mul_i32 s48, s48, s26\n\ts_lshr_b32 s48, s48, 12\n\ts_add_u32 s32, s48, s22\n\t"
      "s_waitcnt lgkmcnt(0)\n\ts_add_u32 vcc_lo, vcc_lo, 70\n\ts_branch 70f\n\t"
      "71:\n\ts_memtime vcc\n\ts_waitcnt lgkmcnt(0)\n\t"
      "70:\n\ts_sub_u32 s48, s32, vcc_lo\n\ts_cmp_gt_i32 s48, 160\n\ts_cbranch_scc1 71b\n\ts_max_i32 s48, s48, 0\n\ts_and_b32 s48, s48, -4\n\ts_getpc_b64 vcc\n\t"
      "72:\n\ts_sub_u32 s48, 73f-72b, s48\n\ts_add_u32 vcc_lo, vcc_lo, s48\n\ts_addc_u32 vcc_hi, vcc_hi, 0\n\ts_setpc_b64 vcc\n\t.rept 40\n\ts_nop 0\n\t.endr\n\t"
      "73:\n\t"
      "50:\n\t"
      TK2
      "buffer_load_dwordx4 v[176:179], v4, s[8:11], s41 offen offset:0 sc1\n\tbuffer_load_dwordx4 v[144:147], v8, s[8:11], s43 offen offset:0 sc1\n\t"
      "buffer_load_dwordx4 v[148:151], v8, s[8:11], s43 offen offset:1024 sc1\n\tbuffer_load_dwordx4 v[192:195], v0, s[8:11], s41 offen offset:0 sc1\n\t"
      "buffer_load_dwordx4 v[196:199], v0, s[8:11], s41 offen offset:1024 sc1\n\tbuffer_load_dwordx4 v[180:183], v5, s[8:11], s41 offen offset:0 sc1\n\t"
      "buffer_load_dwordx4 v[200:203], v0, s[8:11], s41 offen offset:2048 sc1\n\tbuffer_load_dwordx4 v[204:207], v0, s[8:11], s41 offen offset:3072 sc1\n\t"
      "buffer_load_dwordx4 v[152:155], v9, s[8:11], s43 offen offset:0 sc1\n\tbuffer_load_dwordx4 v[156:159], v9, s[8:11], s43 offen offset:1024 sc1\n\t"
      "buffer_load_dwordx4 v[208:211], v1, s[8:11], s41 offen offset:0 sc1\n\tbuffer_load_dwordx4 v[212:215], v1, s[8:11], s41 offen offset:1024 sc1\n\t"
      "buffer_load_dwordx4 v[184:187], v6, s[8:11], s41 offen offset:0 sc1\n\tbuffer_load_dwordx4 v[216:219], v1, s[8:11], s41 offen offset:2048 sc1\n\t"
      "buffer_load_dwordx4 v[220:223], v1, s[8:11], s41 offen offset:3072 sc1\n\tbuffer_load_dwordx4 v[160:163], v10, s[8:11], s43 offen offset:0 sc1\n\t"
      "buffer_load_dwordx4 v[164:167], v10, s[8:11], s43 offen offset:1024 sc1\n\tbuffer_load_dwordx4 v[224:227], v2, s[8:11], s41 offen offset:0 sc1\n\t"
      "buffer_load_dwordx4 v[228:231], v2, s[8:11], s41 offen offset:1024 sc1\n\tbuffer_load_dwordx4 v[188:191], v7, s[8:11], s41 offen offset:0 sc1\n\t"
      "buffer_load_dwordx4 v[232:235], v2, s[8:11], s41 offen offset:2048 sc1\n\tbuffer_load_dwordx4 v[236:239], v2, s[8:11], s41 offen offset:3072 sc1\n\t"
      "buffer_load_dwordx4 v[168:171], v11, s[8:11], s43 offen offset:0 sc1\n\tbuffer_load_dwordx4 v[172:175], v11, s[8:11], s43 offen offset:1024 sc1\n\t"
      "buffer_load_dwordx4 v[240:243], v3, s[8:11], s41 offen offset:0 sc1\n\tbuffer_load_dwordx4 v[244:247], v3, s[8:11], s41 offen offset:1024 sc1\n\t"
      "buffer_load_dwordx4 v[248:251], v3, s[8:11], s41 offen offset:2048 sc1\n\tbuffer_load_dwordx4 v[252:255], v3, s[8:11], s41 offen offset:3072 sc1\n\t"
      "s_cmp_eq_u32 s28, 0\n\ts_cbranch_scc1 34f\n\t"
      "35:\n\ts_mov_b32 vcc_lo, 0x00ff00ff\n\ts_mov_b32 vcc_hi, 0x00ff00ff\n\t"
      TK3
      "s_waitcnt vmcnt(27)\n\t"
      TK4
      "v_cndmask_b32_dpp v128, v178, v176, vcc row_ror:8 row_mask:0xf bank_mask:0xf\n\tv_cndmask_b32_dpp v129, v179, v177, vcc row_ror:8 row_mask:0xf bank_mask:0xf\n\t"
      "s_not_b64 vcc, vcc\n\tv_cndmask_b32_dpp v130, v176, v178, vcc row_ror:8 row_mask:0xf bank_mask:0xf\n\ts_waitcnt vmcnt(24)\n\t"
      "v_smfmac_f32_16x16x64_bf16 v[120:123], v[192:195], a[0:7], v22\n\ts_waitcnt vmcnt(23)\n\tv_cndmask_b32_dpp v131, v177, v179, vcc row_ror:8 row_mask:0xf bank_mask:0xf\n\t"
      "v_smfmac_f32_16x16x64_bf16 v[120:123], v[196:199], a[8:15], v22\n\ts_nop 0\n\tv_smfmac_f32_16x16x64_bf16 v[120:123], v[128:131], v[144:151], v22\n\t"
      "s_waitcnt vmcnt(21)\n\tv_cndmask_b32_dpp v134, v180, v182, vcc row_ror:8 row_mask:0xf bank_mask:0xf\n\t"
      "v_smfmac_f32_16x16x64_bf16 v[120:123], v[200:203], a[16:23], v22\n\ts_waitcnt vmcnt(20)\n\t"
      "v_cndmask_b32_dpp v135, v181, v183, vcc row_ror:8 row_mask:0xf bank_mask:0xf\n\tv_smfmac_f32_16x16x64_bf16 v[120:123], v[204:207], a[24:31], v22\n\t"
      "s_waitcnt vmcnt(17)\n\ts_not_b64 vcc, vcc\n\tv_cndmask_b32_dpp v132, v182, v180, vcc row_ror:8 row_mask:0xf bank_mask:0xf\n\t"
      "v_smfmac_f32_16x16x64_bf16 v[120:123], v[208:211], a[32:39], v22\n\ts_waitcnt vmcnt(16)\n\t"
      "v_cndmask_b32_dpp v133, v183, v181, vcc row_ror:8 row_mask:0xf bank_mask:0xf\n\tv_smfmac_f32_16x16x64_bf16 v[120:123], v[212:215], a[40:47], v22\n\ts_nop 0\n\t"
      "v_smfmac_f32_16x16x64_bf16 v[120:123], v[132:135], v[152:159], v22\n\ts_waitcnt vmcnt(14)\n\t"
      "v_cndmask_b32_dpp v136, v186, v184, vcc row_ror:8 row_mask:0xf bank_mask:0xf\n\tv_smfmac_f32_16x16x64_bf16 v[120:123], v[216:219], a[48:55], v22\n\t"
      "s_waitcnt vmcnt(13)\n\tv_cndmask_b32_dpp v137, v187, v185, vcc row_ror:8 row_mask:0xf bank_mask:0xf\n\t"
      "v_smfmac_f32_16x16x64_bf16 v[120:123], v[220:223], a[56:63], v22\n\ts_waitcnt vmcnt(10)\n\ts_not_b64 vcc, vcc\n\t"
      "v_cndmask_b32_dpp v138, v184, v186, vcc row_ror:8 row_mask:0xf bank_mask:0xf\n\tv_smfmac_f32_16x16x64_bf16 v[120:123], v[224:227], a[64:71], v22\n\ts_waitcnt vmcnt(9)\n\t"
      "v_cndmask_b32_dpp v139, v185, v187, vcc row_ror:8 row_mask:0xf bank_mask:0xf\n\tv_smfmac_f32_16x16x64_bf16 v[120:123], v[228:231], a[72:79], v22\n\ts_nop 0\n\t"
      "v_smfmac_f32_16x16x64_bf16 v[120:123], v[136:139], v[160:167], v22\n\ts_waitcnt vmcnt(7)\n\t"
      "v_cndmask_b32_dpp v142, v188, v190, vcc row_ror:8 row_mask:0xf bank_mask:0xf\n\tv_smfmac_f32_16x16x64_bf16 v[120:123], v[232:235], a[80:87], v22\n\ts_waitcnt vmcnt(6)\n\t"
      "v_cndmask_b32_dpp v143, v189, v191, vcc row_ror:8 row_mask:0xf bank_mask:0xf\n\tv_smfmac_f32_16x16x64_bf16 v[120:123], v[236:239], a[88:95], v22\n\ts_waitcnt vmcnt(3)\n\t"
      "s_not_b64 vcc, vcc\n\tv_cndmask_b32_dpp v140, v190, v188, vcc row_ror:8 row_mask:0xf bank_mask:0xf\n\t"
      "v_smfmac_f32_16x16x64_bf16 v[120:123], v[240:243], a[96:103], v22\n\ts_waitcnt vmcnt(2)\n\t"
      "v_cndmask_b32_dpp v141, v191, v189, vcc row_ror:8 row_mask:0xf bank_mask:0xf\n\tv_smfmac_f32_16x16x64_bf16 v[120:123], v[244:247], a[104:111], v22\n\ts_nop 0\n\t"
      "v_smfmac_f32_16x16x64_bf16 v[120:123], v[140:143], v[168:175], v22\n\ts_waitcnt vmcnt(1)\n\tv_smfmac_f32_16x16x64_bf16 v[120:123], v[248:251], a[112:119], v22\n\t"
      "s_waitcnt vmcnt(0)\n\tv_smfmac_f32_16x16x64_bf16 v[120:123], v[252:255], a[120:127], v22\n\t"
      TK5
      "s_nop 7\n\tv_permlane32_swap_b32 v120, v122\n\tv_permlane32_swap_b32 v121, v123\n\tv_add_f32 v124, v120, v122\n\tv_add_f32 v125, v121, v123\n\t"
      "v_cvt_pk_bf16_f32 v126, v124, v125\n\tv_cmp_u_f32 vcc, v124, v125\n\ts_nop 0\n\tv_mov_b32_dpp v127, v126 quad_perm:[1,0,3,2] row_mask:0xf bank_mask:0xf\n\t"
      "v_perm_b32 v126, v127, v126, v21\n\ts_cbranch_vccnz 60f\n\t"
      "51:\n\tbuffer_store_dword v126, v20, s[8:11], s40 offen sc1\n\t"
      TK6
      STS("s_memrealtime s[54:55]\n\t")
      "s_add_u32 s49, s17, 1\n\tv_mov_b32 v26, s49\n\tds_write_b32 v24, v26 offset:16\n\ts_sub_u32 s49, s17, 3\n\ts_sleep 3\n\t"
      "22:\n\tds_read_b32 v25, v24 offset:8\n\ts_waitcnt lgkmcnt(0)\n\tv_readfirstlane_b32 s48, v25\n\ts_cmp_gt_i32 s48, s49\n\ts_cbranch_scc1 23f\n\ts_cmp_eq_u32 s18, 0\n\t"
      "s_cbranch_scc1 23f\n\ts_sub_u32 s18, s18, 1\n\ts_sleep 1\n\ts_branch 22b\n\t"
      "23:\n\t"
      "24:\n\tds_read_b32 v25, v24 offset:12\n\ts_waitcnt lgkmcnt(0)\n\tv_readfirstlane_b32 s48, v25\n\ts_cmp_gt_i32 s48, s49\n\ts_cbranch_scc1 25f\n\ts_cmp_eq_u32 s18, 0\n\t"
      "s_cbranch_scc1 25f\n\ts_sub_u32 s18, s18, 1\n\ts_sleep 1\n\ts_branch 24b\n\t"
      "25:\n\tv_add_u32 v28, s31, v16\n\tv_add_u32 v29, s31, v17\n\tv_add_u32 v30, s31, v18\n\tv_add_u32 v31, s31, v19\n\tds_write_b128 v28, v[144:147] offset:0\n\t"
      "ds_write_b128 v28, v[148:151] offset:1024\n\tds_write_b128 v29, v[152:155] offset:0\n\tds_write_b128 v29, v[156:159] offset:1024\n\t"
      "ds_write_b128 v30, v[160:163] offset:0\n\tds_write_b128 v30, v[164:167] offset:1024\n\tds_write_b128 v31, v[168:171] offset:0\n\t"
      "ds_write_b128 v31, v[172:175] offset:1024\n\ts_add_u32 s31, s31, 0x2000\n\ts_cmp_eq_u32 s31, 0x6000\n\ts_cselect_b32 s31, 0, s31\n\tds_write_b32 v24, v26 offset:4\n\t"
      STC("s_lshl_b32 s48, s17, 4\n\ts_add_u32 s48, s48, 161056\n\tv_mov_b32 v112, s48\n\ts_sub_u32 s48, s52, s19\n\tv_mov_b32 v116, s48\n\tv_mov_b32 v117, s54\n\tv_mov_b32 v118, s56\n\ts_mul_i32 s58, s58, 45\n\ts_lshr_b32 s58, s58, 8\n\ts_add_u32 s58, s58, s20\n\ts_sub_u32 s58, s52, s58\n\tv_mov_b32 v119, s58\n\tds_write_b128 v112, v[116:119]\n\ts_lshl_b32 s48, s54, 2\n\ts_sub_u32 s48, s48, s52\n\ts_add_u32 s51, s51, s48\n\t")
      "s_sub_u32 s48, s52, s19\n\ts_sub_u32 s48, s48, s62\n\ts_cmp_gt_i32 s48, s72\n\ts_cselect_b32 s19, s73, 0\n\ts_cmp_gt_i32 s48, 100\n\ts_cselect_b32 s19, 40, s19\n\t"
      "s_cmp_gt_i32 s48, 40\n\ts_cselect_b32 s48, 24, s48\n\ts_ashr_i32 s48, s48, 2\n\ts_add_i32 s62, s62, s48\n\ts_min_u32 s62, s62, 1000\n\ts_mul_i32 s63, s62, 3\n\t"
      "s_lshr_b32 s63, s63, 3\n\ts_add_u32 s19, s19, s52\n\ts_add_u32 s19, s19, s23\n\ts_add_u32 s19, s19, s63\n\t"
      TK7
      STS("s_cmp_lt_i32 s17, 8\n\ts_cselect_b32 s50, 0, s50\n\ts_cselect_b32 s51, 0, s51\n\ts_cselect_b32 s61, 0, s61\n\t")
      TKS("s_waitcnt lgkmcnt(0)\n\ts_cmp_lt_i32 s17, 0\n\ts_cbranch_scc1 98f\n\ts_cmp_lg_u32 s16, s91\n\ts_cbranch_scc1 98f\n\ts_mov_b64 exec, 1\n\ts_lshl_b32 s48, s17, 4\n\ts_add_u32 s48, s48, 161056\n\tv_mov_b32 v25, s48\n\ts_sub_u32 s48, s74, s90\n\ts_min_u32 s48, s48, 0xffff\n\tv_mov_b32 v26, s48\n\tds_write_b16 v25, v26 offset:0\n\ts_sub_u32 s48, s76, s74\n\ts_min_u32 s48, s48, 0xffff\n\tv_mov_b32 v26, s48\n\tds_write_b16 v25, v26 offset:2\n\ts_sub_u32 s48, s78, s76\n\ts_min_u32 s48, s48, 0xffff\n\tv_mov_b32 v26, s48\n\tds_write_b16 v25, v26 offset:4\n\ts_sub_u32 s48, s80, s78\n\ts_min_u32 s48, s48, 0xffff\n\tv_mov_b32 v26, s48\n\tds_write_b16 v25, v26 offset:6\n\ts_sub_u32 s48, s82, s80\n\ts_min_u32 s48, s48, 0xffff\n\tv_mov_b32 v26, s48\n\tds_write_b16 v25, v26 offset:8\n\ts_sub_u32 s48, s84, s82\n\ts_min_u32 s48, s48, 0xffff\n\tv_mov_b32 v26, s48\n\tds_write_b16 v25, v26 offset:10\n\ts_sub_u32 s48, s86, s84\n\ts_min_u32 s48, s48, 0xffff\n\tv_mov_b32 v26, s48\n\tds_write_b16 v25, v26 offset:12\n\ts_sub_u32 s48, s88, s86\n\ts_min_u32 s48, s48, 0xffff\n\tv_mov_b32 v26, s48\n\tds_write_b16 v25, v26 offset:14\n\ts_mov_b64 exec, -1\n\t98:\n\ts_mov_b32 s90, s88\n\t")
      "s_add_u32 s17, s17, 1\n\ts_cmp_lt_u32 s17, 63\n\ts_cbranch_scc1 10b\n\ts_branch 40f\n\t"
      "30:\n\ts_sleep 6\n\tbuffer_store_dword v36, v20, s[8:11], s42 offen sc1\n\tbuffer_store_dwordx4 v[36:39], v8, s[8:11], s43 offen offset:0 sc1\n\t"
      "buffer_store_dwordx4 v[36:39], v8, s[8:11], s43 offen offset:1024 sc1\n\tbuffer_store_dwordx4 v[36:39], v9, s[8:11], s43 offen offset:0 sc1\n\t"
      "buffer_store_dwordx4 v[36:39], v9, s[8:11], s43 offen offset:1024 sc1\n\tbuffer_store_dwordx4 v[36:39], v10, s[8:11], s43 offen offset:0 sc1\n\t"
      "buffer_store_dwordx4 v[36:39], v10, s[8:11], s43 offen offset:1024 sc1\n\tbuffer_store_dwordx4 v[36:39], v11, s[8:11], s43 offen offset:0 sc1\n\t"
      "buffer_store_dwordx4 v[36:39], v11, s[8:11], s43 offen offset:1024 sc1\n\ts_branch 31b\n\t"
      "32:\n\tds_read_b32 v25, v24\n\ts_waitcnt lgkmcnt(0)\n\tv_readfirstlane_b32 s48, v25\n\ts_cmp_gt_i32 s48, s17\n\ts_cselect_b32 s28, 1, 0\n\ts_cbranch_scc0 33b\n\t"
      "ds_read_b128 a[0:3], v28 offset:0\n\tds_read_b128 a[4:7], v28 offset:1024\n\tds_read_b128 a[8:11], v28 offset:2048\n\tds_read_b128 a[12:15], v28 offset:3072\n\t"
      "ds_read_b128 a[16:19], v28 offset:4096\n\tds_read_b128 a[20:23], v28 offset:5120\n\tds_read_b128 a[24:27], v28 offset:6144\n\tds_read_b128 a[28:31], v28 offset:7168\n\t"
      "ds_read_b128 a[32:35], v29 offset:0\n\tds_read_b128 a[36:39], v29 offset:1024\n\tds_read_b128 a[40:43], v29 offset:2048\n\tds_read_b128 a[44:47], v29 offset:3072\n\t"
      "ds_read_b128 a[48:51], v29 offset:4096\n\tds_read_b128 a[52:55], v29 offset:5120\n\tds_read_b128 a[56:59], v29 offset:6144\n\tds_read_b128 a[60:63], v29 offset:7168\n\t"
      "ds_read_b128 a[64:67], v30 offset:0\n\tds_read_b128 a[68:71], v30 offset:1024\n\tds_read_b128 a[72:75], v30 offset:2048\n\tds_read_b128 a[76:79], v30 offset:3072\n\t"
      "ds_read_b128 a[80:83], v30 offset:4096\n\tds_read_b128 a[84:87], v30 offset:5120\n\tds_read_b128 a[88:91], v30 offset:6144\n\tds_read_b128 a[92:95], v30 offset:7168\n\t"
      "ds_read_b128 a[96:99], v31 offset:0\n\tds_read_b128 a[100:103], v31 offset:1024\n\tds_read_b128 a[104:107], v31 offset:2048\n\t"
      "ds_read_b128 a[108:111], v31 offset:3072\n\tds_read_b128 a[112:115], v31 offset:4096\n\tds_read_b128 a[116:119], v31 offset:5120\n\t"
      "ds_read_b128 a[120:123], v31 offset:6144\n\tds_read_b128 a[124:127], v31 offset:7168\n\ts_waitcnt lgkmcnt(0)\n\ts_branch 33b\n\t"
      "34:\n\t"
      "20:\n\tds_read_b32 v25, v24 offset:0\n\ts_waitcnt lgkmcnt(0)\n\tv_readfirstlane_b32 s48, v25\n\ts_cmp_gt_i32 s48, s17\n\ts_cbranch_scc1 21f\n\ts_cmp_eq_u32 s18, 0\n\t"
      "s_cbranch_scc1 21f\n\ts_sub_u32 s18, s18, 1\n\ts_sleep 1\n\ts_branch 20b\n\t"
      "21:\n\tds_read_b128 a[0:3], v28 offset:0\n\tds_read_b128 a[4:7], v28 offset:1024\n\tds_read_b128 a[8:11], v28 offset:2048\n\tds_read_b128 a[12:15], v28 offset:3072\n\t"
      "ds_read_b128 a[16:19], v28 offset:4096\n\tds_read_b128 a[20:23], v28 offset:5120\n\tds_read_b128 a[24:27], v28 offset:6144\n\tds_read_b128 a[28:31], v28 offset:7168\n\t"
      "ds_read_b128 a[32:35], v29 offset:0\n\tds_read_b128 a[36:39], v29 offset:1024\n\tds_read_b128 a[40:43], v29 offset:2048\n\tds_read_b128 a[44:47], v29 offset:3072\n\t"
      "ds_read_b128 a[48:51], v29 offset:4096\n\tds_read_b128 a[52:55], v29 offset:5120\n\tds_read_b128 a[56:59], v29 offset:6144\n\tds_read_b128 a[60:63], v29 offset:7168\n\t"
      "ds_read_b128 a[64:67], v30 offset:0\n\tds_read_b128 a[68:71], v30 offset:1024\n\tds_read_b128 a[72:75], v30 offset:2048\n\tds_read_b128 a[76:79], v30 offset:3072\n\t"
      "ds_read_b128 a[80:83], v30 offset:4096\n\tds_read_b128 a[84:87], v30 offset:5120\n\tds_read_b128 a[88:91], v30 offset:6144\n\tds_read_b128 a[92:95], v30 offset:7168\n\t"
      "ds_read_b128 a[96:99], v31 offset:0\n\tds_read_b128 a[100:103], v31 offset:1024\n\tds_read_b128 a[104:107], v31 offset:2048\n\t"
      "ds_read_b128 a[108:111], v31 offset:3072\n\tds_read_b128 a[112:115], v31 offset:4096\n\tds_read_b128 a[116:119], v31 offset:5120\n\t"
      "ds_read_b128 a[120:123], v31 offset:6144\n\tds_read_b128 a[124:127], v31 offset:7168\n\ts_mov_b32 s28, 1\n\ts_waitcnt lgkmcnt(0)\n\ts_branch 35b\n\t"
      "60:\n\ts_cmp_eq_u32 s30, s17\n\ts_mov_b32 s30, s17\n\ts_cbranch_scc0 61f\n\tv_max3_u32 v115, v144, v145, v146\n\tv_max_u32 v115, v115, v147\n\t"
      "v_max3_u32 v115, v115, v148, v149\n\tv_max3_u32 v115, v115, v150, v151\n\tv_max3_u32 v115, v115, v152, v153\n\tv_max3_u32 v115, v115, v154, v155\n\t"
      "v_max3_u32 v115, v115, v156, v157\n\tv_max3_u32 v115, v115, v158, v159\n\tv_max3_u32 v115, v115, v160, v161\n\tv_max3_u32 v115, v115, v162, v163\n\t"
      "v_max3_u32 v115, v115, v164, v165\n\tv_max3_u32 v115, v115, v166, v167\n\tv_max3_u32 v115, v115, v168, v169\n\tv_max3_u32 v115, v115, v170, v171\n\t"
      "v_max3_u32 v115, v115, v172, v173\n\tv_max3_u32 v115, v115, v174, v175\n\tv_max3_u32 v115, v115, v176, v177\n\tv_max3_u32 v115, v115, v178, v179\n\t"
      "v_max3_u32 v115, v115, v180, v181\n\tv_max3_u32 v115, v115, v182, v183\n\tv_max3_u32 v115, v115, v184, v185\n\tv_max3_u32 v115, v115, v186, v187\n\t"
      "v_max3_u32 v115, v115, v188, v189\n\tv_max3_u32 v115, v115, v190, v191\n\tv_max3_u32 v115, v115, v192, v193\n\tv_max3_u32 v115, v115, v194, v195\n\t"
      "v_max3_u32 v115, v115, v196, v197\n\tv_max3_u32 v115, v115, v198, v199\n\tv_max3_u32 v115, v115, v200, v201\n\tv_max3_u32 v115, v115, v202, v203\n\t"
      "v_max3_u32 v115, v115, v204, v205\n\tv_max3_u32 v115, v115, v206, v207\n\tv_max3_u32 v115, v115, v208, v209\n\tv_max3_u32 v115, v115, v210, v211\n\t"
      "v_max3_u32 v115, v115, v212, v213\n\tv_max3_u32 v115, v115, v214, v215\n\tv_max3_u32 v115, v115, v216, v217\n\tv_max3_u32 v115, v115, v218, v219\n\t"
      "v_max3_u32 v115, v115, v220, v221\n\tv_max3_u32 v115, v115, v222, v223\n\tv_max3_u32 v115, v115, v224, v225\n\tv_max3_u32 v115, v115, v226, v227\n\t"
      "v_max3_u32 v115, v115, v228, v229\n\tv_max3_u32 v115, v115, v230, v231\n\tv_max3_u32 v115, v115, v232, v233\n\tv_max3_u32 v115, v115, v234, v235\n\t"
      "v_max3_u32 v115, v115, v236, v237\n\tv_max3_u32 v115, v115, v238, v239\n\tv_max3_u32 v115, v115, v240, v241\n\tv_max3_u32 v115, v115, v242, v243\n\t"
      "v_max3_u32 v115, v115, v244, v245\n\tv_max3_u32 v115, v115, v246, v247\n\tv_max3_u32 v115, v115, v248, v249\n\tv_max3_u32 v115, v115, v250, v251\n\t"
      "v_max3_u32 v115, v115, v252, v253\n\tv_max3_u32 v115, v115, v254, v255\n\tv_cmp_eq_u32 vcc, -1, v115\n\ts_cbranch_vccz 51b\n\ts_cmp_eq_u32 s18, 0\n\t"
      "s_cbranch_scc1 51b\n\ts_sub_u32 s18, s18, 1\n\t"
      "61:\n\tv_mov_b64 v[116:117], 0\n\tv_mov_b64 v[118:119], 0\n\tv_mov_b64 v[120:121], 0\n\tv_mov_b64 v[122:123], 0\n\t"
      STS("s_add_u32 s50, s50, 1\n\t")
      STC("s_add_u32 s56, s56, 1\n\t")
      "s_branch 50b\n\t"
      "40:\n\t"
      ".Lmk_end6:\n\ts_waitcnt vmcnt(0) lgkmcnt(0)\n\t"
      TKS("s_mul_i32 s48, s16, 72\n\ts_add_u32 s48, s48, 162080\n\tv_mov_b32 v224, s48\n\tds_read_b32 v225, v224 offset:0\n\ts_waitcnt lgkmcnt(0)\n\tv_readfirstlane_b32 s74, v225\n\tds_read_b32 v225, v224 offset:4\n\ts_waitcnt lgkmcnt(0)\n\tv_readfirstlane_b32 s75, v225\n\tds_read_b32 v225, v224 offset:8\n\ts_waitcnt lgkmcnt(0)\n\tv_readfirstlane_b32 s76, v225\n\tds_read_b32 v225, v224 offset:12\n\ts_waitcnt lgkmcnt(0)\n\tv_readfirstlane_b32 s77, v225\n\tds_read_b32 v225, v224 offset:16\n\ts_waitcnt lgkmcnt(0)\n\tv_readfirstlane_b32 s78, v225\n\tds_read_b32 v225, v224 offset:20\n\ts_waitcnt lgkmcnt(0)\n\tv_readfirstlane_b32 s79, v225\n\tds_read_b32 v225, v224 offset:24\n\ts_waitcnt lgkmcnt(0)\n\tv_readfirstlane_b32 s80, v225\n\tds_read_b32 v225, v224 offset:28\n\ts_waitcnt lgkmcnt(0)\n\tv_readfirstlane_b32 s81, v225\n\tds_read_b32 v225, v224 offset:32\n\ts_waitcnt lgkmcnt(0)\n\tv_readfirstlane_b32 s82, v225\n\tds_read_b32 v225, v224 offset:36\n\ts_waitcnt lgkmcnt(0)\n\tv_readfirstlane_b32 s83, v225\n\tds_read_b32 v225, v224 offset:40\n\ts_waitcnt lgkmcnt(0)\n\tv_readfirstlane_b32 s84, v225\n\tds_read_b32 v225, v224 offset:44\n\ts_waitcnt lgkmcnt(0)\n\tv_readfirstlane_b32 s85, v225\n\tds_read_b32 v225, v224 offset:48\n\ts_waitcnt lgkmcnt(0)\n\tv_readfirstlane_b32 s86, v225\n\tds_read_b32 v225, v224 offset:52\n\ts_waitcnt lgkmcnt(0)\n\tv_readfirstlane_b32 s87, v225\n\tds_read_b32 v225, v224 offset:56\n\ts_waitcnt lgkmcnt(0)\n\tv_readfirstlane_b32 s88, v225\n\tds_read_b32 v225, v224 offset:60\n\ts_waitcnt lgkmcnt(0)\n\tv_readfirstlane_b32 s89, v225\n\tds_read_b32 v225, v224 offset:64\n\ts_waitcnt lgkmcnt(0)\n\tv_readfirstlane_b32 s90, v225\n\tds_read_b32 v225, v224 offset:68\n\ts_waitcnt lgkmcnt(0)\n\tv_readfirstlane_b32 s91, v225\n\t")
      STS("s_lshl_b32 s48, s16, 2\n\ts_add_u32 s48, s48, 162944\n\tv_mov_b32 v0, s48\n\tv_mov_b32 v1, s50\n\tds_write_b32 v0, v1\n\ts_cmp_lg_u32 s16, 0\n\ts_cbranch_scc1 97f\n\tv_mov_b32 v1, s51\n\tds_write_b32 v0, v1 offset:16\n\t97:\n\ts_cmp_lg_u32 s16, 0\n\ts_cbranch_scc1 95f\n\tv_mov_b32 v1, s61\n\tds_write_b32 v0, v1 offset:20\n\tv_mov_b32 v1, s21\n\tds_write_b32 v0, v1 offset:24\n\tv_mov_b32 v1, s22\n\tds_write_b32 v0, v1 offset:28\n\t95:\n\t")
      "s_cmp_lg_u32 s18, 0\n\ts_cbranch_scc1 96f\n\tv_mov_b32 v1, 1\n\tbuffer_store_dword v1, off, s[12:15], 0 offset:3328 sc1\n\t"
      "96:\n\t"
      : "+{v[0:31]}"(k), "+{v[160:191]}"(s0), "=&{v[32:39]}"(x3), "=&{v[112:127]}"(x0), "=&{v[128:159]}"(x[0]), "=&{v[192:223]}"(x[1]), "=&{v[224:255]}"(x[2]),
        "=&{a[0:31]}"(z[0]), "=&{a[32:63]}"(z[1]), "=&{a[64:95]}"(z[2]), "=&{a[96:127]}"(z[3])
      :: "memory", "vcc", "scc", S_CLOB TK_CLOB);
  } else if (w < 3) {
    v16u k = {(unsigned)(par * 8192 + lane * 16) + ((HACK & 32) << 25), (unsigned)(UL + lane * 16), (unsigned)(ZR + (w == 2 ? lane * 16 : (g >> 1) * 1024 + (g & 1) * 256 + cc * 16)),
              (unsigned)((16 * (w - 1) + cc) * 4096 + (16 * y + 4 * g) * 2) | ((HACK & 1) << 30), CW};
    v32u s0 = {RSRCS, (unsigned)w, 0, c.budget, 0, plo(p->out), phi(p->out), 2048 * 2048 * 2, 0x00020000, 0, 0};
    v32u x[6];
#define TOK_REGS "+{v[0:15]}"(k), "+{v[160:191]}"(s0), "=&{v[32:63]}"(x[0]), "=&{v[64:95]}"(x[1]), "=&{v[96:127]}"(x[2]), "=&{v[128:159]}"(x[3]), "=&{v[192:223]}"(x[4]), "=&{v[224:255]}"(x[5])
    if (w == 1) asm volatile(
      "s_nop 0\n\tv_readfirstlane_b32 s8, v160\n\tv_readfirstlane_b32 s9, v161\n\tv_readfirstlane_b32 s10, v162\n\tv_readfirstlane_b32 s11, v163\n\t"
      "v_readfirstlane_b32 s12, v164\n\tv_readfirstlane_b32 s13, v165\n\tv_readfirstlane_b32 s14, v166\n\tv_readfirstlane_b32 s15, v167\n\tv_readfirstlane_b32 s16, v168\n\t"
      "v_readfirstlane_b32 s17, v169\n\tv_readfirstlane_b32 s18, v170\n\tv_readfirstlane_b32 s19, v171\n\tv_readfirstlane_b32 s20, v172\n\tv_readfirstlane_b32 s21, v173\n\t"
      "v_readfirstlane_b32 s22, v174\n\tv_readfirstlane_b32 s23, v175\n\tv_readfirstlane_b32 s24, v176\n\tv_readfirstlane_b32 s25, v177\n\ts_nop 4\n\t"
      TKS("s_mul_i32 s48, s16, 72\n\ts_add_u32 s48, s48, 162080\n\tv_mov_b32 v224, s48\n\tv_mov_b32 v225, s74\n\tds_write_b32 v224, v225 offset:0\n\tv_mov_b32 v225, s75\n\tds_write_b32 v224, v225 offset:4\n\tv_mov_b32 v225, s76\n\tds_write_b32 v224, v225 offset:8\n\tv_mov_b32 v225, s77\n\tds_write_b32 v224, v225 offset:12\n\tv_mov_b32 v225, s78\n\tds_write_b32 v224, v225 offset:16\n\tv_mov_b32 v225, s79\n\tds_write_b32 v224, v225 offset:20\n\tv_mov_b32 v225, s80\n\tds_write_b32 v224, v225 offset:24\n\tv_mov_b32 v225, s81\n\tds_write_b32 v224, v225 offset:28\n\tv_mov_b32 v225, s82\n\tds_write_b32 v224, v225 offset:32\n\tv_mov_b32 v225, s83\n\tds_write_b32 v224, v225 offset:36\n\tv_mov_b32 v225, s84\n\tds_write_b32 v224, v225 offset:40\n\tv_mov_b32 v225, s85\n\tds_write_b32 v224, v225 offset:44\n\tv_mov_b32 v225, s86\n\tds_write_b32 v224, v225 offset:48\n\tv_mov_b32 v225, s87\n\tds_write_b32 v224, v225 offset:52\n\tv_mov_b32 v225, s88\n\tds_write_b32 v224, v225 offset:56\n\tv_mov_b32 v225, s89\n\tds_write_b32 v224, v225 offset:60\n\tv_mov_b32 v225, s90\n\tds_write_b32 v224, v225 offset:64\n\tv_mov_b32 v225, s91\n\tds_write_b32 v224, v225 offset:68\n\t")
      TKS("s_mov_b32 s91, 0\n\t")
      "10:\n\ts_and_b32 s48, s17, 7\n\ts_lshl_b32 s43, s48, 17\n\ts_add_u32 s43, s43, 0x300000\n\ts_and_b32 s48, s17, 3\n\ts_lshl_b32 s48, s48, 15\n\tv_add_u32 v8, s48, v2\n\t"
      "v_add_u32 v7, s24, v1\n\ts_add_u32 s49, s17, 1\n\ts_min_u32 s49, s49, 62\n\t"
      "20:\n\tds_read_b32 v5, v4 offset:4\n\ts_waitcnt lgkmcnt(0)\n\tv_readfirstlane_b32 s48, v5\n\ts_cmp_gt_i32 s48, s17\n\ts_cbranch_scc1 21f\n\ts_cmp_eq_u32 s18, 0\n\t"
      "s_cbranch_scc1 21f\n\ts_sub_u32 s18, s18, 1\n\ts_sleep 1\n\ts_branch 20b\n\t"
      "21:\n\t"
      "22:\n\tds_read_b32 v5, v4 offset:0\n\ts_waitcnt lgkmcnt(0)\n\tv_readfirstlane_b32 s48, v5\n\ts_cmp_gt_i32 s48, s17\n\ts_cbranch_scc1 23f\n\ts_cmp_eq_u32 s18, 0\n\t"
      "s_cbranch_scc1 23f\n\ts_sub_u32 s18, s18, 1\n\ts_sleep 1\n\ts_branch 22b\n\t"
      "23:\n\t"
      "24:\n\tds_read_b32 v5, v4 offset:16\n\ts_waitcnt lgkmcnt(0)\n\tv_readfirstlane_b32 s48, v5\n\ts_cmp_gt_i32 s48, s49\n\ts_cbranch_scc1 25f\n\ts_cmp_eq_u32 s18, 0\n\t"
      "s_cbranch_scc1 25f\n\ts_sub_u32 s18, s18, 1\n\ts_sleep 1\n\ts_branch 24b\n\t"
      "25:\n\t"
      TK0
      "s_cmp_gt_u32 s17, 60\n\ts_cbranch_scc1 50f\n\ts_sleep 27\n\t"
      "50:\n\t"
      TK1
      "s_add_u32 s26, s43, 98304\n\tbuffer_load_dwordx4 v[64:67], v0, s[8:11], s26 offen offset:0 sc1\n\ts_nop 2\n\t"
      "buffer_load_dwordx4 v[68:71], v0, s[8:11], s26 offen offset:1024 sc1\n\ts_nop 2\n\tbuffer_load_dwordx4 v[72:75], v0, s[8:11], s26 offen offset:2048 sc1\n\ts_nop 2\n\t"
      "buffer_load_dwordx4 v[76:79], v0, s[8:11], s26 offen offset:3072 sc1\n\ts_nop 2\n\ts_add_u32 s26, s43, 102400\n\t"
      "buffer_load_dwordx4 v[80:83], v0, s[8:11], s26 offen offset:0 sc1\n\ts_nop 2\n\tbuffer_load_dwordx4 v[84:87], v0, s[8:11], s26 offen offset:1024 sc1\n\ts_nop 2\n\t"
      "buffer_load_dwordx4 v[88:91], v0, s[8:11], s26 offen offset:2048 sc1\n\ts_nop 2\n\tbuffer_load_dwordx4 v[92:95], v0, s[8:11], s26 offen offset:3072 sc1\n\ts_nop 2\n\t"
      "s_add_u32 s26, s43, 0\n\tbuffer_load_dwordx4 v[96:99], v0, s[8:11], s26 offen offset:0 sc1\n\ts_nop 2\n\t"
      "buffer_load_dwordx4 v[100:103], v0, s[8:11], s26 offen offset:1024 sc1\n\ts_nop 2\n\tbuffer_load_dwordx4 v[104:107], v0, s[8:11], s26 offen offset:2048 sc1\n\t"
      "s_nop 2\n\tbuffer_load_dwordx4 v[108:111], v0, s[8:11], s26 offen offset:3072 sc1\n\ts_nop 2\n\ts_add_u32 s26, s43, 4096\n\t"
      "buffer_load_dwordx4 v[112:115], v0, s[8:11], s26 offen offset:0 sc1\n\ts_nop 2\n\tbuffer_load_dwordx4 v[116:119], v0, s[8:11], s26 offen offset:1024 sc1\n\ts_nop 2\n\t"
      "buffer_load_dwordx4 v[120:123], v0, s[8:11], s26 offen offset:2048 sc1\n\ts_nop 2\n\tbuffer_load_dwordx4 v[124:127], v0, s[8:11], s26 offen offset:3072 sc1\n\t"
      "s_nop 2\n\ts_add_u32 s26, s43, 16384\n\tbuffer_load_dwordx4 v[128:131], v0, s[8:11], s26 offen offset:0 sc1\n\ts_nop 2\n\t"
      "buffer_load_dwordx4 v[132:135], v0, s[8:11], s26 offen offset:1024 sc1\n\ts_nop 2\n\tbuffer_load_dwordx4 v[136:139], v0, s[8:11], s26 offen offset:2048 sc1\n\t"
      "s_nop 2\n\tbuffer_load_dwordx4 v[140:143], v0, s[8:11], s26 offen offset:3072 sc1\n\ts_nop 2\n\t"
      TK2
      "ds_read_b128 v[32:35], v7 offset:0\n\tds_read_b128 v[36:39], v7 offset:1024\n\tds_read_b128 v[40:43], v7 offset:2048\n\ts_add_u32 s26, s43, 20480\n\t"
      "buffer_load_dwordx4 v[144:147], v0, s[8:11], s26 offen offset:0 sc1\n\tds_read_b128 v[44:47], v7 offset:3072\n\ts_waitcnt vmcnt(20) lgkmcnt(3)\n\t"
      TK3
      "v_mfma_f32_16x16x32_bf16 v[48:51], v[32:35], v[64:67], 0\n\tbuffer_load_dwordx4 v[148:151], v0, s[8:11], s26 offen offset:1024 sc1\n\t"
      "ds_read_b128 v[32:35], v7 offset:4096\n\ts_waitcnt vmcnt(20) lgkmcnt(3)\n\tv_mfma_f32_16x16x32_bf16 v[52:55], v[36:39], v[68:71], 0\n\t"
      "buffer_load_dwordx4 v[152:155], v0, s[8:11], s26 offen offset:2048 sc1\n\tds_read_b128 v[36:39], v7 offset:5120\n\ts_waitcnt vmcnt(20) lgkmcnt(3)\n\t"
      "v_mfma_f32_16x16x32_bf16 v[48:51], v[40:43], v[72:75], v[48:51]\n\tbuffer_load_dwordx4 v[156:159], v0, s[8:11], s26 offen offset:3072 sc1\n\t"
      "ds_read_b128 v[40:43], v7 offset:6144\n\ts_waitcnt vmcnt(20) lgkmcnt(3)\n\t"
      TK4
      "v_mfma_f32_16x16x32_bf16 v[52:55], v[44:47], v[76:79], v[52:55]\n\tds_read_b128 v[44:47], v7 offset:7168\n\ts_waitcnt vmcnt(19) lgkmcnt(3)\n\t"
      "v_mfma_f32_16x16x32_bf16 v[48:51], v[32:35], v[80:83], v[48:51]\n\tds_read2st64_b64 v[32:35], v8 offset0:0 offset1:1\n\ts_waitcnt vmcnt(18) lgkmcnt(3)\n\t"
      "v_mfma_f32_16x16x32_bf16 v[52:55], v[36:39], v[84:87], v[52:55]\n\tds_read2st64_b64 v[36:39], v8 offset0:4 offset1:5\n\ts_waitcnt vmcnt(17) lgkmcnt(3)\n\t"
      "v_mfma_f32_16x16x32_bf16 v[48:51], v[40:43], v[88:91], v[48:51]\n\tds_read2st64_b64 v[40:43], v8 offset0:8 offset1:9\n\ts_waitcnt vmcnt(16) lgkmcnt(3)\n\t"
      "v_mfma_f32_16x16x32_bf16 v[52:55], v[44:47], v[92:95], v[52:55]\n\tds_read2st64_b64 v[44:47], v8 offset0:12 offset1:13\n\ts_waitcnt vmcnt(15) lgkmcnt(3)\n\t"
      "v_mfma_f32_16x16x32_bf16 v[48:51], v[32:35], v[96:99], v[48:51]\n\tds_read2st64_b64 v[32:35], v8 offset0:16 offset1:17\n\ts_waitcnt vmcnt(14) lgkmcnt(3)\n\t"
      "v_mfma_f32_16x16x32_bf16 v[52:55], v[36:39], v[100:103], v[52:55]\n\tds_read2st64_b64 v[36:39], v8 offset0:20 offset1:21\n\ts_waitcnt vmcnt(13) lgkmcnt(3)\n\t"
      "v_mfma_f32_16x16x32_bf16 v[48:51], v[40:43], v[104:107], v[48:51]\n\tds_read2st64_b64 v[40:43], v8 offset0:24 offset1:25\n\ts_waitcnt vmcnt(12) lgkmcnt(3)\n\t"
      "v_mfma_f32_16x16x32_bf16 v[52:55], v[44:47], v[108:111], v[52:55]\n\tds_read2st64_b64 v[44:47], v8 offset0:28 offset1:29\n\ts_waitcnt vmcnt(11) lgkmcnt(3)\n\t"
      "v_mfma_f32_16x16x32_bf16 v[48:51], v[32:35], v[112:115], v[48:51]\n\tds_read2st64_b64 v[32:35], v8 offset0:32 offset1:33\n\ts_waitcnt vmcnt(10) lgkmcnt(3)\n\t"
      "v_mfma_f32_16x16x32_bf16 v[52:55], v[36:39], v[116:119], v[52:55]\n\tds_read2st64_b64 v[36:39], v8 offset0:36 offset1:37\n\ts_waitcnt vmcnt(9) lgkmcnt(3)\n\t"
      "v_mfma_f32_16x16x32_bf16 v[48:51], v[40:43], v[120:123], v[48:51]\n\tds_read2st64_b64 v[40:43], v8 offset0:40 offset1:41\n\ts_waitcnt vmcnt(8) lgkmcnt(3)\n\t"
      "v_mfma_f32_16x16x32_bf16 v[52:55], v[44:47], v[124:127], v[52:55]\n\tds_read2st64_b64 v[44:47], v8 offset0:44 offset1:45\n\ts_waitcnt vmcnt(7) lgkmcnt(3)\n\t"
      "v_mfma_f32_16x16x32_bf16 v[48:51], v[32:35], v[128:131], v[48:51]\n\tds_read2st64_b64 v[32:35], v8 offset0:48 offset1:49\n\ts_waitcnt vmcnt(6) lgkmcnt(3)\n\t"
      "v_mfma_f32_16x16x32_bf16 v[52:55], v[36:39], v[132:135], v[52:55]\n\tds_read2st64_b64 v[36:39], v8 offset0:52 offset1:53\n\ts_waitcnt vmcnt(5) lgkmcnt(3)\n\t"
      "v_mfma_f32_16x16x32_bf16 v[48:51], v[40:43], v[136:139], v[48:51]\n\tds_read2st64_b64 v[40:43], v8 offset0:56 offset1:57\n\ts_waitcnt vmcnt(4) lgkmcnt(3)\n\t"
      "v_mfma_f32_16x16x32_bf16 v[52:55], v[44:47], v[140:143], v[52:55]\n\tds_read2st64_b64 v[44:47], v8 offset0:60 offset1:61\n\ts_waitcnt vmcnt(3) lgkmcnt(3)\n\t"
      "v_mfma_f32_16x16x32_bf16 v[48:51], v[32:35], v[144:147], v[48:51]\n\ts_waitcnt vmcnt(2) lgkmcnt(2)\n\t"
      "v_mfma_f32_16x16x32_bf16 v[52:55], v[36:39], v[148:151], v[52:55]\n\ts_waitcnt vmcnt(1) lgkmcnt(1)\n\t"
      "v_mfma_f32_16x16x32_bf16 v[48:51], v[40:43], v[152:155], v[48:51]\n\ts_waitcnt vmcnt(0) lgkmcnt(0)\n\t"
      TK5
      "v_mfma_f32_16x16x32_bf16 v[52:55], v[44:47], v[156:159], v[52:55]\n\ts_add_u32 s49, s17, 1\n\tv_mov_b32 v6, s49\n\ts_nop 5\n\t"
      "v_pk_add_f32 v[48:49], v[48:49], v[52:53]\n\tv_pk_add_f32 v[50:51], v[50:51], v[54:55]\n\tv_cmp_u_f32 vcc, v48, v49\n\tv_cvt_pk_bf16_f32 v56, v48, v49\n\t"
      "v_cvt_pk_bf16_f32 v57, v50, v51\n\ts_cbranch_vccnz 60f\n\t"
      "51:\n\tds_write_b32 v4, v6 offset:8\n\tbuffer_store_dwordx2 v[56:57], v3, s[20:23], s25 offen \n\t"
      TK6
      TK7
      STS("s_cmp_lt_i32 s17, 8\n\ts_cselect_b32 s50, 0, s50\n\ts_cselect_b32 s51, 0, s51\n\ts_cselect_b32 s61, 0, s61\n\t")
      TKS("s_waitcnt lgkmcnt(0)\n\ts_cmp_lt_i32 s17, 0\n\ts_cbranch_scc1 98f\n\ts_cmp_lg_u32 s16, s91\n\ts_cbranch_scc1 98f\n\ts_mov_b64 exec, 1\n\ts_lshl_b32 s48, s17, 4\n\ts_add_u32 s48, s48, 161056\n\tv_mov_b32 v5, s48\n\ts_sub_u32 s48, s74, s90\n\ts_min_u32 s48, s48, 0xffff\n\tv_mov_b32 v6, s48\n\tds_write_b16 v5, v6 offset:0\n\ts_sub_u32 s48, s76, s74\n\ts_min_u32 s48, s48, 0xffff\n\tv_mov_b32 v6, s48\n\tds_write_b16 v5, v6 offset:2\n\ts_sub_u32 s48, s78, s76\n\ts_min_u32 s48, s48, 0xffff\n\tv_mov_b32 v6, s48\n\tds_write_b16 v5, v6 offset:4\n\ts_sub_u32 s48, s80, s78\n\ts_min_u32 s48, s48, 0xffff\n\tv_mov_b32 v6, s48\n\tds_write_b16 v5, v6 offset:6\n\ts_sub_u32 s48, s82, s80\n\ts_min_u32 s48, s48, 0xffff\n\tv_mov_b32 v6, s48\n\tds_write_b16 v5, v6 offset:8\n\ts_sub_u32 s48, s84, s82\n\ts_min_u32 s48, s48, 0xffff\n\tv_mov_b32 v6, s48\n\tds_write_b16 v5, v6 offset:10\n\ts_sub_u32 s48, s86, s84\n\ts_min_u32 s48, s48, 0xffff\n\tv_mov_b32 v6, s48\n\tds_write_b16 v5, v6 offset:12\n\ts_sub_u32 s48, s88, s86\n\ts_min_u32 s48, s48, 0xffff\n\tv_mov_b32 v6, s48\n\tds_write_b16 v5, v6 offset:14\n\ts_mov_b64 exec, -1\n\t98:\n\ts_mov_b32 s90, s88\n\t")
      "s_add_u32 s25, s25, 0x20000\n\ts_add_u32 s24, s24, 0x2000\n\ts_cmp_eq_u32 s24, 0x6000\n\ts_cselect_b32 s24, 0, s24\n\ts_add_u32 s17, s17, 1\n\ts_cmp_lt_u32 s17, 64\n\t"
      "s_cbranch_scc1 10b\n\ts_branch 40f\n\t"
      "60:\n\tv_max3_u32 v5, v64, v65, v66\n\tv_max_u32 v5, v5, v67\n\tv_max3_u32 v5, v5, v68, v69\n\tv_max3_u32 v5, v5, v70, v71\n\tv_max3_u32 v5, v5, v72, v73\n\t"
      "v_max3_u32 v5, v5, v74, v75\n\tv_max3_u32 v5, v5, v76, v77\n\tv_max3_u32 v5, v5, v78, v79\n\tv_max3_u32 v5, v5, v80, v81\n\tv_max3_u32 v5, v5, v82, v83\n\t"
      "v_max3_u32 v5, v5, v84, v85\n\tv_max3_u32 v5, v5, v86, v87\n\tv_max3_u32 v5, v5, v88, v89\n\tv_max3_u32 v5, v5, v90, v91\n\tv_max3_u32 v5, v5, v92, v93\n\t"
      "v_max3_u32 v5, v5, v94, v95\n\tv_max3_u32 v5, v5, v96, v97\n\tv_max3_u32 v5, v5, v98, v99\n\tv_max3_u32 v5, v5, v100, v101\n\tv_max3_u32 v5, v5, v102, v103\n\t"
      "v_max3_u32 v5, v5, v104, v105\n\tv_max3_u32 v5, v5, v106, v107\n\tv_max3_u32 v5, v5, v108, v109\n\tv_max3_u32 v5, v5, v110, v111\n\tv_max3_u32 v5, v5, v112, v113\n\t"
      "v_max3_u32 v5, v5, v114, v115\n\tv_max3_u32 v5, v5, v116, v117\n\tv_max3_u32 v5, v5, v118, v119\n\tv_max3_u32 v5, v5, v120, v121\n\tv_max3_u32 v5, v5, v122, v123\n\t"
      "v_max3_u32 v5, v5, v124, v125\n\tv_max3_u32 v5, v5, v126, v127\n\tv_max3_u32 v5, v5, v128, v129\n\tv_max3_u32 v5, v5, v130, v131\n\tv_max3_u32 v5, v5, v132, v133\n\t"
      "v_max3_u32 v5, v5, v134, v135\n\tv_max3_u32 v5, v5, v136, v137\n\tv_max3_u32 v5, v5, v138, v139\n\tv_max3_u32 v5, v5, v140, v141\n\tv_max3_u32 v5, v5, v142, v143\n\t"
      "v_max3_u32 v5, v5, v144, v145\n\tv_max3_u32 v5, v5, v146, v147\n\tv_max3_u32 v5, v5, v148, v149\n\tv_max3_u32 v5, v5, v150, v151\n\tv_max3_u32 v5, v5, v152, v153\n\t"
      "v_max3_u32 v5, v5, v154, v155\n\tv_max3_u32 v5, v5, v156, v157\n\tv_max3_u32 v5, v5, v158, v159\n\tv_cmp_eq_u32 vcc, -1, v5\n\ts_cbranch_vccz 51b\n\t"
      "s_cmp_eq_u32 s18, 0\n\ts_cbranch_scc1 51b\n\ts_sub_u32 s18, s18, 1\n\ts_branch 50b\n\t"
      "40:\n\t"
      ".Lmk_end0:\n\ts_waitcnt vmcnt(0) lgkmcnt(0)\n\t"
      TKS("s_mul_i32 s48, s16, 72\n\ts_add_u32 s48, s48, 162080\n\tv_mov_b32 v224, s48\n\tds_read_b32 v225, v224 offset:0\n\ts_waitcnt lgkmcnt(0)\n\tv_readfirstlane_b32 s74, v225\n\tds_read_b32 v225, v224 offset:4\n\ts_waitcnt lgkmcnt(0)\n\tv_readfirstlane_b32 s75, v225\n\tds_read_b32 v225, v224 offset:8\n\ts_waitcnt lgkmcnt(0)\n\tv_readfirstlane_b32 s76, v225\n\tds_read_b32 v225, v224 offset:12\n\ts_waitcnt lgkmcnt(0)\n\tv_readfirstlane_b32 s77, v225\n\tds_read_b32 v225, v224 offset:16\n\ts_waitcnt lgkmcnt(0)\n\tv_readfirstlane_b32 s78, v225\n\tds_read_b32 v225, v224 offset:20\n\ts_waitcnt lgkmcnt(0)\n\tv_readfirstlane_b32 s79, v225\n\tds_read_b32 v225, v224 offset:24\n\ts_waitcnt lgkmcnt(0)\n\tv_readfirstlane_b32 s80, v225\n\tds_read_b32 v225, v224 offset:28\n\ts_waitcnt lgkmcnt(0)\n\tv_readfirstlane_b32 s81, v225\n\tds_read_b32 v225, v224 offset:32\n\ts_waitcnt lgkmcnt(0)\n\tv_readfirstlane_b32 s82, v225\n\tds_read_b32 v225, v224 offset:36\n\ts_waitcnt lgkmcnt(0)\n\tv_readfirstlane_b32 s83, v225\n\tds_read_b32 v225, v224 offset:40\n\ts_waitcnt lgkmcnt(0)\n\tv_readfirstlane_b32 s84, v225\n\tds_read_b32 v225, v224 offset:44\n\ts_waitcnt lgkmcnt(0)\n\tv_readfirstlane_b32 s85, v225\n\tds_read_b32 v225, v224 offset:48\n\ts_waitcnt lgkmcnt(0)\n\tv_readfirstlane_b32 s86, v225\n\tds_read_b32 v225, v224 offset:52\n\ts_waitcnt lgkmcnt(0)\n\tv_readfirstlane_b32 s87, v225\n\tds_read_b32 v225, v224 offset:56\n\ts_waitcnt lgkmcnt(0)\n\tv_readfirstlane_b32 s88, v225\n\tds_read_b32 v225, v224 offset:60\n\ts_waitcnt lgkmcnt(0)\n\tv_readfirstlane_b32 s89, v225\n\tds_read_b32 v225, v224 offset:64\n\ts_waitcnt lgkmcnt(0)\n\tv_readfirstlane_b32 s90, v225\n\tds_read_b32 v225, v224 offset:68\n\ts_waitcnt lgkmcnt(0)\n\tv_readfirstlane_b32 s91, v225\n\t")
      STS("s_lshl_b32 s48, s16, 2\n\ts_add_u32 s48, s48, 162944\n\tv_mov_b32 v0, s48\n\tv_mov_b32 v1, s50\n\tds_write_b32 v0, v1\n\ts_cmp_lg_u32 s16, 0\n\ts_cbranch_scc1 97f\n\tv_mov_b32 v1, s51\n\tds_write_b32 v0, v1 offset:16\n\t97:\n\ts_cmp_lg_u32 s16, -1\n\ts_cbranch_scc1 95f\n\tv_mov_b32 v1, s61\n\tds_write_b32 v0, v1 offset:24\n\tv_mov_b32 v1, s21\n\tds_write_b32 v0, v1 offset:28\n\tv_mov_b32 v1, s22\n\tds_write_b32 v0, v1 offset:32\n\t95:\n\t")
      "s_cmp_lg_u32 s18, 0\n\ts_cbranch_scc1 96f\n\tv_mov_b32 v1, 1\n\tbuffer_store_dword v1, off, s[12:15], 0 offset:3328 sc1\n\t"
      "96:\n\t"
      : TOK_REGS :: "memory", "vcc", "scc", S_CLOB TK_CLOB);
    else asm volatile(
      "s_nop 0\n\tv_readfirstlane_b32 s8, v160\n\tv_readfirstlane_b32 s9, v161\n\tv_readfirstlane_b32 s10, v162\n\tv_readfirstlane_b32 s11, v163\n\t"
      "v_readfirstlane_b32 s12, v164\n\tv_readfirstlane_b32 s13, v165\n\tv_readfirstlane_b32 s14, v166\n\tv_readfirstlane_b32 s15, v167\n\tv_readfirstlane_b32 s16, v168\n\t"
      "v_readfirstlane_b32 s17, v169\n\tv_readfirstlane_b32 s18, v170\n\tv_readfirstlane_b32 s19, v171\n\tv_readfirstlane_b32 s20, v172\n\tv_readfirstlane_b32 s21, v173\n\t"
      "v_readfirstlane_b32 s22, v174\n\tv_readfirstlane_b32 s23, v175\n\tv_readfirstlane_b32 s24, v176\n\tv_readfirstlane_b32 s25, v177\n\ts_nop 4\n\t"
      TKS("s_mul_i32 s48, s16, 72\n\ts_add_u32 s48, s48, 162080\n\tv_mov_b32 v224, s48\n\tv_mov_b32 v225, s74\n\tds_write_b32 v224, v225 offset:0\n\tv_mov_b32 v225, s75\n\tds_write_b32 v224, v225 offset:4\n\tv_mov_b32 v225, s76\n\tds_write_b32 v224, v225 offset:8\n\tv_mov_b32 v225, s77\n\tds_write_b32 v224, v225 offset:12\n\tv_mov_b32 v225, s78\n\tds_write_b32 v224, v225 offset:16\n\tv_mov_b32 v225, s79\n\tds_write_b32 v224, v225 offset:20\n\tv_mov_b32 v225, s80\n\tds_write_b32 v224, v225 offset:24\n\tv_mov_b32 v225, s81\n\tds_write_b32 v224, v225 offset:28\n\tv_mov_b32 v225, s82\n\tds_write_b32 v224, v225 offset:32\n\tv_mov_b32 v225, s83\n\tds_write_b32 v224, v225 offset:36\n\tv_mov_b32 v225, s84\n\tds_write_b32 v224, v225 offset:40\n\tv_mov_b32 v225, s85\n\tds_write_b32 v224, v225 offset:44\n\tv_mov_b32 v225, s86\n\tds_write_b32 v224, v225 offset:48\n\tv_mov_b32 v225, s87\n\tds_write_b32 v224, v225 offset:52\n\tv_mov_b32 v225, s88\n\tds_write_b32 v224, v225 offset:56\n\tv_mov_b32 v225, s89\n\tds_write_b32 v224, v225 offset:60\n\tv_mov_b32 v225, s90\n\tds_write_b32 v224, v225 offset:64\n\tv_mov_b32 v225, s91\n\tds_write_b32 v224, v225 offset:68\n\t")
      TKS("s_mov_b32 s91, 0\n\t")
      "10:\n\ts_and_b32 s48, s17, 7\n\ts_lshl_b32 s43, s48, 17\n\ts_add_u32 s43, s43, 0x300000\n\ts_and_b32 s48, s17, 3\n\ts_lshl_b32 s48, s48, 15\n\tv_add_u32 v8, s48, v2\n\t"
      "v_add_u32 v7, s24, v1\n\ts_add_u32 s49, s17, 1\n\ts_min_u32 s49, s49, 62\n\t"
      "20:\n\tds_read_b32 v5, v4 offset:4\n\ts_waitcnt lgkmcnt(0)\n\tv_readfirstlane_b32 s48, v5\n\ts_cmp_gt_i32 s48, s17\n\ts_cbranch_scc1 21f\n\ts_cmp_eq_u32 s18, 0\n\t"
      "s_cbranch_scc1 21f\n\ts_sub_u32 s18, s18, 1\n\ts_sleep 1\n\ts_branch 20b\n\t"
      "21:\n\t"
      "22:\n\tds_read_b32 v5, v4 offset:0\n\ts_waitcnt lgkmcnt(0)\n\tv_readfirstlane_b32 s48, v5\n\ts_cmp_gt_i32 s48, s17\n\ts_cbranch_scc1 23f\n\ts_cmp_eq_u32 s18, 0\n\t"
      "s_cbranch_scc1 23f\n\ts_sub_u32 s18, s18, 1\n\ts_sleep 1\n\ts_branch 22b\n\t"
      "23:\n\t"
      "24:\n\tds_read_b32 v5, v4 offset:16\n\ts_waitcnt lgkmcnt(0)\n\tv_readfirstlane_b32 s48, v5\n\ts_cmp_gt_i32 s48, s49\n\ts_cbranch_scc1 25f\n\ts_cmp_eq_u32 s18, 0\n\t"
      "s_cbranch_scc1 25f\n\ts_sub_u32 s18, s18, 1\n\ts_sleep 1\n\ts_branch 24b\n\t"
      "25:\n\t"
      TK0
      "50:\n\t"
      TK1
      "s_add_u32 s26, s43, 114688\n\tbuffer_load_dwordx4 v[64:67], v0, s[8:11], s26 offen offset:0 sc1\n\ts_nop 4\n\t"
      "buffer_load_dwordx4 v[68:71], v0, s[8:11], s26 offen offset:1024 sc1\n\ts_nop 4\n\tbuffer_load_dwordx4 v[72:75], v0, s[8:11], s26 offen offset:2048 sc1\n\ts_nop 4\n\t"
      "buffer_load_dwordx4 v[76:79], v0, s[8:11], s26 offen offset:3072 sc1\n\ts_nop 4\n\ts_add_u32 s26, s43, 118784\n\t"
      "buffer_load_dwordx4 v[80:83], v0, s[8:11], s26 offen offset:0 sc1\n\ts_nop 4\n\tbuffer_load_dwordx4 v[84:87], v0, s[8:11], s26 offen offset:1024 sc1\n\ts_nop 4\n\t"
      "buffer_load_dwordx4 v[88:91], v0, s[8:11], s26 offen offset:2048 sc1\n\ts_nop 4\n\tbuffer_load_dwordx4 v[92:95], v0, s[8:11], s26 offen offset:3072 sc1\n\ts_nop 4\n\t"
      "s_add_u32 s26, s43, 32768\n\tbuffer_load_dwordx4 v[96:99], v0, s[8:11], s26 offen offset:0 sc1\n\ts_nop 4\n\t"
      "buffer_load_dwordx4 v[100:103], v0, s[8:11], s26 offen offset:1024 sc1\n\ts_nop 4\n\tbuffer_load_dwordx4 v[104:107], v0, s[8:11], s26 offen offset:2048 sc1\n\t"
      "s_nop 4\n\tbuffer_load_dwordx4 v[108:111], v0, s[8:11], s26 offen offset:3072 sc1\n\ts_nop 4\n\ts_add_u32 s26, s43, 36864\n\t"
      "buffer_load_dwordx4 v[112:115], v0, s[8:11], s26 offen offset:0 sc1\n\ts_nop 4\n\tbuffer_load_dwordx4 v[116:119], v0, s[8:11], s26 offen offset:1024 sc1\n\ts_nop 4\n\t"
      "buffer_load_dwordx4 v[120:123], v0, s[8:11], s26 offen offset:2048 sc1\n\ts_nop 4\n\tbuffer_load_dwordx4 v[124:127], v0, s[8:11], s26 offen offset:3072 sc1\n\t"
      "s_nop 4\n\ts_add_u32 s26, s43, 49152\n\tbuffer_load_dwordx4 v[128:131], v0, s[8:11], s26 offen offset:0 sc1\n\ts_nop 4\n\t"
      "buffer_load_dwordx4 v[132:135], v0, s[8:11], s26 offen offset:1024 sc1\n\ts_nop 4\n\tbuffer_load_dwordx4 v[136:139], v0, s[8:11], s26 offen offset:2048 sc1\n\t"
      "s_nop 4\n\tbuffer_load_dwordx4 v[140:143], v0, s[8:11], s26 offen offset:3072 sc1\n\ts_nop 4\n\t"
      TK2
      "ds_read_b128 v[32:35], v7 offset:0\n\tds_read_b128 v[36:39], v7 offset:1024\n\tds_read_b128 v[40:43], v7 offset:2048\n\ts_add_u32 s26, s43, 53248\n\t"
      "buffer_load_dwordx4 v[144:147], v0, s[8:11], s26 offen offset:0 sc1\n\tds_read_b128 v[44:47], v7 offset:3072\n\ts_waitcnt vmcnt(20) lgkmcnt(3)\n\t"
      TK3
      "v_mfma_f32_16x16x32_bf16 v[48:51], v[32:35], v[64:67], 0\n\tbuffer_load_dwordx4 v[148:151], v0, s[8:11], s26 offen offset:1024 sc1\n\t"
      "ds_read_b128 v[32:35], v7 offset:4096\n\ts_waitcnt vmcnt(20) lgkmcnt(3)\n\tv_mfma_f32_16x16x32_bf16 v[52:55], v[36:39], v[68:71], 0\n\t"
      "buffer_load_dwordx4 v[152:155], v0, s[8:11], s26 offen offset:2048 sc1\n\tds_read_b128 v[36:39], v7 offset:5120\n\ts_waitcnt vmcnt(20) lgkmcnt(3)\n\t"
      "v_mfma_f32_16x16x32_bf16 v[48:51], v[40:43], v[72:75], v[48:51]\n\tbuffer_load_dwordx4 v[156:159], v0, s[8:11], s26 offen offset:3072 sc1\n\t"
      "ds_read_b128 v[40:43], v7 offset:6144\n\ts_waitcnt vmcnt(20) lgkmcnt(3)\n\tv_mfma_f32_16x16x32_bf16 v[52:55], v[44:47], v[76:79], v[52:55]\n\t"
      "s_add_u32 s26, s43, 65536\n\tbuffer_load_dwordx4 v[160:163], v0, s[8:11], s26 offen offset:0 sc1\n\tds_read_b128 v[44:47], v7 offset:7168\n\t"
      "s_waitcnt vmcnt(20) lgkmcnt(3)\n\tv_mfma_f32_16x16x32_bf16 v[48:51], v[32:35], v[80:83], v[48:51]\n\t"
      "buffer_load_dwordx4 v[164:167], v0, s[8:11], s26 offen offset:1024 sc1\n\tds_read_b128 v[32:35], v8 offset:0\n\ts_waitcnt vmcnt(20) lgkmcnt(3)\n\t"
      "v_mfma_f32_16x16x32_bf16 v[52:55], v[36:39], v[84:87], v[52:55]\n\tbuffer_load_dwordx4 v[168:171], v0, s[8:11], s26 offen offset:2048 sc1\n\t"
      "ds_read_b128 v[36:39], v8 offset:1024\n\ts_waitcnt vmcnt(20) lgkmcnt(3)\n\tv_mfma_f32_16x16x32_bf16 v[48:51], v[40:43], v[88:91], v[48:51]\n\t"
      "buffer_load_dwordx4 v[172:175], v0, s[8:11], s26 offen offset:3072 sc1\n\tds_read_b128 v[40:43], v8 offset:2048\n\ts_waitcnt vmcnt(20) lgkmcnt(3)\n\t"
      "v_mfma_f32_16x16x32_bf16 v[52:55], v[44:47], v[92:95], v[52:55]\n\ts_add_u32 s26, s43, 69632\n\tbuffer_load_dwordx4 v[176:179], v0, s[8:11], s26 offen offset:0 sc1\n\t"
      "ds_read_b128 v[44:47], v8 offset:3072\n\ts_waitcnt vmcnt(20) lgkmcnt(3)\n\tv_mfma_f32_16x16x32_bf16 v[48:51], v[32:35], v[96:99], v[48:51]\n\t"
      "buffer_load_dwordx4 v[180:183], v0, s[8:11], s26 offen offset:1024 sc1\n\tds_read_b128 v[32:35], v8 offset:4096\n\ts_waitcnt vmcnt(20) lgkmcnt(3)\n\t"
      "v_mfma_f32_16x16x32_bf16 v[52:55], v[36:39], v[100:103], v[52:55]\n\tbuffer_load_dwordx4 v[184:187], v0, s[8:11], s26 offen offset:2048 sc1\n\t"
      "ds_read_b128 v[36:39], v8 offset:5120\n\ts_waitcnt vmcnt(20) lgkmcnt(3)\n\tv_mfma_f32_16x16x32_bf16 v[48:51], v[40:43], v[104:107], v[48:51]\n\t"
      "buffer_load_dwordx4 v[188:191], v0, s[8:11], s26 offen offset:3072 sc1\n\tds_read_b128 v[40:43], v8 offset:6144\n\ts_waitcnt vmcnt(20) lgkmcnt(3)\n\t"
      "v_mfma_f32_16x16x32_bf16 v[52:55], v[44:47], v[108:111], v[52:55]\n\ts_add_u32 s26, s43, 81920\n\tbuffer_load_dwordx4 v[192:195], v0, s[8:11], s26 offen offset:0 sc1\n\t"
      "ds_read_b128 v[44:47], v8 offset:7168\n\ts_waitcnt vmcnt(20) lgkmcnt(3)\n\tv_mfma_f32_16x16x32_bf16 v[48:51], v[32:35], v[112:115], v[48:51]\n\t"
      "buffer_load_dwordx4 v[196:199], v0, s[8:11], s26 offen offset:1024 sc1\n\tds_read_b128 v[32:35], v8 offset:8192\n\ts_waitcnt vmcnt(20) lgkmcnt(3)\n\t"
      "v_mfma_f32_16x16x32_bf16 v[52:55], v[36:39], v[116:119], v[52:55]\n\tbuffer_load_dwordx4 v[200:203], v0, s[8:11], s26 offen offset:2048 sc1\n\t"
      "ds_read_b128 v[36:39], v8 offset:9216\n\ts_waitcnt vmcnt(20) lgkmcnt(3)\n\tv_mfma_f32_16x16x32_bf16 v[48:51], v[40:43], v[120:123], v[48:51]\n\t"
      "buffer_load_dwordx4 v[204:207], v0, s[8:11], s26 offen offset:3072 sc1\n\tds_read_b128 v[40:43], v8 offset:10240\n\ts_waitcnt vmcnt(20) lgkmcnt(3)\n\t"
      "v_mfma_f32_16x16x32_bf16 v[52:55], v[44:47], v[124:127], v[52:55]\n\ts_add_u32 s26, s43, 86016\n\tbuffer_load_dwordx4 v[208:211], v0, s[8:11], s26 offen offset:0 sc1\n\t"
      "ds_read_b128 v[44:47], v8 offset:11264\n\ts_waitcnt vmcnt(20) lgkmcnt(3)\n\tv_mfma_f32_16x16x32_bf16 v[48:51], v[32:35], v[128:131], v[48:51]\n\t"
      "buffer_load_dwordx4 v[212:215], v0, s[8:11], s26 offen offset:1024 sc1\n\tds_read_b128 v[32:35], v8 offset:12288\n\ts_waitcnt vmcnt(20) lgkmcnt(3)\n\t"
      "v_mfma_f32_16x16x32_bf16 v[52:55], v[36:39], v[132:135], v[52:55]\n\tbuffer_load_dwordx4 v[216:219], v0, s[8:11], s26 offen offset:2048 sc1\n\t"
      "ds_read_b128 v[36:39], v8 offset:13312\n\ts_waitcnt vmcnt(20) lgkmcnt(3)\n\tv_mfma_f32_16x16x32_bf16 v[48:51], v[40:43], v[136:139], v[48:51]\n\t"
      "buffer_load_dwordx4 v[220:223], v0, s[8:11], s26 offen offset:3072 sc1\n\tds_read_b128 v[40:43], v8 offset:14336\n\ts_waitcnt vmcnt(20) lgkmcnt(3)\n\t"
      TK4
      "v_mfma_f32_16x16x32_bf16 v[52:55], v[44:47], v[140:143], v[52:55]\n\tds_read_b128 v[44:47], v8 offset:15360\n\ts_waitcnt vmcnt(19) lgkmcnt(3)\n\t"
      "v_mfma_f32_16x16x32_bf16 v[48:51], v[32:35], v[144:147], v[48:51]\n\tds_read_b128 v[32:35], v8 offset:16384\n\ts_waitcnt vmcnt(18) lgkmcnt(3)\n\t"
      "v_mfma_f32_16x16x32_bf16 v[52:55], v[36:39], v[148:151], v[52:55]\n\tds_read_b128 v[36:39], v8 offset:17408\n\ts_waitcnt vmcnt(17) lgkmcnt(3)\n\t"
      "v_mfma_f32_16x16x32_bf16 v[48:51], v[40:43], v[152:155], v[48:51]\n\tds_read_b128 v[40:43], v8 offset:18432\n\ts_waitcnt vmcnt(16) lgkmcnt(3)\n\t"
      "v_mfma_f32_16x16x32_bf16 v[52:55], v[44:47], v[156:159], v[52:55]\n\tds_read_b128 v[44:47], v8 offset:19456\n\ts_waitcnt vmcnt(15) lgkmcnt(3)\n\t"
      "v_mfma_f32_16x16x32_bf16 v[48:51], v[32:35], v[160:163], v[48:51]\n\tds_read_b128 v[32:35], v8 offset:20480\n\ts_waitcnt vmcnt(14) lgkmcnt(3)\n\t"
      "v_mfma_f32_16x16x32_bf16 v[52:55], v[36:39], v[164:167], v[52:55]\n\tds_read_b128 v[36:39], v8 offset:21504\n\ts_waitcnt vmcnt(13) lgkmcnt(3)\n\t"
      "v_mfma_f32_16x16x32_bf16 v[48:51], v[40:43], v[168:171], v[48:51]\n\tds_read_b128 v[40:43], v8 offset:22528\n\ts_waitcnt vmcnt(12) lgkmcnt(3)\n\t"
      "v_mfma_f32_16x16x32_bf16 v[52:55], v[44:47], v[172:175], v[52:55]\n\tds_read_b128 v[44:47], v8 offset:23552\n\ts_waitcnt vmcnt(11) lgkmcnt(3)\n\t"
      "v_mfma_f32_16x16x32_bf16 v[48:51], v[32:35], v[176:179], v[48:51]\n\tds_read_b128 v[32:35], v8 offset:24576\n\ts_waitcnt vmcnt(10) lgkmcnt(3)\n\t"
      "v_mfma_f32_16x16x32_bf16 v[52:55], v[36:39], v[180:183], v[52:55]\n\tds_read_b128 v[36:39], v8 offset:25600\n\ts_waitcnt vmcnt(9) lgkmcnt(3)\n\t"
      "v_mfma_f32_16x16x32_bf16 v[48:51], v[40:43], v[184:187], v[48:51]\n\tds_read_b128 v[40:43], v8 offset:26624\n\ts_waitcnt vmcnt(8) lgkmcnt(3)\n\t"
      "v_mfma_f32_16x16x32_bf16 v[52:55], v[44:47], v[188:191], v[52:55]\n\tds_read_b128 v[44:47], v8 offset:27648\n\ts_waitcnt vmcnt(7) lgkmcnt(3)\n\t"
      "v_mfma_f32_16x16x32_bf16 v[48:51], v[32:35], v[192:195], v[48:51]\n\tds_read_b128 v[32:35], v8 offset:28672\n\ts_waitcnt vmcnt(6) lgkmcnt(3)\n\t"
      "v_mfma_f32_16x16x32_bf16 v[52:55], v[36:39], v[196:199], v[52:55]\n\tds_read_b128 v[36:39], v8 offset:29696\n\ts_waitcnt vmcnt(5) lgkmcnt(3)\n\t"
      "v_mfma_f32_16x16x32_bf16 v[48:51], v[40:43], v[200:203], v[48:51]\n\tds_read_b128 v[40:43], v8 offset:30720\n\ts_waitcnt vmcnt(4) lgkmcnt(3)\n\t"
      "v_mfma_f32_16x16x32_bf16 v[52:55], v[44:47], v[204:207], v[52:55]\n\tds_read_b128 v[44:47], v8 offset:31744\n\ts_waitcnt vmcnt(3) lgkmcnt(3)\n\t"
      "v_mfma_f32_16x16x32_bf16 v[48:51], v[32:35], v[208:211], v[48:51]\n\ts_waitcnt vmcnt(2) lgkmcnt(2)\n\t"
      "v_mfma_f32_16x16x32_bf16 v[52:55], v[36:39], v[212:215], v[52:55]\n\ts_waitcnt vmcnt(1) lgkmcnt(1)\n\t"
      "v_mfma_f32_16x16x32_bf16 v[48:51], v[40:43], v[216:219], v[48:51]\n\ts_waitcnt vmcnt(0) lgkmcnt(0)\n\t"
      TK5
      "v_mfma_f32_16x16x32_bf16 v[52:55], v[44:47], v[220:223], v[52:55]\n\ts_add_u32 s49, s17, 1\n\tv_mov_b32 v6, s49\n\ts_nop 5\n\t"
      "v_pk_add_f32 v[48:49], v[48:49], v[52:53]\n\tv_pk_add_f32 v[50:51], v[50:51], v[54:55]\n\tv_cmp_u_f32 vcc, v48, v49\n\tv_cvt_pk_bf16_f32 v56, v48, v49\n\t"
      "v_cvt_pk_bf16_f32 v57, v50, v51\n\ts_cbranch_vccnz 60f\n\t"
      "51:\n\tds_write_b32 v4, v6 offset:12\n\tbuffer_store_dwordx2 v[56:57], v3, s[20:23], s25 offen \n\t"
      TK6
      TK7
      STS("s_cmp_lt_i32 s17, 8\n\ts_cselect_b32 s50, 0, s50\n\ts_cselect_b32 s51, 0, s51\n\ts_cselect_b32 s61, 0, s61\n\t")
      TKS("s_waitcnt lgkmcnt(0)\n\ts_cmp_lt_i32 s17, 0\n\ts_cbranch_scc1 98f\n\ts_cmp_lg_u32 s16, s91\n\ts_cbranch_scc1 98f\n\ts_mov_b64 exec, 1\n\ts_lshl_b32 s48, s17, 4\n\ts_add_u32 s48, s48, 161056\n\tv_mov_b32 v5, s48\n\ts_sub_u32 s48, s74, s90\n\ts_min_u32 s48, s48, 0xffff\n\tv_mov_b32 v6, s48\n\tds_write_b16 v5, v6 offset:0\n\ts_sub_u32 s48, s76, s74\n\ts_min_u32 s48, s48, 0xffff\n\tv_mov_b32 v6, s48\n\tds_write_b16 v5, v6 offset:2\n\ts_sub_u32 s48, s78, s76\n\ts_min_u32 s48, s48, 0xffff\n\tv_mov_b32 v6, s48\n\tds_write_b16 v5, v6 offset:4\n\ts_sub_u32 s48, s80, s78\n\ts_min_u32 s48, s48, 0xffff\n\tv_mov_b32 v6, s48\n\tds_write_b16 v5, v6 offset:6\n\ts_sub_u32 s48, s82, s80\n\ts_min_u32 s48, s48, 0xffff\n\tv_mov_b32 v6, s48\n\tds_write_b16 v5, v6 offset:8\n\ts_sub_u32 s48, s84, s82\n\ts_min_u32 s48, s48, 0xffff\n\tv_mov_b32 v6, s48\n\tds_write_b16 v5, v6 offset:10\n\ts_sub_u32 s48, s86, s84\n\ts_min_u32 s48, s48, 0xffff\n\tv_mov_b32 v6, s48\n\tds_write_b16 v5, v6 offset:12\n\ts_sub_u32 s48, s88, s86\n\ts_min_u32 s48, s48, 0xffff\n\tv_mov_b32 v6, s48\n\tds_write_b16 v5, v6 offset:14\n\ts_mov_b64 exec, -1\n\t98:\n\ts_mov_b32 s90, s88\n\t")
      "s_add_u32 s25, s25, 0x20000\n\ts_add_u32 s24, s24, 0x2000\n\ts_cmp_eq_u32 s24, 0x6000\n\ts_cselect_b32 s24, 0, s24\n\ts_add_u32 s17, s17, 1\n\ts_cmp_lt_u32 s17, 64\n\t"
      "s_cbranch_scc1 10b\n\ts_branch 40f\n\t"
      "60:\n\tv_max3_u32 v5, v64, v65, v66\n\tv_max_u32 v5, v5, v67\n\tv_max3_u32 v5, v5, v68, v69\n\tv_max3_u32 v5, v5, v70, v71\n\tv_max3_u32 v5, v5, v72, v73\n\t"
      "v_max3_u32 v5, v5, v74, v75\n\tv_max3_u32 v5, v5, v76, v77\n\tv_max3_u32 v5, v5, v78, v79\n\tv_max3_u32 v5, v5, v80, v81\n\tv_max3_u32 v5, v5, v82, v83\n\t"
      "v_max3_u32 v5, v5, v84, v85\n\tv_max3_u32 v5, v5, v86, v87\n\tv_max3_u32 v5, v5, v88, v89\n\tv_max3_u32 v5, v5, v90, v91\n\tv_max3_u32 v5, v5, v92, v93\n\t"
      "v_max3_u32 v5, v5, v94, v95\n\tv_max3_u32 v5, v5, v96, v97\n\tv_max3_u32 v5, v5, v98, v99\n\tv_max3_u32 v5, v5, v100, v101\n\tv_max3_u32 v5, v5, v102, v103\n\t"
      "v_max3_u32 v5, v5, v104, v105\n\tv_max3_u32 v5, v5, v106, v107\n\tv_max3_u32 v5, v5, v108, v109\n\tv_max3_u32 v5, v5, v110, v111\n\tv_max3_u32 v5, v5, v112, v113\n\t"
      "v_max3_u32 v5, v5, v114, v115\n\tv_max3_u32 v5, v5, v116, v117\n\tv_max3_u32 v5, v5, v118, v119\n\tv_max3_u32 v5, v5, v120, v121\n\tv_max3_u32 v5, v5, v122, v123\n\t"
      "v_max3_u32 v5, v5, v124, v125\n\tv_max3_u32 v5, v5, v126, v127\n\tv_max3_u32 v5, v5, v128, v129\n\tv_max3_u32 v5, v5, v130, v131\n\tv_max3_u32 v5, v5, v132, v133\n\t"
      "v_max3_u32 v5, v5, v134, v135\n\tv_max3_u32 v5, v5, v136, v137\n\tv_max3_u32 v5, v5, v138, v139\n\tv_max3_u32 v5, v5, v140, v141\n\tv_max3_u32 v5, v5, v142, v143\n\t"
      "v_max3_u32 v5, v5, v144, v145\n\tv_max3_u32 v5, v5, v146, v147\n\tv_max3_u32 v5, v5, v148, v149\n\tv_max3_u32 v5, v5, v150, v151\n\tv_max3_u32 v5, v5, v152, v153\n\t"
      "v_max3_u32 v5, v5, v154, v155\n\tv_max3_u32 v5, v5, v156, v157\n\tv_max3_u32 v5, v5, v158, v159\n\tv_max3_u32 v5, v5, v160, v161\n\tv_max3_u32 v5, v5, v162, v163\n\t"
      "v_max3_u32 v5, v5, v164, v165\n\tv_max3_u32 v5, v5, v166, v167\n\tv_max3_u32 v5, v5, v168, v169\n\tv_max3_u32 v5, v5, v170, v171\n\tv_max3_u32 v5, v5, v172, v173\n\t"
      "v_max3_u32 v5, v5, v174, v175\n\tv_max3_u32 v5, v5, v176, v177\n\tv_max3_u32 v5, v5, v178, v179\n\tv_max3_u32 v5, v5, v180, v181\n\tv_max3_u32 v5, v5, v182, v183\n\t"
      "v_max3_u32 v5, v5, v184, v185\n\tv_max3_u32 v5, v5, v186, v187\n\tv_max3_u32 v5, v5, v188, v189\n\tv_max3_u32 v5, v5, v190, v191\n\tv_max3_u32 v5, v5, v192, v193\n\t"
      "v_max3_u32 v5, v5, v194, v195\n\tv_max3_u32 v5, v5, v196, v197\n\tv_max3_u32 v5, v5, v198, v199\n\tv_max3_u32 v5, v5, v200, v201\n\tv_max3_u32 v5, v5, v202, v203\n\t"
      "v_max3_u32 v5, v5, v204, v205\n\tv_max3_u32 v5, v5, v206, v207\n\tv_max3_u32 v5, v5, v208, v209\n\tv_max3_u32 v5, v5, v210, v211\n\tv_max3_u32 v5, v5, v212, v213\n\t"
      "v_max3_u32 v5, v5, v214, v215\n\tv_max3_u32 v5, v5, v216, v217\n\tv_max3_u32 v5, v5, v218, v219\n\tv_max3_u32 v5, v5, v220, v221\n\tv_max3_u32 v5, v5, v222, v223\n\t"
      "v_cmp_eq_u32 vcc, -1, v5\n\ts_cbranch_vccz 51b\n\ts_cmp_eq_u32 s18, 0\n\ts_cbranch_scc1 51b\n\ts_sub_u32 s18, s18, 1\n\ts_branch 50b\n\t"
      "40:\n\t"
      ".Lmk_end1:\n\ts_waitcnt vmcnt(0) lgkmcnt(0)\n\t"
      TKS("s_mul_i32 s48, s16, 72\n\ts_add_u32 s48, s48, 162080\n\tv_mov_b32 v224, s48\n\tds_read_b32 v225, v224 offset:0\n\ts_waitcnt lgkmcnt(0)\n\tv_readfirstlane_b32 s74, v225\n\tds_read_b32 v225, v224 offset:4\n\ts_waitcnt lgkmcnt(0)\n\tv_readfirstlane_b32 s75, v225\n\tds_read_b32 v225, v224 offset:8\n\ts_waitcnt lgkmcnt(0)\n\tv_readfirstlane_b32 s76, v225\n\tds_read_b32 v225, v224 offset:12\n\ts_waitcnt lgkmcnt(0)\n\tv_readfirstlane_b32 s77, v225\n\tds_read_b32 v225, v224 offset:16\n\ts_waitcnt lgkmcnt(0)\n\tv_readfirstlane_b32 s78, v225\n\tds_read_b32 v225, v224 offset:20\n\ts_waitcnt lgkmcnt(0)\n\tv_readfirstlane_b32 s79, v225\n\tds_read_b32 v225, v224 offset:24\n\ts_waitcnt lgkmcnt(0)\n\tv_readfirstlane_b32 s80, v225\n\tds_read_b32 v225, v224 offset:28\n\ts_waitcnt lgkmcnt(0)\n\tv_readfirstlane_b32 s81, v225\n\tds_read_b32 v225, v224 offset:32\n\ts_waitcnt lgkmcnt(0)\n\tv_readfirstlane_b32 s82, v225\n\tds_read_b32 v225, v224 offset:36\n\ts_waitcnt lgkmcnt(0)\n\tv_readfirstlane_b32 s83, v225\n\tds_read_b32 v225, v224 offset:40\n\ts_waitcnt lgkmcnt(0)\n\tv_readfirstlane_b32 s84, v225\n\tds_read_b32 v225, v224 offset:44\n\ts_waitcnt lgkmcnt(0)\n\tv_readfirstlane_b32 s85, v225\n\tds_read_b32 v225, v224 offset:48\n\ts_waitcnt lgkmcnt(0)\n\tv_readfirstlane_b32 s86, v225\n\tds_read_b32 v225, v224 offset:52\n\ts_waitcnt lgkmcnt(0)\n\tv_readfirstlane_b32 s87, v225\n\tds_read_b32 v225, v224 offset:56\n\ts_waitcnt lgkmcnt(0)\n\tv_readfirstlane_b32 s88, v225\n\tds_read_b32 v225, v224 offset:60\n\ts_waitcnt lgkmcnt(0)\n\tv_readfirstlane_b32 s89, v225\n\tds_read_b32 v225, v224 offset:64\n\ts_waitcnt lgkmcnt(0)\n\tv_readfirstlane_b32 s90, v225\n\tds_read_b32 v225, v224 offset:68\n\ts_waitcnt lgkmcnt(0)\n\tv_readfirstlane_b32 s91, v225\n\t")
      STS("s_lshl_b32 s48, s16, 2\n\ts_add_u32 s48, s48, 162944\n\tv_mov_b32 v0, s48\n\tv_mov_b32 v1, s50\n\tds_write_b32 v0, v1\n\ts_cmp_lg_u32 s16, 0\n\ts_cbranch_scc1 97f\n\tv_mov_b32 v1, s51\n\tds_write_b32 v0, v1 offset:16\n\t97:\n\ts_cmp_lg_u32 s16, -1\n\ts_cbranch_scc1 95f\n\tv_mov_b32 v1, s61\n\tds_write_b32 v0, v1 offset:24\n\tv_mov_b32 v1, s21\n\tds_write_b32 v0, v1 offset:28\n\tv_mov_b32 v1, s22\n\tds_write_b32 v0, v1 offset:32\n\t95:\n\t")
      "s_cmp_lg_u32 s18, 0\n\ts_cbranch_scc1 96f\n\tv_mov_b32 v1, 1\n\tbuffer_store_dword v1, off, s[12:15], 0 offset:3328 sc1\n\t"
      "96:\n\t"
      : TOK_REGS :: "memory", "vcc", "scc", S_CLOB TK_CLOB);
  } else {
    unsigned seenZ = 0;
    const int f = amap(((y >> 2) & 3) * 16384 + (y >> 4) * 4096 + (lane & 31) * 128, par);
    auto least = [](unsigned v) {
      v = min(v, (unsigned)__builtin_amdgcn_update_dpp(0, v, 0x128, 0xf, 0xf, false)); v = min(v, (unsigned)__builtin_amdgcn_update_dpp(0, v, 0x124, 0xf, 0xf, false));
      v = min(v, (unsigned)__builtin_amdgcn_update_dpp(0, v, 0x122, 0xf, 0xf, false)); v = min(v, (unsigned)__builtin_amdgcn_update_dpp(0, v, 0x121, 0xf, 0xf, false));
      return uni(min((unsigned)__builtin_amdgcn_readlane(v, 0), (unsigned)__builtin_amdgcn_readlane(v, 16)));
    };
    for (int n = 0; n < NSEG; ++n) {
      if (n >= 4) { lds_wait(TDONE, n - 3); lds_wait(TDONE + 1, n - 3); }
      if (n >= 2) lds_wait(PUB, n - 1);
      const unsigned t0 = (unsigned)__builtin_amdgcn_s_memtime();
#pragma unroll 1
      while (seenZ < (unsigned)n + 1u) {
        asm volatile("" ::: "memory");
        seenZ = least(__builtin_amdgcn_raw_buffer_load_b32(c.rs, f, R_FZ, AUX_SC1));
        if (seenZ >= (unsigned)n + 1u || spend(c)) break;
        __builtin_amdgcn_s_sleep(2);
      }
      if (n < 8) DIAG(380 + n, (unsigned)__builtin_amdgcn_s_memrealtime());
      unsigned so;
#define Z_OPS : [so] "=&s"(so) : [o] "v"(lane * 16 + ((HACK & 8) << 27)), [rs] "s"(c.rs), [m] "s"(ZR + (n & 3) * 32768), [zb] "s"(R_Z + ((n % ZRN) * 64 + y) * 65536 + ((para ^ (y >> 5)) << 13)) : "memory", "scc"
      if ((unsigned)__builtin_amdgcn_s_memtime() - t0 > 2500u) asm volatile(
      "s_add_u32 m0, %[m], 0\n\ts_add_u32 %[so], %[zb], 0\n\tbuffer_load_dwordx4 %[o], %[rs], %[so] offen offset:0 sc1 lds\n\t"
      "buffer_load_dwordx4 %[o], %[rs], %[so] offen offset:1024 sc1 lds\n\tbuffer_load_dwordx4 %[o], %[rs], %[so] offen offset:2048 sc1 lds\n\t"
      "buffer_load_dwordx4 %[o], %[rs], %[so] offen offset:3072 sc1 lds\n\ts_add_u32 m0, %[m], 4096\n\ts_add_u32 %[so], %[zb], 4096\n\t"
      "buffer_load_dwordx4 %[o], %[rs], %[so] offen offset:0 sc1 lds\n\tbuffer_load_dwordx4 %[o], %[rs], %[so] offen offset:1024 sc1 lds\n\t"
      "buffer_load_dwordx4 %[o], %[rs], %[so] offen offset:2048 sc1 lds\n\tbuffer_load_dwordx4 %[o], %[rs], %[so] offen offset:3072 sc1 lds\n\ts_add_u32 m0, %[m], 8192\n\t"
      "s_add_u32 %[so], %[zb], 16384\n\tbuffer_load_dwordx4 %[o], %[rs], %[so] offen offset:0 sc1 lds\n\tbuffer_load_dwordx4 %[o], %[rs], %[so] offen offset:1024 sc1 lds\n\t"
      "buffer_load_dwordx4 %[o], %[rs], %[so] offen offset:2048 sc1 lds\n\tbuffer_load_dwordx4 %[o], %[rs], %[so] offen offset:3072 sc1 lds\n\ts_add_u32 m0, %[m], 12288\n\t"
      "s_add_u32 %[so], %[zb], 20480\n\tbuffer_load_dwordx4 %[o], %[rs], %[so] offen offset:0 sc1 lds\n\tbuffer_load_dwordx4 %[o], %[rs], %[so] offen offset:1024 sc1 lds\n\t"
      "buffer_load_dwordx4 %[o], %[rs], %[so] offen offset:2048 sc1 lds\n\tbuffer_load_dwordx4 %[o], %[rs], %[so] offen offset:3072 sc1 lds\n\ts_add_u32 m0, %[m], 16384\n\t"
      "s_add_u32 %[so], %[zb], 32768\n\tbuffer_load_dwordx4 %[o], %[rs], %[so] offen offset:0 sc1 lds\n\tbuffer_load_dwordx4 %[o], %[rs], %[so] offen offset:1024 sc1 lds\n\t"
      "buffer_load_dwordx4 %[o], %[rs], %[so] offen offset:2048 sc1 lds\n\tbuffer_load_dwordx4 %[o], %[rs], %[so] offen offset:3072 sc1 lds\n\ts_add_u32 m0, %[m], 20480\n\t"
      "s_add_u32 %[so], %[zb], 36864\n\tbuffer_load_dwordx4 %[o], %[rs], %[so] offen offset:0 sc1 lds\n\tbuffer_load_dwordx4 %[o], %[rs], %[so] offen offset:1024 sc1 lds\n\t"
      "buffer_load_dwordx4 %[o], %[rs], %[so] offen offset:2048 sc1 lds\n\tbuffer_load_dwordx4 %[o], %[rs], %[so] offen offset:3072 sc1 lds\n\ts_add_u32 m0, %[m], 24576\n\t"
      "s_add_u32 %[so], %[zb], 49152\n\tbuffer_load_dwordx4 %[o], %[rs], %[so] offen offset:0 sc1 lds\n\tbuffer_load_dwordx4 %[o], %[rs], %[so] offen offset:1024 sc1 lds\n\t"
      "buffer_load_dwordx4 %[o], %[rs], %[so] offen offset:2048 sc1 lds\n\tbuffer_load_dwordx4 %[o], %[rs], %[so] offen offset:3072 sc1 lds\n\ts_add_u32 m0, %[m], 28672\n\t"
      "s_add_u32 %[so], %[zb], 53248\n\tbuffer_load_dwordx4 %[o], %[rs], %[so] offen offset:0 sc1 lds\n\tbuffer_load_dwordx4 %[o], %[rs], %[so] offen offset:1024 sc1 lds\n\t"
      "buffer_load_dwordx4 %[o], %[rs], %[so] offen offset:2048 sc1 lds\n\tbuffer_load_dwordx4 %[o], %[rs], %[so] offen offset:3072 sc1 lds\n\t"
        Z_OPS);
      else asm volatile(
      "s_add_u32 m0, %[m], 0\n\ts_add_u32 %[so], %[zb], 0\n\tbuffer_load_dwordx4 %[o], %[rs], %[so] offen offset:0 sc1 lds\n\ts_nop 4\n\t"
      "buffer_load_dwordx4 %[o], %[rs], %[so] offen offset:1024 sc1 lds\n\ts_nop 4\n\tbuffer_load_dwordx4 %[o], %[rs], %[so] offen offset:2048 sc1 lds\n\ts_nop 4\n\t"
      "buffer_load_dwordx4 %[o], %[rs], %[so] offen offset:3072 sc1 lds\n\ts_nop 4\n\ts_add_u32 m0, %[m], 4096\n\ts_add_u32 %[so], %[zb], 4096\n\t"
      "buffer_load_dwordx4 %[o], %[rs], %[so] offen offset:0 sc1 lds\n\ts_nop 4\n\tbuffer_load_dwordx4 %[o], %[rs], %[so] offen offset:1024 sc1 lds\n\ts_nop 4\n\t"
      "buffer_load_dwordx4 %[o], %[rs], %[so] offen offset:2048 sc1 lds\n\ts_nop 4\n\tbuffer_load_dwordx4 %[o], %[rs], %[so] offen offset:3072 sc1 lds\n\ts_nop 4\n\t"
      "s_add_u32 m0, %[m], 8192\n\ts_add_u32 %[so], %[zb], 16384\n\tbuffer_load_dwordx4 %[o], %[rs], %[so] offen offset:0 sc1 lds\n\ts_nop 4\n\t"
      "buffer_load_dwordx4 %[o], %[rs], %[so] offen offset:1024 sc1 lds\n\ts_nop 4\n\tbuffer_load_dwordx4 %[o], %[rs], %[so] offen offset:2048 sc1 lds\n\ts_nop 4\n\t"
      "buffer_load_dwordx4 %[o], %[rs], %[so] offen offset:3072 sc1 lds\n\ts_nop 4\n\ts_add_u32 m0, %[m], 12288\n\ts_add_u32 %[so], %[zb], 20480\n\t"
      "buffer_load_dwordx4 %[o], %[rs], %[so] offen offset:0 sc1 lds\n\ts_nop 4\n\tbuffer_load_dwordx4 %[o], %[rs], %[so] offen offset:1024 sc1 lds\n\ts_nop 4\n\t"
      "buffer_load_dwordx4 %[o], %[rs], %[so] offen offset:2048 sc1 lds\n\ts_nop 4\n\tbuffer_load_dwordx4 %[o], %[rs], %[so] offen offset:3072 sc1 lds\n\ts_nop 4\n\t"
      "s_add_u32 m0, %[m], 16384\n\ts_add_u32 %[so], %[zb], 32768\n\tbuffer_load_dwordx4 %[o], %[rs], %[so] offen offset:0 sc1 lds\n\ts_nop 4\n\t"
      "buffer_load_dwordx4 %[o], %[rs], %[so] offen offset:1024 sc1 lds\n\ts_nop 4\n\tbuffer_load_dwordx4 %[o], %[rs], %[so] offen offset:2048 sc1 lds\n\ts_nop 4\n\t"
      "buffer_load_dwordx4 %[o], %[rs], %[so] offen offset:3072 sc1 lds\n\ts_nop 4\n\ts_add_u32 m0, %[m], 20480\n\ts_add_u32 %[so], %[zb], 36864\n\t"
      "buffer_load_dwordx4 %[o], %[rs], %[so] offen offset:0 sc1 lds\n\ts_nop 4\n\tbuffer_load_dwordx4 %[o], %[rs], %[so] offen offset:1024 sc1 lds\n\ts_nop 4\n\t"
      "buffer_load_dwordx4 %[o], %[rs], %[so] offen offset:2048 sc1 lds\n\ts_nop 4\n\tbuffer_load_dwordx4 %[o], %[rs], %[so] offen offset:3072 sc1 lds\n\ts_nop 4\n\t"
      "s_add_u32 m0, %[m], 24576\n\ts_add_u32 %[so], %[zb], 49152\n\tbuffer_load_dwordx4 %[o], %[rs], %[so] offen offset:0 sc1 lds\n\ts_nop 4\n\t"
      "buffer_load_dwordx4 %[o], %[rs], %[so] offen offset:1024 sc1 lds\n\ts_nop 4\n\tbuffer_load_dwordx4 %[o], %[rs], %[so] offen offset:2048 sc1 lds\n\ts_nop 4\n\t"
      "buffer_load_dwordx4 %[o], %[rs], %[so] offen offset:3072 sc1 lds\n\ts_nop 4\n\ts_add_u32 m0, %[m], 28672\n\ts_add_u32 %[so], %[zb], 53248\n\t"
      "buffer_load_dwordx4 %[o], %[rs], %[so] offen offset:0 sc1 lds\n\ts_nop 4\n\tbuffer_load_dwordx4 %[o], %[rs], %[so] offen offset:1024 sc1 lds\n\ts_nop 4\n\t"
      "buffer_load_dwordx4 %[o], %[rs], %[so] offen offset:2048 sc1 lds\n\ts_nop 4\n\tbuffer_load_dwordx4 %[o], %[rs], %[so] offen offset:3072 sc1 lds\n\ts_nop 4\n\t"
        Z_OPS);
      const unsigned nx = __builtin_amdgcn_raw_buffer_load_b32(c.rs, seenZ > (unsigned)n + FAHEAD ? 1 << 30 : f, R_FZ, AUX_SC1);
      VMWAIT(1);
      lds_post(ZFULL, n + 1);
      seenZ = max(seenZ, least(nx));
      if (n < 8) DIAG(390 + n, (unsigned)__builtin_amdgcn_s_memrealtime());
    }
    constexpr int s = NSEG - 1;
    const int uo = y * 16384 + para * 8192 + lane * 16, ub = R_U + (s & 1) * (64 * 16384);
    lds_wait(GO, s); lds_wait(TDONE, s - 2); lds_wait(TDONE + 1, s - 2);
    v4u u[8];
#pragma unroll 1
    for (;;) {
      asm volatile("" ::: "memory");
#pragma unroll
      for (int i = 0; i < 8; ++i) u[i] = ld16s(c.rs, uo + (i & 3) * 1024, ub + (i >> 2) * 4096);
      SCHED();
      unsigned t = 0;
#pragma unroll
      for (int i = 0; i < 8; ++i) t = umax(t, top(u[i]));
      if (allok(t) || spend(c)) break;
    }
#pragma unroll
    for (int i = 0; i < 8; ++i) sts16(c.L, UL + (s % 3) * 8192 + i * 1024 + lane * 16, u[i]);
    lds_post(GO, s + 1);
  }
  TICK_OUT();
  if (c.budget == 0u) __hip_atomic_store(c.ctrl + C_TMO / 4, 1u, RLX_AGENT);
}

DEV void g_role(Ctx& c, int mat, int h) {
  const KArg p = enter(c);
  c.budget = SPIN_CAP;
  const int w = c.w, lane = fresh_lane(), g = lane >> 4, h2 = lane >> 5, r32 = lane & 31, q = (lane >> 2) & 3, pp = lane & 3;
  v32u k;
#pragma unroll
  for (int i = 0; i < 8; ++i) {
    k[i] = r32 * 2048 + (((2 * i + h2) ^ (r32 & 15)) << 4); k[8 + i] = k[i] + 65536;
    k[16 + i] = (8 * w + i) * 2048 + ((lane ^ ((8 * w + i) & 15)) << 4) + ((i & 1) && (HACK & 128) ? 1 << 30 : 0);
  }
  k[24] = (4 * w + 2 * h2) * 512 + r32 * 16 + ((HACK & 4) << 28);
  k[25] = (((h & 7) * 4 + w) * 16 + (lane & 15)) * 128;
  k[26] = 0; k[27] = (lane | w) ? 1 << 30 : 0; k[28] = 0;
  k[29] = (16 * w + g) * 8192 + head(h) * 256 + (((lane & 15) ^ ((g & 1) | (h2 << 3))) << 4);
  k[30] = 65536 + (8 * h2 + q) * 256 + (((8 * (w >> 1) + 4 * (g & 1) + 2 * (pp >> 1) + (w & 1)) ^ ((q & 1) | ((q >> 1) << 3))) << 4) + 8 * (pp & 1);
  k[31] = k[30] + 65536;
  const u16* W = mat == 0 ? p->Wq : mat == 1 ? p->Wk : p->Wv;
  v32u s0 = {RSRCS, (unsigned)w, 0, c.budget, (unsigned)(R_QX + mat * (R_KX - R_QX) + hblock(h, mat == 2 ? 8192 : QKFAR) + (GRN - 1) * 16384), 0, 4 << 16, (unsigned)(R_FQ + mat * 4096 + h * 128), mat == 2 || (mat ? DK : DQ) == 0 ? 0u : max((unsigned)__builtin_amdgcn_s_memrealtime() + (mat ? DK : DQ), 1u), mat != 2, 0, 0, mat == 2 ? GRN - 1 : L0, mat == 2 ? 0 : LA, (unsigned)(R_FP + (h >> 3) * 128)};
  v32u s1 = {plo(W), phi(W), 1024 * 4096 * 2, 0x00020000, plo(p->x), phi(p->x), 2048 * 2048, 0x00020000, 0, 0, (unsigned)(w * 16384), (unsigned)(65536 + w * 4096), 0, 0, 0, 0,
             0, 0, 0, 0, ENTRY, 0, 0, 0, 0, HELLO};
  v32u wa[8], wv[5];
  asm volatile(
      "s_nop 0\n\tv_readfirstlane_b32 s8, v160\n\tv_readfirstlane_b32 s9, v161\n\tv_readfirstlane_b32 s10, v162\n\tv_readfirstlane_b32 s11, v163\n\t"
      "v_readfirstlane_b32 s12, v164\n\tv_readfirstlane_b32 s13, v165\n\tv_readfirstlane_b32 s14, v166\n\tv_readfirstlane_b32 s15, v167\n\tv_readfirstlane_b32 s16, v168\n\t"
      "v_readfirstlane_b32 s17, v169\n\tv_readfirstlane_b32 s18, v170\n\tv_readfirstlane_b32 s19, v171\n\tv_readfirstlane_b32 s20, v172\n\tv_readfirstlane_b32 s21, v173\n\t"
      "v_readfirstlane_b32 s22, v174\n\tv_readfirstlane_b32 s23, v175\n\tv_readfirstlane_b32 s24, v176\n\tv_readfirstlane_b32 s27, v179\n\tv_readfirstlane_b32 s28, v180\n\t"
      "v_readfirstlane_b32 s29, v181\n\tv_readfirstlane_b32 s40, v192\n\tv_readfirstlane_b32 s41, v193\n\tv_readfirstlane_b32 s42, v194\n\tv_readfirstlane_b32 s43, v195\n\t"
      "v_readfirstlane_b32 s44, v196\n\tv_readfirstlane_b32 s45, v197\n\tv_readfirstlane_b32 s46, v198\n\tv_readfirstlane_b32 s47, v199\n\tv_readfirstlane_b32 s50, v202\n\t"
      "v_readfirstlane_b32 s51, v203\n\tv_readfirstlane_b32 s60, v212\n\tv_readfirstlane_b32 s61, v213\n\tv_readfirstlane_b32 s62, v214\n\tv_readfirstlane_b32 s63, v215\n\t"
      "v_readfirstlane_b32 s68, v220\n\tv_readfirstlane_b32 s69, v221\n\tv_readfirstlane_b32 s70, v222\n\ts_nop 4\n\t"
      TKS("s_mul_i32 s48, s16, 72\n\ts_add_u32 s48, s48, 162080\n\tv_mov_b32 v224, s48\n\tv_mov_b32 v225, s74\n\tds_write_b32 v224, v225 offset:0\n\tv_mov_b32 v225, s75\n\tds_write_b32 v224, v225 offset:4\n\tv_mov_b32 v225, s76\n\tds_write_b32 v224, v225 offset:8\n\tv_mov_b32 v225, s77\n\tds_write_b32 v224, v225 offset:12\n\tv_mov_b32 v225, s78\n\tds_write_b32 v224, v225 offset:16\n\tv_mov_b32 v225, s79\n\tds_write_b32 v224, v225 offset:20\n\tv_mov_b32 v225, s80\n\tds_write_b32 v224, v225 offset:24\n\tv_mov_b32 v225, s81\n\tds_write_b32 v224, v225 offset:28\n\tv_mov_b32 v225, s82\n\tds_write_b32 v224, v225 offset:32\n\tv_mov_b32 v225, s83\n\tds_write_b32 v224, v225 offset:36\n\tv_mov_b32 v225, s84\n\tds_write_b32 v224, v225 offset:40\n\tv_mov_b32 v225, s85\n\tds_write_b32 v224, v225 offset:44\n\tv_mov_b32 v225, s86\n\tds_write_b32 v224, v225 offset:48\n\tv_mov_b32 v225, s87\n\tds_write_b32 v224, v225 offset:52\n\tv_mov_b32 v225, s88\n\tds_write_b32 v224, v225 offset:56\n\tv_mov_b32 v225, s89\n\tds_write_b32 v224, v225 offset:60\n\tv_mov_b32 v225, s90\n\tds_write_b32 v224, v225 offset:64\n\tv_mov_b32 v225, s91\n\tds_write_b32 v224, v225 offset:68\n\t")
      "s_buffer_load_dword s71, s[12:15], s69 glc\n\t"
      "s_cmp_eq_u32 s23, 0\n\ts_cbranch_scc1 8f\n\ts_waitcnt lgkmcnt(0)\n\ts_bitcmp0_b32 s68, 0\n\ts_cbranch_scc1 5f\n\ts_add_u32 s71, s71, 1\n\ts_sub_u32 s48, s69, 32768\n\t"
      "v_mov_b32 v28, s71\n\ts_mov_b64 exec, 1\n\tbuffer_store_dword v28, off, s[12:15], s48 sc1\n\ts_mov_b64 exec, -1\n\t"
      "5:\n\t"
      "9:\n\ts_cmp_lt_u32 s68, 1\n\ts_cbranch_scc1 7f\n\ts_cmp_eq_u32 s68, 2\n\ts_cbranch_scc1 6f\n\t"
      "5:\n\ts_buffer_load_dword s48, s[12:15], s69 glc\n\ts_waitcnt lgkmcnt(0)\n\ts_cmp_eq_u32 s48, s71\n\ts_cbranch_scc1 6f\n\ts_cmp_lg_u32 s68, 3\n\ts_cbranch_scc1 7f\n\t"
      "s_cmp_eq_u32 s18, 0\n\ts_cbranch_scc1 6f\n\ts_sub_u32 s18, s18, 1\n\ts_branch 5b\n\t"
      "6:\n\tv_mov_b32 v28, 1\n\ts_mov_b64 exec, 1\n\tbuffer_atomic_add v28, off, s[12:15], s70\n\ts_mov_b64 exec, -1\n\ts_mov_b32 s68, 0\n\t"
      "7:\n\ts_sleep 8\n\ts_memrealtime vcc\n\ts_waitcnt lgkmcnt(0)\n\ts_cmp_lt_u32 vcc_lo, s23\n\ts_cbranch_scc1 9b\n\ts_sub_u32 s71, s71, 1\n\t"
      "8:\n\ts_add_u32 m0, s51, 0\n\ts_mov_b32 s48, 0x0\n\tbuffer_load_dwordx4 v29, s[40:43], s48 offen  lds\n\ts_add_u32 m0, s51, 1024\n\ts_mov_b32 s48, 0x8000\n\t"
      "buffer_load_dwordx4 v29, s[40:43], s48 offen  lds\n\ts_add_u32 m0, s51, 2048\n\ts_mov_b32 s48, 0x10000\n\tbuffer_load_dwordx4 v29, s[40:43], s48 offen  lds\n\t"
      "s_add_u32 m0, s51, 3072\n\ts_mov_b32 s48, 0x18000\n\tbuffer_load_dwordx4 v29, s[40:43], s48 offen  lds\n\ts_add_u32 m0, s51, 16384\n\ts_mov_b32 s48, 0x80000\n\t"
      "buffer_load_dwordx4 v29, s[40:43], s48 offen  lds\n\ts_add_u32 m0, s51, 17408\n\ts_mov_b32 s48, 0x88000\n\tbuffer_load_dwordx4 v29, s[40:43], s48 offen  lds\n\t"
      "s_add_u32 m0, s51, 18432\n\ts_mov_b32 s48, 0x90000\n\tbuffer_load_dwordx4 v29, s[40:43], s48 offen  lds\n\ts_add_u32 m0, s51, 19456\n\ts_mov_b32 s48, 0x98000\n\t"
      "buffer_load_dwordx4 v29, s[40:43], s48 offen  lds\n\ts_waitcnt lgkmcnt(0)\n\ts_bitcmp0_b32 s68, 0\n\ts_cbranch_scc1 5f\n\ts_add_u32 s71, s71, 1\n\t"
      "s_sub_u32 s48, s69, 32768\n\tv_mov_b32 v28, s71\n\ts_mov_b64 exec, 1\n\tbuffer_store_dword v28, off, s[12:15], s48 sc1\n\ts_mov_b64 exec, -1\n\t"
      "5:\n\ts_add_u32 m0, s51, 32768\n\ts_mov_b32 s48, 0x100000\n\tbuffer_load_dwordx4 v29, s[40:43], s48 offen  lds\n\ts_add_u32 m0, s51, 33792\n\ts_mov_b32 s48, 0x108000\n\t"
      "buffer_load_dwordx4 v29, s[40:43], s48 offen  lds\n\ts_add_u32 m0, s51, 34816\n\ts_mov_b32 s48, 0x110000\n\tbuffer_load_dwordx4 v29, s[40:43], s48 offen  lds\n\t"
      "s_add_u32 m0, s51, 35840\n\ts_mov_b32 s48, 0x118000\n\tbuffer_load_dwordx4 v29, s[40:43], s48 offen  lds\n\ts_add_u32 m0, s51, 49152\n\ts_mov_b32 s48, 0x180000\n\t"
      "buffer_load_dwordx4 v29, s[40:43], s48 offen  lds\n\ts_add_u32 m0, s51, 50176\n\ts_mov_b32 s48, 0x188000\n\tbuffer_load_dwordx4 v29, s[40:43], s48 offen  lds\n\t"
      "s_add_u32 m0, s51, 51200\n\ts_mov_b32 s48, 0x190000\n\tbuffer_load_dwordx4 v29, s[40:43], s48 offen  lds\n\ts_add_u32 m0, s51, 52224\n\ts_mov_b32 s48, 0x198000\n\t"
      "buffer_load_dwordx4 v29, s[40:43], s48 offen  lds\n\ts_cmp_lt_u32 s68, 1\n\ts_cbranch_scc1 7f\n\ts_cmp_eq_u32 s68, 2\n\ts_cbranch_scc1 6f\n\t"
      "5:\n\ts_buffer_load_dword s48, s[12:15], s69 glc\n\ts_waitcnt lgkmcnt(0)\n\ts_cmp_eq_u32 s48, s71\n\ts_cbranch_scc1 6f\n\ts_cmp_lg_u32 s68, 3\n\ts_cbranch_scc1 7f\n\t"
      "s_cmp_eq_u32 s18, 0\n\ts_cbranch_scc1 6f\n\ts_sub_u32 s18, s18, 1\n\ts_branch 5b\n\t"
      "6:\n\tv_mov_b32 v28, 1\n\ts_mov_b64 exec, 1\n\tbuffer_atomic_add v28, off, s[12:15], s70\n\ts_mov_b64 exec, -1\n\ts_mov_b32 s68, 0\n\t"
      "7:\n\ts_waitcnt vmcnt(12)\n\ts_barrier\n\tds_read_b64_tr_b16 a[0:1], v30 offset:0\n\tds_read_b64_tr_b16 a[2:3], v30 offset:1024\n\t"
      "ds_read_b64_tr_b16 a[4:5], v30 offset:4096\n\tds_read_b64_tr_b16 a[6:7], v30 offset:5120\n\tds_read_b64_tr_b16 a[8:9], v30 offset:8192\n\t"
      "ds_read_b64_tr_b16 a[10:11], v30 offset:9216\n\tds_read_b64_tr_b16 a[12:13], v30 offset:12288\n\tds_read_b64_tr_b16 a[14:15], v30 offset:13312\n\ts_waitcnt lgkmcnt(0)\n\t"
      "s_add_u32 m0, s51, 65536\n\ts_mov_b32 s48, 0x200000\n\tbuffer_load_dwordx4 v29, s[40:43], s48 offen  lds\n\ts_add_u32 m0, s51, 66560\n\ts_mov_b32 s48, 0x208000\n\t"
      "buffer_load_dwordx4 v29, s[40:43], s48 offen  lds\n\ts_add_u32 m0, s51, 67584\n\ts_mov_b32 s48, 0x210000\n\tbuffer_load_dwordx4 v29, s[40:43], s48 offen  lds\n\t"
      "s_add_u32 m0, s51, 68608\n\ts_mov_b32 s48, 0x218000\n\tbuffer_load_dwordx4 v29, s[40:43], s48 offen  lds\n\ts_cmp_lt_u32 s68, 1\n\ts_cbranch_scc1 7f\n\t"
      "s_cmp_eq_u32 s68, 2\n\ts_cbranch_scc1 6f\n\t"
      "5:\n\ts_buffer_load_dword s48, s[12:15], s69 glc\n\ts_waitcnt lgkmcnt(0)\n\ts_cmp_eq_u32 s48, s71\n\ts_cbranch_scc0 7f\n\t"
      "6:\n\tv_mov_b32 v28, 1\n\ts_mov_b64 exec, 1\n\tbuffer_atomic_add v28, off, s[12:15], s70\n\ts_mov_b64 exec, -1\n\ts_mov_b32 s68, 0\n\t"
      "7:\n\ts_waitcnt vmcnt(12)\n\ts_barrier\n\tds_read_b64_tr_b16 a[16:17], v30 offset:16384\n\tds_read_b64_tr_b16 a[18:19], v30 offset:17408\n\t"
      "ds_read_b64_tr_b16 a[20:21], v30 offset:20480\n\tds_read_b64_tr_b16 a[22:23], v30 offset:21504\n\tds_read_b64_tr_b16 a[24:25], v30 offset:24576\n\t"
      "ds_read_b64_tr_b16 a[26:27], v30 offset:25600\n\tds_read_b64_tr_b16 a[28:29], v30 offset:28672\n\tds_read_b64_tr_b16 a[30:31], v30 offset:29696\n\t"
      "s_waitcnt lgkmcnt(0)\n\ts_add_u32 m0, s51, 0\n\ts_mov_b32 s48, 0x280000\n\tbuffer_load_dwordx4 v29, s[40:43], s48 offen  lds\n\ts_add_u32 m0, s51, 1024\n\t"
      "s_mov_b32 s48, 0x288000\n\tbuffer_load_dwordx4 v29, s[40:43], s48 offen  lds\n\ts_add_u32 m0, s51, 2048\n\ts_mov_b32 s48, 0x290000\n\t"
      "buffer_load_dwordx4 v29, s[40:43], s48 offen  lds\n\ts_add_u32 m0, s51, 3072\n\ts_mov_b32 s48, 0x298000\n\tbuffer_load_dwordx4 v29, s[40:43], s48 offen  lds\n\t"
      "s_cmp_lt_u32 s68, 1\n\ts_cbranch_scc1 7f\n\ts_cmp_eq_u32 s68, 2\n\ts_cbranch_scc1 6f\n\t"
      "5:\n\ts_buffer_load_dword s48, s[12:15], s69 glc\n\ts_waitcnt lgkmcnt(0)\n\ts_cmp_eq_u32 s48, s71\n\ts_cbranch_scc0 7f\n\t"
      "6:\n\tv_mov_b32 v28, 1\n\ts_mov_b64 exec, 1\n\tbuffer_atomic_add v28, off, s[12:15], s70\n\ts_mov_b64 exec, -1\n\ts_mov_b32 s68, 0\n\t"
      "7:\n\ts_waitcnt vmcnt(12)\n\ts_barrier\n\tds_read_b64_tr_b16 a[32:33], v30 offset:32768\n\tds_read_b64_tr_b16 a[34:35], v30 offset:33792\n\t"
      "ds_read_b64_tr_b16 a[36:37], v30 offset:36864\n\tds_read_b64_tr_b16 a[38:39], v30 offset:37888\n\tds_read_b64_tr_b16 a[40:41], v30 offset:40960\n\t"
      "ds_read_b64_tr_b16 a[42:43], v30 offset:41984\n\tds_read_b64_tr_b16 a[44:45], v30 offset:45056\n\tds_read_b64_tr_b16 a[46:47], v30 offset:46080\n\t"
      "s_waitcnt lgkmcnt(0)\n\ts_add_u32 m0, s51, 16384\n\ts_mov_b32 s48, 0x300000\n\tbuffer_load_dwordx4 v29, s[40:43], s48 offen  lds\n\ts_add_u32 m0, s51, 17408\n\t"
      "s_mov_b32 s48, 0x308000\n\tbuffer_load_dwordx4 v29, s[40:43], s48 offen  lds\n\ts_add_u32 m0, s51, 18432\n\ts_mov_b32 s48, 0x310000\n\t"
      "buffer_load_dwordx4 v29, s[40:43], s48 offen  lds\n\ts_add_u32 m0, s51, 19456\n\ts_mov_b32 s48, 0x318000\n\tbuffer_load_dwordx4 v29, s[40:43], s48 offen  lds\n\t"
      "s_cmp_lt_u32 s68, 1\n\ts_cbranch_scc1 7f\n\ts_cmp_eq_u32 s68, 2\n\ts_cbranch_scc1 6f\n\t"
      "5:\n\ts_buffer_load_dword s48, s[12:15], s69 glc\n\ts_waitcnt lgkmcnt(0)\n\ts_cmp_eq_u32 s48, s71\n\ts_cbranch_scc0 7f\n\t"
      "6:\n\tv_mov_b32 v28, 1\n\ts_mov_b64 exec, 1\n\tbuffer_atomic_add v28, off, s[12:15], s70\n\ts_mov_b64 exec, -1\n\ts_mov_b32 s68, 0\n\t"
      "7:\n\ts_waitcnt vmcnt(12)\n\ts_barrier\n\tds_read_b64_tr_b16 a[48:49], v30 offset:49152\n\tds_read_b64_tr_b16 a[50:51], v30 offset:50176\n\t"
      "ds_read_b64_tr_b16 a[52:53], v30 offset:53248\n\tds_read_b64_tr_b16 a[54:55], v30 offset:54272\n\tds_read_b64_tr_b16 a[56:57], v30 offset:57344\n\t"
      "ds_read_b64_tr_b16 a[58:59], v30 offset:58368\n\tds_read_b64_tr_b16 a[60:61], v30 offset:61440\n\tds_read_b64_tr_b16 a[62:63], v30 offset:62464\n\t"
      "s_waitcnt lgkmcnt(0)\n\ts_add_u32 m0, s51, 32768\n\ts_mov_b32 s48, 0x380000\n\tbuffer_load_dwordx4 v29, s[40:43], s48 offen  lds\n\ts_add_u32 m0, s51, 33792\n\t"
      "s_mov_b32 s48, 0x388000\n\tbuffer_load_dwordx4 v29, s[40:43], s48 offen  lds\n\ts_add_u32 m0, s51, 34816\n\ts_mov_b32 s48, 0x390000\n\t"
      "buffer_load_dwordx4 v29, s[40:43], s48 offen  lds\n\ts_add_u32 m0, s51, 35840\n\ts_mov_b32 s48, 0x398000\n\tbuffer_load_dwordx4 v29, s[40:43], s48 offen  lds\n\t"
      "s_add_u32 m0, s50, 0\n\ts_nop 0\n\tbuffer_load_dwordx4 v16, s[44:47], 0 offen offset:0  lds\n\ts_add_u32 m0, s50, 0\n\ts_nop 0\n\t"
      "buffer_load_dwordx4 v16, s[44:47], 0 offen offset:1024  lds\n\ts_add_u32 m0, s50, 2048\n\ts_nop 0\n\tbuffer_load_dwordx4 v17, s[44:47], 0 offen offset:0  lds\n\t"
      "s_add_u32 m0, s50, 2048\n\ts_nop 0\n\tbuffer_load_dwordx4 v17, s[44:47], 0 offen offset:1024  lds\n\ts_cmp_lt_u32 s68, 1\n\ts_cbranch_scc1 7f\n\ts_cmp_eq_u32 s68, 2\n\t"
      "s_cbranch_scc1 6f\n\t"
      "5:\n\ts_buffer_load_dword s48, s[12:15], s69 glc\n\ts_waitcnt lgkmcnt(0)\n\ts_cmp_eq_u32 s48, s71\n\ts_cbranch_scc0 7f\n\t"
      "6:\n\tv_mov_b32 v28, 1\n\ts_mov_b64 exec, 1\n\tbuffer_atomic_add v28, off, s[12:15], s70\n\ts_mov_b64 exec, -1\n\ts_mov_b32 s68, 0\n\t"
      "7:\n\ts_waitcnt vmcnt(16)\n\ts_barrier\n\tds_read_b64_tr_b16 a[64:65], v31 offset:0\n\tds_read_b64_tr_b16 a[66:67], v31 offset:1024\n\t"
      "ds_read_b64_tr_b16 a[68:69], v31 offset:4096\n\tds_read_b64_tr_b16 a[70:71], v31 offset:5120\n\tds_read_b64_tr_b16 a[72:73], v31 offset:8192\n\t"
      "ds_read_b64_tr_b16 a[74:75], v31 offset:9216\n\tds_read_b64_tr_b16 a[76:77], v31 offset:12288\n\tds_read_b64_tr_b16 a[78:79], v31 offset:13312\n\ts_waitcnt lgkmcnt(0)\n\t"
      "s_add_u32 m0, s51, 49152\n\ts_mov_b32 s48, 0x400000\n\tbuffer_load_dwordx4 v29, s[40:43], s48 offen  lds\n\ts_add_u32 m0, s51, 50176\n\ts_mov_b32 s48, 0x408000\n\t"
      "buffer_load_dwordx4 v29, s[40:43], s48 offen  lds\n\ts_add_u32 m0, s51, 51200\n\ts_mov_b32 s48, 0x410000\n\tbuffer_load_dwordx4 v29, s[40:43], s48 offen  lds\n\t"
      "s_add_u32 m0, s51, 52224\n\ts_mov_b32 s48, 0x418000\n\tbuffer_load_dwordx4 v29, s[40:43], s48 offen  lds\n\ts_add_u32 m0, s50, 4096\n\ts_nop 0\n\t"
      "buffer_load_dwordx4 v18, s[44:47], 0 offen offset:0  lds\n\ts_add_u32 m0, s50, 4096\n\ts_nop 0\n\tbuffer_load_dwordx4 v18, s[44:47], 0 offen offset:1024  lds\n\t"
      "s_add_u32 m0, s50, 6144\n\ts_nop 0\n\tbuffer_load_dwordx4 v19, s[44:47], 0 offen offset:0  lds\n\ts_add_u32 m0, s50, 6144\n\ts_nop 0\n\t"
      "buffer_load_dwordx4 v19, s[44:47], 0 offen offset:1024  lds\n\ts_cmp_lt_u32 s68, 1\n\ts_cbranch_scc1 7f\n\ts_cmp_eq_u32 s68, 2\n\ts_cbranch_scc1 6f\n\t"
      "5:\n\ts_buffer_load_dword s48, s[12:15], s69 glc\n\ts_waitcnt lgkmcnt(0)\n\ts_cmp_eq_u32 s48, s71\n\ts_cbranch_scc0 7f\n\t"
      "6:\n\tv_mov_b32 v28, 1\n\ts_mov_b64 exec, 1\n\tbuffer_atomic_add v28, off, s[12:15], s70\n\ts_mov_b64 exec, -1\n\ts_mov_b32 s68, 0\n\t"
      "7:\n\ts_waitcnt vmcnt(20)\n\ts_barrier\n\tds_read_b64_tr_b16 a[80:81], v30 offset:0\n\tds_read_b64_tr_b16 a[82:83], v30 offset:1024\n\t"
      "ds_read_b64_tr_b16 a[84:85], v30 offset:4096\n\tds_read_b64_tr_b16 a[86:87], v30 offset:5120\n\tds_read_b64_tr_b16 a[88:89], v30 offset:8192\n\t"
      "ds_read_b64_tr_b16 a[90:91], v30 offset:9216\n\tds_read_b64_tr_b16 a[92:93], v30 offset:12288\n\tds_read_b64_tr_b16 a[94:95], v30 offset:13312\n\ts_waitcnt lgkmcnt(0)\n\t"
      "s_add_u32 m0, s51, 65536\n\ts_mov_b32 s48, 0x480000\n\tbuffer_load_dwordx4 v29, s[40:43], s48 offen  lds\n\ts_add_u32 m0, s51, 66560\n\ts_mov_b32 s48, 0x488000\n\t"
      "buffer_load_dwordx4 v29, s[40:43], s48 offen  lds\n\ts_add_u32 m0, s51, 67584\n\ts_mov_b32 s48, 0x490000\n\tbuffer_load_dwordx4 v29, s[40:43], s48 offen  lds\n\t"
      "s_add_u32 m0, s51, 68608\n\ts_mov_b32 s48, 0x498000\n\tbuffer_load_dwordx4 v29, s[40:43], s48 offen  lds\n\ts_add_u32 m0, s50, 8192\n\ts_nop 0\n\t"
      "buffer_load_dwordx4 v20, s[44:47], 0 offen offset:0  lds\n\ts_add_u32 m0, s50, 8192\n\ts_nop 0\n\tbuffer_load_dwordx4 v20, s[44:47], 0 offen offset:1024  lds\n\t"
      "s_add_u32 m0, s50, 10240\n\ts_nop 0\n\tbuffer_load_dwordx4 v21, s[44:47], 0 offen offset:0  lds\n\ts_add_u32 m0, s50, 10240\n\ts_nop 0\n\t"
      "buffer_load_dwordx4 v21, s[44:47], 0 offen offset:1024  lds\n\ts_cmp_lt_u32 s68, 1\n\ts_cbranch_scc1 7f\n\ts_cmp_eq_u32 s68, 2\n\ts_cbranch_scc1 6f\n\t"
      "5:\n\ts_buffer_load_dword s48, s[12:15], s69 glc\n\ts_waitcnt lgkmcnt(0)\n\ts_cmp_eq_u32 s48, s71\n\ts_cbranch_scc0 7f\n\t"
      "6:\n\tv_mov_b32 v28, 1\n\ts_mov_b64 exec, 1\n\tbuffer_atomic_add v28, off, s[12:15], s70\n\ts_mov_b64 exec, -1\n\ts_mov_b32 s68, 0\n\t"
      "7:\n\ts_waitcnt vmcnt(24)\n\ts_barrier\n\tds_read_b64_tr_b16 a[96:97], v30 offset:16384\n\tds_read_b64_tr_b16 a[98:99], v30 offset:17408\n\t"
      "ds_read_b64_tr_b16 a[100:101], v30 offset:20480\n\tds_read_b64_tr_b16 a[102:103], v30 offset:21504\n\tds_read_b64_tr_b16 a[104:105], v30 offset:24576\n\t"
      "ds_read_b64_tr_b16 a[106:107], v30 offset:25600\n\tds_read_b64_tr_b16 a[108:109], v30 offset:28672\n\tds_read_b64_tr_b16 a[110:111], v30 offset:29696\n\t"
      "s_waitcnt lgkmcnt(0)\n\ts_add_u32 m0, s51, 0\n\ts_mov_b32 s48, 0x500000\n\tbuffer_load_dwordx4 v29, s[40:43], s48 offen  lds\n\ts_add_u32 m0, s51, 1024\n\t"
      "s_mov_b32 s48, 0x508000\n\tbuffer_load_dwordx4 v29, s[40:43], s48 offen  lds\n\ts_add_u32 m0, s51, 2048\n\ts_mov_b32 s48, 0x510000\n\t"
      "buffer_load_dwordx4 v29, s[40:43], s48 offen  lds\n\ts_add_u32 m0, s51, 3072\n\ts_mov_b32 s48, 0x518000\n\tbuffer_load_dwordx4 v29, s[40:43], s48 offen  lds\n\t"
      "s_add_u32 m0, s50, 12288\n\ts_nop 0\n\tbuffer_load_dwordx4 v22, s[44:47], 0 offen offset:0  lds\n\ts_add_u32 m0, s50, 12288\n\ts_nop 0\n\t"
      "buffer_load_dwordx4 v22, s[44:47], 0 offen offset:1024  lds\n\ts_add_u32 m0, s50, 14336\n\ts_nop 0\n\tbuffer_load_dwordx4 v23, s[44:47], 0 offen offset:0  lds\n\t"
      "s_add_u32 m0, s50, 14336\n\ts_nop 0\n\tbuffer_load_dwordx4 v23, s[44:47], 0 offen offset:1024  lds\n\ts_cmp_lt_u32 s68, 1\n\ts_cbranch_scc1 7f\n\ts_cmp_eq_u32 s68, 2\n\t"
      "s_cbranch_scc1 6f\n\t"
      "5:\n\ts_buffer_load_dword s48, s[12:15], s69 glc\n\ts_waitcnt lgkmcnt(0)\n\ts_cmp_eq_u32 s48, s71\n\ts_cbranch_scc0 7f\n\t"
      "6:\n\tv_mov_b32 v28, 1\n\ts_mov_b64 exec, 1\n\tbuffer_atomic_add v28, off, s[12:15], s70\n\ts_mov_b64 exec, -1\n\ts_mov_b32 s68, 0\n\t"
      "7:\n\ts_waitcnt vmcnt(28)\n\ts_barrier\n\tds_read_b64_tr_b16 a[112:113], v30 offset:32768\n\tds_read_b64_tr_b16 a[114:115], v30 offset:33792\n\t"
      "ds_read_b64_tr_b16 a[116:117], v30 offset:36864\n\tds_read_b64_tr_b16 a[118:119], v30 offset:37888\n\tds_read_b64_tr_b16 a[120:121], v30 offset:40960\n\t"
      "ds_read_b64_tr_b16 a[122:123], v30 offset:41984\n\tds_read_b64_tr_b16 a[124:125], v30 offset:45056\n\tds_read_b64_tr_b16 a[126:127], v30 offset:46080\n\t"
      "s_waitcnt lgkmcnt(0)\n\ts_add_u32 m0, s51, 16384\n\ts_mov_b32 s48, 0x580000\n\tbuffer_load_dwordx4 v29, s[40:43], s48 offen  lds\n\ts_add_u32 m0, s51, 17408\n\t"
      "s_mov_b32 s48, 0x588000\n\tbuffer_load_dwordx4 v29, s[40:43], s48 offen  lds\n\ts_add_u32 m0, s51, 18432\n\ts_mov_b32 s48, 0x590000\n\t"
      "buffer_load_dwordx4 v29, s[40:43], s48 offen  lds\n\ts_add_u32 m0, s51, 19456\n\ts_mov_b32 s48, 0x598000\n\tbuffer_load_dwordx4 v29, s[40:43], s48 offen  lds\n\t"
      "s_mov_b32 s48, 0x10000\n\tbuffer_load_dword v26, v25, s[44:47], s48 offen\n\ts_mov_b32 s48, 0x20000\n\tbuffer_load_dword v26, v25, s[44:47], s48 offen\n\t"
      "s_mov_b32 s48, 0x30000\n\tbuffer_load_dword v26, v25, s[44:47], s48 offen\n\ts_cmp_lt_u32 s68, 1\n\ts_cbranch_scc1 7f\n\ts_cmp_eq_u32 s68, 2\n\ts_cbranch_scc1 6f\n\t"
      "5:\n\ts_buffer_load_dword s48, s[12:15], s69 glc\n\ts_waitcnt lgkmcnt(0)\n\ts_cmp_eq_u32 s48, s71\n\ts_cbranch_scc0 7f\n\t"
      "6:\n\tv_mov_b32 v28, 1\n\ts_mov_b64 exec, 1\n\tbuffer_atomic_add v28, off, s[12:15], s70\n\ts_mov_b64 exec, -1\n\ts_mov_b32 s68, 0\n\t"
      "7:\n\ts_waitcnt vmcnt(27)\n\ts_barrier\n\tds_read_b64_tr_b16 a[128:129], v30 offset:49152\n\tds_read_b64_tr_b16 a[130:131], v30 offset:50176\n\t"
      "ds_read_b64_tr_b16 a[132:133], v30 offset:53248\n\tds_read_b64_tr_b16 a[134:135], v30 offset:54272\n\tds_read_b64_tr_b16 a[136:137], v30 offset:57344\n\t"
      "ds_read_b64_tr_b16 a[138:139], v30 offset:58368\n\tds_read_b64_tr_b16 a[140:141], v30 offset:61440\n\tds_read_b64_tr_b16 a[142:143], v30 offset:62464\n\t"
      "s_waitcnt lgkmcnt(0)\n\ts_add_u32 m0, s51, 32768\n\ts_mov_b32 s48, 0x600000\n\tbuffer_load_dwordx4 v29, s[40:43], s48 offen  lds\n\ts_add_u32 m0, s51, 33792\n\t"
      "s_mov_b32 s48, 0x608000\n\tbuffer_load_dwordx4 v29, s[40:43], s48 offen  lds\n\ts_add_u32 m0, s51, 34816\n\ts_mov_b32 s48, 0x610000\n\t"
      "buffer_load_dwordx4 v29, s[40:43], s48 offen  lds\n\ts_add_u32 m0, s51, 35840\n\ts_mov_b32 s48, 0x618000\n\tbuffer_load_dwordx4 v29, s[40:43], s48 offen  lds\n\t"
      "s_cmp_lt_u32 s68, 1\n\ts_cbranch_scc1 7f\n\ts_cmp_eq_u32 s68, 2\n\ts_cbranch_scc1 6f\n\t"
      "5:\n\ts_buffer_load_dword s48, s[12:15], s69 glc\n\ts_waitcnt lgkmcnt(0)\n\ts_cmp_eq_u32 s48, s71\n\ts_cbranch_scc0 7f\n\t"
      "6:\n\tv_mov_b32 v28, 1\n\ts_mov_b64 exec, 1\n\tbuffer_atomic_add v28, off, s[12:15], s70\n\ts_mov_b64 exec, -1\n\ts_mov_b32 s68, 0\n\t"
      "7:\n\ts_waitcnt vmcnt(23)\n\ts_barrier\n\tds_read_b64_tr_b16 a[144:145], v31 offset:0\n\tds_read_b64_tr_b16 a[146:147], v31 offset:1024\n\t"
      "ds_read_b64_tr_b16 a[148:149], v31 offset:4096\n\tds_read_b64_tr_b16 a[150:151], v31 offset:5120\n\tds_read_b64_tr_b16 a[152:153], v31 offset:8192\n\t"
      "ds_read_b64_tr_b16 a[154:155], v31 offset:9216\n\tds_read_b64_tr_b16 a[156:157], v31 offset:12288\n\tds_read_b64_tr_b16 a[158:159], v31 offset:13312\n\t"
      "s_waitcnt lgkmcnt(0)\n\ts_add_u32 m0, s51, 49152\n\ts_mov_b32 s48, 0x680000\n\tbuffer_load_dwordx4 v29, s[40:43], s48 offen  lds\n\ts_add_u32 m0, s51, 50176\n\t"
      "s_mov_b32 s48, 0x688000\n\tbuffer_load_dwordx4 v29, s[40:43], s48 offen  lds\n\ts_add_u32 m0, s51, 51200\n\ts_mov_b32 s48, 0x690000\n\t"
      "buffer_load_dwordx4 v29, s[40:43], s48 offen  lds\n\ts_add_u32 m0, s51, 52224\n\ts_mov_b32 s48, 0x698000\n\tbuffer_load_dwordx4 v29, s[40:43], s48 offen  lds\n\t"
      "s_cmp_lt_u32 s68, 1\n\ts_cbranch_scc1 7f\n\ts_cmp_eq_u32 s68, 2\n\ts_cbranch_scc1 6f\n\t"
      "5:\n\ts_buffer_load_dword s48, s[12:15], s69 glc\n\ts_waitcnt lgkmcnt(0)\n\ts_cmp_eq_u32 s48, s71\n\ts_cbranch_scc0 7f\n\t"
      "6:\n\tv_mov_b32 v28, 1\n\ts_mov_b64 exec, 1\n\tbuffer_atomic_add v28, off, s[12:15], s70\n\ts_mov_b64 exec, -1\n\ts_mov_b32 s68, 0\n\t"
      "7:\n\ts_waitcnt vmcnt(19)\n\ts_barrier\n\tds_read_b64_tr_b16 a[160:161], v30 offset:0\n\tds_read_b64_tr_b16 a[162:163], v30 offset:1024\n\t"
      "ds_read_b64_tr_b16 a[164:165], v30 offset:4096\n\tds_read_b64_tr_b16 a[166:167], v30 offset:5120\n\tds_read_b64_tr_b16 a[168:169], v30 offset:8192\n\t"
      "ds_read_b64_tr_b16 a[170:171], v30 offset:9216\n\tds_read_b64_tr_b16 a[172:173], v30 offset:12288\n\tds_read_b64_tr_b16 a[174:175], v30 offset:13312\n\t"
      "s_waitcnt lgkmcnt(0)\n\ts_add_u32 m0, s51, 65536\n\ts_mov_b32 s48, 0x700000\n\tbuffer_load_dwordx4 v29, s[40:43], s48 offen  lds\n\ts_add_u32 m0, s51, 66560\n\t"
      "s_mov_b32 s48, 0x708000\n\tbuffer_load_dwordx4 v29, s[40:43], s48 offen  lds\n\ts_add_u32 m0, s51, 67584\n\ts_mov_b32 s48, 0x710000\n\t"
      "buffer_load_dwordx4 v29, s[40:43], s48 offen  lds\n\ts_add_u32 m0, s51, 68608\n\ts_mov_b32 s48, 0x718000\n\tbuffer_load_dwordx4 v29, s[40:43], s48 offen  lds\n\t"
      "s_cmp_lt_u32 s68, 1\n\ts_cbranch_scc1 7f\n\ts_cmp_eq_u32 s68, 2\n\ts_cbranch_scc1 6f\n\t"
      "5:\n\ts_buffer_load_dword s48, s[12:15], s69 glc\n\ts_waitcnt lgkmcnt(0)\n\ts_cmp_eq_u32 s48, s71\n\ts_cbranch_scc0 7f\n\t"
      "6:\n\tv_mov_b32 v28, 1\n\ts_mov_b64 exec, 1\n\tbuffer_atomic_add v28, off, s[12:15], s70\n\ts_mov_b64 exec, -1\n\ts_mov_b32 s68, 0\n\t"
      "7:\n\ts_waitcnt vmcnt(15)\n\ts_barrier\n\tds_read_b64_tr_b16 a[176:177], v30 offset:16384\n\tds_read_b64_tr_b16 a[178:179], v30 offset:17408\n\t"
      "ds_read_b64_tr_b16 a[180:181], v30 offset:20480\n\tds_read_b64_tr_b16 a[182:183], v30 offset:21504\n\tds_read_b64_tr_b16 a[184:185], v30 offset:24576\n\t"
      "ds_read_b64_tr_b16 a[186:187], v30 offset:25600\n\tds_read_b64_tr_b16 a[188:189], v30 offset:28672\n\tds_read_b64_tr_b16 a[190:191], v30 offset:29696\n\t"
      "s_waitcnt lgkmcnt(0)\n\ts_add_u32 m0, s51, 0\n\ts_mov_b32 s48, 0x780000\n\tbuffer_load_dwordx4 v29, s[40:43], s48 offen  lds\n\ts_add_u32 m0, s51, 1024\n\t"
      "s_mov_b32 s48, 0x788000\n\tbuffer_load_dwordx4 v29, s[40:43], s48 offen  lds\n\ts_add_u32 m0, s51, 2048\n\ts_mov_b32 s48, 0x790000\n\t"
      "buffer_load_dwordx4 v29, s[40:43], s48 offen  lds\n\ts_add_u32 m0, s51, 3072\n\ts_mov_b32 s48, 0x798000\n\tbuffer_load_dwordx4 v29, s[40:43], s48 offen  lds\n\t"
      "s_waitcnt vmcnt(12)\n\ts_barrier\n\tds_read_b64_tr_b16 a[192:193], v30 offset:32768\n\tds_read_b64_tr_b16 a[194:195], v30 offset:33792\n\t"
      "ds_read_b64_tr_b16 a[196:197], v30 offset:36864\n\tds_read_b64_tr_b16 a[198:199], v30 offset:37888\n\tds_read_b64_tr_b16 a[200:201], v30 offset:40960\n\t"
      "ds_read_b64_tr_b16 a[202:203], v30 offset:41984\n\tds_read_b64_tr_b16 a[204:205], v30 offset:45056\n\tds_read_b64_tr_b16 a[206:207], v30 offset:46080\n\t"
      "s_waitcnt lgkmcnt(0)\n\ts_waitcnt vmcnt(8)\n\ts_barrier\n\tds_read_b64_tr_b16 a[208:209], v30 offset:49152\n\tds_read_b64_tr_b16 a[210:211], v30 offset:50176\n\t"
      "ds_read_b64_tr_b16 a[212:213], v30 offset:53248\n\tds_read_b64_tr_b16 a[214:215], v30 offset:54272\n\tds_read_b64_tr_b16 a[216:217], v30 offset:57344\n\t"
      "ds_read_b64_tr_b16 a[218:219], v30 offset:58368\n\tds_read_b64_tr_b16 a[220:221], v30 offset:61440\n\tds_read_b64_tr_b16 a[222:223], v30 offset:62464\n\t"
      "s_waitcnt lgkmcnt(0)\n\ts_waitcnt vmcnt(4)\n\ts_barrier\n\tds_read_b64_tr_b16 a[224:225], v31 offset:0\n\tds_read_b64_tr_b16 a[226:227], v31 offset:1024\n\t"
      "ds_read_b64_tr_b16 a[228:229], v31 offset:4096\n\tds_read_b64_tr_b16 a[230:231], v31 offset:5120\n\tds_read_b64_tr_b16 a[232:233], v31 offset:8192\n\t"
      "ds_read_b64_tr_b16 a[234:235], v31 offset:9216\n\tds_read_b64_tr_b16 a[236:237], v31 offset:12288\n\tds_read_b64_tr_b16 a[238:239], v31 offset:13312\n\t"
      "s_waitcnt lgkmcnt(0)\n\ts_cmp_lt_u32 s68, 1\n\ts_cbranch_scc1 7f\n\ts_cmp_eq_u32 s68, 2\n\ts_cbranch_scc1 6f\n\t"
      "5:\n\ts_buffer_load_dword s48, s[12:15], s69 glc\n\ts_waitcnt lgkmcnt(0)\n\ts_cmp_eq_u32 s48, s71\n\ts_cbranch_scc1 6f\n\ts_cmp_eq_u32 s18, 0\n\ts_cbranch_scc1 6f\n\t"
      "s_sub_u32 s18, s18, 1\n\ts_branch 5b\n\t"
      "6:\n\tv_mov_b32 v28, 1\n\ts_mov_b64 exec, 1\n\tbuffer_atomic_add v28, off, s[12:15], s70\n\ts_mov_b64 exec, -1\n\ts_mov_b32 s68, 0\n\t"
      "7:\n\ts_waitcnt vmcnt(0)\n\ts_barrier\n\tds_read_b64_tr_b16 a[240:241], v30 offset:0\n\tds_read_b64_tr_b16 a[242:243], v30 offset:1024\n\t"
      "ds_read_b64_tr_b16 a[244:245], v30 offset:4096\n\tds_read_b64_tr_b16 a[246:247], v30 offset:5120\n\tds_read_b64_tr_b16 a[248:249], v30 offset:8192\n\t"
      "ds_read_b64_tr_b16 a[250:251], v30 offset:9216\n\tds_read_b64_tr_b16 a[252:253], v30 offset:12288\n\tds_read_b64_tr_b16 a[254:255], v30 offset:13312\n\t"
      "s_waitcnt lgkmcnt(0)\n\t"
      STS("s_memrealtime vcc\n\ts_waitcnt lgkmcnt(0)\n\tv_mov_b32 v95, vcc_lo\n\tv_mov_b32 v94, 161024\n\tds_write_b32 v94, v95\n\ts_memtime vcc\n\ts_waitcnt lgkmcnt(0)\n\tv_mov_b32 v95, vcc_lo\n\tds_write_b32 v94, v95 offset:400\n\ts_waitcnt lgkmcnt(0)\n\t")
      TKS("s_mov_b32 s91, 1\n\t")
      "buffer_load_dword v96, off, s[12:15], s63 sc1\n\ts_mov_b32 s23, 0\n\ts_mov_b32 s26, s27\n\ts_mov_b32 s25, 0\n\tv_mov_b32 v90, 0x40000000\n\tv_mov_b32 v91, 0x40000000\n\t"
      "v_mov_b32 v29, 0x40000000\n\tv_mov_b32 v88, v27\n\tv_mov_b32 v27, 0x40000000\n\tds_read_b128 v[64:67], v0 offset:0\n\tds_read_b128 v[68:71], v1 offset:0\n\t"
      "ds_read_b128 v[72:75], v2 offset:0\n\t"
      "10:\n\ts_waitcnt lgkmcnt(2)\n\tv_mfma_f32_32x32x16_bf16 v[32:47], a[0:3], v[64:67], 0\n\t"
      TK0
      "s_add_u32 s48, s17, 1\n\ts_and_b32 s48, s48, 63\n\ts_lshl_b32 s20, s48, 16\n\tds_read_b128 v[76:79], v3 offset:0\n\ts_waitcnt lgkmcnt(2)\n\t"
      "v_mfma_f32_32x32x16_bf16 v[32:47], a[4:7], v[68:71], v[32:47]\n\tds_read_b128 v[64:67], v4 offset:0\n\ts_barrier\n\tbuffer_load_dword v26, v25, s[44:47], s21 offen \n\t"
      "s_waitcnt lgkmcnt(2)\n\tv_mfma_f32_32x32x16_bf16 v[32:47], a[8:11], v[72:75], v[32:47]\n\tds_read_b128 v[68:71], v5 offset:0\n\ts_add_u32 m0, s50, 65536\n\t"
      "s_add_u32 s23, s23, s26\n\ts_cmp_gt_u32 s17, s23\n\ts_cbranch_scc1 24f\n\t"
      "14:\n\ts_waitcnt lgkmcnt(2)\n\tv_mfma_f32_32x32x16_bf16 v[32:47], a[12:15], v[76:79], v[32:47]\n\tds_read_b128 v[72:75], v6 offset:0\n\t"
      "buffer_load_dwordx4 v16, s[44:47], s20 offen offset:0  lds\n\ts_waitcnt lgkmcnt(2)\n\tv_mfma_f32_32x32x16_bf16 v[32:47], a[16:19], v[64:67], v[32:47]\n\t"
      "ds_read_b128 v[76:79], v7 offset:0\n\tv_cvt_pk_bf16_f32 v80, v48, v52\n\tv_cvt_pk_bf16_f32 v81, v56, v60\n\tv_cvt_pk_bf16_f32 v82, v49, v53\n\t"
      "v_cvt_pk_bf16_f32 v83, v57, v61\n\ts_waitcnt lgkmcnt(2)\n\tv_mfma_f32_32x32x16_bf16 v[32:47], a[20:23], v[68:71], v[32:47]\n\tds_read_b128 v[64:67], v0 offset:256\n\t"
      "buffer_load_dwordx4 v16, s[44:47], s20 offen offset:1024  lds\n\ts_waitcnt lgkmcnt(2)\n\tv_mfma_f32_32x32x16_bf16 v[32:47], a[24:27], v[72:75], v[32:47]\n\t"
      "ds_read_b128 v[68:71], v1 offset:256\n\tbuffer_store_dwordx4 v[80:83], v90, s[8:11], s19 offen offset:0 sc1\n\ts_waitcnt lgkmcnt(2)\n\t"
      "v_mfma_f32_32x32x16_bf16 v[32:47], a[28:31], v[76:79], v[32:47]\n\tds_read_b128 v[72:75], v2 offset:256\n\ts_add_u32 m0, s50, 67584\n\ts_waitcnt lgkmcnt(2)\n\t"
      "v_mfma_f32_32x32x16_bf16 v[32:47], a[32:35], v[64:67], v[32:47]\n\tds_read_b128 v[76:79], v3 offset:256\n\tbuffer_load_dwordx4 v17, s[44:47], s20 offen offset:0  lds\n\t"
      "s_waitcnt lgkmcnt(2)\n\tv_mfma_f32_32x32x16_bf16 v[32:47], a[36:39], v[68:71], v[32:47]\n\tds_read_b128 v[64:67], v4 offset:256\n\tv_cvt_pk_bf16_f32 v84, v50, v54\n\t"
      "v_cvt_pk_bf16_f32 v85, v58, v62\n\tv_cvt_pk_bf16_f32 v86, v51, v55\n\tv_cvt_pk_bf16_f32 v87, v59, v63\n\ts_waitcnt lgkmcnt(2)\n\t"
      "v_mfma_f32_32x32x16_bf16 v[32:47], a[40:43], v[72:75], v[32:47]\n\tds_read_b128 v[68:71], v5 offset:256\n\t"
      "buffer_load_dwordx4 v17, s[44:47], s20 offen offset:1024  lds\n\ts_waitcnt lgkmcnt(2)\n\tv_mfma_f32_32x32x16_bf16 v[32:47], a[44:47], v[76:79], v[32:47]\n\t"
      "ds_read_b128 v[72:75], v6 offset:256\n\tbuffer_store_dwordx4 v[84:87], v90, s[8:11], s19 offen offset:512 sc1\n\ts_waitcnt lgkmcnt(2)\n\t"
      "v_mfma_f32_32x32x16_bf16 v[32:47], a[48:51], v[64:67], v[32:47]\n\tds_read_b128 v[76:79], v7 offset:256\n\ts_add_u32 m0, s50, 69632\n\ts_waitcnt lgkmcnt(2)\n\t"
      "v_mfma_f32_32x32x16_bf16 v[32:47], a[52:55], v[68:71], v[32:47]\n\tds_read_b128 v[64:67], v0 offset:512\n\tbuffer_load_dwordx4 v18, s[44:47], s20 offen offset:0  lds\n\t"
      "s_waitcnt lgkmcnt(2)\n\tv_mfma_f32_32x32x16_bf16 v[32:47], a[56:59], v[72:75], v[32:47]\n\tds_read_b128 v[68:71], v1 offset:512\n\ts_sub_u32 s48, s17, s25\n\t"
      "v_mov_b32 v28, s48\n\ts_waitcnt lgkmcnt(2)\n\tv_mfma_f32_32x32x16_bf16 v[32:47], a[60:63], v[76:79], v[32:47]\n\tds_read_b128 v[72:75], v2 offset:512\n\t"
      "buffer_load_dwordx4 v18, s[44:47], s20 offen offset:1024  lds\n\ts_waitcnt lgkmcnt(2)\n\tv_mfma_f32_32x32x16_bf16 v[32:47], a[64:67], v[64:67], v[32:47]\n\t"
      "ds_read_b128 v[76:79], v3 offset:512\n\ts_waitcnt lgkmcnt(2)\n\tv_mfma_f32_32x32x16_bf16 v[32:47], a[68:71], v[68:71], v[32:47]\n\t"
      "ds_read_b128 v[64:67], v4 offset:512\n\ts_add_u32 m0, s50, 71680\n\ts_waitcnt lgkmcnt(2)\n\tv_mfma_f32_32x32x16_bf16 v[32:47], a[72:75], v[72:75], v[32:47]\n\t"
      "ds_read_b128 v[68:71], v5 offset:512\n\tbuffer_load_dwordx4 v19, s[44:47], s20 offen offset:0  lds\n\ts_waitcnt lgkmcnt(2)\n\t"
      "v_mfma_f32_32x32x16_bf16 v[32:47], a[76:79], v[76:79], v[32:47]\n\tds_read_b128 v[72:75], v6 offset:512\n\ts_waitcnt lgkmcnt(2)\n\t"
      "v_mfma_f32_32x32x16_bf16 v[32:47], a[80:83], v[64:67], v[32:47]\n\tds_read_b128 v[76:79], v7 offset:512\n\t"
      "buffer_load_dwordx4 v19, s[44:47], s20 offen offset:1024  lds\n\ts_waitcnt lgkmcnt(2)\n\tv_mfma_f32_32x32x16_bf16 v[32:47], a[84:87], v[68:71], v[32:47]\n\t"
      "ds_read_b128 v[64:67], v0 offset:768\n\ts_waitcnt lgkmcnt(2)\n\tv_mfma_f32_32x32x16_bf16 v[32:47], a[88:91], v[72:75], v[32:47]\n\t"
      "ds_read_b128 v[68:71], v1 offset:768\n\ts_add_u32 m0, s50, 73728\n\ts_waitcnt lgkmcnt(2)\n\tv_mfma_f32_32x32x16_bf16 v[32:47], a[92:95], v[76:79], v[32:47]\n\t"
      "ds_read_b128 v[72:75], v2 offset:768\n\tbuffer_load_dwordx4 v20, s[44:47], s20 offen offset:0  lds\n\ts_waitcnt lgkmcnt(2)\n\t"
      "v_mfma_f32_32x32x16_bf16 v[32:47], a[96:99], v[64:67], v[32:47]\n\tds_read_b128 v[76:79], v3 offset:768\n\ts_waitcnt lgkmcnt(2)\n\t"
      "v_mfma_f32_32x32x16_bf16 v[32:47], a[100:103], v[68:71], v[32:47]\n\tds_read_b128 v[64:67], v4 offset:768\n\t"
      "buffer_load_dwordx4 v20, s[44:47], s20 offen offset:1024  lds\n\ts_waitcnt lgkmcnt(2)\n\tv_mfma_f32_32x32x16_bf16 v[32:47], a[104:107], v[72:75], v[32:47]\n\t"
      "ds_read_b128 v[68:71], v5 offset:768\n\ts_waitcnt lgkmcnt(2)\n\tv_mfma_f32_32x32x16_bf16 v[32:47], a[108:111], v[76:79], v[32:47]\n\t"
      "ds_read_b128 v[72:75], v6 offset:768\n\ts_add_u32 m0, s50, 75776\n\ts_waitcnt lgkmcnt(2)\n\tv_mfma_f32_32x32x16_bf16 v[32:47], a[112:115], v[64:67], v[32:47]\n\t"
      "ds_read_b128 v[76:79], v7 offset:768\n\tbuffer_load_dwordx4 v21, s[44:47], s20 offen offset:0  lds\n\ts_waitcnt lgkmcnt(2)\n\t"
      "v_mfma_f32_32x32x16_bf16 v[32:47], a[116:119], v[68:71], v[32:47]\n\tds_read_b128 v[64:67], v0 offset:1024\n\ts_waitcnt lgkmcnt(2)\n\t"
      "v_mfma_f32_32x32x16_bf16 v[32:47], a[120:123], v[72:75], v[32:47]\n\tds_read_b128 v[68:71], v1 offset:1024\n\t"
      "buffer_load_dwordx4 v21, s[44:47], s20 offen offset:1024  lds\n\ts_waitcnt lgkmcnt(2)\n\tv_mfma_f32_32x32x16_bf16 v[32:47], a[124:127], v[76:79], v[32:47]\n\t"
      "ds_read_b128 v[72:75], v2 offset:1024\n\ts_waitcnt lgkmcnt(2)\n\tv_mfma_f32_32x32x16_bf16 v[32:47], a[128:131], v[64:67], v[32:47]\n\t"
      "ds_read_b128 v[76:79], v3 offset:1024\n\ts_add_u32 m0, s50, 77824\n\ts_waitcnt lgkmcnt(2)\n\tv_mfma_f32_32x32x16_bf16 v[32:47], a[132:135], v[68:71], v[32:47]\n\t"
      "ds_read_b128 v[64:67], v4 offset:1024\n\tbuffer_load_dwordx4 v22, s[44:47], s20 offen offset:0  lds\n\ts_waitcnt lgkmcnt(2)\n\t"
      "v_mfma_f32_32x32x16_bf16 v[32:47], a[136:139], v[72:75], v[32:47]\n\tds_read_b128 v[68:71], v5 offset:1024\n\ts_waitcnt lgkmcnt(2)\n\t"
      "v_mfma_f32_32x32x16_bf16 v[32:47], a[140:143], v[76:79], v[32:47]\n\tds_read_b128 v[72:75], v6 offset:1024\n\t"
      "buffer_load_dwordx4 v22, s[44:47], s20 offen offset:1024  lds\n\ts_waitcnt lgkmcnt(2)\n\tv_mfma_f32_32x32x16_bf16 v[32:47], a[144:147], v[64:67], v[32:47]\n\t"
      "ds_read_b128 v[76:79], v7 offset:1024\n\ts_waitcnt lgkmcnt(2)\n\tv_mfma_f32_32x32x16_bf16 v[32:47], a[148:151], v[68:71], v[32:47]\n\t"
      "ds_read_b128 v[64:67], v0 offset:1280\n\ts_add_u32 m0, s50, 79872\n\ts_waitcnt lgkmcnt(2)\n\tv_mfma_f32_32x32x16_bf16 v[32:47], a[152:155], v[72:75], v[32:47]\n\t"
      "ds_read_b128 v[68:71], v1 offset:1280\n\tbuffer_load_dwordx4 v23, s[44:47], s20 offen offset:0  lds\n\ts_waitcnt lgkmcnt(2)\n\t"
      "v_mfma_f32_32x32x16_bf16 v[32:47], a[156:159], v[76:79], v[32:47]\n\tds_read_b128 v[72:75], v2 offset:1280\n\ts_waitcnt lgkmcnt(2)\n\t"
      "v_mfma_f32_32x32x16_bf16 v[32:47], a[160:163], v[64:67], v[32:47]\n\tds_read_b128 v[76:79], v3 offset:1280\n\t"
      "buffer_load_dwordx4 v23, s[44:47], s20 offen offset:1024  lds\n\ts_waitcnt lgkmcnt(2)\n\tv_mfma_f32_32x32x16_bf16 v[32:47], a[164:167], v[68:71], v[32:47]\n\t"
      "ds_read_b128 v[64:67], v4 offset:1280\n\t"
      TK1
      "s_waitcnt vmcnt(12)\n\t"
      TK2
      "s_barrier\n\t"
      TK3
      "s_waitcnt lgkmcnt(2)\n\tv_mfma_f32_32x32x16_bf16 v[32:47], a[168:171], v[72:75], v[32:47]\n\tds_read_b128 v[68:71], v5 offset:1280\n\t"
      "buffer_store_dword v28, v27, s[8:11], s22 offen sc1\n\ts_waitcnt lgkmcnt(2)\n\tv_mfma_f32_32x32x16_bf16 v[32:47], a[172:175], v[76:79], v[32:47]\n\t"
      "ds_read_b128 v[72:75], v6 offset:1280\n\ts_waitcnt lgkmcnt(2)\n\tv_mfma_f32_32x32x16_bf16 v[32:47], a[176:179], v[64:67], v[32:47]\n\t"
      "ds_read_b128 v[76:79], v7 offset:1280\n\tbuffer_store_dwordx4 v[80:83], v91, s[8:11], s19 offen offset:0 sc1\n\ts_waitcnt lgkmcnt(2)\n\t"
      "v_mfma_f32_32x32x16_bf16 v[32:47], a[180:183], v[68:71], v[32:47]\n\tds_read_b128 v[64:67], v0 offset:1536\n\ts_waitcnt lgkmcnt(2)\n\t"
      "v_mfma_f32_32x32x16_bf16 v[32:47], a[184:187], v[72:75], v[32:47]\n\tds_read_b128 v[68:71], v1 offset:1536\n\t"
      "buffer_store_dwordx4 v[84:87], v91, s[8:11], s19 offen offset:512 sc1\n\ts_waitcnt lgkmcnt(2)\n\tv_mfma_f32_32x32x16_bf16 v[32:47], a[188:191], v[76:79], v[32:47]\n\t"
      "ds_read_b128 v[72:75], v2 offset:1536\n\ts_waitcnt lgkmcnt(2)\n\tv_mfma_f32_32x32x16_bf16 v[32:47], a[192:195], v[64:67], v[32:47]\n\t"
      "ds_read_b128 v[76:79], v3 offset:1536\n\ts_waitcnt lgkmcnt(2)\n\tv_mfma_f32_32x32x16_bf16 v[32:47], a[196:199], v[68:71], v[32:47]\n\t"
      "ds_read_b128 v[64:67], v4 offset:1536\n\ts_waitcnt lgkmcnt(2)\n\tv_mfma_f32_32x32x16_bf16 v[32:47], a[200:203], v[72:75], v[32:47]\n\t"
      "ds_read_b128 v[68:71], v5 offset:1536\n\ts_waitcnt lgkmcnt(2)\n\tv_mfma_f32_32x32x16_bf16 v[32:47], a[204:207], v[76:79], v[32:47]\n\t"
      "ds_read_b128 v[72:75], v6 offset:1536\n\ts_waitcnt lgkmcnt(2)\n\tv_mfma_f32_32x32x16_bf16 v[32:47], a[208:211], v[64:67], v[32:47]\n\t"
      "ds_read_b128 v[76:79], v7 offset:1536\n\ts_waitcnt lgkmcnt(2)\n\tv_mfma_f32_32x32x16_bf16 v[32:47], a[212:215], v[68:71], v[32:47]\n\t"
      "ds_read_b128 v[64:67], v0 offset:1792\n\ts_add_u32 s48, s17, 5\n\ts_min_u32 s48, s48, 63\n\ts_lshl_b32 s21, s48, 16\n\ts_waitcnt lgkmcnt(2)\n\t"
      "v_mfma_f32_32x32x16_bf16 v[32:47], a[216:219], v[72:75], v[32:47]\n\tds_read_b128 v[68:71], v1 offset:1792\n\ts_add_u32 s48, s19, 0x4000\n\t"
      "s_and_b32 s48, s48, 0x3c000\n\ts_andn2_b32 s19, s19, 0x3c000\n\ts_or_b32 s19, s19, s48\n\ts_waitcnt lgkmcnt(2)\n\t"
      "v_mfma_f32_32x32x16_bf16 v[32:47], a[220:223], v[76:79], v[32:47]\n\tds_read_b128 v[72:75], v2 offset:1792\n\ts_waitcnt lgkmcnt(2)\n\t"
      "v_mfma_f32_32x32x16_bf16 v[32:47], a[224:227], v[64:67], v[32:47]\n\tds_read_b128 v[76:79], v3 offset:1792\n\ts_cmp_ge_u32 s17, 16\n\ts_cselect_b32 s25, s24, 0\n\t"
      "s_lshl_b32 s48, s25, 30\n\tv_or_b32 v90, s48, v29\n\ts_waitcnt lgkmcnt(2)\n\tv_mfma_f32_32x32x16_bf16 v[32:47], a[228:231], v[68:71], v[32:47]\n\t"
      "ds_read_b128 v[64:67], v4 offset:1792\n\ts_xor_b32 s48, s48, 0x40000000\n\tv_or_b32 v91, s48, v29\n\ts_waitcnt lgkmcnt(2)\n\t"
      "v_mfma_f32_32x32x16_bf16 v[32:47], a[232:235], v[72:75], v[32:47]\n\tds_read_b128 v[68:71], v5 offset:1792\n\t"
      "s_mul_i32 s26, s17, s28\n\ts_lshr_b32 s26, s26, 6\n\ts_add_u32 s26, s26, s27\n\ts_waitcnt lgkmcnt(2)\n\t"
      "v_mfma_f32_32x32x16_bf16 v[32:47], a[236:239], v[76:79], v[32:47]\n\tds_read_b128 v[72:75], v6 offset:1792\n\ts_waitcnt lgkmcnt(2)\n\t"
      "v_mfma_f32_32x32x16_bf16 v[32:47], a[240:243], v[64:67], v[32:47]\n\tds_read_b128 v[76:79], v7 offset:1792\n\ts_waitcnt lgkmcnt(2)\n\t"
      "v_mfma_f32_32x32x16_bf16 v[32:47], a[244:247], v[68:71], v[32:47]\n\t"
      TK4
      "s_waitcnt vmcnt(3)\n\t"
      TK5
      "s_barrier\n\t"
      TK6
      "ds_read_b128 v[64:67], v8 offset:0\n\ts_waitcnt lgkmcnt(2)\n\tv_mfma_f32_32x32x16_bf16 v[32:47], a[248:251], v[72:75], v[32:47]\n\tds_read_b128 v[68:71], v9 offset:0\n\t"
      "v_readfirstlane_b32 s23, v89\n\ts_waitcnt lgkmcnt(2)\n\tv_mfma_f32_32x32x16_bf16 v[32:47], a[252:255], v[76:79], v[32:47]\n\tds_read_b128 v[72:75], v10 offset:0\n\t"
      "buffer_load_dword v89, off, s[8:11], s29 sc1\n\t"
      STF("s_memrealtime vcc\n\ts_waitcnt lgkmcnt(0)\n\tv_mov_b32 v95, vcc_lo\n\ts_lshl_b32 vcc_hi, s17, 2\n\ts_add_u32 vcc_hi, vcc_hi, 161056\n\tv_mov_b32 v94, vcc_hi\n\tds_write_b32 v94, v95\n\ts_memtime vcc\n\ts_waitcnt lgkmcnt(0)\n\tv_mov_b32 v95, vcc_lo\n\tds_write_b32 v94, v95 offset:400\n\ts_waitcnt lgkmcnt(0)\n\t")
      TK7
      TKS("s_waitcnt lgkmcnt(0)\n\ts_cmp_lt_i32 s17, 0\n\ts_cbranch_scc1 98f\n\ts_cmp_lg_u32 s16, s91\n\ts_cbranch_scc1 98f\n\ts_mov_b64 exec, 1\n\ts_lshl_b32 s48, s17, 4\n\ts_add_u32 s48, s48, 161056\n\tv_mov_b32 v92, s48\n\ts_sub_u32 s48, s74, s90\n\ts_min_u32 s48, s48, 0xffff\n\tv_mov_b32 v93, s48\n\tds_write_b16 v92, v93 offset:0\n\ts_sub_u32 s48, s76, s74\n\ts_min_u32 s48, s48, 0xffff\n\tv_mov_b32 v93, s48\n\tds_write_b16 v92, v93 offset:2\n\ts_sub_u32 s48, s78, s76\n\ts_min_u32 s48, s48, 0xffff\n\tv_mov_b32 v93, s48\n\tds_write_b16 v92, v93 offset:4\n\ts_sub_u32 s48, s80, s78\n\ts_min_u32 s48, s48, 0xffff\n\tv_mov_b32 v93, s48\n\tds_write_b16 v92, v93 offset:6\n\ts_sub_u32 s48, s82, s80\n\ts_min_u32 s48, s48, 0xffff\n\tv_mov_b32 v93, s48\n\tds_write_b16 v92, v93 offset:8\n\ts_sub_u32 s48, s84, s82\n\ts_min_u32 s48, s48, 0xffff\n\tv_mov_b32 v93, s48\n\tds_write_b16 v92, v93 offset:10\n\ts_sub_u32 s48, s86, s84\n\ts_min_u32 s48, s48, 0xffff\n\tv_mov_b32 v93, s48\n\tds_write_b16 v92, v93 offset:12\n\ts_sub_u32 s48, s88, s86\n\ts_min_u32 s48, s48, 0xffff\n\tv_mov_b32 v93, s48\n\tds_write_b16 v92, v93 offset:14\n\ts_mov_b64 exec, -1\n\t98:\n\ts_mov_b32 s90, s88\n\t")
      "s_add_u32 s17, s17, 1\n\ts_cmp_eq_u32 s17, 1\n\ts_cbranch_scc1 20f\n\t"
      "11:\n\ts_waitcnt lgkmcnt(2)\n\tv_mfma_f32_32x32x16_bf16 v[48:63], a[0:3], v[64:67], 0\n\t"
      TK0
      "s_add_u32 s48, s17, 1\n\ts_and_b32 s48, s48, 63\n\ts_lshl_b32 s20, s48, 16\n\tds_read_b128 v[76:79], v11 offset:0\n\ts_waitcnt lgkmcnt(2)\n\t"
      "v_mfma_f32_32x32x16_bf16 v[48:63], a[4:7], v[68:71], v[48:63]\n\tds_read_b128 v[64:67], v12 offset:0\n\ts_barrier\n\tbuffer_load_dword v26, v25, s[44:47], s21 offen \n\t"
      "s_waitcnt lgkmcnt(2)\n\tv_mfma_f32_32x32x16_bf16 v[48:63], a[8:11], v[72:75], v[48:63]\n\tds_read_b128 v[68:71], v13 offset:0\n\ts_add_u32 m0, s50, 0\n\t"
      "s_add_u32 s23, s23, s26\n\ts_cmp_gt_u32 s17, s23\n\ts_cbranch_scc1 25f\n\t"
      "15:\n\ts_waitcnt lgkmcnt(2)\n\tv_mfma_f32_32x32x16_bf16 v[48:63], a[12:15], v[76:79], v[48:63]\n\tds_read_b128 v[72:75], v14 offset:0\n\t"
      "buffer_load_dwordx4 v16, s[44:47], s20 offen offset:0  lds\n\ts_waitcnt lgkmcnt(2)\n\tv_mfma_f32_32x32x16_bf16 v[48:63], a[16:19], v[64:67], v[48:63]\n\t"
      "ds_read_b128 v[76:79], v15 offset:0\n\tv_cvt_pk_bf16_f32 v80, v32, v36\n\tv_cvt_pk_bf16_f32 v81, v40, v44\n\tv_cvt_pk_bf16_f32 v82, v33, v37\n\t"
      "v_cvt_pk_bf16_f32 v83, v41, v45\n\ts_waitcnt lgkmcnt(2)\n\tv_mfma_f32_32x32x16_bf16 v[48:63], a[20:23], v[68:71], v[48:63]\n\tds_read_b128 v[64:67], v8 offset:256\n\t"
      "buffer_load_dwordx4 v16, s[44:47], s20 offen offset:1024  lds\n\ts_waitcnt lgkmcnt(2)\n\tv_mfma_f32_32x32x16_bf16 v[48:63], a[24:27], v[72:75], v[48:63]\n\t"
      "ds_read_b128 v[68:71], v9 offset:256\n\tbuffer_store_dwordx4 v[80:83], v90, s[8:11], s19 offen offset:0 sc1\n\ts_waitcnt lgkmcnt(2)\n\t"
      "v_mfma_f32_32x32x16_bf16 v[48:63], a[28:31], v[76:79], v[48:63]\n\tds_read_b128 v[72:75], v10 offset:256\n\ts_add_u32 m0, s50, 2048\n\ts_waitcnt lgkmcnt(2)\n\t"
      "v_mfma_f32_32x32x16_bf16 v[48:63], a[32:35], v[64:67], v[48:63]\n\tds_read_b128 v[76:79], v11 offset:256\n\tbuffer_load_dwordx4 v17, s[44:47], s20 offen offset:0  lds\n\t"
      "s_waitcnt lgkmcnt(2)\n\tv_mfma_f32_32x32x16_bf16 v[48:63], a[36:39], v[68:71], v[48:63]\n\tds_read_b128 v[64:67], v12 offset:256\n\tv_cvt_pk_bf16_f32 v84, v34, v38\n\t"
      "v_cvt_pk_bf16_f32 v85, v42, v46\n\tv_cvt_pk_bf16_f32 v86, v35, v39\n\tv_cvt_pk_bf16_f32 v87, v43, v47\n\ts_waitcnt lgkmcnt(2)\n\t"
      "v_mfma_f32_32x32x16_bf16 v[48:63], a[40:43], v[72:75], v[48:63]\n\tds_read_b128 v[68:71], v13 offset:256\n\t"
      "buffer_load_dwordx4 v17, s[44:47], s20 offen offset:1024  lds\n\ts_waitcnt lgkmcnt(2)\n\tv_mfma_f32_32x32x16_bf16 v[48:63], a[44:47], v[76:79], v[48:63]\n\t"
      "ds_read_b128 v[72:75], v14 offset:256\n\tbuffer_store_dwordx4 v[84:87], v90, s[8:11], s19 offen offset:512 sc1\n\ts_waitcnt lgkmcnt(2)\n\t"
      "v_mfma_f32_32x32x16_bf16 v[48:63], a[48:51], v[64:67], v[48:63]\n\tds_read_b128 v[76:79], v15 offset:256\n\ts_add_u32 m0, s50, 4096\n\ts_waitcnt lgkmcnt(2)\n\t"
      "v_mfma_f32_32x32x16_bf16 v[48:63], a[52:55], v[68:71], v[48:63]\n\tds_read_b128 v[64:67], v8 offset:512\n\tbuffer_load_dwordx4 v18, s[44:47], s20 offen offset:0  lds\n\t"
      "s_waitcnt lgkmcnt(2)\n\tv_mfma_f32_32x32x16_bf16 v[48:63], a[56:59], v[72:75], v[48:63]\n\tds_read_b128 v[68:71], v9 offset:512\n\ts_sub_u32 s48, s17, s25\n\t"
      "v_mov_b32 v28, s48\n\ts_waitcnt lgkmcnt(2)\n\tv_mfma_f32_32x32x16_bf16 v[48:63], a[60:63], v[76:79], v[48:63]\n\tds_read_b128 v[72:75], v10 offset:512\n\t"
      "buffer_load_dwordx4 v18, s[44:47], s20 offen offset:1024  lds\n\ts_waitcnt lgkmcnt(2)\n\tv_mfma_f32_32x32x16_bf16 v[48:63], a[64:67], v[64:67], v[48:63]\n\t"
      "ds_read_b128 v[76:79], v11 offset:512\n\ts_waitcnt lgkmcnt(2)\n\tv_mfma_f32_32x32x16_bf16 v[48:63], a[68:71], v[68:71], v[48:63]\n\t"
      "ds_read_b128 v[64:67], v12 offset:512\n\ts_add_u32 m0, s50, 6144\n\ts_waitcnt lgkmcnt(2)\n\tv_mfma_f32_32x32x16_bf16 v[48:63], a[72:75], v[72:75], v[48:63]\n\t"
      "ds_read_b128 v[68:71], v13 offset:512\n\tbuffer_load_dwordx4 v19, s[44:47], s20 offen offset:0  lds\n\ts_waitcnt lgkmcnt(2)\n\t"
      "v_mfma_f32_32x32x16_bf16 v[48:63], a[76:79], v[76:79], v[48:63]\n\tds_read_b128 v[72:75], v14 offset:512\n\ts_waitcnt lgkmcnt(2)\n\t"
      "v_mfma_f32_32x32x16_bf16 v[48:63], a[80:83], v[64:67], v[48:63]\n\tds_read_b128 v[76:79], v15 offset:512\n\t"
      "buffer_load_dwordx4 v19, s[44:47], s20 offen offset:1024  lds\n\ts_waitcnt lgkmcnt(2)\n\tv_mfma_f32_32x32x16_bf16 v[48:63], a[84:87], v[68:71], v[48:63]\n\t"
      "ds_read_b128 v[64:67], v8 offset:768\n\ts_waitcnt lgkmcnt(2)\n\tv_mfma_f32_32x32x16_bf16 v[48:63], a[88:91], v[72:75], v[48:63]\n\t"
      "ds_read_b128 v[68:71], v9 offset:768\n\ts_add_u32 m0, s50, 8192\n\ts_waitcnt lgkmcnt(2)\n\tv_mfma_f32_32x32x16_bf16 v[48:63], a[92:95], v[76:79], v[48:63]\n\t"
      "ds_read_b128 v[72:75], v10 offset:768\n\tbuffer_load_dwordx4 v20, s[44:47], s20 offen offset:0  lds\n\ts_waitcnt lgkmcnt(2)\n\t"
      "v_mfma_f32_32x32x16_bf16 v[48:63], a[96:99], v[64:67], v[48:63]\n\tds_read_b128 v[76:79], v11 offset:768\n\ts_waitcnt lgkmcnt(2)\n\t"
      "v_mfma_f32_32x32x16_bf16 v[48:63], a[100:103], v[68:71], v[48:63]\n\tds_read_b128 v[64:67], v12 offset:768\n\t"
      "buffer_load_dwordx4 v20, s[44:47], s20 offen offset:1024  lds\n\ts_waitcnt lgkmcnt(2)\n\tv_mfma_f32_32x32x16_bf16 v[48:63], a[104:107], v[72:75], v[48:63]\n\t"
      "ds_read_b128 v[68:71], v13 offset:768\n\ts_waitcnt lgkmcnt(2)\n\tv_mfma_f32_32x32x16_bf16 v[48:63], a[108:111], v[76:79], v[48:63]\n\t"
      "ds_read_b128 v[72:75], v14 offset:768\n\ts_add_u32 m0, s50, 10240\n\ts_waitcnt lgkmcnt(2)\n\tv_mfma_f32_32x32x16_bf16 v[48:63], a[112:115], v[64:67], v[48:63]\n\t"
      "ds_read_b128 v[76:79], v15 offset:768\n\tbuffer_load_dwordx4 v21, s[44:47], s20 offen offset:0  lds\n\ts_waitcnt lgkmcnt(2)\n\t"
      "v_mfma_f32_32x32x16_bf16 v[48:63], a[116:119], v[68:71], v[48:63]\n\tds_read_b128 v[64:67], v8 offset:1024\n\ts_waitcnt lgkmcnt(2)\n\t"
      "v_mfma_f32_32x32x16_bf16 v[48:63], a[120:123], v[72:75], v[48:63]\n\tds_read_b128 v[68:71], v9 offset:1024\n\t"
      "buffer_load_dwordx4 v21, s[44:47], s20 offen offset:1024  lds\n\ts_waitcnt lgkmcnt(2)\n\tv_mfma_f32_32x32x16_bf16 v[48:63], a[124:127], v[76:79], v[48:63]\n\t"
      "ds_read_b128 v[72:75], v10 offset:1024\n\ts_waitcnt lgkmcnt(2)\n\tv_mfma_f32_32x32x16_bf16 v[48:63], a[128:131], v[64:67], v[48:63]\n\t"
      "ds_read_b128 v[76:79], v11 offset:1024\n\ts_add_u32 m0, s50, 12288\n\ts_waitcnt lgkmcnt(2)\n\tv_mfma_f32_32x32x16_bf16 v[48:63], a[132:135], v[68:71], v[48:63]\n\t"
      "ds_read_b128 v[64:67], v12 offset:1024\n\tbuffer_load_dwordx4 v22, s[44:47], s20 offen offset:0  lds\n\ts_waitcnt lgkmcnt(2)\n\t"
      "v_mfma_f32_32x32x16_bf16 v[48:63], a[136:139], v[72:75], v[48:63]\n\tds_read_b128 v[68:71], v13 offset:1024\n\ts_waitcnt lgkmcnt(2)\n\t"
      "v_mfma_f32_32x32x16_bf16 v[48:63], a[140:143], v[76:79], v[48:63]\n\tds_read_b128 v[72:75], v14 offset:1024\n\t"
      "buffer_load_dwordx4 v22, s[44:47], s20 offen offset:1024  lds\n\ts_waitcnt lgkmcnt(2)\n\tv_mfma_f32_32x32x16_bf16 v[48:63], a[144:147], v[64:67], v[48:63]\n\t"
      "ds_read_b128 v[76:79], v15 offset:1024\n\ts_waitcnt lgkmcnt(2)\n\tv_mfma_f32_32x32x16_bf16 v[48:63], a[148:151], v[68:71], v[48:63]\n\t"
      "ds_read_b128 v[64:67], v8 offset:1280\n\ts_add_u32 m0, s50, 14336\n\ts_waitcnt lgkmcnt(2)\n\tv_mfma_f32_32x32x16_bf16 v[48:63], a[152:155], v[72:75], v[48:63]\n\t"
      "ds_read_b128 v[68:71], v9 offset:1280\n\tbuffer_load_dwordx4 v23, s[44:47], s20 offen offset:0  lds\n\ts_waitcnt lgkmcnt(2)\n\t"
      "v_mfma_f32_32x32x16_bf16 v[48:63], a[156:159], v[76:79], v[48:63]\n\tds_read_b128 v[72:75], v10 offset:1280\n\ts_waitcnt lgkmcnt(2)\n\t"
      "v_mfma_f32_32x32x16_bf16 v[48:63], a[160:163], v[64:67], v[48:63]\n\tds_read_b128 v[76:79], v11 offset:1280\n\t"
      "buffer_load_dwordx4 v23, s[44:47], s20 offen offset:1024  lds\n\ts_waitcnt lgkmcnt(2)\n\tv_mfma_f32_32x32x16_bf16 v[48:63], a[164:167], v[68:71], v[48:63]\n\t"
      "ds_read_b128 v[64:67], v12 offset:1280\n\t"
      TK1
      "s_waitcnt vmcnt(12)\n\t"
      TK2
      "s_barrier\n\t"
      TK3
      "s_waitcnt lgkmcnt(2)\n\tv_mfma_f32_32x32x16_bf16 v[48:63], a[168:171], v[72:75], v[48:63]\n\tds_read_b128 v[68:71], v13 offset:1280\n\t"
      "buffer_store_dword v28, v27, s[8:11], s22 offen sc1\n\ts_waitcnt lgkmcnt(2)\n\tv_mfma_f32_32x32x16_bf16 v[48:63], a[172:175], v[76:79], v[48:63]\n\t"
      "ds_read_b128 v[72:75], v14 offset:1280\n\ts_waitcnt lgkmcnt(2)\n\tv_mfma_f32_32x32x16_bf16 v[48:63], a[176:179], v[64:67], v[48:63]\n\t"
      "ds_read_b128 v[76:79], v15 offset:1280\n\tbuffer_store_dwordx4 v[80:83], v91, s[8:11], s19 offen offset:0 sc1\n\ts_waitcnt lgkmcnt(2)\n\t"
      "v_mfma_f32_32x32x16_bf16 v[48:63], a[180:183], v[68:71], v[48:63]\n\tds_read_b128 v[64:67], v8 offset:1536\n\ts_waitcnt lgkmcnt(2)\n\t"
      "v_mfma_f32_32x32x16_bf16 v[48:63], a[184:187], v[72:75], v[48:63]\n\tds_read_b128 v[68:71], v9 offset:1536\n\t"
      "buffer_store_dwordx4 v[84:87], v91, s[8:11], s19 offen offset:512 sc1\n\ts_waitcnt lgkmcnt(2)\n\tv_mfma_f32_32x32x16_bf16 v[48:63], a[188:191], v[76:79], v[48:63]\n\t"
      "ds_read_b128 v[72:75], v10 offset:1536\n\ts_waitcnt lgkmcnt(2)\n\tv_mfma_f32_32x32x16_bf16 v[48:63], a[192:195], v[64:67], v[48:63]\n\t"
      "ds_read_b128 v[76:79], v11 offset:1536\n\ts_waitcnt lgkmcnt(2)\n\tv_mfma_f32_32x32x16_bf16 v[48:63], a[196:199], v[68:71], v[48:63]\n\t"
      "ds_read_b128 v[64:67], v12 offset:1536\n\ts_waitcnt lgkmcnt(2)\n\tv_mfma_f32_32x32x16_bf16 v[48:63], a[200:203], v[72:75], v[48:63]\n\t"
      "ds_read_b128 v[68:71], v13 offset:1536\n\ts_waitcnt lgkmcnt(2)\n\tv_mfma_f32_32x32x16_bf16 v[48:63], a[204:207], v[76:79], v[48:63]\n\t"
      "ds_read_b128 v[72:75], v14 offset:1536\n\ts_waitcnt lgkmcnt(2)\n\tv_mfma_f32_32x32x16_bf16 v[48:63], a[208:211], v[64:67], v[48:63]\n\t"
      "ds_read_b128 v[76:79], v15 offset:1536\n\ts_waitcnt lgkmcnt(2)\n\tv_mfma_f32_32x32x16_bf16 v[48:63], a[212:215], v[68:71], v[48:63]\n\t"
      "ds_read_b128 v[64:67], v8 offset:1792\n\ts_add_u32 s48, s17, 5\n\ts_min_u32 s48, s48, 63\n\ts_lshl_b32 s21, s48, 16\n\ts_waitcnt lgkmcnt(2)\n\t"
      "v_mfma_f32_32x32x16_bf16 v[48:63], a[216:219], v[72:75], v[48:63]\n\tds_read_b128 v[68:71], v9 offset:1792\n\ts_add_u32 s48, s19, 0x4000\n\t"
      "s_and_b32 s48, s48, 0x3c000\n\ts_andn2_b32 s19, s19, 0x3c000\n\ts_or_b32 s19, s19, s48\n\ts_waitcnt lgkmcnt(2)\n\t"
      "v_mfma_f32_32x32x16_bf16 v[48:63], a[220:223], v[76:79], v[48:63]\n\tds_read_b128 v[72:75], v10 offset:1792\n\tv_mov_b32 v29, v24\n\ts_waitcnt lgkmcnt(2)\n\t"
      "v_mfma_f32_32x32x16_bf16 v[48:63], a[224:227], v[64:67], v[48:63]\n\tds_read_b128 v[76:79], v11 offset:1792\n\ts_cmp_ge_u32 s17, 16\n\ts_cselect_b32 s25, s24, 0\n\t"
      "s_lshl_b32 s48, s25, 30\n\tv_or_b32 v90, s48, v29\n\ts_waitcnt lgkmcnt(2)\n\tv_mfma_f32_32x32x16_bf16 v[48:63], a[228:231], v[68:71], v[48:63]\n\t"
      "ds_read_b128 v[64:67], v12 offset:1792\n\ts_xor_b32 s48, s48, 0x40000000\n\tv_or_b32 v91, s48, v29\n\ts_waitcnt lgkmcnt(2)\n\t"
      "v_mfma_f32_32x32x16_bf16 v[48:63], a[232:235], v[72:75], v[48:63]\n\tds_read_b128 v[68:71], v13 offset:1792\n\t"
      "s_mul_i32 s26, s17, s28\n\ts_lshr_b32 s26, s26, 6\n\ts_add_u32 s26, s26, s27\n\ts_waitcnt lgkmcnt(2)\n\t"
      "v_mfma_f32_32x32x16_bf16 v[48:63], a[236:239], v[76:79], v[48:63]\n\tds_read_b128 v[72:75], v14 offset:1792\n\ts_waitcnt lgkmcnt(2)\n\t"
      "v_mfma_f32_32x32x16_bf16 v[48:63], a[240:243], v[64:67], v[48:63]\n\tds_read_b128 v[76:79], v15 offset:1792\n\ts_waitcnt lgkmcnt(2)\n\t"
      "v_mfma_f32_32x32x16_bf16 v[48:63], a[244:247], v[68:71], v[48:63]\n\t"
      TK4
      "s_waitcnt vmcnt(3)\n\t"
      TK5
      "s_barrier\n\t"
      TK6
      "ds_read_b128 v[64:67], v0 offset:0\n\ts_waitcnt lgkmcnt(2)\n\tv_mfma_f32_32x32x16_bf16 v[48:63], a[248:251], v[72:75], v[48:63]\n\tds_read_b128 v[68:71], v1 offset:0\n\t"
      "v_readfirstlane_b32 s23, v89\n\ts_waitcnt lgkmcnt(2)\n\tv_mfma_f32_32x32x16_bf16 v[48:63], a[252:255], v[76:79], v[48:63]\n\tds_read_b128 v[72:75], v2 offset:0\n\t"
      "buffer_load_dword v89, off, s[8:11], s29 sc1\n\t"
      STF("s_memrealtime vcc\n\ts_waitcnt lgkmcnt(0)\n\tv_mov_b32 v95, vcc_lo\n\ts_lshl_b32 vcc_hi, s17, 2\n\ts_add_u32 vcc_hi, vcc_hi, 161056\n\tv_mov_b32 v94, vcc_hi\n\tds_write_b32 v94, v95\n\ts_memtime vcc\n\ts_waitcnt lgkmcnt(0)\n\tv_mov_b32 v95, vcc_lo\n\tds_write_b32 v94, v95 offset:400\n\ts_waitcnt lgkmcnt(0)\n\t")
      TK7
      TKS("s_waitcnt lgkmcnt(0)\n\ts_cmp_lt_i32 s17, 0\n\ts_cbranch_scc1 98f\n\ts_cmp_lg_u32 s16, s91\n\ts_cbranch_scc1 98f\n\ts_mov_b64 exec, 1\n\ts_lshl_b32 s48, s17, 4\n\ts_add_u32 s48, s48, 161056\n\tv_mov_b32 v92, s48\n\ts_sub_u32 s48, s74, s90\n\ts_min_u32 s48, s48, 0xffff\n\tv_mov_b32 v93, s48\n\tds_write_b16 v92, v93 offset:0\n\ts_sub_u32 s48, s76, s74\n\ts_min_u32 s48, s48, 0xffff\n\tv_mov_b32 v93, s48\n\tds_write_b16 v92, v93 offset:2\n\ts_sub_u32 s48, s78, s76\n\ts_min_u32 s48, s48, 0xffff\n\tv_mov_b32 v93, s48\n\tds_write_b16 v92, v93 offset:4\n\ts_sub_u32 s48, s80, s78\n\ts_min_u32 s48, s48, 0xffff\n\tv_mov_b32 v93, s48\n\tds_write_b16 v92, v93 offset:6\n\ts_sub_u32 s48, s82, s80\n\ts_min_u32 s48, s48, 0xffff\n\tv_mov_b32 v93, s48\n\tds_write_b16 v92, v93 offset:8\n\ts_sub_u32 s48, s84, s82\n\ts_min_u32 s48, s48, 0xffff\n\tv_mov_b32 v93, s48\n\tds_write_b16 v92, v93 offset:10\n\ts_sub_u32 s48, s86, s84\n\ts_min_u32 s48, s48, 0xffff\n\tv_mov_b32 v93, s48\n\tds_write_b16 v92, v93 offset:12\n\ts_sub_u32 s48, s88, s86\n\ts_min_u32 s48, s48, 0xffff\n\tv_mov_b32 v93, s48\n\tds_write_b16 v92, v93 offset:14\n\ts_mov_b64 exec, -1\n\t98:\n\ts_mov_b32 s90, s88\n\t")
      "s_add_u32 s17, s17, 1\n\ts_cmp_lt_u32 s17, 64\n\ts_cbranch_scc1 10b\n\ts_branch 26f\n\t"
      "16:\n\ts_nop 11\n\tv_cvt_pk_bf16_f32 v80, v48, v52\n\tv_cvt_pk_bf16_f32 v81, v56, v60\n\tv_cvt_pk_bf16_f32 v82, v49, v53\n\tv_cvt_pk_bf16_f32 v83, v57, v61\n\t"
      "v_cvt_pk_bf16_f32 v84, v50, v54\n\tv_cvt_pk_bf16_f32 v85, v58, v62\n\tv_cvt_pk_bf16_f32 v86, v51, v55\n\tv_cvt_pk_bf16_f32 v87, v59, v63\n\t"
      "buffer_store_dwordx4 v[80:83], v24, s[8:11], s19 offen offset:0 sc1\n\tbuffer_store_dwordx4 v[84:87], v24, s[8:11], s19 offen offset:512 sc1\n\tv_mov_b32 v28, 64\n\t"
      "s_waitcnt vmcnt(0) lgkmcnt(0)\n\ts_barrier\n\tbuffer_store_dword v28, v27, s[8:11], s22 offen sc1\n\ts_branch 30f\n\t"
      "24:\n\tbuffer_load_dword v89, off, s[8:11], s29 sc1\n\ts_waitcnt vmcnt(0)\n\tv_readfirstlane_b32 s23, v89\n\ts_add_u32 s23, s23, s26\n\ts_cmp_le_u32 s17, s23\n\t"
      "s_cbranch_scc1 14b\n\ts_cmp_eq_u32 s18, 0\n\ts_cbranch_scc1 14b\n\ts_sub_u32 s18, s18, 1\n\ts_sleep 8\n\ts_branch 24b\n\t"
      "25:\n\tbuffer_load_dword v89, off, s[8:11], s29 sc1\n\ts_waitcnt vmcnt(0)\n\tv_readfirstlane_b32 s23, v89\n\ts_add_u32 s23, s23, s26\n\ts_cmp_le_u32 s17, s23\n\t"
      "s_cbranch_scc1 15b\n\ts_cmp_eq_u32 s18, 0\n\ts_cbranch_scc1 15b\n\ts_sub_u32 s18, s18, 1\n\ts_sleep 8\n\ts_branch 25b\n\t"
      "26:\n\tbuffer_load_dword v89, off, s[8:11], s29 sc1\n\ts_waitcnt vmcnt(0)\n\tv_readfirstlane_b32 s23, v89\n\ts_add_u32 s23, s23, s26\n\ts_cmp_le_u32 s17, s23\n\t"
      "s_cbranch_scc1 16b\n\ts_cmp_eq_u32 s18, 0\n\ts_cbranch_scc1 16b\n\ts_sub_u32 s18, s18, 1\n\ts_sleep 8\n\ts_branch 26b\n\t"
      "20:\n\ts_branch 5f\n\t"
      "1:\n\tbuffer_load_dword v96, off, s[12:15], s63 sc1\n\ts_waitcnt vmcnt(0)\n\t"
      "5:\n\tv_readfirstlane_b32 s49, v96\n\ts_bitcmp1_b32 s49, 0\n\ts_cbranch_scc1 2f\n\ts_cmp_eq_u32 s18, 0\n\ts_cbranch_scc1 2f\n\ts_sub_u32 s18, s18, 1\n\ts_sleep 2\n\t"
      "s_branch 1b\n\t"
      "2:\n\ts_and_b32 s48, s49, 3\n\ts_cmp_lg_u32 s48, 3\n\ts_cbranch_scc1 4f\n\ts_cmp_eq_u32 s62, 0\n\ts_cbranch_scc1 4f\n\ts_waitcnt vmcnt(0) lgkmcnt(0)\n\ts_barrier\n\t"
      "s_mov_b64 s[0:1], s[60:61]\n\ts_mov_b32 s2, s62\n\ts_lshl_b32 s48, s16, 6\n\tv_mbcnt_lo_u32_b32 v0, -1, 0\n\tv_mbcnt_hi_u32_b32 v0, -1, v0\n\tv_add_u32 v0, s48, v0\n\t"
      "s_getpc_b64 s[4:5]\n\t"
      "3:\n\ts_sub_u32 s4, s4, 3b-mk_mega\n\ts_subb_u32 s5, s5, 0\n\ts_setpc_b64 s[4:5]\n\t"
      "4:\n\ts_lshl_b32 s48, s49, 10\n\ts_and_b32 s48, s48, 0x2000\n\ts_xor_b32 s22, s22, s48\n\ts_xor_b32 s29, s29, s48\n\ts_lshl_b32 s49, s49, 11\n\t"
      "s_and_b32 s49, s49, 0x2000\n\ts_xor_b32 s19, s19, s49\n\ts_nop 11\n\tv_cvt_pk_bf16_f32 v80, v32, v36\n\tv_cvt_pk_bf16_f32 v81, v40, v44\n\t"
      "v_cvt_pk_bf16_f32 v82, v33, v37\n\tv_cvt_pk_bf16_f32 v83, v41, v45\n\tv_cvt_pk_bf16_f32 v84, v34, v38\n\tv_cvt_pk_bf16_f32 v85, v42, v46\n\t"
      "v_cvt_pk_bf16_f32 v86, v35, v39\n\tv_cvt_pk_bf16_f32 v87, v43, v47\n\tbuffer_store_dwordx4 v[80:83], v24, s[8:11], s19 offen offset:0 sc1\n\t"
      "buffer_store_dwordx4 v[84:87], v24, s[8:11], s19 offen offset:512 sc1\n\ts_mov_b32 s23, 0\n\tv_mov_b32 v27, v88\n\tv_mov_b32 v28, 1\n\ts_waitcnt vmcnt(0)\n\t"
      "v_mov_b32 v89, 0\n\ts_barrier\n\tbuffer_store_dword v28, v27, s[8:11], s22 offen sc1\n\ts_branch 11b\n\t"
      "30:\n\t"
      ".Lmk_end2:\n\ts_waitcnt vmcnt(0) lgkmcnt(0)\n\t"
      TKS("s_mul_i32 s48, s16, 72\n\ts_add_u32 s48, s48, 162080\n\tv_mov_b32 v224, s48\n\tds_read_b32 v225, v224 offset:0\n\ts_waitcnt lgkmcnt(0)\n\tv_readfirstlane_b32 s74, v225\n\tds_read_b32 v225, v224 offset:4\n\ts_waitcnt lgkmcnt(0)\n\tv_readfirstlane_b32 s75, v225\n\tds_read_b32 v225, v224 offset:8\n\ts_waitcnt lgkmcnt(0)\n\tv_readfirstlane_b32 s76, v225\n\tds_read_b32 v225, v224 offset:12\n\ts_waitcnt lgkmcnt(0)\n\tv_readfirstlane_b32 s77, v225\n\tds_read_b32 v225, v224 offset:16\n\ts_waitcnt lgkmcnt(0)\n\tv_readfirstlane_b32 s78, v225\n\tds_read_b32 v225, v224 offset:20\n\ts_waitcnt lgkmcnt(0)\n\tv_readfirstlane_b32 s79, v225\n\tds_read_b32 v225, v224 offset:24\n\ts_waitcnt lgkmcnt(0)\n\tv_readfirstlane_b32 s80, v225\n\tds_read_b32 v225, v224 offset:28\n\ts_waitcnt lgkmcnt(0)\n\tv_readfirstlane_b32 s81, v225\n\tds_read_b32 v225, v224 offset:32\n\ts_waitcnt lgkmcnt(0)\n\tv_readfirstlane_b32 s82, v225\n\tds_read_b32 v225, v224 offset:36\n\ts_waitcnt lgkmcnt(0)\n\tv_readfirstlane_b32 s83, v225\n\tds_read_b32 v225, v224 offset:40\n\ts_waitcnt lgkmcnt(0)\n\tv_readfirstlane_b32 s84, v225\n\tds_read_b32 v225, v224 offset:44\n\ts_waitcnt lgkmcnt(0)\n\tv_readfirstlane_b32 s85, v225\n\tds_read_b32 v225, v224 offset:48\n\ts_waitcnt lgkmcnt(0)\n\tv_readfirstlane_b32 s86, v225\n\tds_read_b32 v225, v224 offset:52\n\ts_waitcnt lgkmcnt(0)\n\tv_readfirstlane_b32 s87, v225\n\tds_read_b32 v225, v224 offset:56\n\ts_waitcnt lgkmcnt(0)\n\tv_readfirstlane_b32 s88, v225\n\tds_read_b32 v225, v224 offset:60\n\ts_waitcnt lgkmcnt(0)\n\tv_readfirstlane_b32 s89, v225\n\tds_read_b32 v225, v224 offset:64\n\ts_waitcnt lgkmcnt(0)\n\tv_readfirstlane_b32 s90, v225\n\tds_read_b32 v225, v224 offset:68\n\ts_waitcnt lgkmcnt(0)\n\tv_readfirstlane_b32 s91, v225\n\t")
      STS("s_lshl_b32 s48, s16, 2\n\ts_add_u32 s48, s48, 162944\n\tv_mov_b32 v0, s48\n\tv_mov_b32 v1, s50\n\tds_write_b32 v0, v1\n\ts_cmp_lg_u32 s16, 0\n\ts_cbranch_scc1 97f\n\tv_mov_b32 v1, s51\n\tds_write_b32 v0, v1 offset:16\n\t97:\n\ts_cmp_lg_u32 s16, 0\n\ts_cbranch_scc1 95f\n\tv_mov_b32 v1, s61\n\tds_write_b32 v0, v1 offset:20\n\tv_mov_b32 v1, s21\n\tds_write_b32 v0, v1 offset:24\n\tv_mov_b32 v1, s22\n\tds_write_b32 v0, v1 offset:28\n\t95:\n\t")
      "s_cmp_lg_u32 s18, 0\n\ts_cbranch_scc1 96f\n\tv_mov_b32 v1, 1\n\tbuffer_store_dword v1, off, s[12:15], 0 offset:3328 sc1\n\t"
      "96:\n\t"
      : "+{v[0:31]}"(k), "+{v[160:191]}"(s0), "+{v[192:223]}"(s1), ALL_REGS :: "memory", "vcc", "scc", S_CLOB TK_CLOB);
}

DEV void zc_role(Ctx& c, int h) {
  const KArg p = enter(c);
  c.budget = SPIN_CAP;
  const int w = c.w, lane = fresh_lane(), g = lane >> 4, h2 = lane >> 5, q = (lane >> 2) & 3, pp = lane & 3;
  v32u k;
#pragma unroll
  for (int i = 0; i < 32; ++i) k[i] = 0;
  k[0] = lane * 16 + (w && (HACK & 64) ? 1 << 30 : 0);
  k[1] = (16 * w + (g & 1)) * 65536 + amap(h * 1024, w >> 1) + h2 * 256 + (lane & 15) * 16 + ((HACK & 2) << 29);
  k[6] = w ? 1 << 30 : lane == 0 ? R_FV + h * 128 : R_FP + (h >> 3) * 128;
  k[3] = lane < 4 ? lane * 32768 : 1 << 30;
  k[14] = TLBW ? R_Z + min(lane, 31) * 4 * MB + w * MB + h * 128 : 1 << 30;
#pragma unroll
  for (int i = 0; i < 2; ++i) k[8 + i] = (head(h) * 128 + 16 * h2) * 2048 + w * 512 + (((lane & 31) ^ ((2 * i + h2) << 2)) << 4);
#pragma unroll
  for (int i = 0; i < 4; ++i) k[10 + i] = w * 32768 + (8 * h2 + q) * 512 + (((4 * i + 2 * (g & 1) + (pp >> 1)) ^ (q << 2)) << 4) + 8 * (pp & 1);
  v32u s0 = {RSRCS, (unsigned)w, 0, c.budget, (unsigned)(R_VX + hblock(h, 8192)), R_Z, 0, (unsigned)(R_FZ + amap(w * 4096 + h * 128, 0))};
  v32u s1 = {plo(p->Wo), phi(p->Wo), 1024 * 4096 * 2, 0x00020000, 0, 0, 0, 0, 0, 0, 0, (unsigned)(w * 32768), 0, 0, 0, 0, 0, 0, 0, 0, ENTRY, 0, 0, 0, 0, HELLO};
  v32u wa[8], wv[5];
  asm volatile(
      "s_nop 0\n\tv_readfirstlane_b32 s8, v160\n\tv_readfirstlane_b32 s9, v161\n\tv_readfirstlane_b32 s10, v162\n\tv_readfirstlane_b32 s11, v163\n\t"
      "v_readfirstlane_b32 s12, v164\n\tv_readfirstlane_b32 s13, v165\n\tv_readfirstlane_b32 s14, v166\n\tv_readfirstlane_b32 s15, v167\n\tv_readfirstlane_b32 s16, v168\n\t"
      "v_readfirstlane_b32 s17, v169\n\tv_readfirstlane_b32 s18, v170\n\tv_readfirstlane_b32 s19, v171\n\tv_readfirstlane_b32 s20, v172\n\tv_readfirstlane_b32 s21, v173\n\t"
      "v_readfirstlane_b32 s22, v174\n\tv_readfirstlane_b32 s40, v192\n\tv_readfirstlane_b32 s41, v193\n\tv_readfirstlane_b32 s42, v194\n\tv_readfirstlane_b32 s43, v195\n\t"
      "v_readfirstlane_b32 s51, v203\n\tv_readfirstlane_b32 s60, v212\n\tv_readfirstlane_b32 s61, v213\n\tv_readfirstlane_b32 s62, v214\n\tv_readfirstlane_b32 s63, v215\n\t"
      "v_readfirstlane_b32 s68, v220\n\tv_readfirstlane_b32 s69, v221\n\tv_readfirstlane_b32 s70, v222\n\ts_nop 4\n\t"
      TKS("s_mul_i32 s48, s16, 72\n\ts_add_u32 s48, s48, 162080\n\tv_mov_b32 v224, s48\n\tv_mov_b32 v225, s74\n\tds_write_b32 v224, v225 offset:0\n\tv_mov_b32 v225, s75\n\tds_write_b32 v224, v225 offset:4\n\tv_mov_b32 v225, s76\n\tds_write_b32 v224, v225 offset:8\n\tv_mov_b32 v225, s77\n\tds_write_b32 v224, v225 offset:12\n\tv_mov_b32 v225, s78\n\tds_write_b32 v224, v225 offset:16\n\tv_mov_b32 v225, s79\n\tds_write_b32 v224, v225 offset:20\n\tv_mov_b32 v225, s80\n\tds_write_b32 v224, v225 offset:24\n\tv_mov_b32 v225, s81\n\tds_write_b32 v224, v225 offset:28\n\tv_mov_b32 v225, s82\n\tds_write_b32 v224, v225 offset:32\n\tv_mov_b32 v225, s83\n\tds_write_b32 v224, v225 offset:36\n\tv_mov_b32 v225, s84\n\tds_write_b32 v224, v225 offset:40\n\tv_mov_b32 v225, s85\n\tds_write_b32 v224, v225 offset:44\n\tv_mov_b32 v225, s86\n\tds_write_b32 v224, v225 offset:48\n\tv_mov_b32 v225, s87\n\tds_write_b32 v224, v225 offset:52\n\tv_mov_b32 v225, s88\n\tds_write_b32 v224, v225 offset:56\n\tv_mov_b32 v225, s89\n\tds_write_b32 v224, v225 offset:60\n\tv_mov_b32 v225, s90\n\tds_write_b32 v224, v225 offset:64\n\tv_mov_b32 v225, s91\n\tds_write_b32 v224, v225 offset:68\n\t")
      "s_buffer_load_dword s71, s[12:15], s69 glc\n\ts_add_u32 m0, s51, 0\n\ts_mov_b32 s48, 0\n\tbuffer_load_dwordx4 v8, s[40:43], s48 offen  lds\n\ts_add_u32 m0, s51, 1024\n\t"
      "s_mov_b32 s48, 65536\n\tbuffer_load_dwordx4 v9, s[40:43], s48 offen  lds\n\ts_add_u32 m0, s51, 2048\n\ts_mov_b32 s48, 2048\n\t"
      "buffer_load_dwordx4 v8, s[40:43], s48 offen  lds\n\ts_add_u32 m0, s51, 3072\n\ts_mov_b32 s48, 67584\n\tbuffer_load_dwordx4 v9, s[40:43], s48 offen  lds\n\t"
      "s_add_u32 m0, s51, 4096\n\ts_mov_b32 s48, 4096\n\tbuffer_load_dwordx4 v8, s[40:43], s48 offen  lds\n\ts_add_u32 m0, s51, 5120\n\ts_mov_b32 s48, 69632\n\t"
      "buffer_load_dwordx4 v9, s[40:43], s48 offen  lds\n\ts_add_u32 m0, s51, 6144\n\ts_mov_b32 s48, 6144\n\tbuffer_load_dwordx4 v8, s[40:43], s48 offen  lds\n\t"
      "s_add_u32 m0, s51, 7168\n\ts_mov_b32 s48, 71680\n\tbuffer_load_dwordx4 v9, s[40:43], s48 offen  lds\n\ts_waitcnt lgkmcnt(0)\n\ts_bitcmp0_b32 s68, 0\n\t"
      "s_cbranch_scc1 5f\n\ts_add_u32 s71, s71, 1\n\ts_sub_u32 s48, s69, 32768\n\tv_mov_b32 v5, s71\n\ts_mov_b64 exec, 1\n\tbuffer_store_dword v5, off, s[12:15], s48 sc1\n\t"
      "s_mov_b64 exec, -1\n\t"
      "5:\n\ts_add_u32 m0, s51, 8192\n\ts_mov_b32 s48, 8192\n\tbuffer_load_dwordx4 v8, s[40:43], s48 offen  lds\n\ts_add_u32 m0, s51, 9216\n\ts_mov_b32 s48, 73728\n\t"
      "buffer_load_dwordx4 v9, s[40:43], s48 offen  lds\n\ts_add_u32 m0, s51, 10240\n\ts_mov_b32 s48, 10240\n\tbuffer_load_dwordx4 v8, s[40:43], s48 offen  lds\n\t"
      "s_add_u32 m0, s51, 11264\n\ts_mov_b32 s48, 75776\n\tbuffer_load_dwordx4 v9, s[40:43], s48 offen  lds\n\ts_add_u32 m0, s51, 12288\n\ts_mov_b32 s48, 12288\n\t"
      "buffer_load_dwordx4 v8, s[40:43], s48 offen  lds\n\ts_add_u32 m0, s51, 13312\n\ts_mov_b32 s48, 77824\n\tbuffer_load_dwordx4 v9, s[40:43], s48 offen  lds\n\t"
      "s_add_u32 m0, s51, 14336\n\ts_mov_b32 s48, 14336\n\tbuffer_load_dwordx4 v8, s[40:43], s48 offen  lds\n\ts_add_u32 m0, s51, 15360\n\ts_mov_b32 s48, 79872\n\t"
      "buffer_load_dwordx4 v9, s[40:43], s48 offen  lds\n\ts_add_u32 m0, s51, 16384\n\ts_mov_b32 s48, 16384\n\tbuffer_load_dwordx4 v8, s[40:43], s48 offen  lds\n\t"
      "s_add_u32 m0, s51, 17408\n\ts_mov_b32 s48, 81920\n\tbuffer_load_dwordx4 v9, s[40:43], s48 offen  lds\n\ts_add_u32 m0, s51, 18432\n\ts_mov_b32 s48, 18432\n\t"
      "buffer_load_dwordx4 v8, s[40:43], s48 offen  lds\n\ts_add_u32 m0, s51, 19456\n\ts_mov_b32 s48, 83968\n\tbuffer_load_dwordx4 v9, s[40:43], s48 offen  lds\n\t"
      "s_add_u32 m0, s51, 20480\n\ts_mov_b32 s48, 20480\n\tbuffer_load_dwordx4 v8, s[40:43], s48 offen  lds\n\ts_add_u32 m0, s51, 21504\n\ts_mov_b32 s48, 86016\n\t"
      "buffer_load_dwordx4 v9, s[40:43], s48 offen  lds\n\ts_add_u32 m0, s51, 22528\n\ts_mov_b32 s48, 22528\n\tbuffer_load_dwordx4 v8, s[40:43], s48 offen  lds\n\t"
      "s_add_u32 m0, s51, 23552\n\ts_mov_b32 s48, 88064\n\tbuffer_load_dwordx4 v9, s[40:43], s48 offen  lds\n\ts_cmp_lt_u32 s68, 1\n\ts_cbranch_scc1 7f\n\t"
      "s_cmp_eq_u32 s68, 2\n\ts_cbranch_scc1 6f\n\t"
      "5:\n\ts_buffer_load_dword s48, s[12:15], s69 glc\n\ts_waitcnt lgkmcnt(0)\n\ts_cmp_eq_u32 s48, s71\n\ts_cbranch_scc1 6f\n\ts_cmp_lg_u32 s68, 3\n\ts_cbranch_scc1 7f\n\t"
      "s_cmp_eq_u32 s18, 0\n\ts_cbranch_scc1 6f\n\ts_sub_u32 s18, s18, 1\n\ts_branch 5b\n\t"
      "6:\n\tv_mov_b32 v5, 1\n\ts_mov_b64 exec, 1\n\tbuffer_atomic_add v5, off, s[12:15], s70\n\ts_mov_b64 exec, -1\n\ts_mov_b32 s68, 0\n\t"
      "7:\n\ts_waitcnt vmcnt(16)\n\tds_read_b64_tr_b16 a[0:1], v10 offset:0\n\tds_read_b64_tr_b16 a[2:3], v10 offset:2048\n\tds_read_b64_tr_b16 a[32:33], v11 offset:0\n\t"
      "ds_read_b64_tr_b16 a[34:35], v11 offset:2048\n\tds_read_b64_tr_b16 a[64:65], v12 offset:0\n\tds_read_b64_tr_b16 a[66:67], v12 offset:2048\n\t"
      "ds_read_b64_tr_b16 a[96:97], v13 offset:0\n\tds_read_b64_tr_b16 a[98:99], v13 offset:2048\n\tds_read_b64_tr_b16 a[128:129], v10 offset:256\n\t"
      "ds_read_b64_tr_b16 a[130:131], v10 offset:2304\n\tds_read_b64_tr_b16 a[160:161], v11 offset:256\n\tds_read_b64_tr_b16 a[162:163], v11 offset:2304\n\t"
      "ds_read_b64_tr_b16 a[192:193], v12 offset:256\n\tds_read_b64_tr_b16 a[194:195], v12 offset:2304\n\tds_read_b64_tr_b16 a[224:225], v13 offset:256\n\t"
      "ds_read_b64_tr_b16 a[226:227], v13 offset:2304\n\ts_waitcnt lgkmcnt(0)\n\ts_add_u32 m0, s51, 24576\n\ts_mov_b32 s48, 24576\n\t"
      "buffer_load_dwordx4 v8, s[40:43], s48 offen  lds\n\ts_add_u32 m0, s51, 25600\n\ts_mov_b32 s48, 90112\n\tbuffer_load_dwordx4 v9, s[40:43], s48 offen  lds\n\t"
      "s_add_u32 m0, s51, 26624\n\ts_mov_b32 s48, 26624\n\tbuffer_load_dwordx4 v8, s[40:43], s48 offen  lds\n\ts_add_u32 m0, s51, 27648\n\ts_mov_b32 s48, 92160\n\t"
      "buffer_load_dwordx4 v9, s[40:43], s48 offen  lds\n\ts_add_u32 m0, s51, 28672\n\ts_mov_b32 s48, 28672\n\tbuffer_load_dwordx4 v8, s[40:43], s48 offen  lds\n\t"
      "s_add_u32 m0, s51, 29696\n\ts_mov_b32 s48, 94208\n\tbuffer_load_dwordx4 v9, s[40:43], s48 offen  lds\n\ts_add_u32 m0, s51, 30720\n\ts_mov_b32 s48, 30720\n\t"
      "buffer_load_dwordx4 v8, s[40:43], s48 offen  lds\n\ts_add_u32 m0, s51, 31744\n\ts_mov_b32 s48, 96256\n\tbuffer_load_dwordx4 v9, s[40:43], s48 offen  lds\n\t"
      "s_cmp_lt_u32 s68, 1\n\ts_cbranch_scc1 7f\n\ts_cmp_eq_u32 s68, 2\n\ts_cbranch_scc1 6f\n\t"
      "5:\n\ts_buffer_load_dword s48, s[12:15], s69 glc\n\ts_waitcnt lgkmcnt(0)\n\ts_cmp_eq_u32 s48, s71\n\ts_cbranch_scc0 7f\n\t"
      "6:\n\tv_mov_b32 v5, 1\n\ts_mov_b64 exec, 1\n\tbuffer_atomic_add v5, off, s[12:15], s70\n\ts_mov_b64 exec, -1\n\ts_mov_b32 s68, 0\n\t"
      "7:\n\ts_waitcnt vmcnt(16)\n\tds_read_b64_tr_b16 a[4:5], v10 offset:8192\n\tds_read_b64_tr_b16 a[6:7], v10 offset:10240\n\tds_read_b64_tr_b16 a[36:37], v11 offset:8192\n\t"
      "ds_read_b64_tr_b16 a[38:39], v11 offset:10240\n\tds_read_b64_tr_b16 a[68:69], v12 offset:8192\n\tds_read_b64_tr_b16 a[70:71], v12 offset:10240\n\t"
      "ds_read_b64_tr_b16 a[100:101], v13 offset:8192\n\tds_read_b64_tr_b16 a[102:103], v13 offset:10240\n\tds_read_b64_tr_b16 a[132:133], v10 offset:8448\n\t"
      "ds_read_b64_tr_b16 a[134:135], v10 offset:10496\n\tds_read_b64_tr_b16 a[164:165], v11 offset:8448\n\tds_read_b64_tr_b16 a[166:167], v11 offset:10496\n\t"
      "ds_read_b64_tr_b16 a[196:197], v12 offset:8448\n\tds_read_b64_tr_b16 a[198:199], v12 offset:10496\n\tds_read_b64_tr_b16 a[228:229], v13 offset:8448\n\t"
      "ds_read_b64_tr_b16 a[230:231], v13 offset:10496\n\ts_waitcnt lgkmcnt(0)\n\ts_add_u32 m0, s51, 0\n\ts_mov_b32 s48, 131072\n\t"
      "buffer_load_dwordx4 v8, s[40:43], s48 offen  lds\n\ts_add_u32 m0, s51, 1024\n\ts_mov_b32 s48, 196608\n\tbuffer_load_dwordx4 v9, s[40:43], s48 offen  lds\n\t"
      "s_add_u32 m0, s51, 2048\n\ts_mov_b32 s48, 133120\n\tbuffer_load_dwordx4 v8, s[40:43], s48 offen  lds\n\ts_add_u32 m0, s51, 3072\n\ts_mov_b32 s48, 198656\n\t"
      "buffer_load_dwordx4 v9, s[40:43], s48 offen  lds\n\ts_add_u32 m0, s51, 4096\n\ts_mov_b32 s48, 135168\n\tbuffer_load_dwordx4 v8, s[40:43], s48 offen  lds\n\t"
      "s_add_u32 m0, s51, 5120\n\ts_mov_b32 s48, 200704\n\tbuffer_load_dwordx4 v9, s[40:43], s48 offen  lds\n\ts_add_u32 m0, s51, 6144\n\ts_mov_b32 s48, 137216\n\t"
      "buffer_load_dwordx4 v8, s[40:43], s48 offen  lds\n\ts_add_u32 m0, s51, 7168\n\ts_mov_b32 s48, 202752\n\tbuffer_load_dwordx4 v9, s[40:43], s48 offen  lds\n\t"
      "s_cmp_lt_u32 s68, 1\n\ts_cbranch_scc1 7f\n\ts_cmp_eq_u32 s68, 2\n\ts_cbranch_scc1 6f\n\t"
      "5:\n\ts_buffer_load_dword s48, s[12:15], s69 glc\n\ts_waitcnt lgkmcnt(0)\n\ts_cmp_eq_u32 s48, s71\n\ts_cbranch_scc0 7f\n\t"
      "6:\n\tv_mov_b32 v5, 1\n\ts_mov_b64 exec, 1\n\tbuffer_atomic_add v5, off, s[12:15], s70\n\ts_mov_b64 exec, -1\n\ts_mov_b32 s68, 0\n\t"
      "7:\n\ts_waitcnt vmcnt(16)\n\tds_read_b64_tr_b16 a[8:9], v10 offset:16384\n\tds_read_b64_tr_b16 a[10:11], v10 offset:18432\n\t"
      "ds_read_b64_tr_b16 a[40:41], v11 offset:16384\n\tds_read_b64_tr_b16 a[42:43], v11 offset:18432\n\tds_read_b64_tr_b16 a[72:73], v12 offset:16384\n\t"
      "ds_read_b64_tr_b16 a[74:75], v12 offset:18432\n\tds_read_b64_tr_b16 a[104:105], v13 offset:16384\n\tds_read_b64_tr_b16 a[106:107], v13 offset:18432\n\t"
      "ds_read_b64_tr_b16 a[136:137], v10 offset:16640\n\tds_read_b64_tr_b16 a[138:139], v10 offset:18688\n\tds_read_b64_tr_b16 a[168:169], v11 offset:16640\n\t"
      "ds_read_b64_tr_b16 a[170:171], v11 offset:18688\n\tds_read_b64_tr_b16 a[200:201], v12 offset:16640\n\tds_read_b64_tr_b16 a[202:203], v12 offset:18688\n\t"
      "ds_read_b64_tr_b16 a[232:233], v13 offset:16640\n\tds_read_b64_tr_b16 a[234:235], v13 offset:18688\n\ts_waitcnt lgkmcnt(0)\n\ts_add_u32 m0, s51, 8192\n\t"
      "s_mov_b32 s48, 139264\n\tbuffer_load_dwordx4 v8, s[40:43], s48 offen  lds\n\ts_add_u32 m0, s51, 9216\n\ts_mov_b32 s48, 204800\n\t"
      "buffer_load_dwordx4 v9, s[40:43], s48 offen  lds\n\ts_add_u32 m0, s51, 10240\n\ts_mov_b32 s48, 141312\n\tbuffer_load_dwordx4 v8, s[40:43], s48 offen  lds\n\t"
      "s_add_u32 m0, s51, 11264\n\ts_mov_b32 s48, 206848\n\tbuffer_load_dwordx4 v9, s[40:43], s48 offen  lds\n\ts_add_u32 m0, s51, 12288\n\ts_mov_b32 s48, 143360\n\t"
      "buffer_load_dwordx4 v8, s[40:43], s48 offen  lds\n\ts_add_u32 m0, s51, 13312\n\ts_mov_b32 s48, 208896\n\tbuffer_load_dwordx4 v9, s[40:43], s48 offen  lds\n\t"
      "s_add_u32 m0, s51, 14336\n\ts_mov_b32 s48, 145408\n\tbuffer_load_dwordx4 v8, s[40:43], s48 offen  lds\n\ts_add_u32 m0, s51, 15360\n\ts_mov_b32 s48, 210944\n\t"
      "buffer_load_dwordx4 v9, s[40:43], s48 offen  lds\n\ts_cmp_lt_u32 s68, 1\n\ts_cbranch_scc1 7f\n\ts_cmp_eq_u32 s68, 2\n\ts_cbranch_scc1 6f\n\t"
      "5:\n\ts_buffer_load_dword s48, s[12:15], s69 glc\n\ts_waitcnt lgkmcnt(0)\n\ts_cmp_eq_u32 s48, s71\n\ts_cbranch_scc0 7f\n\t"
      "6:\n\tv_mov_b32 v5, 1\n\ts_mov_b64 exec, 1\n\tbuffer_atomic_add v5, off, s[12:15], s70\n\ts_mov_b64 exec, -1\n\ts_mov_b32 s68, 0\n\t"
      "7:\n\ts_waitcnt vmcnt(16)\n\tds_read_b64_tr_b16 a[12:13], v10 offset:24576\n\tds_read_b64_tr_b16 a[14:15], v10 offset:26624\n\t"
      "ds_read_b64_tr_b16 a[44:45], v11 offset:24576\n\tds_read_b64_tr_b16 a[46:47], v11 offset:26624\n\tds_read_b64_tr_b16 a[76:77], v12 offset:24576\n\t"
      "ds_read_b64_tr_b16 a[78:79], v12 offset:26624\n\tds_read_b64_tr_b16 a[108:109], v13 offset:24576\n\tds_read_b64_tr_b16 a[110:111], v13 offset:26624\n\t"
      "ds_read_b64_tr_b16 a[140:141], v10 offset:24832\n\tds_read_b64_tr_b16 a[142:143], v10 offset:26880\n\tds_read_b64_tr_b16 a[172:173], v11 offset:24832\n\t"
      "ds_read_b64_tr_b16 a[174:175], v11 offset:26880\n\tds_read_b64_tr_b16 a[204:205], v12 offset:24832\n\tds_read_b64_tr_b16 a[206:207], v12 offset:26880\n\t"
      "ds_read_b64_tr_b16 a[236:237], v13 offset:24832\n\tds_read_b64_tr_b16 a[238:239], v13 offset:26880\n\ts_waitcnt lgkmcnt(0)\n\ts_add_u32 m0, s51, 16384\n\t"
      "s_mov_b32 s48, 147456\n\tbuffer_load_dwordx4 v8, s[40:43], s48 offen  lds\n\ts_add_u32 m0, s51, 17408\n\ts_mov_b32 s48, 212992\n\t"
      "buffer_load_dwordx4 v9, s[40:43], s48 offen  lds\n\ts_add_u32 m0, s51, 18432\n\ts_mov_b32 s48, 149504\n\tbuffer_load_dwordx4 v8, s[40:43], s48 offen  lds\n\t"
      "s_add_u32 m0, s51, 19456\n\ts_mov_b32 s48, 215040\n\tbuffer_load_dwordx4 v9, s[40:43], s48 offen  lds\n\ts_add_u32 m0, s51, 20480\n\ts_mov_b32 s48, 151552\n\t"
      "buffer_load_dwordx4 v8, s[40:43], s48 offen  lds\n\ts_add_u32 m0, s51, 21504\n\ts_mov_b32 s48, 217088\n\tbuffer_load_dwordx4 v9, s[40:43], s48 offen  lds\n\t"
      "s_add_u32 m0, s51, 22528\n\ts_mov_b32 s48, 153600\n\tbuffer_load_dwordx4 v8, s[40:43], s48 offen  lds\n\ts_add_u32 m0, s51, 23552\n\ts_mov_b32 s48, 219136\n\t"
      "buffer_load_dwordx4 v9, s[40:43], s48 offen  lds\n\ts_cmp_lt_u32 s68, 1\n\ts_cbranch_scc1 7f\n\ts_cmp_eq_u32 s68, 2\n\ts_cbranch_scc1 6f\n\t"
      "5:\n\ts_buffer_load_dword s48, s[12:15], s69 glc\n\ts_waitcnt lgkmcnt(0)\n\ts_cmp_eq_u32 s48, s71\n\ts_cbranch_scc0 7f\n\t"
      "6:\n\tv_mov_b32 v5, 1\n\ts_mov_b64 exec, 1\n\tbuffer_atomic_add v5, off, s[12:15], s70\n\ts_mov_b64 exec, -1\n\ts_mov_b32 s68, 0\n\t"
      "7:\n\ts_waitcnt vmcnt(16)\n\tds_read_b64_tr_b16 a[16:17], v10 offset:0\n\tds_read_b64_tr_b16 a[18:19], v10 offset:2048\n\tds_read_b64_tr_b16 a[48:49], v11 offset:0\n\t"
      "ds_read_b64_tr_b16 a[50:51], v11 offset:2048\n\tds_read_b64_tr_b16 a[80:81], v12 offset:0\n\tds_read_b64_tr_b16 a[82:83], v12 offset:2048\n\t"
      "ds_read_b64_tr_b16 a[112:113], v13 offset:0\n\tds_read_b64_tr_b16 a[114:115], v13 offset:2048\n\tds_read_b64_tr_b16 a[144:145], v10 offset:256\n\t"
      "ds_read_b64_tr_b16 a[146:147], v10 offset:2304\n\tds_read_b64_tr_b16 a[176:177], v11 offset:256\n\tds_read_b64_tr_b16 a[178:179], v11 offset:2304\n\t"
      "ds_read_b64_tr_b16 a[208:209], v12 offset:256\n\tds_read_b64_tr_b16 a[210:211], v12 offset:2304\n\tds_read_b64_tr_b16 a[240:241], v13 offset:256\n\t"
      "ds_read_b64_tr_b16 a[242:243], v13 offset:2304\n\ts_waitcnt lgkmcnt(0)\n\ts_add_u32 m0, s51, 24576\n\ts_mov_b32 s48, 155648\n\t"
      "buffer_load_dwordx4 v8, s[40:43], s48 offen  lds\n\ts_add_u32 m0, s51, 25600\n\ts_mov_b32 s48, 221184\n\tbuffer_load_dwordx4 v9, s[40:43], s48 offen  lds\n\t"
      "s_add_u32 m0, s51, 26624\n\ts_mov_b32 s48, 157696\n\tbuffer_load_dwordx4 v8, s[40:43], s48 offen  lds\n\ts_add_u32 m0, s51, 27648\n\ts_mov_b32 s48, 223232\n\t"
      "buffer_load_dwordx4 v9, s[40:43], s48 offen  lds\n\ts_add_u32 m0, s51, 28672\n\ts_mov_b32 s48, 159744\n\tbuffer_load_dwordx4 v8, s[40:43], s48 offen  lds\n\t"
      "s_add_u32 m0, s51, 29696\n\ts_mov_b32 s48, 225280\n\tbuffer_load_dwordx4 v9, s[40:43], s48 offen  lds\n\ts_add_u32 m0, s51, 30720\n\ts_mov_b32 s48, 161792\n\t"
      "buffer_load_dwordx4 v8, s[40:43], s48 offen  lds\n\ts_add_u32 m0, s51, 31744\n\ts_mov_b32 s48, 227328\n\tbuffer_load_dwordx4 v9, s[40:43], s48 offen  lds\n\t"
      "s_cmp_lt_u32 s68, 1\n\ts_cbranch_scc1 7f\n\ts_cmp_eq_u32 s68, 2\n\ts_cbranch_scc1 6f\n\t"
      "5:\n\ts_buffer_load_dword s48, s[12:15], s69 glc\n\ts_waitcnt lgkmcnt(0)\n\ts_cmp_eq_u32 s48, s71\n\ts_cbranch_scc0 7f\n\t"
      "6:\n\tv_mov_b32 v5, 1\n\ts_mov_b64 exec, 1\n\tbuffer_atomic_add v5, off, s[12:15], s70\n\ts_mov_b64 exec, -1\n\ts_mov_b32 s68, 0\n\t"
      "7:\n\ts_waitcnt vmcnt(16)\n\tds_read_b64_tr_b16 a[20:21], v10 offset:8192\n\tds_read_b64_tr_b16 a[22:23], v10 offset:10240\n\t"
      "ds_read_b64_tr_b16 a[52:53], v11 offset:8192\n\tds_read_b64_tr_b16 a[54:55], v11 offset:10240\n\tds_read_b64_tr_b16 a[84:85], v12 offset:8192\n\t"
      "ds_read_b64_tr_b16 a[86:87], v12 offset:10240\n\tds_read_b64_tr_b16 a[116:117], v13 offset:8192\n\tds_read_b64_tr_b16 a[118:119], v13 offset:10240\n\t"
      "ds_read_b64_tr_b16 a[148:149], v10 offset:8448\n\tds_read_b64_tr_b16 a[150:151], v10 offset:10496\n\tds_read_b64_tr_b16 a[180:181], v11 offset:8448\n\t"
      "ds_read_b64_tr_b16 a[182:183], v11 offset:10496\n\tds_read_b64_tr_b16 a[212:213], v12 offset:8448\n\tds_read_b64_tr_b16 a[214:215], v12 offset:10496\n\t"
      "ds_read_b64_tr_b16 a[244:245], v13 offset:8448\n\tds_read_b64_tr_b16 a[246:247], v13 offset:10496\n\ts_waitcnt lgkmcnt(0)\n\ts_cmp_lt_u32 s68, 1\n\ts_cbranch_scc1 7f\n\t"
      "s_cmp_eq_u32 s68, 2\n\ts_cbranch_scc1 6f\n\t"
      "5:\n\ts_buffer_load_dword s48, s[12:15], s69 glc\n\ts_waitcnt lgkmcnt(0)\n\ts_cmp_eq_u32 s48, s71\n\ts_cbranch_scc0 7f\n\t"
      "6:\n\tv_mov_b32 v5, 1\n\ts_mov_b64 exec, 1\n\tbuffer_atomic_add v5, off, s[12:15], s70\n\ts_mov_b64 exec, -1\n\ts_mov_b32 s68, 0\n\t"
      "7:\n\ts_waitcnt vmcnt(8)\n\tds_read_b64_tr_b16 a[24:25], v10 offset:16384\n\tds_read_b64_tr_b16 a[26:27], v10 offset:18432\n\t"
      "ds_read_b64_tr_b16 a[56:57], v11 offset:16384\n\tds_read_b64_tr_b16 a[58:59], v11 offset:18432\n\tds_read_b64_tr_b16 a[88:89], v12 offset:16384\n\t"
      "ds_read_b64_tr_b16 a[90:91], v12 offset:18432\n\tds_read_b64_tr_b16 a[120:121], v13 offset:16384\n\tds_read_b64_tr_b16 a[122:123], v13 offset:18432\n\t"
      "ds_read_b64_tr_b16 a[152:153], v10 offset:16640\n\tds_read_b64_tr_b16 a[154:155], v10 offset:18688\n\tds_read_b64_tr_b16 a[184:185], v11 offset:16640\n\t"
      "ds_read_b64_tr_b16 a[186:187], v11 offset:18688\n\tds_read_b64_tr_b16 a[216:217], v12 offset:16640\n\tds_read_b64_tr_b16 a[218:219], v12 offset:18688\n\t"
      "ds_read_b64_tr_b16 a[248:249], v13 offset:16640\n\tds_read_b64_tr_b16 a[250:251], v13 offset:18688\n\ts_waitcnt lgkmcnt(0)\n\ts_cmp_lt_u32 s68, 1\n\ts_cbranch_scc1 7f\n\t"
      "s_cmp_eq_u32 s68, 2\n\ts_cbranch_scc1 6f\n\t"
      "5:\n\ts_buffer_load_dword s48, s[12:15], s69 glc\n\ts_waitcnt lgkmcnt(0)\n\ts_cmp_eq_u32 s48, s71\n\ts_cbranch_scc1 6f\n\ts_cmp_eq_u32 s18, 0\n\ts_cbranch_scc1 6f\n\t"
      "s_sub_u32 s18, s18, 1\n\ts_branch 5b\n\t"
      "6:\n\tv_mov_b32 v5, 1\n\ts_mov_b64 exec, 1\n\tbuffer_atomic_add v5, off, s[12:15], s70\n\ts_mov_b64 exec, -1\n\ts_mov_b32 s68, 0\n\t"
      "7:\n\ts_waitcnt vmcnt(0)\n\tds_read_b64_tr_b16 a[28:29], v10 offset:24576\n\tds_read_b64_tr_b16 a[30:31], v10 offset:26624\n\t"
      "ds_read_b64_tr_b16 a[60:61], v11 offset:24576\n\tds_read_b64_tr_b16 a[62:63], v11 offset:26624\n\tds_read_b64_tr_b16 a[92:93], v12 offset:24576\n\t"
      "ds_read_b64_tr_b16 a[94:95], v12 offset:26624\n\tds_read_b64_tr_b16 a[124:125], v13 offset:24576\n\tds_read_b64_tr_b16 a[126:127], v13 offset:26624\n\t"
      "ds_read_b64_tr_b16 a[156:157], v10 offset:24832\n\tds_read_b64_tr_b16 a[158:159], v10 offset:26880\n\tds_read_b64_tr_b16 a[188:189], v11 offset:24832\n\t"
      "ds_read_b64_tr_b16 a[190:191], v11 offset:26880\n\tds_read_b64_tr_b16 a[220:221], v12 offset:24832\n\tds_read_b64_tr_b16 a[222:223], v12 offset:26880\n\t"
      "ds_read_b64_tr_b16 a[252:253], v13 offset:24832\n\tds_read_b64_tr_b16 a[254:255], v13 offset:26880\n\ts_waitcnt lgkmcnt(0)\n\t"
      STS("s_memrealtime vcc\n\ts_waitcnt lgkmcnt(0)\n\tv_mov_b32 v95, vcc_lo\n\tv_mov_b32 v94, 161024\n\tds_write_b32 v94, v95\n\ts_memtime vcc\n\ts_waitcnt lgkmcnt(0)\n\tv_mov_b32 v95, vcc_lo\n\tds_write_b32 v94, v95 offset:400\n\ts_waitcnt lgkmcnt(0)\n\t")
      "buffer_load_dword v16, v14, s[8:11], 0 offen\n\ts_barrier\n\t"
      "1:\n\tbuffer_load_dword v5, off, s[12:15], s63 sc1\n\ts_waitcnt vmcnt(0)\n\t"
      "5:\n\tv_readfirstlane_b32 s49, v5\n\ts_bitcmp1_b32 s49, 0\n\ts_cbranch_scc1 2f\n\ts_cmp_eq_u32 s18, 0\n\ts_cbranch_scc1 2f\n\ts_sub_u32 s18, s18, 1\n\ts_sleep 2\n\t"
      "s_branch 1b\n\t"
      "2:\n\ts_and_b32 s48, s49, 3\n\ts_cmp_lg_u32 s48, 3\n\ts_cbranch_scc1 4f\n\ts_cmp_eq_u32 s62, 0\n\ts_cbranch_scc1 4f\n\ts_waitcnt vmcnt(0) lgkmcnt(0)\n\ts_barrier\n\t"
      "s_mov_b64 s[0:1], s[60:61]\n\ts_mov_b32 s2, s62\n\ts_lshl_b32 s48, s16, 6\n\tv_mbcnt_lo_u32_b32 v0, -1, 0\n\tv_mbcnt_hi_u32_b32 v0, -1, v0\n\tv_add_u32 v0, s48, v0\n\t"
      "s_getpc_b64 s[4:5]\n\t"
      "3:\n\ts_sub_u32 s4, s4, 3b-mk_mega\n\ts_subb_u32 s5, s5, 0\n\ts_setpc_b64 s[4:5]\n\t"
      "4:\n\ts_lshl_b32 s48, s49, 10\n\ts_and_b32 s48, s48, 0x2000\n\ts_xor_b32 s22, s22, s48\n\tv_xor_b32 v6, s48, v6\n\ts_lshl_b32 s49, s49, 11\n\t"
      "s_and_b32 s49, s49, 0x2000\n\ts_xor_b32 s19, s19, s49\n\tv_xor_b32 v1, s49, v1\n\ts_mov_b32 s26, 14\n\t"
      TKS("s_mov_b32 s91, 1\n\t")
      "s_cmp_eq_u32 s16, 0\n\ts_cselect_b32 s27, s10, 0\n\tv_mov_b32 v15, 16384\n\tv_add_u32 v7, s51, v15\n\ts_mov_b64 s[56:57], s[8:9]\n\ts_mov_b32 s59, s11\n\t"
      "s_mov_b32 s23, 0\n\ts_mov_b32 s25, 0\n\tv_mov_b32 v2, 0x40000000\n\ts_branch 43f\n\t"
      "10:\n\tv_mfma_f32_32x32x16_bf16 v[32:47], v[96:99], a[0:3], 0\n\ts_nop 4\n\t"
      TK0
      "v_mfma_f32_32x32x16_bf16 v[32:47], v[100:103], a[4:7], v[32:47]\n\ts_add_u32 s48, s19, 0x4000\n\ts_and_b32 s48, s48, 0x3c000\n\ts_andn2_b32 s19, s19, 0x3c000\n\t"
      "s_or_b32 s19, s19, s48\n\ts_add_u32 s24, s19, 0x1000\n\tv_mfma_f32_32x32x16_bf16 v[32:47], v[104:107], a[8:11], v[32:47]\n\tv_cvt_pk_bf16_f32 v80, v48, v49\n\t"
      "v_cvt_pk_bf16_f32 v81, v50, v51\n\tv_cvt_pk_bf16_f32 v82, v56, v57\n\tv_cvt_pk_bf16_f32 v83, v58, v59\n\t"
      "v_mfma_f32_32x32x16_bf16 v[32:47], v[108:111], a[12:15], v[32:47]\n\tbuffer_store_dwordx4 v[80:83], v2, s[8:11], s25 offen offset:0 sc1\n\t"
      "v_mfma_f32_32x32x16_bf16 v[32:47], v[112:115], a[16:19], v[32:47]\n\tv_cvt_pk_bf16_f32 v84, v52, v53\n\tv_cvt_pk_bf16_f32 v85, v54, v55\n\t"
      "v_cvt_pk_bf16_f32 v86, v60, v61\n\tv_cvt_pk_bf16_f32 v87, v62, v63\n\tv_mfma_f32_32x32x16_bf16 v[32:47], v[116:119], a[20:23], v[32:47]\n\t"
      "buffer_store_dwordx4 v[84:87], v2, s[8:11], s25 offen offset:512 sc1\n\tv_mfma_f32_32x32x16_bf16 v[32:47], v[120:123], a[24:27], v[32:47]\n\ts_add_u32 s25, s20, 0x0\n\t"
      "v_mfma_f32_32x32x16_bf16 v[32:47], v[124:127], a[28:31], v[32:47]\n\tv_mov_b32 v4, s17\n\tv_mov_b32 v2, v1\n\t"
      TK1
      "s_waitcnt vmcnt(8)\n\t"
      TK2
      "v_readlane_b32 s23, v5, 0\n\tv_readlane_b32 s48, v5, 1\n\ts_add_u32 s48, s48, s26\n\ts_min_u32 s23, s23, s48\n\t"
      "v_mfma_f32_32x32x16_bf16 v[48:63], v[96:99], a[32:35], 0\n\t"
      "s_add_u32 s48, s17, 1\n\ts_cmp_gt_u32 s23, s48\n\ts_cselect_b32 s58, s27, 0\n\ts_add_u32 m0, s51, 0\n\tv_mov_b32 v16, s58\n\t"
      "v_mfma_f32_32x32x16_bf16 v[48:63], v[100:103], a[36:39], v[48:63]\n\tds_write_b32 v7, v16 offset:0\n\ts_nop 3\n\t"
      "v_mfma_f32_32x32x16_bf16 v[48:63], v[104:107], a[40:43], v[48:63]\n\tv_cvt_pk_bf16_f32 v80, v32, v33\n\tv_cvt_pk_bf16_f32 v81, v34, v35\n\t"
      "v_cvt_pk_bf16_f32 v82, v40, v41\n\tv_cvt_pk_bf16_f32 v83, v42, v43\n\tbuffer_load_dwordx4 v0, s[56:59], s19 offen offset:0 sc1 lds\n\t"
      "v_mfma_f32_32x32x16_bf16 v[48:63], v[108:111], a[44:47], v[48:63]\n\tbuffer_store_dwordx4 v[80:83], v1, s[8:11], s25 offen offset:0 sc1\n\t"
      "v_mfma_f32_32x32x16_bf16 v[48:63], v[112:115], a[48:51], v[48:63]\n\tv_cvt_pk_bf16_f32 v84, v36, v37\n\tv_cvt_pk_bf16_f32 v85, v38, v39\n\t"
      "v_cvt_pk_bf16_f32 v86, v44, v45\n\tv_cvt_pk_bf16_f32 v87, v46, v47\n\tbuffer_load_dwordx4 v0, s[56:59], s19 offen offset:1024 sc1 lds\n\t"
      "v_mfma_f32_32x32x16_bf16 v[48:63], v[116:119], a[52:55], v[48:63]\n\tbuffer_store_dwordx4 v[84:87], v1, s[8:11], s25 offen offset:512 sc1\n\t"
      "v_mfma_f32_32x32x16_bf16 v[48:63], v[120:123], a[56:59], v[48:63]\n\ts_add_u32 s25, s20, 0x20000\n\tbuffer_load_dwordx4 v0, s[56:59], s19 offen offset:2048 sc1 lds\n\t"
      "v_mfma_f32_32x32x16_bf16 v[48:63], v[124:127], a[60:63], v[48:63]\n\tbuffer_load_dwordx4 v0, s[56:59], s19 offen offset:3072 sc1 lds\n\t"
      "v_mfma_f32_32x32x16_bf16 v[32:47], v[96:99], a[64:67], 0\n\ts_add_u32 m0, s51, 4096\n\ts_nop 3\n\tv_mfma_f32_32x32x16_bf16 v[32:47], v[100:103], a[68:71], v[32:47]\n\t"
      "s_nop 4\n\tv_mfma_f32_32x32x16_bf16 v[32:47], v[104:107], a[72:75], v[32:47]\n\tv_cvt_pk_bf16_f32 v80, v48, v49\n\tv_cvt_pk_bf16_f32 v81, v50, v51\n\t"
      "v_cvt_pk_bf16_f32 v82, v56, v57\n\tv_cvt_pk_bf16_f32 v83, v58, v59\n\tbuffer_load_dwordx4 v0, s[56:59], s24 offen offset:0 sc1 lds\n\t"
      "v_mfma_f32_32x32x16_bf16 v[32:47], v[108:111], a[76:79], v[32:47]\n\tbuffer_store_dwordx4 v[80:83], v1, s[8:11], s25 offen offset:0 sc1\n\t"
      "v_mfma_f32_32x32x16_bf16 v[32:47], v[112:115], a[80:83], v[32:47]\n\tv_cvt_pk_bf16_f32 v84, v52, v53\n\tv_cvt_pk_bf16_f32 v85, v54, v55\n\t"
      "v_cvt_pk_bf16_f32 v86, v60, v61\n\tv_cvt_pk_bf16_f32 v87, v62, v63\n\tbuffer_load_dwordx4 v0, s[56:59], s24 offen offset:1024 sc1 lds\n\t"
      "v_mfma_f32_32x32x16_bf16 v[32:47], v[116:119], a[84:87], v[32:47]\n\tbuffer_store_dwordx4 v[84:87], v1, s[8:11], s25 offen offset:512 sc1\n\t"
      "v_mfma_f32_32x32x16_bf16 v[32:47], v[120:123], a[88:91], v[32:47]\n\ts_add_u32 s25, s20, 0x40000\n\tbuffer_load_dwordx4 v0, s[56:59], s24 offen offset:2048 sc1 lds\n\t"
      "v_mfma_f32_32x32x16_bf16 v[32:47], v[124:127], a[92:95], v[32:47]\n\tbuffer_load_dwordx4 v0, s[56:59], s24 offen offset:3072 sc1 lds\n\t"
      "v_mfma_f32_32x32x16_bf16 v[48:63], v[96:99], a[96:99], 0\n\ts_nop 4\n\tv_mfma_f32_32x32x16_bf16 v[48:63], v[100:103], a[100:103], v[48:63]\n\ts_mul_i32 s26, s17, 0\n\t"
      "s_add_u32 s26, s26, 784\n\ts_mul_i32 s26, s26, 1171\n\ts_lshr_b32 s26, s26, 16\n\ts_min_u32 s26, s26, 14\n\t"
      "v_mfma_f32_32x32x16_bf16 v[48:63], v[104:107], a[104:107], v[48:63]\n\tv_cvt_pk_bf16_f32 v80, v32, v33\n\tv_cvt_pk_bf16_f32 v81, v34, v35\n\t"
      "v_cvt_pk_bf16_f32 v82, v40, v41\n\tv_cvt_pk_bf16_f32 v83, v42, v43\n\tv_mfma_f32_32x32x16_bf16 v[48:63], v[108:111], a[108:111], v[48:63]\n\t"
      "buffer_store_dwordx4 v[80:83], v1, s[8:11], s25 offen offset:0 sc1\n\tv_mfma_f32_32x32x16_bf16 v[48:63], v[112:115], a[112:115], v[48:63]\n\t"
      "v_cvt_pk_bf16_f32 v84, v36, v37\n\tv_cvt_pk_bf16_f32 v85, v38, v39\n\tv_cvt_pk_bf16_f32 v86, v44, v45\n\tv_cvt_pk_bf16_f32 v87, v46, v47\n\t"
      "v_mfma_f32_32x32x16_bf16 v[48:63], v[116:119], a[116:119], v[48:63]\n\tbuffer_store_dwordx4 v[84:87], v1, s[8:11], s25 offen offset:512 sc1\n\t"
      "v_mfma_f32_32x32x16_bf16 v[48:63], v[120:123], a[120:123], v[48:63]\n\ts_add_u32 s25, s20, 0x60000\n\t"
      "v_mfma_f32_32x32x16_bf16 v[48:63], v[124:127], a[124:127], v[48:63]\n\t"
      TK3
      "s_waitcnt vmcnt(14)\n\t"
      TK4
      "buffer_store_dword v4, v3, s[8:11], s22 offen sc1\n\tv_mfma_f32_32x32x16_bf16 v[32:47], v[96:99], a[128:131], 0\n\ts_nop 4\n\t"
      "v_mfma_f32_32x32x16_bf16 v[32:47], v[100:103], a[132:135], v[32:47]\n\ts_nop 4\n\tv_mfma_f32_32x32x16_bf16 v[32:47], v[104:107], a[136:139], v[32:47]\n\t"
      "v_cvt_pk_bf16_f32 v80, v48, v49\n\tv_cvt_pk_bf16_f32 v81, v50, v51\n\tv_cvt_pk_bf16_f32 v82, v56, v57\n\tv_cvt_pk_bf16_f32 v83, v58, v59\n\t"
      "v_mfma_f32_32x32x16_bf16 v[32:47], v[108:111], a[140:143], v[32:47]\n\tbuffer_store_dwordx4 v[80:83], v1, s[8:11], s25 offen offset:0 sc1\n\t"
      "v_mfma_f32_32x32x16_bf16 v[32:47], v[112:115], a[144:147], v[32:47]\n\tv_cvt_pk_bf16_f32 v84, v52, v53\n\tv_cvt_pk_bf16_f32 v85, v54, v55\n\t"
      "v_cvt_pk_bf16_f32 v86, v60, v61\n\tv_cvt_pk_bf16_f32 v87, v62, v63\n\tv_mfma_f32_32x32x16_bf16 v[32:47], v[116:119], a[148:151], v[32:47]\n\t"
      "buffer_store_dwordx4 v[84:87], v1, s[8:11], s25 offen offset:512 sc1\n\tv_mfma_f32_32x32x16_bf16 v[32:47], v[120:123], a[152:155], v[32:47]\n\t"
      "s_add_u32 s25, s20, 0x80000\n\tv_mfma_f32_32x32x16_bf16 v[32:47], v[124:127], a[156:159], v[32:47]\n\tv_mfma_f32_32x32x16_bf16 v[48:63], v[96:99], a[160:163], 0\n\t"
      "buffer_load_dword v5, v6, s[8:11], 0 offen sc1\n\ts_nop 3\n\tv_mfma_f32_32x32x16_bf16 v[48:63], v[100:103], a[164:167], v[48:63]\n\ts_nop 4\n\t"
      "v_mfma_f32_32x32x16_bf16 v[48:63], v[104:107], a[168:171], v[48:63]\n\tv_cvt_pk_bf16_f32 v80, v32, v33\n\tv_cvt_pk_bf16_f32 v81, v34, v35\n\t"
      "v_cvt_pk_bf16_f32 v82, v40, v41\n\tv_cvt_pk_bf16_f32 v83, v42, v43\n\tv_mfma_f32_32x32x16_bf16 v[48:63], v[108:111], a[172:175], v[48:63]\n\t"
      "buffer_store_dwordx4 v[80:83], v1, s[8:11], s25 offen offset:0 sc1\n\tv_mfma_f32_32x32x16_bf16 v[48:63], v[112:115], a[176:179], v[48:63]\n\t"
      "v_cvt_pk_bf16_f32 v84, v36, v37\n\tv_cvt_pk_bf16_f32 v85, v38, v39\n\tv_cvt_pk_bf16_f32 v86, v44, v45\n\tv_cvt_pk_bf16_f32 v87, v46, v47\n\t"
      "v_mfma_f32_32x32x16_bf16 v[48:63], v[116:119], a[180:183], v[48:63]\n\tbuffer_store_dwordx4 v[84:87], v1, s[8:11], s25 offen offset:512 sc1\n\t"
      "v_mfma_f32_32x32x16_bf16 v[48:63], v[120:123], a[184:187], v[48:63]\n\ts_add_u32 s25, s20, 0xa0000\n\t"
      "v_mfma_f32_32x32x16_bf16 v[48:63], v[124:127], a[188:191], v[48:63]\n\tv_mfma_f32_32x32x16_bf16 v[32:47], v[96:99], a[192:195], 0\n\ts_nop 4\n\t"
      "v_mfma_f32_32x32x16_bf16 v[32:47], v[100:103], a[196:199], v[32:47]\n\ts_nop 4\n\tv_mfma_f32_32x32x16_bf16 v[32:47], v[104:107], a[200:203], v[32:47]\n\t"
      "v_cvt_pk_bf16_f32 v80, v48, v49\n\tv_cvt_pk_bf16_f32 v81, v50, v51\n\tv_cvt_pk_bf16_f32 v82, v56, v57\n\tv_cvt_pk_bf16_f32 v83, v58, v59\n\t"
      "v_mfma_f32_32x32x16_bf16 v[32:47], v[108:111], a[204:207], v[32:47]\n\tbuffer_store_dwordx4 v[80:83], v1, s[8:11], s25 offen offset:0 sc1\n\t"
      "v_mfma_f32_32x32x16_bf16 v[32:47], v[112:115], a[208:211], v[32:47]\n\tv_cvt_pk_bf16_f32 v84, v52, v53\n\tv_cvt_pk_bf16_f32 v85, v54, v55\n\t"
      "v_cvt_pk_bf16_f32 v86, v60, v61\n\tv_cvt_pk_bf16_f32 v87, v62, v63\n\tv_mfma_f32_32x32x16_bf16 v[32:47], v[116:119], a[212:215], v[32:47]\n\t"
      "buffer_store_dwordx4 v[84:87], v1, s[8:11], s25 offen offset:512 sc1\n\tv_mfma_f32_32x32x16_bf16 v[32:47], v[120:123], a[216:219], v[32:47]\n\t"
      "s_add_u32 s25, s20, 0xc0000\n\tv_mfma_f32_32x32x16_bf16 v[32:47], v[124:127], a[220:223], v[32:47]\n\t"
      TK5
      "s_waitcnt vmcnt(10) lgkmcnt(0)\n\t"
      TK6
      "s_barrier\n\tds_read_b32 v16, v15 offset:0\n\tv_mfma_f32_32x32x16_bf16 v[48:63], v[96:99], a[224:227], 0\n\tds_read_b128 v[128:131], v0 offset:0\n\t"
      "ds_read_b128 v[132:135], v0 offset:1024\n\tds_read_b128 v[136:139], v0 offset:2048\n\tds_read_b128 v[140:143], v0 offset:3072\n\ts_nop 0\n\t"
      "v_mfma_f32_32x32x16_bf16 v[48:63], v[100:103], a[228:231], v[48:63]\n\tds_read_b128 v[144:147], v0 offset:4096\n\tds_read_b128 v[148:151], v0 offset:5120\n\t"
      "ds_read_b128 v[152:155], v0 offset:6144\n\tds_read_b128 v[156:159], v0 offset:7168\n\ts_nop 0\n\tv_mfma_f32_32x32x16_bf16 v[48:63], v[104:107], a[232:235], v[48:63]\n\t"
      "v_cvt_pk_bf16_f32 v80, v32, v33\n\tv_cvt_pk_bf16_f32 v81, v34, v35\n\tv_cvt_pk_bf16_f32 v82, v40, v41\n\tv_cvt_pk_bf16_f32 v83, v42, v43\n\t"
      "v_mfma_f32_32x32x16_bf16 v[48:63], v[108:111], a[236:239], v[48:63]\n\tbuffer_store_dwordx4 v[80:83], v1, s[8:11], s25 offen offset:0 sc1\n\t"
      "v_mfma_f32_32x32x16_bf16 v[48:63], v[112:115], a[240:243], v[48:63]\n\tv_cvt_pk_bf16_f32 v84, v36, v37\n\tv_cvt_pk_bf16_f32 v85, v38, v39\n\t"
      "v_cvt_pk_bf16_f32 v86, v44, v45\n\tv_cvt_pk_bf16_f32 v87, v46, v47\n\tv_mfma_f32_32x32x16_bf16 v[48:63], v[116:119], a[244:247], v[48:63]\n\t"
      "buffer_store_dwordx4 v[84:87], v1, s[8:11], s25 offen offset:512 sc1\n\tv_mfma_f32_32x32x16_bf16 v[48:63], v[120:123], a[248:251], v[48:63]\n\t"
      "s_add_u32 s25, s20, 0xe0000\n\ts_waitcnt lgkmcnt(0)\n\tv_readfirstlane_b32 s28, v16\n\tv_mfma_f32_32x32x16_bf16 v[48:63], v[124:127], a[252:255], v[48:63]\n\t"
      "s_add_u32 s17, s17, 1\n\ts_add_u32 s20, s20, 0x400000\n\ts_cmp_eq_u32 s20, 0x5400000\n\ts_cselect_b32 s20, 0x1c00000, s20\n\ts_cmp_eq_u32 s28, 0\n\ts_cbranch_scc1 40f\n\t"
      "11:\n\t"
      STF("s_memrealtime vcc\n\ts_waitcnt lgkmcnt(0)\n\tv_mov_b32 v95, vcc_lo\n\ts_lshl_b32 vcc_hi, s17, 2\n\ts_add_u32 vcc_hi, vcc_hi, 161052\n\tv_mov_b32 v94, vcc_hi\n\tds_write_b32 v94, v95\n\ts_memtime vcc\n\ts_waitcnt lgkmcnt(0)\n\tv_mov_b32 v95, vcc_lo\n\tds_write_b32 v94, v95 offset:400\n\ts_waitcnt lgkmcnt(0)\n\t")
      TK7
      TKS("s_waitcnt lgkmcnt(0)\n\ts_cmp_lt_i32 s17, 0\n\ts_cbranch_scc1 98f\n\ts_cmp_lg_u32 s16, s91\n\ts_cbranch_scc1 98f\n\ts_mov_b64 exec, 1\n\ts_lshl_b32 s48, s17, 4\n\ts_add_u32 s48, s48, 161056\n\tv_mov_b32 v92, s48\n\ts_sub_u32 s48, s74, s90\n\ts_min_u32 s48, s48, 0xffff\n\tv_mov_b32 v93, s48\n\tds_write_b16 v92, v93 offset:0\n\ts_sub_u32 s48, s76, s74\n\ts_min_u32 s48, s48, 0xffff\n\tv_mov_b32 v93, s48\n\tds_write_b16 v92, v93 offset:2\n\ts_sub_u32 s48, s78, s76\n\ts_min_u32 s48, s48, 0xffff\n\tv_mov_b32 v93, s48\n\tds_write_b16 v92, v93 offset:4\n\ts_sub_u32 s48, s80, s78\n\ts_min_u32 s48, s48, 0xffff\n\tv_mov_b32 v93, s48\n\tds_write_b16 v92, v93 offset:6\n\ts_sub_u32 s48, s82, s80\n\ts_min_u32 s48, s48, 0xffff\n\tv_mov_b32 v93, s48\n\tds_write_b16 v92, v93 offset:8\n\ts_sub_u32 s48, s84, s82\n\ts_min_u32 s48, s48, 0xffff\n\tv_mov_b32 v93, s48\n\tds_write_b16 v92, v93 offset:10\n\ts_sub_u32 s48, s86, s84\n\ts_min_u32 s48, s48, 0xffff\n\tv_mov_b32 v93, s48\n\tds_write_b16 v92, v93 offset:12\n\ts_sub_u32 s48, s88, s86\n\ts_min_u32 s48, s48, 0xffff\n\tv_mov_b32 v93, s48\n\tds_write_b16 v92, v93 offset:14\n\ts_mov_b64 exec, -1\n\t98:\n\ts_mov_b32 s90, s88\n\t")
      "v_mfma_f32_32x32x16_bf16 v[32:47], v[128:131], a[0:3], 0\n\ts_nop 4\n\t"
      TK0
      "v_mfma_f32_32x32x16_bf16 v[32:47], v[132:135], a[4:7], v[32:47]\n\ts_add_u32 s48, s19, 0x4000\n\ts_and_b32 s48, s48, 0x3c000\n\ts_andn2_b32 s19, s19, 0x3c000\n\t"
      "s_or_b32 s19, s19, s48\n\ts_add_u32 s24, s19, 0x1000\n\tv_mfma_f32_32x32x16_bf16 v[32:47], v[136:139], a[8:11], v[32:47]\n\tv_cvt_pk_bf16_f32 v80, v48, v49\n\t"
      "v_cvt_pk_bf16_f32 v81, v50, v51\n\tv_cvt_pk_bf16_f32 v82, v56, v57\n\tv_cvt_pk_bf16_f32 v83, v58, v59\n\t"
      "v_mfma_f32_32x32x16_bf16 v[32:47], v[140:143], a[12:15], v[32:47]\n\tbuffer_store_dwordx4 v[80:83], v2, s[8:11], s25 offen offset:0 sc1\n\t"
      "v_mfma_f32_32x32x16_bf16 v[32:47], v[144:147], a[16:19], v[32:47]\n\tv_cvt_pk_bf16_f32 v84, v52, v53\n\tv_cvt_pk_bf16_f32 v85, v54, v55\n\t"
      "v_cvt_pk_bf16_f32 v86, v60, v61\n\tv_cvt_pk_bf16_f32 v87, v62, v63\n\tv_mfma_f32_32x32x16_bf16 v[32:47], v[148:151], a[20:23], v[32:47]\n\t"
      "buffer_store_dwordx4 v[84:87], v2, s[8:11], s25 offen offset:512 sc1\n\tv_mfma_f32_32x32x16_bf16 v[32:47], v[152:155], a[24:27], v[32:47]\n\ts_add_u32 s25, s20, 0x0\n\t"
      "v_mfma_f32_32x32x16_bf16 v[32:47], v[156:159], a[28:31], v[32:47]\n\tv_mov_b32 v4, s17\n\tv_mov_b32 v2, v1\n\t"
      TK1
      "s_waitcnt vmcnt(8)\n\t"
      TK2
      "v_readlane_b32 s23, v5, 0\n\tv_readlane_b32 s48, v5, 1\n\ts_add_u32 s48, s48, s26\n\ts_min_u32 s23, s23, s48\n\t"
      "v_mfma_f32_32x32x16_bf16 v[48:63], v[128:131], a[32:35], 0\n\t"
      "s_add_u32 s48, s17, 1\n\ts_cmp_gt_u32 s23, s48\n\ts_cselect_b32 s58, s27, 0\n\ts_add_u32 m0, s51, 8192\n\tv_mov_b32 v16, s58\n\t"
      "v_mfma_f32_32x32x16_bf16 v[48:63], v[132:135], a[36:39], v[48:63]\n\tds_write_b32 v7, v16 offset:4\n\ts_nop 3\n\t"
      "v_mfma_f32_32x32x16_bf16 v[48:63], v[136:139], a[40:43], v[48:63]\n\tv_cvt_pk_bf16_f32 v80, v32, v33\n\tv_cvt_pk_bf16_f32 v81, v34, v35\n\t"
      "v_cvt_pk_bf16_f32 v82, v40, v41\n\tv_cvt_pk_bf16_f32 v83, v42, v43\n\tbuffer_load_dwordx4 v0, s[56:59], s19 offen offset:0 sc1 lds\n\t"
      "v_mfma_f32_32x32x16_bf16 v[48:63], v[140:143], a[44:47], v[48:63]\n\tbuffer_store_dwordx4 v[80:83], v1, s[8:11], s25 offen offset:0 sc1\n\t"
      "v_mfma_f32_32x32x16_bf16 v[48:63], v[144:147], a[48:51], v[48:63]\n\tv_cvt_pk_bf16_f32 v84, v36, v37\n\tv_cvt_pk_bf16_f32 v85, v38, v39\n\t"
      "v_cvt_pk_bf16_f32 v86, v44, v45\n\tv_cvt_pk_bf16_f32 v87, v46, v47\n\tbuffer_load_dwordx4 v0, s[56:59], s19 offen offset:1024 sc1 lds\n\t"
      "v_mfma_f32_32x32x16_bf16 v[48:63], v[148:151], a[52:55], v[48:63]\n\tbuffer_store_dwordx4 v[84:87], v1, s[8:11], s25 offen offset:512 sc1\n\t"
      "v_mfma_f32_32x32x16_bf16 v[48:63], v[152:155], a[56:59], v[48:63]\n\ts_add_u32 s25, s20, 0x20000\n\tbuffer_load_dwordx4 v0, s[56:59], s19 offen offset:2048 sc1 lds\n\t"
      "v_mfma_f32_32x32x16_bf16 v[48:63], v[156:159], a[60:63], v[48:63]\n\tbuffer_load_dwordx4 v0, s[56:59], s19 offen offset:3072 sc1 lds\n\t"
      "v_mfma_f32_32x32x16_bf16 v[32:47], v[128:131], a[64:67], 0\n\ts_add_u32 m0, s51, 12288\n\ts_nop 3\n\tv_mfma_f32_32x32x16_bf16 v[32:47], v[132:135], a[68:71], v[32:47]\n\t"
      "s_nop 4\n\tv_mfma_f32_32x32x16_bf16 v[32:47], v[136:139], a[72:75], v[32:47]\n\tv_cvt_pk_bf16_f32 v80, v48, v49\n\tv_cvt_pk_bf16_f32 v81, v50, v51\n\t"
      "v_cvt_pk_bf16_f32 v82, v56, v57\n\tv_cvt_pk_bf16_f32 v83, v58, v59\n\tbuffer_load_dwordx4 v0, s[56:59], s24 offen offset:0 sc1 lds\n\t"
      "v_mfma_f32_32x32x16_bf16 v[32:47], v[140:143], a[76:79], v[32:47]\n\tbuffer_store_dwordx4 v[80:83], v1, s[8:11], s25 offen offset:0 sc1\n\t"
      "v_mfma_f32_32x32x16_bf16 v[32:47], v[144:147], a[80:83], v[32:47]\n\tv_cvt_pk_bf16_f32 v84, v52, v53\n\tv_cvt_pk_bf16_f32 v85, v54, v55\n\t"
      "v_cvt_pk_bf16_f32 v86, v60, v61\n\tv_cvt_pk_bf16_f32 v87, v62, v63\n\tbuffer_load_dwordx4 v0, s[56:59], s24 offen offset:1024 sc1 lds\n\t"
      "v_mfma_f32_32x32x16_bf16 v[32:47], v[148:151], a[84:87], v[32:47]\n\tbuffer_store_dwordx4 v[84:87], v1, s[8:11], s25 offen offset:512 sc1\n\t"
      "v_mfma_f32_32x32x16_bf16 v[32:47], v[152:155], a[88:91], v[32:47]\n\ts_add_u32 s25, s20, 0x40000\n\tbuffer_load_dwordx4 v0, s[56:59], s24 offen offset:2048 sc1 lds\n\t"
      "v_mfma_f32_32x32x16_bf16 v[32:47], v[156:159], a[92:95], v[32:47]\n\tbuffer_load_dwordx4 v0, s[56:59], s24 offen offset:3072 sc1 lds\n\t"
      "v_mfma_f32_32x32x16_bf16 v[48:63], v[128:131], a[96:99], 0\n\ts_nop 4\n\tv_mfma_f32_32x32x16_bf16 v[48:63], v[132:135], a[100:103], v[48:63]\n\ts_mul_i32 s26, s17, 0\n\t"
      "s_add_u32 s26, s26, 784\n\ts_mul_i32 s26, s26, 1171\n\ts_lshr_b32 s26, s26, 16\n\ts_min_u32 s26, s26, 14\n\t"
      "v_mfma_f32_32x32x16_bf16 v[48:63], v[136:139], a[104:107], v[48:63]\n\tv_cvt_pk_bf16_f32 v80, v32, v33\n\tv_cvt_pk_bf16_f32 v81, v34, v35\n\t"
      "v_cvt_pk_bf16_f32 v82, v40, v41\n\tv_cvt_pk_bf16_f32 v83, v42, v43\n\tv_mfma_f32_32x32x16_bf16 v[48:63], v[140:143], a[108:111], v[48:63]\n\t"
      "buffer_store_dwordx4 v[80:83], v1, s[8:11], s25 offen offset:0 sc1\n\tv_mfma_f32_32x32x16_bf16 v[48:63], v[144:147], a[112:115], v[48:63]\n\t"
      "v_cvt_pk_bf16_f32 v84, v36, v37\n\tv_cvt_pk_bf16_f32 v85, v38, v39\n\tv_cvt_pk_bf16_f32 v86, v44, v45\n\tv_cvt_pk_bf16_f32 v87, v46, v47\n\t"
      "v_mfma_f32_32x32x16_bf16 v[48:63], v[148:151], a[116:119], v[48:63]\n\tbuffer_store_dwordx4 v[84:87], v1, s[8:11], s25 offen offset:512 sc1\n\t"
      "v_mfma_f32_32x32x16_bf16 v[48:63], v[152:155], a[120:123], v[48:63]\n\ts_add_u32 s25, s20, 0x60000\n\t"
      "v_mfma_f32_32x32x16_bf16 v[48:63], v[156:159], a[124:127], v[48:63]\n\t"
      TK3
      "s_waitcnt vmcnt(14)\n\t"
      TK4
      "buffer_store_dword v4, v3, s[8:11], s22 offen sc1\n\tv_mfma_f32_32x32x16_bf16 v[32:47], v[128:131], a[128:131], 0\n\ts_nop 4\n\t"
      "v_mfma_f32_32x32x16_bf16 v[32:47], v[132:135], a[132:135], v[32:47]\n\ts_nop 4\n\tv_mfma_f32_32x32x16_bf16 v[32:47], v[136:139], a[136:139], v[32:47]\n\t"
      "v_cvt_pk_bf16_f32 v80, v48, v49\n\tv_cvt_pk_bf16_f32 v81, v50, v51\n\tv_cvt_pk_bf16_f32 v82, v56, v57\n\tv_cvt_pk_bf16_f32 v83, v58, v59\n\t"
      "v_mfma_f32_32x32x16_bf16 v[32:47], v[140:143], a[140:143], v[32:47]\n\tbuffer_store_dwordx4 v[80:83], v1, s[8:11], s25 offen offset:0 sc1\n\t"
      "v_mfma_f32_32x32x16_bf16 v[32:47], v[144:147], a[144:147], v[32:47]\n\tv_cvt_pk_bf16_f32 v84, v52, v53\n\tv_cvt_pk_bf16_f32 v85, v54, v55\n\t"
      "v_cvt_pk_bf16_f32 v86, v60, v61\n\tv_cvt_pk_bf16_f32 v87, v62, v63\n\tv_mfma_f32_32x32x16_bf16 v[32:47], v[148:151], a[148:151], v[32:47]\n\t"
      "buffer_store_dwordx4 v[84:87], v1, s[8:11], s25 offen offset:512 sc1\n\tv_mfma_f32_32x32x16_bf16 v[32:47], v[152:155], a[152:155], v[32:47]\n\t"
      "s_add_u32 s25, s20, 0x80000\n\tv_mfma_f32_32x32x16_bf16 v[32:47], v[156:159], a[156:159], v[32:47]\n\tv_mfma_f32_32x32x16_bf16 v[48:63], v[128:131], a[160:163], 0\n\t"
      "buffer_load_dword v5, v6, s[8:11], 0 offen sc1\n\ts_nop 3\n\tv_mfma_f32_32x32x16_bf16 v[48:63], v[132:135], a[164:167], v[48:63]\n\ts_nop 4\n\t"
      "v_mfma_f32_32x32x16_bf16 v[48:63], v[136:139], a[168:171], v[48:63]\n\tv_cvt_pk_bf16_f32 v80, v32, v33\n\tv_cvt_pk_bf16_f32 v81, v34, v35\n\t"
      "v_cvt_pk_bf16_f32 v82, v40, v41\n\tv_cvt_pk_bf16_f32 v83, v42, v43\n\tv_mfma_f32_32x32x16_bf16 v[48:63], v[140:143], a[172:175], v[48:63]\n\t"
      "buffer_store_dwordx4 v[80:83], v1, s[8:11], s25 offen offset:0 sc1\n\tv_mfma_f32_32x32x16_bf16 v[48:63], v[144:147], a[176:179], v[48:63]\n\t"
      "v_cvt_pk_bf16_f32 v84, v36, v37\n\tv_cvt_pk_bf16_f32 v85, v38, v39\n\tv_cvt_pk_bf16_f32 v86, v44, v45\n\tv_cvt_pk_bf16_f32 v87, v46, v47\n\t"
      "v_mfma_f32_32x32x16_bf16 v[48:63], v[148:151], a[180:183], v[48:63]\n\tbuffer_store_dwordx4 v[84:87], v1, s[8:11], s25 offen offset:512 sc1\n\t"
      "v_mfma_f32_32x32x16_bf16 v[48:63], v[152:155], a[184:187], v[48:63]\n\ts_add_u32 s25, s20, 0xa0000\n\t"
      "v_mfma_f32_32x32x16_bf16 v[48:63], v[156:159], a[188:191], v[48:63]\n\tv_mfma_f32_32x32x16_bf16 v[32:47], v[128:131], a[192:195], 0\n\ts_nop 4\n\t"
      "v_mfma_f32_32x32x16_bf16 v[32:47], v[132:135], a[196:199], v[32:47]\n\ts_nop 4\n\tv_mfma_f32_32x32x16_bf16 v[32:47], v[136:139], a[200:203], v[32:47]\n\t"
      "v_cvt_pk_bf16_f32 v80, v48, v49\n\tv_cvt_pk_bf16_f32 v81, v50, v51\n\tv_cvt_pk_bf16_f32 v82, v56, v57\n\tv_cvt_pk_bf16_f32 v83, v58, v59\n\t"
      "v_mfma_f32_32x32x16_bf16 v[32:47], v[140:143], a[204:207], v[32:47]\n\tbuffer_store_dwordx4 v[80:83], v1, s[8:11], s25 offen offset:0 sc1\n\t"
      "v_mfma_f32_32x32x16_bf16 v[32:47], v[144:147], a[208:211], v[32:47]\n\tv_cvt_pk_bf16_f32 v84, v52, v53\n\tv_cvt_pk_bf16_f32 v85, v54, v55\n\t"
      "v_cvt_pk_bf16_f32 v86, v60, v61\n\tv_cvt_pk_bf16_f32 v87, v62, v63\n\tv_mfma_f32_32x32x16_bf16 v[32:47], v[148:151], a[212:215], v[32:47]\n\t"
      "buffer_store_dwordx4 v[84:87], v1, s[8:11], s25 offen offset:512 sc1\n\tv_mfma_f32_32x32x16_bf16 v[32:47], v[152:155], a[216:219], v[32:47]\n\t"
      "s_add_u32 s25, s20, 0xc0000\n\tv_mfma_f32_32x32x16_bf16 v[32:47], v[156:159], a[220:223], v[32:47]\n\t"
      TK5
      "s_waitcnt vmcnt(10) lgkmcnt(0)\n\t"
      TK6
      "s_barrier\n\tds_read_b32 v16, v15 offset:4\n\tv_mfma_f32_32x32x16_bf16 v[48:63], v[128:131], a[224:227], 0\n\tds_read_b128 v[96:99], v0 offset:8192\n\t"
      "ds_read_b128 v[100:103], v0 offset:9216\n\tds_read_b128 v[104:107], v0 offset:10240\n\tds_read_b128 v[108:111], v0 offset:11264\n\ts_nop 0\n\t"
      "v_mfma_f32_32x32x16_bf16 v[48:63], v[132:135], a[228:231], v[48:63]\n\tds_read_b128 v[112:115], v0 offset:12288\n\tds_read_b128 v[116:119], v0 offset:13312\n\t"
      "ds_read_b128 v[120:123], v0 offset:14336\n\tds_read_b128 v[124:127], v0 offset:15360\n\ts_nop 0\n\tv_mfma_f32_32x32x16_bf16 v[48:63], v[136:139], a[232:235], v[48:63]\n\t"
      "v_cvt_pk_bf16_f32 v80, v32, v33\n\tv_cvt_pk_bf16_f32 v81, v34, v35\n\tv_cvt_pk_bf16_f32 v82, v40, v41\n\tv_cvt_pk_bf16_f32 v83, v42, v43\n\t"
      "v_mfma_f32_32x32x16_bf16 v[48:63], v[140:143], a[236:239], v[48:63]\n\tbuffer_store_dwordx4 v[80:83], v1, s[8:11], s25 offen offset:0 sc1\n\t"
      "v_mfma_f32_32x32x16_bf16 v[48:63], v[144:147], a[240:243], v[48:63]\n\tv_cvt_pk_bf16_f32 v84, v36, v37\n\tv_cvt_pk_bf16_f32 v85, v38, v39\n\t"
      "v_cvt_pk_bf16_f32 v86, v44, v45\n\tv_cvt_pk_bf16_f32 v87, v46, v47\n\tv_mfma_f32_32x32x16_bf16 v[48:63], v[148:151], a[244:247], v[48:63]\n\t"
      "buffer_store_dwordx4 v[84:87], v1, s[8:11], s25 offen offset:512 sc1\n\tv_mfma_f32_32x32x16_bf16 v[48:63], v[152:155], a[248:251], v[48:63]\n\t"
      "s_add_u32 s25, s20, 0xe0000\n\ts_waitcnt lgkmcnt(0)\n\tv_readfirstlane_b32 s28, v16\n\tv_mfma_f32_32x32x16_bf16 v[48:63], v[156:159], a[252:255], v[48:63]\n\t"
      "s_add_u32 s17, s17, 1\n\ts_add_u32 s20, s20, 0x400000\n\ts_cmp_eq_u32 s20, 0x5400000\n\ts_cselect_b32 s20, 0x1c00000, s20\n\ts_cmp_eq_u32 s28, 0\n\ts_cbranch_scc1 42f\n\t"
      "12:\n\t"
      STF("s_memrealtime vcc\n\ts_waitcnt lgkmcnt(0)\n\tv_mov_b32 v95, vcc_lo\n\ts_lshl_b32 vcc_hi, s17, 2\n\ts_add_u32 vcc_hi, vcc_hi, 161052\n\tv_mov_b32 v94, vcc_hi\n\tds_write_b32 v94, v95\n\ts_memtime vcc\n\ts_waitcnt lgkmcnt(0)\n\tv_mov_b32 v95, vcc_lo\n\tds_write_b32 v94, v95 offset:400\n\ts_waitcnt lgkmcnt(0)\n\t")
      TK7
      TKS("s_waitcnt lgkmcnt(0)\n\ts_cmp_lt_i32 s17, 0\n\ts_cbranch_scc1 98f\n\ts_cmp_lg_u32 s16, s91\n\ts_cbranch_scc1 98f\n\ts_mov_b64 exec, 1\n\ts_lshl_b32 s48, s17, 4\n\ts_add_u32 s48, s48, 161056\n\tv_mov_b32 v92, s48\n\ts_sub_u32 s48, s74, s90\n\ts_min_u32 s48, s48, 0xffff\n\tv_mov_b32 v93, s48\n\tds_write_b16 v92, v93 offset:0\n\ts_sub_u32 s48, s76, s74\n\ts_min_u32 s48, s48, 0xffff\n\tv_mov_b32 v93, s48\n\tds_write_b16 v92, v93 offset:2\n\ts_sub_u32 s48, s78, s76\n\ts_min_u32 s48, s48, 0xffff\n\tv_mov_b32 v93, s48\n\tds_write_b16 v92, v93 offset:4\n\ts_sub_u32 s48, s80, s78\n\ts_min_u32 s48, s48, 0xffff\n\tv_mov_b32 v93, s48\n\tds_write_b16 v92, v93 offset:6\n\ts_sub_u32 s48, s82, s80\n\ts_min_u32 s48, s48, 0xffff\n\tv_mov_b32 v93, s48\n\tds_write_b16 v92, v93 offset:8\n\ts_sub_u32 s48, s84, s82\n\ts_min_u32 s48, s48, 0xffff\n\tv_mov_b32 v93, s48\n\tds_write_b16 v92, v93 offset:10\n\ts_sub_u32 s48, s86, s84\n\ts_min_u32 s48, s48, 0xffff\n\tv_mov_b32 v93, s48\n\tds_write_b16 v92, v93 offset:12\n\ts_sub_u32 s48, s88, s86\n\ts_min_u32 s48, s48, 0xffff\n\tv_mov_b32 v93, s48\n\tds_write_b16 v92, v93 offset:14\n\ts_mov_b64 exec, -1\n\t98:\n\ts_mov_b32 s90, s88\n\t")
      "s_branch 10b\n\t"
      "40:\n\ts_cmp_lg_u32 s16, 0\n\ts_cbranch_scc1 47f\n\ts_waitcnt vmcnt(6)\n\tv_readlane_b32 s23, v5, 0\n\tv_readlane_b32 s48, v5, 1\n\ts_add_u32 s48, s48, s26\n\t"
      "s_min_u32 s23, s23, s48\n\ts_cmp_gt_u32 s23, s17\n\ts_cbranch_scc0 47f\n\ts_mov_b32 m0, 20480\n\ts_nop 0\n\t"
      "buffer_load_dwordx4 v0, s[8:11], s19 offen offset:0 sc1 lds\n\tbuffer_load_dwordx4 v0, s[8:11], s19 offen offset:1024 sc1 lds\n\t"
      "buffer_load_dwordx4 v0, s[8:11], s19 offen offset:2048 sc1 lds\n\tbuffer_load_dwordx4 v0, s[8:11], s19 offen offset:3072 sc1 lds\n\ts_mov_b32 m0, 24576\n\ts_nop 0\n\t"
      "buffer_load_dwordx4 v0, s[8:11], s24 offen offset:0 sc1 lds\n\tbuffer_load_dwordx4 v0, s[8:11], s24 offen offset:1024 sc1 lds\n\t"
      "buffer_load_dwordx4 v0, s[8:11], s24 offen offset:2048 sc1 lds\n\tbuffer_load_dwordx4 v0, s[8:11], s24 offen offset:3072 sc1 lds\n\tv_cvt_pk_bf16_f32 v80, v48, v49\n\t"
      "v_cvt_pk_bf16_f32 v81, v50, v51\n\tv_cvt_pk_bf16_f32 v82, v56, v57\n\tv_cvt_pk_bf16_f32 v83, v58, v59\n\tv_cvt_pk_bf16_f32 v84, v52, v53\n\t"
      "v_cvt_pk_bf16_f32 v85, v54, v55\n\tv_cvt_pk_bf16_f32 v86, v60, v61\n\tv_cvt_pk_bf16_f32 v87, v62, v63\n\t"
      "buffer_store_dwordx4 v[80:83], v1, s[8:11], s25 offen offset:0 sc1\n\tbuffer_store_dwordx4 v[84:87], v1, s[8:11], s25 offen offset:512 sc1\n\tv_mov_b32 v4, s17\n\t"
      "buffer_load_dword v5, v6, s[8:11], 0 offen sc1\n\ts_waitcnt vmcnt(1)\n\tbuffer_store_dword v4, v3, s[8:11], s22 offen sc1\n\ts_branch 48f\n\t"
      "47:\n\tbuffer_load_dword v5, v6, s[8:11], 0 offen sc1\n\ts_nop 8\n\tv_cvt_pk_bf16_f32 v80, v48, v49\n\tv_cvt_pk_bf16_f32 v81, v50, v51\n\t"
      "v_cvt_pk_bf16_f32 v82, v56, v57\n\tv_cvt_pk_bf16_f32 v83, v58, v59\n\tv_cvt_pk_bf16_f32 v84, v52, v53\n\tv_cvt_pk_bf16_f32 v85, v54, v55\n\t"
      "v_cvt_pk_bf16_f32 v86, v60, v61\n\tv_cvt_pk_bf16_f32 v87, v62, v63\n\tbuffer_store_dwordx4 v[80:83], v1, s[8:11], s25 offen offset:0 sc1\n\t"
      "buffer_store_dwordx4 v[84:87], v1, s[8:11], s25 offen offset:512 sc1\n\tv_mov_b32 v4, s17\n\ts_waitcnt vmcnt(0)\n\tbuffer_store_dword v4, v3, s[8:11], s22 offen sc1\n\t"
      "v_readlane_b32 s23, v5, 0\n\tv_readlane_b32 s48, v5, 1\n\ts_add_u32 s48, s48, s26\n\ts_min_u32 s23, s23, s48\n\ts_cmp_eq_u32 s17, 64\n\ts_cbranch_scc1 50f\n\t"
      "41:\n\ts_cmp_lg_u32 s16, 0\n\ts_cbranch_scc1 48f\n\t"
      "45:\n\ts_cmp_gt_u32 s23, s17\n\ts_cbranch_scc1 46f\n\tbuffer_load_dword v5, v6, s[8:11], 0 offen sc1\n\ts_waitcnt vmcnt(0)\n\tv_readlane_b32 s23, v5, 0\n\t"
      "v_readlane_b32 s48, v5, 1\n\ts_add_u32 s48, s48, s26\n\ts_min_u32 s23, s23, s48\n\ts_cmp_eq_u32 s18, 0\n\ts_cbranch_scc1 46f\n\ts_sub_u32 s18, s18, 1\n\ts_sleep 1\n\t"
      "s_branch 45b\n\t"
      "46:\n\ts_add_u32 s24, s19, 0x1000\n\ts_mov_b32 m0, 20480\n\ts_nop 0\n\tbuffer_load_dwordx4 v0, s[8:11], s19 offen offset:0 sc1 lds\n\t"
      "buffer_load_dwordx4 v0, s[8:11], s19 offen offset:1024 sc1 lds\n\tbuffer_load_dwordx4 v0, s[8:11], s19 offen offset:2048 sc1 lds\n\t"
      "buffer_load_dwordx4 v0, s[8:11], s19 offen offset:3072 sc1 lds\n\ts_mov_b32 m0, 24576\n\ts_nop 0\n\tbuffer_load_dwordx4 v0, s[8:11], s24 offen offset:0 sc1 lds\n\t"
      "buffer_load_dwordx4 v0, s[8:11], s24 offen offset:1024 sc1 lds\n\tbuffer_load_dwordx4 v0, s[8:11], s24 offen offset:2048 sc1 lds\n\t"
      "buffer_load_dwordx4 v0, s[8:11], s24 offen offset:3072 sc1 lds\n\tbuffer_load_dword v5, v6, s[8:11], 0 offen sc1\n\t"
      "s_waitcnt vmcnt(1)\n\t"
      "48:\n\ts_barrier\n\tds_read_b128 v[128:131], v0 offset:20480\n\tds_read_b128 v[132:135], v0 offset:21504\n\tds_read_b128 v[136:139], v0 offset:22528\n\t"
      "ds_read_b128 v[140:143], v0 offset:23552\n\tds_read_b128 v[144:147], v0 offset:24576\n\tds_read_b128 v[148:151], v0 offset:25600\n\t"
      "ds_read_b128 v[152:155], v0 offset:26624\n\tds_read_b128 v[156:159], v0 offset:27648\n\ts_waitcnt lgkmcnt(0)\n\ts_branch 11b\n\t"
      "42:\n\ts_cmp_lg_u32 s16, 0\n\ts_cbranch_scc1 47f\n\ts_waitcnt vmcnt(6)\n\tv_readlane_b32 s23, v5, 0\n\tv_readlane_b32 s48, v5, 1\n\ts_add_u32 s48, s48, s26\n\t"
      "s_min_u32 s23, s23, s48\n\ts_cmp_gt_u32 s23, s17\n\ts_cbranch_scc0 47f\n\ts_mov_b32 m0, 20480\n\ts_nop 0\n\t"
      "buffer_load_dwordx4 v0, s[8:11], s19 offen offset:0 sc1 lds\n\tbuffer_load_dwordx4 v0, s[8:11], s19 offen offset:1024 sc1 lds\n\t"
      "buffer_load_dwordx4 v0, s[8:11], s19 offen offset:2048 sc1 lds\n\tbuffer_load_dwordx4 v0, s[8:11], s19 offen offset:3072 sc1 lds\n\ts_mov_b32 m0, 24576\n\ts_nop 0\n\t"
      "buffer_load_dwordx4 v0, s[8:11], s24 offen offset:0 sc1 lds\n\tbuffer_load_dwordx4 v0, s[8:11], s24 offen offset:1024 sc1 lds\n\t"
      "buffer_load_dwordx4 v0, s[8:11], s24 offen offset:2048 sc1 lds\n\tbuffer_load_dwordx4 v0, s[8:11], s24 offen offset:3072 sc1 lds\n\tv_cvt_pk_bf16_f32 v80, v48, v49\n\t"
      "v_cvt_pk_bf16_f32 v81, v50, v51\n\tv_cvt_pk_bf16_f32 v82, v56, v57\n\tv_cvt_pk_bf16_f32 v83, v58, v59\n\tv_cvt_pk_bf16_f32 v84, v52, v53\n\t"
      "v_cvt_pk_bf16_f32 v85, v54, v55\n\tv_cvt_pk_bf16_f32 v86, v60, v61\n\tv_cvt_pk_bf16_f32 v87, v62, v63\n\t"
      "buffer_store_dwordx4 v[80:83], v1, s[8:11], s25 offen offset:0 sc1\n\tbuffer_store_dwordx4 v[84:87], v1, s[8:11], s25 offen offset:512 sc1\n\tv_mov_b32 v4, s17\n\t"
      "buffer_load_dword v5, v6, s[8:11], 0 offen sc1\n\ts_waitcnt vmcnt(1)\n\tbuffer_store_dword v4, v3, s[8:11], s22 offen sc1\n\ts_branch 48f\n\t"
      "47:\n\tbuffer_load_dword v5, v6, s[8:11], 0 offen sc1\n\ts_nop 8\n\tv_cvt_pk_bf16_f32 v80, v48, v49\n\tv_cvt_pk_bf16_f32 v81, v50, v51\n\t"
      "v_cvt_pk_bf16_f32 v82, v56, v57\n\tv_cvt_pk_bf16_f32 v83, v58, v59\n\tv_cvt_pk_bf16_f32 v84, v52, v53\n\tv_cvt_pk_bf16_f32 v85, v54, v55\n\t"
      "v_cvt_pk_bf16_f32 v86, v60, v61\n\tv_cvt_pk_bf16_f32 v87, v62, v63\n\tbuffer_store_dwordx4 v[80:83], v1, s[8:11], s25 offen offset:0 sc1\n\t"
      "buffer_store_dwordx4 v[84:87], v1, s[8:11], s25 offen offset:512 sc1\n\tv_mov_b32 v4, s17\n\ts_waitcnt vmcnt(0)\n\tbuffer_store_dword v4, v3, s[8:11], s22 offen sc1\n\t"
      "v_readlane_b32 s23, v5, 0\n\tv_readlane_b32 s48, v5, 1\n\ts_add_u32 s48, s48, s26\n\ts_min_u32 s23, s23, s48\n\ts_cmp_eq_u32 s17, 64\n\ts_cbranch_scc1 50f\n\t"
      "43:\n\ts_cmp_lg_u32 s16, 0\n\ts_cbranch_scc1 48f\n\t"
      "45:\n\ts_cmp_gt_u32 s23, s17\n\ts_cbranch_scc1 46f\n\tbuffer_load_dword v5, v6, s[8:11], 0 offen sc1\n\ts_waitcnt vmcnt(0)\n\tv_readlane_b32 s23, v5, 0\n\t"
      "v_readlane_b32 s48, v5, 1\n\ts_add_u32 s48, s48, s26\n\ts_min_u32 s23, s23, s48\n\ts_cmp_eq_u32 s18, 0\n\ts_cbranch_scc1 46f\n\ts_sub_u32 s18, s18, 1\n\ts_sleep 1\n\t"
      "s_branch 45b\n\t"
      "46:\n\ts_add_u32 s24, s19, 0x1000\n\ts_mov_b32 m0, 20480\n\ts_nop 0\n\tbuffer_load_dwordx4 v0, s[8:11], s19 offen offset:0 sc1 lds\n\t"
      "buffer_load_dwordx4 v0, s[8:11], s19 offen offset:1024 sc1 lds\n\tbuffer_load_dwordx4 v0, s[8:11], s19 offen offset:2048 sc1 lds\n\t"
      "buffer_load_dwordx4 v0, s[8:11], s19 offen offset:3072 sc1 lds\n\ts_mov_b32 m0, 24576\n\ts_nop 0\n\tbuffer_load_dwordx4 v0, s[8:11], s24 offen offset:0 sc1 lds\n\t"
      "buffer_load_dwordx4 v0, s[8:11], s24 offen offset:1024 sc1 lds\n\tbuffer_load_dwordx4 v0, s[8:11], s24 offen offset:2048 sc1 lds\n\t"
      "buffer_load_dwordx4 v0, s[8:11], s24 offen offset:3072 sc1 lds\n\tbuffer_load_dword v5, v6, s[8:11], 0 offen sc1\n\t"
      "s_waitcnt vmcnt(1)\n\t"
      "48:\n\ts_barrier\n\tds_read_b128 v[96:99], v0 offset:20480\n\tds_read_b128 v[100:103], v0 offset:21504\n\tds_read_b128 v[104:107], v0 offset:22528\n\t"
      "ds_read_b128 v[108:111], v0 offset:23552\n\tds_read_b128 v[112:115], v0 offset:24576\n\tds_read_b128 v[116:119], v0 offset:25600\n\t"
      "ds_read_b128 v[120:123], v0 offset:26624\n\tds_read_b128 v[124:127], v0 offset:27648\n\ts_waitcnt lgkmcnt(0)\n\ts_branch 12b\n\t"
      "50:\n\t"
      STF("s_memrealtime vcc\n\ts_waitcnt lgkmcnt(0)\n\tv_mov_b32 v95, vcc_lo\n\ts_lshl_b32 vcc_hi, s17, 2\n\ts_add_u32 vcc_hi, vcc_hi, 161052\n\tv_mov_b32 v94, vcc_hi\n\tds_write_b32 v94, v95\n\ts_memtime vcc\n\ts_waitcnt lgkmcnt(0)\n\tv_mov_b32 v95, vcc_lo\n\tds_write_b32 v94, v95 offset:400\n\ts_waitcnt lgkmcnt(0)\n\t")
      ".Lmk_end3:\n\ts_waitcnt vmcnt(0) lgkmcnt(0)\n\t"
      TKS("s_mul_i32 s48, s16, 72\n\ts_add_u32 s48, s48, 162080\n\tv_mov_b32 v224, s48\n\tds_read_b32 v225, v224 offset:0\n\ts_waitcnt lgkmcnt(0)\n\tv_readfirstlane_b32 s74, v225\n\tds_read_b32 v225, v224 offset:4\n\ts_waitcnt lgkmcnt(0)\n\tv_readfirstlane_b32 s75, v225\n\tds_read_b32 v225, v224 offset:8\n\ts_waitcnt lgkmcnt(0)\n\tv_readfirstlane_b32 s76, v225\n\tds_read_b32 v225, v224 offset:12\n\ts_waitcnt lgkmcnt(0)\n\tv_readfirstlane_b32 s77, v225\n\tds_read_b32 v225, v224 offset:16\n\ts_waitcnt lgkmcnt(0)\n\tv_readfirstlane_b32 s78, v225\n\tds_read_b32 v225, v224 offset:20\n\ts_waitcnt lgkmcnt(0)\n\tv_readfirstlane_b32 s79, v225\n\tds_read_b32 v225, v224 offset:24\n\ts_waitcnt lgkmcnt(0)\n\tv_readfirstlane_b32 s80, v225\n\tds_read_b32 v225, v224 offset:28\n\ts_waitcnt lgkmcnt(0)\n\tv_readfirstlane_b32 s81, v225\n\tds_read_b32 v225, v224 offset:32\n\ts_waitcnt lgkmcnt(0)\n\tv_readfirstlane_b32 s82, v225\n\tds_read_b32 v225, v224 offset:36\n\ts_waitcnt lgkmcnt(0)\n\tv_readfirstlane_b32 s83, v225\n\tds_read_b32 v225, v224 offset:40\n\ts_waitcnt lgkmcnt(0)\n\tv_readfirstlane_b32 s84, v225\n\tds_read_b32 v225, v224 offset:44\n\ts_waitcnt lgkmcnt(0)\n\tv_readfirstlane_b32 s85, v225\n\tds_read_b32 v225, v224 offset:48\n\ts_waitcnt lgkmcnt(0)\n\tv_readfirstlane_b32 s86, v225\n\tds_read_b32 v225, v224 offset:52\n\ts_waitcnt lgkmcnt(0)\n\tv_readfirstlane_b32 s87, v225\n\tds_read_b32 v225, v224 offset:56\n\ts_waitcnt lgkmcnt(0)\n\tv_readfirstlane_b32 s88, v225\n\tds_read_b32 v225, v224 offset:60\n\ts_waitcnt lgkmcnt(0)\n\tv_readfirstlane_b32 s89, v225\n\tds_read_b32 v225, v224 offset:64\n\ts_waitcnt lgkmcnt(0)\n\tv_readfirstlane_b32 s90, v225\n\tds_read_b32 v225, v224 offset:68\n\ts_waitcnt lgkmcnt(0)\n\tv_readfirstlane_b32 s91, v225\n\t")
      STS("s_lshl_b32 s48, s16, 2\n\ts_add_u32 s48, s48, 162944\n\tv_mov_b32 v0, s48\n\tv_mov_b32 v1, s50\n\tds_write_b32 v0, v1\n\ts_cmp_lg_u32 s16, 0\n\ts_cbranch_scc1 97f\n\tv_mov_b32 v1, s51\n\tds_write_b32 v0, v1 offset:16\n\t97:\n\ts_cmp_lg_u32 s16, 0\n\ts_cbranch_scc1 95f\n\tv_mov_b32 v1, s61\n\tds_write_b32 v0, v1 offset:20\n\tv_mov_b32 v1, s21\n\tds_write_b32 v0, v1 offset:24\n\tv_mov_b32 v1, s22\n\tds_write_b32 v0, v1 offset:28\n\t95:\n\t")
      "s_cmp_lg_u32 s18, 0\n\ts_cbranch_scc1 96f\n\tv_mov_b32 v1, 1\n\tbuffer_store_dword v1, off, s[12:15], 0 offset:3328 sc1\n\t"
      "96:\n\t"
      : "+{v[0:31]}"(k), "+{v[160:191]}"(s0), "+{v[192:223]}"(s1), ALL_REGS :: "memory", "vcc", "scc", S_CLOB TK_CLOB);
}

DEV void conv_role(Ctx& c, int cg) {
  const KArg p = enter(c);
  constexpr int CH = 96 * 80, U8 = 0;
  constexpr int CP = 4160, FC = 8 * CH;
  static_assert(FC + 16 * CP <= 160000, "lds");
  c.budget = SPIN_CAP;
  const int w = c.w, lane = c.lane = fresh_lane(), tid = w * 64 + lane;
  const int d0 = 8 * cg + 2 * w;
  const int de = (cg >> 3) * 64 + (tid & 7) * 8, t0 = (cg & 7) * 256 + (tid >> 3) * 8;
  auto group_wait = [&](int phase, unsigned need) {
    gu32* cnt = c.ctrl + (C_CONV + (phase * 16 + (cg >> 3)) * 128) / 4;
    VMWAIT(0);
    BAR();
    if (w == 0) {
      if ((phase || !KV_EARLY) && lane == 0) __hip_atomic_fetch_add(cnt, 1u, RLX_AGENT);
#pragma unroll 1
      while (__builtin_amdgcn_readfirstlane(__hip_atomic_load(cnt, RLX_AGENT)) < need) { if (spend(c)) break; __builtin_amdgcn_s_sleep(4); }
    }
    BAR();
  };
  const unsigned vote = __builtin_amdgcn_readfirstlane(__hip_atomic_load(c.ctrl + (C_VERDICT + c.att * 128) / 4, RLX_AGENT));
  const int va = vote_a(vote), far = va ^ ((cg >> 5) & 1) ^ 1;
  if (w == 0) {
#pragma unroll 1
    while (uni(__builtin_amdgcn_raw_buffer_load_b32(c.rs, 0, R_FZ + amap(((cg >> 5) * 8 + (cg & 7)) * 128, vote_b(vote)), AUX_SC1)) < ZWAIT) {
      if (spend(c)) break;
      __builtin_amdgcn_s_sleep(16);
      asm volatile("" ::: "memory");
    }
  }
  v4u fd[2][4], qr[8]; v2u fp[2][4];
  unsigned warm = 0;
  const unsigned dd = *(const GAS unsigned*)(p->ssmD + d0);
  auto build = [&](int k) {
#pragma unroll
    for (int i = 0; i < 4; ++i) {
      const v4u d = fd[k][i]; const v2u q = fp[k][i];
      const unsigned o0 = __builtin_amdgcn_alignbyte(q.y, q.x, 2), o1 = __builtin_amdgcn_alignbyte(d.x, q.y, 2), o2 = __builtin_amdgcn_alignbyte(d.y, d.x, 2),
                     o3 = __builtin_amdgcn_alignbyte(d.z, d.y, 2), o4 = __builtin_amdgcn_alignbyte(d.w, d.z, 2);
      const int at = FC + 4 * w * CP + 64 + i * 1024 + lane * 16;
      sts16(c.L, at, d); sts16(c.L, at + CP, v4u{o1, o2, o3, o4}); sts16(c.L, at + 2 * CP, v4u{q.y, d.x, d.y, d.z}); sts16(c.L, at + 3 * CP, v4u{o0, o1, o2, o3});
    }
  };
  {
    v4u u[8];
    BAR();
    KvIn in;
    kv_load(p, in, 4 * cg + w, lane);
#pragma unroll
    for (int k = 0; k < 2; ++k) {
      const rsrc_t rf = mkrs(p->ssmk + (d0 + k) * 2048, 4096);
#pragma unroll
      for (int i = 0; i < 4; ++i) { fd[k][i] = ld16n(rf, i * 1024 + lane * 16, 0); fp[k][i] = ld8n(rf, i * 1024 + lane * 16 - 8, 0); }
      if (k == 0 && KV_EARLY) {
#pragma unroll
        for (int i = 0; i < 8; ++i) u[i] = ld16s(c.rs, (i & 3) * 1024 + lane * 16, R_KVT + amap((d0 + (i >> 2)) * 4096, far));
      }
    }
    const rsrc_t rq = mkrs(p->bq, 2048 * 2048);
#pragma unroll
    for (int i = 0; i < 8; ++i) qr[i] = ld16n(rq, (t0 + i) * 2048 + de * 2, 0);
    if ((cg & 31) < 16) {
      const int n = (cg & 15) * 128 + (w & 1) * 64 + lane, zc = (cg >> 5) * 256 + 192;
      warm = __builtin_amdgcn_raw_buffer_load_b32(mkrs(w < 2 ? p->bq : p->ssmk, 2048 * 2048), w < 2 ? n * 2048 + zc * 2 : zc * 4096 + n * 128, 0, 0);
      if (!KV_EARLY) warm += __builtin_amdgcn_raw_buffer_load_b32(mkrs(w < 2 ? p->bk : p->bv, 2048 * 2048), n * 2048 + zc * 2, 0, 0);
    }
    SCHED();
    kv_make(c, in, FC + 4 * w * CP, lane);
    kv_store(c, FC + 4 * w * CP, 4 * cg + w, lane, va);
    LGKM(0);
#pragma unroll
    for (int i = 0; i < 5; ++i) { const int z = i * 64 + lane; sts16(c.L, U8 + (2 * w + z / 160) * CH + (z % 160) * 16, v4u{0, 0, 0, 0}); }
    if (lane < 16) sts16(c.L, FC + (4 * w + (lane >> 2)) * CP + (lane & 3) * 16, v4u{0, 0, 0, 0});
    build(0);
    group_wait(0, 8);
#pragma unroll
    for (int i = 0; i < 8; ++i) u[i] = ld16s(c.rs, (i & 3) * 1024 + lane * 16, R_KVT + amap((d0 + (i >> 2)) * 4096, far));
#pragma unroll
    for (int i = 0; i < 8; ++i) sts16(c.L, U8 + (2 * w + (i >> 2)) * CH + (32 + (i & 3) * 16 + (lane >> 2)) * 80 + (lane & 3) * 16, u[i]);
  }
  STAMP(5);
  const int h2 = lane >> 5, r = lane & 31;
#pragma unroll
  for (int k = 0; k < 2; ++k) {
    if (k == 1) build(1);
    f32x16 acc[2]; v32u x0, x1;
    const int ub = U8 + (2 * w + k) * CH;
    asm volatile("s_waitcnt lgkmcnt(0)\n\t"
      "ds_read_b64_tr_b16 v[160:161], %[fa] offset:40\n\tds_read_b64_tr_b16 v[162:163], %[fa] offset:32\n\tds_read_b128 v[168:171], %[ua] offset:2480\n\t"
      "ds_read_b128 v[172:175], %[ua] offset:5040\n\tds_read_b64_tr_b16 v[164:165], %[fa] offset:2088\n\tds_read_b64_tr_b16 v[166:167], %[fa] offset:2080\n\t"
      "ds_read_b64_tr_b16 v[176:177], %[fa] offset:8\n\tds_read_b64_tr_b16 v[178:179], %[fa] offset:0\n\tds_read_b128 v[184:187], %[ua] offset:2512\n\t"
      "ds_read_b128 v[188:191], %[ua] offset:5072\n\tds_read_b64_tr_b16 v[180:181], %[fa] offset:2056\n\tds_read_b64_tr_b16 v[182:183], %[fa] offset:2048\n\t"
      "s_waitcnt lgkmcnt(6)\n\tv_mfma_f32_32x32x16_bf16 v[128:143], v[160:163], v[168:171], 0\n\tds_read_b64_tr_b16 v[192:193], %[fa] offset:104\n\t"
      "ds_read_b64_tr_b16 v[194:195], %[fa] offset:96\n\tv_mfma_f32_32x32x16_bf16 v[144:159], v[160:163], v[172:175], 0\n\tds_read_b128 v[200:203], %[ua] offset:2400\n\t"
      "ds_read_b128 v[204:207], %[ua] offset:4960\n\tv_mfma_f32_32x32x16_bf16 v[144:159], v[164:167], v[168:171], v[144:159]\n\t"
      "ds_read_b64_tr_b16 v[196:197], %[fa] offset:2152\n\tds_read_b64_tr_b16 v[198:199], %[fa] offset:2144\n\ts_waitcnt lgkmcnt(6)\n\t"
      "v_mfma_f32_32x32x16_bf16 v[128:143], v[176:179], v[184:187], v[128:143]\n\tds_read_b64_tr_b16 v[208:209], %[fa] offset:72\n\t"
      "ds_read_b64_tr_b16 v[210:211], %[fa] offset:64\n\tv_mfma_f32_32x32x16_bf16 v[144:159], v[176:179], v[188:191], v[144:159]\n\t"
      "ds_read_b128 v[216:219], %[ua] offset:2432\n\tds_read_b128 v[220:223], %[ua] offset:4992\n\tv_mfma_f32_32x32x16_bf16 v[144:159], v[180:183], v[184:187], v[144:159]\n\t"
      "ds_read_b64_tr_b16 v[212:213], %[fa] offset:2120\n\tds_read_b64_tr_b16 v[214:215], %[fa] offset:2112\n\ts_waitcnt lgkmcnt(6)\n\t"
      "v_mfma_f32_32x32x16_bf16 v[128:143], v[192:195], v[200:203], v[128:143]\n\tds_read_b64_tr_b16 v[160:161], %[fa] offset:168\n\t"
      "ds_read_b64_tr_b16 v[162:163], %[fa] offset:160\n\tv_mfma_f32_32x32x16_bf16 v[144:159], v[192:195], v[204:207], v[144:159]\n\t"
      "ds_read_b128 v[168:171], %[ua] offset:2320\n\tds_read_b128 v[172:175], %[ua] offset:4880\n\tv_mfma_f32_32x32x16_bf16 v[144:159], v[196:199], v[200:203], v[144:159]\n\t"
      "ds_read_b64_tr_b16 v[164:165], %[fa] offset:2216\n\tds_read_b64_tr_b16 v[166:167], %[fa] offset:2208\n\ts_waitcnt lgkmcnt(6)\n\t"
      "v_mfma_f32_32x32x16_bf16 v[128:143], v[208:211], v[216:219], v[128:143]\n\tds_read_b64_tr_b16 v[176:177], %[fa] offset:136\n\t"
      "ds_read_b64_tr_b16 v[178:179], %[fa] offset:128\n\tv_mfma_f32_32x32x16_bf16 v[144:159], v[208:211], v[220:223], v[144:159]\n\t"
      "ds_read_b128 v[184:187], %[ua] offset:2352\n\tds_read_b128 v[188:191], %[ua] offset:4912\n\tv_mfma_f32_32x32x16_bf16 v[144:159], v[212:215], v[216:219], v[144:159]\n\t"
      "ds_read_b64_tr_b16 v[180:181], %[fa] offset:2184\n\tds_read_b64_tr_b16 v[182:183], %[fa] offset:2176\n\ts_waitcnt lgkmcnt(6)\n\t"
      "v_mfma_f32_32x32x16_bf16 v[128:143], v[160:163], v[168:171], v[128:143]\n\tds_read_b64_tr_b16 v[192:193], %[fa] offset:232\n\t"
      "ds_read_b64_tr_b16 v[194:195], %[fa] offset:224\n\tv_mfma_f32_32x32x16_bf16 v[144:159], v[160:163], v[172:175], v[144:159]\n\t"
      "ds_read_b128 v[200:203], %[ua] offset:2240\n\tds_read_b128 v[204:207], %[ua] offset:4800\n\tv_mfma_f32_32x32x16_bf16 v[144:159], v[164:167], v[168:171], v[144:159]\n\t"
      "ds_read_b64_tr_b16 v[196:197], %[fa] offset:2280\n\tds_read_b64_tr_b16 v[198:199], %[fa] offset:2272\n\ts_waitcnt lgkmcnt(6)\n\t"
      "v_mfma_f32_32x32x16_bf16 v[128:143], v[176:179], v[184:187], v[128:143]\n\tds_read_b64_tr_b16 v[208:209], %[fa] offset:200\n\t"
      "ds_read_b64_tr_b16 v[210:211], %[fa] offset:192\n\tv_mfma_f32_32x32x16_bf16 v[144:159], v[176:179], v[188:191], v[144:159]\n\t"
      "ds_read_b128 v[216:219], %[ua] offset:2272\n\tds_read_b128 v[220:223], %[ua] offset:4832\n\tv_mfma_f32_32x32x16_bf16 v[144:159], v[180:183], v[184:187], v[144:159]\n\t"
      "ds_read_b64_tr_b16 v[212:213], %[fa] offset:2248\n\tds_read_b64_tr_b16 v[214:215], %[fa] offset:2240\n\ts_waitcnt lgkmcnt(6)\n\t"
      "v_mfma_f32_32x32x16_bf16 v[128:143], v[192:195], v[200:203], v[128:143]\n\tds_read_b64_tr_b16 v[160:161], %[fa] offset:296\n\t"
      "ds_read_b64_tr_b16 v[162:163], %[fa] offset:288\n\tv_mfma_f32_32x32x16_bf16 v[144:159], v[192:195], v[204:207], v[144:159]\n\t"
      "ds_read_b128 v[168:171], %[ua] offset:2160\n\tds_read_b128 v[172:175], %[ua] offset:4720\n\tv_mfma_f32_32x32x16_bf16 v[144:159], v[196:199], v[200:203], v[144:159]\n\t"
      "ds_read_b64_tr_b16 v[164:165], %[fa] offset:2344\n\tds_read_b64_tr_b16 v[166:167], %[fa] offset:2336\n\ts_waitcnt lgkmcnt(6)\n\t"
      "v_mfma_f32_32x32x16_bf16 v[128:143], v[208:211], v[216:219], v[128:143]\n\tds_read_b64_tr_b16 v[176:177], %[fa] offset:264\n\t"
      "ds_read_b64_tr_b16 v[178:179], %[fa] offset:256\n\tv_mfma_f32_32x32x16_bf16 v[144:159], v[208:211], v[220:223], v[144:159]\n\t"
      "ds_read_b128 v[184:187], %[ua] offset:2192\n\tds_read_b128 v[188:191], %[ua] offset:4752\n\tv_mfma_f32_32x32x16_bf16 v[144:159], v[212:215], v[216:219], v[144:159]\n\t"
      "ds_read_b64_tr_b16 v[180:181], %[fa] offset:2312\n\tds_read_b64_tr_b16 v[182:183], %[fa] offset:2304\n\ts_waitcnt lgkmcnt(6)\n\t"
      "v_mfma_f32_32x32x16_bf16 v[128:143], v[160:163], v[168:171], v[128:143]\n\tds_read_b64_tr_b16 v[192:193], %[fa] offset:360\n\t"
      "ds_read_b64_tr_b16 v[194:195], %[fa] offset:352\n\tv_mfma_f32_32x32x16_bf16 v[144:159], v[160:163], v[172:175], v[144:159]\n\t"
      "ds_read_b128 v[200:203], %[ua] offset:2080\n\tds_read_b128 v[204:207], %[ua] offset:4640\n\tv_mfma_f32_32x32x16_bf16 v[144:159], v[164:167], v[168:171], v[144:159]\n\t"
      "ds_read_b64_tr_b16 v[196:197], %[fa] offset:2408\n\tds_read_b64_tr_b16 v[198:199], %[fa] offset:2400\n\ts_waitcnt lgkmcnt(6)\n\t"
      "v_mfma_f32_32x32x16_bf16 v[128:143], v[176:179], v[184:187], v[128:143]\n\tds_read_b64_tr_b16 v[208:209], %[fa] offset:328\n\t"
      "ds_read_b64_tr_b16 v[210:211], %[fa] offset:320\n\tv_mfma_f32_32x32x16_bf16 v[144:159], v[176:179], v[188:191], v[144:159]\n\t"
      "ds_read_b128 v[216:219], %[ua] offset:2112\n\tds_read_b128 v[220:223], %[ua] offset:4672\n\tv_mfma_f32_32x32x16_bf16 v[144:159], v[180:183], v[184:187], v[144:159]\n\t"
      "ds_read_b64_tr_b16 v[212:213], %[fa] offset:2376\n\tds_read_b64_tr_b16 v[214:215], %[fa] offset:2368\n\ts_waitcnt lgkmcnt(6)\n\t"
      "v_mfma_f32_32x32x16_bf16 v[128:143], v[192:195], v[200:203], v[128:143]\n\tds_read_b64_tr_b16 v[160:161], %[fa] offset:424\n\t"
      "ds_read_b64_tr_b16 v[162:163], %[fa] offset:416\n\tv_mfma_f32_32x32x16_bf16 v[144:159], v[192:195], v[204:207], v[144:159]\n\t"
      "ds_read_b128 v[168:171], %[ua] offset:2000\n\tds_read_b128 v[172:175], %[ua] offset:4560\n\tv_mfma_f32_32x32x16_bf16 v[144:159], v[196:199], v[200:203], v[144:159]\n\t"
      "ds_read_b64_tr_b16 v[164:165], %[fa] offset:2472\n\tds_read_b64_tr_b16 v[166:167], %[fa] offset:2464\n\ts_waitcnt lgkmcnt(6)\n\t"
      "v_mfma_f32_32x32x16_bf16 v[128:143], v[208:211], v[216:219], v[128:143]\n\tds_read_b64_tr_b16 v[176:177], %[fa] offset:392\n\t"
      "ds_read_b64_tr_b16 v[178:179], %[fa] offset:384\n\tv_mfma_f32_32x32x16_bf16 v[144:159], v[208:211], v[220:223], v[144:159]\n\t"
      "ds_read_b128 v[184:187], %[ua] offset:2032\n\tds_read_b128 v[188:191], %[ua] offset:4592\n\tv_mfma_f32_32x32x16_bf16 v[144:159], v[212:215], v[216:219], v[144:159]\n\t"
      "ds_read_b64_tr_b16 v[180:181], %[fa] offset:2440\n\tds_read_b64_tr_b16 v[182:183], %[fa] offset:2432\n\ts_waitcnt lgkmcnt(6)\n\t"
      "v_mfma_f32_32x32x16_bf16 v[128:143], v[160:163], v[168:171], v[128:143]\n\tds_read_b64_tr_b16 v[192:193], %[fa] offset:488\n\t"
      "ds_read_b64_tr_b16 v[194:195], %[fa] offset:480\n\tv_mfma_f32_32x32x16_bf16 v[144:159], v[160:163], v[172:175], v[144:159]\n\t"
      "ds_read_b128 v[200:203], %[ua] offset:1920\n\tds_read_b128 v[204:207], %[ua] offset:4480\n\tv_mfma_f32_32x32x16_bf16 v[144:159], v[164:167], v[168:171], v[144:159]\n\t"
      "ds_read_b64_tr_b16 v[196:197], %[fa] offset:2536\n\tds_read_b64_tr_b16 v[198:199], %[fa] offset:2528\n\ts_waitcnt lgkmcnt(6)\n\t"
      "v_mfma_f32_32x32x16_bf16 v[128:143], v[176:179], v[184:187], v[128:143]\n\tds_read_b64_tr_b16 v[208:209], %[fa] offset:456\n\t"
      "ds_read_b64_tr_b16 v[210:211], %[fa] offset:448\n\tv_mfma_f32_32x32x16_bf16 v[144:159], v[176:179], v[188:191], v[144:159]\n\t"
      "ds_read_b128 v[216:219], %[ua] offset:1952\n\tds_read_b128 v[220:223], %[ua] offset:4512\n\tv_mfma_f32_32x32x16_bf16 v[144:159], v[180:183], v[184:187], v[144:159]\n\t"
      "ds_read_b64_tr_b16 v[212:213], %[fa] offset:2504\n\tds_read_b64_tr_b16 v[214:215], %[fa] offset:2496\n\ts_waitcnt lgkmcnt(6)\n\t"
      "v_mfma_f32_32x32x16_bf16 v[128:143], v[192:195], v[200:203], v[128:143]\n\tds_read_b64_tr_b16 v[160:161], %[fa] offset:552\n\t"
      "ds_read_b64_tr_b16 v[162:163], %[fa] offset:544\n\tv_mfma_f32_32x32x16_bf16 v[144:159], v[192:195], v[204:207], v[144:159]\n\t"
      "ds_read_b128 v[168:171], %[ua] offset:1840\n\tds_read_b128 v[172:175], %[ua] offset:4400\n\tv_mfma_f32_32x32x16_bf16 v[144:159], v[196:199], v[200:203], v[144:159]\n\t"
      "ds_read_b64_tr_b16 v[164:165], %[fa] offset:2600\n\tds_read_b64_tr_b16 v[166:167], %[fa] offset:2592\n\ts_waitcnt lgkmcnt(6)\n\t"
      "v_mfma_f32_32x32x16_bf16 v[128:143], v[208:211], v[216:219], v[128:143]\n\tds_read_b64_tr_b16 v[176:177], %[fa] offset:520\n\t"
      "ds_read_b64_tr_b16 v[178:179], %[fa] offset:512\n\tv_mfma_f32_32x32x16_bf16 v[144:159], v[208:211], v[220:223], v[144:159]\n\t"
      "ds_read_b128 v[184:187], %[ua] offset:1872\n\tds_read_b128 v[188:191], %[ua] offset:4432\n\tv_mfma_f32_32x32x16_bf16 v[144:159], v[212:215], v[216:219], v[144:159]\n\t"
      "ds_read_b64_tr_b16 v[180:181], %[fa] offset:2568\n\tds_read_b64_tr_b16 v[182:183], %[fa] offset:2560\n\ts_waitcnt lgkmcnt(6)\n\t"
      "v_mfma_f32_32x32x16_bf16 v[128:143], v[160:163], v[168:171], v[128:143]\n\tds_read_b64_tr_b16 v[192:193], %[fa] offset:616\n\t"
      "ds_read_b64_tr_b16 v[194:195], %[fa] offset:608\n\tv_mfma_f32_32x32x16_bf16 v[144:159], v[160:163], v[172:175], v[144:159]\n\t"
      "ds_read_b128 v[200:203], %[ua] offset:1760\n\tds_read_b128 v[204:207], %[ua] offset:4320\n\tv_mfma_f32_32x32x16_bf16 v[144:159], v[164:167], v[168:171], v[144:159]\n\t"
      "ds_read_b64_tr_b16 v[196:197], %[fa] offset:2664\n\tds_read_b64_tr_b16 v[198:199], %[fa] offset:2656\n\ts_waitcnt lgkmcnt(6)\n\t"
      "v_mfma_f32_32x32x16_bf16 v[128:143], v[176:179], v[184:187], v[128:143]\n\tds_read_b64_tr_b16 v[208:209], %[fa] offset:584\n\t"
      "ds_read_b64_tr_b16 v[210:211], %[fa] offset:576\n\tv_mfma_f32_32x32x16_bf16 v[144:159], v[176:179], v[188:191], v[144:159]\n\t"
      "ds_read_b128 v[216:219], %[ua] offset:1792\n\tds_read_b128 v[220:223], %[ua] offset:4352\n\tv_mfma_f32_32x32x16_bf16 v[144:159], v[180:183], v[184:187], v[144:159]\n\t"
      "ds_read_b64_tr_b16 v[212:213], %[fa] offset:2632\n\tds_read_b64_tr_b16 v[214:215], %[fa] offset:2624\n\ts_waitcnt lgkmcnt(6)\n\t"
      "v_mfma_f32_32x32x16_bf16 v[128:143], v[192:195], v[200:203], v[128:143]\n\tds_read_b64_tr_b16 v[160:161], %[fa] offset:680\n\t"
      "ds_read_b64_tr_b16 v[162:163], %[fa] offset:672\n\tv_mfma_f32_32x32x16_bf16 v[144:159], v[192:195], v[204:207], v[144:159]\n\t"
      "ds_read_b128 v[168:171], %[ua] offset:1680\n\tds_read_b128 v[172:175], %[ua] offset:4240\n\tv_mfma_f32_32x32x16_bf16 v[144:159], v[196:199], v[200:203], v[144:159]\n\t"
      "ds_read_b64_tr_b16 v[164:165], %[fa] offset:2728\n\tds_read_b64_tr_b16 v[166:167], %[fa] offset:2720\n\ts_waitcnt lgkmcnt(6)\n\t"
      "v_mfma_f32_32x32x16_bf16 v[128:143], v[208:211], v[216:219], v[128:143]\n\tds_read_b64_tr_b16 v[176:177], %[fa] offset:648\n\t"
      "ds_read_b64_tr_b16 v[178:179], %[fa] offset:640\n\tv_mfma_f32_32x32x16_bf16 v[144:159], v[208:211], v[220:223], v[144:159]\n\t"
      "ds_read_b128 v[184:187], %[ua] offset:1712\n\tds_read_b128 v[188:191], %[ua] offset:4272\n\tv_mfma_f32_32x32x16_bf16 v[144:159], v[212:215], v[216:219], v[144:159]\n\t"
      "ds_read_b64_tr_b16 v[180:181], %[fa] offset:2696\n\tds_read_b64_tr_b16 v[182:183], %[fa] offset:2688\n\ts_waitcnt lgkmcnt(6)\n\t"
      "v_mfma_f32_32x32x16_bf16 v[128:143], v[160:163], v[168:171], v[128:143]\n\tds_read_b64_tr_b16 v[192:193], %[fa] offset:744\n\t"
      "ds_read_b64_tr_b16 v[194:195], %[fa] offset:736\n\tv_mfma_f32_32x32x16_bf16 v[144:159], v[160:163], v[172:175], v[144:159]\n\t"
      "ds_read_b128 v[200:203], %[ua] offset:1600\n\tds_read_b128 v[204:207], %[ua] offset:4160\n\tv_mfma_f32_32x32x16_bf16 v[144:159], v[164:167], v[168:171], v[144:159]\n\t"
      "ds_read_b64_tr_b16 v[196:197], %[fa] offset:2792\n\tds_read_b64_tr_b16 v[198:199], %[fa] offset:2784\n\ts_waitcnt lgkmcnt(6)\n\t"
      "v_mfma_f32_32x32x16_bf16 v[128:143], v[176:179], v[184:187], v[128:143]\n\tds_read_b64_tr_b16 v[208:209], %[fa] offset:712\n\t"
      "ds_read_b64_tr_b16 v[210:211], %[fa] offset:704\n\tv_mfma_f32_32x32x16_bf16 v[144:159], v[176:179], v[188:191], v[144:159]\n\t"
      "ds_read_b128 v[216:219], %[ua] offset:1632\n\tds_read_b128 v[220:223], %[ua] offset:4192\n\tv_mfma_f32_32x32x16_bf16 v[144:159], v[180:183], v[184:187], v[144:159]\n\t"
      "ds_read_b64_tr_b16 v[212:213], %[fa] offset:2760\n\tds_read_b64_tr_b16 v[214:215], %[fa] offset:2752\n\ts_waitcnt lgkmcnt(6)\n\t"
      "v_mfma_f32_32x32x16_bf16 v[128:143], v[192:195], v[200:203], v[128:143]\n\tds_read_b64_tr_b16 v[160:161], %[fa] offset:808\n\t"
      "ds_read_b64_tr_b16 v[162:163], %[fa] offset:800\n\tv_mfma_f32_32x32x16_bf16 v[144:159], v[192:195], v[204:207], v[144:159]\n\t"
      "ds_read_b128 v[168:171], %[ua] offset:1520\n\tds_read_b128 v[172:175], %[ua] offset:4080\n\tv_mfma_f32_32x32x16_bf16 v[144:159], v[196:199], v[200:203], v[144:159]\n\t"
      "ds_read_b64_tr_b16 v[164:165], %[fa] offset:2856\n\tds_read_b64_tr_b16 v[166:167], %[fa] offset:2848\n\ts_waitcnt lgkmcnt(6)\n\t"
      "v_mfma_f32_32x32x16_bf16 v[128:143], v[208:211], v[216:219], v[128:143]\n\tds_read_b64_tr_b16 v[176:177], %[fa] offset:776\n\t"
      "ds_read_b64_tr_b16 v[178:179], %[fa] offset:768\n\tv_mfma_f32_32x32x16_bf16 v[144:159], v[208:211], v[220:223], v[144:159]\n\t"
      "ds_read_b128 v[184:187], %[ua] offset:1552\n\tds_read_b128 v[188:191], %[ua] offset:4112\n\tv_mfma_f32_32x32x16_bf16 v[144:159], v[212:215], v[216:219], v[144:159]\n\t"
      "ds_read_b64_tr_b16 v[180:181], %[fa] offset:2824\n\tds_read_b64_tr_b16 v[182:183], %[fa] offset:2816\n\ts_waitcnt lgkmcnt(6)\n\t"
      "v_mfma_f32_32x32x16_bf16 v[128:143], v[160:163], v[168:171], v[128:143]\n\tds_read_b64_tr_b16 v[192:193], %[fa] offset:872\n\t"
      "ds_read_b64_tr_b16 v[194:195], %[fa] offset:864\n\tv_mfma_f32_32x32x16_bf16 v[144:159], v[160:163], v[172:175], v[144:159]\n\t"
      "ds_read_b128 v[200:203], %[ua] offset:1440\n\tds_read_b128 v[204:207], %[ua] offset:4000\n\tv_mfma_f32_32x32x16_bf16 v[144:159], v[164:167], v[168:171], v[144:159]\n\t"
      "ds_read_b64_tr_b16 v[196:197], %[fa] offset:2920\n\tds_read_b64_tr_b16 v[198:199], %[fa] offset:2912\n\ts_waitcnt lgkmcnt(6)\n\t"
      "v_mfma_f32_32x32x16_bf16 v[128:143], v[176:179], v[184:187], v[128:143]\n\tds_read_b64_tr_b16 v[208:209], %[fa] offset:840\n\t"
      "ds_read_b64_tr_b16 v[210:211], %[fa] offset:832\n\tv_mfma_f32_32x32x16_bf16 v[144:159], v[176:179], v[188:191], v[144:159]\n\t"
      "ds_read_b128 v[216:219], %[ua] offset:1472\n\tds_read_b128 v[220:223], %[ua] offset:4032\n\tv_mfma_f32_32x32x16_bf16 v[144:159], v[180:183], v[184:187], v[144:159]\n\t"
      "ds_read_b64_tr_b16 v[212:213], %[fa] offset:2888\n\tds_read_b64_tr_b16 v[214:215], %[fa] offset:2880\n\ts_waitcnt lgkmcnt(6)\n\t"
      "v_mfma_f32_32x32x16_bf16 v[128:143], v[192:195], v[200:203], v[128:143]\n\tds_read_b64_tr_b16 v[160:161], %[fa] offset:936\n\t"
      "ds_read_b64_tr_b16 v[162:163], %[fa] offset:928\n\tv_mfma_f32_32x32x16_bf16 v[144:159], v[192:195], v[204:207], v[144:159]\n\t"
      "ds_read_b128 v[168:171], %[ua] offset:1360\n\tds_read_b128 v[172:175], %[ua] offset:3920\n\tv_mfma_f32_32x32x16_bf16 v[144:159], v[196:199], v[200:203], v[144:159]\n\t"
      "ds_read_b64_tr_b16 v[164:165], %[fa] offset:2984\n\tds_read_b64_tr_b16 v[166:167], %[fa] offset:2976\n\ts_waitcnt lgkmcnt(6)\n\t"
      "v_mfma_f32_32x32x16_bf16 v[128:143], v[208:211], v[216:219], v[128:143]\n\tds_read_b64_tr_b16 v[176:177], %[fa] offset:904\n\t"
      "ds_read_b64_tr_b16 v[178:179], %[fa] offset:896\n\tv_mfma_f32_32x32x16_bf16 v[144:159], v[208:211], v[220:223], v[144:159]\n\t"
      "ds_read_b128 v[184:187], %[ua] offset:1392\n\tds_read_b128 v[188:191], %[ua] offset:3952\n\tv_mfma_f32_32x32x16_bf16 v[144:159], v[212:215], v[216:219], v[144:159]\n\t"
      "ds_read_b64_tr_b16 v[180:181], %[fa] offset:2952\n\tds_read_b64_tr_b16 v[182:183], %[fa] offset:2944\n\ts_waitcnt lgkmcnt(6)\n\t"
      "v_mfma_f32_32x32x16_bf16 v[128:143], v[160:163], v[168:171], v[128:143]\n\tds_read_b64_tr_b16 v[192:193], %[fa] offset:1000\n\t"
      "ds_read_b64_tr_b16 v[194:195], %[fa] offset:992\n\tv_mfma_f32_32x32x16_bf16 v[144:159], v[160:163], v[172:175], v[144:159]\n\t"
      "ds_read_b128 v[200:203], %[ua] offset:1280\n\tds_read_b128 v[204:207], %[ua] offset:3840\n\tv_mfma_f32_32x32x16_bf16 v[144:159], v[164:167], v[168:171], v[144:159]\n\t"
      "ds_read_b64_tr_b16 v[196:197], %[fa] offset:3048\n\tds_read_b64_tr_b16 v[198:199], %[fa] offset:3040\n\ts_waitcnt lgkmcnt(6)\n\t"
      "v_mfma_f32_32x32x16_bf16 v[128:143], v[176:179], v[184:187], v[128:143]\n\tds_read_b64_tr_b16 v[208:209], %[fa] offset:968\n\t"
      "ds_read_b64_tr_b16 v[210:211], %[fa] offset:960\n\tv_mfma_f32_32x32x16_bf16 v[144:159], v[176:179], v[188:191], v[144:159]\n\t"
      "ds_read_b128 v[216:219], %[ua] offset:1312\n\tds_read_b128 v[220:223], %[ua] offset:3872\n\tv_mfma_f32_32x32x16_bf16 v[144:159], v[180:183], v[184:187], v[144:159]\n\t"
      "ds_read_b64_tr_b16 v[212:213], %[fa] offset:3016\n\tds_read_b64_tr_b16 v[214:215], %[fa] offset:3008\n\ts_waitcnt lgkmcnt(6)\n\t"
      "v_mfma_f32_32x32x16_bf16 v[128:143], v[192:195], v[200:203], v[128:143]\n\tds_read_b64_tr_b16 v[160:161], %[fa] offset:1064\n\t"
      "ds_read_b64_tr_b16 v[162:163], %[fa] offset:1056\n\tv_mfma_f32_32x32x16_bf16 v[144:159], v[192:195], v[204:207], v[144:159]\n\t"
      "ds_read_b128 v[168:171], %[ua] offset:1200\n\tds_read_b128 v[172:175], %[ua] offset:3760\n\tv_mfma_f32_32x32x16_bf16 v[144:159], v[196:199], v[200:203], v[144:159]\n\t"
      "ds_read_b64_tr_b16 v[164:165], %[fa] offset:3112\n\tds_read_b64_tr_b16 v[166:167], %[fa] offset:3104\n\ts_waitcnt lgkmcnt(6)\n\t"
      "v_mfma_f32_32x32x16_bf16 v[128:143], v[208:211], v[216:219], v[128:143]\n\tds_read_b64_tr_b16 v[176:177], %[fa] offset:1032\n\t"
      "ds_read_b64_tr_b16 v[178:179], %[fa] offset:1024\n\tv_mfma_f32_32x32x16_bf16 v[144:159], v[208:211], v[220:223], v[144:159]\n\t"
      "ds_read_b128 v[184:187], %[ua] offset:1232\n\tds_read_b128 v[188:191], %[ua] offset:3792\n\tv_mfma_f32_32x32x16_bf16 v[144:159], v[212:215], v[216:219], v[144:159]\n\t"
      "ds_read_b64_tr_b16 v[180:181], %[fa] offset:3080\n\tds_read_b64_tr_b16 v[182:183], %[fa] offset:3072\n\ts_waitcnt lgkmcnt(6)\n\t"
      "v_mfma_f32_32x32x16_bf16 v[128:143], v[160:163], v[168:171], v[128:143]\n\tds_read_b64_tr_b16 v[192:193], %[fa] offset:1128\n\t"
      "ds_read_b64_tr_b16 v[194:195], %[fa] offset:1120\n\tv_mfma_f32_32x32x16_bf16 v[144:159], v[160:163], v[172:175], v[144:159]\n\t"
      "ds_read_b128 v[200:203], %[ua] offset:1120\n\tds_read_b128 v[204:207], %[ua] offset:3680\n\tv_mfma_f32_32x32x16_bf16 v[144:159], v[164:167], v[168:171], v[144:159]\n\t"
      "ds_read_b64_tr_b16 v[196:197], %[fa] offset:3176\n\tds_read_b64_tr_b16 v[198:199], %[fa] offset:3168\n\ts_waitcnt lgkmcnt(6)\n\t"
      "v_mfma_f32_32x32x16_bf16 v[128:143], v[176:179], v[184:187], v[128:143]\n\tds_read_b64_tr_b16 v[208:209], %[fa] offset:1096\n\t"
      "ds_read_b64_tr_b16 v[210:211], %[fa] offset:1088\n\tv_mfma_f32_32x32x16_bf16 v[144:159], v[176:179], v[188:191], v[144:159]\n\t"
      "ds_read_b128 v[216:219], %[ua] offset:1152\n\tds_read_b128 v[220:223], %[ua] offset:3712\n\tv_mfma_f32_32x32x16_bf16 v[144:159], v[180:183], v[184:187], v[144:159]\n\t"
      "ds_read_b64_tr_b16 v[212:213], %[fa] offset:3144\n\tds_read_b64_tr_b16 v[214:215], %[fa] offset:3136\n\ts_waitcnt lgkmcnt(6)\n\t"
      "v_mfma_f32_32x32x16_bf16 v[128:143], v[192:195], v[200:203], v[128:143]\n\tds_read_b64_tr_b16 v[160:161], %[fa] offset:1192\n\t"
      "ds_read_b64_tr_b16 v[162:163], %[fa] offset:1184\n\tv_mfma_f32_32x32x16_bf16 v[144:159], v[192:195], v[204:207], v[144:159]\n\t"
      "ds_read_b128 v[168:171], %[ua] offset:1040\n\tds_read_b128 v[172:175], %[ua] offset:3600\n\tv_mfma_f32_32x32x16_bf16 v[144:159], v[196:199], v[200:203], v[144:159]\n\t"
      "ds_read_b64_tr_b16 v[164:165], %[fa] offset:3240\n\tds_read_b64_tr_b16 v[166:167], %[fa] offset:3232\n\ts_waitcnt lgkmcnt(6)\n\t"
      "v_mfma_f32_32x32x16_bf16 v[128:143], v[208:211], v[216:219], v[128:143]\n\tds_read_b64_tr_b16 v[176:177], %[fa] offset:1160\n\t"
      "ds_read_b64_tr_b16 v[178:179], %[fa] offset:1152\n\tv_mfma_f32_32x32x16_bf16 v[144:159], v[208:211], v[220:223], v[144:159]\n\t"
      "ds_read_b128 v[184:187], %[ua] offset:1072\n\tds_read_b128 v[188:191], %[ua] offset:3632\n\tv_mfma_f32_32x32x16_bf16 v[144:159], v[212:215], v[216:219], v[144:159]\n\t"
      "ds_read_b64_tr_b16 v[180:181], %[fa] offset:3208\n\tds_read_b64_tr_b16 v[182:183], %[fa] offset:3200\n\ts_waitcnt lgkmcnt(6)\n\t"
      "v_mfma_f32_32x32x16_bf16 v[128:143], v[160:163], v[168:171], v[128:143]\n\tds_read_b64_tr_b16 v[192:193], %[fa] offset:1256\n\t"
      "ds_read_b64_tr_b16 v[194:195], %[fa] offset:1248\n\tv_mfma_f32_32x32x16_bf16 v[144:159], v[160:163], v[172:175], v[144:159]\n\t"
      "ds_read_b128 v[200:203], %[ua] offset:960\n\tds_read_b128 v[204:207], %[ua] offset:3520\n\tv_mfma_f32_32x32x16_bf16 v[144:159], v[164:167], v[168:171], v[144:159]\n\t"
      "ds_read_b64_tr_b16 v[196:197], %[fa] offset:3304\n\tds_read_b64_tr_b16 v[198:199], %[fa] offset:3296\n\ts_waitcnt lgkmcnt(6)\n\t"
      "v_mfma_f32_32x32x16_bf16 v[128:143], v[176:179], v[184:187], v[128:143]\n\tds_read_b64_tr_b16 v[208:209], %[fa] offset:1224\n\t"
      "ds_read_b64_tr_b16 v[210:211], %[fa] offset:1216\n\tv_mfma_f32_32x32x16_bf16 v[144:159], v[176:179], v[188:191], v[144:159]\n\t"
      "ds_read_b128 v[216:219], %[ua] offset:992\n\tds_read_b128 v[220:223], %[ua] offset:3552\n\tv_mfma_f32_32x32x16_bf16 v[144:159], v[180:183], v[184:187], v[144:159]\n\t"
      "ds_read_b64_tr_b16 v[212:213], %[fa] offset:3272\n\tds_read_b64_tr_b16 v[214:215], %[fa] offset:3264\n\ts_waitcnt lgkmcnt(6)\n\t"
      "v_mfma_f32_32x32x16_bf16 v[128:143], v[192:195], v[200:203], v[128:143]\n\tds_read_b64_tr_b16 v[160:161], %[fa] offset:1320\n\t"
      "ds_read_b64_tr_b16 v[162:163], %[fa] offset:1312\n\tv_mfma_f32_32x32x16_bf16 v[144:159], v[192:195], v[204:207], v[144:159]\n\t"
      "ds_read_b128 v[168:171], %[ua] offset:880\n\tds_read_b128 v[172:175], %[ua] offset:3440\n\tv_mfma_f32_32x32x16_bf16 v[144:159], v[196:199], v[200:203], v[144:159]\n\t"
      "ds_read_b64_tr_b16 v[164:165], %[fa] offset:3368\n\tds_read_b64_tr_b16 v[166:167], %[fa] offset:3360\n\ts_waitcnt lgkmcnt(6)\n\t"
      "v_mfma_f32_32x32x16_bf16 v[128:143], v[208:211], v[216:219], v[128:143]\n\tds_read_b64_tr_b16 v[176:177], %[fa] offset:1288\n\t"
      "ds_read_b64_tr_b16 v[178:179], %[fa] offset:1280\n\tv_mfma_f32_32x32x16_bf16 v[144:159], v[208:211], v[220:223], v[144:159]\n\t"
      "ds_read_b128 v[184:187], %[ua] offset:912\n\tds_read_b128 v[188:191], %[ua] offset:3472\n\tv_mfma_f32_32x32x16_bf16 v[144:159], v[212:215], v[216:219], v[144:159]\n\t"
      "ds_read_b64_tr_b16 v[180:181], %[fa] offset:3336\n\tds_read_b64_tr_b16 v[182:183], %[fa] offset:3328\n\ts_waitcnt lgkmcnt(6)\n\t"
      "v_mfma_f32_32x32x16_bf16 v[128:143], v[160:163], v[168:171], v[128:143]\n\tds_read_b64_tr_b16 v[192:193], %[fa] offset:1384\n\t"
      "ds_read_b64_tr_b16 v[194:195], %[fa] offset:1376\n\tv_mfma_f32_32x32x16_bf16 v[144:159], v[160:163], v[172:175], v[144:159]\n\t"
      "ds_read_b128 v[200:203], %[ua] offset:800\n\tds_read_b128 v[204:207], %[ua] offset:3360\n\tv_mfma_f32_32x32x16_bf16 v[144:159], v[164:167], v[168:171], v[144:159]\n\t"
      "ds_read_b64_tr_b16 v[196:197], %[fa] offset:3432\n\tds_read_b64_tr_b16 v[198:199], %[fa] offset:3424\n\ts_waitcnt lgkmcnt(6)\n\t"
      "v_mfma_f32_32x32x16_bf16 v[128:143], v[176:179], v[184:187], v[128:143]\n\tds_read_b64_tr_b16 v[208:209], %[fa] offset:1352\n\t"
      "ds_read_b64_tr_b16 v[210:211], %[fa] offset:1344\n\tv_mfma_f32_32x32x16_bf16 v[144:159], v[176:179], v[188:191], v[144:159]\n\t"
      "ds_read_b128 v[216:219], %[ua] offset:832\n\tds_read_b128 v[220:223], %[ua] offset:3392\n\tv_mfma_f32_32x32x16_bf16 v[144:159], v[180:183], v[184:187], v[144:159]\n\t"
      "ds_read_b64_tr_b16 v[212:213], %[fa] offset:3400\n\tds_read_b64_tr_b16 v[214:215], %[fa] offset:3392\n\ts_waitcnt lgkmcnt(6)\n\t"
      "v_mfma_f32_32x32x16_bf16 v[128:143], v[192:195], v[200:203], v[128:143]\n\tds_read_b64_tr_b16 v[160:161], %[fa] offset:1448\n\t"
      "ds_read_b64_tr_b16 v[162:163], %[fa] offset:1440\n\tv_mfma_f32_32x32x16_bf16 v[144:159], v[192:195], v[204:207], v[144:159]\n\t"
      "ds_read_b128 v[168:171], %[ua] offset:720\n\tds_read_b128 v[172:175], %[ua] offset:3280\n\tv_mfma_f32_32x32x16_bf16 v[144:159], v[196:199], v[200:203], v[144:159]\n\t"
      "ds_read_b64_tr_b16 v[164:165], %[fa] offset:3496\n\tds_read_b64_tr_b16 v[166:167], %[fa] offset:3488\n\ts_waitcnt lgkmcnt(6)\n\t"
      "v_mfma_f32_32x32x16_bf16 v[128:143], v[208:211], v[216:219], v[128:143]\n\tds_read_b64_tr_b16 v[176:177], %[fa] offset:1416\n\t"
      "ds_read_b64_tr_b16 v[178:179], %[fa] offset:1408\n\tv_mfma_f32_32x32x16_bf16 v[144:159], v[208:211], v[220:223], v[144:159]\n\t"
      "ds_read_b128 v[184:187], %[ua] offset:752\n\tds_read_b128 v[188:191], %[ua] offset:3312\n\tv_mfma_f32_32x32x16_bf16 v[144:159], v[212:215], v[216:219], v[144:159]\n\t"
      "ds_read_b64_tr_b16 v[180:181], %[fa] offset:3464\n\tds_read_b64_tr_b16 v[182:183], %[fa] offset:3456\n\ts_waitcnt lgkmcnt(6)\n\t"
      "v_mfma_f32_32x32x16_bf16 v[128:143], v[160:163], v[168:171], v[128:143]\n\tds_read_b64_tr_b16 v[192:193], %[fa] offset:1512\n\t"
      "ds_read_b64_tr_b16 v[194:195], %[fa] offset:1504\n\tv_mfma_f32_32x32x16_bf16 v[144:159], v[160:163], v[172:175], v[144:159]\n\t"
      "ds_read_b128 v[200:203], %[ua] offset:640\n\tds_read_b128 v[204:207], %[ua] offset:3200\n\tv_mfma_f32_32x32x16_bf16 v[144:159], v[164:167], v[168:171], v[144:159]\n\t"
      "ds_read_b64_tr_b16 v[196:197], %[fa] offset:3560\n\tds_read_b64_tr_b16 v[198:199], %[fa] offset:3552\n\ts_waitcnt lgkmcnt(6)\n\t"
      "v_mfma_f32_32x32x16_bf16 v[128:143], v[176:179], v[184:187], v[128:143]\n\tds_read_b64_tr_b16 v[208:209], %[fa] offset:1480\n\t"
      "ds_read_b64_tr_b16 v[210:211], %[fa] offset:1472\n\tv_mfma_f32_32x32x16_bf16 v[144:159], v[176:179], v[188:191], v[144:159]\n\t"
      "ds_read_b128 v[216:219], %[ua] offset:672\n\tds_read_b128 v[220:223], %[ua] offset:3232\n\tv_mfma_f32_32x32x16_bf16 v[144:159], v[180:183], v[184:187], v[144:159]\n\t"
      "ds_read_b64_tr_b16 v[212:213], %[fa] offset:3528\n\tds_read_b64_tr_b16 v[214:215], %[fa] offset:3520\n\ts_waitcnt lgkmcnt(6)\n\t"
      "v_mfma_f32_32x32x16_bf16 v[128:143], v[192:195], v[200:203], v[128:143]\n\tds_read_b64_tr_b16 v[160:161], %[fa] offset:1576\n\t"
      "ds_read_b64_tr_b16 v[162:163], %[fa] offset:1568\n\tv_mfma_f32_32x32x16_bf16 v[144:159], v[192:195], v[204:207], v[144:159]\n\t"
      "ds_read_b128 v[168:171], %[ua] offset:560\n\tds_read_b128 v[172:175], %[ua] offset:3120\n\tv_mfma_f32_32x32x16_bf16 v[144:159], v[196:199], v[200:203], v[144:159]\n\t"
      "ds_read_b64_tr_b16 v[164:165], %[fa] offset:3624\n\tds_read_b64_tr_b16 v[166:167], %[fa] offset:3616\n\ts_waitcnt lgkmcnt(6)\n\t"
      "v_mfma_f32_32x32x16_bf16 v[128:143], v[208:211], v[216:219], v[128:143]\n\tds_read_b64_tr_b16 v[176:177], %[fa] offset:1544\n\t"
      "ds_read_b64_tr_b16 v[178:179], %[fa] offset:1536\n\tv_mfma_f32_32x32x16_bf16 v[144:159], v[208:211], v[220:223], v[144:159]\n\t"
      "ds_read_b128 v[184:187], %[ua] offset:592\n\tds_read_b128 v[188:191], %[ua] offset:3152\n\tv_mfma_f32_32x32x16_bf16 v[144:159], v[212:215], v[216:219], v[144:159]\n\t"
      "ds_read_b64_tr_b16 v[180:181], %[fa] offset:3592\n\tds_read_b64_tr_b16 v[182:183], %[fa] offset:3584\n\ts_waitcnt lgkmcnt(6)\n\t"
      "v_mfma_f32_32x32x16_bf16 v[128:143], v[160:163], v[168:171], v[128:143]\n\tds_read_b64_tr_b16 v[192:193], %[fa] offset:1640\n\t"
      "ds_read_b64_tr_b16 v[194:195], %[fa] offset:1632\n\tv_mfma_f32_32x32x16_bf16 v[144:159], v[160:163], v[172:175], v[144:159]\n\t"
      "ds_read_b128 v[200:203], %[ua] offset:480\n\tds_read_b128 v[204:207], %[ua] offset:3040\n\tv_mfma_f32_32x32x16_bf16 v[144:159], v[164:167], v[168:171], v[144:159]\n\t"
      "ds_read_b64_tr_b16 v[196:197], %[fa] offset:3688\n\tds_read_b64_tr_b16 v[198:199], %[fa] offset:3680\n\ts_waitcnt lgkmcnt(6)\n\t"
      "v_mfma_f32_32x32x16_bf16 v[128:143], v[176:179], v[184:187], v[128:143]\n\tds_read_b64_tr_b16 v[208:209], %[fa] offset:1608\n\t"
      "ds_read_b64_tr_b16 v[210:211], %[fa] offset:1600\n\tv_mfma_f32_32x32x16_bf16 v[144:159], v[176:179], v[188:191], v[144:159]\n\t"
      "ds_read_b128 v[216:219], %[ua] offset:512\n\tds_read_b128 v[220:223], %[ua] offset:3072\n\tv_mfma_f32_32x32x16_bf16 v[144:159], v[180:183], v[184:187], v[144:159]\n\t"
      "ds_read_b64_tr_b16 v[212:213], %[fa] offset:3656\n\tds_read_b64_tr_b16 v[214:215], %[fa] offset:3648\n\ts_waitcnt lgkmcnt(6)\n\t"
      "v_mfma_f32_32x32x16_bf16 v[128:143], v[192:195], v[200:203], v[128:143]\n\tds_read_b64_tr_b16 v[160:161], %[fa] offset:1704\n\t"
      "ds_read_b64_tr_b16 v[162:163], %[fa] offset:1696\n\tv_mfma_f32_32x32x16_bf16 v[144:159], v[192:195], v[204:207], v[144:159]\n\t"
      "ds_read_b128 v[168:171], %[ua] offset:400\n\tds_read_b128 v[172:175], %[ua] offset:2960\n\tv_mfma_f32_32x32x16_bf16 v[144:159], v[196:199], v[200:203], v[144:159]\n\t"
      "ds_read_b64_tr_b16 v[164:165], %[fa] offset:3752\n\tds_read_b64_tr_b16 v[166:167], %[fa] offset:3744\n\ts_waitcnt lgkmcnt(6)\n\t"
      "v_mfma_f32_32x32x16_bf16 v[128:143], v[208:211], v[216:219], v[128:143]\n\tds_read_b64_tr_b16 v[176:177], %[fa] offset:1672\n\t"
      "ds_read_b64_tr_b16 v[178:179], %[fa] offset:1664\n\tv_mfma_f32_32x32x16_bf16 v[144:159], v[208:211], v[220:223], v[144:159]\n\t"
      "ds_read_b128 v[184:187], %[ua] offset:432\n\tds_read_b128 v[188:191], %[ua] offset:2992\n\tv_mfma_f32_32x32x16_bf16 v[144:159], v[212:215], v[216:219], v[144:159]\n\t"
      "ds_read_b64_tr_b16 v[180:181], %[fa] offset:3720\n\tds_read_b64_tr_b16 v[182:183], %[fa] offset:3712\n\ts_waitcnt lgkmcnt(6)\n\t"
      "v_mfma_f32_32x32x16_bf16 v[128:143], v[160:163], v[168:171], v[128:143]\n\tds_read_b64_tr_b16 v[192:193], %[fa] offset:1768\n\t"
      "ds_read_b64_tr_b16 v[194:195], %[fa] offset:1760\n\tv_mfma_f32_32x32x16_bf16 v[144:159], v[160:163], v[172:175], v[144:159]\n\t"
      "ds_read_b128 v[200:203], %[ua] offset:320\n\tds_read_b128 v[204:207], %[ua] offset:2880\n\tv_mfma_f32_32x32x16_bf16 v[144:159], v[164:167], v[168:171], v[144:159]\n\t"
      "ds_read_b64_tr_b16 v[196:197], %[fa] offset:3816\n\tds_read_b64_tr_b16 v[198:199], %[fa] offset:3808\n\ts_waitcnt lgkmcnt(6)\n\t"
      "v_mfma_f32_32x32x16_bf16 v[128:143], v[176:179], v[184:187], v[128:143]\n\tds_read_b64_tr_b16 v[208:209], %[fa] offset:1736\n\t"
      "ds_read_b64_tr_b16 v[210:211], %[fa] offset:1728\n\tv_mfma_f32_32x32x16_bf16 v[144:159], v[176:179], v[188:191], v[144:159]\n\t"
      "ds_read_b128 v[216:219], %[ua] offset:352\n\tds_read_b128 v[220:223], %[ua] offset:2912\n\tv_mfma_f32_32x32x16_bf16 v[144:159], v[180:183], v[184:187], v[144:159]\n\t"
      "ds_read_b64_tr_b16 v[212:213], %[fa] offset:3784\n\tds_read_b64_tr_b16 v[214:215], %[fa] offset:3776\n\ts_waitcnt lgkmcnt(6)\n\t"
      "v_mfma_f32_32x32x16_bf16 v[128:143], v[192:195], v[200:203], v[128:143]\n\tds_read_b64_tr_b16 v[160:161], %[fa] offset:1832\n\t"
      "ds_read_b64_tr_b16 v[162:163], %[fa] offset:1824\n\tv_mfma_f32_32x32x16_bf16 v[144:159], v[192:195], v[204:207], v[144:159]\n\t"
      "ds_read_b128 v[168:171], %[ua] offset:240\n\tds_read_b128 v[172:175], %[ua] offset:2800\n\tv_mfma_f32_32x32x16_bf16 v[144:159], v[196:199], v[200:203], v[144:159]\n\t"
      "ds_read_b64_tr_b16 v[164:165], %[fa] offset:3880\n\tds_read_b64_tr_b16 v[166:167], %[fa] offset:3872\n\ts_waitcnt lgkmcnt(6)\n\t"
      "v_mfma_f32_32x32x16_bf16 v[128:143], v[208:211], v[216:219], v[128:143]\n\tds_read_b64_tr_b16 v[176:177], %[fa] offset:1800\n\t"
      "ds_read_b64_tr_b16 v[178:179], %[fa] offset:1792\n\tv_mfma_f32_32x32x16_bf16 v[144:159], v[208:211], v[220:223], v[144:159]\n\t"
      "ds_read_b128 v[184:187], %[ua] offset:272\n\tds_read_b128 v[188:191], %[ua] offset:2832\n\tv_mfma_f32_32x32x16_bf16 v[144:159], v[212:215], v[216:219], v[144:159]\n\t"
      "ds_read_b64_tr_b16 v[180:181], %[fa] offset:3848\n\tds_read_b64_tr_b16 v[182:183], %[fa] offset:3840\n\ts_waitcnt lgkmcnt(6)\n\t"
      "v_mfma_f32_32x32x16_bf16 v[128:143], v[160:163], v[168:171], v[128:143]\n\tds_read_b64_tr_b16 v[192:193], %[fa] offset:1896\n\t"
      "ds_read_b64_tr_b16 v[194:195], %[fa] offset:1888\n\tv_mfma_f32_32x32x16_bf16 v[144:159], v[160:163], v[172:175], v[144:159]\n\t"
      "ds_read_b128 v[200:203], %[ua] offset:160\n\tds_read_b128 v[204:207], %[ua] offset:2720\n\tv_mfma_f32_32x32x16_bf16 v[144:159], v[164:167], v[168:171], v[144:159]\n\t"
      "ds_read_b64_tr_b16 v[196:197], %[fa] offset:3944\n\tds_read_b64_tr_b16 v[198:199], %[fa] offset:3936\n\ts_waitcnt lgkmcnt(6)\n\t"
      "v_mfma_f32_32x32x16_bf16 v[128:143], v[176:179], v[184:187], v[128:143]\n\tds_read_b64_tr_b16 v[208:209], %[fa] offset:1864\n\t"
      "ds_read_b64_tr_b16 v[210:211], %[fa] offset:1856\n\tv_mfma_f32_32x32x16_bf16 v[144:159], v[176:179], v[188:191], v[144:159]\n\t"
      "ds_read_b128 v[216:219], %[ua] offset:192\n\tds_read_b128 v[220:223], %[ua] offset:2752\n\tv_mfma_f32_32x32x16_bf16 v[144:159], v[180:183], v[184:187], v[144:159]\n\t"
      "ds_read_b64_tr_b16 v[212:213], %[fa] offset:3912\n\tds_read_b64_tr_b16 v[214:215], %[fa] offset:3904\n\ts_waitcnt lgkmcnt(6)\n\t"
      "v_mfma_f32_32x32x16_bf16 v[128:143], v[192:195], v[200:203], v[128:143]\n\tds_read_b64_tr_b16 v[160:161], %[fa] offset:1960\n\t"
      "ds_read_b64_tr_b16 v[162:163], %[fa] offset:1952\n\tv_mfma_f32_32x32x16_bf16 v[144:159], v[192:195], v[204:207], v[144:159]\n\t"
      "ds_read_b128 v[168:171], %[ua] offset:80\n\tds_read_b128 v[172:175], %[ua] offset:2640\n\tv_mfma_f32_32x32x16_bf16 v[144:159], v[196:199], v[200:203], v[144:159]\n\t"
      "ds_read_b64_tr_b16 v[164:165], %[fa] offset:4008\n\tds_read_b64_tr_b16 v[166:167], %[fa] offset:4000\n\ts_waitcnt lgkmcnt(6)\n\t"
      "v_mfma_f32_32x32x16_bf16 v[128:143], v[208:211], v[216:219], v[128:143]\n\tds_read_b64_tr_b16 v[176:177], %[fa] offset:1928\n\t"
      "ds_read_b64_tr_b16 v[178:179], %[fa] offset:1920\n\tv_mfma_f32_32x32x16_bf16 v[144:159], v[208:211], v[220:223], v[144:159]\n\t"
      "ds_read_b128 v[184:187], %[ua] offset:112\n\tds_read_b128 v[188:191], %[ua] offset:2672\n\tv_mfma_f32_32x32x16_bf16 v[144:159], v[212:215], v[216:219], v[144:159]\n\t"
      "ds_read_b64_tr_b16 v[180:181], %[fa] offset:3976\n\tds_read_b64_tr_b16 v[182:183], %[fa] offset:3968\n\ts_waitcnt lgkmcnt(6)\n\t"
      "v_mfma_f32_32x32x16_bf16 v[128:143], v[160:163], v[168:171], v[128:143]\n\tds_read_b64_tr_b16 v[192:193], %[fa] offset:2024\n\t"
      "ds_read_b64_tr_b16 v[194:195], %[fa] offset:2016\n\tv_mfma_f32_32x32x16_bf16 v[144:159], v[160:163], v[172:175], v[144:159]\n\tds_read_b128 v[200:203], %[ua] offset:0\n\t"
      "ds_read_b128 v[204:207], %[ua] offset:2560\n\tv_mfma_f32_32x32x16_bf16 v[144:159], v[164:167], v[168:171], v[144:159]\n\t"
      "ds_read_b64_tr_b16 v[196:197], %[fa] offset:4072\n\tds_read_b64_tr_b16 v[198:199], %[fa] offset:4064\n\ts_waitcnt lgkmcnt(6)\n\t"
      "v_mfma_f32_32x32x16_bf16 v[128:143], v[176:179], v[184:187], v[128:143]\n\tds_read_b64_tr_b16 v[208:209], %[fa] offset:1992\n\t"
      "ds_read_b64_tr_b16 v[210:211], %[fa] offset:1984\n\tv_mfma_f32_32x32x16_bf16 v[144:159], v[176:179], v[188:191], v[144:159]\n\t"
      "ds_read_b128 v[216:219], %[ua] offset:32\n\tds_read_b128 v[220:223], %[ua] offset:2592\n\tv_mfma_f32_32x32x16_bf16 v[144:159], v[180:183], v[184:187], v[144:159]\n\t"
      "ds_read_b64_tr_b16 v[212:213], %[fa] offset:4040\n\tds_read_b64_tr_b16 v[214:215], %[fa] offset:4032\n\ts_waitcnt lgkmcnt(6)\n\t"
      "v_mfma_f32_32x32x16_bf16 v[128:143], v[192:195], v[200:203], v[128:143]\n\tv_mfma_f32_32x32x16_bf16 v[144:159], v[192:195], v[204:207], v[144:159]\n\t"
      "v_mfma_f32_32x32x16_bf16 v[144:159], v[196:199], v[200:203], v[144:159]\n\ts_waitcnt lgkmcnt(0)\n\t"
      "v_mfma_f32_32x32x16_bf16 v[128:143], v[208:211], v[216:219], v[128:143]\n\tv_mfma_f32_32x32x16_bf16 v[144:159], v[208:211], v[220:223], v[144:159]\n\t"
      "v_mfma_f32_32x32x16_bf16 v[144:159], v[212:215], v[216:219], v[144:159]\n\ts_nop 11\n\t"
      : "=&{v[128:143]}"(acc[0]), "=&{v[144:159]}"(acc[1]), "=&{v[160:191]}"(x0), "=&{v[192:223]}"(x1)
      : [fa] "v"(FC + (4 * w + ((lane >> 2) & 3)) * CP + 24 + (lane & 16) * 2 + (lane & 3) * 8 - h2 * 16), [ua] "v"(ub + (r + 1) * 80 + h2 * 16) : "memory");
    const float dv = k ? bhi(dd) : blo(dd);
#pragma unroll
    for (int i = 0; i < 8; ++i) {
      LAS v2u* at = (LAS v2u*)(c.L + ub + (32 + (i >> 2) * 32 + r) * 80 + (i & 3) * 16 + h2 * 8);
      const v2u uu = *at;
      const f32x16& a = acc[i >> 2];
      const int m = 4 * (i & 3);
      const f32x2 y0 = rb2(f32x2{a[m], a[m + 1]}) + rb2(f32x2{blo(uu.x), bhi(uu.x)} * dv), y1 = rb2(f32x2{a[m + 2], a[m + 3]}) + rb2(f32x2{blo(uu.y), bhi(uu.y)} * dv);
      *at = v2u{pk(y0.x, y0.y), pk(y1.x, y1.y)};
    }
    LGKM(0);
#pragma unroll
    for (int i = 0; i < 4; ++i) st16s(c.rs, i * 1024 + lane * 16, R_YT + amap((d0 + k) * 4096, far), lds16(c.L, ub + (32 + i * 16 + (lane >> 2)) * 80 + (lane & 3) * 16));
  }
  STAMP(6);
  {
    const rsrc_t ro = mkrs(p->out, 2048 * 2048 * 2);
    v4u yv[8];
    asm volatile("" :: "v"(warm));
    group_wait(1, 8);
#pragma unroll
    for (int ch = 0; ch < 8; ++ch) yv[ch] = ld16s(c.rs, seq_off(4 * cg + w, lane, va), R_YT + amap(ch * 4096, 0));
    const int at = FC + 4 * w * CP;
#pragma unroll
    for (int ch = 0; ch < 8; ++ch) sts16(c.L, at + ch * 1152 + (lane & 7) * 16 + (lane >> 3) * 144, yv[ch]);
    LGKM(0);
#pragma unroll
    for (int ch = 0; ch < 8; ++ch) yv[ch] = lds16(c.L, at + ch * 1152 + (lane >> 3) * 16 + (lane & 7) * 144);
#pragma unroll
    for (int i = 0; i < 8; ++i) {
      float o[8];
#pragma unroll
      for (int ch = 0; ch < 8; ++ch) o[ch] = ((ch & 1) ? bhi(qr[i][ch >> 1]) : blo(qr[i][ch >> 1])) * ((i & 1) ? bhi(yv[ch][i >> 1]) : blo(yv[ch][i >> 1]));
      __builtin_amdgcn_raw_buffer_store_b128(v4u{pk(o[0], o[1]), pk(o[2], o[3]), pk(o[4], o[5]), pk(o[6], o[7])}, ro, (t0 + i) * 4096 + (1024 + de) * 2, 0, BAUX);
    }
  }
  if (c.budget == 0u) __hip_atomic_store(c.ctrl + C_TMO / 4, 1u, RLX_AGENT);
}

extern "C" __global__ void __launch_bounds__(256, 1) mk_mega(Params p) {
  extern __shared__ __attribute__((aligned(16))) unsigned char lds_raw[];
  Ctx c;
  c.ctrl = (gu32*)p.ctrl;
  c.L = (lds_t)lds_raw;
  c.w = __builtin_amdgcn_readfirstlane(threadIdx.x >> 6);
  c.lane = threadIdx.x & 63;
  c.b = (int)(blockIdx.x & 255u);
  c.att = (int)(blockIdx.x >> 8);
  c.ask = 0;
  if (c.w == 3) {
    unsigned long long pc; unsigned len;
    asm volatile("s_getpc_b64 %0\n1:\n\ts_mov_b32 %1, 0\n\ts_max_i32 %1, %1, .Lmk_end0-1b\n\ts_max_i32 %1, %1, .Lmk_end1-1b\n\ts_max_i32 %1, %1, .Lmk_end2-1b\n\ts_max_i32 %1, %1, .Lmk_end3-1b\n\t"
                 "s_max_i32 %1, %1, .Lmk_end4-1b\n\ts_max_i32 %1, %1, .Lmk_end5-1b\n\ts_max_i32 %1, %1, .Lmk_end6-1b" : "=s"(pc), "=s"(len) :: "scc");
    const unsigned t = __builtin_amdgcn_raw_buffer_load_b32(mkrs((const void*)pc, len), ((c.b >> 3) + 32 * c.lane) * 128, 0, 0);
    asm volatile("" :: "v"(t));
  }
  c.slot = (c.att ? (c.b & 7) : (int)((unsigned)__builtin_amdgcn_s_getreg(20 | (3 << 11)) & 7u)) * 32 + (c.b >> 3);
  if (c.b == SERVER && c.w == 3) serve(c);
  const int x = c.slot >> 5, t = c.slot & 31;
  if (x < 4) {
#define QKM 0
    const int qk = QKM ? (QKM >> x) & 1 : t < 8, h = QKM ? __builtin_popcount((qk ? QKM : ~QKM) & ((1 << x) - 1)) * 16 + t : x * 8 + (t & 7);
    if (t < 16 && qk) qk_role(c, h);
    else if (t < 16) vo_role(c, h);
    else y_role(c, (t & 12) * 4 + x * 4 + (t & 3));
  } else {
    if (t < 24) g_role(c, t >> 3, (x - 4) * 8 + (t & 7));
    else zc_role(c, (x - 4) * 8 + t - 24);
    STAMP(2);
    conv_role(c, (x - 4) * 32 + t);
  }
  STAMP(3);
  STAMP(7);
}

}

extern "C" void kernel_launch(void* const* d_in, const int* in_sizes, int n_in, void* d_out, int out_size, void* d_ws, size_t ws_size,
                              hipStream_t stream) {
  using namespace mk;
  (void)in_sizes; (void)n_in; (void)out_size; (void)ws_size;
  Params p;
  p.x = (const u16*)d_in[0]; p.mem0 = (const u16*)d_in[1];
  p.Wq = (const u16*)d_in[2]; p.Wk = (const u16*)d_in[3]; p.Wv = (const u16*)d_in[4]; p.Wo = (const u16*)d_in[5];
  p.bq = (const u16*)d_in[7]; p.bk = (const u16*)d_in[8]; p.bv = (const u16*)d_in[9];
  p.shift = (const u16*)d_in[10]; p.ssmk = (const u16*)d_in[11]; p.ssmD = (const u16*)d_in[12];
  p.out = (u16*)d_out;
  p.ctrl = (unsigned char*)d_ws;
  p.data = (unsigned char*)(((size_t)d_ws + ws_size - R_END - MB) & ~(size_t)((2u << 20) - 1));
  (void)hipFuncSetAttribute((const void*)mk_mega, hipFuncAttributeMaxDynamicSharedMemorySize, LDS_BYTES);
  hipLaunchKernelGGL(mk_mega, dim3(256), dim3(256), LDS_BYTES, stream, p);
}
```
